```python
import torch

def _softmax(x, axis=-1):
    return torch.softmax(x, dim=axis)
def _gelu(x):
    return torch.nn.functional.gelu(x, approximate="tanh")
def _silu(x):
    return torch.nn.functional.silu(x)
def _sigmoid(x):
    return torch.sigmoid(x)
def _layernorm(x, axis=-1, eps=1e-5):
    m = x.mean(dim=axis, keepdim=True)
    v = x.var(dim=axis, keepdim=True, unbiased=False)
    return (x - m) / torch.sqrt(v + eps)
def _scatter_add(tgt, idx, src, axis=0):
    return tgt.clone().index_add_(axis, idx, src)
def _segment_sum(src, seg, n):
    out = torch.zeros((n,) + src.shape[1:], dtype=src.dtype)
    return out.index_add_(0, seg, src)
def _pad(x, pw):
    flat = tuple(v for pair in reversed(pw) for v in pair)
    return torch.nn.functional.pad(x, flat)

kernel_name = 'standalone__detection_seg_fpn_lateral__vision'

def setup_inputs():

    _w = (lambda a: (lambda t: t.to(torch.bfloat16) if t.is_floating_point() else t)(torch.as_tensor(a)))
    H = W = (384 // 2) * 2
    C = 384
    k = 1
    return {
        "c_fine": _w((lambda *s: torch.randn(*s, dtype=torch.bfloat16) * 0.05)(1, H, W, C)),
        "p_coarse": _w((lambda *s: torch.randn(*s, dtype=torch.bfloat16) * 0.05)(1, H // 2, W // 2, C)),
        "w_lat": _w((lambda *s: torch.randn(*s, dtype=torch.bfloat16) * 0.05)(C, C)),
        "w_out": _w((lambda *s: torch.randn(*s, dtype=torch.bfloat16) * 0.05)(k, k, C, C)),
    }

def reference(c_fine, p_coarse, w_lat, w_out):
    B, H, W, C = c_fine.shape
    k = 1
    lat = torch.einsum("bhwc,cd->bhwd", c_fine, w_lat)
    up = (lambda x, n, axis: torch.repeat_interleave(x, n, dim=axis))((lambda x, n, axis: torch.repeat_interleave(x, n, dim=axis))(p_coarse, 2, axis=1), 2, axis=2)
    m = lat + up
    pad = k // 2
    mp = _pad(m, ((0, 0), (pad, pad), (pad, pad), (0, 0)))
    o = None
    for di in range(k):
        for dj in range(k):
            t = torch.einsum("bhwc,cd->bhwd", mp[:, di:di + H, dj:dj + W, :], w_out[di, dj])
            o = t if o is None else o + t
    return o
```

```cpp
#include <hip/hip_runtime.h>
#include <hip/hip_bf16.h>

typedef __attribute__((ext_vector_type(4))) unsigned u32x4;
typedef __attribute__((ext_vector_type(2))) unsigned u32x2;
#define NSTAMPS 0

__device__ __forceinline__ unsigned col0(unsigned i) { return i + (i >= 5 ? 6 : 0) + (i >= 10 ? 5 : 0); }

static __device__ const unsigned __attribute__((aligned(128))) lane_tab[2048] = {
  98304, 98368, 98304, 0, 99088, 99152, 98320, 16, 99872, 99936, 98336, 32, 100656, 100720, 98352, 48,
  101440, 101376, 98368, 64, 102224, 102160, 98384, 80, 103008, 102944, 98400, 96, 103792, 103728, 98416, 112,
  104448, 104512, 98432, 128, 105232, 105296, 98448, 144, 106016, 106080, 98464, 160, 106800, 106864, 98480, 176,
  107584, 107520, 98496, 192, 108368, 108304, 98512, 208, 109152, 109088, 98528, 224, 109936, 109872, 98544, 240,
  98320, 98384, 98560, 256, 99072, 99136, 98576, 272, 99888, 99952, 98592, 288, 100640, 100704, 98608, 304,
  101456, 101392, 98624, 320, 102208, 102144, 98640, 336, 103024, 102960, 98656, 352, 103776, 103712, 98672, 368,
  104464, 104528, 98688, 384, 105216, 105280, 98704, 400, 106032, 106096, 98720, 416, 106784, 106848, 98736, 432,
  107600, 107536, 98752, 448, 108352, 108288, 98768, 464, 109168, 109104, 98784, 480, 109920, 109856, 98800, 496,
  98336, 98400, 98816, 512, 99120, 99184, 98832, 528, 99840, 99904, 98848, 544, 100624, 100688, 98864, 560,
  101472, 101408, 98880, 576, 102256, 102192, 98896, 592, 102976, 102912, 98912, 608, 103760, 103696, 98928, 624,
  104480, 104544, 98944, 640, 105264, 105328, 98960, 656, 105984, 106048, 98976, 672, 106768, 106832, 98992, 688,
  107616, 107552, 99008, 704, 108400, 108336, 99024, 720, 109120, 109056, 99040, 736, 109904, 109840, 99056, 752,
  98352, 98416, 99072, 1552, 99104, 99168, 99088, 1536, 99856, 99920, 99104, 1584, 100608, 100672, 99120, 1568,
  101488, 101424, 99136, 1616, 102240, 102176, 99152, 1600, 102992, 102928, 99168, 1648, 103744, 103680, 99184, 1632,
  104496, 104560, 99200, 1680, 105248, 105312, 99216, 1664, 106000, 106064, 99232, 1712, 106752, 106816, 99248, 1696,
  107632, 107568, 99264, 1744, 108384, 108320, 99280, 1728, 109136, 109072, 99296, 1776, 109888, 109824, 99312, 1760,
  98496, 98560, 101376, 6208, 99280, 99344, 101392, 6224, 100064, 100128, 101408, 6240, 100848, 100912, 101424, 6256,
  101504, 101696, 101440, 6144, 102288, 102480, 101456, 6160, 103072, 103264, 101472, 6176, 103856, 104048, 101488, 6192,
  104640, 104704, 101504, 6336, 105424, 105488, 101520, 6352, 106208, 106272, 101536, 6368, 106992, 107056, 101552, 6384,
  107648, 107840, 101568, 6272, 108432, 108624, 101584, 6288, 109216, 109408, 101600, 6304, 110000, 110192, 101616, 6320,
  98512, 98576, 101632, 6464, 99264, 99328, 101648, 6480, 100080, 100144, 101664, 6496, 100832, 100896, 101680, 6512,
  101520, 101712, 101696, 6400, 102272, 102464, 101712, 6416, 103088, 103280, 101728, 6432, 103840, 104032, 101744, 6448,
  104656, 104720, 101760, 6592, 105408, 105472, 101776, 6608, 106224, 106288, 101792, 6624, 106976, 107040, 101808, 6640,
  107664, 107856, 101824, 6528, 108416, 108608, 101840, 6544, 109232, 109424, 101856, 6560, 109984, 110176, 101872, 6576,
  98528, 98592, 101888, 6720, 99312, 99376, 101904, 6736, 100032, 100096, 101920, 6752, 100816, 100880, 101936, 6768,
  101536, 101728, 101952, 6656, 102320, 102512, 101968, 6672, 103040, 103232, 101984, 6688, 103824, 104016, 102000, 6704,
  104672, 104736, 102016, 6848, 105456, 105520, 102032, 6864, 106176, 106240, 102048, 6880, 106960, 107024, 102064, 6896,
  107680, 107872, 102080, 6784, 108464, 108656, 102096, 6800, 109184, 109376, 102112, 6816, 109968, 110160, 102128, 6832,
  98544, 98608, 102144, 16976, 99296, 99360, 102160, 16960, 100048, 100112, 102176, 17008, 100800, 100864, 102192, 16992,
  101552, 101744, 102208, 16912, 102304, 102496, 102224, 16896, 103056, 103248, 102240, 16944, 103808, 104000, 102256, 16928,
  104688, 104752, 102272, 17104, 105440, 105504, 102288, 17088, 106192, 106256, 102304, 17136, 106944, 107008, 102320, 17120,
  107696, 107888, 102336, 17040, 108448, 108640, 102352, 17024, 109200, 109392, 102368, 17072, 109952, 110144, 102384, 17056,
  1808, 3616, 0, 21632, 1792, 3632, 16, 21648, 1840, 3584, 32, 21664, 1824, 3600, 48, 21680,
  1872, 3680, 64, 21696, 1856, 3696, 80, 21712, 1904, 3648, 96, 21728, 1888, 3664, 112, 21744,
  1936, 3744, 128, 21504, 1920, 3760, 144, 21520, 1968, 3712, 160, 21536, 1952, 3728, 176, 21552,
  2000, 3808, 192, 21568, 1984, 3824, 208, 21584, 2032, 3776, 224, 21600, 2016, 3792, 240, 21616,
  2064, 4656, 768, 22400, 2048, 4640, 784, 22416, 2096, 4624, 800, 22432, 2080, 4608, 816, 22448,
  2128, 4720, 832, 22464, 2112, 4704, 848, 22480, 2160, 4688, 864, 22496, 2144, 4672, 880, 22512,
  2192, 4784, 896, 22272, 2176, 4768, 912, 22288, 2224, 4752, 928, 22304, 2208, 4736, 944, 22320,
  2256, 4848, 960, 22336, 2240, 4832, 976, 22352, 2288, 4816, 992, 22368, 2272, 4800, 1008, 22384,
  3104, 4912, 1552, 23184, 3120, 4896, 1536, 23168, 3072, 4880, 1584, 23216, 3088, 4864, 1568, 23200,
  3168, 4976, 1616, 23248, 3184, 4960, 1600, 23232, 3136, 4944, 1648, 23280, 3152, 4928, 1632, 23264,
  3232, 5040, 1680, 23056, 3248, 5024, 1664, 23040, 3200, 5008, 1712, 23088, 3216, 4992, 1696, 23072,
  3296, 5104, 1744, 23120, 3312, 5088, 1728, 23104, 3264, 5072, 1776, 23152, 3280, 5056, 1760, 23136,
  3360, 5168, 2320, 23952, 3376, 5152, 2304, 23936, 3328, 5136, 2352, 23984, 3344, 5120, 2336, 23968,
  3424, 5232, 2384, 24016, 3440, 5216, 2368, 24000, 3392, 5200, 2416, 24048, 3408, 5184, 2400, 24032,
  3488, 5296, 2448, 23824, 3504, 5280, 2432, 23808, 3456, 5264, 2480, 23856, 3472, 5248, 2464, 23840,
  3552, 5360, 2512, 23888, 3568, 5344, 2496, 23872, 3520, 5328, 2544, 23920, 3536, 5312, 2528, 23904,
  17232, 19040, 3104, 32416, 17216, 19056, 3120, 32432, 17264, 19008, 3072, 32384, 17248, 19024, 3088, 32400,
  17168, 18976, 3168, 32480, 17152, 18992, 3184, 32496, 17200, 18944, 3136, 32448, 17184, 18960, 3152, 32464,
  17360, 19168, 3232, 32288, 17344, 19184, 3248, 32304, 17392, 19136, 3200, 32256, 17376, 19152, 3216, 32272,
  17296, 19104, 3296, 32352, 17280, 19120, 3312, 32368, 17328, 19072, 3264, 32320, 17312, 19088, 3280, 32336,
  17488, 20080, 3872, 33184, 17472, 20064, 3888, 33200, 17520, 20048, 3840, 33152, 17504, 20032, 3856, 33168,
  17424, 20016, 3936, 33248, 17408, 20000, 3952, 33264, 17456, 19984, 3904, 33216, 17440, 19968, 3920, 33232,
  17616, 20208, 4000, 33056, 17600, 20192, 4016, 33072, 17648, 20176, 3968, 33024, 17632, 20160, 3984, 33040,
  17552, 20144, 4064, 33120, 17536, 20128, 4080, 33136, 17584, 20112, 4032, 33088, 17568, 20096, 4048, 33104,
  18528, 20336, 4656, 33968, 18544, 20320, 4640, 33952, 18496, 20304, 4624, 33936, 18512, 20288, 4608, 33920,
  18464, 20272, 4720, 34032, 18480, 20256, 4704, 34016, 18432, 20240, 4688, 34000, 18448, 20224, 4672, 33984,
  18656, 20464, 4784, 33840, 18672, 20448, 4768, 33824, 18624, 20432, 4752, 33808, 18640, 20416, 4736, 33792,
  18592, 20400, 4848, 33904, 18608, 20384, 4832, 33888, 18560, 20368, 4816, 33872, 18576, 20352, 4800, 33856,
  18784, 20592, 5424, 34736, 18800, 20576, 5408, 34720, 18752, 20560, 5392, 34704, 18768, 20544, 5376, 34688,
  18720, 20528, 5488, 34800, 18736, 20512, 5472, 34784, 18688, 20496, 5456, 34768, 18704, 20480, 5440, 34752,
  18912, 20720, 5552, 34608, 18928, 20704, 5536, 34592, 18880, 20688, 5520, 34576, 18896, 20672, 5504, 34560,
  18848, 20656, 5616, 34672, 18864, 20640, 5600, 34656, 18816, 20624, 5584, 34640, 18832, 20608, 5568, 34624,
  98688, 98752, 104448, 21504, 99472, 99536, 104464, 21520, 100256, 100320, 104480, 21536, 101040, 101104, 104496, 21552,
  101824, 101760, 104512, 21568, 102608, 102544, 104528, 21584, 103392, 103328, 104544, 21600, 104176, 104112, 104560, 21616,
  104832, 104896, 104576, 21632, 105616, 105680, 104592, 21648, 106400, 106464, 104608, 21664, 107184, 107248, 104624, 21680,
  107968, 107904, 104640, 21696, 108752, 108688, 104656, 21712, 109536, 109472, 104672, 21728, 110320, 110256, 104688, 21744,
  98704, 98768, 104704, 21760, 99456, 99520, 104720, 21776, 100272, 100336, 104736, 21792, 101024, 101088, 104752, 21808,
  101840, 101776, 104768, 21824, 102592, 102528, 104784, 21840, 103408, 103344, 104800, 21856, 104160, 104096, 104816, 21872,
  104848, 104912, 104832, 21888, 105600, 105664, 104848, 21904, 106416, 106480, 104864, 21920, 107168, 107232, 104880, 21936,
  107984, 107920, 104896, 21952, 108736, 108672, 104912, 21968, 109552, 109488, 104928, 21984, 110304, 110240, 104944, 22000,
  98720, 98784, 104960, 22016, 99504, 99568, 104976, 22032, 100224, 100288, 104992, 22048, 101008, 101072, 105008, 22064,
  101856, 101792, 105024, 22080, 102640, 102576, 105040, 22096, 103360, 103296, 105056, 22112, 104144, 104080, 105072, 22128,
  104864, 104928, 105088, 22144, 105648, 105712, 105104, 22160, 106368, 106432, 105120, 22176, 107152, 107216, 105136, 22192,
  108000, 107936, 105152, 22208, 108784, 108720, 105168, 22224, 109504, 109440, 105184, 22240, 110288, 110224, 105200, 22256,
  98736, 98800, 105216, 23056, 99488, 99552, 105232, 23040, 100240, 100304, 105248, 23088, 100992, 101056, 105264, 23072,
  101872, 101808, 105280, 23120, 102624, 102560, 105296, 23104, 103376, 103312, 105312, 23152, 104128, 104064, 105328, 23136,
  104880, 104944, 105344, 23184, 105632, 105696, 105360, 23168, 106384, 106448, 105376, 23216, 107136, 107200, 105392, 23200,
  108016, 107952, 105408, 23248, 108768, 108704, 105424, 23232, 109520, 109456, 105440, 23280, 110272, 110208, 105456, 23264,
  98880, 98944, 107520, 35392, 99664, 99728, 107536, 35408, 100448, 100512, 107552, 35424, 101232, 101296, 107568, 35440,
  101888, 102080, 107584, 35328, 102672, 102864, 107600, 35344, 103456, 103648, 107616, 35360, 104240, 104432, 107632, 35376,
  105024, 105088, 107648, 35520, 105808, 105872, 107664, 35536, 106592, 106656, 107680, 35552, 107376, 107440, 107696, 35568,
  108032, 108224, 107712, 35456, 108816, 109008, 107728, 35472, 109600, 109792, 107744, 35488, 110384, 110576, 107760, 35504,
  98896, 98960, 107776, 35648, 99648, 99712, 107792, 35664, 100464, 100528, 107808, 35680, 101216, 101280, 107824, 35696,
  101904, 102096, 107840, 35584, 102656, 102848, 107856, 35600, 103472, 103664, 107872, 35616, 104224, 104416, 107888, 35632,
  105040, 105104, 107904, 35776, 105792, 105856, 107920, 35792, 106608, 106672, 107936, 35808, 107360, 107424, 107952, 35824,
  108048, 108240, 107968, 35712, 108800, 108992, 107984, 35728, 109616, 109808, 108000, 35744, 110368, 110560, 108016, 35760,
  98912, 98976, 108032, 35904, 99696, 99760, 108048, 35920, 100416, 100480, 108064, 35936, 101200, 101264, 108080, 35952,
  101920, 102112, 108096, 35840, 102704, 102896, 108112, 35856, 103424, 103616, 108128, 35872, 104208, 104400, 108144, 35888,
  105056, 105120, 108160, 36032, 105840, 105904, 108176, 36048, 106560, 106624, 108192, 36064, 107344, 107408, 108208, 36080,
  108064, 108256, 108224, 35968, 108848, 109040, 108240, 35984, 109568, 109760, 108256, 36000, 110352, 110544, 108272, 36016,
  98928, 98992, 108288, 36944, 99680, 99744, 108304, 36928, 100432, 100496, 108320, 36976, 101184, 101248, 108336, 36960,
  101936, 102128, 108352, 36880, 102688, 102880, 108368, 36864, 103440, 103632, 108384, 36912, 104192, 104384, 108400, 36896,
  105072, 105136, 108416, 37072, 105824, 105888, 108432, 37056, 106576, 106640, 108448, 37104, 107328, 107392, 108464, 37088,
  108080, 108272, 108480, 37008, 108832, 109024, 108496, 36992, 109584, 109776, 108512, 37040, 110336, 110528, 108528, 37024,
  23312, 32800, 6208, 35520, 23296, 32816, 6224, 35536, 23344, 32768, 6240, 35552, 23328, 32784, 6256, 35568,
  23376, 32864, 6144, 35456, 23360, 32880, 6160, 35472, 23408, 32832, 6176, 35488, 23392, 32848, 6192, 35504,
  23440, 32928, 6336, 35392, 23424, 32944, 6352, 35408, 23472, 32896, 6368, 35424, 23456, 32912, 6384, 35440,
  23504, 32992, 6272, 35328, 23488, 33008, 6288, 35344, 23536, 32960, 6304, 35360, 23520, 32976, 6320, 35376,
  23568, 33840, 6976, 36288, 23552, 33824, 6992, 36304, 23600, 33808, 7008, 36320, 23584, 33792, 7024, 36336,
  23632, 33904, 6912, 36224, 23616, 33888, 6928, 36240, 23664, 33872, 6944, 36256, 23648, 33856, 6960, 36272,
  23696, 33968, 7104, 36160, 23680, 33952, 7120, 36176, 23728, 33936, 7136, 36192, 23712, 33920, 7152, 36208,
  23760, 34032, 7040, 36096, 23744, 34016, 7056, 36112, 23792, 34000, 7072, 36128, 23776, 33984, 7088, 36144,
  32288, 34096, 16976, 37072, 32304, 34080, 16960, 37056, 32256, 34064, 17008, 37104, 32272, 34048, 16992, 37088,
  32352, 34160, 16912, 37008, 32368, 34144, 16896, 36992, 32320, 34128, 16944, 37040, 32336, 34112, 16928, 37024,
  32416, 34224, 17104, 36944, 32432, 34208, 17088, 36928, 32384, 34192, 17136, 36976, 32400, 34176, 17120, 36960,
  32480, 34288, 17040, 36880, 32496, 34272, 17024, 36864, 32448, 34256, 17072, 36912, 32464, 34240, 17056, 36896,
  32544, 34352, 17744, 37840, 32560, 34336, 17728, 37824, 32512, 34320, 17776, 37872, 32528, 34304, 17760, 37856,
  32608, 34416, 17680, 37776, 32624, 34400, 17664, 37760, 32576, 34384, 17712, 37808, 32592, 34368, 17696, 37792,
  32672, 34480, 17872, 37712, 32688, 34464, 17856, 37696, 32640, 34448, 17904, 37744, 32656, 34432, 17888, 37728,
  32736, 34544, 17808, 37648, 32752, 34528, 17792, 37632, 32704, 34512, 17840, 37680, 32720, 34496, 17824, 37664,
  37200, 39008, 18528, 38624, 37184, 39024, 18544, 38640, 37232, 38976, 18496, 38592, 37216, 38992, 18512, 38608,
  37136, 38944, 18464, 38560, 37120, 38960, 18480, 38576, 37168, 38912, 18432, 38528, 37152, 38928, 18448, 38544,
  37328, 39136, 18656, 38496, 37312, 39152, 18672, 38512, 37360, 39104, 18624, 38464, 37344, 39120, 18640, 38480,
  37264, 39072, 18592, 38432, 37248, 39088, 18608, 38448, 37296, 39040, 18560, 38400, 37280, 39056, 18576, 38416,
  37456, 40048, 19296, 39392, 37440, 40032, 19312, 39408, 37488, 40016, 19264, 39360, 37472, 40000, 19280, 39376,
  37392, 39984, 19232, 39328, 37376, 39968, 19248, 39344, 37424, 39952, 19200, 39296, 37408, 39936, 19216, 39312,
  37584, 40176, 19424, 39264, 37568, 40160, 19440, 39280, 37616, 40144, 19392, 39232, 37600, 40128, 19408, 39248,
  37520, 40112, 19360, 39200, 37504, 40096, 19376, 39216, 37552, 40080, 19328, 39168, 37536, 40064, 19344, 39184,
  38496, 40304, 20080, 40176, 38512, 40288, 20064, 40160, 38464, 40272, 20048, 40144, 38480, 40256, 20032, 40128,
  38432, 40240, 20016, 40112, 38448, 40224, 20000, 40096, 38400, 40208, 19984, 40080, 38416, 40192, 19968, 40064,
  38624, 40432, 20208, 40048, 38640, 40416, 20192, 40032, 38592, 40400, 20176, 40016, 38608, 40384, 20160, 40000,
  38560, 40368, 20144, 39984, 38576, 40352, 20128, 39968, 38528, 40336, 20112, 39952, 38544, 40320, 20096, 39936,
  38752, 40560, 20848, 40944, 38768, 40544, 20832, 40928, 38720, 40528, 20816, 40912, 38736, 40512, 20800, 40896,
  38688, 40496, 20784, 40880, 38704, 40480, 20768, 40864, 38656, 40464, 20752, 40848, 38672, 40448, 20736, 40832,
  38880, 40688, 20976, 40816, 38896, 40672, 20960, 40800, 38848, 40656, 20944, 40784, 38864, 40640, 20928, 40768,
  38816, 40624, 20912, 40752, 38832, 40608, 20896, 40736, 38784, 40592, 20880, 40720, 38800, 40576, 20864, 40704};

__global__ __launch_bounds__(256, 1) void fpn_kernel(const char* __restrict__ cf, const char* __restrict__ pc,
                                                     const char* __restrict__ w1, const char* __restrict__ w2,
                                                     char* __restrict__ ws, char* __restrict__ out) {
  unsigned lane = threadIdx.x & 63;
  unsigned wid = __builtin_amdgcn_readfirstlane(threadIdx.x >> 6);
  unsigned b = blockIdx.x;
  unsigned long long t64;
  asm volatile("s_memrealtime %0" : "={s[88:89]}"(t64), "+s"(b), "+s"(wid));
  const unsigned t_start = (unsigned)t64;
  const unsigned pr = (b >> 3) * 4 + (b & 3);
  unsigned xo[9], pg[9];
#pragma unroll
  for (int j = 0; j < 9; ++j) {
    xo[j] = ((3 * pr) >> 1) * 589824u + (pr & 1) * (147456u + (j >= 3 && j < 6 ? 294912u : 0)) + j * 49152u + (j >= 6 ? 294912u : 0);
    pg[j] = xo[j] >> 21;
  }
  int dj[9];
#pragma unroll
  for (int j = 1; j < 9; ++j) dj[j] = (int)(pg[j] - pg[j - 1]) * (j <= 4 ? -12288 : 12288);
  const int d0 = (int)(pg[4] - pg[0]) * 12288;
  dj[0] = d0 - (int)(pg[8] - pg[4]) * 12288;
  const unsigned scr = 159744 + wid * 4;
  const int sgn = (4 * wid + (lane >> 4)) < 10 ? 1 : -1;
  const unsigned mP = 73728 + wid * 1024;
  unsigned pvo = __umul24(col0(4 * wid + (lane >> 4)), 768) + ((((lane & 15) ^ (lane >> 4) ^ (4 * wid)) & 15) << 4)
                 + pr * ((0 & 1) ? 0 : 9 * 24576);
  int sg1;
  u32x2 cd, cr, po;
  po[0] = xo[4] + lane * 1024 + (b & 4) * 128;
  po[1] = po[0] + 8192;
  const unsigned mJ = 110592 + wid * 6144;
  const unsigned var = (b + 112) >> 8;
  u32x4 ja0, ja1, ja2, ja3, ja4, ja5;
  u32x2 ep;
  unsigned two = min((((b >> 3) - 18) * 4 + wid) * 42 + min(lane, 41u), 2303u) * 128;
  unsigned tcode = ((((b >> 3) - 18) * 4 + wid) * 11 + min(lane, 10u)) * 128;
  const unsigned jb = b < 144 ? b : 0;
  const unsigned G32 = (jb * 171) >> 11, jcb = jb - 12 * G32;
  {
    const unsigned jw = __umul24(lane >> 2, 768) + (lane & 3) * 16 + 64 * G32 + wid * (96 * 768);
    const unsigned ja = __umul24(8 * ((lane & 15) >> 2) + (lane & 3), 768) + 16 * (lane >> 4) + jcb * (32 * 768) + wid * 192;
    const unsigned cla = 1048576 + pr * 128;
    asm volatile(
      "s_nop 4\n"
      "s_cmp_lg_u32 s76, 0\n"
      "s_cbranch_scc1 LQ0\n"
      "s_mul_i32 s62, s88, 0x1080\n"
      "s_and_b32 s62, s62, 0x4180\n"
      "s_add_u32 s56, s30, s62\n"
      "s_addc_u32 s57, s31, 0\n"
      "s_cmp_lg_u32 s76, 0\n"
      "s_mov_b64 exec, 0x1\n"
      "global_load_dword v116, v118, s[56:57] nt\n"
      "global_load_dword v117, v119, s[56:57] nt\n"
      "s_mov_b64 exec, -1\n"
      "LQ0:\n"
      "v_mov_b32 v0, 0\n"
      "s_barrier\n"
      "global_load_dwordx2 v[252:253], v0, s[28:29] sc1\n"
      "s_cbranch_scc1 LQ1\n"
      ".p2align 5\n"
      "s_waitcnt vmcnt(2)\n"
      "s_memtime s[60:61]\n"
      "s_waitcnt vmcnt(1)\n"
      "s_memtime s[62:63]\n"
      "s_waitcnt vmcnt(0) lgkmcnt(0)\n"
      "s_sub_u32 s62, s62, s60\n"
      "s_bfe_u32 s63, s51, 0x10002\n"
      "s_cmp_lt_u32 s62, 30\n"
      "s_cselect_b32 s62, 1, 0\n"
      "v_mov_b32 v116, s62\n"
      "ds_write_b32 v108, v116 offset:16\n"
      "s_xor_b32 s62, s62, s63\n"
      "v_add_co_u32 v110, vcc, 0x7f4a7c15, v252\n"
      "v_mov_b32 v111, 0x9e3779b9\n"
      "v_addc_co_u32 v111, vcc, v111, v253, vcc\n"
      "v_and_or_b32 v110, v110, -2, s62\n"
      "s_mov_b64 exec, 1\n"
      "global_atomic_swap_x2 v[112:113], v114, v[110:111], s[28:29] sc0 sc1\n"
      "s_mov_b64 exec, -1\n"
      "LQ1:\n"
      "s_cmp_lg_u32 s50, 0\n"
      "s_cbranch_scc0 LU1\n"
      "s_getpc_b64 s[56:57]\n"
      "LC0_1:\n"
      "v_min_u32 v106, LCEND-LC0_1-4, v106\n"
      "global_load_dword v106, v106, s[56:57]\n"
      "global_load_dword v105, v105, s[26:27]\n"
      "v_and_b32 v117, -16, v108\n"
      "s_waitcnt lgkmcnt(0)\n"
      "s_barrier\n"
      "ds_read_b32 v116, v117 offset:16\n"
      "s_waitcnt lgkmcnt(0)\n"
      "v_readfirstlane_b32 s62, v116\n"
      "s_nop 3\n"
      "s_mul_i32 s63, s62, 0x3000\n"
      "s_sub_u32 s82, s98, s63\n"
      "s_lshl_b32 s62, s62, 1\n"
      "s_sub_u32 s62, 1, s62\n"
      "v_mul_i32_i24 v107, s62, v251\n"
      "v_mad_i32_i24 v238, v107, s82, v238\n"
      "s_add_u32 s56, s22, 0x0\n"
      "s_addc_u32 s57, s23, 0\n"
      "s_add_u32 m0, s18, 0x0\n"
      "s_nop 0\n"
      "global_load_lds_dwordx4 v238, s[56:57] offset:0 nt\n"
      "v_mad_i32_i24 v238, v107, s90, v238\n"
      "s_add_u32 s56, s22, 0x6000\n"
      "s_addc_u32 s57, s23, 0\n"
      "s_add_u32 m0, s18, 0x1000\n"
      "s_nop 0\n"
      "global_load_lds_dwordx4 v238, s[56:57] offset:0 nt\n"
      "v_mad_i32_i24 v238, v107, s91, v238\n"
      "s_add_u32 s56, s22, 0xc000\n"
      "s_addc_u32 s57, s23, 0\n"
      "s_add_u32 m0, s18, 0x2000\n"
      "s_nop 0\n"
      "global_load_lds_dwordx4 v238, s[56:57] offset:0 nt\n"
      "v_mad_i32_i24 v238, v107, s92, v238\n"
      "s_add_u32 s56, s22, 0x12000\n"
      "s_addc_u32 s57, s23, 0\n"
      "s_add_u32 m0, s18, 0x3000\n"
      "s_nop 0\n"
      "global_load_lds_dwordx4 v238, s[56:57] offset:0 nt\n"
      "v_mad_i32_i24 v238, v107, s93, v238\n"
      "s_add_u32 s56, s22, 0x18000\n"
      "s_addc_u32 s57, s23, 0\n"
      "s_add_u32 m0, s18, 0x4000\n"
      "s_nop 0\n"
      "global_load_lds_dwordx4 v238, s[56:57] offset:0 nt\n"
      "v_mad_i32_i24 v238, v107, s94, v238\n"
      "s_add_u32 s56, s22, 0x1e000\n"
      "s_addc_u32 s57, s23, 0\n"
      "s_add_u32 m0, s18, 0x5000\n"
      "s_nop 0\n"
      "global_load_lds_dwordx4 v238, s[56:57] offset:0 nt\n"
      "v_mad_i32_i24 v238, v107, s95, v238\n"
      "s_add_u32 s56, s22, 0x24000\n"
      "s_addc_u32 s57, s23, 0\n"
      "s_add_u32 m0, s18, 0x6000\n"
      "s_nop 0\n"
      "global_load_lds_dwordx4 v238, s[56:57] offset:0 nt\n"
      "v_mad_i32_i24 v238, v107, s96, v238\n"
      "s_add_u32 s56, s22, 0x2a000\n"
      "s_addc_u32 s57, s23, 0\n"
      "s_add_u32 m0, s18, 0x7000\n"
      "s_nop 0\n"
      "global_load_lds_dwordx4 v238, s[56:57] offset:0 nt\n"
      "v_mad_i32_i24 v238, v107, s97, v238\n"
      "s_add_u32 s56, s22, 0x30000\n"
      "s_addc_u32 s57, s23, 0\n"
      "s_add_u32 m0, s18, 0x8000\n"
      "s_nop 0\n"
      "global_load_lds_dwordx4 v238, s[56:57] offset:0 nt\n"
      "s_branch LUE\n"
      "LU1:\n"
      "s_add_u32 s56, s26, 0x0\n"
      "s_addc_u32 s57, s27, 0\n"
      "s_add_u32 m0, s75, 0x0\n"
      "s_nop 0\n"
      "global_load_lds_dwordx4 v100, s[56:57]\n"
      "s_add_u32 s56, s26, 0x3000\n"
      "s_addc_u32 s57, s27, 0\n"
      "s_add_u32 m0, s75, 0x400\n"
      "s_nop 0\n"
      "global_load_lds_dwordx4 v100, s[56:57]\n"
      "s_add_u32 s56, s26, 0x6000\n"
      "s_addc_u32 s57, s27, 0\n"
      "s_add_u32 m0, s75, 0x800\n"
      "s_nop 0\n"
      "global_load_lds_dwordx4 v100, s[56:57]\n"
      "s_add_u32 s56, s26, 0x9000\n"
      "s_addc_u32 s57, s27, 0\n"
      "s_add_u32 m0, s75, 0xc00\n"
      "s_nop 0\n"
      "global_load_lds_dwordx4 v100, s[56:57]\n"
      "s_add_u32 s56, s26, 0xc000\n"
      "s_addc_u32 s57, s27, 0\n"
      "s_add_u32 m0, s75, 0x1000\n"
      "s_nop 0\n"
      "global_load_lds_dwordx4 v100, s[56:57]\n"
      "s_add_u32 s56, s26, 0xf000\n"
      "s_addc_u32 s57, s27, 0\n"
      "s_add_u32 m0, s75, 0x1400\n"
      "s_nop 0\n"
      "global_load_lds_dwordx4 v100, s[56:57]\n"
      "global_load_dwordx4 v[4:7], v101, s[24:25] offset:0\n"
      "global_load_dwordx4 v[8:11], v101, s[24:25] offset:3072\n"
      "global_load_dwordx4 v[12:15], v101, s[24:25] offset:64\n"
      "global_load_dwordx4 v[16:19], v101, s[24:25] offset:3136\n"
      "global_load_dwordx4 v[20:23], v101, s[24:25] offset:128\n"
      "global_load_dwordx4 v[24:27], v101, s[24:25] offset:3200\n"
      "LUE:\n"

      : "={v[4:7]}"(ja0), "={v[8:11]}"(ja1), "={v[12:15]}"(ja2), "={v[16:19]}"(ja3), "={v[20:23]}"(ja4), "={v[24:27]}"(ja5),
        "={v[252:253]}"(ep), "+{v105}"(two), "+{v106}"(tcode), "={v[110:111]}"(cd), "={v[112:113]}"(cr), "+{v[118:119]}"(po),
        "+{v238}"(pvo), "={v107}"(sg1)
      : "{v114}"(cla), "{s[88:89]}"(t64), "{v100}"(jw), "{v101}"(ja), "{s[20:21]}"(cf), "{s[24:25]}"(w1), "{s[26:27]}"(w2), "{s[28:29]}"(ws), "{s[30:31]}"(out),
        "{s75}"(mJ), "{s51}"(b), "{s50}"(var), "{s76}"(wid), "{s[22:23]}"(pc), "{s18}"(mP), "{v108}"(scr), "{v251}"(sgn),
        "{s90}"(dj[1]), "{s91}"(dj[2]), "{s92}"(dj[3]), "{s93}"(dj[4]), "{s94}"(dj[5]), "{s95}"(dj[6]), "{s96}"(dj[7]), "{s97}"(dj[8]), "{s98}"(d0)
      : "memory", "scc", "vcc", "v0", "v116", "v117", "s56", "s57", "s60", "s61", "s62", "s63", "s82");
  }
  asm volatile("" : "+v"(lane));
  asm volatile("" : "+s"(wid));
  unsigned wro[3];
#pragma unroll
  for (int i = 0; i < 3; ++i) { const unsigned x = lane + 64 * i, q = __umul24(x, 171) >> 11; wro[i] = __umul24(q, 576) + x * 16; }
  const unsigned mW = wid * 6144;
  const char* w2w = w2 + wid * 192;
  const unsigned lo16 = lane * 16;
  const char* wb = ws + (65536 + wid * 73728);
  const unsigned c = lane & 15, g = lane >> 4;
  const unsigned cB0 = 73728 + c * 256 + ((g ^ c) << 4);
  const unsigned r8 = 8 * g + (c >> 2), c3 = (c & 3) * 16;
  const unsigned tra = mW + __umul24(r8, 192) + c3;
  const unsigned mX = wid * 1024;
  const unsigned fla = 4096 + wid * 288 + min(lo16, 272u);
  const unsigned c256 = c * 256 - 73728;
  const unsigned jfl = (b < 144 ? 4096 : 8192) + 8 * b;
  char* jo = ws + (65536 + (24 * G32 + jcb) * 1024);
  const unsigned jt = mJ + r8 * 64 + c3;
  const unsigned jrr = 135168 + lo16;
  const unsigned jrw = jrr + wid * 4096;
  const unsigned pk = 151552 + mX + lo16;
  const char* tab = (const char*)lane_tab + ((wid >> 1) * 4096 + (wid & 1) * 1024);
  asm volatile(
      "s_nop 4\n"
      "s_cmp_lg_u32 s50, 0\n"
      "s_cbranch_scc1 LV1\n"
      "s_waitcnt vmcnt(12)\n"
      "v_readfirstlane_b32 s80, v252\n"
      "v_readfirstlane_b32 s81, v253\n"
      "s_nop 3\n"
      "s_add_u32 s80, s80, 0x7f4a7c15\n"
      "s_addc_u32 s81, s81, 0x9e3779b9\n"
      "v_mov_b32 v252, s80\n"
      "v_mov_b32 v253, s81\n"
      "v_xor_b32 v116, v112, v110\n"
      "v_xor_b32 v117, v113, v111\n"
      "v_and_or_b32 v116, v116, -2, v117\n"
      "v_cmp_eq_u32 vcc, 0, v116\n"
      "v_cndmask_b32 v116, v110, v112, vcc\n"
      "s_mov_b64 exec, 1\n"
      "ds_write_b32 v108, v116\n"
      "s_mov_b64 exec, -1\n"
      "v_and_b32 v108, -16, v108\n"
      "s_waitcnt lgkmcnt(0)\n"
      "s_barrier\n"
      "ds_read_b32 v116, v108\n"
      "s_waitcnt lgkmcnt(0)\n"
      "v_readfirstlane_b32 s62, v116\n"
      "s_lshr_b32 s63, s51, 2\n"
      "s_nop 2\n"
      "s_xor_b32 s62, s62, s63\n"
      "s_and_b32 s62, s62, 1\n"
      "s_mul_i32 s63, s62, 0x3000\n"
      "s_sub_u32 s98, s98, s63\n"
      "s_lshl_b32 s62, s62, 1\n"
      "s_sub_u32 s62, 1, s62\n"
      "v_mul_i32_i24 v251, s62, v251\n"
      "s_lshl_b32 s19, s62, 1\n"
      "s_mul_i32 s100, s100, s62\n"
      "v_mad_i32_i24 v238, v251, s98, v238\n"
      "s_add_u32 s56, s22, 0x0\n"
      "s_addc_u32 s57, s23, 0\n"
      "s_add_u32 m0, s18, 0x0\n"
      "s_nop 0\n"
      "global_load_lds_dwordx4 v238, s[56:57] offset:0 nt\n"
      "v_mad_i32_i24 v238, v251, s90, v238\n"
      "s_add_u32 s56, s22, 0x6000\n"
      "s_addc_u32 s57, s23, 0\n"
      "s_add_u32 m0, s18, 0x1000\n"
      "s_nop 0\n"
      "global_load_lds_dwordx4 v238, s[56:57] offset:0 nt\n"
      "v_mad_i32_i24 v238, v251, s91, v238\n"
      "s_add_u32 s56, s22, 0xc000\n"
      "s_addc_u32 s57, s23, 0\n"
      "s_add_u32 m0, s18, 0x2000\n"
      "s_nop 0\n"
      "global_load_lds_dwordx4 v238, s[56:57] offset:0 nt\n"
      "v_mad_i32_i24 v238, v251, s92, v238\n"
      "s_add_u32 s56, s22, 0x12000\n"
      "s_addc_u32 s57, s23, 0\n"
      "s_add_u32 m0, s18, 0x3000\n"
      "s_nop 0\n"
      "global_load_lds_dwordx4 v238, s[56:57] offset:0 nt\n"
      "v_mad_i32_i24 v238, v251, s93, v238\n"
      "s_add_u32 s56, s22, 0x18000\n"
      "s_addc_u32 s57, s23, 0\n"
      "s_add_u32 m0, s18, 0x4000\n"
      "s_nop 0\n"
      "global_load_lds_dwordx4 v238, s[56:57] offset:0 nt\n"
      "v_mad_i32_i24 v238, v251, s94, v238\n"
      "s_add_u32 s56, s22, 0x1e000\n"
      "s_addc_u32 s57, s23, 0\n"
      "s_add_u32 m0, s18, 0x5000\n"
      "s_nop 0\n"
      "global_load_lds_dwordx4 v238, s[56:57] offset:0 nt\n"
      "v_mad_i32_i24 v238, v251, s95, v238\n"
      "s_add_u32 s56, s22, 0x24000\n"
      "s_addc_u32 s57, s23, 0\n"
      "s_add_u32 m0, s18, 0x6000\n"
      "s_nop 0\n"
      "global_load_lds_dwordx4 v238, s[56:57] offset:0 nt\n"
      "v_mad_i32_i24 v238, v251, s96, v238\n"
      "s_add_u32 s56, s22, 0x2a000\n"
      "s_addc_u32 s57, s23, 0\n"
      "s_add_u32 m0, s18, 0x7000\n"
      "s_nop 0\n"
      "global_load_lds_dwordx4 v238, s[56:57] offset:0 nt\n"
      "v_mad_i32_i24 v238, v251, s97, v238\n"
      "s_add_u32 s56, s22, 0x30000\n"
      "s_addc_u32 s57, s23, 0\n"
      "s_add_u32 m0, s18, 0x8000\n"
      "s_nop 0\n"
      "global_load_lds_dwordx4 v238, s[56:57] offset:0 nt\n"
      "s_waitcnt vmcnt(9)\n"
      "ds_read_b64_tr_b16 v[28:29], v102 offset:0\n"
      "ds_read_b64_tr_b16 v[30:31], v102 offset:256\n"
      "ds_read_b64_tr_b16 v[32:33], v102 offset:8\n"
      "ds_read_b64_tr_b16 v[34:35], v102 offset:264\n"
      "ds_read_b64_tr_b16 v[36:37], v102 offset:2048\n"
      "ds_read_b64_tr_b16 v[38:39], v102 offset:2304\n"
      "ds_read_b64_tr_b16 v[40:41], v102 offset:2056\n"
      "ds_read_b64_tr_b16 v[42:43], v102 offset:2312\n"
      "ds_read_b64_tr_b16 v[44:45], v102 offset:4096\n"
      "ds_read_b64_tr_b16 v[46:47], v102 offset:4352\n"
      "ds_read_b64_tr_b16 v[48:49], v102 offset:4104\n"
      "ds_read_b64_tr_b16 v[50:51], v102 offset:4360\n"
      "s_waitcnt lgkmcnt(0)\n"
      "v_mfma_f32_16x16x32_bf16 v[52:55], v[4:7], v[28:31], 0\n"
      "v_mfma_f32_16x16x32_bf16 v[56:59], v[8:11], v[28:31], 0\n"
      "v_mfma_f32_16x16x32_bf16 v[60:63], v[4:7], v[32:35], 0\n"
      "v_mfma_f32_16x16x32_bf16 v[64:67], v[8:11], v[32:35], 0\n"
      "v_mfma_f32_16x16x32_bf16 v[52:55], v[12:15], v[36:39], v[52:55]\n"
      "v_mfma_f32_16x16x32_bf16 v[56:59], v[16:19], v[36:39], v[56:59]\n"
      "v_mfma_f32_16x16x32_bf16 v[60:63], v[12:15], v[40:43], v[60:63]\n"
      "v_mfma_f32_16x16x32_bf16 v[64:67], v[16:19], v[40:43], v[64:67]\n"
      "v_mfma_f32_16x16x32_bf16 v[52:55], v[20:23], v[44:47], v[52:55]\n"
      "v_mfma_f32_16x16x32_bf16 v[56:59], v[24:27], v[44:47], v[56:59]\n"
      "v_mfma_f32_16x16x32_bf16 v[60:63], v[20:23], v[48:51], v[60:63]\n"
      "v_mfma_f32_16x16x32_bf16 v[64:67], v[24:27], v[48:51], v[64:67]\n"
      "s_nop 7\n"
      "ds_write_b128 v103, v[52:55] offset:0\n"
      "ds_write_b128 v103, v[56:59] offset:1024\n"
      "ds_write_b128 v103, v[60:63] offset:2048\n"
      "ds_write_b128 v103, v[64:67] offset:3072\n"
      "s_waitcnt lgkmcnt(0)\n"
      "s_barrier\n"
      "s_cmp_lg_u32 s76, 1\n"
      "s_cbranch_scc1 LJ1\n"
      "ds_read_b128 v[4:7], v104 offset:0\n"
      "ds_read_b128 v[8:11], v104 offset:1024\n"
      "ds_read_b128 v[12:15], v104 offset:2048\n"
      "ds_read_b128 v[16:19], v104 offset:3072\n"
      "ds_read_b128 v[20:23], v104 offset:4096\n"
      "ds_read_b128 v[24:27], v104 offset:5120\n"
      "ds_read_b128 v[28:31], v104 offset:6144\n"
      "ds_read_b128 v[32:35], v104 offset:7168\n"
      "ds_read_b128 v[36:39], v104 offset:8192\n"
      "ds_read_b128 v[40:43], v104 offset:9216\n"
      "ds_read_b128 v[44:47], v104 offset:10240\n"
      "ds_read_b128 v[48:51], v104 offset:11264\n"
      "ds_read_b128 v[52:55], v104 offset:12288\n"
      "ds_read_b128 v[56:59], v104 offset:13312\n"
      "ds_read_b128 v[60:63], v104 offset:14336\n"
      "ds_read_b128 v[64:67], v104 offset:15360\n"
      "s_waitcnt lgkmcnt(8)\n"
      "v_add_f32 v4, v4, v20\n"
      "v_add_f32 v5, v5, v21\n"
      "v_add_f32 v6, v6, v22\n"
      "v_add_f32 v7, v7, v23\n"
      "v_add_f32 v8, v8, v24\n"
      "v_add_f32 v9, v9, v25\n"
      "v_add_f32 v10, v10, v26\n"
      "v_add_f32 v11, v11, v27\n"
      "v_add_f32 v12, v12, v28\n"
      "v_add_f32 v13, v13, v29\n"
      "v_add_f32 v14, v14, v30\n"
      "v_add_f32 v15, v15, v31\n"
      "v_add_f32 v16, v16, v32\n"
      "v_add_f32 v17, v17, v33\n"
      "v_add_f32 v18, v18, v34\n"
      "v_add_f32 v19, v19, v35\n"
      "s_waitcnt lgkmcnt(4)\n"
      "v_add_f32 v4, v4, v36\n"
      "v_add_f32 v5, v5, v37\n"
      "v_add_f32 v6, v6, v38\n"
      "v_add_f32 v7, v7, v39\n"
      "v_add_f32 v8, v8, v40\n"
      "v_add_f32 v9, v9, v41\n"
      "v_add_f32 v10, v10, v42\n"
      "v_add_f32 v11, v11, v43\n"
      "v_add_f32 v12, v12, v44\n"
      "v_add_f32 v13, v13, v45\n"
      "v_add_f32 v14, v14, v46\n"
      "v_add_f32 v15, v15, v47\n"
      "v_add_f32 v16, v16, v48\n"
      "v_add_f32 v17, v17, v49\n"
      "v_add_f32 v18, v18, v50\n"
      "v_add_f32 v19, v19, v51\n"
      "s_waitcnt lgkmcnt(0)\n"
      "v_add_f32 v4, v4, v52\n"
      "v_add_f32 v5, v5, v53\n"
      "v_add_f32 v6, v6, v54\n"
      "v_add_f32 v7, v7, v55\n"
      "v_add_f32 v8, v8, v56\n"
      "v_add_f32 v9, v9, v57\n"
      "v_add_f32 v10, v10, v58\n"
      "v_add_f32 v11, v11, v59\n"
      "v_add_f32 v12, v12, v60\n"
      "v_add_f32 v13, v13, v61\n"
      "v_add_f32 v14, v14, v62\n"
      "v_add_f32 v15, v15, v63\n"
      "v_add_f32 v16, v16, v64\n"
      "v_add_f32 v17, v17, v65\n"
      "v_add_f32 v18, v18, v66\n"
      "v_add_f32 v19, v19, v67\n"
      "v_cvt_pk_bf16_f32 v68, v4, v5\n"
      "v_cvt_pk_bf16_f32 v69, v6, v7\n"
      "v_cvt_pk_bf16_f32 v70, v8, v9\n"
      "v_cvt_pk_bf16_f32 v71, v10, v11\n"
      "v_cvt_pk_bf16_f32 v72, v12, v13\n"
      "v_cvt_pk_bf16_f32 v73, v14, v15\n"
      "v_cvt_pk_bf16_f32 v74, v16, v17\n"
      "v_cvt_pk_bf16_f32 v75, v18, v19\n"
      "global_store_dwordx4 v239, v[68:71], s[72:73] sc1\n"
      "s_add_u32 s72, s72, 0x3000\n"
      "s_addc_u32 s73, s73, 0\n"
      "global_store_dwordx4 v239, v[72:75], s[72:73] sc1\n"
      "LJ1:\n"
      "s_add_u32 s78, s64, 0x0\n"
      "s_addc_u32 s79, s65, 0\n"
      "s_add_u32 m0, s74, 0x0\n"
      "s_nop 0\n"
      "global_load_lds_dwordx4 v240, s[78:79]\n"
      "s_add_u32 m0, s74, 0x400\n"
      "s_nop 0\n"
      "global_load_lds_dwordx4 v241, s[78:79]\n"
      "s_add_u32 m0, s74, 0x800\n"
      "s_nop 0\n"
      "global_load_lds_dwordx4 v242, s[78:79]\n"
      "s_add_u32 s78, s64, 0x3000\n"
      "s_addc_u32 s79, s65, 0\n"
      "s_add_u32 m0, s74, 0xc00\n"
      "s_nop 0\n"
      "global_load_lds_dwordx4 v240, s[78:79]\n"
      "s_add_u32 m0, s74, 0x1000\n"
      "s_nop 0\n"
      "global_load_lds_dwordx4 v241, s[78:79]\n"
      "s_add_u32 m0, s74, 0x1400\n"
      "s_nop 0\n"
      "global_load_lds_dwordx4 v242, s[78:79]\n"
      "s_add_u32 s78, s64, 0x6000\n"
      "s_addc_u32 s79, s65, 0\n"
      "s_add_u32 m0, s74, 0x6000\n"
      "s_nop 0\n"
      "global_load_lds_dwordx4 v240, s[78:79]\n"
      "s_add_u32 m0, s74, 0x6400\n"
      "s_nop 0\n"
      "global_load_lds_dwordx4 v241, s[78:79]\n"
      "s_add_u32 m0, s74, 0x6800\n"
      "s_nop 0\n"
      "global_load_lds_dwordx4 v242, s[78:79]\n"
      "s_add_u32 s78, s64, 0x9000\n"
      "s_addc_u32 s79, s65, 0\n"
      "s_add_u32 m0, s74, 0x6c00\n"
      "s_nop 0\n"
      "global_load_lds_dwordx4 v240, s[78:79]\n"
      "s_add_u32 m0, s74, 0x7000\n"
      "s_nop 0\n"
      "global_load_lds_dwordx4 v241, s[78:79]\n"
      "s_add_u32 m0, s74, 0x7400\n"
      "s_nop 0\n"
      "global_load_lds_dwordx4 v242, s[78:79]\n"
      "s_add_u32 s78, s64, 0xc000\n"
      "s_addc_u32 s79, s65, 0\n"
      "s_add_u32 m0, s74, 0xc000\n"
      "s_nop 0\n"
      "global_load_lds_dwordx4 v240, s[78:79]\n"
      "s_add_u32 m0, s74, 0xc400\n"
      "s_nop 0\n"
      "global_load_lds_dwordx4 v241, s[78:79]\n"
      "s_add_u32 m0, s74, 0xc800\n"
      "s_nop 0\n"
      "global_load_lds_dwordx4 v242, s[78:79]\n"
      "s_add_u32 s78, s64, 0xf000\n"
      "s_addc_u32 s79, s65, 0\n"
      "s_add_u32 m0, s74, 0xcc00\n"
      "s_nop 0\n"
      "global_load_lds_dwordx4 v240, s[78:79]\n"
      "s_add_u32 m0, s74, 0xd000\n"
      "s_nop 0\n"
      "global_load_lds_dwordx4 v241, s[78:79]\n"
      "s_add_u32 m0, s74, 0xd400\n"
      "s_nop 0\n"
      "global_load_lds_dwordx4 v242, s[78:79]\n"
      "s_add_u32 m0, s17, 0x25000\n"
      "s_nop 0\n"
      "global_load_lds_dwordx4 v239, s[46:47] offset:0\n"
      "s_add_u32 m0, s17, 0x25800\n"
      "s_nop 0\n"
      "global_load_lds_dwordx4 v239, s[46:47] offset:2048\n"
      "s_waitcnt vmcnt(14)\n"
      "ds_read_b64_tr_b16 a[0:1], v243 offset:0\n"
      "ds_read_b64_tr_b16 a[2:3], v243 offset:768\n"
      "ds_read_b64_tr_b16 a[4:5], v243 offset:8\n"
      "ds_read_b64_tr_b16 a[6:7], v243 offset:776\n"
      "ds_read_b64_tr_b16 a[8:9], v243 offset:64\n"
      "ds_read_b64_tr_b16 a[10:11], v243 offset:832\n"
      "ds_read_b64_tr_b16 a[12:13], v243 offset:72\n"
      "ds_read_b64_tr_b16 a[14:15], v243 offset:840\n"
      "ds_read_b64_tr_b16 a[16:17], v243 offset:128\n"
      "ds_read_b64_tr_b16 a[18:19], v243 offset:896\n"
      "ds_read_b64_tr_b16 a[20:21], v243 offset:136\n"
      "ds_read_b64_tr_b16 a[22:23], v243 offset:904\n"
      "s_barrier\n"
      "s_cmp_lg_u32 s76, 1\n"
      "s_cbranch_scc1 LA_0\n"
      "v_mov_b32 v244, s77\n"
      "s_mov_b64 exec, 1\n"
      "global_store_dwordx2 v244, v[252:253], s[28:29] sc1\n"
      "s_mov_b64 exec, -1\n"
      "s_nop 4\n"
      "LA_0:\n"
      "ds_read_b128 v[216:219], v232 offset:0\n"
      "ds_read_b128 v[220:223], v232 offset:4096\n"
      "ds_read_b128 v[224:227], v232 offset:8192\n"
      "s_waitcnt lgkmcnt(2)\n"
      "v_mfma_f32_16x16x32_bf16 v[0:3], a[0:3], v[216:219], 0\n"
      "s_add_u32 s78, s64, 0x12000\n"
      "s_addc_u32 s79, s65, 0\n"
      "s_add_u32 m0, s74, 0x0\n"
      "v_mfma_f32_16x16x32_bf16 v[4:7], a[4:7], v[216:219], 0\n"
      "global_load_lds_dwordx4 v240, s[78:79]\n"
      "v_mfma_f32_16x16x32_bf16 v[8:11], a[8:11], v[216:219], 0\n"
      "ds_read_b128 v[228:231], v232 offset:12288\n"
      "v_mfma_f32_16x16x32_bf16 v[12:15], a[12:15], v[216:219], 0\n"
      "v_mfma_f32_16x16x32_bf16 v[16:19], a[16:19], v[216:219], 0\n"
      "s_add_u32 m0, s74, 0x400\n"
      "v_mfma_f32_16x16x32_bf16 v[20:23], a[20:23], v[216:219], 0\n"
      "global_load_lds_dwordx4 v241, s[78:79]\n"
      "s_waitcnt lgkmcnt(2)\n"
      "v_mfma_f32_16x16x32_bf16 v[24:27], a[0:3], v[220:223], 0\n"
      "s_waitcnt vmcnt(10)\n"
      "v_mfma_f32_16x16x32_bf16 v[28:31], a[4:7], v[220:223], 0\n"
      "ds_read_b64_tr_b16 a[24:25], v243 offset:24576\n"
      "v_mfma_f32_16x16x32_bf16 v[32:35], a[8:11], v[220:223], 0\n"
      "ds_read_b128 v[216:219], v232 offset:16384\n"
      "s_add_u32 m0, s74, 0x800\n"
      "v_mfma_f32_16x16x32_bf16 v[36:39], a[12:15], v[220:223], 0\n"
      "ds_read_b64_tr_b16 a[26:27], v243 offset:25344\n"
      "global_load_lds_dwordx4 v242, s[78:79]\n"
      "v_mfma_f32_16x16x32_bf16 v[40:43], a[16:19], v[220:223], 0\n"
      "ds_read_b64_tr_b16 a[28:29], v243 offset:24584\n"
      "v_mfma_f32_16x16x32_bf16 v[44:47], a[20:23], v[220:223], 0\n"
      "s_waitcnt lgkmcnt(5)\n"
      "v_mfma_f32_16x16x32_bf16 v[48:51], a[0:3], v[224:227], 0\n"
      "ds_read_b64_tr_b16 a[30:31], v243 offset:25352\n"
      "s_add_u32 s78, s64, 0x15000\n"
      "s_addc_u32 s79, s65, 0\n"
      "s_add_u32 m0, s74, 0xc00\n"
      "v_mfma_f32_16x16x32_bf16 v[52:55], a[4:7], v[224:227], 0\n"
      "ds_read_b64_tr_b16 a[32:33], v243 offset:24640\n"
      "global_load_lds_dwordx4 v240, s[78:79]\n"
      "v_mfma_f32_16x16x32_bf16 v[56:59], a[8:11], v[224:227], 0\n"
      "ds_read_b128 v[220:223], v232 offset:20480\n"
      "v_mfma_f32_16x16x32_bf16 v[60:63], a[12:15], v[224:227], 0\n"
      "ds_read_b64_tr_b16 a[34:35], v243 offset:25408\n"
      "v_mfma_f32_16x16x32_bf16 v[64:67], a[16:19], v[224:227], 0\n"
      "ds_read_b64_tr_b16 a[36:37], v243 offset:24648\n"
      "s_add_u32 m0, s74, 0x1000\n"
      "v_mfma_f32_16x16x32_bf16 v[68:71], a[20:23], v[224:227], 0\n"
      "global_load_lds_dwordx4 v241, s[78:79]\n"
      "s_waitcnt lgkmcnt(9)\n"
      "v_mfma_f32_16x16x32_bf16 v[72:75], a[0:3], v[228:231], 0\n"
      "ds_read_b64_tr_b16 a[38:39], v243 offset:25416\n"
      "v_mfma_f32_16x16x32_bf16 v[76:79], a[4:7], v[228:231], 0\n"
      "ds_read_b64_tr_b16 a[40:41], v243 offset:24704\n"
      "v_mfma_f32_16x16x32_bf16 v[80:83], a[8:11], v[228:231], 0\n"
      "ds_read_b128 v[224:227], v232 offset:24576\n"
      "s_add_u32 m0, s74, 0x1400\n"
      "v_mfma_f32_16x16x32_bf16 v[84:87], a[12:15], v[228:231], 0\n"
      "ds_read_b64_tr_b16 a[42:43], v243 offset:25472\n"
      "global_load_lds_dwordx4 v242, s[78:79]\n"
      "v_mfma_f32_16x16x32_bf16 v[88:91], a[16:19], v[228:231], 0\n"
      "ds_read_b64_tr_b16 a[44:45], v243 offset:24712\n"
      "v_mfma_f32_16x16x32_bf16 v[92:95], a[20:23], v[228:231], 0\n"
      "s_waitcnt lgkmcnt(12)\n"
      "v_mfma_f32_16x16x32_bf16 v[96:99], a[0:3], v[216:219], 0\n"
      "ds_read_b64_tr_b16 a[46:47], v243 offset:25480\n"
      "v_mfma_f32_16x16x32_bf16 v[100:103], a[4:7], v[216:219], 0\n"
      "s_waitcnt vmcnt(8)\n"
      "v_mfma_f32_16x16x32_bf16 v[104:107], a[8:11], v[216:219], 0\n"
      "ds_read_b128 v[228:231], v232 offset:28672\n"
      "v_mfma_f32_16x16x32_bf16 v[108:111], a[12:15], v[216:219], 0\n"
      "ds_read_b64_tr_b16 a[48:49], v243 offset:49152\n"
      "v_mfma_f32_16x16x32_bf16 v[112:115], a[16:19], v[216:219], 0\n"
      "ds_read_b64_tr_b16 a[50:51], v243 offset:49920\n"
      "v_mfma_f32_16x16x32_bf16 v[116:119], a[20:23], v[216:219], 0\n"
      "s_waitcnt lgkmcnt(11)\n"
      "v_mfma_f32_16x16x32_bf16 v[120:123], a[0:3], v[220:223], 0\n"
      "ds_read_b64_tr_b16 a[52:53], v243 offset:49160\n"
      "v_mfma_f32_16x16x32_bf16 v[124:127], a[4:7], v[220:223], 0\n"
      "ds_read_b64_tr_b16 a[54:55], v243 offset:49928\n"
      "v_mfma_f32_16x16x32_bf16 v[128:131], a[8:11], v[220:223], 0\n"
      "ds_read_b128 v[216:219], v232 offset:32768\n"
      "v_add_u32 v232, 0x9000, v232\n"
      "v_mfma_f32_16x16x32_bf16 v[132:135], a[12:15], v[220:223], 0\n"
      "ds_read_b64_tr_b16 a[56:57], v243 offset:49216\n"
      "v_mfma_f32_16x16x32_bf16 v[136:139], a[16:19], v[220:223], 0\n"
      "ds_read_b64_tr_b16 a[58:59], v243 offset:49984\n"
      "s_waitcnt lgkmcnt(8)\n"
      "s_add_u32 s78, s64, 0x18000\n"
      "s_addc_u32 s79, s65, 0\n"
      "s_add_u32 m0, s74, 0x6000\n"
      "v_mfma_f32_16x16x32_bf16 v[140:143], a[20:23], v[220:223], 0\n"
      "global_load_lds_dwordx4 v240, s[78:79]\n"
      "v_mfma_f32_16x16x32_bf16 v[144:147], a[0:3], v[224:227], 0\n"
      "ds_read_b64_tr_b16 a[60:61], v243 offset:49224\n"
      "v_mfma_f32_16x16x32_bf16 v[148:151], a[4:7], v[224:227], 0\n"
      "ds_read_b64_tr_b16 a[62:63], v243 offset:49992\n"
      "v_mfma_f32_16x16x32_bf16 v[152:155], a[8:11], v[224:227], 0\n"
      "ds_read_b128 v[220:223], v233 offset:0\n"
      "s_add_u32 m0, s74, 0x6400\n"
      "v_mfma_f32_16x16x32_bf16 v[156:159], a[12:15], v[224:227], 0\n"
      "ds_read_b64_tr_b16 a[64:65], v243 offset:49280\n"
      "global_load_lds_dwordx4 v241, s[78:79]\n"
      "v_mfma_f32_16x16x32_bf16 v[160:163], a[16:19], v[224:227], 0\n"
      "ds_read_b64_tr_b16 a[66:67], v243 offset:50048\n"
      "v_mfma_f32_16x16x32_bf16 v[164:167], a[20:23], v[224:227], 0\n"
      "s_waitcnt lgkmcnt(12)\n"
      "v_mfma_f32_16x16x32_bf16 v[168:171], a[0:3], v[228:231], 0\n"
      "ds_read_b64_tr_b16 a[68:69], v243 offset:49288\n"
      "s_add_u32 m0, s74, 0x6800\n"
      "v_mfma_f32_16x16x32_bf16 v[172:175], a[4:7], v[228:231], 0\n"
      "ds_read_b64_tr_b16 a[70:71], v243 offset:50056\n"
      "global_load_lds_dwordx4 v242, s[78:79]\n"
      "v_mfma_f32_16x16x32_bf16 v[176:179], a[8:11], v[228:231], 0\n"
      "ds_read_b128 v[224:227], v233 offset:4096\n"
      "v_mfma_f32_16x16x32_bf16 v[180:183], a[12:15], v[228:231], 0\n"
      "v_mfma_f32_16x16x32_bf16 v[184:187], a[16:19], v[228:231], 0\n"
      "s_add_u32 s78, s64, 0x1b000\n"
      "s_addc_u32 s79, s65, 0\n"
      "s_add_u32 m0, s74, 0x6c00\n"
      "v_mfma_f32_16x16x32_bf16 v[188:191], a[20:23], v[228:231], 0\n"
      "global_load_lds_dwordx4 v240, s[78:79]\n"
      "s_waitcnt lgkmcnt(10)\n"
      "v_mfma_f32_16x16x32_bf16 v[192:195], a[0:3], v[216:219], 0\n"
      "v_mfma_f32_16x16x32_bf16 v[196:199], a[4:7], v[216:219], 0\n"
      "v_mfma_f32_16x16x32_bf16 v[200:203], a[8:11], v[216:219], 0\n"
      "ds_read_b128 v[228:231], v233 offset:8192\n"
      "s_add_u32 m0, s74, 0x7000\n"
      "v_mfma_f32_16x16x32_bf16 v[204:207], a[12:15], v[216:219], 0\n"
      "global_load_lds_dwordx4 v241, s[78:79]\n"
      "v_mfma_f32_16x16x32_bf16 v[208:211], a[16:19], v[216:219], 0\n"
      "v_mfma_f32_16x16x32_bf16 v[212:215], a[20:23], v[216:219], 0\n"
      "s_waitcnt lgkmcnt(6)\n"
      "v_mfma_f32_16x16x32_bf16 v[0:3], a[24:27], v[220:223], v[0:3]\n"
      "s_add_u32 m0, s74, 0x7400\n"
      "v_mfma_f32_16x16x32_bf16 v[4:7], a[28:31], v[220:223], v[4:7]\n"
      "global_load_lds_dwordx4 v242, s[78:79]\n"
      "v_mfma_f32_16x16x32_bf16 v[8:11], a[32:35], v[220:223], v[8:11]\n"
      "ds_read_b128 v[216:219], v233 offset:12288\n"
      "s_waitcnt vmcnt(6)\n"
      "v_mfma_f32_16x16x32_bf16 v[12:15], a[36:39], v[220:223], v[12:15]\n"
      "ds_read_b64_tr_b16 a[72:73], v243 offset:0\n"
      "v_mfma_f32_16x16x32_bf16 v[16:19], a[40:43], v[220:223], v[16:19]\n"
      "ds_read_b64_tr_b16 a[74:75], v243 offset:768\n"
      "v_mad_i32_i24 v238, v251, s89, v238\n"
      "s_add_u32 s56, s22, 0x0\n"
      "s_addc_u32 s57, s23, 0\n"
      "s_add_u32 m0, s18, 0x8f00\n"
      "v_mfma_f32_16x16x32_bf16 v[20:23], a[44:47], v[220:223], v[20:23]\n"
      "global_load_lds_dwordx4 v238, s[56:57] offset:256 nt\n"
      "s_waitcnt lgkmcnt(4)\n"
      "v_mfma_f32_16x16x32_bf16 v[24:27], a[24:27], v[224:227], v[24:27]\n"
      "ds_read_b64_tr_b16 a[76:77], v243 offset:8\n"
      "v_mfma_f32_16x16x32_bf16 v[28:31], a[28:31], v[224:227], v[28:31]\n"
      "ds_read_b64_tr_b16 a[78:79], v243 offset:776\n"
      "v_mad_i32_i24 v238, v251, s90, v238\n"
      "s_add_u32 s56, s22, 0x6000\n"
      "s_addc_u32 s57, s23, 0\n"
      "s_add_u32 m0, s18, 0x9f00\n"
      "v_mfma_f32_16x16x32_bf16 v[32:35], a[32:35], v[224:227], v[32:35]\n"
      "ds_read_b128 v[220:223], v233 offset:16384\n"
      "global_load_lds_dwordx4 v238, s[56:57] offset:256 nt\n"
      "v_mfma_f32_16x16x32_bf16 v[36:39], a[36:39], v[224:227], v[36:39]\n"
      "ds_read_b64_tr_b16 a[80:81], v243 offset:64\n"
      "v_mfma_f32_16x16x32_bf16 v[40:43], a[40:43], v[224:227], v[40:43]\n"
      "ds_read_b64_tr_b16 a[82:83], v243 offset:832\n"
      "v_mad_i32_i24 v238, v251, s91, v238\n"
      "s_add_u32 s56, s22, 0xc000\n"
      "s_addc_u32 s57, s23, 0\n"
      "s_add_u32 m0, s18, 0xaf00\n"
      "v_mfma_f32_16x16x32_bf16 v[44:47], a[44:47], v[224:227], v[44:47]\n"
      "global_load_lds_dwordx4 v238, s[56:57] offset:256 nt\n"
      "s_waitcnt lgkmcnt(8)\n"
      "v_mfma_f32_16x16x32_bf16 v[48:51], a[24:27], v[228:231], v[48:51]\n"
      "ds_read_b64_tr_b16 a[84:85], v243 offset:72\n"
      "v_mfma_f32_16x16x32_bf16 v[52:55], a[28:31], v[228:231], v[52:55]\n"
      "ds_read_b64_tr_b16 a[86:87], v243 offset:840\n"
      "v_mad_i32_i24 v238, v251, s92, v238\n"
      "s_add_u32 s56, s22, 0x12000\n"
      "s_addc_u32 s57, s23, 0\n"
      "s_add_u32 m0, s18, 0xbf00\n"
      "v_mfma_f32_16x16x32_bf16 v[56:59], a[32:35], v[228:231], v[56:59]\n"
      "ds_read_b128 v[224:227], v233 offset:20480\n"
      "global_load_lds_dwordx4 v238, s[56:57] offset:256 nt\n"
      "v_mfma_f32_16x16x32_bf16 v[60:63], a[36:39], v[228:231], v[60:63]\n"
      "ds_read_b64_tr_b16 a[88:89], v243 offset:128\n"
      "v_mfma_f32_16x16x32_bf16 v[64:67], a[40:43], v[228:231], v[64:67]\n"
      "ds_read_b64_tr_b16 a[90:91], v243 offset:896\n"
      "v_mad_i32_i24 v238, v251, s93, v238\n"
      "s_add_u32 s56, s22, 0x18000\n"
      "s_addc_u32 s57, s23, 0\n"
      "s_add_u32 m0, s18, 0xcf00\n"
      "v_mfma_f32_16x16x32_bf16 v[68:71], a[44:47], v[228:231], v[68:71]\n"
      "global_load_lds_dwordx4 v238, s[56:57] offset:256 nt\n"
      "s_waitcnt lgkmcnt(12)\n"
      "v_mfma_f32_16x16x32_bf16 v[72:75], a[24:27], v[216:219], v[72:75]\n"
      "ds_read_b64_tr_b16 a[92:93], v243 offset:136\n"
      "v_mfma_f32_16x16x32_bf16 v[76:79], a[28:31], v[216:219], v[76:79]\n"
      "ds_read_b64_tr_b16 a[94:95], v243 offset:904\n"
      "v_mad_i32_i24 v238, v251, s94, v238\n"
      "s_add_u32 s56, s22, 0x1e000\n"
      "s_addc_u32 s57, s23, 0\n"
      "s_add_u32 m0, s18, 0xdf00\n"
      "v_mfma_f32_16x16x32_bf16 v[80:83], a[32:35], v[216:219], v[80:83]\n"
      "ds_read_b128 v[228:231], v233 offset:24576\n"
      "global_load_lds_dwordx4 v238, s[56:57] offset:256 nt\n"
      "v_mfma_f32_16x16x32_bf16 v[84:87], a[36:39], v[216:219], v[84:87]\n"
      "v_mfma_f32_16x16x32_bf16 v[88:91], a[40:43], v[216:219], v[88:91]\n"
      "v_mad_i32_i24 v238, v251, s95, v238\n"
      "s_add_u32 s56, s22, 0x24000\n"
      "s_addc_u32 s57, s23, 0\n"
      "s_add_u32 m0, s18, 0xef00\n"
      "v_mfma_f32_16x16x32_bf16 v[92:95], a[44:47], v[216:219], v[92:95]\n"
      "global_load_lds_dwordx4 v238, s[56:57] offset:256 nt\n"
      "s_waitcnt lgkmcnt(10)\n"
      "v_mfma_f32_16x16x32_bf16 v[96:99], a[24:27], v[220:223], v[96:99]\n"
      "v_mfma_f32_16x16x32_bf16 v[100:103], a[28:31], v[220:223], v[100:103]\n"
      "v_mad_i32_i24 v238, v251, s96, v238\n"
      "s_add_u32 s56, s22, 0x2a000\n"
      "s_addc_u32 s57, s23, 0\n"
      "s_add_u32 m0, s18, 0xff00\n"
      "v_mfma_f32_16x16x32_bf16 v[104:107], a[32:35], v[220:223], v[104:107]\n"
      "ds_read_b128 v[216:219], v233 offset:28672\n"
      "global_load_lds_dwordx4 v238, s[56:57] offset:256 nt\n"
      "v_mfma_f32_16x16x32_bf16 v[108:111], a[36:39], v[220:223], v[108:111]\n"
      "v_mfma_f32_16x16x32_bf16 v[112:115], a[40:43], v[220:223], v[112:115]\n"
      "v_mad_i32_i24 v238, v251, s97, v238\n"
      "s_add_u32 s56, s22, 0x30000\n"
      "s_addc_u32 s57, s23, 0\n"
      "s_add_u32 m0, s18, 0x10f00\n"
      "v_mfma_f32_16x16x32_bf16 v[116:119], a[44:47], v[220:223], v[116:119]\n"
      "global_load_lds_dwordx4 v238, s[56:57] offset:256 nt\n"
      "s_waitcnt lgkmcnt(6)\n"
      "v_mfma_f32_16x16x32_bf16 v[120:123], a[24:27], v[224:227], v[120:123]\n"
      "v_mfma_f32_16x16x32_bf16 v[124:127], a[28:31], v[224:227], v[124:127]\n"
      "v_mfma_f32_16x16x32_bf16 v[128:131], a[32:35], v[224:227], v[128:131]\n"
      "ds_read_b128 v[220:223], v233 offset:32768\n"
      "v_add_u32 v233, 0x9000, v233\n"
      "s_add_u32 s78, s64, 0x1e000\n"
      "s_addc_u32 s79, s65, 0\n"
      "s_add_u32 m0, s74, 0xc000\n"
      "v_mfma_f32_16x16x32_bf16 v[132:135], a[36:39], v[224:227], v[132:135]\n"
      "global_load_lds_dwordx4 v240, s[78:79]\n"
      "v_mfma_f32_16x16x32_bf16 v[136:139], a[40:43], v[224:227], v[136:139]\n"
      "v_mfma_f32_16x16x32_bf16 v[140:143], a[44:47], v[224:227], v[140:143]\n"
      "s_waitcnt lgkmcnt(2)\n"
      "v_mfma_f32_16x16x32_bf16 v[144:147], a[24:27], v[228:231], v[144:147]\n"
      "s_waitcnt vmcnt(10)\n"
      "s_add_u32 m0, s74, 0xc400\n"
      "v_mfma_f32_16x16x32_bf16 v[148:151], a[28:31], v[228:231], v[148:151]\n"
      "ds_read_b64_tr_b16 a[96:97], v243 offset:24576\n"
      "global_load_lds_dwordx4 v241, s[78:79]\n"
      "v_mfma_f32_16x16x32_bf16 v[152:155], a[32:35], v[228:231], v[152:155]\n"
      "ds_read_b128 v[224:227], v234 offset:0\n"
      "v_mfma_f32_16x16x32_bf16 v[156:159], a[36:39], v[228:231], v[156:159]\n"
      "ds_read_b64_tr_b16 a[98:99], v243 offset:25344\n"
      "v_mfma_f32_16x16x32_bf16 v[160:163], a[40:43], v[228:231], v[160:163]\n"
      "ds_read_b64_tr_b16 a[100:101], v243 offset:24584\n"
      "s_add_u32 m0, s74, 0xc800\n"
      "v_mfma_f32_16x16x32_bf16 v[164:167], a[44:47], v[228:231], v[164:167]\n"
      "global_load_lds_dwordx4 v242, s[78:79]\n"
      "s_waitcnt lgkmcnt(5)\n"
      "v_mfma_f32_16x16x32_bf16 v[168:171], a[24:27], v[216:219], v[168:171]\n"
      "ds_read_b64_tr_b16 a[102:103], v243 offset:25352\n"
      "v_mfma_f32_16x16x32_bf16 v[172:175], a[28:31], v[216:219], v[172:175]\n"
      "ds_read_b64_tr_b16 a[104:105], v243 offset:24640\n"
      "v_mfma_f32_16x16x32_bf16 v[176:179], a[32:35], v[216:219], v[176:179]\n"
      "ds_read_b128 v[228:231], v234 offset:4096\n"
      "s_add_u32 s78, s64, 0x21000\n"
      "s_addc_u32 s79, s65, 0\n"
      "s_add_u32 m0, s74, 0xcc00\n"
      "v_mfma_f32_16x16x32_bf16 v[180:183], a[36:39], v[216:219], v[180:183]\n"
      "ds_read_b64_tr_b16 a[106:107], v243 offset:25408\n"
      "global_load_lds_dwordx4 v240, s[78:79]\n"
      "v_mfma_f32_16x16x32_bf16 v[184:187], a[40:43], v[216:219], v[184:187]\n"
      "ds_read_b64_tr_b16 a[108:109], v243 offset:24648\n"
      "v_mfma_f32_16x16x32_bf16 v[188:191], a[44:47], v[216:219], v[188:191]\n"
      "s_waitcnt lgkmcnt(9)\n"
      "v_mfma_f32_16x16x32_bf16 v[192:195], a[24:27], v[220:223], v[192:195]\n"
      "ds_read_b64_tr_b16 a[110:111], v243 offset:25416\n"
      "s_add_u32 m0, s74, 0xd000\n"
      "v_mfma_f32_16x16x32_bf16 v[196:199], a[28:31], v[220:223], v[196:199]\n"
      "ds_read_b64_tr_b16 a[112:113], v243 offset:24704\n"
      "global_load_lds_dwordx4 v241, s[78:79]\n"
      "v_mfma_f32_16x16x32_bf16 v[200:203], a[32:35], v[220:223], v[200:203]\n"
      "ds_read_b128 v[216:219], v234 offset:8192\n"
      "v_mfma_f32_16x16x32_bf16 v[204:207], a[36:39], v[220:223], v[204:207]\n"
      "ds_read_b64_tr_b16 a[114:115], v243 offset:25472\n"
      "v_mfma_f32_16x16x32_bf16 v[208:211], a[40:43], v[220:223], v[208:211]\n"
      "ds_read_b64_tr_b16 a[116:117], v243 offset:24712\n"
      "s_add_u32 m0, s74, 0xd400\n"
      "v_mfma_f32_16x16x32_bf16 v[212:215], a[44:47], v[220:223], v[212:215]\n"
      "global_load_lds_dwordx4 v242, s[78:79]\n"
      "s_waitcnt lgkmcnt(12)\n"
      "v_mfma_f32_16x16x32_bf16 v[0:3], a[48:51], v[224:227], v[0:3]\n"
      "ds_read_b64_tr_b16 a[118:119], v243 offset:25480\n"
      "v_mfma_f32_16x16x32_bf16 v[4:7], a[52:55], v[224:227], v[4:7]\n"
      "v_mfma_f32_16x16x32_bf16 v[8:11], a[56:59], v[224:227], v[8:11]\n"
      "ds_read_b128 v[220:223], v234 offset:12288\n"
      "s_add_u32 s78, s64, 0x24000\n"
      "s_addc_u32 s79, s65, 0\n"
      "s_add_u32 m0, s74, 0x0\n"
      "v_mfma_f32_16x16x32_bf16 v[12:15], a[60:63], v[224:227], v[12:15]\n"
      "global_load_lds_dwordx4 v240, s[78:79]\n"
      "v_mfma_f32_16x16x32_bf16 v[16:19], a[64:67], v[224:227], v[16:19]\n"
      "v_mfma_f32_16x16x32_bf16 v[20:23], a[68:71], v[224:227], v[20:23]\n"
      "s_waitcnt lgkmcnt(9)\n"
      "v_mfma_f32_16x16x32_bf16 v[24:27], a[48:51], v[228:231], v[24:27]\n"
      "s_add_u32 m0, s74, 0x400\n"
      "v_mfma_f32_16x16x32_bf16 v[28:31], a[52:55], v[228:231], v[28:31]\n"
      "global_load_lds_dwordx4 v241, s[78:79]\n"
      "v_mfma_f32_16x16x32_bf16 v[32:35], a[56:59], v[228:231], v[32:35]\n"
      "ds_read_b128 v[224:227], v234 offset:16384\n"
      "v_mfma_f32_16x16x32_bf16 v[36:39], a[60:63], v[228:231], v[36:39]\n"
      "v_mfma_f32_16x16x32_bf16 v[40:43], a[64:67], v[228:231], v[40:43]\n"
      "s_add_u32 m0, s74, 0x800\n"
      "v_mfma_f32_16x16x32_bf16 v[44:47], a[68:71], v[228:231], v[44:47]\n"
      "global_load_lds_dwordx4 v242, s[78:79]\n"
      "s_waitcnt lgkmcnt(5)\n"
      "v_mfma_f32_16x16x32_bf16 v[48:51], a[48:51], v[216:219], v[48:51]\n"
      "v_mfma_f32_16x16x32_bf16 v[52:55], a[52:55], v[216:219], v[52:55]\n"
      "v_mfma_f32_16x16x32_bf16 v[56:59], a[56:59], v[216:219], v[56:59]\n"
      "ds_read_b128 v[228:231], v234 offset:20480\n"
      "s_add_u32 s78, s64, 0x27000\n"
      "s_addc_u32 s79, s65, 0\n"
      "s_add_u32 m0, s74, 0xc00\n"
      "v_mfma_f32_16x16x32_bf16 v[60:63], a[60:63], v[216:219], v[60:63]\n"
      "global_load_lds_dwordx4 v240, s[78:79]\n"
      "v_mfma_f32_16x16x32_bf16 v[64:67], a[64:67], v[216:219], v[64:67]\n"
      "v_mfma_f32_16x16x32_bf16 v[68:71], a[68:71], v[216:219], v[68:71]\n"
      "s_waitcnt lgkmcnt(2)\n"
      "v_mfma_f32_16x16x32_bf16 v[72:75], a[48:51], v[220:223], v[72:75]\n"
      "s_add_u32 m0, s74, 0x1000\n"
      "v_mfma_f32_16x16x32_bf16 v[76:79], a[52:55], v[220:223], v[76:79]\n"
      "global_load_lds_dwordx4 v241, s[78:79]\n"
      "v_mfma_f32_16x16x32_bf16 v[80:83], a[56:59], v[220:223], v[80:83]\n"
      "ds_read_b128 v[216:219], v234 offset:24576\n"
      "v_mfma_f32_16x16x32_bf16 v[84:87], a[60:63], v[220:223], v[84:87]\n"
      "v_mfma_f32_16x16x32_bf16 v[88:91], a[64:67], v[220:223], v[88:91]\n"
      "s_add_u32 m0, s74, 0x1400\n"
      "v_mfma_f32_16x16x32_bf16 v[92:95], a[68:71], v[220:223], v[92:95]\n"
      "global_load_lds_dwordx4 v242, s[78:79]\n"
      "s_waitcnt lgkmcnt(2)\n"
      "v_mfma_f32_16x16x32_bf16 v[96:99], a[48:51], v[224:227], v[96:99]\n"
      "v_mfma_f32_16x16x32_bf16 v[100:103], a[52:55], v[224:227], v[100:103]\n"
      "v_mfma_f32_16x16x32_bf16 v[104:107], a[56:59], v[224:227], v[104:107]\n"
      "ds_read_b128 v[220:223], v234 offset:28672\n"
      "s_add_u32 s78, s64, 0x2a000\n"
      "s_addc_u32 s79, s65, 0\n"
      "s_add_u32 m0, s74, 0x6000\n"
      "v_mfma_f32_16x16x32_bf16 v[108:111], a[60:63], v[224:227], v[108:111]\n"
      "global_load_lds_dwordx4 v240, s[78:79]\n"
      "v_mfma_f32_16x16x32_bf16 v[112:115], a[64:67], v[224:227], v[112:115]\n"
      "v_mfma_f32_16x16x32_bf16 v[116:119], a[68:71], v[224:227], v[116:119]\n"
      "s_waitcnt lgkmcnt(2)\n"
      "v_mfma_f32_16x16x32_bf16 v[120:123], a[48:51], v[228:231], v[120:123]\n"
      "s_add_u32 m0, s74, 0x6400\n"
      "v_mfma_f32_16x16x32_bf16 v[124:127], a[52:55], v[228:231], v[124:127]\n"
      "global_load_lds_dwordx4 v241, s[78:79]\n"
      "v_mfma_f32_16x16x32_bf16 v[128:131], a[56:59], v[228:231], v[128:131]\n"
      "ds_read_b128 v[224:227], v234 offset:32768\n"
      "v_add_u32 v234, 0x9000, v234\n"
      "v_mfma_f32_16x16x32_bf16 v[132:135], a[60:63], v[228:231], v[132:135]\n"
      "v_mfma_f32_16x16x32_bf16 v[136:139], a[64:67], v[228:231], v[136:139]\n"
      "s_waitcnt vmcnt(8)\n"
      "s_add_u32 m0, s74, 0x6800\n"
      "v_mfma_f32_16x16x32_bf16 v[140:143], a[68:71], v[228:231], v[140:143]\n"
      "global_load_lds_dwordx4 v242, s[78:79]\n"
      "s_waitcnt lgkmcnt(2)\n"
      "v_mfma_f32_16x16x32_bf16 v[144:147], a[48:51], v[216:219], v[144:147]\n"
      "ds_read_b64_tr_b16 a[120:121], v243 offset:49152\n"
      "v_mfma_f32_16x16x32_bf16 v[148:151], a[52:55], v[216:219], v[148:151]\n"
      "ds_read_b64_tr_b16 a[122:123], v243 offset:49920\n"
      "v_mfma_f32_16x16x32_bf16 v[152:155], a[56:59], v[216:219], v[152:155]\n"
      "ds_read_b128 v[228:231], v235 offset:0\n"
      "s_add_u32 s78, s64, 0x2d000\n"
      "s_addc_u32 s79, s65, 0\n"
      "s_add_u32 m0, s74, 0x6c00\n"
      "v_mfma_f32_16x16x32_bf16 v[156:159], a[60:63], v[216:219], v[156:159]\n"
      "ds_read_b64_tr_b16 a[124:125], v243 offset:49160\n"
      "global_load_lds_dwordx4 v240, s[78:79]\n"
      "v_mfma_f32_16x16x32_bf16 v[160:163], a[64:67], v[216:219], v[160:163]\n"
      "ds_read_b64_tr_b16 a[126:127], v243 offset:49928\n"
      "v_mfma_f32_16x16x32_bf16 v[164:167], a[68:71], v[216:219], v[164:167]\n"
      "s_waitcnt lgkmcnt(6)\n"
      "v_mfma_f32_16x16x32_bf16 v[168:171], a[48:51], v[220:223], v[168:171]\n"
      "ds_read_b64_tr_b16 a[128:129], v243 offset:49216\n"
      "s_add_u32 m0, s74, 0x7000\n"
      "v_mfma_f32_16x16x32_bf16 v[172:175], a[52:55], v[220:223], v[172:175]\n"
      "ds_read_b64_tr_b16 a[130:131], v243 offset:49984\n"
      "global_load_lds_dwordx4 v241, s[78:79]\n"
      "v_mfma_f32_16x16x32_bf16 v[176:179], a[56:59], v[220:223], v[176:179]\n"
      "ds_read_b128 v[216:219], v235 offset:4096\n"
      "v_mfma_f32_16x16x32_bf16 v[180:183], a[60:63], v[220:223], v[180:183]\n"
      "ds_read_b64_tr_b16 a[132:133], v243 offset:49224\n"
      "v_mfma_f32_16x16x32_bf16 v[184:187], a[64:67], v[220:223], v[184:187]\n"
      "ds_read_b64_tr_b16 a[134:135], v243 offset:49992\n"
      "s_add_u32 m0, s74, 0x7400\n"
      "v_mfma_f32_16x16x32_bf16 v[188:191], a[68:71], v[220:223], v[188:191]\n"
      "global_load_lds_dwordx4 v242, s[78:79]\n"
      "s_waitcnt lgkmcnt(10)\n"
      "v_mfma_f32_16x16x32_bf16 v[192:195], a[48:51], v[224:227], v[192:195]\n"
      "ds_read_b64_tr_b16 a[136:137], v243 offset:49280\n"
      "v_mfma_f32_16x16x32_bf16 v[196:199], a[52:55], v[224:227], v[196:199]\n"
      "ds_read_b64_tr_b16 a[138:139], v243 offset:50048\n"
      "v_mfma_f32_16x16x32_bf16 v[200:203], a[56:59], v[224:227], v[200:203]\n"
      "ds_read_b128 v[220:223], v235 offset:8192\n"
      "global_load_dwordx4 v[244:247], v248, s[28:29] sc1\n"
      "v_mfma_f32_16x16x32_bf16 v[204:207], a[60:63], v[224:227], v[204:207]\n"
      "ds_read_b64_tr_b16 a[140:141], v243 offset:49288\n"
      "v_mfma_f32_16x16x32_bf16 v[208:211], a[64:67], v[224:227], v[208:211]\n"
      "ds_read_b64_tr_b16 a[142:143], v243 offset:50056\n"
      "v_mfma_f32_16x16x32_bf16 v[212:215], a[68:71], v[224:227], v[212:215]\n"
      "s_waitcnt lgkmcnt(12)\n"
      "v_mfma_f32_16x16x32_bf16 v[0:3], a[72:75], v[228:231], v[0:3]\n"
      "v_mfma_f32_16x16x32_bf16 v[4:7], a[76:79], v[228:231], v[4:7]\n"
      "v_mfma_f32_16x16x32_bf16 v[8:11], a[80:83], v[228:231], v[8:11]\n"
      "ds_read_b128 v[224:227], v235 offset:12288\n"
      "v_mfma_f32_16x16x32_bf16 v[12:15], a[84:87], v[228:231], v[12:15]\n"
      "v_mfma_f32_16x16x32_bf16 v[16:19], a[88:91], v[228:231], v[16:19]\n"
      "s_waitcnt vmcnt(7)\n"
      "v_mfma_f32_16x16x32_bf16 v[20:23], a[92:95], v[228:231], v[20:23]\n"
      "s_waitcnt lgkmcnt(8)\n"
      "v_mfma_f32_16x16x32_bf16 v[24:27], a[72:75], v[216:219], v[24:27]\n"
      "ds_read_b64_tr_b16 a[144:145], v243 offset:0\n"
      "v_mfma_f32_16x16x32_bf16 v[28:31], a[76:79], v[216:219], v[28:31]\n"
      "ds_read_b64_tr_b16 a[146:147], v243 offset:768\n"
      "v_mfma_f32_16x16x32_bf16 v[32:35], a[80:83], v[216:219], v[32:35]\n"
      "ds_read_b128 v[228:231], v235 offset:16384\n"
      "s_waitcnt lgkmcnt(4)\n"
      "s_add_u32 s78, s64, 0x30000\n"
      "s_addc_u32 s79, s65, 0\n"
      "s_add_u32 m0, s74, 0xc000\n"
      "v_mfma_f32_16x16x32_bf16 v[36:39], a[84:87], v[216:219], v[36:39]\n"
      "ds_read_b64_tr_b16 a[148:149], v243 offset:8\n"
      "global_load_lds_dwordx4 v240, s[78:79]\n"
      "v_mfma_f32_16x16x32_bf16 v[40:43], a[88:91], v[216:219], v[40:43]\n"
      "ds_read_b64_tr_b16 a[150:151], v243 offset:776\n"
      "v_mfma_f32_16x16x32_bf16 v[44:47], a[92:95], v[216:219], v[44:47]\n"
      "v_mfma_f32_16x16x32_bf16 v[48:51], a[72:75], v[220:223], v[48:51]\n"
      "ds_read_b64_tr_b16 a[152:153], v243 offset:64\n"
      "s_add_u32 m0, s74, 0xc400\n"
      "v_mfma_f32_16x16x32_bf16 v[52:55], a[76:79], v[220:223], v[52:55]\n"
      "ds_read_b64_tr_b16 a[154:155], v243 offset:832\n"
      "global_load_lds_dwordx4 v241, s[78:79]\n"
      "v_mfma_f32_16x16x32_bf16 v[56:59], a[80:83], v[220:223], v[56:59]\n"
      "ds_read_b128 v[216:219], v235 offset:20480\n"
      "v_mfma_f32_16x16x32_bf16 v[60:63], a[84:87], v[220:223], v[60:63]\n"
      "ds_read_b64_tr_b16 a[156:157], v243 offset:72\n"
      "v_mfma_f32_16x16x32_bf16 v[64:67], a[88:91], v[220:223], v[64:67]\n"
      "ds_read_b64_tr_b16 a[158:159], v243 offset:840\n"
      "s_add_u32 m0, s74, 0xc800\n"
      "v_mfma_f32_16x16x32_bf16 v[68:71], a[92:95], v[220:223], v[68:71]\n"
      "global_load_lds_dwordx4 v242, s[78:79]\n"
      "s_waitcnt lgkmcnt(10)\n"
      "v_mfma_f32_16x16x32_bf16 v[72:75], a[72:75], v[224:227], v[72:75]\n"
      "ds_read_b64_tr_b16 a[160:161], v243 offset:128\n"
      "v_mfma_f32_16x16x32_bf16 v[76:79], a[76:79], v[224:227], v[76:79]\n"
      "ds_read_b64_tr_b16 a[162:163], v243 offset:896\n"
      "v_mfma_f32_16x16x32_bf16 v[80:83], a[80:83], v[224:227], v[80:83]\n"
      "ds_read_b128 v[220:223], v235 offset:24576\n"
      "s_add_u32 s78, s64, 0x33000\n"
      "s_addc_u32 s79, s65, 0\n"
      "s_add_u32 m0, s74, 0xcc00\n"
      "v_mfma_f32_16x16x32_bf16 v[84:87], a[84:87], v[224:227], v[84:87]\n"
      "ds_read_b64_tr_b16 a[164:165], v243 offset:136\n"
      "global_load_lds_dwordx4 v240, s[78:79]\n"
      "v_mfma_f32_16x16x32_bf16 v[88:91], a[88:91], v[224:227], v[88:91]\n"
      "ds_read_b64_tr_b16 a[166:167], v243 offset:904\n"
      "v_mfma_f32_16x16x32_bf16 v[92:95], a[92:95], v[224:227], v[92:95]\n"
      "s_waitcnt lgkmcnt(12)\n"
      "v_mfma_f32_16x16x32_bf16 v[96:99], a[72:75], v[228:231], v[96:99]\n"
      "s_add_u32 m0, s74, 0xd000\n"
      "v_mfma_f32_16x16x32_bf16 v[100:103], a[76:79], v[228:231], v[100:103]\n"
      "global_load_lds_dwordx4 v241, s[78:79]\n"
      "v_mfma_f32_16x16x32_bf16 v[104:107], a[80:83], v[228:231], v[104:107]\n"
      "ds_read_b128 v[224:227], v235 offset:28672\n"
      "v_mfma_f32_16x16x32_bf16 v[108:111], a[84:87], v[228:231], v[108:111]\n"
      "v_mfma_f32_16x16x32_bf16 v[112:115], a[88:91], v[228:231], v[112:115]\n"
      "s_waitcnt vmcnt(6)\n"
      "s_add_u32 m0, s74, 0xd400\n"
      "v_mfma_f32_16x16x32_bf16 v[116:119], a[92:95], v[228:231], v[116:119]\n"
      "global_load_lds_dwordx4 v242, s[78:79]\n"
      "s_waitcnt lgkmcnt(8)\n"
      "v_mfma_f32_16x16x32_bf16 v[120:123], a[72:75], v[216:219], v[120:123]\n"
      "ds_read_b64_tr_b16 a[168:169], v243 offset:24576\n"
      "v_mfma_f32_16x16x32_bf16 v[124:127], a[76:79], v[216:219], v[124:127]\n"
      "ds_read_b64_tr_b16 a[170:171], v243 offset:25344\n"
      "v_mfma_f32_16x16x32_bf16 v[128:131], a[80:83], v[216:219], v[128:131]\n"
      "ds_read_b128 v[228:231], v235 offset:32768\n"
      "v_add_u32 v235, 0x9000, v235\n"
      "s_waitcnt lgkmcnt(4)\n"
      "s_add_u32 s78, s64, 0x36000\n"
      "s_addc_u32 s79, s65, 0\n"
      "s_add_u32 m0, s74, 0x0\n"
      "v_mfma_f32_16x16x32_bf16 v[132:135], a[84:87], v[216:219], v[132:135]\n"
      "ds_read_b64_tr_b16 a[172:173], v243 offset:24584\n"
      "global_load_lds_dwordx4 v240, s[78:79]\n"
      "v_mfma_f32_16x16x32_bf16 v[136:139], a[88:91], v[216:219], v[136:139]\n"
      "ds_read_b64_tr_b16 a[174:175], v243 offset:25352\n"
      "v_mfma_f32_16x16x32_bf16 v[140:143], a[92:95], v[216:219], v[140:143]\n"
      "v_mfma_f32_16x16x32_bf16 v[144:147], a[72:75], v[220:223], v[144:147]\n"
      "ds_read_b64_tr_b16 a[176:177], v243 offset:24640\n"
      "s_waitcnt vmcnt(7)\n"
      "v_cmp_ne_u64 vcc, s[80:81], v[244:245]\n"
      "v_cmp_ne_u64 s[56:57], s[80:81], v[246:247]\n"
      "s_nop 3\n"
      "s_or_b64 vcc, vcc, s[56:57]\n"
      "s_cbranch_scc0 LP1_0\n"
      "s_mov_b32 s82, 0x40000\n"
      "LP0_0:\n"
      "s_sleep 2\n"
      "global_load_dwordx4 v[244:247], v248, s[28:29] sc1\n"
      "s_sub_u32 s82, s82, 1\n"
      "s_cbranch_scc1 LP2_0\n"
      "s_waitcnt vmcnt(0)\n"
      "v_cmp_ne_u64 vcc, s[80:81], v[244:245]\n"
      "v_cmp_ne_u64 s[56:57], s[80:81], v[246:247]\n"
      "s_nop 3\n"
      "s_or_b64 vcc, vcc, s[56:57]\n"
      "s_cbranch_scc1 LP0_0\n"
      "LP2_0:\n"
      "s_waitcnt vmcnt(0)\n"
      "LP1_0:\n"
      "s_nop 0\n"
      "v_mfma_f32_16x16x32_bf16 v[148:151], a[76:79], v[220:223], v[148:151]\n"
      "s_barrier\n"
      "s_add_u32 m0, s74, 0x400\n"
      "v_mfma_f32_16x16x32_bf16 v[152:155], a[80:83], v[220:223], v[152:155]\n"
      "ds_read_b128 v[216:219], v232 offset:0\n"
      "global_load_lds_dwordx4 v241, s[78:79]\n"
      "v_mfma_f32_16x16x32_bf16 v[156:159], a[84:87], v[220:223], v[156:159]\n"
      "ds_read_b64_tr_b16 a[178:179], v243 offset:25408\n"
      "v_mfma_f32_16x16x32_bf16 v[160:163], a[88:91], v[220:223], v[160:163]\n"
      "ds_read_b64_tr_b16 a[180:181], v243 offset:24648\n"
      "v_mfma_f32_16x16x32_bf16 v[164:167], a[92:95], v[220:223], v[164:167]\n"
      "s_add_u32 m0, s74, 0x800\n"
      "s_waitcnt lgkmcnt(9)\n"
      "v_mfma_f32_16x16x32_bf16 v[168:171], a[72:75], v[224:227], v[168:171]\n"
      "ds_read_b64_tr_b16 a[182:183], v243 offset:25416\n"
      "global_load_lds_dwordx4 v242, s[78:79]\n"
      "v_mfma_f32_16x16x32_bf16 v[172:175], a[76:79], v[224:227], v[172:175]\n"
      "ds_read_b64_tr_b16 a[184:185], v243 offset:24704\n"
      "v_mfma_f32_16x16x32_bf16 v[176:179], a[80:83], v[224:227], v[176:179]\n"
      "ds_read_b128 v[220:223], v232 offset:4096\n"
      "v_mfma_f32_16x16x32_bf16 v[180:183], a[84:87], v[224:227], v[180:183]\n"
      "ds_read_b64_tr_b16 a[186:187], v243 offset:25472\n"
      "s_add_u32 s78, s64, 0x39000\n"
      "s_addc_u32 s79, s65, 0\n"
      "s_add_u32 m0, s74, 0xc00\n"
      "v_mfma_f32_16x16x32_bf16 v[184:187], a[88:91], v[224:227], v[184:187]\n"
      "ds_read_b64_tr_b16 a[188:189], v243 offset:24712\n"
      "global_load_lds_dwordx4 v240, s[78:79]\n"
      "v_mfma_f32_16x16x32_bf16 v[188:191], a[92:95], v[224:227], v[188:191]\n"
      "s_waitcnt lgkmcnt(11)\n"
      "v_mfma_f32_16x16x32_bf16 v[192:195], a[72:75], v[228:231], v[192:195]\n"
      "ds_read_b64_tr_b16 a[190:191], v243 offset:25480\n"
      "v_mfma_f32_16x16x32_bf16 v[196:199], a[76:79], v[228:231], v[196:199]\n"
      "s_add_u32 m0, s74, 0x1000\n"
      "v_mfma_f32_16x16x32_bf16 v[200:203], a[80:83], v[228:231], v[200:203]\n"
      "ds_read_b128 v[224:227], v232 offset:8192\n"
      "global_load_lds_dwordx4 v241, s[78:79]\n"
      "v_mfma_f32_16x16x32_bf16 v[204:207], a[84:87], v[228:231], v[204:207]\n"
      "v_mfma_f32_16x16x32_bf16 v[208:211], a[88:91], v[228:231], v[208:211]\n"
      "v_mfma_f32_16x16x32_bf16 v[212:215], a[92:95], v[228:231], v[212:215]\n"
      "s_add_u32 m0, s74, 0x1400\n"
      "s_waitcnt lgkmcnt(9)\n"
      "v_mfma_f32_16x16x32_bf16 v[0:3], a[96:99], v[216:219], v[0:3]\n"
      "global_load_lds_dwordx4 v242, s[78:79]\n"
      "v_mfma_f32_16x16x32_bf16 v[4:7], a[100:103], v[216:219], v[4:7]\n"
      "s_barrier\n"
      "v_mfma_f32_16x16x32_bf16 v[8:11], a[104:107], v[216:219], v[8:11]\n"
      "ds_read_b128 v[228:231], v232 offset:12288\n"
      "v_mfma_f32_16x16x32_bf16 v[12:15], a[108:111], v[216:219], v[12:15]\n"
      "s_add_u32 s58, s24, 0x0\n"
      "s_addc_u32 s59, s25, 0\n"
      "global_load_dwordx4 a[240:243], v239, s[58:59] offset:0 sc1\n"
      "v_mfma_f32_16x16x32_bf16 v[16:19], a[112:115], v[216:219], v[16:19]\n"
      "v_mfma_f32_16x16x32_bf16 v[20:23], a[116:119], v[216:219], v[20:23]\n"
      "s_waitcnt lgkmcnt(5)\n"
      "v_mfma_f32_16x16x32_bf16 v[24:27], a[96:99], v[220:223], v[24:27]\n"
      "v_mfma_f32_16x16x32_bf16 v[28:31], a[100:103], v[220:223], v[28:31]\n"
      "s_waitcnt lgkmcnt(2)\n"
      "s_add_u32 s78, s64, 0x3c000\n"
      "s_addc_u32 s79, s65, 0\n"
      "s_add_u32 m0, s74, 0x6000\n"
      "v_mfma_f32_16x16x32_bf16 v[32:35], a[104:107], v[220:223], v[32:35]\n"
      "ds_read_b128 v[216:219], v232 offset:16384\n"
      "global_load_lds_dwordx4 v240, s[78:79]\n"
      "v_mfma_f32_16x16x32_bf16 v[36:39], a[108:111], v[220:223], v[36:39]\n"
      "v_mfma_f32_16x16x32_bf16 v[40:43], a[112:115], v[220:223], v[40:43]\n"
      "s_waitcnt vmcnt(8)\n"
      "v_mfma_f32_16x16x32_bf16 v[44:47], a[116:119], v[220:223], v[44:47]\n"
      "s_add_u32 m0, s74, 0x6400\n"
      "s_waitcnt lgkmcnt(2)\n"
      "v_mfma_f32_16x16x32_bf16 v[48:51], a[96:99], v[224:227], v[48:51]\n"
      "ds_read_b64_tr_b16 a[192:193], v243 offset:49152\n"
      "global_load_lds_dwordx4 v241, s[78:79]\n"
      "v_mfma_f32_16x16x32_bf16 v[52:55], a[100:103], v[224:227], v[52:55]\n"
      "ds_read_b64_tr_b16 a[194:195], v243 offset:49920\n"
      "v_mfma_f32_16x16x32_bf16 v[56:59], a[104:107], v[224:227], v[56:59]\n"
      "ds_read_b128 v[220:223], v232 offset:20480\n"
      "v_mfma_f32_16x16x32_bf16 v[60:63], a[108:111], v[224:227], v[60:63]\n"
      "ds_read_b64_tr_b16 a[196:197], v243 offset:49160\n"
      "s_add_u32 m0, s74, 0x6800\n"
      "v_mfma_f32_16x16x32_bf16 v[64:67], a[112:115], v[224:227], v[64:67]\n"
      "ds_read_b64_tr_b16 a[198:199], v243 offset:49928\n"
      "global_load_lds_dwordx4 v242, s[78:79]\n"
      "v_mfma_f32_16x16x32_bf16 v[68:71], a[116:119], v[224:227], v[68:71]\n"
      "s_waitcnt lgkmcnt(6)\n"
      "v_mfma_f32_16x16x32_bf16 v[72:75], a[96:99], v[228:231], v[72:75]\n"
      "ds_read_b64_tr_b16 a[200:201], v243 offset:49216\n"
      "v_mfma_f32_16x16x32_bf16 v[76:79], a[100:103], v[228:231], v[76:79]\n"
      "ds_read_b64_tr_b16 a[202:203], v243 offset:49984\n"
      "s_add_u32 s78, s64, 0x3f000\n"
      "s_addc_u32 s79, s65, 0\n"
      "s_add_u32 m0, s74, 0x6c00\n"
      "v_mfma_f32_16x16x32_bf16 v[80:83], a[104:107], v[228:231], v[80:83]\n"
      "ds_read_b128 v[224:227], v232 offset:24576\n"
      "global_load_lds_dwordx4 v240, s[78:79]\n"
      "v_mfma_f32_16x16x32_bf16 v[84:87], a[108:111], v[228:231], v[84:87]\n"
      "ds_read_b64_tr_b16 a[204:205], v243 offset:49224\n"
      "v_mfma_f32_16x16x32_bf16 v[88:91], a[112:115], v[228:231], v[88:91]\n"
      "ds_read_b64_tr_b16 a[206:207], v243 offset:49992\n"
      "v_mfma_f32_16x16x32_bf16 v[92:95], a[116:119], v[228:231], v[92:95]\n"
      "s_add_u32 m0, s74, 0x7000\n"
      "s_waitcnt lgkmcnt(10)\n"
      "v_mfma_f32_16x16x32_bf16 v[96:99], a[96:99], v[216:219], v[96:99]\n"
      "ds_read_b64_tr_b16 a[208:209], v243 offset:49280\n"
      "global_load_lds_dwordx4 v241, s[78:79]\n"
      "v_mfma_f32_16x16x32_bf16 v[100:103], a[100:103], v[216:219], v[100:103]\n"
      "ds_read_b64_tr_b16 a[210:211], v243 offset:50048\n"
      "v_mfma_f32_16x16x32_bf16 v[104:107], a[104:107], v[216:219], v[104:107]\n"
      "ds_read_b128 v[228:231], v232 offset:28672\n"
      "v_mfma_f32_16x16x32_bf16 v[108:111], a[108:111], v[216:219], v[108:111]\n"
      "ds_read_b64_tr_b16 a[212:213], v243 offset:49288\n"
      "s_add_u32 m0, s74, 0x7400\n"
      "v_mfma_f32_16x16x32_bf16 v[112:115], a[112:115], v[216:219], v[112:115]\n"
      "ds_read_b64_tr_b16 a[214:215], v243 offset:50056\n"
      "global_load_lds_dwordx4 v242, s[78:79]\n"
      "v_mfma_f32_16x16x32_bf16 v[116:119], a[116:119], v[216:219], v[116:119]\n"
      "s_waitcnt lgkmcnt(12)\n"
      "v_mfma_f32_16x16x32_bf16 v[120:123], a[96:99], v[220:223], v[120:123]\n"
      "v_mfma_f32_16x16x32_bf16 v[124:127], a[100:103], v[220:223], v[124:127]\n"
      "global_load_dwordx4 a[244:247], v239, s[58:59] offset:1024 sc1\n"
      "v_mfma_f32_16x16x32_bf16 v[128:131], a[104:107], v[220:223], v[128:131]\n"
      "ds_read_b128 v[216:219], v232 offset:32768\n"
      "v_add_u32 v232, 0xffff7000, v232\n"
      "v_mfma_f32_16x16x32_bf16 v[132:135], a[108:111], v[220:223], v[132:135]\n"
      "v_mfma_f32_16x16x32_bf16 v[136:139], a[112:115], v[220:223], v[136:139]\n"
      "v_mfma_f32_16x16x32_bf16 v[140:143], a[116:119], v[220:223], v[140:143]\n"
      "global_load_dwordx4 a[248:251], v239, s[58:59] offset:2048 sc1\n"
      "s_waitcnt lgkmcnt(8)\n"
      "v_mfma_f32_16x16x32_bf16 v[144:147], a[96:99], v[224:227], v[144:147]\n"
      "s_waitcnt vmcnt(9)\n"
      "v_mfma_f32_16x16x32_bf16 v[148:151], a[100:103], v[224:227], v[148:151]\n"
      "ds_read_b64_tr_b16 a[216:217], v243 offset:0\n"
      "v_mfma_f32_16x16x32_bf16 v[152:155], a[104:107], v[224:227], v[152:155]\n"
      "ds_read_b128 v[220:223], v233 offset:0\n"
      "v_mfma_f32_16x16x32_bf16 v[156:159], a[108:111], v[224:227], v[156:159]\n"
      "ds_read_b64_tr_b16 a[218:219], v243 offset:768\n"
      "s_waitcnt lgkmcnt(4)\n"
      "s_add_u32 s78, s64, 0x42000\n"
      "s_addc_u32 s79, s65, 0\n"
      "s_add_u32 m0, s74, 0xc000\n"
      "v_mfma_f32_16x16x32_bf16 v[160:163], a[112:115], v[224:227], v[160:163]\n"
      "ds_read_b64_tr_b16 a[220:221], v243 offset:8\n"
      "global_load_lds_dwordx4 v240, s[78:79]\n"
      "v_mfma_f32_16x16x32_bf16 v[164:167], a[116:119], v[224:227], v[164:167]\n"
      "v_mfma_f32_16x16x32_bf16 v[168:171], a[96:99], v[228:231], v[168:171]\n"
      "ds_read_b64_tr_b16 a[222:223], v243 offset:776\n"
      "v_mfma_f32_16x16x32_bf16 v[172:175], a[100:103], v[228:231], v[172:175]\n"
      "ds_read_b64_tr_b16 a[224:225], v243 offset:64\n"
      "s_add_u32 m0, s74, 0xc400\n"
      "v_mfma_f32_16x16x32_bf16 v[176:179], a[104:107], v[228:231], v[176:179]\n"
      "ds_read_b128 v[224:227], v233 offset:4096\n"
      "global_load_lds_dwordx4 v241, s[78:79]\n"
      "v_mfma_f32_16x16x32_bf16 v[180:183], a[108:111], v[228:231], v[180:183]\n"
      "ds_read_b64_tr_b16 a[226:227], v243 offset:832\n"
      "v_mfma_f32_16x16x32_bf16 v[184:187], a[112:115], v[228:231], v[184:187]\n"
      "ds_read_b64_tr_b16 a[228:229], v243 offset:72\n"
      "v_mfma_f32_16x16x32_bf16 v[188:191], a[116:119], v[228:231], v[188:191]\n"
      "s_add_u32 m0, s74, 0xc800\n"
      "s_waitcnt lgkmcnt(9)\n"
      "v_mfma_f32_16x16x32_bf16 v[192:195], a[96:99], v[216:219], v[192:195]\n"
      "ds_read_b64_tr_b16 a[230:231], v243 offset:840\n"
      "global_load_lds_dwordx4 v242, s[78:79]\n"
      "v_mfma_f32_16x16x32_bf16 v[196:199], a[100:103], v[216:219], v[196:199]\n"
      "ds_read_b64_tr_b16 a[232:233], v243 offset:128\n"
      "v_mfma_f32_16x16x32_bf16 v[200:203], a[104:107], v[216:219], v[200:203]\n"
      "ds_read_b128 v[228:231], v233 offset:8192\n"
      "v_mfma_f32_16x16x32_bf16 v[204:207], a[108:111], v[216:219], v[204:207]\n"
      "ds_read_b64_tr_b16 a[234:235], v243 offset:896\n"
      "s_add_u32 s78, s64, 0x45000\n"
      "s_addc_u32 s79, s65, 0\n"
      "s_add_u32 m0, s74, 0xcc00\n"
      "v_mfma_f32_16x16x32_bf16 v[208:211], a[112:115], v[216:219], v[208:211]\n"
      "ds_read_b64_tr_b16 a[236:237], v243 offset:136\n"
      "global_load_lds_dwordx4 v240, s[78:79]\n"
      "v_mfma_f32_16x16x32_bf16 v[212:215], a[116:119], v[216:219], v[212:215]\n"
      "s_waitcnt lgkmcnt(12)\n"
      "v_mfma_f32_16x16x32_bf16 v[0:3], a[120:123], v[220:223], v[0:3]\n"
      "ds_read_b64_tr_b16 a[238:239], v243 offset:904\n"
      "v_mfma_f32_16x16x32_bf16 v[4:7], a[124:127], v[220:223], v[4:7]\n"
      "s_add_u32 m0, s74, 0xd000\n"
      "v_mfma_f32_16x16x32_bf16 v[8:11], a[128:131], v[220:223], v[8:11]\n"
      "ds_read_b128 v[216:219], v233 offset:12288\n"
      "global_load_lds_dwordx4 v241, s[78:79]\n"
      "v_mfma_f32_16x16x32_bf16 v[12:15], a[132:135], v[220:223], v[12:15]\n"
      "v_mfma_f32_16x16x32_bf16 v[16:19], a[136:139], v[220:223], v[16:19]\n"
      "v_mfma_f32_16x16x32_bf16 v[20:23], a[140:143], v[220:223], v[20:23]\n"
      "s_add_u32 m0, s74, 0xd400\n"
      "s_waitcnt lgkmcnt(9)\n"
      "v_mfma_f32_16x16x32_bf16 v[24:27], a[120:123], v[224:227], v[24:27]\n"
      "global_load_lds_dwordx4 v242, s[78:79]\n"
      "v_mfma_f32_16x16x32_bf16 v[28:31], a[124:127], v[224:227], v[28:31]\n"
      "v_mfma_f32_16x16x32_bf16 v[32:35], a[128:131], v[224:227], v[32:35]\n"
      "ds_read_b128 v[220:223], v233 offset:16384\n"
      "v_mfma_f32_16x16x32_bf16 v[36:39], a[132:135], v[224:227], v[36:39]\n"
      "s_waitcnt vmcnt(8)\n"
      "v_mad_i32_i24 v238, v251, s89, v238\n"
      "s_add_u32 s56, s22, 0x0\n"
      "s_addc_u32 s57, s23, 0\n"
      "s_add_u32 m0, s18, 0xfffffe00\n"
      "v_mfma_f32_16x16x32_bf16 v[40:43], a[136:139], v[224:227], v[40:43]\n"
      "ds_read_b64_tr_b16 a[0:1], v243 offset:24576\n"
      "global_load_lds_dwordx4 v238, s[56:57] offset:512 nt\n"
      "v_mfma_f32_16x16x32_bf16 v[44:47], a[140:143], v[224:227], v[44:47]\n"
      "s_waitcnt lgkmcnt(6)\n"
      "v_mfma_f32_16x16x32_bf16 v[48:51], a[120:123], v[228:231], v[48:51]\n"
      "ds_read_b64_tr_b16 a[2:3], v243 offset:25344\n"
      "v_mad_i32_i24 v238, v251, s90, v238\n"
      "s_add_u32 s56, s22, 0x6000\n"
      "s_addc_u32 s57, s23, 0\n"
      "s_add_u32 m0, s18, 0xe00\n"
      "v_mfma_f32_16x16x32_bf16 v[52:55], a[124:127], v[228:231], v[52:55]\n"
      "ds_read_b64_tr_b16 a[4:5], v243 offset:24584\n"
      "global_load_lds_dwordx4 v238, s[56:57] offset:512 nt\n"
      "v_mfma_f32_16x16x32_bf16 v[56:59], a[128:131], v[228:231], v[56:59]\n"
      "ds_read_b128 v[224:227], v233 offset:20480\n"
      "v_mfma_f32_16x16x32_bf16 v[60:63], a[132:135], v[228:231], v[60:63]\n"
      "ds_read_b64_tr_b16 a[6:7], v243 offset:25352\n"
      "v_mad_i32_i24 v238, v251, s91, v238\n"
      "s_add_u32 s56, s22, 0xc000\n"
      "s_addc_u32 s57, s23, 0\n"
      "s_add_u32 m0, s18, 0x1e00\n"
      "v_mfma_f32_16x16x32_bf16 v[64:67], a[136:139], v[228:231], v[64:67]\n"
      "ds_read_b64_tr_b16 a[8:9], v243 offset:24640\n"
      "global_load_lds_dwordx4 v238, s[56:57] offset:512 nt\n"
      "v_mfma_f32_16x16x32_bf16 v[68:71], a[140:143], v[228:231], v[68:71]\n"
      "s_waitcnt lgkmcnt(7)\n"
      "v_mfma_f32_16x16x32_bf16 v[72:75], a[120:123], v[216:219], v[72:75]\n"
      "ds_read_b64_tr_b16 a[10:11], v243 offset:25408\n"
      "v_mad_i32_i24 v238, v251, s92, v238\n"
      "s_add_u32 s56, s22, 0x12000\n"
      "s_addc_u32 s57, s23, 0\n"
      "s_add_u32 m0, s18, 0x2e00\n"
      "v_mfma_f32_16x16x32_bf16 v[76:79], a[124:127], v[216:219], v[76:79]\n"
      "ds_read_b64_tr_b16 a[12:13], v243 offset:24648\n"
      "global_load_lds_dwordx4 v238, s[56:57] offset:512 nt\n"
      "v_mfma_f32_16x16x32_bf16 v[80:83], a[128:131], v[216:219], v[80:83]\n"
      "ds_read_b128 v[228:231], v233 offset:24576\n"
      "v_mfma_f32_16x16x32_bf16 v[84:87], a[132:135], v[216:219], v[84:87]\n"
      "ds_read_b64_tr_b16 a[14:15], v243 offset:25416\n"
      "v_mad_i32_i24 v238, v251, s93, v238\n"
      "s_add_u32 s56, s22, 0x18000\n"
      "s_addc_u32 s57, s23, 0\n"
      "s_add_u32 m0, s18, 0x3e00\n"
      "v_mfma_f32_16x16x32_bf16 v[88:91], a[136:139], v[216:219], v[88:91]\n"
      "ds_read_b64_tr_b16 a[16:17], v243 offset:24704\n"
      "global_load_lds_dwordx4 v238, s[56:57] offset:512 nt\n"
      "v_mfma_f32_16x16x32_bf16 v[92:95], a[140:143], v[216:219], v[92:95]\n"
      "s_waitcnt lgkmcnt(11)\n"
      "v_mfma_f32_16x16x32_bf16 v[96:99], a[120:123], v[220:223], v[96:99]\n"
      "ds_read_b64_tr_b16 a[18:19], v243 offset:25472\n"
      "v_mad_i32_i24 v238, v251, s94, v238\n"
      "s_add_u32 s56, s22, 0x1e000\n"
      "s_addc_u32 s57, s23, 0\n"
      "s_add_u32 m0, s18, 0x4e00\n"
      "v_mfma_f32_16x16x32_bf16 v[100:103], a[124:127], v[220:223], v[100:103]\n"
      "ds_read_b64_tr_b16 a[20:21], v243 offset:24712\n"
      "global_load_lds_dwordx4 v238, s[56:57] offset:512 nt\n"
      "v_mfma_f32_16x16x32_bf16 v[104:107], a[128:131], v[220:223], v[104:107]\n"
      "ds_read_b128 v[216:219], v233 offset:28672\n"
      "v_mfma_f32_16x16x32_bf16 v[108:111], a[132:135], v[220:223], v[108:111]\n"
      "ds_read_b64_tr_b16 a[22:23], v243 offset:25480\n"
      "v_mad_i32_i24 v238, v251, s95, v238\n"
      "s_add_u32 s56, s22, 0x24000\n"
      "s_addc_u32 s57, s23, 0\n"
      "s_add_u32 m0, s18, 0x5e00\n"
      "v_mfma_f32_16x16x32_bf16 v[112:115], a[136:139], v[220:223], v[112:115]\n"
      "global_load_lds_dwordx4 v238, s[56:57] offset:512 nt\n"
      "v_mfma_f32_16x16x32_bf16 v[116:119], a[140:143], v[220:223], v[116:119]\n"
      "s_waitcnt lgkmcnt(11)\n"
      "v_mfma_f32_16x16x32_bf16 v[120:123], a[120:123], v[224:227], v[120:123]\n"
      "v_mad_i32_i24 v238, v251, s96, v238\n"
      "s_add_u32 s56, s22, 0x2a000\n"
      "s_addc_u32 s57, s23, 0\n"
      "s_add_u32 m0, s18, 0x6e00\n"
      "v_mfma_f32_16x16x32_bf16 v[124:127], a[124:127], v[224:227], v[124:127]\n"
      "global_load_lds_dwordx4 v238, s[56:57] offset:512 nt\n"
      "v_mfma_f32_16x16x32_bf16 v[128:131], a[128:131], v[224:227], v[128:131]\n"
      "ds_read_b128 v[220:223], v233 offset:32768\n"
      "v_add_u32 v233, 0xffff7000, v233\n"
      "v_mfma_f32_16x16x32_bf16 v[132:135], a[132:135], v[224:227], v[132:135]\n"
      "v_mad_i32_i24 v238, v251, s97, v238\n"
      "s_add_u32 s56, s22, 0x30000\n"
      "s_addc_u32 s57, s23, 0\n"
      "s_add_u32 m0, s18, 0x7e00\n"
      "v_mfma_f32_16x16x32_bf16 v[136:139], a[136:139], v[224:227], v[136:139]\n"
      "global_load_lds_dwordx4 v238, s[56:57] offset:512 nt\n"
      "v_mfma_f32_16x16x32_bf16 v[140:143], a[140:143], v[224:227], v[140:143]\n"
      "s_waitcnt lgkmcnt(7)\n"
      "v_mfma_f32_16x16x32_bf16 v[144:147], a[120:123], v[228:231], v[144:147]\n"
      "v_mfma_f32_16x16x32_bf16 v[148:151], a[124:127], v[228:231], v[148:151]\n"
      "global_load_dwordx4 a[252:255], v239, s[58:59] offset:3072 sc1\n"
      "v_mfma_f32_16x16x32_bf16 v[152:155], a[128:131], v[228:231], v[152:155]\n"
      "ds_read_b128 v[224:227], v234 offset:0\n"
      "v_mfma_f32_16x16x32_bf16 v[156:159], a[132:135], v[228:231], v[156:159]\n"
      "v_mfma_f32_16x16x32_bf16 v[160:163], a[136:139], v[228:231], v[160:163]\n"
      "v_mfma_f32_16x16x32_bf16 v[164:167], a[140:143], v[228:231], v[164:167]\n"
      "s_add_u32 s58, s24, 0x1000\n"
      "s_addc_u32 s59, s25, 0\n"
      "global_load_dwordx4 a[48:51], v239, s[58:59] offset:0 sc1\n"
      "s_waitcnt lgkmcnt(3)\n"
      "v_mfma_f32_16x16x32_bf16 v[168:171], a[120:123], v[216:219], v[168:171]\n"
      "s_waitcnt vmcnt(11)\n"
      "v_mfma_f32_16x16x32_bf16 v[172:175], a[124:127], v[216:219], v[172:175]\n"
      "ds_read_b64_tr_b16 a[24:25], v243 offset:49152\n"
      "v_mfma_f32_16x16x32_bf16 v[176:179], a[128:131], v[216:219], v[176:179]\n"
      "ds_read_b128 v[228:231], v234 offset:4096\n"
      "v_mfma_f32_16x16x32_bf16 v[180:183], a[132:135], v[216:219], v[180:183]\n"
      "ds_read_b64_tr_b16 a[26:27], v243 offset:49920\n"
      "global_load_dwordx4 a[52:55], v239, s[58:59] offset:1024 sc1\n"
      "v_mfma_f32_16x16x32_bf16 v[184:187], a[136:139], v[216:219], v[184:187]\n"
      "ds_read_b64_tr_b16 a[28:29], v243 offset:49160\n"
      "v_mfma_f32_16x16x32_bf16 v[188:191], a[140:143], v[216:219], v[188:191]\n"
      "s_waitcnt lgkmcnt(5)\n"
      "v_mfma_f32_16x16x32_bf16 v[192:195], a[120:123], v[220:223], v[192:195]\n"
      "ds_read_b64_tr_b16 a[30:31], v243 offset:49928\n"
      "v_mfma_f32_16x16x32_bf16 v[196:199], a[124:127], v[220:223], v[196:199]\n"
      "ds_read_b64_tr_b16 a[32:33], v243 offset:49216\n"
      "global_load_dwordx4 a[56:59], v239, s[58:59] offset:2048 sc1\n"
      "v_mfma_f32_16x16x32_bf16 v[200:203], a[128:131], v[220:223], v[200:203]\n"
      "ds_read_b128 v[216:219], v234 offset:8192\n"
      "v_mfma_f32_16x16x32_bf16 v[204:207], a[132:135], v[220:223], v[204:207]\n"
      "ds_read_b64_tr_b16 a[34:35], v243 offset:49984\n"
      "v_mfma_f32_16x16x32_bf16 v[208:211], a[136:139], v[220:223], v[208:211]\n"
      "ds_read_b64_tr_b16 a[36:37], v243 offset:49224\n"
      "v_mfma_f32_16x16x32_bf16 v[212:215], a[140:143], v[220:223], v[212:215]\n"
      "global_load_dwordx4 a[60:63], v239, s[58:59] offset:3072 sc1\n"
      "s_waitcnt lgkmcnt(9)\n"
      "v_mfma_f32_16x16x32_bf16 v[0:3], a[144:147], v[224:227], v[0:3]\n"
      "ds_read_b64_tr_b16 a[38:39], v243 offset:49992\n"
      "v_mfma_f32_16x16x32_bf16 v[4:7], a[148:151], v[224:227], v[4:7]\n"
      "ds_read_b64_tr_b16 a[40:41], v243 offset:49280\n"
      "v_mfma_f32_16x16x32_bf16 v[8:11], a[152:155], v[224:227], v[8:11]\n"
      "ds_read_b128 v[220:223], v234 offset:12288\n"
      "v_mfma_f32_16x16x32_bf16 v[12:15], a[156:159], v[224:227], v[12:15]\n"
      "ds_read_b64_tr_b16 a[42:43], v243 offset:50048\n"
      "s_add_u32 s58, s24, 0x2000\n"
      "s_addc_u32 s59, s25, 0\n"
      "global_load_dwordx4 a[64:67], v239, s[58:59] offset:0 sc1\n"
      "v_mfma_f32_16x16x32_bf16 v[16:19], a[160:163], v[224:227], v[16:19]\n"
      "ds_read_b64_tr_b16 a[44:45], v243 offset:49288\n"
      "v_mfma_f32_16x16x32_bf16 v[20:23], a[164:167], v[224:227], v[20:23]\n"
      "s_waitcnt lgkmcnt(12)\n"
      "v_mfma_f32_16x16x32_bf16 v[24:27], a[144:147], v[228:231], v[24:27]\n"
      "ds_read_b64_tr_b16 a[46:47], v243 offset:50056\n"
      "v_mfma_f32_16x16x32_bf16 v[28:31], a[148:151], v[228:231], v[28:31]\n"
      "global_load_dwordx4 a[68:71], v239, s[58:59] offset:1024 sc1\n"
      "v_mfma_f32_16x16x32_bf16 v[32:35], a[152:155], v[228:231], v[32:35]\n"
      "ds_read_b128 v[224:227], v234 offset:16384\n"
      "v_mfma_f32_16x16x32_bf16 v[36:39], a[156:159], v[228:231], v[36:39]\n"
      "v_mfma_f32_16x16x32_bf16 v[40:43], a[160:163], v[228:231], v[40:43]\n"
      "v_mfma_f32_16x16x32_bf16 v[44:47], a[164:167], v[228:231], v[44:47]\n"
      "global_load_dwordx4 a[72:75], v239, s[58:59] offset:2048 sc1\n"
      "s_waitcnt lgkmcnt(9)\n"
      "v_mfma_f32_16x16x32_bf16 v[48:51], a[144:147], v[216:219], v[48:51]\n"
      "v_mfma_f32_16x16x32_bf16 v[52:55], a[148:151], v[216:219], v[52:55]\n"
      "v_mfma_f32_16x16x32_bf16 v[56:59], a[152:155], v[216:219], v[56:59]\n"
      "ds_read_b128 v[228:231], v234 offset:20480\n"
      "v_mfma_f32_16x16x32_bf16 v[60:63], a[156:159], v[216:219], v[60:63]\n"
      "global_load_dwordx4 a[76:79], v239, s[58:59] offset:3072 sc1\n"
      "v_mfma_f32_16x16x32_bf16 v[64:67], a[160:163], v[216:219], v[64:67]\n"
      "v_mfma_f32_16x16x32_bf16 v[68:71], a[164:167], v[216:219], v[68:71]\n"
      "s_waitcnt lgkmcnt(5)\n"
      "v_mfma_f32_16x16x32_bf16 v[72:75], a[144:147], v[220:223], v[72:75]\n"
      "v_mfma_f32_16x16x32_bf16 v[76:79], a[148:151], v[220:223], v[76:79]\n"
      "s_add_u32 s58, s24, 0x3000\n"
      "s_addc_u32 s59, s25, 0\n"
      "global_load_dwordx4 a[80:83], v239, s[58:59] offset:0 sc1\n"
      "v_mfma_f32_16x16x32_bf16 v[80:83], a[152:155], v[220:223], v[80:83]\n"
      "ds_read_b128 v[216:219], v234 offset:24576\n"
      "v_mfma_f32_16x16x32_bf16 v[84:87], a[156:159], v[220:223], v[84:87]\n"
      "v_mfma_f32_16x16x32_bf16 v[88:91], a[160:163], v[220:223], v[88:91]\n"
      "v_mfma_f32_16x16x32_bf16 v[92:95], a[164:167], v[220:223], v[92:95]\n"
      "global_load_dwordx4 a[84:87], v239, s[58:59] offset:1024 sc1\n"
      "s_waitcnt lgkmcnt(2)\n"
      "v_mfma_f32_16x16x32_bf16 v[96:99], a[144:147], v[224:227], v[96:99]\n"
      "v_mfma_f32_16x16x32_bf16 v[100:103], a[148:151], v[224:227], v[100:103]\n"
      "v_mfma_f32_16x16x32_bf16 v[104:107], a[152:155], v[224:227], v[104:107]\n"
      "ds_read_b128 v[220:223], v234 offset:28672\n"
      "v_mfma_f32_16x16x32_bf16 v[108:111], a[156:159], v[224:227], v[108:111]\n"
      "global_load_dwordx4 a[88:91], v239, s[58:59] offset:2048 sc1\n"
      "v_mfma_f32_16x16x32_bf16 v[112:115], a[160:163], v[224:227], v[112:115]\n"
      "v_mfma_f32_16x16x32_bf16 v[116:119], a[164:167], v[224:227], v[116:119]\n"
      "s_waitcnt lgkmcnt(2)\n"
      "v_mfma_f32_16x16x32_bf16 v[120:123], a[144:147], v[228:231], v[120:123]\n"
      "v_mfma_f32_16x16x32_bf16 v[124:127], a[148:151], v[228:231], v[124:127]\n"
      "global_load_dwordx4 a[92:95], v239, s[58:59] offset:3072 sc1\n"
      "v_mfma_f32_16x16x32_bf16 v[128:131], a[152:155], v[228:231], v[128:131]\n"
      "ds_read_b128 v[224:227], v234 offset:32768\n"
      "v_add_u32 v234, 0xffff7000, v234\n"
      "v_mfma_f32_16x16x32_bf16 v[132:135], a[156:159], v[228:231], v[132:135]\n"
      "v_mfma_f32_16x16x32_bf16 v[136:139], a[160:163], v[228:231], v[136:139]\n"
      "v_mfma_f32_16x16x32_bf16 v[140:143], a[164:167], v[228:231], v[140:143]\n"
      "s_add_u32 s58, s24, 0x4000\n"
      "s_addc_u32 s59, s25, 0\n"
      "global_load_dwordx4 a[96:99], v239, s[58:59] offset:0 sc1\n"
      "s_waitcnt lgkmcnt(2)\n"
      "v_mfma_f32_16x16x32_bf16 v[144:147], a[144:147], v[216:219], v[144:147]\n"
      "ds_read_b64 v[236:237], v250 offset:4104\n"
      "v_mfma_f32_16x16x32_bf16 v[148:151], a[148:151], v[216:219], v[148:151]\n"
      "v_mfma_f32_16x16x32_bf16 v[152:155], a[152:155], v[216:219], v[152:155]\n"
      "ds_read_b128 v[228:231], v235 offset:0\n"
      "v_mfma_f32_16x16x32_bf16 v[156:159], a[156:159], v[216:219], v[156:159]\n"
      "global_load_dwordx4 a[100:103], v239, s[58:59] offset:1024 sc1\n"
      "v_mfma_f32_16x16x32_bf16 v[160:163], a[160:163], v[216:219], v[160:163]\n"
      "v_mfma_f32_16x16x32_bf16 v[164:167], a[164:167], v[216:219], v[164:167]\n"
      "s_waitcnt lgkmcnt(3)\n"
      "v_mfma_f32_16x16x32_bf16 v[168:171], a[144:147], v[220:223], v[168:171]\n"
      "v_mfma_f32_16x16x32_bf16 v[172:175], a[148:151], v[220:223], v[172:175]\n"
      "global_load_dwordx4 a[104:107], v239, s[58:59] offset:2048 sc1\n"
      "v_mfma_f32_16x16x32_bf16 v[176:179], a[152:155], v[220:223], v[176:179]\n"
      "ds_read_b128 v[216:219], v235 offset:4096\n"
      "v_mfma_f32_16x16x32_bf16 v[180:183], a[156:159], v[220:223], v[180:183]\n"
      "v_mfma_f32_16x16x32_bf16 v[184:187], a[160:163], v[220:223], v[184:187]\n"
      "v_mfma_f32_16x16x32_bf16 v[188:191], a[164:167], v[220:223], v[188:191]\n"
      "global_load_dwordx4 a[108:111], v239, s[58:59] offset:3072 sc1\n"
      "s_waitcnt lgkmcnt(3)\n"
      "v_mfma_f32_16x16x32_bf16 v[192:195], a[144:147], v[224:227], v[192:195]\n"
      "v_mfma_f32_16x16x32_bf16 v[196:199], a[148:151], v[224:227], v[196:199]\n"
      "v_mfma_f32_16x16x32_bf16 v[200:203], a[152:155], v[224:227], v[200:203]\n"
      "ds_read_b128 v[220:223], v235 offset:8192\n"
      "v_mfma_f32_16x16x32_bf16 v[204:207], a[156:159], v[224:227], v[204:207]\n"
      "s_add_u32 s58, s24, 0x5000\n"
      "s_addc_u32 s59, s25, 0\n"
      "global_load_dwordx4 a[112:115], v239, s[58:59] offset:0 sc1\n"
      "v_mfma_f32_16x16x32_bf16 v[208:211], a[160:163], v[224:227], v[208:211]\n"
      "v_mfma_f32_16x16x32_bf16 v[212:215], a[164:167], v[224:227], v[212:215]\n"
      "s_waitcnt lgkmcnt(2)\n"
      "v_mfma_f32_16x16x32_bf16 v[0:3], a[168:171], v[228:231], v[0:3]\n"
      "v_mfma_f32_16x16x32_bf16 v[4:7], a[172:175], v[228:231], v[4:7]\n"
      "global_load_dwordx4 a[116:119], v239, s[58:59] offset:1024 sc1\n"
      "v_mfma_f32_16x16x32_bf16 v[8:11], a[176:179], v[228:231], v[8:11]\n"
      "ds_read_b128 v[224:227], v235 offset:12288\n"
      "v_mfma_f32_16x16x32_bf16 v[12:15], a[180:183], v[228:231], v[12:15]\n"
      "v_mfma_f32_16x16x32_bf16 v[16:19], a[184:187], v[228:231], v[16:19]\n"
      "v_mfma_f32_16x16x32_bf16 v[20:23], a[188:191], v[228:231], v[20:23]\n"
      "global_load_dwordx4 a[120:123], v239, s[58:59] offset:2048 sc1\n"
      "s_waitcnt lgkmcnt(2)\n"
      "v_mfma_f32_16x16x32_bf16 v[24:27], a[168:171], v[216:219], v[24:27]\n"
      "v_mfma_f32_16x16x32_bf16 v[28:31], a[172:175], v[216:219], v[28:31]\n"
      "v_mfma_f32_16x16x32_bf16 v[32:35], a[176:179], v[216:219], v[32:35]\n"
      "ds_read_b128 v[228:231], v235 offset:16384\n"
      "v_mfma_f32_16x16x32_bf16 v[36:39], a[180:183], v[216:219], v[36:39]\n"
      "global_load_dwordx4 a[124:127], v239, s[58:59] offset:3072 sc1\n"
      "v_mfma_f32_16x16x32_bf16 v[40:43], a[184:187], v[216:219], v[40:43]\n"
      "v_mfma_f32_16x16x32_bf16 v[44:47], a[188:191], v[216:219], v[44:47]\n"
      "s_waitcnt lgkmcnt(2)\n"
      "v_mfma_f32_16x16x32_bf16 v[48:51], a[168:171], v[220:223], v[48:51]\n"
      "v_mfma_f32_16x16x32_bf16 v[52:55], a[172:175], v[220:223], v[52:55]\n"
      "s_add_u32 s58, s24, 0x6000\n"
      "s_addc_u32 s59, s25, 0\n"
      "global_load_dwordx4 a[128:131], v239, s[58:59] offset:0 sc1\n"
      "v_mfma_f32_16x16x32_bf16 v[56:59], a[176:179], v[220:223], v[56:59]\n"
      "ds_read_b128 v[216:219], v235 offset:20480\n"
      "v_mfma_f32_16x16x32_bf16 v[60:63], a[180:183], v[220:223], v[60:63]\n"
      "v_mfma_f32_16x16x32_bf16 v[64:67], a[184:187], v[220:223], v[64:67]\n"
      "v_mfma_f32_16x16x32_bf16 v[68:71], a[188:191], v[220:223], v[68:71]\n"
      "global_load_dwordx4 a[132:135], v239, s[58:59] offset:1024 sc1\n"
      "s_waitcnt lgkmcnt(2)\n"
      "v_mfma_f32_16x16x32_bf16 v[72:75], a[168:171], v[224:227], v[72:75]\n"
      "v_mfma_f32_16x16x32_bf16 v[76:79], a[172:175], v[224:227], v[76:79]\n"
      "v_mfma_f32_16x16x32_bf16 v[80:83], a[176:179], v[224:227], v[80:83]\n"
      "ds_read_b128 v[220:223], v235 offset:24576\n"
      "v_mfma_f32_16x16x32_bf16 v[84:87], a[180:183], v[224:227], v[84:87]\n"
      "global_load_dwordx4 a[136:139], v239, s[58:59] offset:2048 sc1\n"
      "v_mfma_f32_16x16x32_bf16 v[88:91], a[184:187], v[224:227], v[88:91]\n"
      "v_mfma_f32_16x16x32_bf16 v[92:95], a[188:191], v[224:227], v[92:95]\n"
      "s_waitcnt lgkmcnt(2)\n"
      "v_mfma_f32_16x16x32_bf16 v[96:99], a[168:171], v[228:231], v[96:99]\n"
      "v_mfma_f32_16x16x32_bf16 v[100:103], a[172:175], v[228:231], v[100:103]\n"
      "global_load_dwordx4 a[140:143], v239, s[58:59] offset:3072 sc1\n"
      "v_mfma_f32_16x16x32_bf16 v[104:107], a[176:179], v[228:231], v[104:107]\n"
      "ds_read_b128 v[224:227], v235 offset:28672\n"
      "v_mfma_f32_16x16x32_bf16 v[108:111], a[180:183], v[228:231], v[108:111]\n"
      "v_mfma_f32_16x16x32_bf16 v[112:115], a[184:187], v[228:231], v[112:115]\n"
      "v_mfma_f32_16x16x32_bf16 v[116:119], a[188:191], v[228:231], v[116:119]\n"
      "s_add_u32 s58, s24, 0x7000\n"
      "s_addc_u32 s59, s25, 0\n"
      "global_load_dwordx4 a[144:147], v239, s[58:59] offset:0 sc1\n"
      "s_waitcnt lgkmcnt(2)\n"
      "v_mfma_f32_16x16x32_bf16 v[120:123], a[168:171], v[216:219], v[120:123]\n"
      "v_mfma_f32_16x16x32_bf16 v[124:127], a[172:175], v[216:219], v[124:127]\n"
      "v_mfma_f32_16x16x32_bf16 v[128:131], a[176:179], v[216:219], v[128:131]\n"
      "ds_read_b128 v[228:231], v235 offset:32768\n"
      "v_add_u32 v235, 0xffff7000, v235\n"
      "v_mfma_f32_16x16x32_bf16 v[132:135], a[180:183], v[216:219], v[132:135]\n"
      "global_load_dwordx4 a[148:151], v239, s[58:59] offset:1024 sc1\n"
      "v_mfma_f32_16x16x32_bf16 v[136:139], a[184:187], v[216:219], v[136:139]\n"
      "v_mfma_f32_16x16x32_bf16 v[140:143], a[188:191], v[216:219], v[140:143]\n"
      "s_waitcnt lgkmcnt(2)\n"
      "v_mfma_f32_16x16x32_bf16 v[144:147], a[168:171], v[220:223], v[144:147]\n"
      "v_mfma_f32_16x16x32_bf16 v[148:151], a[172:175], v[220:223], v[148:151]\n"
      "s_waitcnt vmcnt(27)\n"
      "s_barrier\n"
      "global_load_dwordx4 a[152:155], v239, s[58:59] offset:2048 sc1\n"
      "v_mfma_f32_16x16x32_bf16 v[152:155], a[176:179], v[220:223], v[152:155]\n"
      "ds_read_b128 v[216:219], v232 offset:0\n"
      "v_mfma_f32_16x16x32_bf16 v[156:159], a[180:183], v[220:223], v[156:159]\n"
      "v_mfma_f32_16x16x32_bf16 v[160:163], a[184:187], v[220:223], v[160:163]\n"
      "v_mfma_f32_16x16x32_bf16 v[164:167], a[188:191], v[220:223], v[164:167]\n"
      "global_load_dwordx4 a[156:159], v239, s[58:59] offset:3072 sc1\n"
      "s_waitcnt lgkmcnt(2)\n"
      "v_mfma_f32_16x16x32_bf16 v[168:171], a[168:171], v[224:227], v[168:171]\n"
      "v_mfma_f32_16x16x32_bf16 v[172:175], a[172:175], v[224:227], v[172:175]\n"
      "v_mfma_f32_16x16x32_bf16 v[176:179], a[176:179], v[224:227], v[176:179]\n"
      "ds_read_b128 v[220:223], v232 offset:4096\n"
      "v_mfma_f32_16x16x32_bf16 v[180:183], a[180:183], v[224:227], v[180:183]\n"
      "s_add_u32 s58, s24, 0x8000\n"
      "s_addc_u32 s59, s25, 0\n"
      "global_load_dwordx4 a[160:163], v239, s[58:59] offset:0 sc1\n"
      "v_mfma_f32_16x16x32_bf16 v[184:187], a[184:187], v[224:227], v[184:187]\n"
      "v_mfma_f32_16x16x32_bf16 v[188:191], a[188:191], v[224:227], v[188:191]\n"
      "s_waitcnt lgkmcnt(2)\n"
      "v_mfma_f32_16x16x32_bf16 v[192:195], a[168:171], v[228:231], v[192:195]\n"
      "v_mfma_f32_16x16x32_bf16 v[196:199], a[172:175], v[228:231], v[196:199]\n"
      "global_load_dwordx4 a[164:167], v239, s[58:59] offset:1024 sc1\n"
      "v_mfma_f32_16x16x32_bf16 v[200:203], a[176:179], v[228:231], v[200:203]\n"
      "ds_read_b128 v[224:227], v232 offset:8192\n"
      "v_mfma_f32_16x16x32_bf16 v[204:207], a[180:183], v[228:231], v[204:207]\n"
      "v_mfma_f32_16x16x32_bf16 v[208:211], a[184:187], v[228:231], v[208:211]\n"
      "v_mfma_f32_16x16x32_bf16 v[212:215], a[188:191], v[228:231], v[212:215]\n"
      "s_waitcnt lgkmcnt(2)\n"
      "v_mfma_f32_16x16x32_bf16 v[0:3], a[192:195], v[216:219], v[0:3]\n"
      "global_load_dwordx4 a[168:171], v239, s[58:59] offset:2048 sc1\n"
      "v_mfma_f32_16x16x32_bf16 v[4:7], a[196:199], v[216:219], v[4:7]\n"
      "v_mfma_f32_16x16x32_bf16 v[8:11], a[200:203], v[216:219], v[8:11]\n"
      "ds_read_b128 v[228:231], v232 offset:12288\n"
      "v_mfma_f32_16x16x32_bf16 v[12:15], a[204:207], v[216:219], v[12:15]\n"
      "v_mfma_f32_16x16x32_bf16 v[16:19], a[208:211], v[216:219], v[16:19]\n"
      "global_load_dwordx4 a[172:175], v239, s[58:59] offset:3072 sc1\n"
      "v_mfma_f32_16x16x32_bf16 v[20:23], a[212:215], v[216:219], v[20:23]\n"
      "s_waitcnt lgkmcnt(2)\n"
      "v_mfma_f32_16x16x32_bf16 v[24:27], a[192:195], v[220:223], v[24:27]\n"
      "v_mfma_f32_16x16x32_bf16 v[28:31], a[196:199], v[220:223], v[28:31]\n"
      "v_mfma_f32_16x16x32_bf16 v[32:35], a[200:203], v[220:223], v[32:35]\n"
      "ds_read_b128 v[216:219], v232 offset:16384\n"
      "s_add_u32 s58, s24, 0x9000\n"
      "s_addc_u32 s59, s25, 0\n"
      "global_load_dwordx4 a[176:179], v239, s[58:59] offset:0 sc1\n"
      "v_mfma_f32_16x16x32_bf16 v[36:39], a[204:207], v[220:223], v[36:39]\n"
      "v_mfma_f32_16x16x32_bf16 v[40:43], a[208:211], v[220:223], v[40:43]\n"
      "v_mfma_f32_16x16x32_bf16 v[44:47], a[212:215], v[220:223], v[44:47]\n"
      "s_waitcnt lgkmcnt(2)\n"
      "v_mfma_f32_16x16x32_bf16 v[48:51], a[192:195], v[224:227], v[48:51]\n"
      "global_load_dwordx4 a[180:183], v239, s[58:59] offset:1024 sc1\n"
      "v_mfma_f32_16x16x32_bf16 v[52:55], a[196:199], v[224:227], v[52:55]\n"
      "v_mfma_f32_16x16x32_bf16 v[56:59], a[200:203], v[224:227], v[56:59]\n"
      "ds_read_b128 v[220:223], v232 offset:20480\n"
      "v_mfma_f32_16x16x32_bf16 v[60:63], a[204:207], v[224:227], v[60:63]\n"
      "v_mfma_f32_16x16x32_bf16 v[64:67], a[208:211], v[224:227], v[64:67]\n"
      "global_load_dwordx4 a[184:187], v239, s[58:59] offset:2048 sc1\n"
      "v_mfma_f32_16x16x32_bf16 v[68:71], a[212:215], v[224:227], v[68:71]\n"
      "s_waitcnt lgkmcnt(2)\n"
      "v_mfma_f32_16x16x32_bf16 v[72:75], a[192:195], v[228:231], v[72:75]\n"
      "v_mfma_f32_16x16x32_bf16 v[76:79], a[196:199], v[228:231], v[76:79]\n"
      "v_mfma_f32_16x16x32_bf16 v[80:83], a[200:203], v[228:231], v[80:83]\n"
      "ds_read_b128 v[224:227], v232 offset:24576\n"
      "global_load_dwordx4 a[188:191], v239, s[58:59] offset:3072 sc1\n"
      "v_mfma_f32_16x16x32_bf16 v[84:87], a[204:207], v[228:231], v[84:87]\n"
      "v_mfma_f32_16x16x32_bf16 v[88:91], a[208:211], v[228:231], v[88:91]\n"
      "v_mfma_f32_16x16x32_bf16 v[92:95], a[212:215], v[228:231], v[92:95]\n"
      "s_waitcnt lgkmcnt(2)\n"
      "v_mfma_f32_16x16x32_bf16 v[96:99], a[192:195], v[216:219], v[96:99]\n"
      "s_mul_i32 s78, s98, s19\n"
      "v_mfma_f32_16x16x32_bf16 v[100:103], a[196:199], v[216:219], v[100:103]\n"
      "v_add_u32 v236, s78, v236\n"
      "v_mfma_f32_16x16x32_bf16 v[104:107], a[200:203], v[216:219], v[104:107]\n"
      "ds_read_b128 v[228:231], v232 offset:28672\n"
      "s_mul_i32 s78, s78, s99\n"
      "v_mfma_f32_16x16x32_bf16 v[108:111], a[204:207], v[216:219], v[108:111]\n"
      "v_add_u32 v237, s78, v237\n"
      "v_mfma_f32_16x16x32_bf16 v[112:115], a[208:211], v[216:219], v[112:115]\n"
      "s_add_u32 s52, s20, s32\n"
      "s_addc_u32 s53, s21, 0\n"
      "s_add_u32 m0, s17, 0x0\n"
      "v_mfma_f32_16x16x32_bf16 v[116:119], a[212:215], v[216:219], v[116:119]\n"
      "global_load_lds_dwordx4 v236, s[52:53] offset:0 nt\n"
      "s_waitcnt lgkmcnt(2)\n"
      "v_mfma_f32_16x16x32_bf16 v[120:123], a[192:195], v[220:223], v[120:123]\n"
      "v_mfma_f32_16x16x32_bf16 v[124:127], a[196:199], v[220:223], v[124:127]\n"
      "s_add_u32 m0, s17, 0x1f00\n"
      "v_mfma_f32_16x16x32_bf16 v[128:131], a[200:203], v[220:223], v[128:131]\n"
      "ds_read_b128 v[216:219], v232 offset:32768\n"
      "v_add_u32 v232, v249, v232\n"
      "global_load_lds_dwordx4 v236, s[52:53] offset:256 nt\n"
      "v_mfma_f32_16x16x32_bf16 v[132:135], a[204:207], v[220:223], v[132:135]\n"
      "v_mfma_f32_16x16x32_bf16 v[136:139], a[208:211], v[220:223], v[136:139]\n"
      "s_add_u32 m0, s17, 0x3e00\n"
      "v_mfma_f32_16x16x32_bf16 v[140:143], a[212:215], v[220:223], v[140:143]\n"
      "global_load_lds_dwordx4 v236, s[52:53] offset:512 nt\n"
      "s_waitcnt lgkmcnt(2)\n"
      "v_mfma_f32_16x16x32_bf16 v[144:147], a[192:195], v[224:227], v[144:147]\n"
      "v_mfma_f32_16x16x32_bf16 v[148:151], a[196:199], v[224:227], v[148:151]\n"
      "s_add_u32 m0, s17, 0x1000\n"
      "v_mfma_f32_16x16x32_bf16 v[152:155], a[200:203], v[224:227], v[152:155]\n"
      "ds_read_b128 v[220:223], v233 offset:0\n"
      "global_load_lds_dwordx4 v237, s[52:53] offset:0 nt\n"
      "v_mfma_f32_16x16x32_bf16 v[156:159], a[204:207], v[224:227], v[156:159]\n"
      "v_mfma_f32_16x16x32_bf16 v[160:163], a[208:211], v[224:227], v[160:163]\n"
      "s_add_u32 m0, s17, 0x2f00\n"
      "v_mfma_f32_16x16x32_bf16 v[164:167], a[212:215], v[224:227], v[164:167]\n"
      "global_load_lds_dwordx4 v237, s[52:53] offset:256 nt\n"
      "s_waitcnt lgkmcnt(2)\n"
      "v_mfma_f32_16x16x32_bf16 v[168:171], a[192:195], v[228:231], v[168:171]\n"
      "v_mfma_f32_16x16x32_bf16 v[172:175], a[196:199], v[228:231], v[172:175]\n"
      "s_add_u32 m0, s17, 0x4e00\n"
      "v_mfma_f32_16x16x32_bf16 v[176:179], a[200:203], v[228:231], v[176:179]\n"
      "ds_read_b128 v[224:227], v233 offset:4096\n"
      "global_load_lds_dwordx4 v237, s[52:53] offset:512 nt\n"
      "v_mfma_f32_16x16x32_bf16 v[180:183], a[204:207], v[228:231], v[180:183]\n"
      "v_mfma_f32_16x16x32_bf16 v[184:187], a[208:211], v[228:231], v[184:187]\n"
      "v_mfma_f32_16x16x32_bf16 v[188:191], a[212:215], v[228:231], v[188:191]\n"
      "s_waitcnt lgkmcnt(2)\n"
      "v_mfma_f32_16x16x32_bf16 v[192:195], a[192:195], v[216:219], v[192:195]\n"
      "v_mfma_f32_16x16x32_bf16 v[196:199], a[196:199], v[216:219], v[196:199]\n"
      "v_mfma_f32_16x16x32_bf16 v[200:203], a[200:203], v[216:219], v[200:203]\n"
      "ds_read_b128 v[228:231], v233 offset:8192\n"
      "v_mfma_f32_16x16x32_bf16 v[204:207], a[204:207], v[216:219], v[204:207]\n"
      "v_mfma_f32_16x16x32_bf16 v[208:211], a[208:211], v[216:219], v[208:211]\n"
      "v_mfma_f32_16x16x32_bf16 v[212:215], a[212:215], v[216:219], v[212:215]\n"
      "s_waitcnt lgkmcnt(2)\n"
      "v_mfma_f32_16x16x32_bf16 v[0:3], a[216:219], v[220:223], v[0:3]\n"
      "s_add_u32 s58, s24, 0xa000\n"
      "s_addc_u32 s59, s25, 0\n"
      "global_load_dwordx4 a[192:195], v239, s[58:59] offset:0 sc1\n"
      "v_mfma_f32_16x16x32_bf16 v[4:7], a[220:223], v[220:223], v[4:7]\n"
      "v_mfma_f32_16x16x32_bf16 v[8:11], a[224:227], v[220:223], v[8:11]\n"
      "ds_read_b128 v[216:219], v233 offset:12288\n"
      "v_mfma_f32_16x16x32_bf16 v[12:15], a[228:231], v[220:223], v[12:15]\n"
      "v_mfma_f32_16x16x32_bf16 v[16:19], a[232:235], v[220:223], v[16:19]\n"
      "global_load_dwordx4 a[196:199], v239, s[58:59] offset:1024 sc1\n"
      "v_mfma_f32_16x16x32_bf16 v[20:23], a[236:239], v[220:223], v[20:23]\n"
      "s_waitcnt lgkmcnt(2)\n"
      "v_mfma_f32_16x16x32_bf16 v[24:27], a[216:219], v[224:227], v[24:27]\n"
      "v_mfma_f32_16x16x32_bf16 v[28:31], a[220:223], v[224:227], v[28:31]\n"
      "v_mfma_f32_16x16x32_bf16 v[32:35], a[224:227], v[224:227], v[32:35]\n"
      "ds_read_b128 v[220:223], v233 offset:16384\n"
      "global_load_dwordx4 a[200:203], v239, s[58:59] offset:2048 sc1\n"
      "v_mfma_f32_16x16x32_bf16 v[36:39], a[228:231], v[224:227], v[36:39]\n"
      "v_mfma_f32_16x16x32_bf16 v[40:43], a[232:235], v[224:227], v[40:43]\n"
      "v_mfma_f32_16x16x32_bf16 v[44:47], a[236:239], v[224:227], v[44:47]\n"
      "s_waitcnt lgkmcnt(2)\n"
      "v_mfma_f32_16x16x32_bf16 v[48:51], a[216:219], v[228:231], v[48:51]\n"
      "global_load_dwordx4 a[204:207], v239, s[58:59] offset:3072 sc1\n"
      "v_mfma_f32_16x16x32_bf16 v[52:55], a[220:223], v[228:231], v[52:55]\n"
      "v_mfma_f32_16x16x32_bf16 v[56:59], a[224:227], v[228:231], v[56:59]\n"
      "ds_read_b128 v[224:227], v233 offset:20480\n"
      "v_mfma_f32_16x16x32_bf16 v[60:63], a[228:231], v[228:231], v[60:63]\n"
      "v_mfma_f32_16x16x32_bf16 v[64:67], a[232:235], v[228:231], v[64:67]\n"
      "s_add_u32 s58, s24, 0xb000\n"
      "s_addc_u32 s59, s25, 0\n"
      "global_load_dwordx4 a[208:211], v239, s[58:59] offset:0 sc1\n"
      "v_mfma_f32_16x16x32_bf16 v[68:71], a[236:239], v[228:231], v[68:71]\n"
      "s_waitcnt lgkmcnt(2)\n"
      "v_mfma_f32_16x16x32_bf16 v[72:75], a[216:219], v[216:219], v[72:75]\n"
      "v_mfma_f32_16x16x32_bf16 v[76:79], a[220:223], v[216:219], v[76:79]\n"
      "v_mfma_f32_16x16x32_bf16 v[80:83], a[224:227], v[216:219], v[80:83]\n"
      "ds_read_b128 v[228:231], v233 offset:24576\n"
      "global_load_dwordx4 a[212:215], v239, s[58:59] offset:1024 sc1\n"
      "v_mfma_f32_16x16x32_bf16 v[84:87], a[228:231], v[216:219], v[84:87]\n"
      "v_mfma_f32_16x16x32_bf16 v[88:91], a[232:235], v[216:219], v[88:91]\n"
      "v_mfma_f32_16x16x32_bf16 v[92:95], a[236:239], v[216:219], v[92:95]\n"
      "s_waitcnt lgkmcnt(2)\n"
      "v_mfma_f32_16x16x32_bf16 v[96:99], a[216:219], v[220:223], v[96:99]\n"
      "v_mfma_f32_16x16x32_bf16 v[100:103], a[220:223], v[220:223], v[100:103]\n"
      "v_mfma_f32_16x16x32_bf16 v[104:107], a[224:227], v[220:223], v[104:107]\n"
      "ds_read_b128 v[216:219], v233 offset:28672\n"
      "v_mfma_f32_16x16x32_bf16 v[108:111], a[228:231], v[220:223], v[108:111]\n"
      "v_mfma_f32_16x16x32_bf16 v[112:115], a[232:235], v[220:223], v[112:115]\n"
      "v_mfma_f32_16x16x32_bf16 v[116:119], a[236:239], v[220:223], v[116:119]\n"
      "s_waitcnt lgkmcnt(2)\n"
      "v_mfma_f32_16x16x32_bf16 v[120:123], a[216:219], v[224:227], v[120:123]\n"
      "v_mfma_f32_16x16x32_bf16 v[124:127], a[220:223], v[224:227], v[124:127]\n"
      "v_mfma_f32_16x16x32_bf16 v[128:131], a[224:227], v[224:227], v[128:131]\n"
      "ds_read_b128 v[220:223], v233 offset:32768\n"
      "v_add_u32 v233, v249, v233\n"
      "v_mfma_f32_16x16x32_bf16 v[132:135], a[228:231], v[224:227], v[132:135]\n"
      "v_mfma_f32_16x16x32_bf16 v[136:139], a[232:235], v[224:227], v[136:139]\n"
      "v_mfma_f32_16x16x32_bf16 v[140:143], a[236:239], v[224:227], v[140:143]\n"
      "s_waitcnt lgkmcnt(2)\n"
      "v_mfma_f32_16x16x32_bf16 v[144:147], a[216:219], v[228:231], v[144:147]\n"
      "v_mfma_f32_16x16x32_bf16 v[148:151], a[220:223], v[228:231], v[148:151]\n"
      "v_mfma_f32_16x16x32_bf16 v[152:155], a[224:227], v[228:231], v[152:155]\n"
      "ds_read_b128 v[224:227], v234 offset:0\n"
      "v_mfma_f32_16x16x32_bf16 v[156:159], a[228:231], v[228:231], v[156:159]\n"
      "v_mfma_f32_16x16x32_bf16 v[160:163], a[232:235], v[228:231], v[160:163]\n"
      "v_mfma_f32_16x16x32_bf16 v[164:167], a[236:239], v[228:231], v[164:167]\n"
      "s_waitcnt lgkmcnt(2)\n"
      "v_mfma_f32_16x16x32_bf16 v[168:171], a[216:219], v[216:219], v[168:171]\n"
      "v_mfma_f32_16x16x32_bf16 v[172:175], a[220:223], v[216:219], v[172:175]\n"
      "v_mfma_f32_16x16x32_bf16 v[176:179], a[224:227], v[216:219], v[176:179]\n"
      "ds_read_b128 v[228:231], v234 offset:4096\n"
      "v_mfma_f32_16x16x32_bf16 v[180:183], a[228:231], v[216:219], v[180:183]\n"
      "v_mfma_f32_16x16x32_bf16 v[184:187], a[232:235], v[216:219], v[184:187]\n"
      "s_add_u32 s52, s42, s32\n"
      "s_addc_u32 s53, s43, 0\n"
      "s_add_u32 m0, s17, 0x6000\n"
      "v_mfma_f32_16x16x32_bf16 v[188:191], a[236:239], v[216:219], v[188:191]\n"
      "global_load_lds_dwordx4 v236, s[52:53] offset:0 nt\n"
      "s_waitcnt lgkmcnt(2)\n"
      "v_mfma_f32_16x16x32_bf16 v[192:195], a[216:219], v[220:223], v[192:195]\n"
      "v_mfma_f32_16x16x32_bf16 v[196:199], a[220:223], v[220:223], v[196:199]\n"
      "s_add_u32 m0, s17, 0x7f00\n"
      "v_mfma_f32_16x16x32_bf16 v[200:203], a[224:227], v[220:223], v[200:203]\n"
      "ds_read_b128 v[216:219], v234 offset:8192\n"
      "global_load_lds_dwordx4 v236, s[52:53] offset:256 nt\n"
      "v_mfma_f32_16x16x32_bf16 v[204:207], a[228:231], v[220:223], v[204:207]\n"
      "v_mfma_f32_16x16x32_bf16 v[208:211], a[232:235], v[220:223], v[208:211]\n"
      "s_add_u32 m0, s17, 0x9e00\n"
      "v_mfma_f32_16x16x32_bf16 v[212:215], a[236:239], v[220:223], v[212:215]\n"
      "global_load_lds_dwordx4 v236, s[52:53] offset:512 nt\n"
      "s_waitcnt lgkmcnt(2)\n"
      "v_mfma_f32_16x16x32_bf16 v[0:3], a[0:3], v[224:227], v[0:3]\n"
      "ds_read_b128 v[244:247], v250\n"
      "v_mfma_f32_16x16x32_bf16 v[4:7], a[4:7], v[224:227], v[4:7]\n"
      "s_add_u32 m0, s17, 0x7000\n"
      "ds_read_b64 v[252:253], v250 offset:4096\n"
      "v_mfma_f32_16x16x32_bf16 v[8:11], a[8:11], v[224:227], v[8:11]\n"
      "ds_read_b128 v[220:223], v234 offset:12288\n"
      "global_load_lds_dwordx4 v237, s[52:53] offset:0 nt\n"
      "v_mfma_f32_16x16x32_bf16 v[12:15], a[12:15], v[224:227], v[12:15]\n"
      "v_mfma_f32_16x16x32_bf16 v[16:19], a[16:19], v[224:227], v[16:19]\n"
      "s_add_u32 m0, s17, 0x8f00\n"
      "v_mfma_f32_16x16x32_bf16 v[20:23], a[20:23], v[224:227], v[20:23]\n"
      "global_load_lds_dwordx4 v237, s[52:53] offset:256 nt\n"
      "s_waitcnt lgkmcnt(4)\n"
      "v_mfma_f32_16x16x32_bf16 v[24:27], a[0:3], v[228:231], v[24:27]\n"
      "v_mfma_f32_16x16x32_bf16 v[28:31], a[4:7], v[228:231], v[28:31]\n"
      "s_add_u32 m0, s17, 0xae00\n"
      "v_mfma_f32_16x16x32_bf16 v[32:35], a[8:11], v[228:231], v[32:35]\n"
      "ds_read_b128 v[224:227], v234 offset:16384\n"
      "global_load_lds_dwordx4 v237, s[52:53] offset:512 nt\n"
      "v_mfma_f32_16x16x32_bf16 v[36:39], a[12:15], v[228:231], v[36:39]\n"
      "v_mfma_f32_16x16x32_bf16 v[40:43], a[16:19], v[228:231], v[40:43]\n"
      "v_mfma_f32_16x16x32_bf16 v[44:47], a[20:23], v[228:231], v[44:47]\n"
      "global_load_dwordx4 a[216:219], v239, s[58:59] offset:2048 sc1\n"
      "s_waitcnt lgkmcnt(4)\n"
      "v_mfma_f32_16x16x32_bf16 v[48:51], a[0:3], v[216:219], v[48:51]\n"
      "v_mfma_f32_16x16x32_bf16 v[52:55], a[4:7], v[216:219], v[52:55]\n"
      "v_mfma_f32_16x16x32_bf16 v[56:59], a[8:11], v[216:219], v[56:59]\n"
      "ds_read_b128 v[228:231], v234 offset:20480\n"
      "v_mfma_f32_16x16x32_bf16 v[60:63], a[12:15], v[216:219], v[60:63]\n"
      "global_load_dwordx4 a[220:223], v239, s[58:59] offset:3072 sc1\n"
      "v_mfma_f32_16x16x32_bf16 v[64:67], a[16:19], v[216:219], v[64:67]\n"
      "v_mfma_f32_16x16x32_bf16 v[68:71], a[20:23], v[216:219], v[68:71]\n"
      "s_waitcnt lgkmcnt(2)\n"
      "v_mfma_f32_16x16x32_bf16 v[72:75], a[0:3], v[220:223], v[72:75]\n"
      "v_mfma_f32_16x16x32_bf16 v[76:79], a[4:7], v[220:223], v[76:79]\n"
      "s_add_u32 s58, s24, 0xc000\n"
      "s_addc_u32 s59, s25, 0\n"
      "global_load_dwordx4 a[224:227], v239, s[58:59] offset:0 sc1\n"
      "v_mfma_f32_16x16x32_bf16 v[80:83], a[8:11], v[220:223], v[80:83]\n"
      "ds_read_b128 v[216:219], v234 offset:24576\n"
      "v_mfma_f32_16x16x32_bf16 v[84:87], a[12:15], v[220:223], v[84:87]\n"
      "v_mfma_f32_16x16x32_bf16 v[88:91], a[16:19], v[220:223], v[88:91]\n"
      "v_mfma_f32_16x16x32_bf16 v[92:95], a[20:23], v[220:223], v[92:95]\n"
      "global_load_dwordx4 a[228:231], v239, s[58:59] offset:1024 sc1\n"
      "s_waitcnt lgkmcnt(2)\n"
      "v_mfma_f32_16x16x32_bf16 v[96:99], a[0:3], v[224:227], v[96:99]\n"
      "v_mfma_f32_16x16x32_bf16 v[100:103], a[4:7], v[224:227], v[100:103]\n"
      "v_mfma_f32_16x16x32_bf16 v[104:107], a[8:11], v[224:227], v[104:107]\n"
      "ds_read_b128 v[220:223], v234 offset:28672\n"
      "v_mfma_f32_16x16x32_bf16 v[108:111], a[12:15], v[224:227], v[108:111]\n"
      "global_load_dwordx4 a[232:235], v239, s[58:59] offset:2048 sc1\n"
      "v_mfma_f32_16x16x32_bf16 v[112:115], a[16:19], v[224:227], v[112:115]\n"
      "v_mfma_f32_16x16x32_bf16 v[116:119], a[20:23], v[224:227], v[116:119]\n"
      "s_waitcnt lgkmcnt(2)\n"
      "v_mfma_f32_16x16x32_bf16 v[120:123], a[0:3], v[228:231], v[120:123]\n"
      "v_mfma_f32_16x16x32_bf16 v[124:127], a[4:7], v[228:231], v[124:127]\n"
      "global_load_dwordx4 a[236:239], v239, s[58:59] offset:3072 sc1\n"
      "v_mfma_f32_16x16x32_bf16 v[128:131], a[8:11], v[228:231], v[128:131]\n"
      "ds_read_b128 v[224:227], v234 offset:32768\n"
      "v_add_u32 v234, v249, v234\n"
      "v_mfma_f32_16x16x32_bf16 v[132:135], a[12:15], v[228:231], v[132:135]\n"
      "v_mfma_f32_16x16x32_bf16 v[136:139], a[16:19], v[228:231], v[136:139]\n"
      "v_mfma_f32_16x16x32_bf16 v[140:143], a[20:23], v[228:231], v[140:143]\n"
      "s_waitcnt lgkmcnt(2)\n"
      "v_mfma_f32_16x16x32_bf16 v[144:147], a[0:3], v[216:219], v[144:147]\n"
      "v_mfma_f32_16x16x32_bf16 v[148:151], a[4:7], v[216:219], v[148:151]\n"
      "v_mfma_f32_16x16x32_bf16 v[152:155], a[8:11], v[216:219], v[152:155]\n"
      "ds_read_b128 v[228:231], v235 offset:4096\n"
      "v_mfma_f32_16x16x32_bf16 v[156:159], a[12:15], v[216:219], v[156:159]\n"
      "v_mfma_f32_16x16x32_bf16 v[160:163], a[16:19], v[216:219], v[160:163]\n"
      "v_mfma_f32_16x16x32_bf16 v[164:167], a[20:23], v[216:219], v[164:167]\n"
      "s_waitcnt lgkmcnt(2)\n"
      "v_mfma_f32_16x16x32_bf16 v[168:171], a[0:3], v[220:223], v[168:171]\n"
      "v_mfma_f32_16x16x32_bf16 v[172:175], a[4:7], v[220:223], v[172:175]\n"
      "v_mfma_f32_16x16x32_bf16 v[176:179], a[8:11], v[220:223], v[176:179]\n"
      "ds_read_b128 v[216:219], v235 offset:8192\n"
      "v_mfma_f32_16x16x32_bf16 v[180:183], a[12:15], v[220:223], v[180:183]\n"
      "v_mfma_f32_16x16x32_bf16 v[184:187], a[16:19], v[220:223], v[184:187]\n"
      "v_mfma_f32_16x16x32_bf16 v[188:191], a[20:23], v[220:223], v[188:191]\n"
      "s_waitcnt lgkmcnt(2)\n"
      "v_mfma_f32_16x16x32_bf16 v[192:195], a[0:3], v[224:227], v[192:195]\n"
      "v_mfma_f32_16x16x32_bf16 v[196:199], a[4:7], v[224:227], v[196:199]\n"
      "v_mfma_f32_16x16x32_bf16 v[200:203], a[8:11], v[224:227], v[200:203]\n"
      "ds_read_b128 v[220:223], v235 offset:12288\n"
      "v_mfma_f32_16x16x32_bf16 v[204:207], a[12:15], v[224:227], v[204:207]\n"
      "v_mfma_f32_16x16x32_bf16 v[208:211], a[16:19], v[224:227], v[208:211]\n"
      "v_mfma_f32_16x16x32_bf16 v[212:215], a[20:23], v[224:227], v[212:215]\n"
      "s_waitcnt lgkmcnt(2)\n"
      "v_mfma_f32_16x16x32_bf16 v[24:27], a[24:27], v[228:231], v[24:27]\n"
      "s_add_u32 s58, s24, 0xd000\n"
      "s_addc_u32 s59, s25, 0\n"
      "global_load_dwordx4 a[0:3], v239, s[58:59] offset:0 sc1\n"
      "v_mfma_f32_16x16x32_bf16 v[28:31], a[28:31], v[228:231], v[28:31]\n"
      "v_mfma_f32_16x16x32_bf16 v[32:35], a[32:35], v[228:231], v[32:35]\n"
      "ds_read_b128 v[224:227], v235 offset:16384\n"
      "v_mfma_f32_16x16x32_bf16 v[36:39], a[36:39], v[228:231], v[36:39]\n"
      "v_mfma_f32_16x16x32_bf16 v[40:43], a[40:43], v[228:231], v[40:43]\n"
      "global_load_dwordx4 a[4:7], v239, s[58:59] offset:1024 sc1\n"
      "v_mfma_f32_16x16x32_bf16 v[44:47], a[44:47], v[228:231], v[44:47]\n"
      "s_waitcnt lgkmcnt(2)\n"
      "v_mfma_f32_16x16x32_bf16 v[48:51], a[24:27], v[216:219], v[48:51]\n"
      "v_cvt_pk_bf16_f32 v24, v24, v25\n"
      "v_cvt_pk_bf16_f32 v25, v26, v27\n"
      "v_mfma_f32_16x16x32_bf16 v[52:55], a[28:31], v[216:219], v[52:55]\n"
      "v_cvt_pk_bf16_f32 v26, v28, v29\n"
      "v_cvt_pk_bf16_f32 v27, v30, v31\n"
      "v_mfma_f32_16x16x32_bf16 v[56:59], a[32:35], v[216:219], v[56:59]\n"
      "ds_read_b128 v[228:231], v235 offset:20480\n"
      "v_cvt_pk_bf16_f32 v28, v32, v33\n"
      "v_cvt_pk_bf16_f32 v29, v34, v35\n"
      "global_load_dwordx4 a[8:11], v239, s[58:59] offset:2048 sc1\n"
      "v_mfma_f32_16x16x32_bf16 v[60:63], a[36:39], v[216:219], v[60:63]\n"
      "v_cvt_pk_bf16_f32 v30, v36, v37\n"
      "v_cvt_pk_bf16_f32 v31, v38, v39\n"
      "v_mfma_f32_16x16x32_bf16 v[64:67], a[40:43], v[216:219], v[64:67]\n"
      "v_cvt_pk_bf16_f32 v32, v40, v41\n"
      "v_cvt_pk_bf16_f32 v33, v42, v43\n"
      "v_mfma_f32_16x16x32_bf16 v[68:71], a[44:47], v[216:219], v[68:71]\n"
      "v_cvt_pk_bf16_f32 v34, v44, v45\n"
      "v_cvt_pk_bf16_f32 v35, v46, v47\n"
      "s_waitcnt lgkmcnt(2)\n"
      "v_mfma_f32_16x16x32_bf16 v[72:75], a[24:27], v[220:223], v[72:75]\n"
      "v_cvt_pk_bf16_f32 v48, v48, v49\n"
      "v_cvt_pk_bf16_f32 v49, v50, v51\n"
      "global_load_dwordx4 a[12:15], v239, s[58:59] offset:3072 sc1\n"
      "v_mfma_f32_16x16x32_bf16 v[76:79], a[28:31], v[220:223], v[76:79]\n"
      "v_cvt_pk_bf16_f32 v50, v52, v53\n"
      "v_cvt_pk_bf16_f32 v51, v54, v55\n"
      "v_mfma_f32_16x16x32_bf16 v[80:83], a[32:35], v[220:223], v[80:83]\n"
      "ds_read_b128 v[216:219], v235 offset:24576\n"
      "v_cvt_pk_bf16_f32 v52, v56, v57\n"
      "v_cvt_pk_bf16_f32 v53, v58, v59\n"
      "v_mfma_f32_16x16x32_bf16 v[84:87], a[36:39], v[220:223], v[84:87]\n"
      "v_cvt_pk_bf16_f32 v54, v60, v61\n"
      "v_cvt_pk_bf16_f32 v55, v62, v63\n"
      "v_mfma_f32_16x16x32_bf16 v[88:91], a[40:43], v[220:223], v[88:91]\n"
      "v_cvt_pk_bf16_f32 v56, v64, v65\n"
      "v_cvt_pk_bf16_f32 v57, v66, v67\n"
      "s_add_u32 s58, s24, 0xe000\n"
      "s_addc_u32 s59, s25, 0\n"
      "global_load_dwordx4 a[16:19], v239, s[58:59] offset:0 sc1\n"
      "v_mfma_f32_16x16x32_bf16 v[92:95], a[44:47], v[220:223], v[92:95]\n"
      "v_cvt_pk_bf16_f32 v58, v68, v69\n"
      "v_cvt_pk_bf16_f32 v59, v70, v71\n"
      "s_waitcnt lgkmcnt(2)\n"
      "v_mfma_f32_16x16x32_bf16 v[96:99], a[24:27], v[224:227], v[96:99]\n"
      "v_cvt_pk_bf16_f32 v72, v72, v73\n"
      "v_cvt_pk_bf16_f32 v73, v74, v75\n"
      "v_mfma_f32_16x16x32_bf16 v[100:103], a[28:31], v[224:227], v[100:103]\n"
      "v_cvt_pk_bf16_f32 v74, v76, v77\n"
      "v_cvt_pk_bf16_f32 v75, v78, v79\n"
      "v_mfma_f32_16x16x32_bf16 v[104:107], a[32:35], v[224:227], v[104:107]\n"
      "ds_read_b128 v[220:223], v235 offset:28672\n"
      "v_cvt_pk_bf16_f32 v76, v80, v81\n"
      "v_cvt_pk_bf16_f32 v77, v82, v83\n"
      "global_load_dwordx4 a[20:23], v239, s[58:59] offset:1024 sc1\n"
      "v_mfma_f32_16x16x32_bf16 v[108:111], a[36:39], v[224:227], v[108:111]\n"
      "v_cvt_pk_bf16_f32 v78, v84, v85\n"
      "v_cvt_pk_bf16_f32 v79, v86, v87\n"
      "v_mfma_f32_16x16x32_bf16 v[112:115], a[40:43], v[224:227], v[112:115]\n"
      "v_cvt_pk_bf16_f32 v80, v88, v89\n"
      "v_cvt_pk_bf16_f32 v81, v90, v91\n"
      "v_mfma_f32_16x16x32_bf16 v[116:119], a[44:47], v[224:227], v[116:119]\n"
      "v_cvt_pk_bf16_f32 v82, v92, v93\n"
      "v_cvt_pk_bf16_f32 v83, v94, v95\n"
      "s_waitcnt lgkmcnt(2)\n"
      "v_mfma_f32_16x16x32_bf16 v[120:123], a[24:27], v[228:231], v[120:123]\n"
      "v_cvt_pk_bf16_f32 v96, v96, v97\n"
      "v_cvt_pk_bf16_f32 v97, v98, v99\n"
      "v_mfma_f32_16x16x32_bf16 v[124:127], a[28:31], v[228:231], v[124:127]\n"
      "v_cvt_pk_bf16_f32 v98, v100, v101\n"
      "v_cvt_pk_bf16_f32 v99, v102, v103\n"
      "v_mfma_f32_16x16x32_bf16 v[128:131], a[32:35], v[228:231], v[128:131]\n"
      "ds_read_b128 v[224:227], v235 offset:32768\n"
      "v_cvt_pk_bf16_f32 v100, v104, v105\n"
      "v_cvt_pk_bf16_f32 v101, v106, v107\n"
      "v_mfma_f32_16x16x32_bf16 v[132:135], a[36:39], v[228:231], v[132:135]\n"
      "v_cvt_pk_bf16_f32 v102, v108, v109\n"
      "v_cvt_pk_bf16_f32 v103, v110, v111\n"
      "v_mfma_f32_16x16x32_bf16 v[136:139], a[40:43], v[228:231], v[136:139]\n"
      "v_cvt_pk_bf16_f32 v104, v112, v113\n"
      "v_cvt_pk_bf16_f32 v105, v114, v115\n"
      "v_mfma_f32_16x16x32_bf16 v[140:143], a[44:47], v[228:231], v[140:143]\n"
      "v_cvt_pk_bf16_f32 v106, v116, v117\n"
      "v_cvt_pk_bf16_f32 v107, v118, v119\n"
      "s_waitcnt lgkmcnt(2)\n"
      "v_mfma_f32_16x16x32_bf16 v[144:147], a[24:27], v[216:219], v[144:147]\n"
      "v_cvt_pk_bf16_f32 v120, v120, v121\n"
      "v_cvt_pk_bf16_f32 v121, v122, v123\n"
      "v_mfma_f32_16x16x32_bf16 v[148:151], a[28:31], v[216:219], v[148:151]\n"
      "v_cvt_pk_bf16_f32 v122, v124, v125\n"
      "v_cvt_pk_bf16_f32 v123, v126, v127\n"
      "v_mfma_f32_16x16x32_bf16 v[152:155], a[32:35], v[216:219], v[152:155]\n"
      "ds_read_b128 v[228:231], v235 offset:0\n"
      "v_add_u32 v235, v249, v235\n"
      "s_waitcnt vmcnt(24)\n"
      "s_barrier\n"
      "v_cvt_pk_bf16_f32 v124, v128, v129\n"
      "v_cvt_pk_bf16_f32 v125, v130, v131\n"
      "v_mfma_f32_16x16x32_bf16 v[156:159], a[36:39], v[216:219], v[156:159]\n"
      "ds_read_b128 v[36:39], v232 offset:0\n"
      "v_cvt_pk_bf16_f32 v126, v132, v133\n"
      "v_cvt_pk_bf16_f32 v127, v134, v135\n"
      "v_mfma_f32_16x16x32_bf16 v[160:163], a[40:43], v[216:219], v[160:163]\n"
      "v_cvt_pk_bf16_f32 v128, v136, v137\n"
      "v_cvt_pk_bf16_f32 v129, v138, v139\n"
      "v_mfma_f32_16x16x32_bf16 v[164:167], a[44:47], v[216:219], v[164:167]\n"
      "ds_read_b128 v[40:43], v233 offset:0\n"
      "v_cvt_pk_bf16_f32 v130, v140, v141\n"
      "v_cvt_pk_bf16_f32 v131, v142, v143\n"
      "s_waitcnt lgkmcnt(4)\n"
      "v_mfma_f32_16x16x32_bf16 v[168:171], a[24:27], v[220:223], v[168:171]\n"
      "v_cvt_pk_bf16_f32 v144, v144, v145\n"
      "v_cvt_pk_bf16_f32 v145, v146, v147\n"
      "global_load_dwordx4 v[140:143], v239, s[58:59] offset:2048 sc1\n"
      "v_mfma_f32_16x16x32_bf16 v[172:175], a[28:31], v[220:223], v[172:175]\n"
      "ds_read_b128 v[44:47], v234 offset:0\n"
      "v_cvt_pk_bf16_f32 v146, v148, v149\n"
      "v_cvt_pk_bf16_f32 v147, v150, v151\n"
      "v_mfma_f32_16x16x32_bf16 v[176:179], a[32:35], v[220:223], v[176:179]\n"
      "v_cvt_pk_bf16_f32 v148, v152, v153\n"
      "v_cvt_pk_bf16_f32 v149, v154, v155\n"
      "v_mfma_f32_16x16x32_bf16 v[180:183], a[36:39], v[220:223], v[180:183]\n"
      "ds_read_b128 v[60:63], v235 offset:0\n"
      "v_cvt_pk_bf16_f32 v150, v156, v157\n"
      "v_cvt_pk_bf16_f32 v151, v158, v159\n"
      "v_mfma_f32_16x16x32_bf16 v[184:187], a[40:43], v[220:223], v[184:187]\n"
      "v_cvt_pk_bf16_f32 v152, v160, v161\n"
      "v_cvt_pk_bf16_f32 v153, v162, v163\n"
      "v_mfma_f32_16x16x32_bf16 v[188:191], a[44:47], v[220:223], v[188:191]\n"
      "ds_read_b128 v[64:67], v232 offset:8192\n"
      "v_cvt_pk_bf16_f32 v154, v164, v165\n"
      "v_cvt_pk_bf16_f32 v155, v166, v167\n"
      "s_waitcnt lgkmcnt(6)\n"
      "v_mfma_f32_16x16x32_bf16 v[192:195], a[24:27], v[224:227], v[192:195]\n"
      "v_cvt_pk_bf16_f32 v168, v168, v169\n"
      "v_cvt_pk_bf16_f32 v169, v170, v171\n"
      "global_load_dwordx4 v[156:159], v239, s[58:59] offset:3072 sc1\n"
      "v_mfma_f32_16x16x32_bf16 v[196:199], a[28:31], v[224:227], v[196:199]\n"
      "ds_read_b128 v[68:71], v233 offset:8192\n"
      "v_cvt_pk_bf16_f32 v170, v172, v173\n"
      "v_cvt_pk_bf16_f32 v171, v174, v175\n"
      "v_mfma_f32_16x16x32_bf16 v[200:203], a[32:35], v[224:227], v[200:203]\n"
      "v_cvt_pk_bf16_f32 v172, v176, v177\n"
      "v_cvt_pk_bf16_f32 v173, v178, v179\n"
      "v_mfma_f32_16x16x32_bf16 v[204:207], a[36:39], v[224:227], v[204:207]\n"
      "ds_read_b128 v[84:87], v234 offset:8192\n"
      "v_cvt_pk_bf16_f32 v174, v180, v181\n"
      "v_cvt_pk_bf16_f32 v175, v182, v183\n"
      "v_mfma_f32_16x16x32_bf16 v[208:211], a[40:43], v[224:227], v[208:211]\n"
      "v_cvt_pk_bf16_f32 v176, v184, v185\n"
      "v_cvt_pk_bf16_f32 v177, v186, v187\n"
      "s_add_u32 s58, s24, 0xf000\n"
      "s_addc_u32 s59, s25, 0\n"
      "global_load_dwordx4 v[160:163], v239, s[58:59] offset:0 sc1\n"
      "v_mfma_f32_16x16x32_bf16 v[212:215], a[44:47], v[224:227], v[212:215]\n"
      "ds_read_b128 v[88:91], v235 offset:8192\n"
      "v_cvt_pk_bf16_f32 v178, v188, v189\n"
      "v_cvt_pk_bf16_f32 v179, v190, v191\n"
      "s_waitcnt lgkmcnt(8)\n"
      "v_mfma_f32_16x16x32_bf16 v[0:3], a[24:27], v[228:231], v[0:3]\n"
      "v_cvt_pk_bf16_f32 v192, v192, v193\n"
      "v_cvt_pk_bf16_f32 v193, v194, v195\n"
      "v_mfma_f32_16x16x32_bf16 v[4:7], a[28:31], v[228:231], v[4:7]\n"
      "ds_read_b128 v[92:95], v232 offset:16384\n"
      "v_cvt_pk_bf16_f32 v194, v196, v197\n"
      "v_cvt_pk_bf16_f32 v195, v198, v199\n"
      "v_mfma_f32_16x16x32_bf16 v[8:11], a[32:35], v[228:231], v[8:11]\n"
      "v_cvt_pk_bf16_f32 v196, v200, v201\n"
      "v_cvt_pk_bf16_f32 v197, v202, v203\n"
      "global_load_dwordx4 v[164:167], v239, s[58:59] offset:1024 sc1\n"
      "v_mfma_f32_16x16x32_bf16 v[12:15], a[36:39], v[228:231], v[12:15]\n"
      "ds_read_b128 v[108:111], v233 offset:16384\n"
      "v_cvt_pk_bf16_f32 v198, v204, v205\n"
      "v_cvt_pk_bf16_f32 v199, v206, v207\n"
      "v_mfma_f32_16x16x32_bf16 v[16:19], a[40:43], v[228:231], v[16:19]\n"
      "ds_read_b128 v[112:115], v234 offset:16384\n"
      "v_cvt_pk_bf16_f32 v200, v208, v209\n"
      "v_cvt_pk_bf16_f32 v201, v210, v211\n"
      "v_mfma_f32_16x16x32_bf16 v[20:23], a[44:47], v[228:231], v[20:23]\n"
      "ds_read_b128 v[116:119], v235 offset:16384\n"
      "v_cvt_pk_bf16_f32 v202, v212, v213\n"
      "v_cvt_pk_bf16_f32 v203, v214, v215\n"
      "s_waitcnt lgkmcnt(8)\n"
      "v_mfma_f32_16x16x32_bf16 v[132:135], a[240:243], v[36:39], v[0:3]\n"
      "global_load_dwordx4 v[180:183], v239, s[58:59] offset:2048 sc1\n"
      "s_mov_b64 vcc, 0\n"
      "v_mfma_f32_16x16x32_bf16 v[132:135], a[244:247], v[40:43], v[132:135]\n"
      "v_mfma_f32_16x16x32_bf16 v[132:135], a[248:251], v[44:47], v[132:135]\n"
      "v_mfma_f32_16x16x32_bf16 v[132:135], a[252:255], v[60:63], v[132:135]\n"
      "global_load_dwordx4 v[184:187], v239, s[58:59] offset:3072 sc1\n"
      "s_waitcnt lgkmcnt(4)\n"
      "v_mfma_f32_16x16x32_bf16 v[132:135], a[48:51], v[64:67], v[132:135]\n"
      "v_mfma_f32_16x16x32_bf16 v[132:135], a[52:55], v[68:71], v[132:135]\n"
      "v_mfma_f32_16x16x32_bf16 v[132:135], a[56:59], v[84:87], v[132:135]\n"
      "s_add_u32 s58, s24, 0x10000\n"
      "s_addc_u32 s59, s25, 0\n"
      "global_load_dwordx4 v[188:191], v239, s[58:59] offset:0 sc1\n"
      "v_mfma_f32_16x16x32_bf16 v[132:135], a[60:63], v[88:91], v[132:135]\n"
      "s_waitcnt lgkmcnt(0)\n"
      "v_mfma_f32_16x16x32_bf16 v[132:135], a[64:67], v[92:95], v[132:135]\n"
      "v_mfma_f32_16x16x32_bf16 v[132:135], a[68:71], v[108:111], v[132:135]\n"
      "global_load_dwordx4 v[204:207], v239, s[58:59] offset:1024 sc1\n"
      "v_mfma_f32_16x16x32_bf16 v[132:135], a[72:75], v[112:115], v[132:135]\n"
      "v_mfma_f32_16x16x32_bf16 v[132:135], a[76:79], v[116:119], v[132:135]\n"
      "v_mfma_f32_16x16x32_bf16 v[136:139], a[80:83], v[36:39], v[4:7]\n"
      "global_load_dwordx4 a[24:27], v239, s[58:59] offset:2048 sc1\n"
      "v_mfma_f32_16x16x32_bf16 v[136:139], a[84:87], v[40:43], v[136:139]\n"
      "v_mfma_f32_16x16x32_bf16 v[136:139], a[88:91], v[44:47], v[136:139]\n"
      "v_mfma_f32_16x16x32_bf16 v[136:139], a[92:95], v[60:63], v[136:139]\n"
      "global_load_dwordx4 a[28:31], v239, s[58:59] offset:3072 sc1\n"
      "v_mfma_f32_16x16x32_bf16 v[136:139], a[96:99], v[64:67], v[136:139]\n"
      "v_cvt_pk_bf16_f32 v208, v132, v133\n"
      "v_mfma_f32_16x16x32_bf16 v[136:139], a[100:103], v[68:71], v[136:139]\n"
      "v_cvt_pk_bf16_f32 v209, v134, v135\n"
      "v_mfma_f32_16x16x32_bf16 v[136:139], a[104:107], v[84:87], v[136:139]\n"
      "s_add_u32 s58, s24, 0x11000\n"
      "s_addc_u32 s59, s25, 0\n"
      "global_load_dwordx4 a[32:35], v239, s[58:59] offset:0 sc1\n"
      "v_mfma_f32_16x16x32_bf16 v[136:139], a[108:111], v[88:91], v[136:139]\n"
      "v_mfma_f32_16x16x32_bf16 v[136:139], a[112:115], v[92:95], v[136:139]\n"
      "v_mfma_f32_16x16x32_bf16 v[136:139], a[116:119], v[108:111], v[136:139]\n"
      "global_load_dwordx4 a[36:39], v239, s[58:59] offset:1024 sc1\n"
      "v_mfma_f32_16x16x32_bf16 v[136:139], a[120:123], v[112:115], v[136:139]\n"
      "ds_write_b64 v244, v[208:209] offset:12288\n"
      "v_mfma_f32_16x16x32_bf16 v[136:139], a[124:127], v[116:119], v[136:139]\n"
      "v_mfma_f32_16x16x32_bf16 v[132:135], a[128:131], v[36:39], v[8:11]\n"
      "global_load_dwordx4 a[40:43], v239, s[58:59] offset:2048 sc1\n"
      "v_mfma_f32_16x16x32_bf16 v[132:135], a[132:135], v[40:43], v[132:135]\n"
      "v_mfma_f32_16x16x32_bf16 v[132:135], a[136:139], v[44:47], v[132:135]\n"
      "v_mfma_f32_16x16x32_bf16 v[132:135], a[140:143], v[60:63], v[132:135]\n"
      "global_load_dwordx4 a[44:47], v239, s[58:59] offset:3072 sc1\n"
      "v_mfma_f32_16x16x32_bf16 v[132:135], a[144:147], v[64:67], v[132:135]\n"
      "v_cvt_pk_bf16_f32 v210, v136, v137\n"
      "v_mfma_f32_16x16x32_bf16 v[132:135], a[148:151], v[68:71], v[132:135]\n"
      "v_cvt_pk_bf16_f32 v211, v138, v139\n"
      "v_mfma_f32_16x16x32_bf16 v[132:135], a[152:155], v[84:87], v[132:135]\n"
      "s_mul_i32 s78, s90, s19\n"
      "v_mfma_f32_16x16x32_bf16 v[132:135], a[156:159], v[88:91], v[132:135]\n"
      "v_add_u32 v236, s78, v236\n"
      "v_mfma_f32_16x16x32_bf16 v[132:135], a[160:163], v[92:95], v[132:135]\n"
      "s_mul_i32 s78, s78, s99\n"
      "v_mfma_f32_16x16x32_bf16 v[132:135], a[164:167], v[108:111], v[132:135]\n"
      "v_add_u32 v237, s78, v237\n"
      "v_mfma_f32_16x16x32_bf16 v[132:135], a[168:171], v[112:115], v[132:135]\n"
      "s_add_u32 s52, s20, s33\n"
      "s_addc_u32 s53, s21, 0\n"
      "s_add_u32 m0, s17, 0xc000\n"
      "v_mfma_f32_16x16x32_bf16 v[132:135], a[172:175], v[116:119], v[132:135]\n"
      "global_load_lds_dwordx4 v236, s[52:53] offset:0 nt\n"
      "v_mfma_f32_16x16x32_bf16 v[136:139], a[176:179], v[36:39], v[12:15]\n"
      "ds_write_b64 v244, v[210:211] offset:12296\n"
      "v_mfma_f32_16x16x32_bf16 v[136:139], a[180:183], v[40:43], v[136:139]\n"
      "v_mfma_f32_16x16x32_bf16 v[136:139], a[184:187], v[44:47], v[136:139]\n"
      "v_mfma_f32_16x16x32_bf16 v[136:139], a[188:191], v[60:63], v[136:139]\n"
      "v_cvt_pk_bf16_f32 v212, v132, v133\n"
      "s_waitcnt vmcnt(35)\n"
      "v_mfma_f32_16x16x32_bf16 v[136:139], a[192:195], v[64:67], v[136:139]\n"
      "s_add_u32 m0, s17, 0xdf00\n"
      "v_mfma_f32_16x16x32_bf16 v[136:139], a[196:199], v[68:71], v[136:139]\n"
      "global_load_lds_dwordx4 v236, s[52:53] offset:256 nt\n"
      "v_mfma_f32_16x16x32_bf16 v[136:139], a[200:203], v[84:87], v[136:139]\n"
      "v_cvt_pk_bf16_f32 v213, v134, v135\n"
      "v_mfma_f32_16x16x32_bf16 v[136:139], a[204:207], v[88:91], v[136:139]\n"
      "s_waitcnt vmcnt(26)\n"
      "v_mfma_f32_16x16x32_bf16 v[136:139], a[208:211], v[92:95], v[136:139]\n"
      "v_mfma_f32_16x16x32_bf16 v[136:139], a[212:215], v[108:111], v[136:139]\n"
      "v_mfma_f32_16x16x32_bf16 v[136:139], a[216:219], v[112:115], v[136:139]\n"
      "s_add_u32 m0, s17, 0xfe00\n"
      "v_mfma_f32_16x16x32_bf16 v[136:139], a[220:223], v[116:119], v[136:139]\n"
      "global_load_lds_dwordx4 v236, s[52:53] offset:512 nt\n"
      "s_waitcnt vmcnt(23)\n"
      "v_mfma_f32_16x16x32_bf16 v[132:135], a[224:227], v[36:39], v[16:19]\n"
      "ds_write_b64 v245, v[212:213] offset:12288\n"
      "v_mfma_f32_16x16x32_bf16 v[132:135], a[228:231], v[40:43], v[132:135]\n"
      "v_mfma_f32_16x16x32_bf16 v[132:135], a[232:235], v[44:47], v[132:135]\n"
      "v_mfma_f32_16x16x32_bf16 v[132:135], a[236:239], v[60:63], v[132:135]\n"
      "v_cvt_pk_bf16_f32 v214, v136, v137\n"
      "s_waitcnt vmcnt(19)\n"
      "v_mfma_f32_16x16x32_bf16 v[132:135], a[0:3], v[64:67], v[132:135]\n"
      "s_add_u32 m0, s17, 0xd000\n"
      "v_mfma_f32_16x16x32_bf16 v[132:135], a[4:7], v[68:71], v[132:135]\n"
      "global_load_lds_dwordx4 v237, s[52:53] offset:0 nt\n"
      "v_mfma_f32_16x16x32_bf16 v[132:135], a[8:11], v[84:87], v[132:135]\n"
      "v_cvt_pk_bf16_f32 v215, v138, v139\n"
      "v_mfma_f32_16x16x32_bf16 v[132:135], a[12:15], v[88:91], v[132:135]\n"
      "s_waitcnt vmcnt(16)\n"
      "v_mfma_f32_16x16x32_bf16 v[132:135], a[16:19], v[92:95], v[132:135]\n"
      "v_mfma_f32_16x16x32_bf16 v[132:135], a[20:23], v[108:111], v[132:135]\n"
      "v_mfma_f32_16x16x32_bf16 v[132:135], v[140:143], v[112:115], v[132:135]\n"
      "s_add_u32 m0, s17, 0xef00\n"
      "v_mfma_f32_16x16x32_bf16 v[132:135], v[156:159], v[116:119], v[132:135]\n"
      "s_barrier\n"
      "global_load_lds_dwordx4 v237, s[52:53] offset:256 nt\n"
      "s_waitcnt vmcnt(13)\n"
      "v_mfma_f32_16x16x32_bf16 v[136:139], v[160:163], v[36:39], v[20:23]\n"
      "ds_read_b128 v[36:39], v232 offset:256\n"
      "ds_write_b64 v245, v[214:215] offset:12296\n"
      "v_mfma_f32_16x16x32_bf16 v[136:139], v[164:167], v[40:43], v[136:139]\n"
      "ds_read_b128 v[40:43], v233 offset:256\n"
      "v_mfma_f32_16x16x32_bf16 v[136:139], v[180:183], v[44:47], v[136:139]\n"
      "ds_read_b128 v[44:47], v234 offset:256\n"
      "v_mfma_f32_16x16x32_bf16 v[136:139], v[184:187], v[60:63], v[136:139]\n"
      "ds_read_b128 v[60:63], v235 offset:256\n"
      "v_cvt_pk_bf16_f32 v208, v132, v133\n"
      "s_waitcnt vmcnt(9)\n"
      "v_mfma_f32_16x16x32_bf16 v[136:139], v[188:191], v[64:67], v[136:139]\n"
      "ds_read_b128 v[64:67], v232 offset:8448\n"
      "s_add_u32 m0, s17, 0x10e00\n"
      "v_mfma_f32_16x16x32_bf16 v[136:139], v[204:207], v[68:71], v[136:139]\n"
      "ds_read_b128 v[68:71], v233 offset:8448\n"
      "global_load_lds_dwordx4 v237, s[52:53] offset:512 nt\n"
      "v_mfma_f32_16x16x32_bf16 v[136:139], a[24:27], v[84:87], v[136:139]\n"
      "ds_read_b128 v[84:87], v234 offset:8448\n"
      "v_cvt_pk_bf16_f32 v209, v134, v135\n"
      "v_mfma_f32_16x16x32_bf16 v[136:139], a[28:31], v[88:91], v[136:139]\n"
      "ds_read_b128 v[88:91], v235 offset:8448\n"
      "s_waitcnt vmcnt(6)\n"
      "v_mfma_f32_16x16x32_bf16 v[136:139], a[32:35], v[92:95], v[136:139]\n"
      "ds_read_b128 v[92:95], v232 offset:16640\n"
      "v_mfma_f32_16x16x32_bf16 v[136:139], a[36:39], v[108:111], v[136:139]\n"
      "ds_read_b128 v[108:111], v233 offset:16640\n"
      "v_mfma_f32_16x16x32_bf16 v[136:139], a[40:43], v[112:115], v[136:139]\n"
      "ds_read_b128 v[112:115], v234 offset:16640\n"
      "v_mfma_f32_16x16x32_bf16 v[136:139], a[44:47], v[116:119], v[136:139]\n"
      "ds_read_b128 v[116:119], v235 offset:16640\n"
      "s_waitcnt lgkmcnt(8)\n"
      "v_mfma_f32_16x16x32_bf16 v[132:135], a[240:243], v[36:39], v[0:3]\n"
      "s_add_u32 s52, s42, s33\n"
      "s_addc_u32 s53, s43, 0\n"
      "s_add_u32 m0, s17, 0x12000\n"
      "v_mfma_f32_16x16x32_bf16 v[132:135], a[244:247], v[40:43], v[132:135]\n"
      "global_load_lds_dwordx4 v236, s[52:53] offset:0 nt\n"
      "v_mfma_f32_16x16x32_bf16 v[132:135], a[248:251], v[44:47], v[132:135]\n"
      "ds_write_b64 v244, v[208:209] offset:12416\n"
      "v_mfma_f32_16x16x32_bf16 v[132:135], a[252:255], v[60:63], v[132:135]\n"
      "v_cvt_pk_bf16_f32 v210, v136, v137\n"
      "s_waitcnt lgkmcnt(5)\n"
      "v_mfma_f32_16x16x32_bf16 v[132:135], a[48:51], v[64:67], v[132:135]\n"
      "v_cvt_pk_bf16_f32 v211, v138, v139\n"
      "v_mfma_f32_16x16x32_bf16 v[132:135], a[52:55], v[68:71], v[132:135]\n"
      "v_mfma_f32_16x16x32_bf16 v[132:135], a[56:59], v[84:87], v[132:135]\n"
      "s_add_u32 m0, s17, 0x13f00\n"
      "v_mfma_f32_16x16x32_bf16 v[132:135], a[60:63], v[88:91], v[132:135]\n"
      "global_load_lds_dwordx4 v236, s[52:53] offset:256 nt\n"
      "s_waitcnt lgkmcnt(1)\n"
      "v_mfma_f32_16x16x32_bf16 v[132:135], a[64:67], v[92:95], v[132:135]\n"
      "v_mfma_f32_16x16x32_bf16 v[132:135], a[68:71], v[108:111], v[132:135]\n"
      "ds_write_b64 v244, v[210:211] offset:12424\n"
      "v_mfma_f32_16x16x32_bf16 v[132:135], a[72:75], v[112:115], v[132:135]\n"
      "v_mfma_f32_16x16x32_bf16 v[132:135], a[76:79], v[116:119], v[132:135]\n"
      "v_mfma_f32_16x16x32_bf16 v[136:139], a[80:83], v[36:39], v[4:7]\n"
      "s_add_u32 m0, s17, 0x15e00\n"
      "v_mfma_f32_16x16x32_bf16 v[136:139], a[84:87], v[40:43], v[136:139]\n"
      "global_load_lds_dwordx4 v236, s[52:53] offset:512 nt\n"
      "v_mfma_f32_16x16x32_bf16 v[136:139], a[88:91], v[44:47], v[136:139]\n"
      "v_mfma_f32_16x16x32_bf16 v[136:139], a[92:95], v[60:63], v[136:139]\n"
      "v_cvt_pk_bf16_f32 v208, v132, v133\n"
      "v_mfma_f32_16x16x32_bf16 v[136:139], a[96:99], v[64:67], v[136:139]\n"
      "v_cvt_pk_bf16_f32 v209, v134, v135\n"
      "v_mfma_f32_16x16x32_bf16 v[136:139], a[100:103], v[68:71], v[136:139]\n"
      "v_mfma_f32_16x16x32_bf16 v[136:139], a[104:107], v[84:87], v[136:139]\n"
      "s_add_u32 m0, s17, 0x13000\n"
      "v_mfma_f32_16x16x32_bf16 v[136:139], a[108:111], v[88:91], v[136:139]\n"
      "global_load_lds_dwordx4 v237, s[52:53] offset:0 nt\n"
      "v_mfma_f32_16x16x32_bf16 v[136:139], a[112:115], v[92:95], v[136:139]\n"
      "v_mfma_f32_16x16x32_bf16 v[136:139], a[116:119], v[108:111], v[136:139]\n"
      "ds_write_b64 v244, v[208:209] offset:24576\n"
      "v_mfma_f32_16x16x32_bf16 v[136:139], a[120:123], v[112:115], v[136:139]\n"
      "v_mfma_f32_16x16x32_bf16 v[136:139], a[124:127], v[116:119], v[136:139]\n"
      "v_mfma_f32_16x16x32_bf16 v[132:135], a[128:131], v[36:39], v[8:11]\n"
      "s_add_u32 m0, s17, 0x14f00\n"
      "v_mfma_f32_16x16x32_bf16 v[132:135], a[132:135], v[40:43], v[132:135]\n"
      "global_load_lds_dwordx4 v237, s[52:53] offset:256 nt\n"
      "v_mfma_f32_16x16x32_bf16 v[132:135], a[136:139], v[44:47], v[132:135]\n"
      "v_mfma_f32_16x16x32_bf16 v[132:135], a[140:143], v[60:63], v[132:135]\n"
      "v_cvt_pk_bf16_f32 v210, v136, v137\n"
      "v_mfma_f32_16x16x32_bf16 v[132:135], a[144:147], v[64:67], v[132:135]\n"
      "v_cvt_pk_bf16_f32 v211, v138, v139\n"
      "v_mfma_f32_16x16x32_bf16 v[132:135], a[148:151], v[68:71], v[132:135]\n"
      "v_mfma_f32_16x16x32_bf16 v[132:135], a[152:155], v[84:87], v[132:135]\n"
      "s_add_u32 m0, s17, 0x16e00\n"
      "v_mfma_f32_16x16x32_bf16 v[132:135], a[156:159], v[88:91], v[132:135]\n"
      "global_load_lds_dwordx4 v237, s[52:53] offset:512 nt\n"
      "v_mfma_f32_16x16x32_bf16 v[132:135], a[160:163], v[92:95], v[132:135]\n"
      "v_mfma_f32_16x16x32_bf16 v[132:135], a[164:167], v[108:111], v[132:135]\n"
      "ds_write_b64 v244, v[210:211] offset:24584\n"
      "v_mfma_f32_16x16x32_bf16 v[132:135], a[168:171], v[112:115], v[132:135]\n"
      "v_mfma_f32_16x16x32_bf16 v[132:135], a[172:175], v[116:119], v[132:135]\n"
      "v_mfma_f32_16x16x32_bf16 v[136:139], a[176:179], v[36:39], v[12:15]\n"
      "v_mfma_f32_16x16x32_bf16 v[136:139], a[180:183], v[40:43], v[136:139]\n"
      "v_mfma_f32_16x16x32_bf16 v[136:139], a[184:187], v[44:47], v[136:139]\n"
      "v_mfma_f32_16x16x32_bf16 v[136:139], a[188:191], v[60:63], v[136:139]\n"
      "v_cvt_pk_bf16_f32 v212, v132, v133\n"
      "v_mfma_f32_16x16x32_bf16 v[136:139], a[192:195], v[64:67], v[136:139]\n"
      "v_cvt_pk_bf16_f32 v213, v134, v135\n"
      "v_mfma_f32_16x16x32_bf16 v[136:139], a[196:199], v[68:71], v[136:139]\n"
      "v_mfma_f32_16x16x32_bf16 v[136:139], a[200:203], v[84:87], v[136:139]\n"
      "v_mfma_f32_16x16x32_bf16 v[136:139], a[204:207], v[88:91], v[136:139]\n"
      "v_mfma_f32_16x16x32_bf16 v[136:139], a[208:211], v[92:95], v[136:139]\n"
      "v_mfma_f32_16x16x32_bf16 v[136:139], a[212:215], v[108:111], v[136:139]\n"
      "ds_write_b64 v245, v[212:213] offset:24576\n"
      "v_mfma_f32_16x16x32_bf16 v[136:139], a[216:219], v[112:115], v[136:139]\n"
      "v_mfma_f32_16x16x32_bf16 v[136:139], a[220:223], v[116:119], v[136:139]\n"
      "v_mfma_f32_16x16x32_bf16 v[132:135], a[224:227], v[36:39], v[16:19]\n"
      "v_mfma_f32_16x16x32_bf16 v[132:135], a[228:231], v[40:43], v[132:135]\n"
      "v_mfma_f32_16x16x32_bf16 v[132:135], a[232:235], v[44:47], v[132:135]\n"
      "v_mfma_f32_16x16x32_bf16 v[132:135], a[236:239], v[60:63], v[132:135]\n"
      "v_cvt_pk_bf16_f32 v214, v136, v137\n"
      "v_mfma_f32_16x16x32_bf16 v[132:135], a[0:3], v[64:67], v[132:135]\n"
      "v_cvt_pk_bf16_f32 v215, v138, v139\n"
      "v_mfma_f32_16x16x32_bf16 v[132:135], a[4:7], v[68:71], v[132:135]\n"
      "v_mfma_f32_16x16x32_bf16 v[132:135], a[8:11], v[84:87], v[132:135]\n"
      "v_mfma_f32_16x16x32_bf16 v[132:135], a[12:15], v[88:91], v[132:135]\n"
      "v_mfma_f32_16x16x32_bf16 v[132:135], a[16:19], v[92:95], v[132:135]\n"
      "v_mfma_f32_16x16x32_bf16 v[132:135], a[20:23], v[108:111], v[132:135]\n"
      "ds_write_b64 v245, v[214:215] offset:24584\n"
      "v_mfma_f32_16x16x32_bf16 v[132:135], v[140:143], v[112:115], v[132:135]\n"
      "s_lshl_b32 s79, s98, 1\n"
      "v_mfma_f32_16x16x32_bf16 v[132:135], v[156:159], v[116:119], v[132:135]\n"
      "s_waitcnt lgkmcnt(4)\n"
      "s_barrier\n"
      "v_mad_i32_i24 v252, v251, s79, v252\n"
      "v_mfma_f32_16x16x32_bf16 v[136:139], v[160:163], v[36:39], v[20:23]\n"
      "ds_read_b128 v[36:39], v232 offset:24576\n"
      "s_mul_i32 s79, s79, s100\n"
      "v_mfma_f32_16x16x32_bf16 v[136:139], v[164:167], v[40:43], v[136:139]\n"
      "ds_read_b128 v[40:43], v233 offset:24576\n"
      "v_add_u32 v247, s79, v247\n"
      "v_mfma_f32_16x16x32_bf16 v[136:139], v[180:183], v[44:47], v[136:139]\n"
      "ds_read_b128 v[44:47], v234 offset:24576\n"
      "s_mul_i32 s79, s79, s101\n"
      "v_mfma_f32_16x16x32_bf16 v[136:139], v[184:187], v[60:63], v[136:139]\n"
      "ds_read_b128 v[60:63], v235 offset:24576\n"
      "v_cvt_pk_bf16_f32 v208, v132, v133\n"
      "v_mfma_f32_16x16x32_bf16 v[136:139], v[188:191], v[64:67], v[136:139]\n"
      "ds_read_b128 v[64:67], v232 offset:32768\n"
      "v_cvt_pk_bf16_f32 v209, v134, v135\n"
      "v_mfma_f32_16x16x32_bf16 v[136:139], v[204:207], v[68:71], v[136:139]\n"
      "ds_read_b128 v[68:71], v233 offset:32768\n"
      "v_add_u32 v253, s79, v253\n"
      "v_mfma_f32_16x16x32_bf16 v[136:139], a[24:27], v[84:87], v[136:139]\n"
      "ds_read_b128 v[84:87], v234 offset:32768\n"
      "v_mfma_f32_16x16x32_bf16 v[136:139], a[28:31], v[88:91], v[136:139]\n"
      "ds_read_b128 v[88:91], v235 offset:32768\n"
      "v_mfma_f32_16x16x32_bf16 v[136:139], a[32:35], v[92:95], v[136:139]\n"
      "ds_read_b128 v[92:95], v232 offset:40960\n"
      "v_mfma_f32_16x16x32_bf16 v[136:139], a[36:39], v[108:111], v[136:139]\n"
      "ds_read_b128 v[108:111], v233 offset:40960\n"
      "v_mfma_f32_16x16x32_bf16 v[136:139], a[40:43], v[112:115], v[136:139]\n"
      "ds_read_b128 v[112:115], v234 offset:40960\n"
      "v_mfma_f32_16x16x32_bf16 v[136:139], a[44:47], v[116:119], v[136:139]\n"
      "ds_read_b128 v[116:119], v235 offset:40960\n"
      "s_branch LJOIN\n"
      "LF_1:\n"
      "s_mul_i32 s62, s62, 0x3000\n"
      "s_sub_u32 s62, s98, s62\n"
      "s_sub_u32 s62, s62, s89\n"
      "v_mad_i32_i24 v238, v251, s62, v238\n"
      "v_mad_i32_i24 v238, v251, s98, v238\n"
      "s_add_u32 s56, s22, 0x0\n"
      "s_addc_u32 s57, s23, 0\n"
      "s_add_u32 m0, s18, 0x0\n"
      "s_nop 0\n"
      "global_load_lds_dwordx4 v238, s[56:57] offset:0 nt\n"
      "v_mad_i32_i24 v238, v251, s90, v238\n"
      "s_add_u32 s56, s22, 0x6000\n"
      "s_addc_u32 s57, s23, 0\n"
      "s_add_u32 m0, s18, 0x1000\n"
      "s_nop 0\n"
      "global_load_lds_dwordx4 v238, s[56:57] offset:0 nt\n"
      "v_mad_i32_i24 v238, v251, s91, v238\n"
      "s_add_u32 s56, s22, 0xc000\n"
      "s_addc_u32 s57, s23, 0\n"
      "s_add_u32 m0, s18, 0x2000\n"
      "s_nop 0\n"
      "global_load_lds_dwordx4 v238, s[56:57] offset:0 nt\n"
      "v_mad_i32_i24 v238, v251, s92, v238\n"
      "s_add_u32 s56, s22, 0x12000\n"
      "s_addc_u32 s57, s23, 0\n"
      "s_add_u32 m0, s18, 0x3000\n"
      "s_nop 0\n"
      "global_load_lds_dwordx4 v238, s[56:57] offset:0 nt\n"
      "v_mad_i32_i24 v238, v251, s93, v238\n"
      "s_add_u32 s56, s22, 0x18000\n"
      "s_addc_u32 s57, s23, 0\n"
      "s_add_u32 m0, s18, 0x4000\n"
      "s_nop 0\n"
      "global_load_lds_dwordx4 v238, s[56:57] offset:0 nt\n"
      "v_mad_i32_i24 v238, v251, s94, v238\n"
      "s_add_u32 s56, s22, 0x1e000\n"
      "s_addc_u32 s57, s23, 0\n"
      "s_add_u32 m0, s18, 0x5000\n"
      "s_nop 0\n"
      "global_load_lds_dwordx4 v238, s[56:57] offset:0 nt\n"
      "v_mad_i32_i24 v238, v251, s95, v238\n"
      "s_add_u32 s56, s22, 0x24000\n"
      "s_addc_u32 s57, s23, 0\n"
      "s_add_u32 m0, s18, 0x6000\n"
      "s_nop 0\n"
      "global_load_lds_dwordx4 v238, s[56:57] offset:0 nt\n"
      "v_mad_i32_i24 v238, v251, s96, v238\n"
      "s_add_u32 s56, s22, 0x2a000\n"
      "s_addc_u32 s57, s23, 0\n"
      "s_add_u32 m0, s18, 0x7000\n"
      "s_nop 0\n"
      "global_load_lds_dwordx4 v238, s[56:57] offset:0 nt\n"
      "v_mad_i32_i24 v238, v251, s97, v238\n"
      "s_add_u32 s56, s22, 0x30000\n"
      "s_addc_u32 s57, s23, 0\n"
      "s_add_u32 m0, s18, 0x8000\n"
      "s_nop 0\n"
      "global_load_lds_dwordx4 v238, s[56:57] offset:0 nt\n"
      "s_branch LR_1\n"
      "LV1:\n"
      "s_add_u32 s78, s64, 0x0\n"
      "s_addc_u32 s79, s65, 0\n"
      "s_add_u32 m0, s74, 0x0\n"
      "s_nop 0\n"
      "global_load_lds_dwordx4 v240, s[78:79]\n"
      "s_add_u32 m0, s74, 0x400\n"
      "s_nop 0\n"
      "global_load_lds_dwordx4 v241, s[78:79]\n"
      "s_add_u32 m0, s74, 0x800\n"
      "s_nop 0\n"
      "global_load_lds_dwordx4 v242, s[78:79]\n"
      "s_add_u32 s78, s64, 0x3000\n"
      "s_addc_u32 s79, s65, 0\n"
      "s_add_u32 m0, s74, 0xc00\n"
      "s_nop 0\n"
      "global_load_lds_dwordx4 v240, s[78:79]\n"
      "s_add_u32 m0, s74, 0x1000\n"
      "s_nop 0\n"
      "global_load_lds_dwordx4 v241, s[78:79]\n"
      "s_add_u32 m0, s74, 0x1400\n"
      "s_nop 0\n"
      "global_load_lds_dwordx4 v242, s[78:79]\n"
      "s_waitcnt vmcnt(17)\n"
      "v_readfirstlane_b32 s80, v252\n"
      "v_readfirstlane_b32 s81, v253\n"
      "s_nop 3\n"
      "s_add_u32 s80, s80, 0x7f4a7c15\n"
      "s_addc_u32 s81, s81, 0x9e3779b9\n"
      "v_mov_b32 v252, s80\n"
      "v_mov_b32 v253, s81\n"
      "v_xor_b32 v116, v112, v110\n"
      "v_xor_b32 v117, v113, v111\n"
      "v_and_or_b32 v116, v116, -2, v117\n"
      "v_cmp_eq_u32 vcc, 0, v116\n"
      "v_cndmask_b32 v116, v110, v112, vcc\n"
      "s_mov_b64 exec, 1\n"
      "ds_write_b32 v108, v116\n"
      "s_mov_b64 exec, -1\n"
      "v_and_b32 v108, -16, v108\n"
      "s_waitcnt lgkmcnt(0)\n"
      "s_barrier\n"
      "ds_read_b32 v116, v108\n"
      "s_waitcnt lgkmcnt(0)\n"
      "v_readfirstlane_b32 s62, v116\n"
      "s_lshr_b32 s63, s51, 2\n"
      "s_nop 2\n"
      "s_xor_b32 s62, s62, s63\n"
      "s_and_b32 s62, s62, 1\n"
      "s_mul_i32 s63, s62, 0x3000\n"
      "s_sub_u32 s98, s98, s63\n"
      "s_lshl_b32 s62, s62, 1\n"
      "s_sub_u32 s62, 1, s62\n"
      "v_mul_i32_i24 v251, s62, v251\n"
      "s_lshl_b32 s19, s62, 1\n"
      "s_mul_i32 s100, s100, s62\n"
      "v_cmp_ne_u32 vcc, v107, v251\n"
      "s_nop 3\n"
      "s_cbranch_vccnz LF_1\n"
      "LR_1:\n"
      "s_nop 0\n"
      "s_add_u32 s78, s64, 0x6000\n"
      "s_addc_u32 s79, s65, 0\n"
      "s_add_u32 m0, s74, 0x6000\n"
      "s_nop 0\n"
      "global_load_lds_dwordx4 v240, s[78:79]\n"
      "s_add_u32 m0, s74, 0x6400\n"
      "s_nop 0\n"
      "global_load_lds_dwordx4 v241, s[78:79]\n"
      "s_add_u32 m0, s74, 0x6800\n"
      "s_nop 0\n"
      "global_load_lds_dwordx4 v242, s[78:79]\n"
      "s_add_u32 s78, s64, 0x9000\n"
      "s_addc_u32 s79, s65, 0\n"
      "s_add_u32 m0, s74, 0x6c00\n"
      "s_nop 0\n"
      "global_load_lds_dwordx4 v240, s[78:79]\n"
      "s_add_u32 m0, s74, 0x7000\n"
      "s_nop 0\n"
      "global_load_lds_dwordx4 v241, s[78:79]\n"
      "s_add_u32 m0, s74, 0x7400\n"
      "s_nop 0\n"
      "global_load_lds_dwordx4 v242, s[78:79]\n"
      "s_add_u32 s78, s64, 0xc000\n"
      "s_addc_u32 s79, s65, 0\n"
      "s_add_u32 m0, s74, 0xc000\n"
      "s_nop 0\n"
      "global_load_lds_dwordx4 v240, s[78:79]\n"
      "s_add_u32 m0, s74, 0xc400\n"
      "s_nop 0\n"
      "global_load_lds_dwordx4 v241, s[78:79]\n"
      "s_add_u32 m0, s74, 0xc800\n"
      "s_nop 0\n"
      "global_load_lds_dwordx4 v242, s[78:79]\n"
      "s_add_u32 s78, s64, 0xf000\n"
      "s_addc_u32 s79, s65, 0\n"
      "s_add_u32 m0, s74, 0xcc00\n"
      "s_nop 0\n"
      "global_load_lds_dwordx4 v240, s[78:79]\n"
      "s_add_u32 m0, s74, 0xd000\n"
      "s_nop 0\n"
      "global_load_lds_dwordx4 v241, s[78:79]\n"
      "s_add_u32 m0, s74, 0xd400\n"
      "s_nop 0\n"
      "global_load_lds_dwordx4 v242, s[78:79]\n"
      "s_add_u32 m0, s17, 0x25000\n"
      "s_nop 0\n"
      "global_load_lds_dwordx4 v239, s[46:47] offset:0\n"
      "s_add_u32 m0, s17, 0x25800\n"
      "s_nop 0\n"
      "global_load_lds_dwordx4 v239, s[46:47] offset:2048\n"
      "s_waitcnt vmcnt(14)\n"
      "ds_read_b64_tr_b16 a[0:1], v243 offset:0\n"
      "ds_read_b64_tr_b16 a[2:3], v243 offset:768\n"
      "ds_read_b64_tr_b16 a[4:5], v243 offset:8\n"
      "ds_read_b64_tr_b16 a[6:7], v243 offset:776\n"
      "ds_read_b64_tr_b16 a[8:9], v243 offset:64\n"
      "ds_read_b64_tr_b16 a[10:11], v243 offset:832\n"
      "ds_read_b64_tr_b16 a[12:13], v243 offset:72\n"
      "ds_read_b64_tr_b16 a[14:15], v243 offset:840\n"
      "ds_read_b64_tr_b16 a[16:17], v243 offset:128\n"
      "ds_read_b64_tr_b16 a[18:19], v243 offset:896\n"
      "ds_read_b64_tr_b16 a[20:21], v243 offset:136\n"
      "ds_read_b64_tr_b16 a[22:23], v243 offset:904\n"
      "s_waitcnt vmcnt(13)\n"
      "s_barrier\n"
      "ds_read_b128 v[216:219], v232 offset:0\n"
      "ds_read_b128 v[220:223], v232 offset:4096\n"
      "ds_read_b128 v[224:227], v232 offset:8192\n"
      "s_waitcnt lgkmcnt(2)\n"
      "v_mfma_f32_16x16x32_bf16 v[0:3], a[0:3], v[216:219], 0\n"
      "s_add_u32 s78, s64, 0x12000\n"
      "s_addc_u32 s79, s65, 0\n"
      "s_add_u32 m0, s74, 0x0\n"
      "v_mfma_f32_16x16x32_bf16 v[4:7], a[4:7], v[216:219], 0\n"
      "global_load_lds_dwordx4 v240, s[78:79]\n"
      "v_mfma_f32_16x16x32_bf16 v[8:11], a[8:11], v[216:219], 0\n"
      "ds_read_b128 v[228:231], v232 offset:12288\n"
      "v_mfma_f32_16x16x32_bf16 v[12:15], a[12:15], v[216:219], 0\n"
      "v_mfma_f32_16x16x32_bf16 v[16:19], a[16:19], v[216:219], 0\n"
      "s_add_u32 m0, s74, 0x400\n"
      "v_mfma_f32_16x16x32_bf16 v[20:23], a[20:23], v[216:219], 0\n"
      "global_load_lds_dwordx4 v241, s[78:79]\n"
      "s_waitcnt lgkmcnt(2)\n"
      "v_mfma_f32_16x16x32_bf16 v[24:27], a[0:3], v[220:223], 0\n"
      "s_waitcnt vmcnt(10)\n"
      "s_cmp_lg_u32 s76, 1\n"
      "s_cbranch_scc1 LA_1\n"
      "v_mov_b32 v244, s77\n"
      "s_mov_b64 exec, 1\n"
      "global_store_dwordx2 v244, v[252:253], s[28:29] sc1\n"
      "s_mov_b64 exec, -1\n"
      "s_nop 4\n"
      "LA_1:\n"
      "v_mfma_f32_16x16x32_bf16 v[28:31], a[4:7], v[220:223], 0\n"
      "ds_read_b64_tr_b16 a[24:25], v243 offset:24576\n"
      "v_mfma_f32_16x16x32_bf16 v[32:35], a[8:11], v[220:223], 0\n"
      "ds_read_b128 v[216:219], v232 offset:16384\n"
      "s_add_u32 m0, s74, 0x800\n"
      "v_mfma_f32_16x16x32_bf16 v[36:39], a[12:15], v[220:223], 0\n"
      "ds_read_b64_tr_b16 a[26:27], v243 offset:25344\n"
      "global_load_lds_dwordx4 v242, s[78:79]\n"
      "v_mfma_f32_16x16x32_bf16 v[40:43], a[16:19], v[220:223], 0\n"
      "ds_read_b64_tr_b16 a[28:29], v243 offset:24584\n"
      "v_mfma_f32_16x16x32_bf16 v[44:47], a[20:23], v[220:223], 0\n"
      "s_waitcnt lgkmcnt(5)\n"
      "v_mfma_f32_16x16x32_bf16 v[48:51], a[0:3], v[224:227], 0\n"
      "ds_read_b64_tr_b16 a[30:31], v243 offset:25352\n"
      "s_add_u32 s78, s64, 0x15000\n"
      "s_addc_u32 s79, s65, 0\n"
      "s_add_u32 m0, s74, 0xc00\n"
      "v_mfma_f32_16x16x32_bf16 v[52:55], a[4:7], v[224:227], 0\n"
      "ds_read_b64_tr_b16 a[32:33], v243 offset:24640\n"
      "global_load_lds_dwordx4 v240, s[78:79]\n"
      "v_mfma_f32_16x16x32_bf16 v[56:59], a[8:11], v[224:227], 0\n"
      "ds_read_b128 v[220:223], v232 offset:20480\n"
      "v_mfma_f32_16x16x32_bf16 v[60:63], a[12:15], v[224:227], 0\n"
      "ds_read_b64_tr_b16 a[34:35], v243 offset:25408\n"
      "v_mfma_f32_16x16x32_bf16 v[64:67], a[16:19], v[224:227], 0\n"
      "ds_read_b64_tr_b16 a[36:37], v243 offset:24648\n"
      "s_add_u32 m0, s74, 0x1000\n"
      "v_mfma_f32_16x16x32_bf16 v[68:71], a[20:23], v[224:227], 0\n"
      "global_load_lds_dwordx4 v241, s[78:79]\n"
      "s_waitcnt lgkmcnt(9)\n"
      "v_mfma_f32_16x16x32_bf16 v[72:75], a[0:3], v[228:231], 0\n"
      "ds_read_b64_tr_b16 a[38:39], v243 offset:25416\n"
      "v_mfma_f32_16x16x32_bf16 v[76:79], a[4:7], v[228:231], 0\n"
      "ds_read_b64_tr_b16 a[40:41], v243 offset:24704\n"
      "v_mfma_f32_16x16x32_bf16 v[80:83], a[8:11], v[228:231], 0\n"
      "ds_read_b128 v[224:227], v232 offset:24576\n"
      "s_add_u32 m0, s74, 0x1400\n"
      "v_mfma_f32_16x16x32_bf16 v[84:87], a[12:15], v[228:231], 0\n"
      "ds_read_b64_tr_b16 a[42:43], v243 offset:25472\n"
      "global_load_lds_dwordx4 v242, s[78:79]\n"
      "v_mfma_f32_16x16x32_bf16 v[88:91], a[16:19], v[228:231], 0\n"
      "ds_read_b64_tr_b16 a[44:45], v243 offset:24712\n"
      "v_mfma_f32_16x16x32_bf16 v[92:95], a[20:23], v[228:231], 0\n"
      "s_waitcnt lgkmcnt(12)\n"
      "v_mfma_f32_16x16x32_bf16 v[96:99], a[0:3], v[216:219], 0\n"
      "ds_read_b64_tr_b16 a[46:47], v243 offset:25480\n"
      "v_mfma_f32_16x16x32_bf16 v[100:103], a[4:7], v[216:219], 0\n"
      "s_waitcnt vmcnt(8)\n"
      "v_mfma_f32_16x16x32_bf16 v[104:107], a[8:11], v[216:219], 0\n"
      "ds_read_b128 v[228:231], v232 offset:28672\n"
      "v_mfma_f32_16x16x32_bf16 v[108:111], a[12:15], v[216:219], 0\n"
      "ds_read_b64_tr_b16 a[48:49], v243 offset:49152\n"
      "v_mfma_f32_16x16x32_bf16 v[112:115], a[16:19], v[216:219], 0\n"
      "ds_read_b64_tr_b16 a[50:51], v243 offset:49920\n"
      "v_mfma_f32_16x16x32_bf16 v[116:119], a[20:23], v[216:219], 0\n"
      "s_waitcnt lgkmcnt(11)\n"
      "v_mfma_f32_16x16x32_bf16 v[120:123], a[0:3], v[220:223], 0\n"
      "ds_read_b64_tr_b16 a[52:53], v243 offset:49160\n"
      "v_mfma_f32_16x16x32_bf16 v[124:127], a[4:7], v[220:223], 0\n"
      "ds_read_b64_tr_b16 a[54:55], v243 offset:49928\n"
      "v_mfma_f32_16x16x32_bf16 v[128:131], a[8:11], v[220:223], 0\n"
      "ds_read_b128 v[216:219], v232 offset:32768\n"
      "v_add_u32 v232, 0x9000, v232\n"
      "v_mfma_f32_16x16x32_bf16 v[132:135], a[12:15], v[220:223], 0\n"
      "ds_read_b64_tr_b16 a[56:57], v243 offset:49216\n"
      "v_mfma_f32_16x16x32_bf16 v[136:139], a[16:19], v[220:223], 0\n"
      "ds_read_b64_tr_b16 a[58:59], v243 offset:49984\n"
      "s_waitcnt lgkmcnt(8)\n"
      "s_add_u32 s78, s64, 0x18000\n"
      "s_addc_u32 s79, s65, 0\n"
      "s_add_u32 m0, s74, 0x6000\n"
      "v_mfma_f32_16x16x32_bf16 v[140:143], a[20:23], v[220:223], 0\n"
      "global_load_lds_dwordx4 v240, s[78:79]\n"
      "v_mfma_f32_16x16x32_bf16 v[144:147], a[0:3], v[224:227], 0\n"
      "ds_read_b64_tr_b16 a[60:61], v243 offset:49224\n"
      "v_mfma_f32_16x16x32_bf16 v[148:151], a[4:7], v[224:227], 0\n"
      "ds_read_b64_tr_b16 a[62:63], v243 offset:49992\n"
      "v_mfma_f32_16x16x32_bf16 v[152:155], a[8:11], v[224:227], 0\n"
      "ds_read_b128 v[220:223], v233 offset:0\n"
      "s_add_u32 m0, s74, 0x6400\n"
      "v_mfma_f32_16x16x32_bf16 v[156:159], a[12:15], v[224:227], 0\n"
      "ds_read_b64_tr_b16 a[64:65], v243 offset:49280\n"
      "global_load_lds_dwordx4 v241, s[78:79]\n"
      "v_mfma_f32_16x16x32_bf16 v[160:163], a[16:19], v[224:227], 0\n"
      "ds_read_b64_tr_b16 a[66:67], v243 offset:50048\n"
      "v_mfma_f32_16x16x32_bf16 v[164:167], a[20:23], v[224:227], 0\n"
      "s_waitcnt lgkmcnt(12)\n"
      "v_mfma_f32_16x16x32_bf16 v[168:171], a[0:3], v[228:231], 0\n"
      "ds_read_b64_tr_b16 a[68:69], v243 offset:49288\n"
      "s_add_u32 m0, s74, 0x6800\n"
      "v_mfma_f32_16x16x32_bf16 v[172:175], a[4:7], v[228:231], 0\n"
      "ds_read_b64_tr_b16 a[70:71], v243 offset:50056\n"
      "global_load_lds_dwordx4 v242, s[78:79]\n"
      "v_mfma_f32_16x16x32_bf16 v[176:179], a[8:11], v[228:231], 0\n"
      "ds_read_b128 v[224:227], v233 offset:4096\n"
      "v_mfma_f32_16x16x32_bf16 v[180:183], a[12:15], v[228:231], 0\n"
      "v_mfma_f32_16x16x32_bf16 v[184:187], a[16:19], v[228:231], 0\n"
      "s_add_u32 s78, s64, 0x1b000\n"
      "s_addc_u32 s79, s65, 0\n"
      "s_add_u32 m0, s74, 0x6c00\n"
      "v_mfma_f32_16x16x32_bf16 v[188:191], a[20:23], v[228:231], 0\n"
      "global_load_lds_dwordx4 v240, s[78:79]\n"
      "s_waitcnt lgkmcnt(10)\n"
      "v_mfma_f32_16x16x32_bf16 v[192:195], a[0:3], v[216:219], 0\n"
      "v_mfma_f32_16x16x32_bf16 v[196:199], a[4:7], v[216:219], 0\n"
      "v_mfma_f32_16x16x32_bf16 v[200:203], a[8:11], v[216:219], 0\n"
      "ds_read_b128 v[228:231], v233 offset:8192\n"
      "s_add_u32 m0, s74, 0x7000\n"
      "v_mfma_f32_16x16x32_bf16 v[204:207], a[12:15], v[216:219], 0\n"
      "global_load_lds_dwordx4 v241, s[78:79]\n"
      "v_mfma_f32_16x16x32_bf16 v[208:211], a[16:19], v[216:219], 0\n"
      "v_mfma_f32_16x16x32_bf16 v[212:215], a[20:23], v[216:219], 0\n"
      "s_waitcnt lgkmcnt(6)\n"
      "v_mfma_f32_16x16x32_bf16 v[0:3], a[24:27], v[220:223], v[0:3]\n"
      "s_add_u32 m0, s74, 0x7400\n"
      "v_mfma_f32_16x16x32_bf16 v[4:7], a[28:31], v[220:223], v[4:7]\n"
      "global_load_lds_dwordx4 v242, s[78:79]\n"
      "v_mfma_f32_16x16x32_bf16 v[8:11], a[32:35], v[220:223], v[8:11]\n"
      "ds_read_b128 v[216:219], v233 offset:12288\n"
      "s_waitcnt vmcnt(6)\n"
      "v_mfma_f32_16x16x32_bf16 v[12:15], a[36:39], v[220:223], v[12:15]\n"
      "ds_read_b64_tr_b16 a[72:73], v243 offset:0\n"
      "v_mfma_f32_16x16x32_bf16 v[16:19], a[40:43], v[220:223], v[16:19]\n"
      "ds_read_b64_tr_b16 a[74:75], v243 offset:768\n"
      "v_mad_i32_i24 v238, v251, s89, v238\n"
      "s_add_u32 s56, s22, 0x0\n"
      "s_addc_u32 s57, s23, 0\n"
      "s_add_u32 m0, s18, 0x8f00\n"
      "v_mfma_f32_16x16x32_bf16 v[20:23], a[44:47], v[220:223], v[20:23]\n"
      "global_load_lds_dwordx4 v238, s[56:57] offset:256 nt\n"
      "s_waitcnt lgkmcnt(4)\n"
      "v_mfma_f32_16x16x32_bf16 v[24:27], a[24:27], v[224:227], v[24:27]\n"
      "ds_read_b64_tr_b16 a[76:77], v243 offset:8\n"
      "v_mfma_f32_16x16x32_bf16 v[28:31], a[28:31], v[224:227], v[28:31]\n"
      "ds_read_b64_tr_b16 a[78:79], v243 offset:776\n"
      "v_mad_i32_i24 v238, v251, s90, v238\n"
      "s_add_u32 s56, s22, 0x6000\n"
      "s_addc_u32 s57, s23, 0\n"
      "s_add_u32 m0, s18, 0x9f00\n"
      "v_mfma_f32_16x16x32_bf16 v[32:35], a[32:35], v[224:227], v[32:35]\n"
      "ds_read_b128 v[220:223], v233 offset:16384\n"
      "global_load_lds_dwordx4 v238, s[56:57] offset:256 nt\n"
      "v_mfma_f32_16x16x32_bf16 v[36:39], a[36:39], v[224:227], v[36:39]\n"
      "ds_read_b64_tr_b16 a[80:81], v243 offset:64\n"
      "v_mfma_f32_16x16x32_bf16 v[40:43], a[40:43], v[224:227], v[40:43]\n"
      "ds_read_b64_tr_b16 a[82:83], v243 offset:832\n"
      "v_mad_i32_i24 v238, v251, s91, v238\n"
      "s_add_u32 s56, s22, 0xc000\n"
      "s_addc_u32 s57, s23, 0\n"
      "s_add_u32 m0, s18, 0xaf00\n"
      "v_mfma_f32_16x16x32_bf16 v[44:47], a[44:47], v[224:227], v[44:47]\n"
      "global_load_lds_dwordx4 v238, s[56:57] offset:256 nt\n"
      "s_waitcnt lgkmcnt(8)\n"
      "v_mfma_f32_16x16x32_bf16 v[48:51], a[24:27], v[228:231], v[48:51]\n"
      "ds_read_b64_tr_b16 a[84:85], v243 offset:72\n"
      "v_mfma_f32_16x16x32_bf16 v[52:55], a[28:31], v[228:231], v[52:55]\n"
      "ds_read_b64_tr_b16 a[86:87], v243 offset:840\n"
      "v_mad_i32_i24 v238, v251, s92, v238\n"
      "s_add_u32 s56, s22, 0x12000\n"
      "s_addc_u32 s57, s23, 0\n"
      "s_add_u32 m0, s18, 0xbf00\n"
      "v_mfma_f32_16x16x32_bf16 v[56:59], a[32:35], v[228:231], v[56:59]\n"
      "ds_read_b128 v[224:227], v233 offset:20480\n"
      "global_load_lds_dwordx4 v238, s[56:57] offset:256 nt\n"
      "v_mfma_f32_16x16x32_bf16 v[60:63], a[36:39], v[228:231], v[60:63]\n"
      "ds_read_b64_tr_b16 a[88:89], v243 offset:128\n"
      "v_mfma_f32_16x16x32_bf16 v[64:67], a[40:43], v[228:231], v[64:67]\n"
      "ds_read_b64_tr_b16 a[90:91], v243 offset:896\n"
      "v_mad_i32_i24 v238, v251, s93, v238\n"
      "s_add_u32 s56, s22, 0x18000\n"
      "s_addc_u32 s57, s23, 0\n"
      "s_add_u32 m0, s18, 0xcf00\n"
      "v_mfma_f32_16x16x32_bf16 v[68:71], a[44:47], v[228:231], v[68:71]\n"
      "global_load_lds_dwordx4 v238, s[56:57] offset:256 nt\n"
      "s_waitcnt lgkmcnt(12)\n"
      "v_mfma_f32_16x16x32_bf16 v[72:75], a[24:27], v[216:219], v[72:75]\n"
      "ds_read_b64_tr_b16 a[92:93], v243 offset:136\n"
      "v_mfma_f32_16x16x32_bf16 v[76:79], a[28:31], v[216:219], v[76:79]\n"
      "ds_read_b64_tr_b16 a[94:95], v243 offset:904\n"
      "v_mad_i32_i24 v238, v251, s94, v238\n"
      "s_add_u32 s56, s22, 0x1e000\n"
      "s_addc_u32 s57, s23, 0\n"
      "s_add_u32 m0, s18, 0xdf00\n"
      "v_mfma_f32_16x16x32_bf16 v[80:83], a[32:35], v[216:219], v[80:83]\n"
      "ds_read_b128 v[228:231], v233 offset:24576\n"
      "global_load_lds_dwordx4 v238, s[56:57] offset:256 nt\n"
      "v_mfma_f32_16x16x32_bf16 v[84:87], a[36:39], v[216:219], v[84:87]\n"
      "v_mfma_f32_16x16x32_bf16 v[88:91], a[40:43], v[216:219], v[88:91]\n"
      "v_mad_i32_i24 v238, v251, s95, v238\n"
      "s_add_u32 s56, s22, 0x24000\n"
      "s_addc_u32 s57, s23, 0\n"
      "s_add_u32 m0, s18, 0xef00\n"
      "v_mfma_f32_16x16x32_bf16 v[92:95], a[44:47], v[216:219], v[92:95]\n"
      "global_load_lds_dwordx4 v238, s[56:57] offset:256 nt\n"
      "s_waitcnt lgkmcnt(10)\n"
      "v_mfma_f32_16x16x32_bf16 v[96:99], a[24:27], v[220:223], v[96:99]\n"
      "v_mfma_f32_16x16x32_bf16 v[100:103], a[28:31], v[220:223], v[100:103]\n"
      "v_mad_i32_i24 v238, v251, s96, v238\n"
      "s_add_u32 s56, s22, 0x2a000\n"
      "s_addc_u32 s57, s23, 0\n"
      "s_add_u32 m0, s18, 0xff00\n"
      "v_mfma_f32_16x16x32_bf16 v[104:107], a[32:35], v[220:223], v[104:107]\n"
      "ds_read_b128 v[216:219], v233 offset:28672\n"
      "global_load_lds_dwordx4 v238, s[56:57] offset:256 nt\n"
      "v_mfma_f32_16x16x32_bf16 v[108:111], a[36:39], v[220:223], v[108:111]\n"
      "v_mfma_f32_16x16x32_bf16 v[112:115], a[40:43], v[220:223], v[112:115]\n"
      "v_mad_i32_i24 v238, v251, s97, v238\n"
      "s_add_u32 s56, s22, 0x30000\n"
      "s_addc_u32 s57, s23, 0\n"
      "s_add_u32 m0, s18, 0x10f00\n"
      "v_mfma_f32_16x16x32_bf16 v[116:119], a[44:47], v[220:223], v[116:119]\n"
      "global_load_lds_dwordx4 v238, s[56:57] offset:256 nt\n"
      "s_waitcnt lgkmcnt(6)\n"
      "v_mfma_f32_16x16x32_bf16 v[120:123], a[24:27], v[224:227], v[120:123]\n"
      "v_mfma_f32_16x16x32_bf16 v[124:127], a[28:31], v[224:227], v[124:127]\n"
      "v_mfma_f32_16x16x32_bf16 v[128:131], a[32:35], v[224:227], v[128:131]\n"
      "ds_read_b128 v[220:223], v233 offset:32768\n"
      "v_add_u32 v233, 0x9000, v233\n"
      "s_add_u32 s78, s64, 0x1e000\n"
      "s_addc_u32 s79, s65, 0\n"
      "s_add_u32 m0, s74, 0xc000\n"
      "v_mfma_f32_16x16x32_bf16 v[132:135], a[36:39], v[224:227], v[132:135]\n"
      "global_load_lds_dwordx4 v240, s[78:79]\n"
      "v_mfma_f32_16x16x32_bf16 v[136:139], a[40:43], v[224:227], v[136:139]\n"
      "v_mfma_f32_16x16x32_bf16 v[140:143], a[44:47], v[224:227], v[140:143]\n"
      "s_waitcnt lgkmcnt(2)\n"
      "v_mfma_f32_16x16x32_bf16 v[144:147], a[24:27], v[228:231], v[144:147]\n"
      "s_waitcnt vmcnt(10)\n"
      "s_add_u32 m0, s74, 0xc400\n"
      "v_mfma_f32_16x16x32_bf16 v[148:151], a[28:31], v[228:231], v[148:151]\n"
      "ds_read_b64_tr_b16 a[96:97], v243 offset:24576\n"
      "global_load_lds_dwordx4 v241, s[78:79]\n"
      "v_mfma_f32_16x16x32_bf16 v[152:155], a[32:35], v[228:231], v[152:155]\n"
      "ds_read_b128 v[224:227], v234 offset:0\n"
      "v_mfma_f32_16x16x32_bf16 v[156:159], a[36:39], v[228:231], v[156:159]\n"
      "ds_read_b64_tr_b16 a[98:99], v243 offset:25344\n"
      "v_mfma_f32_16x16x32_bf16 v[160:163], a[40:43], v[228:231], v[160:163]\n"
      "ds_read_b64_tr_b16 a[100:101], v243 offset:24584\n"
      "s_add_u32 m0, s74, 0xc800\n"
      "v_mfma_f32_16x16x32_bf16 v[164:167], a[44:47], v[228:231], v[164:167]\n"
      "global_load_lds_dwordx4 v242, s[78:79]\n"
      "s_waitcnt lgkmcnt(5)\n"
      "v_mfma_f32_16x16x32_bf16 v[168:171], a[24:27], v[216:219], v[168:171]\n"
      "ds_read_b64_tr_b16 a[102:103], v243 offset:25352\n"
      "v_mfma_f32_16x16x32_bf16 v[172:175], a[28:31], v[216:219], v[172:175]\n"
      "ds_read_b64_tr_b16 a[104:105], v243 offset:24640\n"
      "v_mfma_f32_16x16x32_bf16 v[176:179], a[32:35], v[216:219], v[176:179]\n"
      "ds_read_b128 v[228:231], v234 offset:4096\n"
      "s_add_u32 s78, s64, 0x21000\n"
      "s_addc_u32 s79, s65, 0\n"
      "s_add_u32 m0, s74, 0xcc00\n"
      "v_mfma_f32_16x16x32_bf16 v[180:183], a[36:39], v[216:219], v[180:183]\n"
      "ds_read_b64_tr_b16 a[106:107], v243 offset:25408\n"
      "global_load_lds_dwordx4 v240, s[78:79]\n"
      "v_mfma_f32_16x16x32_bf16 v[184:187], a[40:43], v[216:219], v[184:187]\n"
      "ds_read_b64_tr_b16 a[108:109], v243 offset:24648\n"
      "v_mfma_f32_16x16x32_bf16 v[188:191], a[44:47], v[216:219], v[188:191]\n"
      "s_waitcnt lgkmcnt(9)\n"
      "v_mfma_f32_16x16x32_bf16 v[192:195], a[24:27], v[220:223], v[192:195]\n"
      "ds_read_b64_tr_b16 a[110:111], v243 offset:25416\n"
      "s_add_u32 m0, s74, 0xd000\n"
      "v_mfma_f32_16x16x32_bf16 v[196:199], a[28:31], v[220:223], v[196:199]\n"
      "ds_read_b64_tr_b16 a[112:113], v243 offset:24704\n"
      "global_load_lds_dwordx4 v241, s[78:79]\n"
      "v_mfma_f32_16x16x32_bf16 v[200:203], a[32:35], v[220:223], v[200:203]\n"
      "ds_read_b128 v[216:219], v234 offset:8192\n"
      "v_mfma_f32_16x16x32_bf16 v[204:207], a[36:39], v[220:223], v[204:207]\n"
      "ds_read_b64_tr_b16 a[114:115], v243 offset:25472\n"
      "v_mfma_f32_16x16x32_bf16 v[208:211], a[40:43], v[220:223], v[208:211]\n"
      "ds_read_b64_tr_b16 a[116:117], v243 offset:24712\n"
      "s_add_u32 m0, s74, 0xd400\n"
      "v_mfma_f32_16x16x32_bf16 v[212:215], a[44:47], v[220:223], v[212:215]\n"
      "global_load_lds_dwordx4 v242, s[78:79]\n"
      "s_waitcnt lgkmcnt(12)\n"
      "v_mfma_f32_16x16x32_bf16 v[0:3], a[48:51], v[224:227], v[0:3]\n"
      "ds_read_b64_tr_b16 a[118:119], v243 offset:25480\n"
      "v_mfma_f32_16x16x32_bf16 v[4:7], a[52:55], v[224:227], v[4:7]\n"
      "v_mfma_f32_16x16x32_bf16 v[8:11], a[56:59], v[224:227], v[8:11]\n"
      "ds_read_b128 v[220:223], v234 offset:12288\n"
      "s_add_u32 s78, s64, 0x24000\n"
      "s_addc_u32 s79, s65, 0\n"
      "s_add_u32 m0, s74, 0x0\n"
      "v_mfma_f32_16x16x32_bf16 v[12:15], a[60:63], v[224:227], v[12:15]\n"
      "global_load_lds_dwordx4 v240, s[78:79]\n"
      "v_mfma_f32_16x16x32_bf16 v[16:19], a[64:67], v[224:227], v[16:19]\n"
      "v_mfma_f32_16x16x32_bf16 v[20:23], a[68:71], v[224:227], v[20:23]\n"
      "s_waitcnt lgkmcnt(9)\n"
      "v_mfma_f32_16x16x32_bf16 v[24:27], a[48:51], v[228:231], v[24:27]\n"
      "s_add_u32 m0, s74, 0x400\n"
      "v_mfma_f32_16x16x32_bf16 v[28:31], a[52:55], v[228:231], v[28:31]\n"
      "global_load_lds_dwordx4 v241, s[78:79]\n"
      "v_mfma_f32_16x16x32_bf16 v[32:35], a[56:59], v[228:231], v[32:35]\n"
      "ds_read_b128 v[224:227], v234 offset:16384\n"
      "v_mfma_f32_16x16x32_bf16 v[36:39], a[60:63], v[228:231], v[36:39]\n"
      "v_mfma_f32_16x16x32_bf16 v[40:43], a[64:67], v[228:231], v[40:43]\n"
      "s_add_u32 m0, s74, 0x800\n"
      "v_mfma_f32_16x16x32_bf16 v[44:47], a[68:71], v[228:231], v[44:47]\n"
      "global_load_lds_dwordx4 v242, s[78:79]\n"
      "s_waitcnt lgkmcnt(5)\n"
      "v_mfma_f32_16x16x32_bf16 v[48:51], a[48:51], v[216:219], v[48:51]\n"
      "v_mfma_f32_16x16x32_bf16 v[52:55], a[52:55], v[216:219], v[52:55]\n"
      "v_mfma_f32_16x16x32_bf16 v[56:59], a[56:59], v[216:219], v[56:59]\n"
      "ds_read_b128 v[228:231], v234 offset:20480\n"
      "s_add_u32 s78, s64, 0x27000\n"
      "s_addc_u32 s79, s65, 0\n"
      "s_add_u32 m0, s74, 0xc00\n"
      "v_mfma_f32_16x16x32_bf16 v[60:63], a[60:63], v[216:219], v[60:63]\n"
      "global_load_lds_dwordx4 v240, s[78:79]\n"
      "v_mfma_f32_16x16x32_bf16 v[64:67], a[64:67], v[216:219], v[64:67]\n"
      "v_mfma_f32_16x16x32_bf16 v[68:71], a[68:71], v[216:219], v[68:71]\n"
      "s_waitcnt lgkmcnt(2)\n"
      "v_mfma_f32_16x16x32_bf16 v[72:75], a[48:51], v[220:223], v[72:75]\n"
      "s_add_u32 m0, s74, 0x1000\n"
      "v_mfma_f32_16x16x32_bf16 v[76:79], a[52:55], v[220:223], v[76:79]\n"
      "global_load_lds_dwordx4 v241, s[78:79]\n"
      "v_mfma_f32_16x16x32_bf16 v[80:83], a[56:59], v[220:223], v[80:83]\n"
      "ds_read_b128 v[216:219], v234 offset:24576\n"
      "v_mfma_f32_16x16x32_bf16 v[84:87], a[60:63], v[220:223], v[84:87]\n"
      "v_mfma_f32_16x16x32_bf16 v[88:91], a[64:67], v[220:223], v[88:91]\n"
      "s_add_u32 m0, s74, 0x1400\n"
      "v_mfma_f32_16x16x32_bf16 v[92:95], a[68:71], v[220:223], v[92:95]\n"
      "global_load_lds_dwordx4 v242, s[78:79]\n"
      "s_waitcnt lgkmcnt(2)\n"
      "v_mfma_f32_16x16x32_bf16 v[96:99], a[48:51], v[224:227], v[96:99]\n"
      "v_mfma_f32_16x16x32_bf16 v[100:103], a[52:55], v[224:227], v[100:103]\n"
      "v_mfma_f32_16x16x32_bf16 v[104:107], a[56:59], v[224:227], v[104:107]\n"
      "ds_read_b128 v[220:223], v234 offset:28672\n"
      "s_add_u32 s78, s64, 0x2a000\n"
      "s_addc_u32 s79, s65, 0\n"
      "s_add_u32 m0, s74, 0x6000\n"
      "v_mfma_f32_16x16x32_bf16 v[108:111], a[60:63], v[224:227], v[108:111]\n"
      "global_load_lds_dwordx4 v240, s[78:79]\n"
      "v_mfma_f32_16x16x32_bf16 v[112:115], a[64:67], v[224:227], v[112:115]\n"
      "v_mfma_f32_16x16x32_bf16 v[116:119], a[68:71], v[224:227], v[116:119]\n"
      "s_waitcnt lgkmcnt(2)\n"
      "v_mfma_f32_16x16x32_bf16 v[120:123], a[48:51], v[228:231], v[120:123]\n"
      "s_add_u32 m0, s74, 0x6400\n"
      "v_mfma_f32_16x16x32_bf16 v[124:127], a[52:55], v[228:231], v[124:127]\n"
      "global_load_lds_dwordx4 v241, s[78:79]\n"
      "v_mfma_f32_16x16x32_bf16 v[128:131], a[56:59], v[228:231], v[128:131]\n"
      "ds_read_b128 v[224:227], v234 offset:32768\n"
      "v_add_u32 v234, 0x9000, v234\n"
      "v_mfma_f32_16x16x32_bf16 v[132:135], a[60:63], v[228:231], v[132:135]\n"
      "v_mfma_f32_16x16x32_bf16 v[136:139], a[64:67], v[228:231], v[136:139]\n"
      "s_waitcnt vmcnt(8)\n"
      "s_add_u32 m0, s74, 0x6800\n"
      "v_mfma_f32_16x16x32_bf16 v[140:143], a[68:71], v[228:231], v[140:143]\n"
      "global_load_lds_dwordx4 v242, s[78:79]\n"
      "s_waitcnt lgkmcnt(2)\n"
      "v_mfma_f32_16x16x32_bf16 v[144:147], a[48:51], v[216:219], v[144:147]\n"
      "ds_read_b64_tr_b16 a[120:121], v243 offset:49152\n"
      "v_mfma_f32_16x16x32_bf16 v[148:151], a[52:55], v[216:219], v[148:151]\n"
      "ds_read_b64_tr_b16 a[122:123], v243 offset:49920\n"
      "v_mfma_f32_16x16x32_bf16 v[152:155], a[56:59], v[216:219], v[152:155]\n"
      "ds_read_b128 v[228:231], v235 offset:0\n"
      "s_add_u32 s78, s64, 0x2d000\n"
      "s_addc_u32 s79, s65, 0\n"
      "s_add_u32 m0, s74, 0x6c00\n"
      "v_mfma_f32_16x16x32_bf16 v[156:159], a[60:63], v[216:219], v[156:159]\n"
      "ds_read_b64_tr_b16 a[124:125], v243 offset:49160\n"
      "global_load_lds_dwordx4 v240, s[78:79]\n"
      "v_mfma_f32_16x16x32_bf16 v[160:163], a[64:67], v[216:219], v[160:163]\n"
      "ds_read_b64_tr_b16 a[126:127], v243 offset:49928\n"
      "v_mfma_f32_16x16x32_bf16 v[164:167], a[68:71], v[216:219], v[164:167]\n"
      "s_waitcnt lgkmcnt(6)\n"
      "v_mfma_f32_16x16x32_bf16 v[168:171], a[48:51], v[220:223], v[168:171]\n"
      "ds_read_b64_tr_b16 a[128:129], v243 offset:49216\n"
      "s_add_u32 m0, s74, 0x7000\n"
      "v_mfma_f32_16x16x32_bf16 v[172:175], a[52:55], v[220:223], v[172:175]\n"
      "ds_read_b64_tr_b16 a[130:131], v243 offset:49984\n"
      "global_load_lds_dwordx4 v241, s[78:79]\n"
      "v_mfma_f32_16x16x32_bf16 v[176:179], a[56:59], v[220:223], v[176:179]\n"
      "ds_read_b128 v[216:219], v235 offset:4096\n"
      "v_mfma_f32_16x16x32_bf16 v[180:183], a[60:63], v[220:223], v[180:183]\n"
      "ds_read_b64_tr_b16 a[132:133], v243 offset:49224\n"
      "v_mfma_f32_16x16x32_bf16 v[184:187], a[64:67], v[220:223], v[184:187]\n"
      "ds_read_b64_tr_b16 a[134:135], v243 offset:49992\n"
      "s_add_u32 m0, s74, 0x7400\n"
      "v_mfma_f32_16x16x32_bf16 v[188:191], a[68:71], v[220:223], v[188:191]\n"
      "global_load_lds_dwordx4 v242, s[78:79]\n"
      "s_waitcnt lgkmcnt(10)\n"
      "v_mfma_f32_16x16x32_bf16 v[192:195], a[48:51], v[224:227], v[192:195]\n"
      "ds_read_b64_tr_b16 a[136:137], v243 offset:49280\n"
      "v_mfma_f32_16x16x32_bf16 v[196:199], a[52:55], v[224:227], v[196:199]\n"
      "ds_read_b64_tr_b16 a[138:139], v243 offset:50048\n"
      "v_mfma_f32_16x16x32_bf16 v[200:203], a[56:59], v[224:227], v[200:203]\n"
      "ds_read_b128 v[220:223], v235 offset:8192\n"
      "global_load_dwordx4 v[244:247], v248, s[28:29] sc1\n"
      "v_mfma_f32_16x16x32_bf16 v[204:207], a[60:63], v[224:227], v[204:207]\n"
      "ds_read_b64_tr_b16 a[140:141], v243 offset:49288\n"
      "v_mfma_f32_16x16x32_bf16 v[208:211], a[64:67], v[224:227], v[208:211]\n"
      "ds_read_b64_tr_b16 a[142:143], v243 offset:50056\n"
      "v_mfma_f32_16x16x32_bf16 v[212:215], a[68:71], v[224:227], v[212:215]\n"
      "s_waitcnt lgkmcnt(12)\n"
      "v_mfma_f32_16x16x32_bf16 v[0:3], a[72:75], v[228:231], v[0:3]\n"
      "v_mfma_f32_16x16x32_bf16 v[4:7], a[76:79], v[228:231], v[4:7]\n"
      "v_mfma_f32_16x16x32_bf16 v[8:11], a[80:83], v[228:231], v[8:11]\n"
      "ds_read_b128 v[224:227], v235 offset:12288\n"
      "v_mfma_f32_16x16x32_bf16 v[12:15], a[84:87], v[228:231], v[12:15]\n"
      "v_mfma_f32_16x16x32_bf16 v[16:19], a[88:91], v[228:231], v[16:19]\n"
      "s_waitcnt vmcnt(7)\n"
      "v_mfma_f32_16x16x32_bf16 v[20:23], a[92:95], v[228:231], v[20:23]\n"
      "s_waitcnt lgkmcnt(8)\n"
      "v_mfma_f32_16x16x32_bf16 v[24:27], a[72:75], v[216:219], v[24:27]\n"
      "ds_read_b64_tr_b16 a[144:145], v243 offset:0\n"
      "v_mfma_f32_16x16x32_bf16 v[28:31], a[76:79], v[216:219], v[28:31]\n"
      "ds_read_b64_tr_b16 a[146:147], v243 offset:768\n"
      "v_mfma_f32_16x16x32_bf16 v[32:35], a[80:83], v[216:219], v[32:35]\n"
      "ds_read_b128 v[228:231], v235 offset:16384\n"
      "s_waitcnt lgkmcnt(4)\n"
      "s_add_u32 s78, s64, 0x30000\n"
      "s_addc_u32 s79, s65, 0\n"
      "s_add_u32 m0, s74, 0xc000\n"
      "v_mfma_f32_16x16x32_bf16 v[36:39], a[84:87], v[216:219], v[36:39]\n"
      "ds_read_b64_tr_b16 a[148:149], v243 offset:8\n"
      "global_load_lds_dwordx4 v240, s[78:79]\n"
      "v_mfma_f32_16x16x32_bf16 v[40:43], a[88:91], v[216:219], v[40:43]\n"
      "ds_read_b64_tr_b16 a[150:151], v243 offset:776\n"
      "v_mfma_f32_16x16x32_bf16 v[44:47], a[92:95], v[216:219], v[44:47]\n"
      "v_mfma_f32_16x16x32_bf16 v[48:51], a[72:75], v[220:223], v[48:51]\n"
      "ds_read_b64_tr_b16 a[152:153], v243 offset:64\n"
      "s_add_u32 m0, s74, 0xc400\n"
      "v_mfma_f32_16x16x32_bf16 v[52:55], a[76:79], v[220:223], v[52:55]\n"
      "ds_read_b64_tr_b16 a[154:155], v243 offset:832\n"
      "global_load_lds_dwordx4 v241, s[78:79]\n"
      "v_mfma_f32_16x16x32_bf16 v[56:59], a[80:83], v[220:223], v[56:59]\n"
      "ds_read_b128 v[216:219], v235 offset:20480\n"
      "v_mfma_f32_16x16x32_bf16 v[60:63], a[84:87], v[220:223], v[60:63]\n"
      "ds_read_b64_tr_b16 a[156:157], v243 offset:72\n"
      "v_mfma_f32_16x16x32_bf16 v[64:67], a[88:91], v[220:223], v[64:67]\n"
      "ds_read_b64_tr_b16 a[158:159], v243 offset:840\n"
      "s_add_u32 m0, s74, 0xc800\n"
      "v_mfma_f32_16x16x32_bf16 v[68:71], a[92:95], v[220:223], v[68:71]\n"
      "global_load_lds_dwordx4 v242, s[78:79]\n"
      "s_waitcnt lgkmcnt(10)\n"
      "v_mfma_f32_16x16x32_bf16 v[72:75], a[72:75], v[224:227], v[72:75]\n"
      "ds_read_b64_tr_b16 a[160:161], v243 offset:128\n"
      "v_mfma_f32_16x16x32_bf16 v[76:79], a[76:79], v[224:227], v[76:79]\n"
      "ds_read_b64_tr_b16 a[162:163], v243 offset:896\n"
      "v_mfma_f32_16x16x32_bf16 v[80:83], a[80:83], v[224:227], v[80:83]\n"
      "ds_read_b128 v[220:223], v235 offset:24576\n"
      "s_add_u32 s78, s64, 0x33000\n"
      "s_addc_u32 s79, s65, 0\n"
      "s_add_u32 m0, s74, 0xcc00\n"
      "v_mfma_f32_16x16x32_bf16 v[84:87], a[84:87], v[224:227], v[84:87]\n"
      "ds_read_b64_tr_b16 a[164:165], v243 offset:136\n"
      "global_load_lds_dwordx4 v240, s[78:79]\n"
      "v_mfma_f32_16x16x32_bf16 v[88:91], a[88:91], v[224:227], v[88:91]\n"
      "ds_read_b64_tr_b16 a[166:167], v243 offset:904\n"
      "v_mfma_f32_16x16x32_bf16 v[92:95], a[92:95], v[224:227], v[92:95]\n"
      "s_waitcnt lgkmcnt(12)\n"
      "v_mfma_f32_16x16x32_bf16 v[96:99], a[72:75], v[228:231], v[96:99]\n"
      "s_add_u32 m0, s74, 0xd000\n"
      "v_mfma_f32_16x16x32_bf16 v[100:103], a[76:79], v[228:231], v[100:103]\n"
      "global_load_lds_dwordx4 v241, s[78:79]\n"
      "v_mfma_f32_16x16x32_bf16 v[104:107], a[80:83], v[228:231], v[104:107]\n"
      "ds_read_b128 v[224:227], v235 offset:28672\n"
      "v_mfma_f32_16x16x32_bf16 v[108:111], a[84:87], v[228:231], v[108:111]\n"
      "v_mfma_f32_16x16x32_bf16 v[112:115], a[88:91], v[228:231], v[112:115]\n"
      "s_waitcnt vmcnt(6)\n"
      "s_add_u32 m0, s74, 0xd400\n"
      "v_mfma_f32_16x16x32_bf16 v[116:119], a[92:95], v[228:231], v[116:119]\n"
      "global_load_lds_dwordx4 v242, s[78:79]\n"
      "s_waitcnt lgkmcnt(8)\n"
      "v_mfma_f32_16x16x32_bf16 v[120:123], a[72:75], v[216:219], v[120:123]\n"
      "ds_read_b64_tr_b16 a[168:169], v243 offset:24576\n"
      "v_mfma_f32_16x16x32_bf16 v[124:127], a[76:79], v[216:219], v[124:127]\n"
      "ds_read_b64_tr_b16 a[170:171], v243 offset:25344\n"
      "v_mfma_f32_16x16x32_bf16 v[128:131], a[80:83], v[216:219], v[128:131]\n"
      "ds_read_b128 v[228:231], v235 offset:32768\n"
      "v_add_u32 v235, 0x9000, v235\n"
      "s_waitcnt lgkmcnt(4)\n"
      "s_add_u32 s78, s64, 0x36000\n"
      "s_addc_u32 s79, s65, 0\n"
      "s_add_u32 m0, s74, 0x0\n"
      "v_mfma_f32_16x16x32_bf16 v[132:135], a[84:87], v[216:219], v[132:135]\n"
      "ds_read_b64_tr_b16 a[172:173], v243 offset:24584\n"
      "global_load_lds_dwordx4 v240, s[78:79]\n"
      "v_mfma_f32_16x16x32_bf16 v[136:139], a[88:91], v[216:219], v[136:139]\n"
      "ds_read_b64_tr_b16 a[174:175], v243 offset:25352\n"
      "v_mfma_f32_16x16x32_bf16 v[140:143], a[92:95], v[216:219], v[140:143]\n"
      "v_mfma_f32_16x16x32_bf16 v[144:147], a[72:75], v[220:223], v[144:147]\n"
      "ds_read_b64_tr_b16 a[176:177], v243 offset:24640\n"
      "s_waitcnt vmcnt(7)\n"
      "v_cmp_ne_u64 vcc, s[80:81], v[244:245]\n"
      "v_cmp_ne_u64 s[56:57], s[80:81], v[246:247]\n"
      "s_nop 3\n"
      "s_or_b64 vcc, vcc, s[56:57]\n"
      "s_cbranch_scc0 LP1_1\n"
      "s_mov_b32 s82, 0x40000\n"
      "LP0_1:\n"
      "s_sleep 2\n"
      "global_load_dwordx4 v[244:247], v248, s[28:29] sc1\n"
      "s_sub_u32 s82, s82, 1\n"
      "s_cbranch_scc1 LP2_1\n"
      "s_waitcnt vmcnt(0)\n"
      "v_cmp_ne_u64 vcc, s[80:81], v[244:245]\n"
      "v_cmp_ne_u64 s[56:57], s[80:81], v[246:247]\n"
      "s_nop 3\n"
      "s_or_b64 vcc, vcc, s[56:57]\n"
      "s_cbranch_scc1 LP0_1\n"
      "LP2_1:\n"
      "s_waitcnt vmcnt(0)\n"
      "LP1_1:\n"
      "s_nop 0\n"
      "v_mfma_f32_16x16x32_bf16 v[148:151], a[76:79], v[220:223], v[148:151]\n"
      "s_barrier\n"
      "s_add_u32 m0, s74, 0x400\n"
      "v_mfma_f32_16x16x32_bf16 v[152:155], a[80:83], v[220:223], v[152:155]\n"
      "ds_read_b128 v[216:219], v232 offset:0\n"
      "global_load_lds_dwordx4 v241, s[78:79]\n"
      "v_mfma_f32_16x16x32_bf16 v[156:159], a[84:87], v[220:223], v[156:159]\n"
      "ds_read_b64_tr_b16 a[178:179], v243 offset:25408\n"
      "v_mfma_f32_16x16x32_bf16 v[160:163], a[88:91], v[220:223], v[160:163]\n"
      "ds_read_b64_tr_b16 a[180:181], v243 offset:24648\n"
      "v_mfma_f32_16x16x32_bf16 v[164:167], a[92:95], v[220:223], v[164:167]\n"
      "s_add_u32 m0, s74, 0x800\n"
      "s_waitcnt lgkmcnt(9)\n"
      "v_mfma_f32_16x16x32_bf16 v[168:171], a[72:75], v[224:227], v[168:171]\n"
      "ds_read_b64_tr_b16 a[182:183], v243 offset:25416\n"
      "global_load_lds_dwordx4 v242, s[78:79]\n"
      "v_mfma_f32_16x16x32_bf16 v[172:175], a[76:79], v[224:227], v[172:175]\n"
      "ds_read_b64_tr_b16 a[184:185], v243 offset:24704\n"
      "v_mfma_f32_16x16x32_bf16 v[176:179], a[80:83], v[224:227], v[176:179]\n"
      "ds_read_b128 v[220:223], v232 offset:4096\n"
      "v_mfma_f32_16x16x32_bf16 v[180:183], a[84:87], v[224:227], v[180:183]\n"
      "ds_read_b64_tr_b16 a[186:187], v243 offset:25472\n"
      "s_add_u32 s78, s64, 0x39000\n"
      "s_addc_u32 s79, s65, 0\n"
      "s_add_u32 m0, s74, 0xc00\n"
      "v_mfma_f32_16x16x32_bf16 v[184:187], a[88:91], v[224:227], v[184:187]\n"
      "ds_read_b64_tr_b16 a[188:189], v243 offset:24712\n"
      "global_load_lds_dwordx4 v240, s[78:79]\n"
      "v_mfma_f32_16x16x32_bf16 v[188:191], a[92:95], v[224:227], v[188:191]\n"
      "s_waitcnt lgkmcnt(11)\n"
      "v_mfma_f32_16x16x32_bf16 v[192:195], a[72:75], v[228:231], v[192:195]\n"
      "ds_read_b64_tr_b16 a[190:191], v243 offset:25480\n"
      "v_mfma_f32_16x16x32_bf16 v[196:199], a[76:79], v[228:231], v[196:199]\n"
      "s_add_u32 m0, s74, 0x1000\n"
      "v_mfma_f32_16x16x32_bf16 v[200:203], a[80:83], v[228:231], v[200:203]\n"
      "ds_read_b128 v[224:227], v232 offset:8192\n"
      "global_load_lds_dwordx4 v241, s[78:79]\n"
      "v_mfma_f32_16x16x32_bf16 v[204:207], a[84:87], v[228:231], v[204:207]\n"
      "v_mfma_f32_16x16x32_bf16 v[208:211], a[88:91], v[228:231], v[208:211]\n"
      "v_mfma_f32_16x16x32_bf16 v[212:215], a[92:95], v[228:231], v[212:215]\n"
      "s_add_u32 m0, s74, 0x1400\n"
      "s_waitcnt lgkmcnt(9)\n"
      "v_mfma_f32_16x16x32_bf16 v[0:3], a[96:99], v[216:219], v[0:3]\n"
      "global_load_lds_dwordx4 v242, s[78:79]\n"
      "v_mfma_f32_16x16x32_bf16 v[4:7], a[100:103], v[216:219], v[4:7]\n"
      "s_barrier\n"
      "v_mfma_f32_16x16x32_bf16 v[8:11], a[104:107], v[216:219], v[8:11]\n"
      "ds_read_b128 v[228:231], v232 offset:12288\n"
      "v_mfma_f32_16x16x32_bf16 v[12:15], a[108:111], v[216:219], v[12:15]\n"
      "s_add_u32 s58, s24, 0x0\n"
      "s_addc_u32 s59, s25, 0\n"
      "global_load_dwordx4 a[240:243], v239, s[58:59] offset:0 sc1\n"
      "v_mfma_f32_16x16x32_bf16 v[16:19], a[112:115], v[216:219], v[16:19]\n"
      "v_mfma_f32_16x16x32_bf16 v[20:23], a[116:119], v[216:219], v[20:23]\n"
      "s_waitcnt lgkmcnt(5)\n"
      "v_mfma_f32_16x16x32_bf16 v[24:27], a[96:99], v[220:223], v[24:27]\n"
      "v_mfma_f32_16x16x32_bf16 v[28:31], a[100:103], v[220:223], v[28:31]\n"
      "s_waitcnt lgkmcnt(2)\n"
      "s_add_u32 s78, s64, 0x3c000\n"
      "s_addc_u32 s79, s65, 0\n"
      "s_add_u32 m0, s74, 0x6000\n"
      "v_mfma_f32_16x16x32_bf16 v[32:35], a[104:107], v[220:223], v[32:35]\n"
      "ds_read_b128 v[216:219], v232 offset:16384\n"
      "global_load_lds_dwordx4 v240, s[78:79]\n"
      "v_mfma_f32_16x16x32_bf16 v[36:39], a[108:111], v[220:223], v[36:39]\n"
      "v_mfma_f32_16x16x32_bf16 v[40:43], a[112:115], v[220:223], v[40:43]\n"
      "s_waitcnt vmcnt(8)\n"
      "v_mfma_f32_16x16x32_bf16 v[44:47], a[116:119], v[220:223], v[44:47]\n"
      "s_add_u32 m0, s74, 0x6400\n"
      "s_waitcnt lgkmcnt(2)\n"
      "v_mfma_f32_16x16x32_bf16 v[48:51], a[96:99], v[224:227], v[48:51]\n"
      "ds_read_b64_tr_b16 a[192:193], v243 offset:49152\n"
      "global_load_lds_dwordx4 v241, s[78:79]\n"
      "v_mfma_f32_16x16x32_bf16 v[52:55], a[100:103], v[224:227], v[52:55]\n"
      "ds_read_b64_tr_b16 a[194:195], v243 offset:49920\n"
      "v_mfma_f32_16x16x32_bf16 v[56:59], a[104:107], v[224:227], v[56:59]\n"
      "ds_read_b128 v[220:223], v232 offset:20480\n"
      "v_mfma_f32_16x16x32_bf16 v[60:63], a[108:111], v[224:227], v[60:63]\n"
      "ds_read_b64_tr_b16 a[196:197], v243 offset:49160\n"
      "s_add_u32 m0, s74, 0x6800\n"
      "v_mfma_f32_16x16x32_bf16 v[64:67], a[112:115], v[224:227], v[64:67]\n"
      "ds_read_b64_tr_b16 a[198:199], v243 offset:49928\n"
      "global_load_lds_dwordx4 v242, s[78:79]\n"
      "v_mfma_f32_16x16x32_bf16 v[68:71], a[116:119], v[224:227], v[68:71]\n"
      "s_waitcnt lgkmcnt(6)\n"
      "v_mfma_f32_16x16x32_bf16 v[72:75], a[96:99], v[228:231], v[72:75]\n"
      "ds_read_b64_tr_b16 a[200:201], v243 offset:49216\n"
      "v_mfma_f32_16x16x32_bf16 v[76:79], a[100:103], v[228:231], v[76:79]\n"
      "ds_read_b64_tr_b16 a[202:203], v243 offset:49984\n"
      "s_add_u32 s78, s64, 0x3f000\n"
      "s_addc_u32 s79, s65, 0\n"
      "s_add_u32 m0, s74, 0x6c00\n"
      "v_mfma_f32_16x16x32_bf16 v[80:83], a[104:107], v[228:231], v[80:83]\n"
      "ds_read_b128 v[224:227], v232 offset:24576\n"
      "global_load_lds_dwordx4 v240, s[78:79]\n"
      "v_mfma_f32_16x16x32_bf16 v[84:87], a[108:111], v[228:231], v[84:87]\n"
      "ds_read_b64_tr_b16 a[204:205], v243 offset:49224\n"
      "v_mfma_f32_16x16x32_bf16 v[88:91], a[112:115], v[228:231], v[88:91]\n"
      "ds_read_b64_tr_b16 a[206:207], v243 offset:49992\n"
      "v_mfma_f32_16x16x32_bf16 v[92:95], a[116:119], v[228:231], v[92:95]\n"
      "s_add_u32 m0, s74, 0x7000\n"
      "s_waitcnt lgkmcnt(10)\n"
      "v_mfma_f32_16x16x32_bf16 v[96:99], a[96:99], v[216:219], v[96:99]\n"
      "ds_read_b64_tr_b16 a[208:209], v243 offset:49280\n"
      "global_load_lds_dwordx4 v241, s[78:79]\n"
      "v_mfma_f32_16x16x32_bf16 v[100:103], a[100:103], v[216:219], v[100:103]\n"
      "ds_read_b64_tr_b16 a[210:211], v243 offset:50048\n"
      "v_mfma_f32_16x16x32_bf16 v[104:107], a[104:107], v[216:219], v[104:107]\n"
      "ds_read_b128 v[228:231], v232 offset:28672\n"
      "v_mfma_f32_16x16x32_bf16 v[108:111], a[108:111], v[216:219], v[108:111]\n"
      "ds_read_b64_tr_b16 a[212:213], v243 offset:49288\n"
      "s_add_u32 m0, s74, 0x7400\n"
      "v_mfma_f32_16x16x32_bf16 v[112:115], a[112:115], v[216:219], v[112:115]\n"
      "ds_read_b64_tr_b16 a[214:215], v243 offset:50056\n"
      "global_load_lds_dwordx4 v242, s[78:79]\n"
      "v_mfma_f32_16x16x32_bf16 v[116:119], a[116:119], v[216:219], v[116:119]\n"
      "s_waitcnt lgkmcnt(12)\n"
      "v_mfma_f32_16x16x32_bf16 v[120:123], a[96:99], v[220:223], v[120:123]\n"
      "v_mfma_f32_16x16x32_bf16 v[124:127], a[100:103], v[220:223], v[124:127]\n"
      "global_load_dwordx4 a[244:247], v239, s[58:59] offset:1024 sc1\n"
      "v_mfma_f32_16x16x32_bf16 v[128:131], a[104:107], v[220:223], v[128:131]\n"
      "ds_read_b128 v[216:219], v232 offset:32768\n"
      "v_add_u32 v232, 0xffff7000, v232\n"
      "v_mfma_f32_16x16x32_bf16 v[132:135], a[108:111], v[220:223], v[132:135]\n"
      "v_mfma_f32_16x16x32_bf16 v[136:139], a[112:115], v[220:223], v[136:139]\n"
      "v_mfma_f32_16x16x32_bf16 v[140:143], a[116:119], v[220:223], v[140:143]\n"
      "global_load_dwordx4 a[248:251], v239, s[58:59] offset:2048 sc1\n"
      "s_waitcnt lgkmcnt(8)\n"
      "v_mfma_f32_16x16x32_bf16 v[144:147], a[96:99], v[224:227], v[144:147]\n"
      "s_waitcnt vmcnt(9)\n"
      "v_mfma_f32_16x16x32_bf16 v[148:151], a[100:103], v[224:227], v[148:151]\n"
      "ds_read_b64_tr_b16 a[216:217], v243 offset:0\n"
      "v_mfma_f32_16x16x32_bf16 v[152:155], a[104:107], v[224:227], v[152:155]\n"
      "ds_read_b128 v[220:223], v233 offset:0\n"
      "v_mfma_f32_16x16x32_bf16 v[156:159], a[108:111], v[224:227], v[156:159]\n"
      "ds_read_b64_tr_b16 a[218:219], v243 offset:768\n"
      "s_waitcnt lgkmcnt(4)\n"
      "s_add_u32 s78, s64, 0x42000\n"
      "s_addc_u32 s79, s65, 0\n"
      "s_add_u32 m0, s74, 0xc000\n"
      "v_mfma_f32_16x16x32_bf16 v[160:163], a[112:115], v[224:227], v[160:163]\n"
      "ds_read_b64_tr_b16 a[220:221], v243 offset:8\n"
      "global_load_lds_dwordx4 v240, s[78:79]\n"
      "v_mfma_f32_16x16x32_bf16 v[164:167], a[116:119], v[224:227], v[164:167]\n"
      "v_mfma_f32_16x16x32_bf16 v[168:171], a[96:99], v[228:231], v[168:171]\n"
      "ds_read_b64_tr_b16 a[222:223], v243 offset:776\n"
      "v_mfma_f32_16x16x32_bf16 v[172:175], a[100:103], v[228:231], v[172:175]\n"
      "ds_read_b64_tr_b16 a[224:225], v243 offset:64\n"
      "s_add_u32 m0, s74, 0xc400\n"
      "v_mfma_f32_16x16x32_bf16 v[176:179], a[104:107], v[228:231], v[176:179]\n"
      "ds_read_b128 v[224:227], v233 offset:4096\n"
      "global_load_lds_dwordx4 v241, s[78:79]\n"
      "v_mfma_f32_16x16x32_bf16 v[180:183], a[108:111], v[228:231], v[180:183]\n"
      "ds_read_b64_tr_b16 a[226:227], v243 offset:832\n"
      "v_mfma_f32_16x16x32_bf16 v[184:187], a[112:115], v[228:231], v[184:187]\n"
      "ds_read_b64_tr_b16 a[228:229], v243 offset:72\n"
      "v_mfma_f32_16x16x32_bf16 v[188:191], a[116:119], v[228:231], v[188:191]\n"
      "s_add_u32 m0, s74, 0xc800\n"
      "s_waitcnt lgkmcnt(9)\n"
      "v_mfma_f32_16x16x32_bf16 v[192:195], a[96:99], v[216:219], v[192:195]\n"
      "ds_read_b64_tr_b16 a[230:231], v243 offset:840\n"
      "global_load_lds_dwordx4 v242, s[78:79]\n"
      "v_mfma_f32_16x16x32_bf16 v[196:199], a[100:103], v[216:219], v[196:199]\n"
      "ds_read_b64_tr_b16 a[232:233], v243 offset:128\n"
      "v_mfma_f32_16x16x32_bf16 v[200:203], a[104:107], v[216:219], v[200:203]\n"
      "ds_read_b128 v[228:231], v233 offset:8192\n"
      "v_mfma_f32_16x16x32_bf16 v[204:207], a[108:111], v[216:219], v[204:207]\n"
      "ds_read_b64_tr_b16 a[234:235], v243 offset:896\n"
      "s_add_u32 s78, s64, 0x45000\n"
      "s_addc_u32 s79, s65, 0\n"
      "s_add_u32 m0, s74, 0xcc00\n"
      "v_mfma_f32_16x16x32_bf16 v[208:211], a[112:115], v[216:219], v[208:211]\n"
      "ds_read_b64_tr_b16 a[236:237], v243 offset:136\n"
      "global_load_lds_dwordx4 v240, s[78:79]\n"
      "v_mfma_f32_16x16x32_bf16 v[212:215], a[116:119], v[216:219], v[212:215]\n"
      "s_waitcnt lgkmcnt(12)\n"
      "v_mfma_f32_16x16x32_bf16 v[0:3], a[120:123], v[220:223], v[0:3]\n"
      "ds_read_b64_tr_b16 a[238:239], v243 offset:904\n"
      "v_mfma_f32_16x16x32_bf16 v[4:7], a[124:127], v[220:223], v[4:7]\n"
      "s_add_u32 m0, s74, 0xd000\n"
      "v_mfma_f32_16x16x32_bf16 v[8:11], a[128:131], v[220:223], v[8:11]\n"
      "ds_read_b128 v[216:219], v233 offset:12288\n"
      "global_load_lds_dwordx4 v241, s[78:79]\n"
      "v_mfma_f32_16x16x32_bf16 v[12:15], a[132:135], v[220:223], v[12:15]\n"
      "v_mfma_f32_16x16x32_bf16 v[16:19], a[136:139], v[220:223], v[16:19]\n"
      "v_mfma_f32_16x16x32_bf16 v[20:23], a[140:143], v[220:223], v[20:23]\n"
      "s_add_u32 m0, s74, 0xd400\n"
      "s_waitcnt lgkmcnt(9)\n"
      "v_mfma_f32_16x16x32_bf16 v[24:27], a[120:123], v[224:227], v[24:27]\n"
      "global_load_lds_dwordx4 v242, s[78:79]\n"
      "v_mfma_f32_16x16x32_bf16 v[28:31], a[124:127], v[224:227], v[28:31]\n"
      "v_mfma_f32_16x16x32_bf16 v[32:35], a[128:131], v[224:227], v[32:35]\n"
      "ds_read_b128 v[220:223], v233 offset:16384\n"
      "v_mfma_f32_16x16x32_bf16 v[36:39], a[132:135], v[224:227], v[36:39]\n"
      "s_waitcnt vmcnt(8)\n"
      "v_mad_i32_i24 v238, v251, s89, v238\n"
      "s_add_u32 s56, s22, 0x0\n"
      "s_addc_u32 s57, s23, 0\n"
      "s_add_u32 m0, s18, 0xfffffe00\n"
      "v_mfma_f32_16x16x32_bf16 v[40:43], a[136:139], v[224:227], v[40:43]\n"
      "ds_read_b64_tr_b16 a[0:1], v243 offset:24576\n"
      "global_load_lds_dwordx4 v238, s[56:57] offset:512 nt\n"
      "v_mfma_f32_16x16x32_bf16 v[44:47], a[140:143], v[224:227], v[44:47]\n"
      "s_waitcnt lgkmcnt(6)\n"
      "v_mfma_f32_16x16x32_bf16 v[48:51], a[120:123], v[228:231], v[48:51]\n"
      "ds_read_b64_tr_b16 a[2:3], v243 offset:25344\n"
      "v_mad_i32_i24 v238, v251, s90, v238\n"
      "s_add_u32 s56, s22, 0x6000\n"
      "s_addc_u32 s57, s23, 0\n"
      "s_add_u32 m0, s18, 0xe00\n"
      "v_mfma_f32_16x16x32_bf16 v[52:55], a[124:127], v[228:231], v[52:55]\n"
      "ds_read_b64_tr_b16 a[4:5], v243 offset:24584\n"
      "global_load_lds_dwordx4 v238, s[56:57] offset:512 nt\n"
      "v_mfma_f32_16x16x32_bf16 v[56:59], a[128:131], v[228:231], v[56:59]\n"
      "ds_read_b128 v[224:227], v233 offset:20480\n"
      "v_mfma_f32_16x16x32_bf16 v[60:63], a[132:135], v[228:231], v[60:63]\n"
      "ds_read_b64_tr_b16 a[6:7], v243 offset:25352\n"
      "v_mad_i32_i24 v238, v251, s91, v238\n"
      "s_add_u32 s56, s22, 0xc000\n"
      "s_addc_u32 s57, s23, 0\n"
      "s_add_u32 m0, s18, 0x1e00\n"
      "v_mfma_f32_16x16x32_bf16 v[64:67], a[136:139], v[228:231], v[64:67]\n"
      "ds_read_b64_tr_b16 a[8:9], v243 offset:24640\n"
      "global_load_lds_dwordx4 v238, s[56:57] offset:512 nt\n"
      "v_mfma_f32_16x16x32_bf16 v[68:71], a[140:143], v[228:231], v[68:71]\n"
      "s_waitcnt lgkmcnt(7)\n"
      "v_mfma_f32_16x16x32_bf16 v[72:75], a[120:123], v[216:219], v[72:75]\n"
      "ds_read_b64_tr_b16 a[10:11], v243 offset:25408\n"
      "v_mad_i32_i24 v238, v251, s92, v238\n"
      "s_add_u32 s56, s22, 0x12000\n"
      "s_addc_u32 s57, s23, 0\n"
      "s_add_u32 m0, s18, 0x2e00\n"
      "v_mfma_f32_16x16x32_bf16 v[76:79], a[124:127], v[216:219], v[76:79]\n"
      "ds_read_b64_tr_b16 a[12:13], v243 offset:24648\n"
      "global_load_lds_dwordx4 v238, s[56:57] offset:512 nt\n"
      "v_mfma_f32_16x16x32_bf16 v[80:83], a[128:131], v[216:219], v[80:83]\n"
      "ds_read_b128 v[228:231], v233 offset:24576\n"
      "v_mfma_f32_16x16x32_bf16 v[84:87], a[132:135], v[216:219], v[84:87]\n"
      "ds_read_b64_tr_b16 a[14:15], v243 offset:25416\n"
      "v_mad_i32_i24 v238, v251, s93, v238\n"
      "s_add_u32 s56, s22, 0x18000\n"
      "s_addc_u32 s57, s23, 0\n"
      "s_add_u32 m0, s18, 0x3e00\n"
      "v_mfma_f32_16x16x32_bf16 v[88:91], a[136:139], v[216:219], v[88:91]\n"
      "ds_read_b64_tr_b16 a[16:17], v243 offset:24704\n"
      "global_load_lds_dwordx4 v238, s[56:57] offset:512 nt\n"
      "v_mfma_f32_16x16x32_bf16 v[92:95], a[140:143], v[216:219], v[92:95]\n"
      "s_waitcnt lgkmcnt(11)\n"
      "v_mfma_f32_16x16x32_bf16 v[96:99], a[120:123], v[220:223], v[96:99]\n"
      "ds_read_b64_tr_b16 a[18:19], v243 offset:25472\n"
      "v_mad_i32_i24 v238, v251, s94, v238\n"
      "s_add_u32 s56, s22, 0x1e000\n"
      "s_addc_u32 s57, s23, 0\n"
      "s_add_u32 m0, s18, 0x4e00\n"
      "v_mfma_f32_16x16x32_bf16 v[100:103], a[124:127], v[220:223], v[100:103]\n"
      "ds_read_b64_tr_b16 a[20:21], v243 offset:24712\n"
      "global_load_lds_dwordx4 v238, s[56:57] offset:512 nt\n"
      "v_mfma_f32_16x16x32_bf16 v[104:107], a[128:131], v[220:223], v[104:107]\n"
      "ds_read_b128 v[216:219], v233 offset:28672\n"
      "v_mfma_f32_16x16x32_bf16 v[108:111], a[132:135], v[220:223], v[108:111]\n"
      "ds_read_b64_tr_b16 a[22:23], v243 offset:25480\n"
      "v_mad_i32_i24 v238, v251, s95, v238\n"
      "s_add_u32 s56, s22, 0x24000\n"
      "s_addc_u32 s57, s23, 0\n"
      "s_add_u32 m0, s18, 0x5e00\n"
      "v_mfma_f32_16x16x32_bf16 v[112:115], a[136:139], v[220:223], v[112:115]\n"
      "global_load_lds_dwordx4 v238, s[56:57] offset:512 nt\n"
      "v_mfma_f32_16x16x32_bf16 v[116:119], a[140:143], v[220:223], v[116:119]\n"
      "s_waitcnt lgkmcnt(11)\n"
      "v_mfma_f32_16x16x32_bf16 v[120:123], a[120:123], v[224:227], v[120:123]\n"
      "v_mad_i32_i24 v238, v251, s96, v238\n"
      "s_add_u32 s56, s22, 0x2a000\n"
      "s_addc_u32 s57, s23, 0\n"
      "s_add_u32 m0, s18, 0x6e00\n"
      "v_mfma_f32_16x16x32_bf16 v[124:127], a[124:127], v[224:227], v[124:127]\n"
      "global_load_lds_dwordx4 v238, s[56:57] offset:512 nt\n"
      "v_mfma_f32_16x16x32_bf16 v[128:131], a[128:131], v[224:227], v[128:131]\n"
      "ds_read_b128 v[220:223], v233 offset:32768\n"
      "v_add_u32 v233, 0xffff7000, v233\n"
      "v_mfma_f32_16x16x32_bf16 v[132:135], a[132:135], v[224:227], v[132:135]\n"
      "v_mad_i32_i24 v238, v251, s97, v238\n"
      "s_add_u32 s56, s22, 0x30000\n"
      "s_addc_u32 s57, s23, 0\n"
      "s_add_u32 m0, s18, 0x7e00\n"
      "v_mfma_f32_16x16x32_bf16 v[136:139], a[136:139], v[224:227], v[136:139]\n"
      "global_load_lds_dwordx4 v238, s[56:57] offset:512 nt\n"
      "v_mfma_f32_16x16x32_bf16 v[140:143], a[140:143], v[224:227], v[140:143]\n"
      "s_waitcnt lgkmcnt(7)\n"
      "v_mfma_f32_16x16x32_bf16 v[144:147], a[120:123], v[228:231], v[144:147]\n"
      "v_mfma_f32_16x16x32_bf16 v[148:151], a[124:127], v[228:231], v[148:151]\n"
      "global_load_dwordx4 a[252:255], v239, s[58:59] offset:3072 sc1\n"
      "v_mfma_f32_16x16x32_bf16 v[152:155], a[128:131], v[228:231], v[152:155]\n"
      "ds_read_b128 v[224:227], v234 offset:0\n"
      "v_mfma_f32_16x16x32_bf16 v[156:159], a[132:135], v[228:231], v[156:159]\n"
      "v_mfma_f32_16x16x32_bf16 v[160:163], a[136:139], v[228:231], v[160:163]\n"
      "v_mfma_f32_16x16x32_bf16 v[164:167], a[140:143], v[228:231], v[164:167]\n"
      "s_add_u32 s58, s24, 0x1000\n"
      "s_addc_u32 s59, s25, 0\n"
      "global_load_dwordx4 a[48:51], v239, s[58:59] offset:0 sc1\n"
      "s_waitcnt lgkmcnt(3)\n"
      "v_mfma_f32_16x16x32_bf16 v[168:171], a[120:123], v[216:219], v[168:171]\n"
      "s_waitcnt vmcnt(11)\n"
      "v_mfma_f32_16x16x32_bf16 v[172:175], a[124:127], v[216:219], v[172:175]\n"
      "ds_read_b64_tr_b16 a[24:25], v243 offset:49152\n"
      "v_mfma_f32_16x16x32_bf16 v[176:179], a[128:131], v[216:219], v[176:179]\n"
      "ds_read_b128 v[228:231], v234 offset:4096\n"
      "v_mfma_f32_16x16x32_bf16 v[180:183], a[132:135], v[216:219], v[180:183]\n"
      "ds_read_b64_tr_b16 a[26:27], v243 offset:49920\n"
      "global_load_dwordx4 a[52:55], v239, s[58:59] offset:1024 sc1\n"
      "v_mfma_f32_16x16x32_bf16 v[184:187], a[136:139], v[216:219], v[184:187]\n"
      "ds_read_b64_tr_b16 a[28:29], v243 offset:49160\n"
      "v_mfma_f32_16x16x32_bf16 v[188:191], a[140:143], v[216:219], v[188:191]\n"
      "s_waitcnt lgkmcnt(5)\n"
      "v_mfma_f32_16x16x32_bf16 v[192:195], a[120:123], v[220:223], v[192:195]\n"
      "ds_read_b64_tr_b16 a[30:31], v243 offset:49928\n"
      "v_mfma_f32_16x16x32_bf16 v[196:199], a[124:127], v[220:223], v[196:199]\n"
      "ds_read_b64_tr_b16 a[32:33], v243 offset:49216\n"
      "global_load_dwordx4 a[56:59], v239, s[58:59] offset:2048 sc1\n"
      "v_mfma_f32_16x16x32_bf16 v[200:203], a[128:131], v[220:223], v[200:203]\n"
      "ds_read_b128 v[216:219], v234 offset:8192\n"
      "v_mfma_f32_16x16x32_bf16 v[204:207], a[132:135], v[220:223], v[204:207]\n"
      "ds_read_b64_tr_b16 a[34:35], v243 offset:49984\n"
      "v_mfma_f32_16x16x32_bf16 v[208:211], a[136:139], v[220:223], v[208:211]\n"
      "ds_read_b64_tr_b16 a[36:37], v243 offset:49224\n"
      "v_mfma_f32_16x16x32_bf16 v[212:215], a[140:143], v[220:223], v[212:215]\n"
      "global_load_dwordx4 a[60:63], v239, s[58:59] offset:3072 sc1\n"
      "s_waitcnt lgkmcnt(9)\n"
      "v_mfma_f32_16x16x32_bf16 v[0:3], a[144:147], v[224:227], v[0:3]\n"
      "ds_read_b64_tr_b16 a[38:39], v243 offset:49992\n"
      "v_mfma_f32_16x16x32_bf16 v[4:7], a[148:151], v[224:227], v[4:7]\n"
      "ds_read_b64_tr_b16 a[40:41], v243 offset:49280\n"
      "v_mfma_f32_16x16x32_bf16 v[8:11], a[152:155], v[224:227], v[8:11]\n"
      "ds_read_b128 v[220:223], v234 offset:12288\n"
      "v_mfma_f32_16x16x32_bf16 v[12:15], a[156:159], v[224:227], v[12:15]\n"
      "ds_read_b64_tr_b16 a[42:43], v243 offset:50048\n"
      "s_add_u32 s58, s24, 0x2000\n"
      "s_addc_u32 s59, s25, 0\n"
      "global_load_dwordx4 a[64:67], v239, s[58:59] offset:0 sc1\n"
      "v_mfma_f32_16x16x32_bf16 v[16:19], a[160:163], v[224:227], v[16:19]\n"
      "ds_read_b64_tr_b16 a[44:45], v243 offset:49288\n"
      "v_mfma_f32_16x16x32_bf16 v[20:23], a[164:167], v[224:227], v[20:23]\n"
      "s_waitcnt lgkmcnt(12)\n"
      "v_mfma_f32_16x16x32_bf16 v[24:27], a[144:147], v[228:231], v[24:27]\n"
      "ds_read_b64_tr_b16 a[46:47], v243 offset:50056\n"
      "v_mfma_f32_16x16x32_bf16 v[28:31], a[148:151], v[228:231], v[28:31]\n"
      "global_load_dwordx4 a[68:71], v239, s[58:59] offset:1024 sc1\n"
      "v_mfma_f32_16x16x32_bf16 v[32:35], a[152:155], v[228:231], v[32:35]\n"
      "ds_read_b128 v[224:227], v234 offset:16384\n"
      "v_mfma_f32_16x16x32_bf16 v[36:39], a[156:159], v[228:231], v[36:39]\n"
      "v_mfma_f32_16x16x32_bf16 v[40:43], a[160:163], v[228:231], v[40:43]\n"
      "v_mfma_f32_16x16x32_bf16 v[44:47], a[164:167], v[228:231], v[44:47]\n"
      "global_load_dwordx4 a[72:75], v239, s[58:59] offset:2048 sc1\n"
      "s_waitcnt lgkmcnt(9)\n"
      "v_mfma_f32_16x16x32_bf16 v[48:51], a[144:147], v[216:219], v[48:51]\n"
      "v_mfma_f32_16x16x32_bf16 v[52:55], a[148:151], v[216:219], v[52:55]\n"
      "v_mfma_f32_16x16x32_bf16 v[56:59], a[152:155], v[216:219], v[56:59]\n"
      "ds_read_b128 v[228:231], v234 offset:20480\n"
      "v_mfma_f32_16x16x32_bf16 v[60:63], a[156:159], v[216:219], v[60:63]\n"
      "global_load_dwordx4 a[76:79], v239, s[58:59] offset:3072 sc1\n"
      "v_mfma_f32_16x16x32_bf16 v[64:67], a[160:163], v[216:219], v[64:67]\n"
      "v_mfma_f32_16x16x32_bf16 v[68:71], a[164:167], v[216:219], v[68:71]\n"
      "s_waitcnt lgkmcnt(5)\n"
      "v_mfma_f32_16x16x32_bf16 v[72:75], a[144:147], v[220:223], v[72:75]\n"
      "v_mfma_f32_16x16x32_bf16 v[76:79], a[148:151], v[220:223], v[76:79]\n"
      "s_add_u32 s58, s24, 0x3000\n"
      "s_addc_u32 s59, s25, 0\n"
      "global_load_dwordx4 a[80:83], v239, s[58:59] offset:0 sc1\n"
      "v_mfma_f32_16x16x32_bf16 v[80:83], a[152:155], v[220:223], v[80:83]\n"
      "ds_read_b128 v[216:219], v234 offset:24576\n"
      "v_mfma_f32_16x16x32_bf16 v[84:87], a[156:159], v[220:223], v[84:87]\n"
      "v_mfma_f32_16x16x32_bf16 v[88:91], a[160:163], v[220:223], v[88:91]\n"
      "v_mfma_f32_16x16x32_bf16 v[92:95], a[164:167], v[220:223], v[92:95]\n"
      "global_load_dwordx4 a[84:87], v239, s[58:59] offset:1024 sc1\n"
      "s_waitcnt lgkmcnt(2)\n"
      "v_mfma_f32_16x16x32_bf16 v[96:99], a[144:147], v[224:227], v[96:99]\n"
      "v_mfma_f32_16x16x32_bf16 v[100:103], a[148:151], v[224:227], v[100:103]\n"
      "v_mfma_f32_16x16x32_bf16 v[104:107], a[152:155], v[224:227], v[104:107]\n"
      "ds_read_b128 v[220:223], v234 offset:28672\n"
      "v_mfma_f32_16x16x32_bf16 v[108:111], a[156:159], v[224:227], v[108:111]\n"
      "global_load_dwordx4 a[88:91], v239, s[58:59] offset:2048 sc1\n"
      "v_mfma_f32_16x16x32_bf16 v[112:115], a[160:163], v[224:227], v[112:115]\n"
      "v_mfma_f32_16x16x32_bf16 v[116:119], a[164:167], v[224:227], v[116:119]\n"
      "s_waitcnt lgkmcnt(2)\n"
      "v_mfma_f32_16x16x32_bf16 v[120:123], a[144:147], v[228:231], v[120:123]\n"
      "v_mfma_f32_16x16x32_bf16 v[124:127], a[148:151], v[228:231], v[124:127]\n"
      "global_load_dwordx4 a[92:95], v239, s[58:59] offset:3072 sc1\n"
      "v_mfma_f32_16x16x32_bf16 v[128:131], a[152:155], v[228:231], v[128:131]\n"
      "ds_read_b128 v[224:227], v234 offset:32768\n"
      "v_add_u32 v234, 0xffff7000, v234\n"
      "v_mfma_f32_16x16x32_bf16 v[132:135], a[156:159], v[228:231], v[132:135]\n"
      "v_mfma_f32_16x16x32_bf16 v[136:139], a[160:163], v[228:231], v[136:139]\n"
      "v_mfma_f32_16x16x32_bf16 v[140:143], a[164:167], v[228:231], v[140:143]\n"
      "s_add_u32 s58, s24, 0x4000\n"
      "s_addc_u32 s59, s25, 0\n"
      "global_load_dwordx4 a[96:99], v239, s[58:59] offset:0 sc1\n"
      "s_waitcnt lgkmcnt(2)\n"
      "v_mfma_f32_16x16x32_bf16 v[144:147], a[144:147], v[216:219], v[144:147]\n"
      "ds_read_b64 v[236:237], v250 offset:4104\n"
      "v_mfma_f32_16x16x32_bf16 v[148:151], a[148:151], v[216:219], v[148:151]\n"
      "v_mfma_f32_16x16x32_bf16 v[152:155], a[152:155], v[216:219], v[152:155]\n"
      "ds_read_b128 v[228:231], v235 offset:0\n"
      "v_mfma_f32_16x16x32_bf16 v[156:159], a[156:159], v[216:219], v[156:159]\n"
      "global_load_dwordx4 a[100:103], v239, s[58:59] offset:1024 sc1\n"
      "v_mfma_f32_16x16x32_bf16 v[160:163], a[160:163], v[216:219], v[160:163]\n"
      "v_mfma_f32_16x16x32_bf16 v[164:167], a[164:167], v[216:219], v[164:167]\n"
      "s_waitcnt lgkmcnt(3)\n"
      "v_mfma_f32_16x16x32_bf16 v[168:171], a[144:147], v[220:223], v[168:171]\n"
      "v_mfma_f32_16x16x32_bf16 v[172:175], a[148:151], v[220:223], v[172:175]\n"
      "global_load_dwordx4 a[104:107], v239, s[58:59] offset:2048 sc1\n"
      "v_mfma_f32_16x16x32_bf16 v[176:179], a[152:155], v[220:223], v[176:179]\n"
      "ds_read_b128 v[216:219], v235 offset:4096\n"
      "v_mfma_f32_16x16x32_bf16 v[180:183], a[156:159], v[220:223], v[180:183]\n"
      "v_mfma_f32_16x16x32_bf16 v[184:187], a[160:163], v[220:223], v[184:187]\n"
      "v_mfma_f32_16x16x32_bf16 v[188:191], a[164:167], v[220:223], v[188:191]\n"
      "global_load_dwordx4 a[108:111], v239, s[58:59] offset:3072 sc1\n"
      "s_waitcnt lgkmcnt(3)\n"
      "v_mfma_f32_16x16x32_bf16 v[192:195], a[144:147], v[224:227], v[192:195]\n"
      "v_mfma_f32_16x16x32_bf16 v[196:199], a[148:151], v[224:227], v[196:199]\n"
      "v_mfma_f32_16x16x32_bf16 v[200:203], a[152:155], v[224:227], v[200:203]\n"
      "ds_read_b128 v[220:223], v235 offset:8192\n"
      "v_mfma_f32_16x16x32_bf16 v[204:207], a[156:159], v[224:227], v[204:207]\n"
      "s_add_u32 s58, s24, 0x5000\n"
      "s_addc_u32 s59, s25, 0\n"
      "global_load_dwordx4 a[112:115], v239, s[58:59] offset:0 sc1\n"
      "v_mfma_f32_16x16x32_bf16 v[208:211], a[160:163], v[224:227], v[208:211]\n"
      "v_mfma_f32_16x16x32_bf16 v[212:215], a[164:167], v[224:227], v[212:215]\n"
      "s_waitcnt lgkmcnt(2)\n"
      "v_mfma_f32_16x16x32_bf16 v[0:3], a[168:171], v[228:231], v[0:3]\n"
      "v_mfma_f32_16x16x32_bf16 v[4:7], a[172:175], v[228:231], v[4:7]\n"
      "global_load_dwordx4 a[116:119], v239, s[58:59] offset:1024 sc1\n"
      "v_mfma_f32_16x16x32_bf16 v[8:11], a[176:179], v[228:231], v[8:11]\n"
      "ds_read_b128 v[224:227], v235 offset:12288\n"
      "v_mfma_f32_16x16x32_bf16 v[12:15], a[180:183], v[228:231], v[12:15]\n"
      "v_mfma_f32_16x16x32_bf16 v[16:19], a[184:187], v[228:231], v[16:19]\n"
      "v_mfma_f32_16x16x32_bf16 v[20:23], a[188:191], v[228:231], v[20:23]\n"
      "global_load_dwordx4 a[120:123], v239, s[58:59] offset:2048 sc1\n"
      "s_waitcnt lgkmcnt(2)\n"
      "v_mfma_f32_16x16x32_bf16 v[24:27], a[168:171], v[216:219], v[24:27]\n"
      "v_mfma_f32_16x16x32_bf16 v[28:31], a[172:175], v[216:219], v[28:31]\n"
      "v_mfma_f32_16x16x32_bf16 v[32:35], a[176:179], v[216:219], v[32:35]\n"
      "ds_read_b128 v[228:231], v235 offset:16384\n"
      "v_mfma_f32_16x16x32_bf16 v[36:39], a[180:183], v[216:219], v[36:39]\n"
      "global_load_dwordx4 a[124:127], v239, s[58:59] offset:3072 sc1\n"
      "v_mfma_f32_16x16x32_bf16 v[40:43], a[184:187], v[216:219], v[40:43]\n"
      "v_mfma_f32_16x16x32_bf16 v[44:47], a[188:191], v[216:219], v[44:47]\n"
      "s_waitcnt lgkmcnt(2)\n"
      "v_mfma_f32_16x16x32_bf16 v[48:51], a[168:171], v[220:223], v[48:51]\n"
      "v_mfma_f32_16x16x32_bf16 v[52:55], a[172:175], v[220:223], v[52:55]\n"
      "s_add_u32 s58, s24, 0x6000\n"
      "s_addc_u32 s59, s25, 0\n"
      "global_load_dwordx4 a[128:131], v239, s[58:59] offset:0 sc1\n"
      "v_mfma_f32_16x16x32_bf16 v[56:59], a[176:179], v[220:223], v[56:59]\n"
      "ds_read_b128 v[216:219], v235 offset:20480\n"
      "v_mfma_f32_16x16x32_bf16 v[60:63], a[180:183], v[220:223], v[60:63]\n"
      "v_mfma_f32_16x16x32_bf16 v[64:67], a[184:187], v[220:223], v[64:67]\n"
      "v_mfma_f32_16x16x32_bf16 v[68:71], a[188:191], v[220:223], v[68:71]\n"
      "global_load_dwordx4 a[132:135], v239, s[58:59] offset:1024 sc1\n"
      "s_waitcnt lgkmcnt(2)\n"
      "v_mfma_f32_16x16x32_bf16 v[72:75], a[168:171], v[224:227], v[72:75]\n"
      "v_mfma_f32_16x16x32_bf16 v[76:79], a[172:175], v[224:227], v[76:79]\n"
      "v_mfma_f32_16x16x32_bf16 v[80:83], a[176:179], v[224:227], v[80:83]\n"
      "ds_read_b128 v[220:223], v235 offset:24576\n"
      "v_mfma_f32_16x16x32_bf16 v[84:87], a[180:183], v[224:227], v[84:87]\n"
      "global_load_dwordx4 a[136:139], v239, s[58:59] offset:2048 sc1\n"
      "v_mfma_f32_16x16x32_bf16 v[88:91], a[184:187], v[224:227], v[88:91]\n"
      "v_mfma_f32_16x16x32_bf16 v[92:95], a[188:191], v[224:227], v[92:95]\n"
      "s_waitcnt lgkmcnt(2)\n"
      "v_mfma_f32_16x16x32_bf16 v[96:99], a[168:171], v[228:231], v[96:99]\n"
      "v_mfma_f32_16x16x32_bf16 v[100:103], a[172:175], v[228:231], v[100:103]\n"
      "global_load_dwordx4 a[140:143], v239, s[58:59] offset:3072 sc1\n"
      "v_mfma_f32_16x16x32_bf16 v[104:107], a[176:179], v[228:231], v[104:107]\n"
      "ds_read_b128 v[224:227], v235 offset:28672\n"
      "v_mfma_f32_16x16x32_bf16 v[108:111], a[180:183], v[228:231], v[108:111]\n"
      "v_mfma_f32_16x16x32_bf16 v[112:115], a[184:187], v[228:231], v[112:115]\n"
      "v_mfma_f32_16x16x32_bf16 v[116:119], a[188:191], v[228:231], v[116:119]\n"
      "s_add_u32 s58, s24, 0x7000\n"
      "s_addc_u32 s59, s25, 0\n"
      "global_load_dwordx4 a[144:147], v239, s[58:59] offset:0 sc1\n"
      "s_waitcnt lgkmcnt(2)\n"
      "v_mfma_f32_16x16x32_bf16 v[120:123], a[168:171], v[216:219], v[120:123]\n"
      "v_mfma_f32_16x16x32_bf16 v[124:127], a[172:175], v[216:219], v[124:127]\n"
      "v_mfma_f32_16x16x32_bf16 v[128:131], a[176:179], v[216:219], v[128:131]\n"
      "ds_read_b128 v[228:231], v235 offset:32768\n"
      "v_add_u32 v235, 0xffff7000, v235\n"
      "v_mfma_f32_16x16x32_bf16 v[132:135], a[180:183], v[216:219], v[132:135]\n"
      "global_load_dwordx4 a[148:151], v239, s[58:59] offset:1024 sc1\n"
      "v_mfma_f32_16x16x32_bf16 v[136:139], a[184:187], v[216:219], v[136:139]\n"
      "v_mfma_f32_16x16x32_bf16 v[140:143], a[188:191], v[216:219], v[140:143]\n"
      "s_waitcnt lgkmcnt(2)\n"
      "v_mfma_f32_16x16x32_bf16 v[144:147], a[168:171], v[220:223], v[144:147]\n"
      "v_mfma_f32_16x16x32_bf16 v[148:151], a[172:175], v[220:223], v[148:151]\n"
      "s_waitcnt vmcnt(27)\n"
      "s_barrier\n"
      "global_load_dwordx4 a[152:155], v239, s[58:59] offset:2048 sc1\n"
      "v_mfma_f32_16x16x32_bf16 v[152:155], a[176:179], v[220:223], v[152:155]\n"
      "ds_read_b128 v[216:219], v232 offset:0\n"
      "v_mfma_f32_16x16x32_bf16 v[156:159], a[180:183], v[220:223], v[156:159]\n"
      "v_mfma_f32_16x16x32_bf16 v[160:163], a[184:187], v[220:223], v[160:163]\n"
      "v_mfma_f32_16x16x32_bf16 v[164:167], a[188:191], v[220:223], v[164:167]\n"
      "global_load_dwordx4 a[156:159], v239, s[58:59] offset:3072 sc1\n"
      "s_waitcnt lgkmcnt(2)\n"
      "v_mfma_f32_16x16x32_bf16 v[168:171], a[168:171], v[224:227], v[168:171]\n"
      "v_mfma_f32_16x16x32_bf16 v[172:175], a[172:175], v[224:227], v[172:175]\n"
      "v_mfma_f32_16x16x32_bf16 v[176:179], a[176:179], v[224:227], v[176:179]\n"
      "ds_read_b128 v[220:223], v232 offset:4096\n"
      "v_mfma_f32_16x16x32_bf16 v[180:183], a[180:183], v[224:227], v[180:183]\n"
      "s_add_u32 s58, s24, 0x8000\n"
      "s_addc_u32 s59, s25, 0\n"
      "global_load_dwordx4 a[160:163], v239, s[58:59] offset:0 sc1\n"
      "v_mfma_f32_16x16x32_bf16 v[184:187], a[184:187], v[224:227], v[184:187]\n"
      "v_mfma_f32_16x16x32_bf16 v[188:191], a[188:191], v[224:227], v[188:191]\n"
      "s_waitcnt lgkmcnt(2)\n"
      "v_mfma_f32_16x16x32_bf16 v[192:195], a[168:171], v[228:231], v[192:195]\n"
      "v_mfma_f32_16x16x32_bf16 v[196:199], a[172:175], v[228:231], v[196:199]\n"
      "global_load_dwordx4 a[164:167], v239, s[58:59] offset:1024 sc1\n"
      "v_mfma_f32_16x16x32_bf16 v[200:203], a[176:179], v[228:231], v[200:203]\n"
      "ds_read_b128 v[224:227], v232 offset:8192\n"
      "v_mfma_f32_16x16x32_bf16 v[204:207], a[180:183], v[228:231], v[204:207]\n"
      "v_mfma_f32_16x16x32_bf16 v[208:211], a[184:187], v[228:231], v[208:211]\n"
      "v_mfma_f32_16x16x32_bf16 v[212:215], a[188:191], v[228:231], v[212:215]\n"
      "s_waitcnt lgkmcnt(2)\n"
      "v_mfma_f32_16x16x32_bf16 v[0:3], a[192:195], v[216:219], v[0:3]\n"
      "global_load_dwordx4 a[168:171], v239, s[58:59] offset:2048 sc1\n"
      "v_mfma_f32_16x16x32_bf16 v[4:7], a[196:199], v[216:219], v[4:7]\n"
      "v_mfma_f32_16x16x32_bf16 v[8:11], a[200:203], v[216:219], v[8:11]\n"
      "ds_read_b128 v[228:231], v232 offset:12288\n"
      "v_mfma_f32_16x16x32_bf16 v[12:15], a[204:207], v[216:219], v[12:15]\n"
      "v_mfma_f32_16x16x32_bf16 v[16:19], a[208:211], v[216:219], v[16:19]\n"
      "global_load_dwordx4 a[172:175], v239, s[58:59] offset:3072 sc1\n"
      "v_mfma_f32_16x16x32_bf16 v[20:23], a[212:215], v[216:219], v[20:23]\n"
      "s_waitcnt lgkmcnt(2)\n"
      "v_mfma_f32_16x16x32_bf16 v[24:27], a[192:195], v[220:223], v[24:27]\n"
      "v_mfma_f32_16x16x32_bf16 v[28:31], a[196:199], v[220:223], v[28:31]\n"
      "v_mfma_f32_16x16x32_bf16 v[32:35], a[200:203], v[220:223], v[32:35]\n"
      "ds_read_b128 v[216:219], v232 offset:16384\n"
      "s_add_u32 s58, s24, 0x9000\n"
      "s_addc_u32 s59, s25, 0\n"
      "global_load_dwordx4 a[176:179], v239, s[58:59] offset:0 sc1\n"
      "v_mfma_f32_16x16x32_bf16 v[36:39], a[204:207], v[220:223], v[36:39]\n"
      "v_mfma_f32_16x16x32_bf16 v[40:43], a[208:211], v[220:223], v[40:43]\n"
      "v_mfma_f32_16x16x32_bf16 v[44:47], a[212:215], v[220:223], v[44:47]\n"
      "s_waitcnt lgkmcnt(2)\n"
      "v_mfma_f32_16x16x32_bf16 v[48:51], a[192:195], v[224:227], v[48:51]\n"
      "global_load_dwordx4 a[180:183], v239, s[58:59] offset:1024 sc1\n"
      "v_mfma_f32_16x16x32_bf16 v[52:55], a[196:199], v[224:227], v[52:55]\n"
      "v_mfma_f32_16x16x32_bf16 v[56:59], a[200:203], v[224:227], v[56:59]\n"
      "ds_read_b128 v[220:223], v232 offset:20480\n"
      "v_mfma_f32_16x16x32_bf16 v[60:63], a[204:207], v[224:227], v[60:63]\n"
      "v_mfma_f32_16x16x32_bf16 v[64:67], a[208:211], v[224:227], v[64:67]\n"
      "global_load_dwordx4 a[184:187], v239, s[58:59] offset:2048 sc1\n"
      "v_mfma_f32_16x16x32_bf16 v[68:71], a[212:215], v[224:227], v[68:71]\n"
      "s_waitcnt lgkmcnt(2)\n"
      "v_mfma_f32_16x16x32_bf16 v[72:75], a[192:195], v[228:231], v[72:75]\n"
      "v_mfma_f32_16x16x32_bf16 v[76:79], a[196:199], v[228:231], v[76:79]\n"
      "v_mfma_f32_16x16x32_bf16 v[80:83], a[200:203], v[228:231], v[80:83]\n"
      "ds_read_b128 v[224:227], v232 offset:24576\n"
      "global_load_dwordx4 a[188:191], v239, s[58:59] offset:3072 sc1\n"
      "v_mfma_f32_16x16x32_bf16 v[84:87], a[204:207], v[228:231], v[84:87]\n"
      "v_mfma_f32_16x16x32_bf16 v[88:91], a[208:211], v[228:231], v[88:91]\n"
      "v_mfma_f32_16x16x32_bf16 v[92:95], a[212:215], v[228:231], v[92:95]\n"
      "s_waitcnt lgkmcnt(2)\n"
      "v_mfma_f32_16x16x32_bf16 v[96:99], a[192:195], v[216:219], v[96:99]\n"
      "s_mul_i32 s78, s98, s19\n"
      "v_mfma_f32_16x16x32_bf16 v[100:103], a[196:199], v[216:219], v[100:103]\n"
      "v_add_u32 v236, s78, v236\n"
      "v_mfma_f32_16x16x32_bf16 v[104:107], a[200:203], v[216:219], v[104:107]\n"
      "ds_read_b128 v[228:231], v232 offset:28672\n"
      "s_mul_i32 s78, s78, s99\n"
      "v_mfma_f32_16x16x32_bf16 v[108:111], a[204:207], v[216:219], v[108:111]\n"
      "v_add_u32 v237, s78, v237\n"
      "v_mfma_f32_16x16x32_bf16 v[112:115], a[208:211], v[216:219], v[112:115]\n"
      "s_add_u32 s52, s20, s32\n"
      "s_addc_u32 s53, s21, 0\n"
      "s_add_u32 m0, s17, 0x0\n"
      "v_mfma_f32_16x16x32_bf16 v[116:119], a[212:215], v[216:219], v[116:119]\n"
      "global_load_lds_dwordx4 v236, s[52:53] offset:0 nt\n"
      "s_waitcnt lgkmcnt(2)\n"
      "v_mfma_f32_16x16x32_bf16 v[120:123], a[192:195], v[220:223], v[120:123]\n"
      "v_mfma_f32_16x16x32_bf16 v[124:127], a[196:199], v[220:223], v[124:127]\n"
      "s_add_u32 m0, s17, 0x1f00\n"
      "v_mfma_f32_16x16x32_bf16 v[128:131], a[200:203], v[220:223], v[128:131]\n"
      "ds_read_b128 v[216:219], v232 offset:32768\n"
      "v_add_u32 v232, v249, v232\n"
      "global_load_lds_dwordx4 v236, s[52:53] offset:256 nt\n"
      "v_mfma_f32_16x16x32_bf16 v[132:135], a[204:207], v[220:223], v[132:135]\n"
      "v_mfma_f32_16x16x32_bf16 v[136:139], a[208:211], v[220:223], v[136:139]\n"
      "s_add_u32 m0, s17, 0x3e00\n"
      "v_mfma_f32_16x16x32_bf16 v[140:143], a[212:215], v[220:223], v[140:143]\n"
      "global_load_lds_dwordx4 v236, s[52:53] offset:512 nt\n"
      "s_waitcnt lgkmcnt(2)\n"
      "v_mfma_f32_16x16x32_bf16 v[144:147], a[192:195], v[224:227], v[144:147]\n"
      "v_mfma_f32_16x16x32_bf16 v[148:151], a[196:199], v[224:227], v[148:151]\n"
      "s_add_u32 m0, s17, 0x1000\n"
      "v_mfma_f32_16x16x32_bf16 v[152:155], a[200:203], v[224:227], v[152:155]\n"
      "ds_read_b128 v[220:223], v233 offset:0\n"
      "global_load_lds_dwordx4 v237, s[52:53] offset:0 nt\n"
      "v_mfma_f32_16x16x32_bf16 v[156:159], a[204:207], v[224:227], v[156:159]\n"
      "v_mfma_f32_16x16x32_bf16 v[160:163], a[208:211], v[224:227], v[160:163]\n"
      "s_add_u32 m0, s17, 0x2f00\n"
      "v_mfma_f32_16x16x32_bf16 v[164:167], a[212:215], v[224:227], v[164:167]\n"
      "global_load_lds_dwordx4 v237, s[52:53] offset:256 nt\n"
      "s_waitcnt lgkmcnt(2)\n"
      "v_mfma_f32_16x16x32_bf16 v[168:171], a[192:195], v[228:231], v[168:171]\n"
      "v_mfma_f32_16x16x32_bf16 v[172:175], a[196:199], v[228:231], v[172:175]\n"
      "s_add_u32 m0, s17, 0x4e00\n"
      "v_mfma_f32_16x16x32_bf16 v[176:179], a[200:203], v[228:231], v[176:179]\n"
      "ds_read_b128 v[224:227], v233 offset:4096\n"
      "global_load_lds_dwordx4 v237, s[52:53] offset:512 nt\n"
      "v_mfma_f32_16x16x32_bf16 v[180:183], a[204:207], v[228:231], v[180:183]\n"
      "v_mfma_f32_16x16x32_bf16 v[184:187], a[208:211], v[228:231], v[184:187]\n"
      "v_mfma_f32_16x16x32_bf16 v[188:191], a[212:215], v[228:231], v[188:191]\n"
      "s_waitcnt lgkmcnt(2)\n"
      "v_mfma_f32_16x16x32_bf16 v[192:195], a[192:195], v[216:219], v[192:195]\n"
      "v_mfma_f32_16x16x32_bf16 v[196:199], a[196:199], v[216:219], v[196:199]\n"
      "v_mfma_f32_16x16x32_bf16 v[200:203], a[200:203], v[216:219], v[200:203]\n"
      "ds_read_b128 v[228:231], v233 offset:8192\n"
      "v_mfma_f32_16x16x32_bf16 v[204:207], a[204:207], v[216:219], v[204:207]\n"
      "v_mfma_f32_16x16x32_bf16 v[208:211], a[208:211], v[216:219], v[208:211]\n"
      "v_mfma_f32_16x16x32_bf16 v[212:215], a[212:215], v[216:219], v[212:215]\n"
      "s_waitcnt lgkmcnt(2)\n"
      "v_mfma_f32_16x16x32_bf16 v[0:3], a[216:219], v[220:223], v[0:3]\n"
      "s_add_u32 s58, s24, 0xa000\n"
      "s_addc_u32 s59, s25, 0\n"
      "global_load_dwordx4 a[192:195], v239, s[58:59] offset:0 sc1\n"
      "v_mfma_f32_16x16x32_bf16 v[4:7], a[220:223], v[220:223], v[4:7]\n"
      "v_mfma_f32_16x16x32_bf16 v[8:11], a[224:227], v[220:223], v[8:11]\n"
      "ds_read_b128 v[216:219], v233 offset:12288\n"
      "v_mfma_f32_16x16x32_bf16 v[12:15], a[228:231], v[220:223], v[12:15]\n"
      "v_mfma_f32_16x16x32_bf16 v[16:19], a[232:235], v[220:223], v[16:19]\n"
      "global_load_dwordx4 a[196:199], v239, s[58:59] offset:1024 sc1\n"
      "v_mfma_f32_16x16x32_bf16 v[20:23], a[236:239], v[220:223], v[20:23]\n"
      "s_waitcnt lgkmcnt(2)\n"
      "v_mfma_f32_16x16x32_bf16 v[24:27], a[216:219], v[224:227], v[24:27]\n"
      "v_mfma_f32_16x16x32_bf16 v[28:31], a[220:223], v[224:227], v[28:31]\n"
      "v_mfma_f32_16x16x32_bf16 v[32:35], a[224:227], v[224:227], v[32:35]\n"
      "ds_read_b128 v[220:223], v233 offset:16384\n"
      "global_load_dwordx4 a[200:203], v239, s[58:59] offset:2048 sc1\n"
      "v_mfma_f32_16x16x32_bf16 v[36:39], a[228:231], v[224:227], v[36:39]\n"
      "v_mfma_f32_16x16x32_bf16 v[40:43], a[232:235], v[224:227], v[40:43]\n"
      "v_mfma_f32_16x16x32_bf16 v[44:47], a[236:239], v[224:227], v[44:47]\n"
      "s_waitcnt lgkmcnt(2)\n"
      "v_mfma_f32_16x16x32_bf16 v[48:51], a[216:219], v[228:231], v[48:51]\n"
      "global_load_dwordx4 a[204:207], v239, s[58:59] offset:3072 sc1\n"
      "v_mfma_f32_16x16x32_bf16 v[52:55], a[220:223], v[228:231], v[52:55]\n"
      "v_mfma_f32_16x16x32_bf16 v[56:59], a[224:227], v[228:231], v[56:59]\n"
      "ds_read_b128 v[224:227], v233 offset:20480\n"
      "v_mfma_f32_16x16x32_bf16 v[60:63], a[228:231], v[228:231], v[60:63]\n"
      "v_mfma_f32_16x16x32_bf16 v[64:67], a[232:235], v[228:231], v[64:67]\n"
      "s_add_u32 s58, s24, 0xb000\n"
      "s_addc_u32 s59, s25, 0\n"
      "global_load_dwordx4 a[208:211], v239, s[58:59] offset:0 sc1\n"
      "v_mfma_f32_16x16x32_bf16 v[68:71], a[236:239], v[228:231], v[68:71]\n"
      "s_waitcnt lgkmcnt(2)\n"
      "v_mfma_f32_16x16x32_bf16 v[72:75], a[216:219], v[216:219], v[72:75]\n"
      "v_mfma_f32_16x16x32_bf16 v[76:79], a[220:223], v[216:219], v[76:79]\n"
      "v_mfma_f32_16x16x32_bf16 v[80:83], a[224:227], v[216:219], v[80:83]\n"
      "ds_read_b128 v[228:231], v233 offset:24576\n"
      "global_load_dwordx4 a[212:215], v239, s[58:59] offset:1024 sc1\n"
      "v_mfma_f32_16x16x32_bf16 v[84:87], a[228:231], v[216:219], v[84:87]\n"
      "v_mfma_f32_16x16x32_bf16 v[88:91], a[232:235], v[216:219], v[88:91]\n"
      "v_mfma_f32_16x16x32_bf16 v[92:95], a[236:239], v[216:219], v[92:95]\n"
      "s_waitcnt lgkmcnt(2)\n"
      "v_mfma_f32_16x16x32_bf16 v[96:99], a[216:219], v[220:223], v[96:99]\n"
      "v_mfma_f32_16x16x32_bf16 v[100:103], a[220:223], v[220:223], v[100:103]\n"
      "v_mfma_f32_16x16x32_bf16 v[104:107], a[224:227], v[220:223], v[104:107]\n"
      "ds_read_b128 v[216:219], v233 offset:28672\n"
      "v_mfma_f32_16x16x32_bf16 v[108:111], a[228:231], v[220:223], v[108:111]\n"
      "v_mfma_f32_16x16x32_bf16 v[112:115], a[232:235], v[220:223], v[112:115]\n"
      "v_mfma_f32_16x16x32_bf16 v[116:119], a[236:239], v[220:223], v[116:119]\n"
      "s_waitcnt lgkmcnt(2)\n"
      "v_mfma_f32_16x16x32_bf16 v[120:123], a[216:219], v[224:227], v[120:123]\n"
      "v_mfma_f32_16x16x32_bf16 v[124:127], a[220:223], v[224:227], v[124:127]\n"
      "v_mfma_f32_16x16x32_bf16 v[128:131], a[224:227], v[224:227], v[128:131]\n"
      "ds_read_b128 v[220:223], v233 offset:32768\n"
      "v_add_u32 v233, v249, v233\n"
      "v_mfma_f32_16x16x32_bf16 v[132:135], a[228:231], v[224:227], v[132:135]\n"
      "v_mfma_f32_16x16x32_bf16 v[136:139], a[232:235], v[224:227], v[136:139]\n"
      "v_mfma_f32_16x16x32_bf16 v[140:143], a[236:239], v[224:227], v[140:143]\n"
      "s_waitcnt lgkmcnt(2)\n"
      "v_mfma_f32_16x16x32_bf16 v[144:147], a[216:219], v[228:231], v[144:147]\n"
      "v_mfma_f32_16x16x32_bf16 v[148:151], a[220:223], v[228:231], v[148:151]\n"
      "v_mfma_f32_16x16x32_bf16 v[152:155], a[224:227], v[228:231], v[152:155]\n"
      "ds_read_b128 v[224:227], v234 offset:0\n"
      "v_mfma_f32_16x16x32_bf16 v[156:159], a[228:231], v[228:231], v[156:159]\n"
      "v_mfma_f32_16x16x32_bf16 v[160:163], a[232:235], v[228:231], v[160:163]\n"
      "v_mfma_f32_16x16x32_bf16 v[164:167], a[236:239], v[228:231], v[164:167]\n"
      "s_waitcnt lgkmcnt(2)\n"
      "v_mfma_f32_16x16x32_bf16 v[168:171], a[216:219], v[216:219], v[168:171]\n"
      "v_mfma_f32_16x16x32_bf16 v[172:175], a[220:223], v[216:219], v[172:175]\n"
      "v_mfma_f32_16x16x32_bf16 v[176:179], a[224:227], v[216:219], v[176:179]\n"
      "ds_read_b128 v[228:231], v234 offset:4096\n"
      "v_mfma_f32_16x16x32_bf16 v[180:183], a[228:231], v[216:219], v[180:183]\n"
      "v_mfma_f32_16x16x32_bf16 v[184:187], a[232:235], v[216:219], v[184:187]\n"
      "s_add_u32 s52, s42, s32\n"
      "s_addc_u32 s53, s43, 0\n"
      "s_add_u32 m0, s17, 0x6000\n"
      "v_mfma_f32_16x16x32_bf16 v[188:191], a[236:239], v[216:219], v[188:191]\n"
      "global_load_lds_dwordx4 v236, s[52:53] offset:0 nt\n"
      "s_waitcnt lgkmcnt(2)\n"
      "v_mfma_f32_16x16x32_bf16 v[192:195], a[216:219], v[220:223], v[192:195]\n"
      "v_mfma_f32_16x16x32_bf16 v[196:199], a[220:223], v[220:223], v[196:199]\n"
      "s_add_u32 m0, s17, 0x7f00\n"
      "v_mfma_f32_16x16x32_bf16 v[200:203], a[224:227], v[220:223], v[200:203]\n"
      "ds_read_b128 v[216:219], v234 offset:8192\n"
      "global_load_lds_dwordx4 v236, s[52:53] offset:256 nt\n"
      "v_mfma_f32_16x16x32_bf16 v[204:207], a[228:231], v[220:223], v[204:207]\n"
      "v_mfma_f32_16x16x32_bf16 v[208:211], a[232:235], v[220:223], v[208:211]\n"
      "s_add_u32 m0, s17, 0x9e00\n"
      "v_mfma_f32_16x16x32_bf16 v[212:215], a[236:239], v[220:223], v[212:215]\n"
      "global_load_lds_dwordx4 v236, s[52:53] offset:512 nt\n"
      "s_waitcnt lgkmcnt(2)\n"
      "v_mfma_f32_16x16x32_bf16 v[0:3], a[0:3], v[224:227], v[0:3]\n"
      "ds_read_b128 v[244:247], v250\n"
      "v_mfma_f32_16x16x32_bf16 v[4:7], a[4:7], v[224:227], v[4:7]\n"
      "s_add_u32 m0, s17, 0x7000\n"
      "ds_read_b64 v[252:253], v250 offset:4096\n"
      "v_mfma_f32_16x16x32_bf16 v[8:11], a[8:11], v[224:227], v[8:11]\n"
      "ds_read_b128 v[220:223], v234 offset:12288\n"
      "global_load_lds_dwordx4 v237, s[52:53] offset:0 nt\n"
      "v_mfma_f32_16x16x32_bf16 v[12:15], a[12:15], v[224:227], v[12:15]\n"
      "v_mfma_f32_16x16x32_bf16 v[16:19], a[16:19], v[224:227], v[16:19]\n"
      "s_add_u32 m0, s17, 0x8f00\n"
      "v_mfma_f32_16x16x32_bf16 v[20:23], a[20:23], v[224:227], v[20:23]\n"
      "global_load_lds_dwordx4 v237, s[52:53] offset:256 nt\n"
      "s_waitcnt lgkmcnt(4)\n"
      "v_mfma_f32_16x16x32_bf16 v[24:27], a[0:3], v[228:231], v[24:27]\n"
      "v_mfma_f32_16x16x32_bf16 v[28:31], a[4:7], v[228:231], v[28:31]\n"
      "s_add_u32 m0, s17, 0xae00\n"
      "v_mfma_f32_16x16x32_bf16 v[32:35], a[8:11], v[228:231], v[32:35]\n"
      "ds_read_b128 v[224:227], v234 offset:16384\n"
      "global_load_lds_dwordx4 v237, s[52:53] offset:512 nt\n"
      "v_mfma_f32_16x16x32_bf16 v[36:39], a[12:15], v[228:231], v[36:39]\n"
      "v_mfma_f32_16x16x32_bf16 v[40:43], a[16:19], v[228:231], v[40:43]\n"
      "v_mfma_f32_16x16x32_bf16 v[44:47], a[20:23], v[228:231], v[44:47]\n"
      "global_load_dwordx4 a[216:219], v239, s[58:59] offset:2048 sc1\n"
      "s_waitcnt lgkmcnt(4)\n"
      "v_mfma_f32_16x16x32_bf16 v[48:51], a[0:3], v[216:219], v[48:51]\n"
      "v_mfma_f32_16x16x32_bf16 v[52:55], a[4:7], v[216:219], v[52:55]\n"
      "v_mfma_f32_16x16x32_bf16 v[56:59], a[8:11], v[216:219], v[56:59]\n"
      "ds_read_b128 v[228:231], v234 offset:20480\n"
      "v_mfma_f32_16x16x32_bf16 v[60:63], a[12:15], v[216:219], v[60:63]\n"
      "global_load_dwordx4 a[220:223], v239, s[58:59] offset:3072 sc1\n"
      "v_mfma_f32_16x16x32_bf16 v[64:67], a[16:19], v[216:219], v[64:67]\n"
      "v_mfma_f32_16x16x32_bf16 v[68:71], a[20:23], v[216:219], v[68:71]\n"
      "s_waitcnt lgkmcnt(2)\n"
      "v_mfma_f32_16x16x32_bf16 v[72:75], a[0:3], v[220:223], v[72:75]\n"
      "v_mfma_f32_16x16x32_bf16 v[76:79], a[4:7], v[220:223], v[76:79]\n"
      "s_add_u32 s58, s24, 0xc000\n"
      "s_addc_u32 s59, s25, 0\n"
      "global_load_dwordx4 a[224:227], v239, s[58:59] offset:0 sc1\n"
      "v_mfma_f32_16x16x32_bf16 v[80:83], a[8:11], v[220:223], v[80:83]\n"
      "ds_read_b128 v[216:219], v234 offset:24576\n"
      "v_mfma_f32_16x16x32_bf16 v[84:87], a[12:15], v[220:223], v[84:87]\n"
      "v_mfma_f32_16x16x32_bf16 v[88:91], a[16:19], v[220:223], v[88:91]\n"
      "v_mfma_f32_16x16x32_bf16 v[92:95], a[20:23], v[220:223], v[92:95]\n"
      "global_load_dwordx4 a[228:231], v239, s[58:59] offset:1024 sc1\n"
      "s_waitcnt lgkmcnt(2)\n"
      "v_mfma_f32_16x16x32_bf16 v[96:99], a[0:3], v[224:227], v[96:99]\n"
      "v_mfma_f32_16x16x32_bf16 v[100:103], a[4:7], v[224:227], v[100:103]\n"
      "v_mfma_f32_16x16x32_bf16 v[104:107], a[8:11], v[224:227], v[104:107]\n"
      "ds_read_b128 v[220:223], v234 offset:28672\n"
      "v_mfma_f32_16x16x32_bf16 v[108:111], a[12:15], v[224:227], v[108:111]\n"
      "global_load_dwordx4 a[232:235], v239, s[58:59] offset:2048 sc1\n"
      "v_mfma_f32_16x16x32_bf16 v[112:115], a[16:19], v[224:227], v[112:115]\n"
      "v_mfma_f32_16x16x32_bf16 v[116:119], a[20:23], v[224:227], v[116:119]\n"
      "s_waitcnt lgkmcnt(2)\n"
      "v_mfma_f32_16x16x32_bf16 v[120:123], a[0:3], v[228:231], v[120:123]\n"
      "v_mfma_f32_16x16x32_bf16 v[124:127], a[4:7], v[228:231], v[124:127]\n"
      "global_load_dwordx4 a[236:239], v239, s[58:59] offset:3072 sc1\n"
      "v_mfma_f32_16x16x32_bf16 v[128:131], a[8:11], v[228:231], v[128:131]\n"
      "ds_read_b128 v[224:227], v234 offset:32768\n"
      "v_add_u32 v234, v249, v234\n"
      "v_mfma_f32_16x16x32_bf16 v[132:135], a[12:15], v[228:231], v[132:135]\n"
      "v_mfma_f32_16x16x32_bf16 v[136:139], a[16:19], v[228:231], v[136:139]\n"
      "v_mfma_f32_16x16x32_bf16 v[140:143], a[20:23], v[228:231], v[140:143]\n"
      "s_waitcnt lgkmcnt(2)\n"
      "v_mfma_f32_16x16x32_bf16 v[144:147], a[0:3], v[216:219], v[144:147]\n"
      "v_mfma_f32_16x16x32_bf16 v[148:151], a[4:7], v[216:219], v[148:151]\n"
      "v_mfma_f32_16x16x32_bf16 v[152:155], a[8:11], v[216:219], v[152:155]\n"
      "ds_read_b128 v[228:231], v235 offset:4096\n"
      "v_mfma_f32_16x16x32_bf16 v[156:159], a[12:15], v[216:219], v[156:159]\n"
      "v_mfma_f32_16x16x32_bf16 v[160:163], a[16:19], v[216:219], v[160:163]\n"
      "v_mfma_f32_16x16x32_bf16 v[164:167], a[20:23], v[216:219], v[164:167]\n"
      "s_waitcnt lgkmcnt(2)\n"
      "v_mfma_f32_16x16x32_bf16 v[168:171], a[0:3], v[220:223], v[168:171]\n"
      "v_mfma_f32_16x16x32_bf16 v[172:175], a[4:7], v[220:223], v[172:175]\n"
      "v_mfma_f32_16x16x32_bf16 v[176:179], a[8:11], v[220:223], v[176:179]\n"
      "ds_read_b128 v[216:219], v235 offset:8192\n"
      "v_mfma_f32_16x16x32_bf16 v[180:183], a[12:15], v[220:223], v[180:183]\n"
      "v_mfma_f32_16x16x32_bf16 v[184:187], a[16:19], v[220:223], v[184:187]\n"
      "v_mfma_f32_16x16x32_bf16 v[188:191], a[20:23], v[220:223], v[188:191]\n"
      "s_waitcnt lgkmcnt(2)\n"
      "v_mfma_f32_16x16x32_bf16 v[192:195], a[0:3], v[224:227], v[192:195]\n"
      "v_mfma_f32_16x16x32_bf16 v[196:199], a[4:7], v[224:227], v[196:199]\n"
      "v_mfma_f32_16x16x32_bf16 v[200:203], a[8:11], v[224:227], v[200:203]\n"
      "ds_read_b128 v[220:223], v235 offset:12288\n"
      "v_mfma_f32_16x16x32_bf16 v[204:207], a[12:15], v[224:227], v[204:207]\n"
      "v_mfma_f32_16x16x32_bf16 v[208:211], a[16:19], v[224:227], v[208:211]\n"
      "v_mfma_f32_16x16x32_bf16 v[212:215], a[20:23], v[224:227], v[212:215]\n"
      "s_waitcnt lgkmcnt(2)\n"
      "v_mfma_f32_16x16x32_bf16 v[24:27], a[24:27], v[228:231], v[24:27]\n"
      "s_add_u32 s58, s24, 0xd000\n"
      "s_addc_u32 s59, s25, 0\n"
      "global_load_dwordx4 a[0:3], v239, s[58:59] offset:0 sc1\n"
      "v_mfma_f32_16x16x32_bf16 v[28:31], a[28:31], v[228:231], v[28:31]\n"
      "v_mfma_f32_16x16x32_bf16 v[32:35], a[32:35], v[228:231], v[32:35]\n"
      "ds_read_b128 v[224:227], v235 offset:16384\n"
      "v_mfma_f32_16x16x32_bf16 v[36:39], a[36:39], v[228:231], v[36:39]\n"
      "v_mfma_f32_16x16x32_bf16 v[40:43], a[40:43], v[228:231], v[40:43]\n"
      "global_load_dwordx4 a[4:7], v239, s[58:59] offset:1024 sc1\n"
      "v_mfma_f32_16x16x32_bf16 v[44:47], a[44:47], v[228:231], v[44:47]\n"
      "s_waitcnt lgkmcnt(2)\n"
      "v_mfma_f32_16x16x32_bf16 v[48:51], a[24:27], v[216:219], v[48:51]\n"
      "v_cvt_pk_bf16_f32 v24, v24, v25\n"
      "v_cvt_pk_bf16_f32 v25, v26, v27\n"
      "v_mfma_f32_16x16x32_bf16 v[52:55], a[28:31], v[216:219], v[52:55]\n"
      "v_cvt_pk_bf16_f32 v26, v28, v29\n"
      "v_cvt_pk_bf16_f32 v27, v30, v31\n"
      "v_mfma_f32_16x16x32_bf16 v[56:59], a[32:35], v[216:219], v[56:59]\n"
      "ds_read_b128 v[228:231], v235 offset:20480\n"
      "v_cvt_pk_bf16_f32 v28, v32, v33\n"
      "v_cvt_pk_bf16_f32 v29, v34, v35\n"
      "global_load_dwordx4 a[8:11], v239, s[58:59] offset:2048 sc1\n"
      "v_mfma_f32_16x16x32_bf16 v[60:63], a[36:39], v[216:219], v[60:63]\n"
      "v_cvt_pk_bf16_f32 v30, v36, v37\n"
      "v_cvt_pk_bf16_f32 v31, v38, v39\n"
      "v_mfma_f32_16x16x32_bf16 v[64:67], a[40:43], v[216:219], v[64:67]\n"
      "v_cvt_pk_bf16_f32 v32, v40, v41\n"
      "v_cvt_pk_bf16_f32 v33, v42, v43\n"
      "v_mfma_f32_16x16x32_bf16 v[68:71], a[44:47], v[216:219], v[68:71]\n"
      "v_cvt_pk_bf16_f32 v34, v44, v45\n"
      "v_cvt_pk_bf16_f32 v35, v46, v47\n"
      "s_waitcnt lgkmcnt(2)\n"
      "v_mfma_f32_16x16x32_bf16 v[72:75], a[24:27], v[220:223], v[72:75]\n"
      "v_cvt_pk_bf16_f32 v48, v48, v49\n"
      "v_cvt_pk_bf16_f32 v49, v50, v51\n"
      "global_load_dwordx4 a[12:15], v239, s[58:59] offset:3072 sc1\n"
      "v_mfma_f32_16x16x32_bf16 v[76:79], a[28:31], v[220:223], v[76:79]\n"
      "v_cvt_pk_bf16_f32 v50, v52, v53\n"
      "v_cvt_pk_bf16_f32 v51, v54, v55\n"
      "v_mfma_f32_16x16x32_bf16 v[80:83], a[32:35], v[220:223], v[80:83]\n"
      "ds_read_b128 v[216:219], v235 offset:24576\n"
      "v_cvt_pk_bf16_f32 v52, v56, v57\n"
      "v_cvt_pk_bf16_f32 v53, v58, v59\n"
      "v_mfma_f32_16x16x32_bf16 v[84:87], a[36:39], v[220:223], v[84:87]\n"
      "v_cvt_pk_bf16_f32 v54, v60, v61\n"
      "v_cvt_pk_bf16_f32 v55, v62, v63\n"
      "v_mfma_f32_16x16x32_bf16 v[88:91], a[40:43], v[220:223], v[88:91]\n"
      "v_cvt_pk_bf16_f32 v56, v64, v65\n"
      "v_cvt_pk_bf16_f32 v57, v66, v67\n"
      "s_add_u32 s58, s24, 0xe000\n"
      "s_addc_u32 s59, s25, 0\n"
      "global_load_dwordx4 a[16:19], v239, s[58:59] offset:0 sc1\n"
      "v_mfma_f32_16x16x32_bf16 v[92:95], a[44:47], v[220:223], v[92:95]\n"
      "v_cvt_pk_bf16_f32 v58, v68, v69\n"
      "v_cvt_pk_bf16_f32 v59, v70, v71\n"
      "s_waitcnt lgkmcnt(2)\n"
      "v_mfma_f32_16x16x32_bf16 v[96:99], a[24:27], v[224:227], v[96:99]\n"
      "v_cvt_pk_bf16_f32 v72, v72, v73\n"
      "v_cvt_pk_bf16_f32 v73, v74, v75\n"
      "v_mfma_f32_16x16x32_bf16 v[100:103], a[28:31], v[224:227], v[100:103]\n"
      "v_cvt_pk_bf16_f32 v74, v76, v77\n"
      "v_cvt_pk_bf16_f32 v75, v78, v79\n"
      "v_mfma_f32_16x16x32_bf16 v[104:107], a[32:35], v[224:227], v[104:107]\n"
      "ds_read_b128 v[220:223], v235 offset:28672\n"
      "v_cvt_pk_bf16_f32 v76, v80, v81\n"
      "v_cvt_pk_bf16_f32 v77, v82, v83\n"
      "global_load_dwordx4 a[20:23], v239, s[58:59] offset:1024 sc1\n"
      "v_mfma_f32_16x16x32_bf16 v[108:111], a[36:39], v[224:227], v[108:111]\n"
      "v_cvt_pk_bf16_f32 v78, v84, v85\n"
      "v_cvt_pk_bf16_f32 v79, v86, v87\n"
      "v_mfma_f32_16x16x32_bf16 v[112:115], a[40:43], v[224:227], v[112:115]\n"
      "v_cvt_pk_bf16_f32 v80, v88, v89\n"
      "v_cvt_pk_bf16_f32 v81, v90, v91\n"
      "v_mfma_f32_16x16x32_bf16 v[116:119], a[44:47], v[224:227], v[116:119]\n"
      "v_cvt_pk_bf16_f32 v82, v92, v93\n"
      "v_cvt_pk_bf16_f32 v83, v94, v95\n"
      "s_waitcnt lgkmcnt(2)\n"
      "v_mfma_f32_16x16x32_bf16 v[120:123], a[24:27], v[228:231], v[120:123]\n"
      "v_cvt_pk_bf16_f32 v96, v96, v97\n"
      "v_cvt_pk_bf16_f32 v97, v98, v99\n"
      "v_mfma_f32_16x16x32_bf16 v[124:127], a[28:31], v[228:231], v[124:127]\n"
      "v_cvt_pk_bf16_f32 v98, v100, v101\n"
      "v_cvt_pk_bf16_f32 v99, v102, v103\n"
      "v_mfma_f32_16x16x32_bf16 v[128:131], a[32:35], v[228:231], v[128:131]\n"
      "ds_read_b128 v[224:227], v235 offset:32768\n"
      "v_cvt_pk_bf16_f32 v100, v104, v105\n"
      "v_cvt_pk_bf16_f32 v101, v106, v107\n"
      "v_mfma_f32_16x16x32_bf16 v[132:135], a[36:39], v[228:231], v[132:135]\n"
      "v_cvt_pk_bf16_f32 v102, v108, v109\n"
      "v_cvt_pk_bf16_f32 v103, v110, v111\n"
      "v_mfma_f32_16x16x32_bf16 v[136:139], a[40:43], v[228:231], v[136:139]\n"
      "v_cvt_pk_bf16_f32 v104, v112, v113\n"
      "v_cvt_pk_bf16_f32 v105, v114, v115\n"
      "v_mfma_f32_16x16x32_bf16 v[140:143], a[44:47], v[228:231], v[140:143]\n"
      "v_cvt_pk_bf16_f32 v106, v116, v117\n"
      "v_cvt_pk_bf16_f32 v107, v118, v119\n"
      "s_waitcnt lgkmcnt(2)\n"
      "v_mfma_f32_16x16x32_bf16 v[144:147], a[24:27], v[216:219], v[144:147]\n"
      "v_cvt_pk_bf16_f32 v120, v120, v121\n"
      "v_cvt_pk_bf16_f32 v121, v122, v123\n"
      "v_mfma_f32_16x16x32_bf16 v[148:151], a[28:31], v[216:219], v[148:151]\n"
      "v_cvt_pk_bf16_f32 v122, v124, v125\n"
      "v_cvt_pk_bf16_f32 v123, v126, v127\n"
      "v_mfma_f32_16x16x32_bf16 v[152:155], a[32:35], v[216:219], v[152:155]\n"
      "ds_read_b128 v[228:231], v235 offset:0\n"
      "v_add_u32 v235, v249, v235\n"
      "s_waitcnt vmcnt(24)\n"
      "s_barrier\n"
      "v_cvt_pk_bf16_f32 v124, v128, v129\n"
      "v_cvt_pk_bf16_f32 v125, v130, v131\n"
      "v_mfma_f32_16x16x32_bf16 v[156:159], a[36:39], v[216:219], v[156:159]\n"
      "ds_read_b128 v[36:39], v232 offset:0\n"
      "v_cvt_pk_bf16_f32 v126, v132, v133\n"
      "v_cvt_pk_bf16_f32 v127, v134, v135\n"
      "v_mfma_f32_16x16x32_bf16 v[160:163], a[40:43], v[216:219], v[160:163]\n"
      "v_cvt_pk_bf16_f32 v128, v136, v137\n"
      "v_cvt_pk_bf16_f32 v129, v138, v139\n"
      "v_mfma_f32_16x16x32_bf16 v[164:167], a[44:47], v[216:219], v[164:167]\n"
      "ds_read_b128 v[40:43], v233 offset:0\n"
      "v_cvt_pk_bf16_f32 v130, v140, v141\n"
      "v_cvt_pk_bf16_f32 v131, v142, v143\n"
      "s_waitcnt lgkmcnt(4)\n"
      "v_mfma_f32_16x16x32_bf16 v[168:171], a[24:27], v[220:223], v[168:171]\n"
      "v_cvt_pk_bf16_f32 v144, v144, v145\n"
      "v_cvt_pk_bf16_f32 v145, v146, v147\n"
      "global_load_dwordx4 v[140:143], v239, s[58:59] offset:2048 sc1\n"
      "v_mfma_f32_16x16x32_bf16 v[172:175], a[28:31], v[220:223], v[172:175]\n"
      "ds_read_b128 v[44:47], v234 offset:0\n"
      "v_cvt_pk_bf16_f32 v146, v148, v149\n"
      "v_cvt_pk_bf16_f32 v147, v150, v151\n"
      "v_mfma_f32_16x16x32_bf16 v[176:179], a[32:35], v[220:223], v[176:179]\n"
      "v_cvt_pk_bf16_f32 v148, v152, v153\n"
      "v_cvt_pk_bf16_f32 v149, v154, v155\n"
      "v_mfma_f32_16x16x32_bf16 v[180:183], a[36:39], v[220:223], v[180:183]\n"
      "ds_read_b128 v[60:63], v235 offset:0\n"
      "v_cvt_pk_bf16_f32 v150, v156, v157\n"
      "v_cvt_pk_bf16_f32 v151, v158, v159\n"
      "v_mfma_f32_16x16x32_bf16 v[184:187], a[40:43], v[220:223], v[184:187]\n"
      "v_cvt_pk_bf16_f32 v152, v160, v161\n"
      "v_cvt_pk_bf16_f32 v153, v162, v163\n"
      "v_mfma_f32_16x16x32_bf16 v[188:191], a[44:47], v[220:223], v[188:191]\n"
      "ds_read_b128 v[64:67], v232 offset:8192\n"
      "v_cvt_pk_bf16_f32 v154, v164, v165\n"
      "v_cvt_pk_bf16_f32 v155, v166, v167\n"
      "s_waitcnt lgkmcnt(6)\n"
      "v_mfma_f32_16x16x32_bf16 v[192:195], a[24:27], v[224:227], v[192:195]\n"
      "v_cvt_pk_bf16_f32 v168, v168, v169\n"
      "v_cvt_pk_bf16_f32 v169, v170, v171\n"
      "global_load_dwordx4 v[156:159], v239, s[58:59] offset:3072 sc1\n"
      "v_mfma_f32_16x16x32_bf16 v[196:199], a[28:31], v[224:227], v[196:199]\n"
      "ds_read_b128 v[68:71], v233 offset:8192\n"
      "v_cvt_pk_bf16_f32 v170, v172, v173\n"
      "v_cvt_pk_bf16_f32 v171, v174, v175\n"
      "v_mfma_f32_16x16x32_bf16 v[200:203], a[32:35], v[224:227], v[200:203]\n"
      "v_cvt_pk_bf16_f32 v172, v176, v177\n"
      "v_cvt_pk_bf16_f32 v173, v178, v179\n"
      "v_mfma_f32_16x16x32_bf16 v[204:207], a[36:39], v[224:227], v[204:207]\n"
      "ds_read_b128 v[84:87], v234 offset:8192\n"
      "v_cvt_pk_bf16_f32 v174, v180, v181\n"
      "v_cvt_pk_bf16_f32 v175, v182, v183\n"
      "v_mfma_f32_16x16x32_bf16 v[208:211], a[40:43], v[224:227], v[208:211]\n"
      "v_cvt_pk_bf16_f32 v176, v184, v185\n"
      "v_cvt_pk_bf16_f32 v177, v186, v187\n"
      "s_add_u32 s58, s24, 0xf000\n"
      "s_addc_u32 s59, s25, 0\n"
      "global_load_dwordx4 v[160:163], v239, s[58:59] offset:0 sc1\n"
      "v_mfma_f32_16x16x32_bf16 v[212:215], a[44:47], v[224:227], v[212:215]\n"
      "ds_read_b128 v[88:91], v235 offset:8192\n"
      "v_cvt_pk_bf16_f32 v178, v188, v189\n"
      "v_cvt_pk_bf16_f32 v179, v190, v191\n"
      "s_waitcnt lgkmcnt(8)\n"
      "v_mfma_f32_16x16x32_bf16 v[0:3], a[24:27], v[228:231], v[0:3]\n"
      "v_cvt_pk_bf16_f32 v192, v192, v193\n"
      "v_cvt_pk_bf16_f32 v193, v194, v195\n"
      "v_mfma_f32_16x16x32_bf16 v[4:7], a[28:31], v[228:231], v[4:7]\n"
      "ds_read_b128 v[92:95], v232 offset:16384\n"
      "v_cvt_pk_bf16_f32 v194, v196, v197\n"
      "v_cvt_pk_bf16_f32 v195, v198, v199\n"
      "v_mfma_f32_16x16x32_bf16 v[8:11], a[32:35], v[228:231], v[8:11]\n"
      "v_cvt_pk_bf16_f32 v196, v200, v201\n"
      "v_cvt_pk_bf16_f32 v197, v202, v203\n"
      "global_load_dwordx4 v[164:167], v239, s[58:59] offset:1024 sc1\n"
      "v_mfma_f32_16x16x32_bf16 v[12:15], a[36:39], v[228:231], v[12:15]\n"
      "ds_read_b128 v[108:111], v233 offset:16384\n"
      "v_cvt_pk_bf16_f32 v198, v204, v205\n"
      "v_cvt_pk_bf16_f32 v199, v206, v207\n"
      "v_mfma_f32_16x16x32_bf16 v[16:19], a[40:43], v[228:231], v[16:19]\n"
      "ds_read_b128 v[112:115], v234 offset:16384\n"
      "v_cvt_pk_bf16_f32 v200, v208, v209\n"
      "v_cvt_pk_bf16_f32 v201, v210, v211\n"
      "v_mfma_f32_16x16x32_bf16 v[20:23], a[44:47], v[228:231], v[20:23]\n"
      "ds_read_b128 v[116:119], v235 offset:16384\n"
      "v_cvt_pk_bf16_f32 v202, v212, v213\n"
      "v_cvt_pk_bf16_f32 v203, v214, v215\n"
      "s_waitcnt lgkmcnt(8)\n"
      "v_mfma_f32_16x16x32_bf16 v[132:135], a[240:243], v[36:39], v[0:3]\n"
      "global_load_dwordx4 v[180:183], v239, s[58:59] offset:2048 sc1\n"
      "s_mov_b64 vcc, 0\n"
      "v_mfma_f32_16x16x32_bf16 v[132:135], a[244:247], v[40:43], v[132:135]\n"
      "v_mfma_f32_16x16x32_bf16 v[132:135], a[248:251], v[44:47], v[132:135]\n"
      "v_mfma_f32_16x16x32_bf16 v[132:135], a[252:255], v[60:63], v[132:135]\n"
      "global_load_dwordx4 v[184:187], v239, s[58:59] offset:3072 sc1\n"
      "s_waitcnt lgkmcnt(4)\n"
      "v_mfma_f32_16x16x32_bf16 v[132:135], a[48:51], v[64:67], v[132:135]\n"
      "v_mfma_f32_16x16x32_bf16 v[132:135], a[52:55], v[68:71], v[132:135]\n"
      "v_mfma_f32_16x16x32_bf16 v[132:135], a[56:59], v[84:87], v[132:135]\n"
      "s_add_u32 s58, s24, 0x10000\n"
      "s_addc_u32 s59, s25, 0\n"
      "global_load_dwordx4 v[188:191], v239, s[58:59] offset:0 sc1\n"
      "v_mfma_f32_16x16x32_bf16 v[132:135], a[60:63], v[88:91], v[132:135]\n"
      "s_waitcnt lgkmcnt(0)\n"
      "v_mfma_f32_16x16x32_bf16 v[132:135], a[64:67], v[92:95], v[132:135]\n"
      "v_mfma_f32_16x16x32_bf16 v[132:135], a[68:71], v[108:111], v[132:135]\n"
      "global_load_dwordx4 v[204:207], v239, s[58:59] offset:1024 sc1\n"
      "v_mfma_f32_16x16x32_bf16 v[132:135], a[72:75], v[112:115], v[132:135]\n"
      "v_mfma_f32_16x16x32_bf16 v[132:135], a[76:79], v[116:119], v[132:135]\n"
      "v_mfma_f32_16x16x32_bf16 v[136:139], a[80:83], v[36:39], v[4:7]\n"
      "global_load_dwordx4 a[24:27], v239, s[58:59] offset:2048 sc1\n"
      "v_mfma_f32_16x16x32_bf16 v[136:139], a[84:87], v[40:43], v[136:139]\n"
      "v_mfma_f32_16x16x32_bf16 v[136:139], a[88:91], v[44:47], v[136:139]\n"
      "v_mfma_f32_16x16x32_bf16 v[136:139], a[92:95], v[60:63], v[136:139]\n"
      "global_load_dwordx4 a[28:31], v239, s[58:59] offset:3072 sc1\n"
      "v_mfma_f32_16x16x32_bf16 v[136:139], a[96:99], v[64:67], v[136:139]\n"
      "v_cvt_pk_bf16_f32 v208, v132, v133\n"
      "v_mfma_f32_16x16x32_bf16 v[136:139], a[100:103], v[68:71], v[136:139]\n"
      "v_cvt_pk_bf16_f32 v209, v134, v135\n"
      "v_mfma_f32_16x16x32_bf16 v[136:139], a[104:107], v[84:87], v[136:139]\n"
      "s_add_u32 s58, s24, 0x11000\n"
      "s_addc_u32 s59, s25, 0\n"
      "global_load_dwordx4 a[32:35], v239, s[58:59] offset:0 sc1\n"
      "v_mfma_f32_16x16x32_bf16 v[136:139], a[108:111], v[88:91], v[136:139]\n"
      "v_mfma_f32_16x16x32_bf16 v[136:139], a[112:115], v[92:95], v[136:139]\n"
      "v_mfma_f32_16x16x32_bf16 v[136:139], a[116:119], v[108:111], v[136:139]\n"
      "global_load_dwordx4 a[36:39], v239, s[58:59] offset:1024 sc1\n"
      "v_mfma_f32_16x16x32_bf16 v[136:139], a[120:123], v[112:115], v[136:139]\n"
      "ds_write_b64 v244, v[208:209] offset:12288\n"
      "v_mfma_f32_16x16x32_bf16 v[136:139], a[124:127], v[116:119], v[136:139]\n"
      "v_mfma_f32_16x16x32_bf16 v[132:135], a[128:131], v[36:39], v[8:11]\n"
      "global_load_dwordx4 a[40:43], v239, s[58:59] offset:2048 sc1\n"
      "v_mfma_f32_16x16x32_bf16 v[132:135], a[132:135], v[40:43], v[132:135]\n"
      "v_mfma_f32_16x16x32_bf16 v[132:135], a[136:139], v[44:47], v[132:135]\n"
      "v_mfma_f32_16x16x32_bf16 v[132:135], a[140:143], v[60:63], v[132:135]\n"
      "global_load_dwordx4 a[44:47], v239, s[58:59] offset:3072 sc1\n"
      "v_mfma_f32_16x16x32_bf16 v[132:135], a[144:147], v[64:67], v[132:135]\n"
      "v_cvt_pk_bf16_f32 v210, v136, v137\n"
      "v_mfma_f32_16x16x32_bf16 v[132:135], a[148:151], v[68:71], v[132:135]\n"
      "v_cvt_pk_bf16_f32 v211, v138, v139\n"
      "v_mfma_f32_16x16x32_bf16 v[132:135], a[152:155], v[84:87], v[132:135]\n"
      "s_mul_i32 s78, s90, s19\n"
      "v_mfma_f32_16x16x32_bf16 v[132:135], a[156:159], v[88:91], v[132:135]\n"
      "v_add_u32 v236, s78, v236\n"
      "v_mfma_f32_16x16x32_bf16 v[132:135], a[160:163], v[92:95], v[132:135]\n"
      "s_mul_i32 s78, s78, s99\n"
      "v_mfma_f32_16x16x32_bf16 v[132:135], a[164:167], v[108:111], v[132:135]\n"
      "v_add_u32 v237, s78, v237\n"
      "v_mfma_f32_16x16x32_bf16 v[132:135], a[168:171], v[112:115], v[132:135]\n"
      "s_add_u32 s52, s20, s33\n"
      "s_addc_u32 s53, s21, 0\n"
      "s_add_u32 m0, s17, 0xc000\n"
      "v_mfma_f32_16x16x32_bf16 v[132:135], a[172:175], v[116:119], v[132:135]\n"
      "global_load_lds_dwordx4 v236, s[52:53] offset:0 nt\n"
      "v_mfma_f32_16x16x32_bf16 v[136:139], a[176:179], v[36:39], v[12:15]\n"
      "ds_write_b64 v244, v[210:211] offset:12296\n"
      "v_mfma_f32_16x16x32_bf16 v[136:139], a[180:183], v[40:43], v[136:139]\n"
      "v_mfma_f32_16x16x32_bf16 v[136:139], a[184:187], v[44:47], v[136:139]\n"
      "v_mfma_f32_16x16x32_bf16 v[136:139], a[188:191], v[60:63], v[136:139]\n"
      "v_cvt_pk_bf16_f32 v212, v132, v133\n"
      "s_waitcnt vmcnt(35)\n"
      "v_mfma_f32_16x16x32_bf16 v[136:139], a[192:195], v[64:67], v[136:139]\n"
      "s_add_u32 m0, s17, 0xdf00\n"
      "v_mfma_f32_16x16x32_bf16 v[136:139], a[196:199], v[68:71], v[136:139]\n"
      "global_load_lds_dwordx4 v236, s[52:53] offset:256 nt\n"
      "v_mfma_f32_16x16x32_bf16 v[136:139], a[200:203], v[84:87], v[136:139]\n"
      "v_cvt_pk_bf16_f32 v213, v134, v135\n"
      "v_mfma_f32_16x16x32_bf16 v[136:139], a[204:207], v[88:91], v[136:139]\n"
      "s_waitcnt vmcnt(26)\n"
      "v_mfma_f32_16x16x32_bf16 v[136:139], a[208:211], v[92:95], v[136:139]\n"
      "v_mfma_f32_16x16x32_bf16 v[136:139], a[212:215], v[108:111], v[136:139]\n"
      "v_mfma_f32_16x16x32_bf16 v[136:139], a[216:219], v[112:115], v[136:139]\n"
      "s_add_u32 m0, s17, 0xfe00\n"
      "v_mfma_f32_16x16x32_bf16 v[136:139], a[220:223], v[116:119], v[136:139]\n"
      "global_load_lds_dwordx4 v236, s[52:53] offset:512 nt\n"
      "s_waitcnt vmcnt(23)\n"
      "v_mfma_f32_16x16x32_bf16 v[132:135], a[224:227], v[36:39], v[16:19]\n"
      "ds_write_b64 v245, v[212:213] offset:12288\n"
      "v_mfma_f32_16x16x32_bf16 v[132:135], a[228:231], v[40:43], v[132:135]\n"
      "v_mfma_f32_16x16x32_bf16 v[132:135], a[232:235], v[44:47], v[132:135]\n"
      "v_mfma_f32_16x16x32_bf16 v[132:135], a[236:239], v[60:63], v[132:135]\n"
      "v_cvt_pk_bf16_f32 v214, v136, v137\n"
      "s_waitcnt vmcnt(19)\n"
      "v_mfma_f32_16x16x32_bf16 v[132:135], a[0:3], v[64:67], v[132:135]\n"
      "s_add_u32 m0, s17, 0xd000\n"
      "v_mfma_f32_16x16x32_bf16 v[132:135], a[4:7], v[68:71], v[132:135]\n"
      "global_load_lds_dwordx4 v237, s[52:53] offset:0 nt\n"
      "v_mfma_f32_16x16x32_bf16 v[132:135], a[8:11], v[84:87], v[132:135]\n"
      "v_cvt_pk_bf16_f32 v215, v138, v139\n"
      "v_mfma_f32_16x16x32_bf16 v[132:135], a[12:15], v[88:91], v[132:135]\n"
      "s_waitcnt vmcnt(16)\n"
      "v_mfma_f32_16x16x32_bf16 v[132:135], a[16:19], v[92:95], v[132:135]\n"
      "v_mfma_f32_16x16x32_bf16 v[132:135], a[20:23], v[108:111], v[132:135]\n"
      "v_mfma_f32_16x16x32_bf16 v[132:135], v[140:143], v[112:115], v[132:135]\n"
      "s_add_u32 m0, s17, 0xef00\n"
      "v_mfma_f32_16x16x32_bf16 v[132:135], v[156:159], v[116:119], v[132:135]\n"
      "s_barrier\n"
      "global_load_lds_dwordx4 v237, s[52:53] offset:256 nt\n"
      "s_waitcnt vmcnt(13)\n"
      "v_mfma_f32_16x16x32_bf16 v[136:139], v[160:163], v[36:39], v[20:23]\n"
      "ds_read_b128 v[36:39], v232 offset:256\n"
      "ds_write_b64 v245, v[214:215] offset:12296\n"
      "v_mfma_f32_16x16x32_bf16 v[136:139], v[164:167], v[40:43], v[136:139]\n"
      "ds_read_b128 v[40:43], v233 offset:256\n"
      "v_mfma_f32_16x16x32_bf16 v[136:139], v[180:183], v[44:47], v[136:139]\n"
      "ds_read_b128 v[44:47], v234 offset:256\n"
      "v_mfma_f32_16x16x32_bf16 v[136:139], v[184:187], v[60:63], v[136:139]\n"
      "ds_read_b128 v[60:63], v235 offset:256\n"
      "v_cvt_pk_bf16_f32 v208, v132, v133\n"
      "s_waitcnt vmcnt(9)\n"
      "v_mfma_f32_16x16x32_bf16 v[136:139], v[188:191], v[64:67], v[136:139]\n"
      "ds_read_b128 v[64:67], v232 offset:8448\n"
      "s_add_u32 m0, s17, 0x10e00\n"
      "v_mfma_f32_16x16x32_bf16 v[136:139], v[204:207], v[68:71], v[136:139]\n"
      "ds_read_b128 v[68:71], v233 offset:8448\n"
      "global_load_lds_dwordx4 v237, s[52:53] offset:512 nt\n"
      "v_mfma_f32_16x16x32_bf16 v[136:139], a[24:27], v[84:87], v[136:139]\n"
      "ds_read_b128 v[84:87], v234 offset:8448\n"
      "v_cvt_pk_bf16_f32 v209, v134, v135\n"
      "v_mfma_f32_16x16x32_bf16 v[136:139], a[28:31], v[88:91], v[136:139]\n"
      "ds_read_b128 v[88:91], v235 offset:8448\n"
      "s_waitcnt vmcnt(6)\n"
      "v_mfma_f32_16x16x32_bf16 v[136:139], a[32:35], v[92:95], v[136:139]\n"
      "ds_read_b128 v[92:95], v232 offset:16640\n"
      "v_mfma_f32_16x16x32_bf16 v[136:139], a[36:39], v[108:111], v[136:139]\n"
      "ds_read_b128 v[108:111], v233 offset:16640\n"
      "v_mfma_f32_16x16x32_bf16 v[136:139], a[40:43], v[112:115], v[136:139]\n"
      "ds_read_b128 v[112:115], v234 offset:16640\n"
      "v_mfma_f32_16x16x32_bf16 v[136:139], a[44:47], v[116:119], v[136:139]\n"
      "ds_read_b128 v[116:119], v235 offset:16640\n"
      "s_waitcnt lgkmcnt(8)\n"
      "v_mfma_f32_16x16x32_bf16 v[132:135], a[240:243], v[36:39], v[0:3]\n"
      "s_add_u32 s52, s42, s33\n"
      "s_addc_u32 s53, s43, 0\n"
      "s_add_u32 m0, s17, 0x12000\n"
      "v_mfma_f32_16x16x32_bf16 v[132:135], a[244:247], v[40:43], v[132:135]\n"
      "global_load_lds_dwordx4 v236, s[52:53] offset:0 nt\n"
      "v_mfma_f32_16x16x32_bf16 v[132:135], a[248:251], v[44:47], v[132:135]\n"
      "ds_write_b64 v244, v[208:209] offset:12416\n"
      "v_mfma_f32_16x16x32_bf16 v[132:135], a[252:255], v[60:63], v[132:135]\n"
      "v_cvt_pk_bf16_f32 v210, v136, v137\n"
      "s_waitcnt lgkmcnt(5)\n"
      "v_mfma_f32_16x16x32_bf16 v[132:135], a[48:51], v[64:67], v[132:135]\n"
      "v_cvt_pk_bf16_f32 v211, v138, v139\n"
      "v_mfma_f32_16x16x32_bf16 v[132:135], a[52:55], v[68:71], v[132:135]\n"
      "v_mfma_f32_16x16x32_bf16 v[132:135], a[56:59], v[84:87], v[132:135]\n"
      "s_add_u32 m0, s17, 0x13f00\n"
      "v_mfma_f32_16x16x32_bf16 v[132:135], a[60:63], v[88:91], v[132:135]\n"
      "global_load_lds_dwordx4 v236, s[52:53] offset:256 nt\n"
      "s_waitcnt lgkmcnt(1)\n"
      "v_mfma_f32_16x16x32_bf16 v[132:135], a[64:67], v[92:95], v[132:135]\n"
      "v_mfma_f32_16x16x32_bf16 v[132:135], a[68:71], v[108:111], v[132:135]\n"
      "ds_write_b64 v244, v[210:211] offset:12424\n"
      "v_mfma_f32_16x16x32_bf16 v[132:135], a[72:75], v[112:115], v[132:135]\n"
      "v_mfma_f32_16x16x32_bf16 v[132:135], a[76:79], v[116:119], v[132:135]\n"
      "v_mfma_f32_16x16x32_bf16 v[136:139], a[80:83], v[36:39], v[4:7]\n"
      "s_add_u32 m0, s17, 0x15e00\n"
      "v_mfma_f32_16x16x32_bf16 v[136:139], a[84:87], v[40:43], v[136:139]\n"
      "global_load_lds_dwordx4 v236, s[52:53] offset:512 nt\n"
      "v_mfma_f32_16x16x32_bf16 v[136:139], a[88:91], v[44:47], v[136:139]\n"
      "v_mfma_f32_16x16x32_bf16 v[136:139], a[92:95], v[60:63], v[136:139]\n"
      "v_cvt_pk_bf16_f32 v208, v132, v133\n"
      "v_mfma_f32_16x16x32_bf16 v[136:139], a[96:99], v[64:67], v[136:139]\n"
      "v_cvt_pk_bf16_f32 v209, v134, v135\n"
      "v_mfma_f32_16x16x32_bf16 v[136:139], a[100:103], v[68:71], v[136:139]\n"
      "v_mfma_f32_16x16x32_bf16 v[136:139], a[104:107], v[84:87], v[136:139]\n"
      "s_add_u32 m0, s17, 0x13000\n"
      "v_mfma_f32_16x16x32_bf16 v[136:139], a[108:111], v[88:91], v[136:139]\n"
      "global_load_lds_dwordx4 v237, s[52:53] offset:0 nt\n"
      "v_mfma_f32_16x16x32_bf16 v[136:139], a[112:115], v[92:95], v[136:139]\n"
      "v_mfma_f32_16x16x32_bf16 v[136:139], a[116:119], v[108:111], v[136:139]\n"
      "ds_write_b64 v244, v[208:209] offset:24576\n"
      "v_mfma_f32_16x16x32_bf16 v[136:139], a[120:123], v[112:115], v[136:139]\n"
      "v_mfma_f32_16x16x32_bf16 v[136:139], a[124:127], v[116:119], v[136:139]\n"
      "v_mfma_f32_16x16x32_bf16 v[132:135], a[128:131], v[36:39], v[8:11]\n"
      "s_add_u32 m0, s17, 0x14f00\n"
      "v_mfma_f32_16x16x32_bf16 v[132:135], a[132:135], v[40:43], v[132:135]\n"
      "global_load_lds_dwordx4 v237, s[52:53] offset:256 nt\n"
      "v_mfma_f32_16x16x32_bf16 v[132:135], a[136:139], v[44:47], v[132:135]\n"
      "v_mfma_f32_16x16x32_bf16 v[132:135], a[140:143], v[60:63], v[132:135]\n"
      "v_cvt_pk_bf16_f32 v210, v136, v137\n"
      "v_mfma_f32_16x16x32_bf16 v[132:135], a[144:147], v[64:67], v[132:135]\n"
      "v_cvt_pk_bf16_f32 v211, v138, v139\n"
      "v_mfma_f32_16x16x32_bf16 v[132:135], a[148:151], v[68:71], v[132:135]\n"
      "v_mfma_f32_16x16x32_bf16 v[132:135], a[152:155], v[84:87], v[132:135]\n"
      "s_add_u32 m0, s17, 0x16e00\n"
      "v_mfma_f32_16x16x32_bf16 v[132:135], a[156:159], v[88:91], v[132:135]\n"
      "global_load_lds_dwordx4 v237, s[52:53] offset:512 nt\n"
      "v_mfma_f32_16x16x32_bf16 v[132:135], a[160:163], v[92:95], v[132:135]\n"
      "v_mfma_f32_16x16x32_bf16 v[132:135], a[164:167], v[108:111], v[132:135]\n"
      "ds_write_b64 v244, v[210:211] offset:24584\n"
      "v_mfma_f32_16x16x32_bf16 v[132:135], a[168:171], v[112:115], v[132:135]\n"
      "v_mfma_f32_16x16x32_bf16 v[132:135], a[172:175], v[116:119], v[132:135]\n"
      "v_mfma_f32_16x16x32_bf16 v[136:139], a[176:179], v[36:39], v[12:15]\n"
      "v_mfma_f32_16x16x32_bf16 v[136:139], a[180:183], v[40:43], v[136:139]\n"
      "v_mfma_f32_16x16x32_bf16 v[136:139], a[184:187], v[44:47], v[136:139]\n"
      "v_mfma_f32_16x16x32_bf16 v[136:139], a[188:191], v[60:63], v[136:139]\n"
      "v_cvt_pk_bf16_f32 v212, v132, v133\n"
      "v_mfma_f32_16x16x32_bf16 v[136:139], a[192:195], v[64:67], v[136:139]\n"
      "v_cvt_pk_bf16_f32 v213, v134, v135\n"
      "v_mfma_f32_16x16x32_bf16 v[136:139], a[196:199], v[68:71], v[136:139]\n"
      "v_mfma_f32_16x16x32_bf16 v[136:139], a[200:203], v[84:87], v[136:139]\n"
      "v_mfma_f32_16x16x32_bf16 v[136:139], a[204:207], v[88:91], v[136:139]\n"
      "v_mfma_f32_16x16x32_bf16 v[136:139], a[208:211], v[92:95], v[136:139]\n"
      "v_mfma_f32_16x16x32_bf16 v[136:139], a[212:215], v[108:111], v[136:139]\n"
      "ds_write_b64 v245, v[212:213] offset:24576\n"
      "v_mfma_f32_16x16x32_bf16 v[136:139], a[216:219], v[112:115], v[136:139]\n"
      "v_mfma_f32_16x16x32_bf16 v[136:139], a[220:223], v[116:119], v[136:139]\n"
      "v_mfma_f32_16x16x32_bf16 v[132:135], a[224:227], v[36:39], v[16:19]\n"
      "v_mfma_f32_16x16x32_bf16 v[132:135], a[228:231], v[40:43], v[132:135]\n"
      "v_mfma_f32_16x16x32_bf16 v[132:135], a[232:235], v[44:47], v[132:135]\n"
      "v_mfma_f32_16x16x32_bf16 v[132:135], a[236:239], v[60:63], v[132:135]\n"
      "v_cvt_pk_bf16_f32 v214, v136, v137\n"
      "v_mfma_f32_16x16x32_bf16 v[132:135], a[0:3], v[64:67], v[132:135]\n"
      "v_cvt_pk_bf16_f32 v215, v138, v139\n"
      "v_mfma_f32_16x16x32_bf16 v[132:135], a[4:7], v[68:71], v[132:135]\n"
      "v_mfma_f32_16x16x32_bf16 v[132:135], a[8:11], v[84:87], v[132:135]\n"
      "v_mfma_f32_16x16x32_bf16 v[132:135], a[12:15], v[88:91], v[132:135]\n"
      "v_mfma_f32_16x16x32_bf16 v[132:135], a[16:19], v[92:95], v[132:135]\n"
      "v_mfma_f32_16x16x32_bf16 v[132:135], a[20:23], v[108:111], v[132:135]\n"
      "ds_write_b64 v245, v[214:215] offset:24584\n"
      "v_mfma_f32_16x16x32_bf16 v[132:135], v[140:143], v[112:115], v[132:135]\n"
      "s_lshl_b32 s79, s98, 1\n"
      "v_mfma_f32_16x16x32_bf16 v[132:135], v[156:159], v[116:119], v[132:135]\n"
      "s_waitcnt lgkmcnt(4)\n"
      "s_barrier\n"
      "v_mad_i32_i24 v252, v251, s79, v252\n"
      "v_mfma_f32_16x16x32_bf16 v[136:139], v[160:163], v[36:39], v[20:23]\n"
      "ds_read_b128 v[36:39], v232 offset:24576\n"
      "s_mul_i32 s79, s79, s100\n"
      "v_mfma_f32_16x16x32_bf16 v[136:139], v[164:167], v[40:43], v[136:139]\n"
      "ds_read_b128 v[40:43], v233 offset:24576\n"
      "v_add_u32 v247, s79, v247\n"
      "v_mfma_f32_16x16x32_bf16 v[136:139], v[180:183], v[44:47], v[136:139]\n"
      "ds_read_b128 v[44:47], v234 offset:24576\n"
      "s_mul_i32 s79, s79, s101\n"
      "v_mfma_f32_16x16x32_bf16 v[136:139], v[184:187], v[60:63], v[136:139]\n"
      "ds_read_b128 v[60:63], v235 offset:24576\n"
      "v_cvt_pk_bf16_f32 v208, v132, v133\n"
      "v_mfma_f32_16x16x32_bf16 v[136:139], v[188:191], v[64:67], v[136:139]\n"
      "ds_read_b128 v[64:67], v232 offset:32768\n"
      "v_cvt_pk_bf16_f32 v209, v134, v135\n"
      "v_mfma_f32_16x16x32_bf16 v[136:139], v[204:207], v[68:71], v[136:139]\n"
      "ds_read_b128 v[68:71], v233 offset:32768\n"
      "v_add_u32 v253, s79, v253\n"
      "v_mfma_f32_16x16x32_bf16 v[136:139], a[24:27], v[84:87], v[136:139]\n"
      "ds_read_b128 v[84:87], v234 offset:32768\n"
      "v_mfma_f32_16x16x32_bf16 v[136:139], a[28:31], v[88:91], v[136:139]\n"
      "ds_read_b128 v[88:91], v235 offset:32768\n"
      "v_mfma_f32_16x16x32_bf16 v[136:139], a[32:35], v[92:95], v[136:139]\n"
      "ds_read_b128 v[92:95], v232 offset:40960\n"
      "v_mfma_f32_16x16x32_bf16 v[136:139], a[36:39], v[108:111], v[136:139]\n"
      "ds_read_b128 v[108:111], v233 offset:40960\n"
      "v_mfma_f32_16x16x32_bf16 v[136:139], a[40:43], v[112:115], v[136:139]\n"
      "ds_read_b128 v[112:115], v234 offset:40960\n"
      "v_mfma_f32_16x16x32_bf16 v[136:139], a[44:47], v[116:119], v[136:139]\n"
      "ds_read_b128 v[116:119], v235 offset:40960\n"
      "LJOIN:\n"
      "s_waitcnt lgkmcnt(8)\n"
      "v_mfma_f32_16x16x32_bf16 v[132:135], a[240:243], v[36:39], v[0:3]\n"
      "ds_write_b64 v244, v[208:209] offset:24704\n"
      "v_mfma_f32_16x16x32_bf16 v[132:135], a[244:247], v[40:43], v[132:135]\n"
      "s_add_u32 s54, s30, s32\n"
      "s_addc_u32 s55, s31, 0\n"
      "v_mfma_f32_16x16x32_bf16 v[132:135], a[248:251], v[44:47], v[132:135]\n"
      "v_mfma_f32_16x16x32_bf16 v[132:135], a[252:255], v[60:63], v[132:135]\n"
      "v_cvt_pk_bf16_f32 v210, v136, v137\n"
      "s_waitcnt lgkmcnt(5)\n"
      "v_mfma_f32_16x16x32_bf16 v[132:135], a[48:51], v[64:67], v[132:135]\n"
      "v_cvt_pk_bf16_f32 v211, v138, v139\n"
      "v_mfma_f32_16x16x32_bf16 v[132:135], a[52:55], v[68:71], v[132:135]\n"
      "s_mul_i32 s78, s91, s19\n"
      "v_mfma_f32_16x16x32_bf16 v[132:135], a[56:59], v[84:87], v[132:135]\n"
      "ds_read_b128 v[216:219], v246 offset:12288\n"
      "v_add_u32 v236, s78, v236\n"
      "v_mfma_f32_16x16x32_bf16 v[132:135], a[60:63], v[88:91], v[132:135]\n"
      "ds_read_b128 v[220:223], v246 offset:13312\n"
      "s_waitcnt lgkmcnt(3)\n"
      "v_mfma_f32_16x16x32_bf16 v[132:135], a[64:67], v[92:95], v[132:135]\n"
      "ds_read_b128 v[224:227], v246 offset:14336\n"
      "v_mfma_f32_16x16x32_bf16 v[132:135], a[68:71], v[108:111], v[132:135]\n"
      "ds_write_b64 v244, v[210:211] offset:24712\n"
      "v_mfma_f32_16x16x32_bf16 v[132:135], a[72:75], v[112:115], v[132:135]\n"
      "s_mul_i32 s78, s78, s99\n"
      "v_mfma_f32_16x16x32_bf16 v[132:135], a[76:79], v[116:119], v[132:135]\n"
      "v_add_u32 v237, s78, v237\n"
      "v_mfma_f32_16x16x32_bf16 v[136:139], a[80:83], v[36:39], v[4:7]\n"
      "v_mfma_f32_16x16x32_bf16 v[136:139], a[84:87], v[40:43], v[136:139]\n"
      "v_mfma_f32_16x16x32_bf16 v[136:139], a[88:91], v[44:47], v[136:139]\n"
      "v_mfma_f32_16x16x32_bf16 v[136:139], a[92:95], v[60:63], v[136:139]\n"
      "v_cvt_pk_bf16_f32 v208, v132, v133\n"
      "v_mfma_f32_16x16x32_bf16 v[136:139], a[96:99], v[64:67], v[136:139]\n"
      "v_cvt_pk_bf16_f32 v209, v134, v135\n"
      "v_mfma_f32_16x16x32_bf16 v[136:139], a[100:103], v[68:71], v[136:139]\n"
      "s_waitcnt lgkmcnt(3)\n"
      "v_mfma_f32_16x16x32_bf16 v[136:139], a[104:107], v[84:87], v[136:139]\n"
      "global_store_dwordx4 v247, v[216:219], s[54:55] offset:0 sc1\n"
      "v_mfma_f32_16x16x32_bf16 v[136:139], a[108:111], v[88:91], v[136:139]\n"
      "s_add_u32 s52, s20, s34\n"
      "s_addc_u32 s53, s21, 0\n"
      "v_mfma_f32_16x16x32_bf16 v[136:139], a[112:115], v[92:95], v[136:139]\n"
      "s_add_u32 m0, s17, 0x0\n"
      "v_mfma_f32_16x16x32_bf16 v[136:139], a[116:119], v[108:111], v[136:139]\n"
      "ds_write_b64 v244, v[208:209] offset:0\n"
      "v_mfma_f32_16x16x32_bf16 v[136:139], a[120:123], v[112:115], v[136:139]\n"
      "global_load_lds_dwordx4 v236, s[52:53] offset:0 nt\n"
      "v_mfma_f32_16x16x32_bf16 v[136:139], a[124:127], v[116:119], v[136:139]\n"
      "v_mfma_f32_16x16x32_bf16 v[132:135], a[128:131], v[36:39], v[8:11]\n"
      "v_mfma_f32_16x16x32_bf16 v[132:135], a[132:135], v[40:43], v[132:135]\n"
      "v_mfma_f32_16x16x32_bf16 v[132:135], a[136:139], v[44:47], v[132:135]\n"
      "v_mfma_f32_16x16x32_bf16 v[132:135], a[140:143], v[60:63], v[132:135]\n"
      "v_cvt_pk_bf16_f32 v210, v136, v137\n"
      "v_mfma_f32_16x16x32_bf16 v[132:135], a[144:147], v[64:67], v[132:135]\n"
      "v_cvt_pk_bf16_f32 v211, v138, v139\n"
      "v_mfma_f32_16x16x32_bf16 v[132:135], a[148:151], v[68:71], v[132:135]\n"
      "v_mfma_f32_16x16x32_bf16 v[132:135], a[152:155], v[84:87], v[132:135]\n"
      "s_waitcnt lgkmcnt(3)\n"
      "v_mfma_f32_16x16x32_bf16 v[132:135], a[156:159], v[88:91], v[132:135]\n"
      "global_store_dwordx4 v252, v[220:223], s[54:55] offset:0 sc1\n"
      "v_mfma_f32_16x16x32_bf16 v[132:135], a[160:163], v[92:95], v[132:135]\n"
      "s_add_u32 m0, s17, 0x1f00\n"
      "v_mfma_f32_16x16x32_bf16 v[132:135], a[164:167], v[108:111], v[132:135]\n"
      "ds_write_b64 v244, v[210:211] offset:8\n"
      "v_mfma_f32_16x16x32_bf16 v[132:135], a[168:171], v[112:115], v[132:135]\n"
      "global_load_lds_dwordx4 v236, s[52:53] offset:256 nt\n"
      "v_mfma_f32_16x16x32_bf16 v[132:135], a[172:175], v[116:119], v[132:135]\n"
      "v_mfma_f32_16x16x32_bf16 v[136:139], a[176:179], v[36:39], v[12:15]\n"
      "v_mfma_f32_16x16x32_bf16 v[136:139], a[180:183], v[40:43], v[136:139]\n"
      "v_mfma_f32_16x16x32_bf16 v[136:139], a[184:187], v[44:47], v[136:139]\n"
      "v_mfma_f32_16x16x32_bf16 v[136:139], a[188:191], v[60:63], v[136:139]\n"
      "v_cvt_pk_bf16_f32 v212, v132, v133\n"
      "v_mfma_f32_16x16x32_bf16 v[136:139], a[192:195], v[64:67], v[136:139]\n"
      "v_cvt_pk_bf16_f32 v213, v134, v135\n"
      "v_mfma_f32_16x16x32_bf16 v[136:139], a[196:199], v[68:71], v[136:139]\n"
      "v_mfma_f32_16x16x32_bf16 v[136:139], a[200:203], v[84:87], v[136:139]\n"
      "v_mfma_f32_16x16x32_bf16 v[136:139], a[204:207], v[88:91], v[136:139]\n"
      "s_waitcnt lgkmcnt(3)\n"
      "v_mfma_f32_16x16x32_bf16 v[136:139], a[208:211], v[92:95], v[136:139]\n"
      "global_store_dwordx4 v253, v[224:227], s[54:55] offset:0 sc1\n"
      "v_mfma_f32_16x16x32_bf16 v[136:139], a[212:215], v[108:111], v[136:139]\n"
      "ds_write_b64 v245, v[212:213] offset:0\n"
      "v_mfma_f32_16x16x32_bf16 v[136:139], a[216:219], v[112:115], v[136:139]\n"
      "s_add_u32 m0, s17, 0x3e00\n"
      "v_mfma_f32_16x16x32_bf16 v[136:139], a[220:223], v[116:119], v[136:139]\n"
      "global_load_lds_dwordx4 v236, s[52:53] offset:512 nt\n"
      "v_mfma_f32_16x16x32_bf16 v[132:135], a[224:227], v[36:39], v[16:19]\n"
      "v_mfma_f32_16x16x32_bf16 v[132:135], a[228:231], v[40:43], v[132:135]\n"
      "v_mfma_f32_16x16x32_bf16 v[132:135], a[232:235], v[44:47], v[132:135]\n"
      "v_mfma_f32_16x16x32_bf16 v[132:135], a[236:239], v[60:63], v[132:135]\n"
      "v_cvt_pk_bf16_f32 v214, v136, v137\n"
      "v_mfma_f32_16x16x32_bf16 v[132:135], a[0:3], v[64:67], v[132:135]\n"
      "v_cvt_pk_bf16_f32 v215, v138, v139\n"
      "v_mfma_f32_16x16x32_bf16 v[132:135], a[4:7], v[68:71], v[132:135]\n"
      "v_mfma_f32_16x16x32_bf16 v[132:135], a[8:11], v[84:87], v[132:135]\n"
      "v_mfma_f32_16x16x32_bf16 v[132:135], a[12:15], v[88:91], v[132:135]\n"
      "v_mfma_f32_16x16x32_bf16 v[132:135], a[16:19], v[92:95], v[132:135]\n"
      "v_mfma_f32_16x16x32_bf16 v[132:135], a[20:23], v[108:111], v[132:135]\n"
      "ds_write_b64 v245, v[214:215] offset:8\n"
      "v_mfma_f32_16x16x32_bf16 v[132:135], v[140:143], v[112:115], v[132:135]\n"
      "v_mfma_f32_16x16x32_bf16 v[132:135], v[156:159], v[116:119], v[132:135]\n"
      "s_waitcnt lgkmcnt(4)\n"
      "s_barrier\n"
      "v_mfma_f32_16x16x32_bf16 v[136:139], v[160:163], v[36:39], v[20:23]\n"
      "ds_read_b128 v[36:39], v232 offset:24832\n"
      "v_mfma_f32_16x16x32_bf16 v[136:139], v[164:167], v[40:43], v[136:139]\n"
      "ds_read_b128 v[40:43], v233 offset:24832\n"
      "v_mfma_f32_16x16x32_bf16 v[136:139], v[180:183], v[44:47], v[136:139]\n"
      "ds_read_b128 v[44:47], v234 offset:24832\n"
      "v_mfma_f32_16x16x32_bf16 v[136:139], v[184:187], v[60:63], v[136:139]\n"
      "ds_read_b128 v[60:63], v235 offset:24832\n"
      "v_cvt_pk_bf16_f32 v208, v132, v133\n"
      "v_mfma_f32_16x16x32_bf16 v[136:139], v[188:191], v[64:67], v[136:139]\n"
      "ds_read_b128 v[64:67], v232 offset:33024\n"
      "v_cvt_pk_bf16_f32 v209, v134, v135\n"
      "v_mfma_f32_16x16x32_bf16 v[136:139], v[204:207], v[68:71], v[136:139]\n"
      "ds_read_b128 v[68:71], v233 offset:33024\n"
      "v_mfma_f32_16x16x32_bf16 v[136:139], a[24:27], v[84:87], v[136:139]\n"
      "ds_read_b128 v[84:87], v234 offset:33024\n"
      "v_mfma_f32_16x16x32_bf16 v[136:139], a[28:31], v[88:91], v[136:139]\n"
      "ds_read_b128 v[88:91], v235 offset:33024\n"
      "v_mfma_f32_16x16x32_bf16 v[136:139], a[32:35], v[92:95], v[136:139]\n"
      "ds_read_b128 v[92:95], v232 offset:41216\n"
      "v_mfma_f32_16x16x32_bf16 v[136:139], a[36:39], v[108:111], v[136:139]\n"
      "ds_read_b128 v[108:111], v233 offset:41216\n"
      "ds_write_b64 v244, v[208:209] offset:128\n"
      "v_mfma_f32_16x16x32_bf16 v[136:139], a[40:43], v[112:115], v[136:139]\n"
      "ds_read_b128 v[112:115], v234 offset:41216\n"
      "v_mfma_f32_16x16x32_bf16 v[136:139], a[44:47], v[116:119], v[136:139]\n"
      "ds_read_b128 v[116:119], v235 offset:41216\n"
      "s_waitcnt lgkmcnt(9)\n"
      "v_mfma_f32_16x16x32_bf16 v[132:135], a[240:243], v[36:39], v[0:3]\n"
      "v_add_u32 v232, 0xc000, v232\n"
      "v_mfma_f32_16x16x32_bf16 v[132:135], a[244:247], v[40:43], v[132:135]\n"
      "v_add_u32 v233, 0xc000, v233\n"
      "v_mfma_f32_16x16x32_bf16 v[132:135], a[248:251], v[44:47], v[132:135]\n"
      "v_add_u32 v234, 0xc000, v234\n"
      "v_mfma_f32_16x16x32_bf16 v[132:135], a[252:255], v[60:63], v[132:135]\n"
      "v_add_u32 v235, 0xc000, v235\n"
      "s_waitcnt lgkmcnt(5)\n"
      "v_mfma_f32_16x16x32_bf16 v[132:135], a[48:51], v[64:67], v[132:135]\n"
      "v_cvt_pk_bf16_f32 v210, v136, v137\n"
      "v_mfma_f32_16x16x32_bf16 v[132:135], a[52:55], v[68:71], v[132:135]\n"
      "v_cvt_pk_bf16_f32 v211, v138, v139\n"
      "v_mfma_f32_16x16x32_bf16 v[132:135], a[56:59], v[84:87], v[132:135]\n"
      "ds_read_b128 v[216:219], v246 offset:24576\n"
      "v_mfma_f32_16x16x32_bf16 v[132:135], a[60:63], v[88:91], v[132:135]\n"
      "ds_read_b128 v[220:223], v246 offset:25600\n"
      "s_waitcnt lgkmcnt(2)\n"
      "v_mfma_f32_16x16x32_bf16 v[132:135], a[64:67], v[92:95], v[132:135]\n"
      "ds_read_b128 v[224:227], v246 offset:26624\n"
      "v_mfma_f32_16x16x32_bf16 v[132:135], a[68:71], v[108:111], v[132:135]\n"
      "v_lshlrev_b32 v0, 16, v24\n"
      "v_mfma_f32_16x16x32_bf16 v[132:135], a[72:75], v[112:115], v[132:135]\n"
      "ds_write_b64 v244, v[210:211] offset:136\n"
      "v_mfma_f32_16x16x32_bf16 v[132:135], a[76:79], v[116:119], v[132:135]\n"
      "v_and_b32 v1, 0xffff0000, v24\n"
      "v_mfma_f32_16x16x32_bf16 v[136:139], a[80:83], v[36:39], v[4:7]\n"
      "v_lshlrev_b32 v2, 16, v25\n"
      "v_mfma_f32_16x16x32_bf16 v[136:139], a[84:87], v[40:43], v[136:139]\n"
      "v_and_b32 v3, 0xffff0000, v25\n"
      "v_mfma_f32_16x16x32_bf16 v[136:139], a[88:91], v[44:47], v[136:139]\n"
      "v_mfma_f32_16x16x32_bf16 v[136:139], a[92:95], v[60:63], v[136:139]\n"
      "v_cvt_pk_bf16_f32 v208, v132, v133\n"
      "v_mfma_f32_16x16x32_bf16 v[136:139], a[96:99], v[64:67], v[136:139]\n"
      "v_cvt_pk_bf16_f32 v209, v134, v135\n"
      "v_mfma_f32_16x16x32_bf16 v[136:139], a[100:103], v[68:71], v[136:139]\n"
      "s_waitcnt lgkmcnt(3)\n"
      "v_mfma_f32_16x16x32_bf16 v[136:139], a[104:107], v[84:87], v[136:139]\n"
      "global_store_dwordx4 v247, v[216:219], s[54:55] offset:768 sc1\n"
      "v_mfma_f32_16x16x32_bf16 v[136:139], a[108:111], v[88:91], v[136:139]\n"
      "s_add_u32 m0, s17, 0x1000\n"
      "v_mfma_f32_16x16x32_bf16 v[136:139], a[112:115], v[92:95], v[136:139]\n"
      "global_load_lds_dwordx4 v237, s[52:53] offset:0 nt\n"
      "v_mfma_f32_16x16x32_bf16 v[136:139], a[116:119], v[108:111], v[136:139]\n"
      "ds_write_b64 v244, v[208:209] offset:12288\n"
      "v_mfma_f32_16x16x32_bf16 v[136:139], a[120:123], v[112:115], v[136:139]\n"
      "v_lshlrev_b32 v4, 16, v26\n"
      "v_mfma_f32_16x16x32_bf16 v[136:139], a[124:127], v[116:119], v[136:139]\n"
      "v_and_b32 v5, 0xffff0000, v26\n"
      "v_mfma_f32_16x16x32_bf16 v[132:135], a[128:131], v[36:39], v[8:11]\n"
      "v_lshlrev_b32 v6, 16, v27\n"
      "v_mfma_f32_16x16x32_bf16 v[132:135], a[132:135], v[40:43], v[132:135]\n"
      "v_and_b32 v7, 0xffff0000, v27\n"
      "v_mfma_f32_16x16x32_bf16 v[132:135], a[136:139], v[44:47], v[132:135]\n"
      "v_mfma_f32_16x16x32_bf16 v[132:135], a[140:143], v[60:63], v[132:135]\n"
      "v_cvt_pk_bf16_f32 v210, v136, v137\n"
      "v_mfma_f32_16x16x32_bf16 v[132:135], a[144:147], v[64:67], v[132:135]\n"
      "v_cvt_pk_bf16_f32 v211, v138, v139\n"
      "v_mfma_f32_16x16x32_bf16 v[132:135], a[148:151], v[68:71], v[132:135]\n"
      "v_lshlrev_b32 v8, 16, v28\n"
      "v_mfma_f32_16x16x32_bf16 v[132:135], a[152:155], v[84:87], v[132:135]\n"
      "s_waitcnt lgkmcnt(3)\n"
      "v_mfma_f32_16x16x32_bf16 v[132:135], a[156:159], v[88:91], v[132:135]\n"
      "global_store_dwordx4 v252, v[220:223], s[54:55] offset:768 sc1\n"
      "v_mfma_f32_16x16x32_bf16 v[132:135], a[160:163], v[92:95], v[132:135]\n"
      "s_add_u32 m0, s17, 0x2f00\n"
      "v_mfma_f32_16x16x32_bf16 v[132:135], a[164:167], v[108:111], v[132:135]\n"
      "ds_write_b64 v244, v[210:211] offset:12296\n"
      "v_mfma_f32_16x16x32_bf16 v[132:135], a[168:171], v[112:115], v[132:135]\n"
      "global_load_lds_dwordx4 v237, s[52:53] offset:256 nt\n"
      "v_mfma_f32_16x16x32_bf16 v[132:135], a[172:175], v[116:119], v[132:135]\n"
      "v_and_b32 v9, 0xffff0000, v28\n"
      "v_mfma_f32_16x16x32_bf16 v[136:139], a[176:179], v[36:39], v[12:15]\n"
      "v_lshlrev_b32 v10, 16, v29\n"
      "v_mfma_f32_16x16x32_bf16 v[136:139], a[180:183], v[40:43], v[136:139]\n"
      "v_and_b32 v11, 0xffff0000, v29\n"
      "v_mfma_f32_16x16x32_bf16 v[136:139], a[184:187], v[44:47], v[136:139]\n"
      "v_mfma_f32_16x16x32_bf16 v[136:139], a[188:191], v[60:63], v[136:139]\n"
      "v_cvt_pk_bf16_f32 v212, v132, v133\n"
      "v_mfma_f32_16x16x32_bf16 v[136:139], a[192:195], v[64:67], v[136:139]\n"
      "v_cvt_pk_bf16_f32 v213, v134, v135\n"
      "v_mfma_f32_16x16x32_bf16 v[136:139], a[196:199], v[68:71], v[136:139]\n"
      "v_lshlrev_b32 v12, 16, v30\n"
      "v_mfma_f32_16x16x32_bf16 v[136:139], a[200:203], v[84:87], v[136:139]\n"
      "v_and_b32 v13, 0xffff0000, v30\n"
      "v_mfma_f32_16x16x32_bf16 v[136:139], a[204:207], v[88:91], v[136:139]\n"
      "s_waitcnt lgkmcnt(3)\n"
      "v_mfma_f32_16x16x32_bf16 v[136:139], a[208:211], v[92:95], v[136:139]\n"
      "global_store_dwordx4 v253, v[224:227], s[54:55] offset:768 sc1\n"
      "v_mfma_f32_16x16x32_bf16 v[136:139], a[212:215], v[108:111], v[136:139]\n"
      "ds_write_b64 v245, v[212:213] offset:12288\n"
      "v_mfma_f32_16x16x32_bf16 v[136:139], a[216:219], v[112:115], v[136:139]\n"
      "s_add_u32 m0, s17, 0x4e00\n"
      "v_mfma_f32_16x16x32_bf16 v[136:139], a[220:223], v[116:119], v[136:139]\n"
      "global_load_lds_dwordx4 v237, s[52:53] offset:512 nt\n"
      "v_mfma_f32_16x16x32_bf16 v[132:135], a[224:227], v[36:39], v[16:19]\n"
      "v_lshlrev_b32 v14, 16, v31\n"
      "v_mfma_f32_16x16x32_bf16 v[132:135], a[228:231], v[40:43], v[132:135]\n"
      "v_and_b32 v15, 0xffff0000, v31\n"
      "v_mfma_f32_16x16x32_bf16 v[132:135], a[232:235], v[44:47], v[132:135]\n"
      "v_mfma_f32_16x16x32_bf16 v[132:135], a[236:239], v[60:63], v[132:135]\n"
      "s_memrealtime s[84:85]\n"
      "v_mfma_f32_16x16x32_bf16 v[132:135], a[0:3], v[64:67], v[132:135]\n"
      "v_cvt_pk_bf16_f32 v214, v136, v137\n"
      "v_mfma_f32_16x16x32_bf16 v[132:135], a[4:7], v[68:71], v[132:135]\n"
      "v_cvt_pk_bf16_f32 v215, v138, v139\n"
      "v_mfma_f32_16x16x32_bf16 v[132:135], a[8:11], v[84:87], v[132:135]\n"
      "v_lshlrev_b32 v16, 16, v32\n"
      "v_mfma_f32_16x16x32_bf16 v[132:135], a[12:15], v[88:91], v[132:135]\n"
      "v_and_b32 v17, 0xffff0000, v32\n"
      "v_mfma_f32_16x16x32_bf16 v[132:135], a[16:19], v[92:95], v[132:135]\n"
      "v_lshlrev_b32 v18, 16, v33\n"
      "v_mfma_f32_16x16x32_bf16 v[132:135], a[20:23], v[108:111], v[132:135]\n"
      "v_and_b32 v19, 0xffff0000, v33\n"
      "v_mfma_f32_16x16x32_bf16 v[132:135], v[140:143], v[112:115], v[132:135]\n"
      "s_waitcnt lgkmcnt(0)\n"
      "s_mov_b32 s48, s84\n"
      "v_mfma_f32_16x16x32_bf16 v[132:135], v[156:159], v[116:119], v[132:135]\n"
      "s_waitcnt vmcnt(18)\n"
      "s_barrier\n"
      "ds_write_b64 v245, v[214:215] offset:12296\n"
      "v_mfma_f32_16x16x32_bf16 v[136:139], v[160:163], v[36:39], v[20:23]\n"
      "ds_read_b128 v[36:39], v232 offset:0\n"
      "v_mfma_f32_16x16x32_bf16 v[136:139], v[164:167], v[40:43], v[136:139]\n"
      "ds_read_b128 v[40:43], v233 offset:0\n"
      "v_mfma_f32_16x16x32_bf16 v[136:139], v[180:183], v[44:47], v[136:139]\n"
      "ds_read_b128 v[44:47], v234 offset:0\n"
      "v_mfma_f32_16x16x32_bf16 v[136:139], v[184:187], v[60:63], v[136:139]\n"
      "ds_read_b128 v[60:63], v235 offset:0\n"
      "v_cvt_pk_bf16_f32 v208, v132, v133\n"
      "v_mfma_f32_16x16x32_bf16 v[136:139], v[188:191], v[64:67], v[136:139]\n"
      "ds_read_b128 v[64:67], v232 offset:8192\n"
      "v_cvt_pk_bf16_f32 v209, v134, v135\n"
      "v_mfma_f32_16x16x32_bf16 v[136:139], v[204:207], v[68:71], v[136:139]\n"
      "ds_read_b128 v[68:71], v233 offset:8192\n"
      "v_mfma_f32_16x16x32_bf16 v[136:139], a[24:27], v[84:87], v[136:139]\n"
      "ds_read_b128 v[84:87], v234 offset:8192\n"
      "v_mfma_f32_16x16x32_bf16 v[136:139], a[28:31], v[88:91], v[136:139]\n"
      "ds_read_b128 v[88:91], v235 offset:8192\n"
      "v_mfma_f32_16x16x32_bf16 v[136:139], a[32:35], v[92:95], v[136:139]\n"
      "ds_read_b128 v[92:95], v232 offset:16384\n"
      "v_mfma_f32_16x16x32_bf16 v[136:139], a[36:39], v[108:111], v[136:139]\n"
      "ds_read_b128 v[108:111], v233 offset:16384\n"
      "v_mfma_f32_16x16x32_bf16 v[136:139], a[40:43], v[112:115], v[136:139]\n"
      "ds_read_b128 v[112:115], v234 offset:16384\n"
      "v_mfma_f32_16x16x32_bf16 v[136:139], a[44:47], v[116:119], v[136:139]\n"
      "ds_read_b128 v[116:119], v235 offset:16384\n"
      "s_waitcnt lgkmcnt(8)\n"
      "v_mfma_f32_16x16x32_bf16 v[132:135], a[240:243], v[36:39], v[0:3]\n"
      "ds_write_b64 v244, v[208:209] offset:12416\n"
      "v_mfma_f32_16x16x32_bf16 v[132:135], a[244:247], v[40:43], v[132:135]\n"
      "v_lshlrev_b32 v20, 16, v34\n"
      "v_mfma_f32_16x16x32_bf16 v[132:135], a[248:251], v[44:47], v[132:135]\n"
      "v_and_b32 v21, 0xffff0000, v34\n"
      "v_mfma_f32_16x16x32_bf16 v[132:135], a[252:255], v[60:63], v[132:135]\n"
      "v_lshlrev_b32 v22, 16, v35\n"
      "s_waitcnt lgkmcnt(5)\n"
      "v_mfma_f32_16x16x32_bf16 v[132:135], a[48:51], v[64:67], v[132:135]\n"
      "v_and_b32 v23, 0xffff0000, v35\n"
      "v_mfma_f32_16x16x32_bf16 v[132:135], a[52:55], v[68:71], v[132:135]\n"
      "v_cvt_pk_bf16_f32 v210, v136, v137\n"
      "v_mfma_f32_16x16x32_bf16 v[132:135], a[56:59], v[84:87], v[132:135]\n"
      "v_cvt_pk_bf16_f32 v211, v138, v139\n"
      "v_mfma_f32_16x16x32_bf16 v[132:135], a[60:63], v[88:91], v[132:135]\n"
      "s_add_u32 s54, s44, s32\n"
      "s_addc_u32 s55, s45, 0\n"
      "s_waitcnt lgkmcnt(1)\n"
      "v_mfma_f32_16x16x32_bf16 v[132:135], a[64:67], v[92:95], v[132:135]\n"
      "ds_read_b128 v[216:219], v246 offset:0\n"
      "v_mfma_f32_16x16x32_bf16 v[132:135], a[68:71], v[108:111], v[132:135]\n"
      "ds_read_b128 v[220:223], v246 offset:1024\n"
      "v_mfma_f32_16x16x32_bf16 v[132:135], a[72:75], v[112:115], v[132:135]\n"
      "ds_read_b128 v[224:227], v246 offset:2048\n"
      "v_mfma_f32_16x16x32_bf16 v[132:135], a[76:79], v[116:119], v[132:135]\n"
      "ds_write_b64 v244, v[210:211] offset:12424\n"
      "v_mfma_f32_16x16x32_bf16 v[136:139], a[80:83], v[36:39], v[4:7]\n"
      "v_mfma_f32_16x16x32_bf16 v[136:139], a[84:87], v[40:43], v[136:139]\n"
      "v_mfma_f32_16x16x32_bf16 v[136:139], a[88:91], v[44:47], v[136:139]\n"
      "v_mfma_f32_16x16x32_bf16 v[136:139], a[92:95], v[60:63], v[136:139]\n"
      "v_cvt_pk_bf16_f32 v208, v132, v133\n"
      "v_mfma_f32_16x16x32_bf16 v[136:139], a[96:99], v[64:67], v[136:139]\n"
      "v_cvt_pk_bf16_f32 v209, v134, v135\n"
      "v_mfma_f32_16x16x32_bf16 v[136:139], a[100:103], v[68:71], v[136:139]\n"
      "s_waitcnt lgkmcnt(3)\n"
      "v_mfma_f32_16x16x32_bf16 v[136:139], a[104:107], v[84:87], v[136:139]\n"
      "global_store_dwordx4 v247, v[216:219], s[54:55] offset:0 sc1\n"
      "v_mfma_f32_16x16x32_bf16 v[136:139], a[108:111], v[88:91], v[136:139]\n"
      "s_add_u32 s52, s42, s34\n"
      "s_addc_u32 s53, s43, 0\n"
      "v_mfma_f32_16x16x32_bf16 v[136:139], a[112:115], v[92:95], v[136:139]\n"
      "s_add_u32 m0, s17, 0x6000\n"
      "v_mfma_f32_16x16x32_bf16 v[136:139], a[116:119], v[108:111], v[136:139]\n"
      "ds_write_b64 v244, v[208:209] offset:24576\n"
      "v_mfma_f32_16x16x32_bf16 v[136:139], a[120:123], v[112:115], v[136:139]\n"
      "global_load_lds_dwordx4 v236, s[52:53] offset:0 nt\n"
      "v_mfma_f32_16x16x32_bf16 v[136:139], a[124:127], v[116:119], v[136:139]\n"
      "v_mfma_f32_16x16x32_bf16 v[132:135], a[128:131], v[36:39], v[8:11]\n"
      "v_mfma_f32_16x16x32_bf16 v[132:135], a[132:135], v[40:43], v[132:135]\n"
      "v_mfma_f32_16x16x32_bf16 v[132:135], a[136:139], v[44:47], v[132:135]\n"
      "v_mfma_f32_16x16x32_bf16 v[132:135], a[140:143], v[60:63], v[132:135]\n"
      "v_cvt_pk_bf16_f32 v210, v136, v137\n"
      "v_mfma_f32_16x16x32_bf16 v[132:135], a[144:147], v[64:67], v[132:135]\n"
      "v_cvt_pk_bf16_f32 v211, v138, v139\n"
      "v_mfma_f32_16x16x32_bf16 v[132:135], a[148:151], v[68:71], v[132:135]\n"
      "v_mfma_f32_16x16x32_bf16 v[132:135], a[152:155], v[84:87], v[132:135]\n"
      "s_waitcnt lgkmcnt(3)\n"
      "v_mfma_f32_16x16x32_bf16 v[132:135], a[156:159], v[88:91], v[132:135]\n"
      "global_store_dwordx4 v252, v[220:223], s[54:55] offset:0 sc1\n"
      "v_mfma_f32_16x16x32_bf16 v[132:135], a[160:163], v[92:95], v[132:135]\n"
      "s_add_u32 m0, s17, 0x7f00\n"
      "v_mfma_f32_16x16x32_bf16 v[132:135], a[164:167], v[108:111], v[132:135]\n"
      "ds_write_b64 v244, v[210:211] offset:24584\n"
      "v_mfma_f32_16x16x32_bf16 v[132:135], a[168:171], v[112:115], v[132:135]\n"
      "global_load_lds_dwordx4 v236, s[52:53] offset:256 nt\n"
      "v_mfma_f32_16x16x32_bf16 v[132:135], a[172:175], v[116:119], v[132:135]\n"
      "v_mfma_f32_16x16x32_bf16 v[136:139], a[176:179], v[36:39], v[12:15]\n"
      "v_mfma_f32_16x16x32_bf16 v[136:139], a[180:183], v[40:43], v[136:139]\n"
      "v_mfma_f32_16x16x32_bf16 v[136:139], a[184:187], v[44:47], v[136:139]\n"
      "v_mfma_f32_16x16x32_bf16 v[136:139], a[188:191], v[60:63], v[136:139]\n"
      "v_cvt_pk_bf16_f32 v212, v132, v133\n"
      "v_mfma_f32_16x16x32_bf16 v[136:139], a[192:195], v[64:67], v[136:139]\n"
      "v_cvt_pk_bf16_f32 v213, v134, v135\n"
      "v_mfma_f32_16x16x32_bf16 v[136:139], a[196:199], v[68:71], v[136:139]\n"
      "v_mfma_f32_16x16x32_bf16 v[136:139], a[200:203], v[84:87], v[136:139]\n"
      "v_mfma_f32_16x16x32_bf16 v[136:139], a[204:207], v[88:91], v[136:139]\n"
      "s_waitcnt lgkmcnt(3)\n"
      "v_mfma_f32_16x16x32_bf16 v[136:139], a[208:211], v[92:95], v[136:139]\n"
      "global_store_dwordx4 v253, v[224:227], s[54:55] offset:0 sc1\n"
      "v_mfma_f32_16x16x32_bf16 v[136:139], a[212:215], v[108:111], v[136:139]\n"
      "ds_write_b64 v245, v[212:213] offset:24576\n"
      "v_mfma_f32_16x16x32_bf16 v[136:139], a[216:219], v[112:115], v[136:139]\n"
      "s_add_u32 m0, s17, 0x9e00\n"
      "v_mfma_f32_16x16x32_bf16 v[136:139], a[220:223], v[116:119], v[136:139]\n"
      "global_load_lds_dwordx4 v236, s[52:53] offset:512 nt\n"
      "v_mfma_f32_16x16x32_bf16 v[132:135], a[224:227], v[36:39], v[16:19]\n"
      "v_mfma_f32_16x16x32_bf16 v[132:135], a[228:231], v[40:43], v[132:135]\n"
      "v_mfma_f32_16x16x32_bf16 v[132:135], a[232:235], v[44:47], v[132:135]\n"
      "v_mfma_f32_16x16x32_bf16 v[132:135], a[236:239], v[60:63], v[132:135]\n"
      "v_cvt_pk_bf16_f32 v214, v136, v137\n"
      "v_mfma_f32_16x16x32_bf16 v[132:135], a[0:3], v[64:67], v[132:135]\n"
      "v_cvt_pk_bf16_f32 v215, v138, v139\n"
      "v_mfma_f32_16x16x32_bf16 v[132:135], a[4:7], v[68:71], v[132:135]\n"
      "v_mfma_f32_16x16x32_bf16 v[132:135], a[8:11], v[84:87], v[132:135]\n"
      "v_mfma_f32_16x16x32_bf16 v[132:135], a[12:15], v[88:91], v[132:135]\n"
      "v_mfma_f32_16x16x32_bf16 v[132:135], a[16:19], v[92:95], v[132:135]\n"
      "v_mfma_f32_16x16x32_bf16 v[132:135], a[20:23], v[108:111], v[132:135]\n"
      "ds_write_b64 v245, v[214:215] offset:24584\n"
      "v_mfma_f32_16x16x32_bf16 v[132:135], v[140:143], v[112:115], v[132:135]\n"
      "v_mfma_f32_16x16x32_bf16 v[132:135], v[156:159], v[116:119], v[132:135]\n"
      "s_waitcnt lgkmcnt(4)\n"
      "s_barrier\n"
      "v_mfma_f32_16x16x32_bf16 v[136:139], v[160:163], v[36:39], v[20:23]\n"
      "ds_read_b128 v[36:39], v232 offset:256\n"
      "v_mfma_f32_16x16x32_bf16 v[136:139], v[164:167], v[40:43], v[136:139]\n"
      "ds_read_b128 v[40:43], v233 offset:256\n"
      "v_mfma_f32_16x16x32_bf16 v[136:139], v[180:183], v[44:47], v[136:139]\n"
      "ds_read_b128 v[44:47], v234 offset:256\n"
      "v_mfma_f32_16x16x32_bf16 v[136:139], v[184:187], v[60:63], v[136:139]\n"
      "ds_read_b128 v[60:63], v235 offset:256\n"
      "v_cvt_pk_bf16_f32 v208, v132, v133\n"
      "v_mfma_f32_16x16x32_bf16 v[136:139], v[188:191], v[64:67], v[136:139]\n"
      "ds_read_b128 v[64:67], v232 offset:8448\n"
      "v_cvt_pk_bf16_f32 v209, v134, v135\n"
      "v_mfma_f32_16x16x32_bf16 v[136:139], v[204:207], v[68:71], v[136:139]\n"
      "ds_read_b128 v[68:71], v233 offset:8448\n"
      "v_mfma_f32_16x16x32_bf16 v[136:139], a[24:27], v[84:87], v[136:139]\n"
      "ds_read_b128 v[84:87], v234 offset:8448\n"
      "v_mfma_f32_16x16x32_bf16 v[136:139], a[28:31], v[88:91], v[136:139]\n"
      "ds_read_b128 v[88:91], v235 offset:8448\n"
      "v_mfma_f32_16x16x32_bf16 v[136:139], a[32:35], v[92:95], v[136:139]\n"
      "ds_read_b128 v[92:95], v232 offset:16640\n"
      "v_mfma_f32_16x16x32_bf16 v[136:139], a[36:39], v[108:111], v[136:139]\n"
      "ds_read_b128 v[108:111], v233 offset:16640\n"
      "v_mfma_f32_16x16x32_bf16 v[136:139], a[40:43], v[112:115], v[136:139]\n"
      "ds_read_b128 v[112:115], v234 offset:16640\n"
      "v_mfma_f32_16x16x32_bf16 v[136:139], a[44:47], v[116:119], v[136:139]\n"
      "ds_read_b128 v[116:119], v235 offset:16640\n"
      "s_waitcnt lgkmcnt(8)\n"
      "v_mfma_f32_16x16x32_bf16 v[132:135], a[240:243], v[36:39], v[0:3]\n"
      "ds_write_b64 v244, v[208:209] offset:24704\n"
      "v_mfma_f32_16x16x32_bf16 v[132:135], a[244:247], v[40:43], v[132:135]\n"
      "v_mfma_f32_16x16x32_bf16 v[132:135], a[248:251], v[44:47], v[132:135]\n"
      "v_mfma_f32_16x16x32_bf16 v[132:135], a[252:255], v[60:63], v[132:135]\n"
      "v_cvt_pk_bf16_f32 v210, v136, v137\n"
      "s_waitcnt lgkmcnt(5)\n"
      "v_mfma_f32_16x16x32_bf16 v[132:135], a[48:51], v[64:67], v[132:135]\n"
      "v_cvt_pk_bf16_f32 v211, v138, v139\n"
      "v_mfma_f32_16x16x32_bf16 v[132:135], a[52:55], v[68:71], v[132:135]\n"
      "v_mfma_f32_16x16x32_bf16 v[132:135], a[56:59], v[84:87], v[132:135]\n"
      "ds_read_b128 v[216:219], v246 offset:12288\n"
      "v_mfma_f32_16x16x32_bf16 v[132:135], a[60:63], v[88:91], v[132:135]\n"
      "ds_read_b128 v[220:223], v246 offset:13312\n"
      "s_waitcnt lgkmcnt(3)\n"
      "v_mfma_f32_16x16x32_bf16 v[132:135], a[64:67], v[92:95], v[132:135]\n"
      "ds_read_b128 v[224:227], v246 offset:14336\n"
      "v_mfma_f32_16x16x32_bf16 v[132:135], a[68:71], v[108:111], v[132:135]\n"
      "ds_write_b64 v244, v[210:211] offset:24712\n"
      "v_mfma_f32_16x16x32_bf16 v[132:135], a[72:75], v[112:115], v[132:135]\n"
      "v_mfma_f32_16x16x32_bf16 v[132:135], a[76:79], v[116:119], v[132:135]\n"
      "v_mfma_f32_16x16x32_bf16 v[136:139], a[80:83], v[36:39], v[4:7]\n"
      "v_mfma_f32_16x16x32_bf16 v[136:139], a[84:87], v[40:43], v[136:139]\n"
      "v_mfma_f32_16x16x32_bf16 v[136:139], a[88:91], v[44:47], v[136:139]\n"
      "v_mfma_f32_16x16x32_bf16 v[136:139], a[92:95], v[60:63], v[136:139]\n"
      "v_cvt_pk_bf16_f32 v208, v132, v133\n"
      "v_mfma_f32_16x16x32_bf16 v[136:139], a[96:99], v[64:67], v[136:139]\n"
      "v_cvt_pk_bf16_f32 v209, v134, v135\n"
      "v_mfma_f32_16x16x32_bf16 v[136:139], a[100:103], v[68:71], v[136:139]\n"
      "s_waitcnt lgkmcnt(3)\n"
      "v_mfma_f32_16x16x32_bf16 v[136:139], a[104:107], v[84:87], v[136:139]\n"
      "global_store_dwordx4 v247, v[216:219], s[54:55] offset:768 sc1\n"
      "v_mfma_f32_16x16x32_bf16 v[136:139], a[108:111], v[88:91], v[136:139]\n"
      "s_add_u32 m0, s17, 0x7000\n"
      "v_mfma_f32_16x16x32_bf16 v[136:139], a[112:115], v[92:95], v[136:139]\n"
      "global_load_lds_dwordx4 v237, s[52:53] offset:0 nt\n"
      "v_mfma_f32_16x16x32_bf16 v[136:139], a[116:119], v[108:111], v[136:139]\n"
      "ds_write_b64 v244, v[208:209] offset:0\n"
      "v_mfma_f32_16x16x32_bf16 v[136:139], a[120:123], v[112:115], v[136:139]\n"
      "v_mfma_f32_16x16x32_bf16 v[136:139], a[124:127], v[116:119], v[136:139]\n"
      "v_mfma_f32_16x16x32_bf16 v[132:135], a[128:131], v[36:39], v[8:11]\n"
      "v_mfma_f32_16x16x32_bf16 v[132:135], a[132:135], v[40:43], v[132:135]\n"
      "v_mfma_f32_16x16x32_bf16 v[132:135], a[136:139], v[44:47], v[132:135]\n"
      "v_mfma_f32_16x16x32_bf16 v[132:135], a[140:143], v[60:63], v[132:135]\n"
      "v_cvt_pk_bf16_f32 v210, v136, v137\n"
      "v_mfma_f32_16x16x32_bf16 v[132:135], a[144:147], v[64:67], v[132:135]\n"
      "v_cvt_pk_bf16_f32 v211, v138, v139\n"
      "v_mfma_f32_16x16x32_bf16 v[132:135], a[148:151], v[68:71], v[132:135]\n"
      "v_mfma_f32_16x16x32_bf16 v[132:135], a[152:155], v[84:87], v[132:135]\n"
      "s_waitcnt lgkmcnt(3)\n"
      "v_mfma_f32_16x16x32_bf16 v[132:135], a[156:159], v[88:91], v[132:135]\n"
      "global_store_dwordx4 v252, v[220:223], s[54:55] offset:768 sc1\n"
      "v_mfma_f32_16x16x32_bf16 v[132:135], a[160:163], v[92:95], v[132:135]\n"
      "s_add_u32 m0, s17, 0x8f00\n"
      "v_mfma_f32_16x16x32_bf16 v[132:135], a[164:167], v[108:111], v[132:135]\n"
      "ds_write_b64 v244, v[210:211] offset:8\n"
      "v_mfma_f32_16x16x32_bf16 v[132:135], a[168:171], v[112:115], v[132:135]\n"
      "global_load_lds_dwordx4 v237, s[52:53] offset:256 nt\n"
      "v_mfma_f32_16x16x32_bf16 v[132:135], a[172:175], v[116:119], v[132:135]\n"
      "v_mfma_f32_16x16x32_bf16 v[136:139], a[176:179], v[36:39], v[12:15]\n"
      "v_mfma_f32_16x16x32_bf16 v[136:139], a[180:183], v[40:43], v[136:139]\n"
      "v_mfma_f32_16x16x32_bf16 v[136:139], a[184:187], v[44:47], v[136:139]\n"
      "v_mfma_f32_16x16x32_bf16 v[136:139], a[188:191], v[60:63], v[136:139]\n"
      "v_cvt_pk_bf16_f32 v212, v132, v133\n"
      "v_mfma_f32_16x16x32_bf16 v[136:139], a[192:195], v[64:67], v[136:139]\n"
      "v_cvt_pk_bf16_f32 v213, v134, v135\n"
      "v_mfma_f32_16x16x32_bf16 v[136:139], a[196:199], v[68:71], v[136:139]\n"
      "v_mfma_f32_16x16x32_bf16 v[136:139], a[200:203], v[84:87], v[136:139]\n"
      "v_mfma_f32_16x16x32_bf16 v[136:139], a[204:207], v[88:91], v[136:139]\n"
      "s_waitcnt lgkmcnt(3)\n"
      "v_mfma_f32_16x16x32_bf16 v[136:139], a[208:211], v[92:95], v[136:139]\n"
      "global_store_dwordx4 v253, v[224:227], s[54:55] offset:768 sc1\n"
      "v_mfma_f32_16x16x32_bf16 v[136:139], a[212:215], v[108:111], v[136:139]\n"
      "ds_write_b64 v245, v[212:213] offset:0\n"
      "v_mfma_f32_16x16x32_bf16 v[136:139], a[216:219], v[112:115], v[136:139]\n"
      "s_add_u32 m0, s17, 0xae00\n"
      "v_mfma_f32_16x16x32_bf16 v[136:139], a[220:223], v[116:119], v[136:139]\n"
      "global_load_lds_dwordx4 v237, s[52:53] offset:512 nt\n"
      "v_mfma_f32_16x16x32_bf16 v[132:135], a[224:227], v[36:39], v[16:19]\n"
      "v_mfma_f32_16x16x32_bf16 v[132:135], a[228:231], v[40:43], v[132:135]\n"
      "v_mfma_f32_16x16x32_bf16 v[132:135], a[232:235], v[44:47], v[132:135]\n"
      "v_mfma_f32_16x16x32_bf16 v[132:135], a[236:239], v[60:63], v[132:135]\n"
      "v_cvt_pk_bf16_f32 v214, v136, v137\n"
      "v_mfma_f32_16x16x32_bf16 v[132:135], a[0:3], v[64:67], v[132:135]\n"
      "v_cvt_pk_bf16_f32 v215, v138, v139\n"
      "v_mfma_f32_16x16x32_bf16 v[132:135], a[4:7], v[68:71], v[132:135]\n"
      "v_mfma_f32_16x16x32_bf16 v[132:135], a[8:11], v[84:87], v[132:135]\n"
      "v_mfma_f32_16x16x32_bf16 v[132:135], a[12:15], v[88:91], v[132:135]\n"
      "v_mfma_f32_16x16x32_bf16 v[132:135], a[16:19], v[92:95], v[132:135]\n"
      "v_mfma_f32_16x16x32_bf16 v[132:135], a[20:23], v[108:111], v[132:135]\n"
      "ds_write_b64 v245, v[214:215] offset:8\n"
      "v_mfma_f32_16x16x32_bf16 v[132:135], v[140:143], v[112:115], v[132:135]\n"
      "s_lshl_b32 s79, s90, 1\n"
      "v_mfma_f32_16x16x32_bf16 v[132:135], v[156:159], v[116:119], v[132:135]\n"
      "s_waitcnt vmcnt(24) lgkmcnt(4)\n"
      "s_barrier\n"
      "v_mad_i32_i24 v252, v251, s79, v252\n"
      "v_mfma_f32_16x16x32_bf16 v[136:139], v[160:163], v[36:39], v[20:23]\n"
      "ds_read_b128 v[36:39], v232 offset:24576\n"
      "s_mul_i32 s79, s79, s100\n"
      "v_mfma_f32_16x16x32_bf16 v[136:139], v[164:167], v[40:43], v[136:139]\n"
      "ds_read_b128 v[40:43], v233 offset:24576\n"
      "v_add_u32 v247, s79, v247\n"
      "v_mfma_f32_16x16x32_bf16 v[136:139], v[180:183], v[44:47], v[136:139]\n"
      "ds_read_b128 v[44:47], v234 offset:24576\n"
      "s_mul_i32 s79, s79, s101\n"
      "v_mfma_f32_16x16x32_bf16 v[136:139], v[184:187], v[60:63], v[136:139]\n"
      "ds_read_b128 v[60:63], v235 offset:24576\n"
      "v_cvt_pk_bf16_f32 v208, v132, v133\n"
      "v_mfma_f32_16x16x32_bf16 v[136:139], v[188:191], v[64:67], v[136:139]\n"
      "ds_read_b128 v[64:67], v232 offset:32768\n"
      "v_cvt_pk_bf16_f32 v209, v134, v135\n"
      "v_mfma_f32_16x16x32_bf16 v[136:139], v[204:207], v[68:71], v[136:139]\n"
      "ds_read_b128 v[68:71], v233 offset:32768\n"
      "v_add_u32 v253, s79, v253\n"
      "v_mfma_f32_16x16x32_bf16 v[136:139], a[24:27], v[84:87], v[136:139]\n"
      "ds_read_b128 v[84:87], v234 offset:32768\n"
      "v_mfma_f32_16x16x32_bf16 v[136:139], a[28:31], v[88:91], v[136:139]\n"
      "ds_read_b128 v[88:91], v235 offset:32768\n"
      "v_mfma_f32_16x16x32_bf16 v[136:139], a[32:35], v[92:95], v[136:139]\n"
      "ds_read_b128 v[92:95], v232 offset:40960\n"
      "v_mfma_f32_16x16x32_bf16 v[136:139], a[36:39], v[108:111], v[136:139]\n"
      "ds_read_b128 v[108:111], v233 offset:40960\n"
      "v_mfma_f32_16x16x32_bf16 v[136:139], a[40:43], v[112:115], v[136:139]\n"
      "ds_read_b128 v[112:115], v234 offset:40960\n"
      "v_mfma_f32_16x16x32_bf16 v[136:139], a[44:47], v[116:119], v[136:139]\n"
      "ds_read_b128 v[116:119], v235 offset:40960\n"
      "s_waitcnt lgkmcnt(8)\n"
      "v_mfma_f32_16x16x32_bf16 v[132:135], a[240:243], v[36:39], v[0:3]\n"
      "ds_write_b64 v244, v[208:209] offset:128\n"
      "v_mfma_f32_16x16x32_bf16 v[132:135], a[244:247], v[40:43], v[132:135]\n"
      "s_add_u32 s54, s30, s33\n"
      "s_addc_u32 s55, s31, 0\n"
      "v_mfma_f32_16x16x32_bf16 v[132:135], a[248:251], v[44:47], v[132:135]\n"
      "v_mfma_f32_16x16x32_bf16 v[132:135], a[252:255], v[60:63], v[132:135]\n"
      "v_cvt_pk_bf16_f32 v210, v136, v137\n"
      "s_waitcnt lgkmcnt(5)\n"
      "v_mfma_f32_16x16x32_bf16 v[132:135], a[48:51], v[64:67], v[132:135]\n"
      "v_cvt_pk_bf16_f32 v211, v138, v139\n"
      "v_mfma_f32_16x16x32_bf16 v[132:135], a[52:55], v[68:71], v[132:135]\n"
      "s_mul_i32 s78, s92, s19\n"
      "v_mfma_f32_16x16x32_bf16 v[132:135], a[56:59], v[84:87], v[132:135]\n"
      "ds_read_b128 v[216:219], v246 offset:24576\n"
      "v_add_u32 v236, s78, v236\n"
      "v_mfma_f32_16x16x32_bf16 v[132:135], a[60:63], v[88:91], v[132:135]\n"
      "ds_read_b128 v[220:223], v246 offset:25600\n"
      "s_waitcnt lgkmcnt(3)\n"
      "v_mfma_f32_16x16x32_bf16 v[132:135], a[64:67], v[92:95], v[132:135]\n"
      "ds_read_b128 v[224:227], v246 offset:26624\n"
      "v_mfma_f32_16x16x32_bf16 v[132:135], a[68:71], v[108:111], v[132:135]\n"
      "ds_write_b64 v244, v[210:211] offset:136\n"
      "v_mfma_f32_16x16x32_bf16 v[132:135], a[72:75], v[112:115], v[132:135]\n"
      "s_mul_i32 s78, s78, s99\n"
      "v_mfma_f32_16x16x32_bf16 v[132:135], a[76:79], v[116:119], v[132:135]\n"
      "v_add_u32 v237, s78, v237\n"
      "v_mfma_f32_16x16x32_bf16 v[136:139], a[80:83], v[36:39], v[4:7]\n"
      "v_mfma_f32_16x16x32_bf16 v[136:139], a[84:87], v[40:43], v[136:139]\n"
      "v_mfma_f32_16x16x32_bf16 v[136:139], a[88:91], v[44:47], v[136:139]\n"
      "v_mfma_f32_16x16x32_bf16 v[136:139], a[92:95], v[60:63], v[136:139]\n"
      "v_cvt_pk_bf16_f32 v208, v132, v133\n"
      "v_mfma_f32_16x16x32_bf16 v[136:139], a[96:99], v[64:67], v[136:139]\n"
      "v_cvt_pk_bf16_f32 v209, v134, v135\n"
      "v_mfma_f32_16x16x32_bf16 v[136:139], a[100:103], v[68:71], v[136:139]\n"
      "s_waitcnt lgkmcnt(3)\n"
      "v_mfma_f32_16x16x32_bf16 v[136:139], a[104:107], v[84:87], v[136:139]\n"
      "global_store_dwordx4 v247, v[216:219], s[54:55] offset:0 sc1\n"
      "v_mfma_f32_16x16x32_bf16 v[136:139], a[108:111], v[88:91], v[136:139]\n"
      "s_add_u32 s52, s20, s35\n"
      "s_addc_u32 s53, s21, 0\n"
      "v_mfma_f32_16x16x32_bf16 v[136:139], a[112:115], v[92:95], v[136:139]\n"
      "s_add_u32 m0, s17, 0xc000\n"
      "v_mfma_f32_16x16x32_bf16 v[136:139], a[116:119], v[108:111], v[136:139]\n"
      "ds_write_b64 v244, v[208:209] offset:12288\n"
      "v_mfma_f32_16x16x32_bf16 v[136:139], a[120:123], v[112:115], v[136:139]\n"
      "global_load_lds_dwordx4 v236, s[52:53] offset:0 nt\n"
      "v_mfma_f32_16x16x32_bf16 v[136:139], a[124:127], v[116:119], v[136:139]\n"
      "v_mfma_f32_16x16x32_bf16 v[132:135], a[128:131], v[36:39], v[8:11]\n"
      "v_min_u32 v28, 0x370, v239\n"
      "v_mfma_f32_16x16x32_bf16 v[132:135], a[132:135], v[40:43], v[132:135]\n"
      "v_mfma_f32_16x16x32_bf16 v[132:135], a[136:139], v[44:47], v[132:135]\n"
      "v_mfma_f32_16x16x32_bf16 v[132:135], a[140:143], v[60:63], v[132:135]\n"
      "v_cvt_pk_bf16_f32 v210, v136, v137\n"
      "v_mfma_f32_16x16x32_bf16 v[132:135], a[144:147], v[64:67], v[132:135]\n"
      "v_cvt_pk_bf16_f32 v211, v138, v139\n"
      "v_mfma_f32_16x16x32_bf16 v[132:135], a[148:151], v[68:71], v[132:135]\n"
      "v_add_u32 v28, 0x2480, v28\n"
      "v_mfma_f32_16x16x32_bf16 v[132:135], a[152:155], v[84:87], v[132:135]\n"
      "s_waitcnt lgkmcnt(3)\n"
      "global_load_dwordx4 v[24:27], v28, s[28:29] sc1\n"
      "v_mfma_f32_16x16x32_bf16 v[132:135], a[156:159], v[88:91], v[132:135]\n"
      "global_store_dwordx4 v252, v[220:223], s[54:55] offset:0 sc1\n"
      "v_mfma_f32_16x16x32_bf16 v[132:135], a[160:163], v[92:95], v[132:135]\n"
      "s_add_u32 m0, s17, 0xdf00\n"
      "v_mfma_f32_16x16x32_bf16 v[132:135], a[164:167], v[108:111], v[132:135]\n"
      "ds_write_b64 v244, v[210:211] offset:12296\n"
      "v_mfma_f32_16x16x32_bf16 v[132:135], a[168:171], v[112:115], v[132:135]\n"
      "global_load_lds_dwordx4 v236, s[52:53] offset:256 nt\n"
      "v_mfma_f32_16x16x32_bf16 v[132:135], a[172:175], v[116:119], v[132:135]\n"
      "v_mfma_f32_16x16x32_bf16 v[136:139], a[176:179], v[36:39], v[12:15]\n"
      "v_mfma_f32_16x16x32_bf16 v[136:139], a[180:183], v[40:43], v[136:139]\n"
      "v_mfma_f32_16x16x32_bf16 v[136:139], a[184:187], v[44:47], v[136:139]\n"
      "v_mfma_f32_16x16x32_bf16 v[136:139], a[188:191], v[60:63], v[136:139]\n"
      "v_cvt_pk_bf16_f32 v212, v132, v133\n"
      "v_mfma_f32_16x16x32_bf16 v[136:139], a[192:195], v[64:67], v[136:139]\n"
      "v_cvt_pk_bf16_f32 v213, v134, v135\n"
      "v_mfma_f32_16x16x32_bf16 v[136:139], a[196:199], v[68:71], v[136:139]\n"
      "v_mfma_f32_16x16x32_bf16 v[136:139], a[200:203], v[84:87], v[136:139]\n"
      "v_mfma_f32_16x16x32_bf16 v[136:139], a[204:207], v[88:91], v[136:139]\n"
      "s_waitcnt lgkmcnt(3)\n"
      "v_mfma_f32_16x16x32_bf16 v[136:139], a[208:211], v[92:95], v[136:139]\n"
      "global_store_dwordx4 v253, v[224:227], s[54:55] offset:0 sc1\n"
      "v_mfma_f32_16x16x32_bf16 v[136:139], a[212:215], v[108:111], v[136:139]\n"
      "ds_write_b64 v245, v[212:213] offset:12288\n"
      "v_mfma_f32_16x16x32_bf16 v[136:139], a[216:219], v[112:115], v[136:139]\n"
      "s_add_u32 m0, s17, 0xfe00\n"
      "v_mfma_f32_16x16x32_bf16 v[136:139], a[220:223], v[116:119], v[136:139]\n"
      "global_load_lds_dwordx4 v236, s[52:53] offset:512 nt\n"
      "v_mfma_f32_16x16x32_bf16 v[132:135], a[224:227], v[36:39], v[16:19]\n"
      "v_mfma_f32_16x16x32_bf16 v[132:135], a[228:231], v[40:43], v[132:135]\n"
      "v_mfma_f32_16x16x32_bf16 v[132:135], a[232:235], v[44:47], v[132:135]\n"
      "v_mfma_f32_16x16x32_bf16 v[132:135], a[236:239], v[60:63], v[132:135]\n"
      "v_cvt_pk_bf16_f32 v214, v136, v137\n"
      "v_mfma_f32_16x16x32_bf16 v[132:135], a[0:3], v[64:67], v[132:135]\n"
      "v_cvt_pk_bf16_f32 v215, v138, v139\n"
      "v_mfma_f32_16x16x32_bf16 v[132:135], a[4:7], v[68:71], v[132:135]\n"
      "v_mfma_f32_16x16x32_bf16 v[132:135], a[8:11], v[84:87], v[132:135]\n"
      "v_mfma_f32_16x16x32_bf16 v[132:135], a[12:15], v[88:91], v[132:135]\n"
      "v_mfma_f32_16x16x32_bf16 v[132:135], a[16:19], v[92:95], v[132:135]\n"
      "v_mfma_f32_16x16x32_bf16 v[132:135], a[20:23], v[108:111], v[132:135]\n"
      "ds_write_b64 v245, v[214:215] offset:12296\n"
      "v_mfma_f32_16x16x32_bf16 v[132:135], v[140:143], v[112:115], v[132:135]\n"
      "v_mfma_f32_16x16x32_bf16 v[132:135], v[156:159], v[116:119], v[132:135]\n"
      "s_waitcnt lgkmcnt(4)\n"
      "s_barrier\n"
      "v_mfma_f32_16x16x32_bf16 v[136:139], v[160:163], v[36:39], v[20:23]\n"
      "ds_read_b128 v[36:39], v232 offset:24832\n"
      "v_mfma_f32_16x16x32_bf16 v[136:139], v[164:167], v[40:43], v[136:139]\n"
      "ds_read_b128 v[40:43], v233 offset:24832\n"
      "v_mfma_f32_16x16x32_bf16 v[136:139], v[180:183], v[44:47], v[136:139]\n"
      "ds_read_b128 v[44:47], v234 offset:24832\n"
      "v_mfma_f32_16x16x32_bf16 v[136:139], v[184:187], v[60:63], v[136:139]\n"
      "ds_read_b128 v[60:63], v235 offset:24832\n"
      "v_cvt_pk_bf16_f32 v208, v132, v133\n"
      "v_mfma_f32_16x16x32_bf16 v[136:139], v[188:191], v[64:67], v[136:139]\n"
      "ds_read_b128 v[64:67], v232 offset:33024\n"
      "v_cvt_pk_bf16_f32 v209, v134, v135\n"
      "v_mfma_f32_16x16x32_bf16 v[136:139], v[204:207], v[68:71], v[136:139]\n"
      "ds_read_b128 v[68:71], v233 offset:33024\n"
      "v_mfma_f32_16x16x32_bf16 v[136:139], a[24:27], v[84:87], v[136:139]\n"
      "ds_read_b128 v[84:87], v234 offset:33024\n"
      "v_mfma_f32_16x16x32_bf16 v[136:139], a[28:31], v[88:91], v[136:139]\n"
      "ds_read_b128 v[88:91], v235 offset:33024\n"
      "v_mfma_f32_16x16x32_bf16 v[136:139], a[32:35], v[92:95], v[136:139]\n"
      "ds_read_b128 v[92:95], v232 offset:41216\n"
      "v_mfma_f32_16x16x32_bf16 v[136:139], a[36:39], v[108:111], v[136:139]\n"
      "ds_read_b128 v[108:111], v233 offset:41216\n"
      "ds_write_b64 v244, v[208:209] offset:12416\n"
      "v_mfma_f32_16x16x32_bf16 v[136:139], a[40:43], v[112:115], v[136:139]\n"
      "ds_read_b128 v[112:115], v234 offset:41216\n"
      "v_mfma_f32_16x16x32_bf16 v[136:139], a[44:47], v[116:119], v[136:139]\n"
      "ds_read_b128 v[116:119], v235 offset:41216\n"
      "s_waitcnt lgkmcnt(9)\n"
      "v_mfma_f32_16x16x32_bf16 v[132:135], a[240:243], v[36:39], v[0:3]\n"
      "v_add_u32 v232, 0xffff4000, v232\n"
      "v_mfma_f32_16x16x32_bf16 v[132:135], a[244:247], v[40:43], v[132:135]\n"
      "v_add_u32 v233, 0xffff4000, v233\n"
      "v_mfma_f32_16x16x32_bf16 v[132:135], a[248:251], v[44:47], v[132:135]\n"
      "v_add_u32 v234, 0xffff4000, v234\n"
      "v_mfma_f32_16x16x32_bf16 v[132:135], a[252:255], v[60:63], v[132:135]\n"
      "v_add_u32 v235, 0xffff4000, v235\n"
      "s_waitcnt lgkmcnt(5)\n"
      "v_mfma_f32_16x16x32_bf16 v[132:135], a[48:51], v[64:67], v[132:135]\n"
      "v_cvt_pk_bf16_f32 v210, v136, v137\n"
      "v_mfma_f32_16x16x32_bf16 v[132:135], a[52:55], v[68:71], v[132:135]\n"
      "v_cvt_pk_bf16_f32 v211, v138, v139\n"
      "v_mfma_f32_16x16x32_bf16 v[132:135], a[56:59], v[84:87], v[132:135]\n"
      "ds_read_b128 v[216:219], v246 offset:0\n"
      "v_mfma_f32_16x16x32_bf16 v[132:135], a[60:63], v[88:91], v[132:135]\n"
      "ds_read_b128 v[220:223], v246 offset:1024\n"
      "s_waitcnt lgkmcnt(2)\n"
      "v_mfma_f32_16x16x32_bf16 v[132:135], a[64:67], v[92:95], v[132:135]\n"
      "ds_read_b128 v[224:227], v246 offset:2048\n"
      "v_mfma_f32_16x16x32_bf16 v[132:135], a[68:71], v[108:111], v[132:135]\n"
      "v_lshlrev_b32 v0, 16, v48\n"
      "v_mfma_f32_16x16x32_bf16 v[132:135], a[72:75], v[112:115], v[132:135]\n"
      "ds_write_b64 v244, v[210:211] offset:12424\n"
      "v_mfma_f32_16x16x32_bf16 v[132:135], a[76:79], v[116:119], v[132:135]\n"
      "v_and_b32 v1, 0xffff0000, v48\n"
      "v_mfma_f32_16x16x32_bf16 v[136:139], a[80:83], v[36:39], v[4:7]\n"
      "v_lshlrev_b32 v2, 16, v49\n"
      "v_mfma_f32_16x16x32_bf16 v[136:139], a[84:87], v[40:43], v[136:139]\n"
      "v_and_b32 v3, 0xffff0000, v49\n"
      "v_mfma_f32_16x16x32_bf16 v[136:139], a[88:91], v[44:47], v[136:139]\n"
      "v_mfma_f32_16x16x32_bf16 v[136:139], a[92:95], v[60:63], v[136:139]\n"
      "v_cvt_pk_bf16_f32 v208, v132, v133\n"
      "v_mfma_f32_16x16x32_bf16 v[136:139], a[96:99], v[64:67], v[136:139]\n"
      "v_cvt_pk_bf16_f32 v209, v134, v135\n"
      "v_mfma_f32_16x16x32_bf16 v[136:139], a[100:103], v[68:71], v[136:139]\n"
      "s_waitcnt lgkmcnt(3)\n"
      "v_mfma_f32_16x16x32_bf16 v[136:139], a[104:107], v[84:87], v[136:139]\n"
      "global_store_dwordx4 v247, v[216:219], s[54:55] offset:768 sc1\n"
      "v_mfma_f32_16x16x32_bf16 v[136:139], a[108:111], v[88:91], v[136:139]\n"
      "s_add_u32 m0, s17, 0xd000\n"
      "v_mfma_f32_16x16x32_bf16 v[136:139], a[112:115], v[92:95], v[136:139]\n"
      "global_load_lds_dwordx4 v237, s[52:53] offset:0 nt\n"
      "v_mfma_f32_16x16x32_bf16 v[136:139], a[116:119], v[108:111], v[136:139]\n"
      "ds_write_b64 v244, v[208:209] offset:24576\n"
      "v_mfma_f32_16x16x32_bf16 v[136:139], a[120:123], v[112:115], v[136:139]\n"
      "v_lshlrev_b32 v4, 16, v50\n"
      "v_mfma_f32_16x16x32_bf16 v[136:139], a[124:127], v[116:119], v[136:139]\n"
      "v_and_b32 v5, 0xffff0000, v50\n"
      "v_mfma_f32_16x16x32_bf16 v[132:135], a[128:131], v[36:39], v[8:11]\n"
      "v_lshlrev_b32 v6, 16, v51\n"
      "v_mfma_f32_16x16x32_bf16 v[132:135], a[132:135], v[40:43], v[132:135]\n"
      "v_and_b32 v7, 0xffff0000, v51\n"
      "v_mfma_f32_16x16x32_bf16 v[132:135], a[136:139], v[44:47], v[132:135]\n"
      "v_mfma_f32_16x16x32_bf16 v[132:135], a[140:143], v[60:63], v[132:135]\n"
      "v_cvt_pk_bf16_f32 v210, v136, v137\n"
      "v_mfma_f32_16x16x32_bf16 v[132:135], a[144:147], v[64:67], v[132:135]\n"
      "v_cvt_pk_bf16_f32 v211, v138, v139\n"
      "v_mfma_f32_16x16x32_bf16 v[132:135], a[148:151], v[68:71], v[132:135]\n"
      "v_lshlrev_b32 v8, 16, v52\n"
      "v_mfma_f32_16x16x32_bf16 v[132:135], a[152:155], v[84:87], v[132:135]\n"
      "s_waitcnt lgkmcnt(3)\n"
      "v_mfma_f32_16x16x32_bf16 v[132:135], a[156:159], v[88:91], v[132:135]\n"
      "global_store_dwordx4 v252, v[220:223], s[54:55] offset:768 sc1\n"
      "v_mfma_f32_16x16x32_bf16 v[132:135], a[160:163], v[92:95], v[132:135]\n"
      "s_add_u32 m0, s17, 0xef00\n"
      "v_mfma_f32_16x16x32_bf16 v[132:135], a[164:167], v[108:111], v[132:135]\n"
      "ds_write_b64 v244, v[210:211] offset:24584\n"
      "v_mfma_f32_16x16x32_bf16 v[132:135], a[168:171], v[112:115], v[132:135]\n"
      "global_load_lds_dwordx4 v237, s[52:53] offset:256 nt\n"
      "v_mfma_f32_16x16x32_bf16 v[132:135], a[172:175], v[116:119], v[132:135]\n"
      "v_and_b32 v9, 0xffff0000, v52\n"
      "v_mfma_f32_16x16x32_bf16 v[136:139], a[176:179], v[36:39], v[12:15]\n"
      "v_lshlrev_b32 v10, 16, v53\n"
      "v_mfma_f32_16x16x32_bf16 v[136:139], a[180:183], v[40:43], v[136:139]\n"
      "v_and_b32 v11, 0xffff0000, v53\n"
      "v_mfma_f32_16x16x32_bf16 v[136:139], a[184:187], v[44:47], v[136:139]\n"
      "v_mfma_f32_16x16x32_bf16 v[136:139], a[188:191], v[60:63], v[136:139]\n"
      "v_cvt_pk_bf16_f32 v212, v132, v133\n"
      "v_mfma_f32_16x16x32_bf16 v[136:139], a[192:195], v[64:67], v[136:139]\n"
      "v_cvt_pk_bf16_f32 v213, v134, v135\n"
      "v_mfma_f32_16x16x32_bf16 v[136:139], a[196:199], v[68:71], v[136:139]\n"
      "v_lshlrev_b32 v12, 16, v54\n"
      "v_mfma_f32_16x16x32_bf16 v[136:139], a[200:203], v[84:87], v[136:139]\n"
      "v_and_b32 v13, 0xffff0000, v54\n"
      "v_mfma_f32_16x16x32_bf16 v[136:139], a[204:207], v[88:91], v[136:139]\n"
      "s_waitcnt lgkmcnt(3)\n"
      "v_mfma_f32_16x16x32_bf16 v[136:139], a[208:211], v[92:95], v[136:139]\n"
      "global_store_dwordx4 v253, v[224:227], s[54:55] offset:768 sc1\n"
      "v_mfma_f32_16x16x32_bf16 v[136:139], a[212:215], v[108:111], v[136:139]\n"
      "ds_write_b64 v245, v[212:213] offset:24576\n"
      "v_mfma_f32_16x16x32_bf16 v[136:139], a[216:219], v[112:115], v[136:139]\n"
      "s_add_u32 m0, s17, 0x10e00\n"
      "v_mfma_f32_16x16x32_bf16 v[136:139], a[220:223], v[116:119], v[136:139]\n"
      "global_load_lds_dwordx4 v237, s[52:53] offset:512 nt\n"
      "v_mfma_f32_16x16x32_bf16 v[132:135], a[224:227], v[36:39], v[16:19]\n"
      "v_lshlrev_b32 v14, 16, v55\n"
      "v_mfma_f32_16x16x32_bf16 v[132:135], a[228:231], v[40:43], v[132:135]\n"
      "v_and_b32 v15, 0xffff0000, v55\n"
      "v_mfma_f32_16x16x32_bf16 v[132:135], a[232:235], v[44:47], v[132:135]\n"
      "v_mfma_f32_16x16x32_bf16 v[132:135], a[236:239], v[60:63], v[132:135]\n"
      "v_cvt_pk_bf16_f32 v214, v136, v137\n"
      "v_mfma_f32_16x16x32_bf16 v[132:135], a[0:3], v[64:67], v[132:135]\n"
      "v_cvt_pk_bf16_f32 v215, v138, v139\n"
      "v_mfma_f32_16x16x32_bf16 v[132:135], a[4:7], v[68:71], v[132:135]\n"
      "v_lshlrev_b32 v16, 16, v56\n"
      "v_mfma_f32_16x16x32_bf16 v[132:135], a[8:11], v[84:87], v[132:135]\n"
      "v_and_b32 v17, 0xffff0000, v56\n"
      "v_mfma_f32_16x16x32_bf16 v[132:135], a[12:15], v[88:91], v[132:135]\n"
      "v_lshlrev_b32 v18, 16, v57\n"
      "v_mfma_f32_16x16x32_bf16 v[132:135], a[16:19], v[92:95], v[132:135]\n"
      "v_and_b32 v19, 0xffff0000, v57\n"
      "v_mfma_f32_16x16x32_bf16 v[132:135], a[20:23], v[108:111], v[132:135]\n"
      "ds_write_b64 v245, v[214:215] offset:24584\n"
      "v_mfma_f32_16x16x32_bf16 v[132:135], v[140:143], v[112:115], v[132:135]\n"
      "v_mfma_f32_16x16x32_bf16 v[132:135], v[156:159], v[116:119], v[132:135]\n"
      "s_waitcnt vmcnt(25) lgkmcnt(4)\n"
      "s_barrier\n"
      "v_mfma_f32_16x16x32_bf16 v[136:139], v[160:163], v[36:39], v[20:23]\n"
      "ds_read_b128 v[36:39], v232 offset:0\n"
      "v_mfma_f32_16x16x32_bf16 v[136:139], v[164:167], v[40:43], v[136:139]\n"
      "ds_read_b128 v[40:43], v233 offset:0\n"
      "v_mfma_f32_16x16x32_bf16 v[136:139], v[180:183], v[44:47], v[136:139]\n"
      "ds_read_b128 v[44:47], v234 offset:0\n"
      "v_mfma_f32_16x16x32_bf16 v[136:139], v[184:187], v[60:63], v[136:139]\n"
      "ds_read_b128 v[60:63], v235 offset:0\n"
      "v_cvt_pk_bf16_f32 v208, v132, v133\n"
      "v_mfma_f32_16x16x32_bf16 v[136:139], v[188:191], v[64:67], v[136:139]\n"
      "ds_read_b128 v[64:67], v232 offset:8192\n"
      "v_cvt_pk_bf16_f32 v209, v134, v135\n"
      "v_mfma_f32_16x16x32_bf16 v[136:139], v[204:207], v[68:71], v[136:139]\n"
      "ds_read_b128 v[68:71], v233 offset:8192\n"
      "v_mfma_f32_16x16x32_bf16 v[136:139], a[24:27], v[84:87], v[136:139]\n"
      "ds_read_b128 v[84:87], v234 offset:8192\n"
      "v_mfma_f32_16x16x32_bf16 v[136:139], a[28:31], v[88:91], v[136:139]\n"
      "ds_read_b128 v[88:91], v235 offset:8192\n"
      "v_mfma_f32_16x16x32_bf16 v[136:139], a[32:35], v[92:95], v[136:139]\n"
      "ds_read_b128 v[92:95], v232 offset:16384\n"
      "v_mfma_f32_16x16x32_bf16 v[136:139], a[36:39], v[108:111], v[136:139]\n"
      "ds_read_b128 v[108:111], v233 offset:16384\n"
      "v_mfma_f32_16x16x32_bf16 v[136:139], a[40:43], v[112:115], v[136:139]\n"
      "ds_read_b128 v[112:115], v234 offset:16384\n"
      "v_mfma_f32_16x16x32_bf16 v[136:139], a[44:47], v[116:119], v[136:139]\n"
      "ds_read_b128 v[116:119], v235 offset:16384\n"
      "s_waitcnt lgkmcnt(8)\n"
      "v_mfma_f32_16x16x32_bf16 v[132:135], a[240:243], v[36:39], v[0:3]\n"
      "ds_write_b64 v244, v[208:209] offset:24704\n"
      "v_mfma_f32_16x16x32_bf16 v[132:135], a[244:247], v[40:43], v[132:135]\n"
      "v_lshlrev_b32 v20, 16, v58\n"
      "v_mfma_f32_16x16x32_bf16 v[132:135], a[248:251], v[44:47], v[132:135]\n"
      "v_and_b32 v21, 0xffff0000, v58\n"
      "v_mfma_f32_16x16x32_bf16 v[132:135], a[252:255], v[60:63], v[132:135]\n"
      "v_lshlrev_b32 v22, 16, v59\n"
      "s_waitcnt lgkmcnt(5)\n"
      "v_mfma_f32_16x16x32_bf16 v[132:135], a[48:51], v[64:67], v[132:135]\n"
      "v_and_b32 v23, 0xffff0000, v59\n"
      "v_mfma_f32_16x16x32_bf16 v[132:135], a[52:55], v[68:71], v[132:135]\n"
      "v_cvt_pk_bf16_f32 v210, v136, v137\n"
      "v_mfma_f32_16x16x32_bf16 v[132:135], a[56:59], v[84:87], v[132:135]\n"
      "v_cvt_pk_bf16_f32 v211, v138, v139\n"
      "v_mfma_f32_16x16x32_bf16 v[132:135], a[60:63], v[88:91], v[132:135]\n"
      "s_add_u32 s54, s44, s33\n"
      "s_addc_u32 s55, s45, 0\n"
      "s_waitcnt lgkmcnt(1)\n"
      "v_mfma_f32_16x16x32_bf16 v[132:135], a[64:67], v[92:95], v[132:135]\n"
      "ds_read_b128 v[216:219], v246 offset:12288\n"
      "v_mfma_f32_16x16x32_bf16 v[132:135], a[68:71], v[108:111], v[132:135]\n"
      "ds_read_b128 v[220:223], v246 offset:13312\n"
      "v_mfma_f32_16x16x32_bf16 v[132:135], a[72:75], v[112:115], v[132:135]\n"
      "ds_read_b128 v[224:227], v246 offset:14336\n"
      "v_mfma_f32_16x16x32_bf16 v[132:135], a[76:79], v[116:119], v[132:135]\n"
      "ds_write_b64 v244, v[210:211] offset:24712\n"
      "v_mfma_f32_16x16x32_bf16 v[136:139], a[80:83], v[36:39], v[4:7]\n"
      "v_mfma_f32_16x16x32_bf16 v[136:139], a[84:87], v[40:43], v[136:139]\n"
      "v_mfma_f32_16x16x32_bf16 v[136:139], a[88:91], v[44:47], v[136:139]\n"
      "v_mfma_f32_16x16x32_bf16 v[136:139], a[92:95], v[60:63], v[136:139]\n"
      "v_cvt_pk_bf16_f32 v208, v132, v133\n"
      "v_mfma_f32_16x16x32_bf16 v[136:139], a[96:99], v[64:67], v[136:139]\n"
      "v_cvt_pk_bf16_f32 v209, v134, v135\n"
      "v_mfma_f32_16x16x32_bf16 v[136:139], a[100:103], v[68:71], v[136:139]\n"
      "s_waitcnt lgkmcnt(3)\n"
      "v_mfma_f32_16x16x32_bf16 v[136:139], a[104:107], v[84:87], v[136:139]\n"
      "global_store_dwordx4 v247, v[216:219], s[54:55] offset:0 sc1\n"
      "v_mfma_f32_16x16x32_bf16 v[136:139], a[108:111], v[88:91], v[136:139]\n"
      "s_add_u32 s52, s42, s35\n"
      "s_addc_u32 s53, s43, 0\n"
      "v_mfma_f32_16x16x32_bf16 v[136:139], a[112:115], v[92:95], v[136:139]\n"
      "s_add_u32 m0, s17, 0x12000\n"
      "v_mfma_f32_16x16x32_bf16 v[136:139], a[116:119], v[108:111], v[136:139]\n"
      "ds_write_b64 v244, v[208:209] offset:0\n"
      "v_mfma_f32_16x16x32_bf16 v[136:139], a[120:123], v[112:115], v[136:139]\n"
      "global_load_lds_dwordx4 v236, s[52:53] offset:0 nt\n"
      "v_mfma_f32_16x16x32_bf16 v[136:139], a[124:127], v[116:119], v[136:139]\n"
      "v_mfma_f32_16x16x32_bf16 v[132:135], a[128:131], v[36:39], v[8:11]\n"
      "v_mfma_f32_16x16x32_bf16 v[132:135], a[132:135], v[40:43], v[132:135]\n"
      "v_mfma_f32_16x16x32_bf16 v[132:135], a[136:139], v[44:47], v[132:135]\n"
      "v_mfma_f32_16x16x32_bf16 v[132:135], a[140:143], v[60:63], v[132:135]\n"
      "v_cvt_pk_bf16_f32 v210, v136, v137\n"
      "v_mfma_f32_16x16x32_bf16 v[132:135], a[144:147], v[64:67], v[132:135]\n"
      "v_cvt_pk_bf16_f32 v211, v138, v139\n"
      "v_mfma_f32_16x16x32_bf16 v[132:135], a[148:151], v[68:71], v[132:135]\n"
      "v_mfma_f32_16x16x32_bf16 v[132:135], a[152:155], v[84:87], v[132:135]\n"
      "s_waitcnt lgkmcnt(3)\n"
      "v_mfma_f32_16x16x32_bf16 v[132:135], a[156:159], v[88:91], v[132:135]\n"
      "global_store_dwordx4 v252, v[220:223], s[54:55] offset:0 sc1\n"
      "v_mfma_f32_16x16x32_bf16 v[132:135], a[160:163], v[92:95], v[132:135]\n"
      "s_add_u32 m0, s17, 0x13f00\n"
      "v_mfma_f32_16x16x32_bf16 v[132:135], a[164:167], v[108:111], v[132:135]\n"
      "ds_write_b64 v244, v[210:211] offset:8\n"
      "v_mfma_f32_16x16x32_bf16 v[132:135], a[168:171], v[112:115], v[132:135]\n"
      "global_load_lds_dwordx4 v236, s[52:53] offset:256 nt\n"
      "v_mfma_f32_16x16x32_bf16 v[132:135], a[172:175], v[116:119], v[132:135]\n"
      "v_mfma_f32_16x16x32_bf16 v[136:139], a[176:179], v[36:39], v[12:15]\n"
      "v_mfma_f32_16x16x32_bf16 v[136:139], a[180:183], v[40:43], v[136:139]\n"
      "v_mfma_f32_16x16x32_bf16 v[136:139], a[184:187], v[44:47], v[136:139]\n"
      "v_mfma_f32_16x16x32_bf16 v[136:139], a[188:191], v[60:63], v[136:139]\n"
      "v_cvt_pk_bf16_f32 v212, v132, v133\n"
      "v_mfma_f32_16x16x32_bf16 v[136:139], a[192:195], v[64:67], v[136:139]\n"
      "v_cvt_pk_bf16_f32 v213, v134, v135\n"
      "v_mfma_f32_16x16x32_bf16 v[136:139], a[196:199], v[68:71], v[136:139]\n"
      "v_mfma_f32_16x16x32_bf16 v[136:139], a[200:203], v[84:87], v[136:139]\n"
      "v_mfma_f32_16x16x32_bf16 v[136:139], a[204:207], v[88:91], v[136:139]\n"
      "s_waitcnt lgkmcnt(3)\n"
      "v_mfma_f32_16x16x32_bf16 v[136:139], a[208:211], v[92:95], v[136:139]\n"
      "global_store_dwordx4 v253, v[224:227], s[54:55] offset:0 sc1\n"
      "v_mfma_f32_16x16x32_bf16 v[136:139], a[212:215], v[108:111], v[136:139]\n"
      "ds_write_b64 v245, v[212:213] offset:0\n"
      "v_mfma_f32_16x16x32_bf16 v[136:139], a[216:219], v[112:115], v[136:139]\n"
      "s_add_u32 m0, s17, 0x15e00\n"
      "v_mfma_f32_16x16x32_bf16 v[136:139], a[220:223], v[116:119], v[136:139]\n"
      "global_load_lds_dwordx4 v236, s[52:53] offset:512 nt\n"
      "v_mfma_f32_16x16x32_bf16 v[132:135], a[224:227], v[36:39], v[16:19]\n"
      "v_mfma_f32_16x16x32_bf16 v[132:135], a[228:231], v[40:43], v[132:135]\n"
      "v_mfma_f32_16x16x32_bf16 v[132:135], a[232:235], v[44:47], v[132:135]\n"
      "v_mfma_f32_16x16x32_bf16 v[132:135], a[236:239], v[60:63], v[132:135]\n"
      "v_cvt_pk_bf16_f32 v214, v136, v137\n"
      "v_mfma_f32_16x16x32_bf16 v[132:135], a[0:3], v[64:67], v[132:135]\n"
      "v_cvt_pk_bf16_f32 v215, v138, v139\n"
      "v_mfma_f32_16x16x32_bf16 v[132:135], a[4:7], v[68:71], v[132:135]\n"
      "v_mfma_f32_16x16x32_bf16 v[132:135], a[8:11], v[84:87], v[132:135]\n"
      "v_mfma_f32_16x16x32_bf16 v[132:135], a[12:15], v[88:91], v[132:135]\n"
      "v_mfma_f32_16x16x32_bf16 v[132:135], a[16:19], v[92:95], v[132:135]\n"
      "v_mfma_f32_16x16x32_bf16 v[132:135], a[20:23], v[108:111], v[132:135]\n"
      "ds_write_b64 v245, v[214:215] offset:8\n"
      "v_mfma_f32_16x16x32_bf16 v[132:135], v[140:143], v[112:115], v[132:135]\n"
      "v_mfma_f32_16x16x32_bf16 v[132:135], v[156:159], v[116:119], v[132:135]\n"
      "s_waitcnt lgkmcnt(4)\n"
      "s_barrier\n"
      "v_mfma_f32_16x16x32_bf16 v[136:139], v[160:163], v[36:39], v[20:23]\n"
      "ds_read_b128 v[36:39], v232 offset:256\n"
      "v_mfma_f32_16x16x32_bf16 v[136:139], v[164:167], v[40:43], v[136:139]\n"
      "ds_read_b128 v[40:43], v233 offset:256\n"
      "v_mfma_f32_16x16x32_bf16 v[136:139], v[180:183], v[44:47], v[136:139]\n"
      "ds_read_b128 v[44:47], v234 offset:256\n"
      "v_mfma_f32_16x16x32_bf16 v[136:139], v[184:187], v[60:63], v[136:139]\n"
      "ds_read_b128 v[60:63], v235 offset:256\n"
      "v_cvt_pk_bf16_f32 v208, v132, v133\n"
      "v_mfma_f32_16x16x32_bf16 v[136:139], v[188:191], v[64:67], v[136:139]\n"
      "ds_read_b128 v[64:67], v232 offset:8448\n"
      "v_cvt_pk_bf16_f32 v209, v134, v135\n"
      "v_mfma_f32_16x16x32_bf16 v[136:139], v[204:207], v[68:71], v[136:139]\n"
      "ds_read_b128 v[68:71], v233 offset:8448\n"
      "v_mfma_f32_16x16x32_bf16 v[136:139], a[24:27], v[84:87], v[136:139]\n"
      "ds_read_b128 v[84:87], v234 offset:8448\n"
      "v_mfma_f32_16x16x32_bf16 v[136:139], a[28:31], v[88:91], v[136:139]\n"
      "ds_read_b128 v[88:91], v235 offset:8448\n"
      "v_mfma_f32_16x16x32_bf16 v[136:139], a[32:35], v[92:95], v[136:139]\n"
      "ds_read_b128 v[92:95], v232 offset:16640\n"
      "v_mfma_f32_16x16x32_bf16 v[136:139], a[36:39], v[108:111], v[136:139]\n"
      "ds_read_b128 v[108:111], v233 offset:16640\n"
      "v_mfma_f32_16x16x32_bf16 v[136:139], a[40:43], v[112:115], v[136:139]\n"
      "ds_read_b128 v[112:115], v234 offset:16640\n"
      "v_mfma_f32_16x16x32_bf16 v[136:139], a[44:47], v[116:119], v[136:139]\n"
      "ds_read_b128 v[116:119], v235 offset:16640\n"
      "s_waitcnt lgkmcnt(8)\n"
      "v_mfma_f32_16x16x32_bf16 v[132:135], a[240:243], v[36:39], v[0:3]\n"
      "ds_write_b64 v244, v[208:209] offset:128\n"
      "v_mfma_f32_16x16x32_bf16 v[132:135], a[244:247], v[40:43], v[132:135]\n"
      "v_mfma_f32_16x16x32_bf16 v[132:135], a[248:251], v[44:47], v[132:135]\n"
      "v_mfma_f32_16x16x32_bf16 v[132:135], a[252:255], v[60:63], v[132:135]\n"
      "v_cvt_pk_bf16_f32 v210, v136, v137\n"
      "s_waitcnt lgkmcnt(5)\n"
      "v_mfma_f32_16x16x32_bf16 v[132:135], a[48:51], v[64:67], v[132:135]\n"
      "v_cvt_pk_bf16_f32 v211, v138, v139\n"
      "v_mfma_f32_16x16x32_bf16 v[132:135], a[52:55], v[68:71], v[132:135]\n"
      "v_mfma_f32_16x16x32_bf16 v[132:135], a[56:59], v[84:87], v[132:135]\n"
      "ds_read_b128 v[216:219], v246 offset:24576\n"
      "v_mfma_f32_16x16x32_bf16 v[132:135], a[60:63], v[88:91], v[132:135]\n"
      "ds_read_b128 v[220:223], v246 offset:25600\n"
      "s_waitcnt lgkmcnt(3)\n"
      "v_mfma_f32_16x16x32_bf16 v[132:135], a[64:67], v[92:95], v[132:135]\n"
      "ds_read_b128 v[224:227], v246 offset:26624\n"
      "v_mfma_f32_16x16x32_bf16 v[132:135], a[68:71], v[108:111], v[132:135]\n"
      "ds_write_b64 v244, v[210:211] offset:136\n"
      "v_mfma_f32_16x16x32_bf16 v[132:135], a[72:75], v[112:115], v[132:135]\n"
      "v_mfma_f32_16x16x32_bf16 v[132:135], a[76:79], v[116:119], v[132:135]\n"
      "v_mfma_f32_16x16x32_bf16 v[136:139], a[80:83], v[36:39], v[4:7]\n"
      "v_mfma_f32_16x16x32_bf16 v[136:139], a[84:87], v[40:43], v[136:139]\n"
      "v_mfma_f32_16x16x32_bf16 v[136:139], a[88:91], v[44:47], v[136:139]\n"
      "v_mfma_f32_16x16x32_bf16 v[136:139], a[92:95], v[60:63], v[136:139]\n"
      "v_cvt_pk_bf16_f32 v208, v132, v133\n"
      "v_mfma_f32_16x16x32_bf16 v[136:139], a[96:99], v[64:67], v[136:139]\n"
      "v_cvt_pk_bf16_f32 v209, v134, v135\n"
      "v_mfma_f32_16x16x32_bf16 v[136:139], a[100:103], v[68:71], v[136:139]\n"
      "s_waitcnt lgkmcnt(3)\n"
      "v_mfma_f32_16x16x32_bf16 v[136:139], a[104:107], v[84:87], v[136:139]\n"
      "global_store_dwordx4 v247, v[216:219], s[54:55] offset:768 sc1\n"
      "v_mfma_f32_16x16x32_bf16 v[136:139], a[108:111], v[88:91], v[136:139]\n"
      "s_add_u32 m0, s17, 0x13000\n"
      "v_mfma_f32_16x16x32_bf16 v[136:139], a[112:115], v[92:95], v[136:139]\n"
      "global_load_lds_dwordx4 v237, s[52:53] offset:0 nt\n"
      "v_mfma_f32_16x16x32_bf16 v[136:139], a[116:119], v[108:111], v[136:139]\n"
      "ds_write_b64 v244, v[208:209] offset:12288\n"
      "v_mfma_f32_16x16x32_bf16 v[136:139], a[120:123], v[112:115], v[136:139]\n"
      "v_mfma_f32_16x16x32_bf16 v[136:139], a[124:127], v[116:119], v[136:139]\n"
      "v_mfma_f32_16x16x32_bf16 v[132:135], a[128:131], v[36:39], v[8:11]\n"
      "v_mfma_f32_16x16x32_bf16 v[132:135], a[132:135], v[40:43], v[132:135]\n"
      "v_mfma_f32_16x16x32_bf16 v[132:135], a[136:139], v[44:47], v[132:135]\n"
      "v_mfma_f32_16x16x32_bf16 v[132:135], a[140:143], v[60:63], v[132:135]\n"
      "v_cvt_pk_bf16_f32 v210, v136, v137\n"
      "v_mfma_f32_16x16x32_bf16 v[132:135], a[144:147], v[64:67], v[132:135]\n"
      "v_cvt_pk_bf16_f32 v211, v138, v139\n"
      "v_mfma_f32_16x16x32_bf16 v[132:135], a[148:151], v[68:71], v[132:135]\n"
      "v_mfma_f32_16x16x32_bf16 v[132:135], a[152:155], v[84:87], v[132:135]\n"
      "s_waitcnt lgkmcnt(3)\n"
      "s_waitcnt vmcnt(18)\n"
      "s_mov_b32 s87, 1\n"
      "s_or_b32 s56, s51, s76\n"
      "s_cmp_lg_u32 s56, 0\n"
      "s_cbranch_scc1 LG1\n"
      "v_cmp_ne_u64 s[58:59], s[80:81], v[24:25]\n"
      "v_cmp_ne_u64 s[56:57], s[80:81], v[26:27]\n"
      "s_mov_b32 s87, 0\n"
      "s_nop 3\n"
      "s_or_b64 s[56:57], s[56:57], s[58:59]\n"
      "s_cbranch_scc1 LG1\n"
      "v_mov_b32 v28, 0\n"
      "v_mov_b32 v30, s80\n"
      "v_mov_b32 v31, s81\n"
      "s_mov_b32 s87, 1\n"
      "s_mov_b64 exec, 1\n"
      "global_store_dwordx2 v28, v[30:31], s[28:29] sc1\n"
      "s_mov_b64 exec, -1\n"
      "s_nop 4\n"
      "LG1:\n"
      "v_mfma_f32_16x16x32_bf16 v[132:135], a[156:159], v[88:91], v[132:135]\n"
      "global_store_dwordx4 v252, v[220:223], s[54:55] offset:768 sc1\n"
      "v_mfma_f32_16x16x32_bf16 v[132:135], a[160:163], v[92:95], v[132:135]\n"
      "s_add_u32 m0, s17, 0x14f00\n"
      "v_mfma_f32_16x16x32_bf16 v[132:135], a[164:167], v[108:111], v[132:135]\n"
      "ds_write_b64 v244, v[210:211] offset:12296\n"
      "v_mfma_f32_16x16x32_bf16 v[132:135], a[168:171], v[112:115], v[132:135]\n"
      "global_load_lds_dwordx4 v237, s[52:53] offset:256 nt\n"
      "v_mfma_f32_16x16x32_bf16 v[132:135], a[172:175], v[116:119], v[132:135]\n"
      "v_mfma_f32_16x16x32_bf16 v[136:139], a[176:179], v[36:39], v[12:15]\n"
      "v_mfma_f32_16x16x32_bf16 v[136:139], a[180:183], v[40:43], v[136:139]\n"
      "v_mfma_f32_16x16x32_bf16 v[136:139], a[184:187], v[44:47], v[136:139]\n"
      "v_mfma_f32_16x16x32_bf16 v[136:139], a[188:191], v[60:63], v[136:139]\n"
      "v_cvt_pk_bf16_f32 v212, v132, v133\n"
      "v_mfma_f32_16x16x32_bf16 v[136:139], a[192:195], v[64:67], v[136:139]\n"
      "v_cvt_pk_bf16_f32 v213, v134, v135\n"
      "v_mfma_f32_16x16x32_bf16 v[136:139], a[196:199], v[68:71], v[136:139]\n"
      "v_mfma_f32_16x16x32_bf16 v[136:139], a[200:203], v[84:87], v[136:139]\n"
      "v_mfma_f32_16x16x32_bf16 v[136:139], a[204:207], v[88:91], v[136:139]\n"
      "s_waitcnt lgkmcnt(3)\n"
      "v_mfma_f32_16x16x32_bf16 v[136:139], a[208:211], v[92:95], v[136:139]\n"
      "global_store_dwordx4 v253, v[224:227], s[54:55] offset:768 sc1\n"
      "v_mfma_f32_16x16x32_bf16 v[136:139], a[212:215], v[108:111], v[136:139]\n"
      "ds_write_b64 v245, v[212:213] offset:12288\n"
      "v_mfma_f32_16x16x32_bf16 v[136:139], a[216:219], v[112:115], v[136:139]\n"
      "s_add_u32 m0, s17, 0x16e00\n"
      "v_mfma_f32_16x16x32_bf16 v[136:139], a[220:223], v[116:119], v[136:139]\n"
      "global_load_lds_dwordx4 v237, s[52:53] offset:512 nt\n"
      "v_mfma_f32_16x16x32_bf16 v[132:135], a[224:227], v[36:39], v[16:19]\n"
      "v_mfma_f32_16x16x32_bf16 v[132:135], a[228:231], v[40:43], v[132:135]\n"
      "v_mfma_f32_16x16x32_bf16 v[132:135], a[232:235], v[44:47], v[132:135]\n"
      "v_mfma_f32_16x16x32_bf16 v[132:135], a[236:239], v[60:63], v[132:135]\n"
      "s_memrealtime s[84:85]\n"
      "v_mfma_f32_16x16x32_bf16 v[132:135], a[0:3], v[64:67], v[132:135]\n"
      "v_cvt_pk_bf16_f32 v214, v136, v137\n"
      "v_mfma_f32_16x16x32_bf16 v[132:135], a[4:7], v[68:71], v[132:135]\n"
      "v_cvt_pk_bf16_f32 v215, v138, v139\n"
      "v_mfma_f32_16x16x32_bf16 v[132:135], a[8:11], v[84:87], v[132:135]\n"
      "v_mfma_f32_16x16x32_bf16 v[132:135], a[12:15], v[88:91], v[132:135]\n"
      "v_mfma_f32_16x16x32_bf16 v[132:135], a[16:19], v[92:95], v[132:135]\n"
      "v_mfma_f32_16x16x32_bf16 v[132:135], a[20:23], v[108:111], v[132:135]\n"
      "v_mfma_f32_16x16x32_bf16 v[132:135], v[140:143], v[112:115], v[132:135]\n"
      "s_waitcnt lgkmcnt(0)\n"
      "s_sub_u32 s48, s84, s48\n"
      "v_mfma_f32_16x16x32_bf16 v[132:135], v[156:159], v[116:119], v[132:135]\n"
      "s_barrier\n"
      "ds_write_b64 v245, v[214:215] offset:12296\n"
      "v_mfma_f32_16x16x32_bf16 v[136:139], v[160:163], v[36:39], v[20:23]\n"
      "ds_read_b128 v[36:39], v232 offset:24576\n"
      "s_lshl_b32 s79, s91, 1\n"
      "v_mfma_f32_16x16x32_bf16 v[136:139], v[164:167], v[40:43], v[136:139]\n"
      "ds_read_b128 v[40:43], v233 offset:24576\n"
      "v_mad_i32_i24 v252, v251, s79, v252\n"
      "v_mfma_f32_16x16x32_bf16 v[136:139], v[180:183], v[44:47], v[136:139]\n"
      "ds_read_b128 v[44:47], v234 offset:24576\n"
      "s_mul_i32 s79, s79, s100\n"
      "v_mfma_f32_16x16x32_bf16 v[136:139], v[184:187], v[60:63], v[136:139]\n"
      "ds_read_b128 v[60:63], v235 offset:24576\n"
      "v_cvt_pk_bf16_f32 v208, v132, v133\n"
      "v_mfma_f32_16x16x32_bf16 v[136:139], v[188:191], v[64:67], v[136:139]\n"
      "ds_read_b128 v[64:67], v232 offset:32768\n"
      "v_cvt_pk_bf16_f32 v209, v134, v135\n"
      "v_mfma_f32_16x16x32_bf16 v[136:139], v[204:207], v[68:71], v[136:139]\n"
      "ds_read_b128 v[68:71], v233 offset:32768\n"
      "v_add_u32 v247, s79, v247\n"
      "v_mfma_f32_16x16x32_bf16 v[136:139], a[24:27], v[84:87], v[136:139]\n"
      "ds_read_b128 v[84:87], v234 offset:32768\n"
      "s_mul_i32 s79, s79, s101\n"
      "v_mfma_f32_16x16x32_bf16 v[136:139], a[28:31], v[88:91], v[136:139]\n"
      "ds_read_b128 v[88:91], v235 offset:32768\n"
      "v_add_u32 v253, s79, v253\n"
      "v_mfma_f32_16x16x32_bf16 v[136:139], a[32:35], v[92:95], v[136:139]\n"
      "ds_read_b128 v[92:95], v232 offset:40960\n"
      "v_mfma_f32_16x16x32_bf16 v[136:139], a[36:39], v[108:111], v[136:139]\n"
      "ds_read_b128 v[108:111], v233 offset:40960\n"
      "v_mfma_f32_16x16x32_bf16 v[136:139], a[40:43], v[112:115], v[136:139]\n"
      "ds_read_b128 v[112:115], v234 offset:40960\n"
      "v_mfma_f32_16x16x32_bf16 v[136:139], a[44:47], v[116:119], v[136:139]\n"
      "ds_read_b128 v[116:119], v235 offset:40960\n"
      "s_waitcnt lgkmcnt(8)\n"
      "v_mfma_f32_16x16x32_bf16 v[132:135], a[240:243], v[36:39], v[0:3]\n"
      "ds_write_b64 v244, v[208:209] offset:12416\n"
      "v_mfma_f32_16x16x32_bf16 v[132:135], a[244:247], v[40:43], v[132:135]\n"
      "s_add_u32 s54, s30, s34\n"
      "s_addc_u32 s55, s31, 0\n"
      "v_mfma_f32_16x16x32_bf16 v[132:135], a[248:251], v[44:47], v[132:135]\n"
      "s_mul_i32 s48, s48, 939\n"
      "v_mfma_f32_16x16x32_bf16 v[132:135], a[252:255], v[60:63], v[132:135]\n"
      "s_lshr_b32 s48, s48, 8\n"
      "v_cvt_pk_bf16_f32 v210, v136, v137\n"
      "s_waitcnt lgkmcnt(5)\n"
      "v_mfma_f32_16x16x32_bf16 v[132:135], a[48:51], v[64:67], v[132:135]\n"
      "s_add_u32 s48, s48, s84\n"
      "v_cvt_pk_bf16_f32 v211, v138, v139\n"
      "v_mfma_f32_16x16x32_bf16 v[132:135], a[52:55], v[68:71], v[132:135]\n"
      "s_xor_b32 s49, s48, s80\n"
      "s_mul_i32 s78, s93, s19\n"
      "v_mfma_f32_16x16x32_bf16 v[132:135], a[56:59], v[84:87], v[132:135]\n"
      "v_mov_b32 v53, s49\n"
      "v_add_u32 v236, s78, v236\n"
      "v_mfma_f32_16x16x32_bf16 v[132:135], a[60:63], v[88:91], v[132:135]\n"
      "s_mul_i32 s49, s51, 0x1100\n"
      "s_mul_i32 s78, s78, s99\n"
      "s_waitcnt lgkmcnt(1)\n"
      "v_mfma_f32_16x16x32_bf16 v[132:135], a[64:67], v[92:95], v[132:135]\n"
      "s_and_b32 s49, s49, 0xf000\n"
      "v_add_u32 v237, s78, v237\n"
      "v_mfma_f32_16x16x32_bf16 v[132:135], a[68:71], v[108:111], v[132:135]\n"
      "s_lshr_b32 s49, s49, 0\n"
      "ds_write_b64 v244, v[210:211] offset:12424\n"
      "v_mfma_f32_16x16x32_bf16 v[132:135], a[72:75], v[112:115], v[132:135]\n"
      "s_add_u32 s49, s49, 1114112\n"
      "v_mfma_f32_16x16x32_bf16 v[132:135], a[76:79], v[116:119], v[132:135]\n"
      "v_mov_b32 v52, s49\n"
      "v_mfma_f32_16x16x32_bf16 v[136:139], a[80:83], v[36:39], v[4:7]\n"
      "s_and_b32 s49, s51, 0x78\n"
      "v_mfma_f32_16x16x32_bf16 v[136:139], a[84:87], v[40:43], v[136:139]\n"
      "s_xor_b32 s49, s49, 0x40\n"
      "v_mfma_f32_16x16x32_bf16 v[136:139], a[88:91], v[44:47], v[136:139]\n"
      "s_or_b32 s49, s49, s76\n"
      "v_mfma_f32_16x16x32_bf16 v[136:139], a[92:95], v[60:63], v[136:139]\n"
      "v_cvt_pk_bf16_f32 v208, v132, v133\n"
      "v_mfma_f32_16x16x32_bf16 v[136:139], a[96:99], v[64:67], v[136:139]\n"
      "s_cmp_lg_u32 s49, 0\n"
      "s_cbranch_scc0 LPS\n"
      "LPR:\n"
      "v_mfma_f32_16x16x32_bf16 v[136:139], a[100:103], v[68:71], v[136:139]\n"
      "v_cvt_pk_bf16_f32 v209, v134, v135\n"
      "v_mfma_f32_16x16x32_bf16 v[136:139], a[104:107], v[84:87], v[136:139]\n"
      "ds_read_b128 v[216:219], v246 offset:0\n"
      "v_mfma_f32_16x16x32_bf16 v[136:139], a[108:111], v[88:91], v[136:139]\n"
      "ds_read_b128 v[220:223], v246 offset:1024\n"
      "v_mfma_f32_16x16x32_bf16 v[136:139], a[112:115], v[92:95], v[136:139]\n"
      "ds_read_b128 v[224:227], v246 offset:2048\n"
      "v_mfma_f32_16x16x32_bf16 v[136:139], a[116:119], v[108:111], v[136:139]\n"
      "s_waitcnt lgkmcnt(2)\n"
      "v_mfma_f32_16x16x32_bf16 v[136:139], a[120:123], v[112:115], v[136:139]\n"
      "ds_write_b64 v244, v[208:209] offset:24576\n"
      "v_mfma_f32_16x16x32_bf16 v[136:139], a[124:127], v[116:119], v[136:139]\n"
      "global_store_dwordx4 v247, v[216:219], s[54:55] offset:0 sc1\n"
      "v_mfma_f32_16x16x32_bf16 v[132:135], a[128:131], v[36:39], v[8:11]\n"
      "s_add_u32 s52, s20, s36\n"
      "s_addc_u32 s53, s21, 0\n"
      "v_mfma_f32_16x16x32_bf16 v[132:135], a[132:135], v[40:43], v[132:135]\n"
      "s_add_u32 m0, s17, 0x0\n"
      "v_mfma_f32_16x16x32_bf16 v[132:135], a[136:139], v[44:47], v[132:135]\n"
      "global_load_lds_dwordx4 v236, s[52:53] offset:0 nt\n"
      "v_mfma_f32_16x16x32_bf16 v[132:135], a[140:143], v[60:63], v[132:135]\n"
      "v_cvt_pk_bf16_f32 v210, v136, v137\n"
      "v_mfma_f32_16x16x32_bf16 v[132:135], a[144:147], v[64:67], v[132:135]\n"
      "v_cvt_pk_bf16_f32 v211, v138, v139\n"
      "v_mfma_f32_16x16x32_bf16 v[132:135], a[148:151], v[68:71], v[132:135]\n"
      "v_mfma_f32_16x16x32_bf16 v[132:135], a[152:155], v[84:87], v[132:135]\n"
      "s_waitcnt lgkmcnt(2)\n"
      "v_mfma_f32_16x16x32_bf16 v[132:135], a[156:159], v[88:91], v[132:135]\n"
      "global_store_dwordx4 v252, v[220:223], s[54:55] offset:0 sc1\n"
      "v_mfma_f32_16x16x32_bf16 v[132:135], a[160:163], v[92:95], v[132:135]\n"
      "v_mfma_f32_16x16x32_bf16 v[132:135], a[164:167], v[108:111], v[132:135]\n"
      "ds_write_b64 v244, v[210:211] offset:24584\n"
      "v_mfma_f32_16x16x32_bf16 v[132:135], a[168:171], v[112:115], v[132:135]\n"
      "v_mfma_f32_16x16x32_bf16 v[132:135], a[172:175], v[116:119], v[132:135]\n"
      "v_mfma_f32_16x16x32_bf16 v[136:139], a[176:179], v[36:39], v[12:15]\n"
      "s_add_u32 m0, s17, 0x1f00\n"
      "v_mfma_f32_16x16x32_bf16 v[136:139], a[180:183], v[40:43], v[136:139]\n"
      "global_load_lds_dwordx4 v236, s[52:53] offset:256 nt\n"
      "v_mfma_f32_16x16x32_bf16 v[136:139], a[184:187], v[44:47], v[136:139]\n"
      "v_mfma_f32_16x16x32_bf16 v[136:139], a[188:191], v[60:63], v[136:139]\n"
      "v_cvt_pk_bf16_f32 v212, v132, v133\n"
      "v_mfma_f32_16x16x32_bf16 v[136:139], a[192:195], v[64:67], v[136:139]\n"
      "v_cvt_pk_bf16_f32 v213, v134, v135\n"
      "v_mfma_f32_16x16x32_bf16 v[136:139], a[196:199], v[68:71], v[136:139]\n"
      "v_mfma_f32_16x16x32_bf16 v[136:139], a[200:203], v[84:87], v[136:139]\n"
      "v_mfma_f32_16x16x32_bf16 v[136:139], a[204:207], v[88:91], v[136:139]\n"
      "s_waitcnt lgkmcnt(2)\n"
      "v_mfma_f32_16x16x32_bf16 v[136:139], a[208:211], v[92:95], v[136:139]\n"
      "global_store_dwordx4 v253, v[224:227], s[54:55] offset:0 sc1\n"
      "v_mfma_f32_16x16x32_bf16 v[136:139], a[212:215], v[108:111], v[136:139]\n"
      "ds_write_b64 v245, v[212:213] offset:24576\n"
      "v_mfma_f32_16x16x32_bf16 v[136:139], a[216:219], v[112:115], v[136:139]\n"
      "v_mfma_f32_16x16x32_bf16 v[136:139], a[220:223], v[116:119], v[136:139]\n"
      "s_add_u32 m0, s17, 0x3e00\n"
      "v_mfma_f32_16x16x32_bf16 v[132:135], a[224:227], v[36:39], v[16:19]\n"
      "global_load_lds_dwordx4 v236, s[52:53] offset:512 nt\n"
      "v_mfma_f32_16x16x32_bf16 v[132:135], a[228:231], v[40:43], v[132:135]\n"
      "v_mfma_f32_16x16x32_bf16 v[132:135], a[232:235], v[44:47], v[132:135]\n"
      "v_mfma_f32_16x16x32_bf16 v[132:135], a[236:239], v[60:63], v[132:135]\n"
      "v_cvt_pk_bf16_f32 v214, v136, v137\n"
      "v_mfma_f32_16x16x32_bf16 v[132:135], a[0:3], v[64:67], v[132:135]\n"
      "v_cvt_pk_bf16_f32 v215, v138, v139\n"
      "v_mfma_f32_16x16x32_bf16 v[132:135], a[4:7], v[68:71], v[132:135]\n"
      "v_mfma_f32_16x16x32_bf16 v[132:135], a[8:11], v[84:87], v[132:135]\n"
      "v_mfma_f32_16x16x32_bf16 v[132:135], a[12:15], v[88:91], v[132:135]\n"
      "v_mfma_f32_16x16x32_bf16 v[132:135], a[16:19], v[92:95], v[132:135]\n"
      "v_mfma_f32_16x16x32_bf16 v[132:135], a[20:23], v[108:111], v[132:135]\n"
      "ds_write_b64 v245, v[214:215] offset:24584\n"
      "v_mfma_f32_16x16x32_bf16 v[132:135], v[140:143], v[112:115], v[132:135]\n"
      "v_mfma_f32_16x16x32_bf16 v[132:135], v[156:159], v[116:119], v[132:135]\n"
      "s_barrier\n"
      "v_mfma_f32_16x16x32_bf16 v[136:139], v[160:163], v[36:39], v[20:23]\n"
      "ds_read_b128 v[36:39], v232 offset:24832\n"
      "v_mfma_f32_16x16x32_bf16 v[136:139], v[164:167], v[40:43], v[136:139]\n"
      "ds_read_b128 v[40:43], v233 offset:24832\n"
      "v_mfma_f32_16x16x32_bf16 v[136:139], v[180:183], v[44:47], v[136:139]\n"
      "ds_read_b128 v[44:47], v234 offset:24832\n"
      "v_mfma_f32_16x16x32_bf16 v[136:139], v[184:187], v[60:63], v[136:139]\n"
      "ds_read_b128 v[60:63], v235 offset:24832\n"
      "v_cvt_pk_bf16_f32 v208, v132, v133\n"
      "v_mfma_f32_16x16x32_bf16 v[136:139], v[188:191], v[64:67], v[136:139]\n"
      "ds_read_b128 v[64:67], v232 offset:33024\n"
      "v_cvt_pk_bf16_f32 v209, v134, v135\n"
      "v_mfma_f32_16x16x32_bf16 v[136:139], v[204:207], v[68:71], v[136:139]\n"
      "ds_read_b128 v[68:71], v233 offset:33024\n"
      "v_mfma_f32_16x16x32_bf16 v[136:139], a[24:27], v[84:87], v[136:139]\n"
      "ds_read_b128 v[84:87], v234 offset:33024\n"
      "v_mfma_f32_16x16x32_bf16 v[136:139], a[28:31], v[88:91], v[136:139]\n"
      "ds_read_b128 v[88:91], v235 offset:33024\n"
      "v_mfma_f32_16x16x32_bf16 v[136:139], a[32:35], v[92:95], v[136:139]\n"
      "ds_read_b128 v[92:95], v232 offset:41216\n"
      "v_mfma_f32_16x16x32_bf16 v[136:139], a[36:39], v[108:111], v[136:139]\n"
      "ds_read_b128 v[108:111], v233 offset:41216\n"
      "ds_write_b64 v244, v[208:209] offset:24704\n"
      "v_mfma_f32_16x16x32_bf16 v[136:139], a[40:43], v[112:115], v[136:139]\n"
      "ds_read_b128 v[112:115], v234 offset:41216\n"
      "v_mfma_f32_16x16x32_bf16 v[136:139], a[44:47], v[116:119], v[136:139]\n"
      "ds_read_b128 v[116:119], v235 offset:41216\n"
      "s_waitcnt lgkmcnt(9)\n"
      "v_mfma_f32_16x16x32_bf16 v[132:135], a[240:243], v[36:39], v[0:3]\n"
      "v_add_u32 v232, 0xc000, v232\n"
      "v_mfma_f32_16x16x32_bf16 v[132:135], a[244:247], v[40:43], v[132:135]\n"
      "v_add_u32 v233, 0xc000, v233\n"
      "v_mfma_f32_16x16x32_bf16 v[132:135], a[248:251], v[44:47], v[132:135]\n"
      "v_add_u32 v234, 0xc000, v234\n"
      "v_mfma_f32_16x16x32_bf16 v[132:135], a[252:255], v[60:63], v[132:135]\n"
      "v_add_u32 v235, 0xc000, v235\n"
      "s_waitcnt lgkmcnt(5)\n"
      "v_mfma_f32_16x16x32_bf16 v[132:135], a[48:51], v[64:67], v[132:135]\n"
      "v_cvt_pk_bf16_f32 v210, v136, v137\n"
      "v_mfma_f32_16x16x32_bf16 v[132:135], a[52:55], v[68:71], v[132:135]\n"
      "v_cvt_pk_bf16_f32 v211, v138, v139\n"
      "v_mfma_f32_16x16x32_bf16 v[132:135], a[56:59], v[84:87], v[132:135]\n"
      "ds_read_b128 v[216:219], v246 offset:12288\n"
      "v_mfma_f32_16x16x32_bf16 v[132:135], a[60:63], v[88:91], v[132:135]\n"
      "ds_read_b128 v[220:223], v246 offset:13312\n"
      "s_waitcnt lgkmcnt(2)\n"
      "v_mfma_f32_16x16x32_bf16 v[132:135], a[64:67], v[92:95], v[132:135]\n"
      "ds_read_b128 v[224:227], v246 offset:14336\n"
      "v_mfma_f32_16x16x32_bf16 v[132:135], a[68:71], v[108:111], v[132:135]\n"
      "v_lshlrev_b32 v0, 16, v72\n"
      "v_mfma_f32_16x16x32_bf16 v[132:135], a[72:75], v[112:115], v[132:135]\n"
      "ds_write_b64 v244, v[210:211] offset:24712\n"
      "v_mfma_f32_16x16x32_bf16 v[132:135], a[76:79], v[116:119], v[132:135]\n"
      "v_and_b32 v1, 0xffff0000, v72\n"
      "v_mfma_f32_16x16x32_bf16 v[136:139], a[80:83], v[36:39], v[4:7]\n"
      "v_lshlrev_b32 v2, 16, v73\n"
      "v_mfma_f32_16x16x32_bf16 v[136:139], a[84:87], v[40:43], v[136:139]\n"
      "v_and_b32 v3, 0xffff0000, v73\n"
      "v_mfma_f32_16x16x32_bf16 v[136:139], a[88:91], v[44:47], v[136:139]\n"
      "v_mfma_f32_16x16x32_bf16 v[136:139], a[92:95], v[60:63], v[136:139]\n"
      "v_cvt_pk_bf16_f32 v208, v132, v133\n"
      "v_mfma_f32_16x16x32_bf16 v[136:139], a[96:99], v[64:67], v[136:139]\n"
      "v_cvt_pk_bf16_f32 v209, v134, v135\n"
      "v_mfma_f32_16x16x32_bf16 v[136:139], a[100:103], v[68:71], v[136:139]\n"
      "s_waitcnt lgkmcnt(3)\n"
      "v_mfma_f32_16x16x32_bf16 v[136:139], a[104:107], v[84:87], v[136:139]\n"
      "global_store_dwordx4 v247, v[216:219], s[54:55] offset:768 sc1\n"
      "v_mfma_f32_16x16x32_bf16 v[136:139], a[108:111], v[88:91], v[136:139]\n"
      "s_add_u32 m0, s17, 0x1000\n"
      "v_mfma_f32_16x16x32_bf16 v[136:139], a[112:115], v[92:95], v[136:139]\n"
      "global_load_lds_dwordx4 v237, s[52:53] offset:0 nt\n"
      "v_mfma_f32_16x16x32_bf16 v[136:139], a[116:119], v[108:111], v[136:139]\n"
      "ds_write_b64 v244, v[208:209] offset:0\n"
      "v_mfma_f32_16x16x32_bf16 v[136:139], a[120:123], v[112:115], v[136:139]\n"
      "v_lshlrev_b32 v4, 16, v74\n"
      "v_mfma_f32_16x16x32_bf16 v[136:139], a[124:127], v[116:119], v[136:139]\n"
      "v_and_b32 v5, 0xffff0000, v74\n"
      "v_mfma_f32_16x16x32_bf16 v[132:135], a[128:131], v[36:39], v[8:11]\n"
      "v_lshlrev_b32 v6, 16, v75\n"
      "v_mfma_f32_16x16x32_bf16 v[132:135], a[132:135], v[40:43], v[132:135]\n"
      "v_and_b32 v7, 0xffff0000, v75\n"
      "v_mfma_f32_16x16x32_bf16 v[132:135], a[136:139], v[44:47], v[132:135]\n"
      "v_mfma_f32_16x16x32_bf16 v[132:135], a[140:143], v[60:63], v[132:135]\n"
      "v_cvt_pk_bf16_f32 v210, v136, v137\n"
      "v_mfma_f32_16x16x32_bf16 v[132:135], a[144:147], v[64:67], v[132:135]\n"
      "v_cvt_pk_bf16_f32 v211, v138, v139\n"
      "v_mfma_f32_16x16x32_bf16 v[132:135], a[148:151], v[68:71], v[132:135]\n"
      "v_lshlrev_b32 v8, 16, v76\n"
      "v_mfma_f32_16x16x32_bf16 v[132:135], a[152:155], v[84:87], v[132:135]\n"
      "s_waitcnt lgkmcnt(3)\n"
      "v_mfma_f32_16x16x32_bf16 v[132:135], a[156:159], v[88:91], v[132:135]\n"
      "global_store_dwordx4 v252, v[220:223], s[54:55] offset:768 sc1\n"
      "v_mfma_f32_16x16x32_bf16 v[132:135], a[160:163], v[92:95], v[132:135]\n"
      "s_add_u32 m0, s17, 0x2f00\n"
      "v_mfma_f32_16x16x32_bf16 v[132:135], a[164:167], v[108:111], v[132:135]\n"
      "ds_write_b64 v244, v[210:211] offset:8\n"
      "v_mfma_f32_16x16x32_bf16 v[132:135], a[168:171], v[112:115], v[132:135]\n"
      "global_load_lds_dwordx4 v237, s[52:53] offset:256 nt\n"
      "v_mfma_f32_16x16x32_bf16 v[132:135], a[172:175], v[116:119], v[132:135]\n"
      "v_and_b32 v9, 0xffff0000, v76\n"
      "v_mfma_f32_16x16x32_bf16 v[136:139], a[176:179], v[36:39], v[12:15]\n"
      "v_lshlrev_b32 v10, 16, v77\n"
      "v_mfma_f32_16x16x32_bf16 v[136:139], a[180:183], v[40:43], v[136:139]\n"
      "v_and_b32 v11, 0xffff0000, v77\n"
      "v_mfma_f32_16x16x32_bf16 v[136:139], a[184:187], v[44:47], v[136:139]\n"
      "v_mfma_f32_16x16x32_bf16 v[136:139], a[188:191], v[60:63], v[136:139]\n"
      "v_cvt_pk_bf16_f32 v212, v132, v133\n"
      "v_mfma_f32_16x16x32_bf16 v[136:139], a[192:195], v[64:67], v[136:139]\n"
      "v_cvt_pk_bf16_f32 v213, v134, v135\n"
      "v_mfma_f32_16x16x32_bf16 v[136:139], a[196:199], v[68:71], v[136:139]\n"
      "v_lshlrev_b32 v12, 16, v78\n"
      "v_mfma_f32_16x16x32_bf16 v[136:139], a[200:203], v[84:87], v[136:139]\n"
      "v_and_b32 v13, 0xffff0000, v78\n"
      "v_mfma_f32_16x16x32_bf16 v[136:139], a[204:207], v[88:91], v[136:139]\n"
      "s_waitcnt lgkmcnt(3)\n"
      "v_mfma_f32_16x16x32_bf16 v[136:139], a[208:211], v[92:95], v[136:139]\n"
      "global_store_dwordx4 v253, v[224:227], s[54:55] offset:768 sc1\n"
      "v_mfma_f32_16x16x32_bf16 v[136:139], a[212:215], v[108:111], v[136:139]\n"
      "ds_write_b64 v245, v[212:213] offset:0\n"
      "v_mfma_f32_16x16x32_bf16 v[136:139], a[216:219], v[112:115], v[136:139]\n"
      "s_add_u32 m0, s17, 0x4e00\n"
      "v_mfma_f32_16x16x32_bf16 v[136:139], a[220:223], v[116:119], v[136:139]\n"
      "global_load_lds_dwordx4 v237, s[52:53] offset:512 nt\n"
      "v_mfma_f32_16x16x32_bf16 v[132:135], a[224:227], v[36:39], v[16:19]\n"
      "v_lshlrev_b32 v14, 16, v79\n"
      "v_mfma_f32_16x16x32_bf16 v[132:135], a[228:231], v[40:43], v[132:135]\n"
      "v_and_b32 v15, 0xffff0000, v79\n"
      "v_mfma_f32_16x16x32_bf16 v[132:135], a[232:235], v[44:47], v[132:135]\n"
      "v_mfma_f32_16x16x32_bf16 v[132:135], a[236:239], v[60:63], v[132:135]\n"
      "v_cvt_pk_bf16_f32 v214, v136, v137\n"
      "v_mfma_f32_16x16x32_bf16 v[132:135], a[0:3], v[64:67], v[132:135]\n"
      "v_cvt_pk_bf16_f32 v215, v138, v139\n"
      "v_mfma_f32_16x16x32_bf16 v[132:135], a[4:7], v[68:71], v[132:135]\n"
      "v_lshlrev_b32 v16, 16, v80\n"
      "v_mfma_f32_16x16x32_bf16 v[132:135], a[8:11], v[84:87], v[132:135]\n"
      "v_and_b32 v17, 0xffff0000, v80\n"
      "v_mfma_f32_16x16x32_bf16 v[132:135], a[12:15], v[88:91], v[132:135]\n"
      "v_lshlrev_b32 v18, 16, v81\n"
      "v_mfma_f32_16x16x32_bf16 v[132:135], a[16:19], v[92:95], v[132:135]\n"
      "v_and_b32 v19, 0xffff0000, v81\n"
      "v_mfma_f32_16x16x32_bf16 v[132:135], a[20:23], v[108:111], v[132:135]\n"
      "ds_write_b64 v245, v[214:215] offset:8\n"
      "v_mfma_f32_16x16x32_bf16 v[132:135], v[140:143], v[112:115], v[132:135]\n"
      "v_lshlrev_b32 v52, 8, v239\n"
      "v_mfma_f32_16x16x32_bf16 v[132:135], v[156:159], v[116:119], v[132:135]\n"
      "s_waitcnt vmcnt(24) lgkmcnt(4)\n"
      "s_barrier\n"
      "v_add_u32 v52, 1114112, v52\n"
      "v_mfma_f32_16x16x32_bf16 v[136:139], v[160:163], v[36:39], v[20:23]\n"
      "ds_read_b128 v[36:39], v232 offset:0\n"
      "v_mfma_f32_16x16x32_bf16 v[136:139], v[164:167], v[40:43], v[136:139]\n"
      "ds_read_b128 v[40:43], v233 offset:0\n"
      "s_mov_b64 exec, 0xffff\n"
      "global_load_dword v48, v52, s[28:29] sc1\n"
      "s_mov_b64 exec, -1\n"
      "s_nop 1\n"
      "v_mfma_f32_16x16x32_bf16 v[136:139], v[180:183], v[44:47], v[136:139]\n"
      "ds_read_b128 v[44:47], v234 offset:0\n"
      "v_mfma_f32_16x16x32_bf16 v[136:139], v[184:187], v[60:63], v[136:139]\n"
      "ds_read_b128 v[60:63], v235 offset:0\n"
      "v_cvt_pk_bf16_f32 v208, v132, v133\n"
      "v_mfma_f32_16x16x32_bf16 v[136:139], v[188:191], v[64:67], v[136:139]\n"
      "ds_read_b128 v[64:67], v232 offset:8192\n"
      "v_cvt_pk_bf16_f32 v209, v134, v135\n"
      "v_mfma_f32_16x16x32_bf16 v[136:139], v[204:207], v[68:71], v[136:139]\n"
      "ds_read_b128 v[68:71], v233 offset:8192\n"
      "v_mfma_f32_16x16x32_bf16 v[136:139], a[24:27], v[84:87], v[136:139]\n"
      "ds_read_b128 v[84:87], v234 offset:8192\n"
      "v_mfma_f32_16x16x32_bf16 v[136:139], a[28:31], v[88:91], v[136:139]\n"
      "ds_read_b128 v[88:91], v235 offset:8192\n"
      "v_mfma_f32_16x16x32_bf16 v[136:139], a[32:35], v[92:95], v[136:139]\n"
      "ds_read_b128 v[92:95], v232 offset:16384\n"
      "v_mfma_f32_16x16x32_bf16 v[136:139], a[36:39], v[108:111], v[136:139]\n"
      "ds_read_b128 v[108:111], v233 offset:16384\n"
      "v_mfma_f32_16x16x32_bf16 v[136:139], a[40:43], v[112:115], v[136:139]\n"
      "ds_read_b128 v[112:115], v234 offset:16384\n"
      "v_mfma_f32_16x16x32_bf16 v[136:139], a[44:47], v[116:119], v[136:139]\n"
      "ds_read_b128 v[116:119], v235 offset:16384\n"
      "s_waitcnt lgkmcnt(8)\n"
      "v_mfma_f32_16x16x32_bf16 v[132:135], a[240:243], v[36:39], v[0:3]\n"
      "ds_write_b64 v244, v[208:209] offset:128\n"
      "v_mfma_f32_16x16x32_bf16 v[132:135], a[244:247], v[40:43], v[132:135]\n"
      "v_lshlrev_b32 v20, 16, v82\n"
      "v_mfma_f32_16x16x32_bf16 v[132:135], a[248:251], v[44:47], v[132:135]\n"
      "v_and_b32 v21, 0xffff0000, v82\n"
      "v_mfma_f32_16x16x32_bf16 v[132:135], a[252:255], v[60:63], v[132:135]\n"
      "v_lshlrev_b32 v22, 16, v83\n"
      "s_waitcnt lgkmcnt(5)\n"
      "v_mfma_f32_16x16x32_bf16 v[132:135], a[48:51], v[64:67], v[132:135]\n"
      "v_and_b32 v23, 0xffff0000, v83\n"
      "v_mfma_f32_16x16x32_bf16 v[132:135], a[52:55], v[68:71], v[132:135]\n"
      "v_cvt_pk_bf16_f32 v210, v136, v137\n"
      "v_mfma_f32_16x16x32_bf16 v[132:135], a[56:59], v[84:87], v[132:135]\n"
      "v_cvt_pk_bf16_f32 v211, v138, v139\n"
      "v_mfma_f32_16x16x32_bf16 v[132:135], a[60:63], v[88:91], v[132:135]\n"
      "s_add_u32 s54, s44, s34\n"
      "s_addc_u32 s55, s45, 0\n"
      "s_waitcnt lgkmcnt(1)\n"
      "v_mfma_f32_16x16x32_bf16 v[132:135], a[64:67], v[92:95], v[132:135]\n"
      "ds_read_b128 v[216:219], v246 offset:24576\n"
      "v_mfma_f32_16x16x32_bf16 v[132:135], a[68:71], v[108:111], v[132:135]\n"
      "ds_read_b128 v[220:223], v246 offset:25600\n"
      "v_mfma_f32_16x16x32_bf16 v[132:135], a[72:75], v[112:115], v[132:135]\n"
      "ds_read_b128 v[224:227], v246 offset:26624\n"
      "v_mfma_f32_16x16x32_bf16 v[132:135], a[76:79], v[116:119], v[132:135]\n"
      "ds_write_b64 v244, v[210:211] offset:136\n"
      "v_mfma_f32_16x16x32_bf16 v[136:139], a[80:83], v[36:39], v[4:7]\n"
      "v_mfma_f32_16x16x32_bf16 v[136:139], a[84:87], v[40:43], v[136:139]\n"
      "v_mfma_f32_16x16x32_bf16 v[136:139], a[88:91], v[44:47], v[136:139]\n"
      "v_mfma_f32_16x16x32_bf16 v[136:139], a[92:95], v[60:63], v[136:139]\n"
      "v_cvt_pk_bf16_f32 v208, v132, v133\n"
      "v_mfma_f32_16x16x32_bf16 v[136:139], a[96:99], v[64:67], v[136:139]\n"
      "v_cvt_pk_bf16_f32 v209, v134, v135\n"
      "v_mfma_f32_16x16x32_bf16 v[136:139], a[100:103], v[68:71], v[136:139]\n"
      "s_waitcnt lgkmcnt(3)\n"
      "v_mfma_f32_16x16x32_bf16 v[136:139], a[104:107], v[84:87], v[136:139]\n"
      "global_store_dwordx4 v247, v[216:219], s[54:55] offset:0 sc1\n"
      "v_mfma_f32_16x16x32_bf16 v[136:139], a[108:111], v[88:91], v[136:139]\n"
      "s_add_u32 s52, s42, s36\n"
      "s_addc_u32 s53, s43, 0\n"
      "v_mfma_f32_16x16x32_bf16 v[136:139], a[112:115], v[92:95], v[136:139]\n"
      "s_add_u32 m0, s17, 0x6000\n"
      "v_mfma_f32_16x16x32_bf16 v[136:139], a[116:119], v[108:111], v[136:139]\n"
      "ds_write_b64 v244, v[208:209] offset:12288\n"
      "v_mfma_f32_16x16x32_bf16 v[136:139], a[120:123], v[112:115], v[136:139]\n"
      "global_load_lds_dwordx4 v236, s[52:53] offset:0 nt\n"
      "v_mfma_f32_16x16x32_bf16 v[136:139], a[124:127], v[116:119], v[136:139]\n"
      "v_mfma_f32_16x16x32_bf16 v[132:135], a[128:131], v[36:39], v[8:11]\n"
      "v_mfma_f32_16x16x32_bf16 v[132:135], a[132:135], v[40:43], v[132:135]\n"
      "v_mfma_f32_16x16x32_bf16 v[132:135], a[136:139], v[44:47], v[132:135]\n"
      "v_mfma_f32_16x16x32_bf16 v[132:135], a[140:143], v[60:63], v[132:135]\n"
      "v_cvt_pk_bf16_f32 v210, v136, v137\n"
      "v_mfma_f32_16x16x32_bf16 v[132:135], a[144:147], v[64:67], v[132:135]\n"
      "v_cvt_pk_bf16_f32 v211, v138, v139\n"
      "v_mfma_f32_16x16x32_bf16 v[132:135], a[148:151], v[68:71], v[132:135]\n"
      "v_mfma_f32_16x16x32_bf16 v[132:135], a[152:155], v[84:87], v[132:135]\n"
      "s_waitcnt lgkmcnt(3)\n"
      "v_mfma_f32_16x16x32_bf16 v[132:135], a[156:159], v[88:91], v[132:135]\n"
      "global_store_dwordx4 v252, v[220:223], s[54:55] offset:0 sc1\n"
      "v_mfma_f32_16x16x32_bf16 v[132:135], a[160:163], v[92:95], v[132:135]\n"
      "s_add_u32 m0, s17, 0x7f00\n"
      "v_mfma_f32_16x16x32_bf16 v[132:135], a[164:167], v[108:111], v[132:135]\n"
      "ds_write_b64 v244, v[210:211] offset:12296\n"
      "v_mfma_f32_16x16x32_bf16 v[132:135], a[168:171], v[112:115], v[132:135]\n"
      "global_load_lds_dwordx4 v236, s[52:53] offset:256 nt\n"
      "v_mfma_f32_16x16x32_bf16 v[132:135], a[172:175], v[116:119], v[132:135]\n"
      "v_mfma_f32_16x16x32_bf16 v[136:139], a[176:179], v[36:39], v[12:15]\n"
      "v_mfma_f32_16x16x32_bf16 v[136:139], a[180:183], v[40:43], v[136:139]\n"
      "v_mfma_f32_16x16x32_bf16 v[136:139], a[184:187], v[44:47], v[136:139]\n"
      "v_mfma_f32_16x16x32_bf16 v[136:139], a[188:191], v[60:63], v[136:139]\n"
      "v_cvt_pk_bf16_f32 v212, v132, v133\n"
      "v_mfma_f32_16x16x32_bf16 v[136:139], a[192:195], v[64:67], v[136:139]\n"
      "v_cvt_pk_bf16_f32 v213, v134, v135\n"
      "v_mfma_f32_16x16x32_bf16 v[136:139], a[196:199], v[68:71], v[136:139]\n"
      "v_mfma_f32_16x16x32_bf16 v[136:139], a[200:203], v[84:87], v[136:139]\n"
      "v_mfma_f32_16x16x32_bf16 v[136:139], a[204:207], v[88:91], v[136:139]\n"
      "s_waitcnt lgkmcnt(3)\n"
      "v_mfma_f32_16x16x32_bf16 v[136:139], a[208:211], v[92:95], v[136:139]\n"
      "global_store_dwordx4 v253, v[224:227], s[54:55] offset:0 sc1\n"
      "v_mfma_f32_16x16x32_bf16 v[136:139], a[212:215], v[108:111], v[136:139]\n"
      "ds_write_b64 v245, v[212:213] offset:12288\n"
      "v_mfma_f32_16x16x32_bf16 v[136:139], a[216:219], v[112:115], v[136:139]\n"
      "s_add_u32 m0, s17, 0x9e00\n"
      "v_mfma_f32_16x16x32_bf16 v[136:139], a[220:223], v[116:119], v[136:139]\n"
      "global_load_lds_dwordx4 v236, s[52:53] offset:512 nt\n"
      "v_mfma_f32_16x16x32_bf16 v[132:135], a[224:227], v[36:39], v[16:19]\n"
      "v_mfma_f32_16x16x32_bf16 v[132:135], a[228:231], v[40:43], v[132:135]\n"
      "v_mfma_f32_16x16x32_bf16 v[132:135], a[232:235], v[44:47], v[132:135]\n"
      "v_mfma_f32_16x16x32_bf16 v[132:135], a[236:239], v[60:63], v[132:135]\n"
      "v_cvt_pk_bf16_f32 v214, v136, v137\n"
      "v_mfma_f32_16x16x32_bf16 v[132:135], a[0:3], v[64:67], v[132:135]\n"
      "v_cvt_pk_bf16_f32 v215, v138, v139\n"
      "v_mfma_f32_16x16x32_bf16 v[132:135], a[4:7], v[68:71], v[132:135]\n"
      "v_mfma_f32_16x16x32_bf16 v[132:135], a[8:11], v[84:87], v[132:135]\n"
      "v_mfma_f32_16x16x32_bf16 v[132:135], a[12:15], v[88:91], v[132:135]\n"
      "v_mfma_f32_16x16x32_bf16 v[132:135], a[16:19], v[92:95], v[132:135]\n"
      "v_mfma_f32_16x16x32_bf16 v[132:135], a[20:23], v[108:111], v[132:135]\n"
      "ds_write_b64 v245, v[214:215] offset:12296\n"
      "v_mfma_f32_16x16x32_bf16 v[132:135], v[140:143], v[112:115], v[132:135]\n"
      "v_mfma_f32_16x16x32_bf16 v[132:135], v[156:159], v[116:119], v[132:135]\n"
      "s_waitcnt lgkmcnt(4)\n"
      "s_barrier\n"
      "v_mfma_f32_16x16x32_bf16 v[136:139], v[160:163], v[36:39], v[20:23]\n"
      "ds_read_b128 v[36:39], v232 offset:256\n"
      "v_mfma_f32_16x16x32_bf16 v[136:139], v[164:167], v[40:43], v[136:139]\n"
      "ds_read_b128 v[40:43], v233 offset:256\n"
      "v_mfma_f32_16x16x32_bf16 v[136:139], v[180:183], v[44:47], v[136:139]\n"
      "ds_read_b128 v[44:47], v234 offset:256\n"
      "v_mfma_f32_16x16x32_bf16 v[136:139], v[184:187], v[60:63], v[136:139]\n"
      "ds_read_b128 v[60:63], v235 offset:256\n"
      "v_cvt_pk_bf16_f32 v208, v132, v133\n"
      "v_mfma_f32_16x16x32_bf16 v[136:139], v[188:191], v[64:67], v[136:139]\n"
      "ds_read_b128 v[64:67], v232 offset:8448\n"
      "v_cvt_pk_bf16_f32 v209, v134, v135\n"
      "v_mfma_f32_16x16x32_bf16 v[136:139], v[204:207], v[68:71], v[136:139]\n"
      "ds_read_b128 v[68:71], v233 offset:8448\n"
      "v_mfma_f32_16x16x32_bf16 v[136:139], a[24:27], v[84:87], v[136:139]\n"
      "ds_read_b128 v[84:87], v234 offset:8448\n"
      "v_mfma_f32_16x16x32_bf16 v[136:139], a[28:31], v[88:91], v[136:139]\n"
      "ds_read_b128 v[88:91], v235 offset:8448\n"
      "v_mfma_f32_16x16x32_bf16 v[136:139], a[32:35], v[92:95], v[136:139]\n"
      "ds_read_b128 v[92:95], v232 offset:16640\n"
      "v_mfma_f32_16x16x32_bf16 v[136:139], a[36:39], v[108:111], v[136:139]\n"
      "ds_read_b128 v[108:111], v233 offset:16640\n"
      "v_mfma_f32_16x16x32_bf16 v[136:139], a[40:43], v[112:115], v[136:139]\n"
      "ds_read_b128 v[112:115], v234 offset:16640\n"
      "v_mfma_f32_16x16x32_bf16 v[136:139], a[44:47], v[116:119], v[136:139]\n"
      "ds_read_b128 v[116:119], v235 offset:16640\n"
      "s_waitcnt lgkmcnt(8)\n"
      "v_mfma_f32_16x16x32_bf16 v[132:135], a[240:243], v[36:39], v[0:3]\n"
      "ds_write_b64 v244, v[208:209] offset:12416\n"
      "v_mfma_f32_16x16x32_bf16 v[132:135], a[244:247], v[40:43], v[132:135]\n"
      "v_mfma_f32_16x16x32_bf16 v[132:135], a[248:251], v[44:47], v[132:135]\n"
      "v_mfma_f32_16x16x32_bf16 v[132:135], a[252:255], v[60:63], v[132:135]\n"
      "v_cvt_pk_bf16_f32 v210, v136, v137\n"
      "s_waitcnt lgkmcnt(5)\n"
      "v_mfma_f32_16x16x32_bf16 v[132:135], a[48:51], v[64:67], v[132:135]\n"
      "v_cvt_pk_bf16_f32 v211, v138, v139\n"
      "v_mfma_f32_16x16x32_bf16 v[132:135], a[52:55], v[68:71], v[132:135]\n"
      "v_mfma_f32_16x16x32_bf16 v[132:135], a[56:59], v[84:87], v[132:135]\n"
      "ds_read_b128 v[216:219], v246 offset:0\n"
      "v_mfma_f32_16x16x32_bf16 v[132:135], a[60:63], v[88:91], v[132:135]\n"
      "ds_read_b128 v[220:223], v246 offset:1024\n"
      "s_waitcnt lgkmcnt(3)\n"
      "v_mfma_f32_16x16x32_bf16 v[132:135], a[64:67], v[92:95], v[132:135]\n"
      "ds_read_b128 v[224:227], v246 offset:2048\n"
      "v_mfma_f32_16x16x32_bf16 v[132:135], a[68:71], v[108:111], v[132:135]\n"
      "ds_write_b64 v244, v[210:211] offset:12424\n"
      "v_mfma_f32_16x16x32_bf16 v[132:135], a[72:75], v[112:115], v[132:135]\n"
      "v_mfma_f32_16x16x32_bf16 v[132:135], a[76:79], v[116:119], v[132:135]\n"
      "v_mfma_f32_16x16x32_bf16 v[136:139], a[80:83], v[36:39], v[4:7]\n"
      "v_mfma_f32_16x16x32_bf16 v[136:139], a[84:87], v[40:43], v[136:139]\n"
      "v_mfma_f32_16x16x32_bf16 v[136:139], a[88:91], v[44:47], v[136:139]\n"
      "v_mfma_f32_16x16x32_bf16 v[136:139], a[92:95], v[60:63], v[136:139]\n"
      "v_cvt_pk_bf16_f32 v208, v132, v133\n"
      "v_mfma_f32_16x16x32_bf16 v[136:139], a[96:99], v[64:67], v[136:139]\n"
      "v_cvt_pk_bf16_f32 v209, v134, v135\n"
      "v_mfma_f32_16x16x32_bf16 v[136:139], a[100:103], v[68:71], v[136:139]\n"
      "s_waitcnt lgkmcnt(3)\n"
      "v_mfma_f32_16x16x32_bf16 v[136:139], a[104:107], v[84:87], v[136:139]\n"
      "global_store_dwordx4 v247, v[216:219], s[54:55] offset:768 sc1\n"
      "v_mfma_f32_16x16x32_bf16 v[136:139], a[108:111], v[88:91], v[136:139]\n"
      "s_add_u32 m0, s17, 0x7000\n"
      "v_mfma_f32_16x16x32_bf16 v[136:139], a[112:115], v[92:95], v[136:139]\n"
      "global_load_lds_dwordx4 v237, s[52:53] offset:0 nt\n"
      "v_mfma_f32_16x16x32_bf16 v[136:139], a[116:119], v[108:111], v[136:139]\n"
      "ds_write_b64 v244, v[208:209] offset:24576\n"
      "v_mfma_f32_16x16x32_bf16 v[136:139], a[120:123], v[112:115], v[136:139]\n"
      "v_mfma_f32_16x16x32_bf16 v[136:139], a[124:127], v[116:119], v[136:139]\n"
      "v_mfma_f32_16x16x32_bf16 v[132:135], a[128:131], v[36:39], v[8:11]\n"
      "v_mfma_f32_16x16x32_bf16 v[132:135], a[132:135], v[40:43], v[132:135]\n"
      "v_mfma_f32_16x16x32_bf16 v[132:135], a[136:139], v[44:47], v[132:135]\n"
      "v_mfma_f32_16x16x32_bf16 v[132:135], a[140:143], v[60:63], v[132:135]\n"
      "v_cvt_pk_bf16_f32 v210, v136, v137\n"
      "v_mfma_f32_16x16x32_bf16 v[132:135], a[144:147], v[64:67], v[132:135]\n"
      "v_cvt_pk_bf16_f32 v211, v138, v139\n"
      "v_mfma_f32_16x16x32_bf16 v[132:135], a[148:151], v[68:71], v[132:135]\n"
      "v_mfma_f32_16x16x32_bf16 v[132:135], a[152:155], v[84:87], v[132:135]\n"
      "s_waitcnt lgkmcnt(3)\n"
      "v_mfma_f32_16x16x32_bf16 v[132:135], a[156:159], v[88:91], v[132:135]\n"
      "global_store_dwordx4 v252, v[220:223], s[54:55] offset:768 sc1\n"
      "v_mfma_f32_16x16x32_bf16 v[132:135], a[160:163], v[92:95], v[132:135]\n"
      "s_add_u32 m0, s17, 0x8f00\n"
      "v_mfma_f32_16x16x32_bf16 v[132:135], a[164:167], v[108:111], v[132:135]\n"
      "ds_write_b64 v244, v[210:211] offset:24584\n"
      "v_mfma_f32_16x16x32_bf16 v[132:135], a[168:171], v[112:115], v[132:135]\n"
      "global_load_lds_dwordx4 v237, s[52:53] offset:256 nt\n"
      "v_mfma_f32_16x16x32_bf16 v[132:135], a[172:175], v[116:119], v[132:135]\n"
      "v_mfma_f32_16x16x32_bf16 v[136:139], a[176:179], v[36:39], v[12:15]\n"
      "v_mfma_f32_16x16x32_bf16 v[136:139], a[180:183], v[40:43], v[136:139]\n"
      "v_mfma_f32_16x16x32_bf16 v[136:139], a[184:187], v[44:47], v[136:139]\n"
      "v_mfma_f32_16x16x32_bf16 v[136:139], a[188:191], v[60:63], v[136:139]\n"
      "v_cvt_pk_bf16_f32 v212, v132, v133\n"
      "v_mfma_f32_16x16x32_bf16 v[136:139], a[192:195], v[64:67], v[136:139]\n"
      "v_cvt_pk_bf16_f32 v213, v134, v135\n"
      "v_mfma_f32_16x16x32_bf16 v[136:139], a[196:199], v[68:71], v[136:139]\n"
      "v_mfma_f32_16x16x32_bf16 v[136:139], a[200:203], v[84:87], v[136:139]\n"
      "v_mfma_f32_16x16x32_bf16 v[136:139], a[204:207], v[88:91], v[136:139]\n"
      "s_waitcnt lgkmcnt(3)\n"
      "v_mfma_f32_16x16x32_bf16 v[136:139], a[208:211], v[92:95], v[136:139]\n"
      "global_store_dwordx4 v253, v[224:227], s[54:55] offset:768 sc1\n"
      "v_mfma_f32_16x16x32_bf16 v[136:139], a[212:215], v[108:111], v[136:139]\n"
      "ds_write_b64 v245, v[212:213] offset:24576\n"
      "v_mfma_f32_16x16x32_bf16 v[136:139], a[216:219], v[112:115], v[136:139]\n"
      "s_add_u32 m0, s17, 0xae00\n"
      "v_mfma_f32_16x16x32_bf16 v[136:139], a[220:223], v[116:119], v[136:139]\n"
      "global_load_lds_dwordx4 v237, s[52:53] offset:512 nt\n"
      "v_mfma_f32_16x16x32_bf16 v[132:135], a[224:227], v[36:39], v[16:19]\n"
      "v_mfma_f32_16x16x32_bf16 v[132:135], a[228:231], v[40:43], v[132:135]\n"
      "v_mfma_f32_16x16x32_bf16 v[132:135], a[232:235], v[44:47], v[132:135]\n"
      "v_mfma_f32_16x16x32_bf16 v[132:135], a[236:239], v[60:63], v[132:135]\n"
      "v_cvt_pk_bf16_f32 v214, v136, v137\n"
      "v_mfma_f32_16x16x32_bf16 v[132:135], a[0:3], v[64:67], v[132:135]\n"
      "v_cvt_pk_bf16_f32 v215, v138, v139\n"
      "v_mfma_f32_16x16x32_bf16 v[132:135], a[4:7], v[68:71], v[132:135]\n"
      "v_mfma_f32_16x16x32_bf16 v[132:135], a[8:11], v[84:87], v[132:135]\n"
      "v_mfma_f32_16x16x32_bf16 v[132:135], a[12:15], v[88:91], v[132:135]\n"
      "v_mfma_f32_16x16x32_bf16 v[132:135], a[16:19], v[92:95], v[132:135]\n"
      "v_mfma_f32_16x16x32_bf16 v[132:135], a[20:23], v[108:111], v[132:135]\n"
      "ds_write_b64 v245, v[214:215] offset:24584\n"
      "v_mfma_f32_16x16x32_bf16 v[132:135], v[140:143], v[112:115], v[132:135]\n"
      "s_lshl_b32 s79, s92, 1\n"
      "v_mfma_f32_16x16x32_bf16 v[132:135], v[156:159], v[116:119], v[132:135]\n"
      "s_waitcnt vmcnt(25) lgkmcnt(4)\n"
      "s_barrier\n"
      "v_mad_i32_i24 v252, v251, s79, v252\n"
      "v_mfma_f32_16x16x32_bf16 v[136:139], v[160:163], v[36:39], v[20:23]\n"
      "ds_read_b128 v[36:39], v232 offset:24576\n"
      "s_mul_i32 s79, s79, s100\n"
      "v_mfma_f32_16x16x32_bf16 v[136:139], v[164:167], v[40:43], v[136:139]\n"
      "ds_read_b128 v[40:43], v233 offset:24576\n"
      "v_add_u32 v247, s79, v247\n"
      "v_mfma_f32_16x16x32_bf16 v[136:139], v[180:183], v[44:47], v[136:139]\n"
      "ds_read_b128 v[44:47], v234 offset:24576\n"
      "s_mul_i32 s79, s79, s101\n"
      "v_mfma_f32_16x16x32_bf16 v[136:139], v[184:187], v[60:63], v[136:139]\n"
      "ds_read_b128 v[60:63], v235 offset:24576\n"
      "v_cvt_pk_bf16_f32 v208, v132, v133\n"
      "v_mfma_f32_16x16x32_bf16 v[136:139], v[188:191], v[64:67], v[136:139]\n"
      "ds_read_b128 v[64:67], v232 offset:32768\n"
      "v_cvt_pk_bf16_f32 v209, v134, v135\n"
      "v_mfma_f32_16x16x32_bf16 v[136:139], v[204:207], v[68:71], v[136:139]\n"
      "ds_read_b128 v[68:71], v233 offset:32768\n"
      "v_add_u32 v253, s79, v253\n"
      "v_mfma_f32_16x16x32_bf16 v[136:139], a[24:27], v[84:87], v[136:139]\n"
      "ds_read_b128 v[84:87], v234 offset:32768\n"
      "v_mfma_f32_16x16x32_bf16 v[136:139], a[28:31], v[88:91], v[136:139]\n"
      "ds_read_b128 v[88:91], v235 offset:32768\n"
      "v_mfma_f32_16x16x32_bf16 v[136:139], a[32:35], v[92:95], v[136:139]\n"
      "ds_read_b128 v[92:95], v232 offset:40960\n"
      "v_mfma_f32_16x16x32_bf16 v[136:139], a[36:39], v[108:111], v[136:139]\n"
      "ds_read_b128 v[108:111], v233 offset:40960\n"
      "v_mfma_f32_16x16x32_bf16 v[136:139], a[40:43], v[112:115], v[136:139]\n"
      "ds_read_b128 v[112:115], v234 offset:40960\n"
      "v_mfma_f32_16x16x32_bf16 v[136:139], a[44:47], v[116:119], v[136:139]\n"
      "ds_read_b128 v[116:119], v235 offset:40960\n"
      "s_waitcnt lgkmcnt(8)\n"
      "v_mfma_f32_16x16x32_bf16 v[132:135], a[240:243], v[36:39], v[0:3]\n"
      "ds_write_b64 v244, v[208:209] offset:24704\n"
      "v_mfma_f32_16x16x32_bf16 v[132:135], a[244:247], v[40:43], v[132:135]\n"
      "s_add_u32 s54, s30, s35\n"
      "s_addc_u32 s55, s31, 0\n"
      "v_mfma_f32_16x16x32_bf16 v[132:135], a[248:251], v[44:47], v[132:135]\n"
      "v_mfma_f32_16x16x32_bf16 v[132:135], a[252:255], v[60:63], v[132:135]\n"
      "v_cvt_pk_bf16_f32 v210, v136, v137\n"
      "s_waitcnt lgkmcnt(5)\n"
      "v_mfma_f32_16x16x32_bf16 v[132:135], a[48:51], v[64:67], v[132:135]\n"
      "v_cvt_pk_bf16_f32 v211, v138, v139\n"
      "v_mfma_f32_16x16x32_bf16 v[132:135], a[52:55], v[68:71], v[132:135]\n"
      "s_mul_i32 s78, s94, s19\n"
      "v_mfma_f32_16x16x32_bf16 v[132:135], a[56:59], v[84:87], v[132:135]\n"
      "ds_read_b128 v[216:219], v246 offset:12288\n"
      "v_add_u32 v236, s78, v236\n"
      "v_mfma_f32_16x16x32_bf16 v[132:135], a[60:63], v[88:91], v[132:135]\n"
      "ds_read_b128 v[220:223], v246 offset:13312\n"
      "s_waitcnt lgkmcnt(3)\n"
      "v_mfma_f32_16x16x32_bf16 v[132:135], a[64:67], v[92:95], v[132:135]\n"
      "ds_read_b128 v[224:227], v246 offset:14336\n"
      "v_mfma_f32_16x16x32_bf16 v[132:135], a[68:71], v[108:111], v[132:135]\n"
      "ds_write_b64 v244, v[210:211] offset:24712\n"
      "v_mfma_f32_16x16x32_bf16 v[132:135], a[72:75], v[112:115], v[132:135]\n"
      "s_mul_i32 s78, s78, s99\n"
      "v_mfma_f32_16x16x32_bf16 v[132:135], a[76:79], v[116:119], v[132:135]\n"
      "v_add_u32 v237, s78, v237\n"
      "v_mfma_f32_16x16x32_bf16 v[136:139], a[80:83], v[36:39], v[4:7]\n"
      "v_mfma_f32_16x16x32_bf16 v[136:139], a[84:87], v[40:43], v[136:139]\n"
      "v_mfma_f32_16x16x32_bf16 v[136:139], a[88:91], v[44:47], v[136:139]\n"
      "v_mfma_f32_16x16x32_bf16 v[136:139], a[92:95], v[60:63], v[136:139]\n"
      "v_cvt_pk_bf16_f32 v208, v132, v133\n"
      "v_mfma_f32_16x16x32_bf16 v[136:139], a[96:99], v[64:67], v[136:139]\n"
      "v_cvt_pk_bf16_f32 v209, v134, v135\n"
      "v_mfma_f32_16x16x32_bf16 v[136:139], a[100:103], v[68:71], v[136:139]\n"
      "s_waitcnt lgkmcnt(3)\n"
      "v_mfma_f32_16x16x32_bf16 v[136:139], a[104:107], v[84:87], v[136:139]\n"
      "global_store_dwordx4 v247, v[216:219], s[54:55] offset:0 sc1\n"
      "v_mfma_f32_16x16x32_bf16 v[136:139], a[108:111], v[88:91], v[136:139]\n"
      "s_add_u32 s52, s20, s37\n"
      "s_addc_u32 s53, s21, 0\n"
      "v_mfma_f32_16x16x32_bf16 v[136:139], a[112:115], v[92:95], v[136:139]\n"
      "s_add_u32 m0, s17, 0xc000\n"
      "v_mfma_f32_16x16x32_bf16 v[136:139], a[116:119], v[108:111], v[136:139]\n"
      "ds_write_b64 v244, v[208:209] offset:0\n"
      "v_mfma_f32_16x16x32_bf16 v[136:139], a[120:123], v[112:115], v[136:139]\n"
      "global_load_lds_dwordx4 v236, s[52:53] offset:0 nt\n"
      "v_mfma_f32_16x16x32_bf16 v[136:139], a[124:127], v[116:119], v[136:139]\n"
      "v_mfma_f32_16x16x32_bf16 v[132:135], a[128:131], v[36:39], v[8:11]\n"
      "v_mfma_f32_16x16x32_bf16 v[132:135], a[132:135], v[40:43], v[132:135]\n"
      "v_mfma_f32_16x16x32_bf16 v[132:135], a[136:139], v[44:47], v[132:135]\n"
      "v_mfma_f32_16x16x32_bf16 v[132:135], a[140:143], v[60:63], v[132:135]\n"
      "v_cvt_pk_bf16_f32 v210, v136, v137\n"
      "v_mfma_f32_16x16x32_bf16 v[132:135], a[144:147], v[64:67], v[132:135]\n"
      "v_cvt_pk_bf16_f32 v211, v138, v139\n"
      "v_mfma_f32_16x16x32_bf16 v[132:135], a[148:151], v[68:71], v[132:135]\n"
      "v_mfma_f32_16x16x32_bf16 v[132:135], a[152:155], v[84:87], v[132:135]\n"
      "s_waitcnt lgkmcnt(3)\n"
      "v_mfma_f32_16x16x32_bf16 v[132:135], a[156:159], v[88:91], v[132:135]\n"
      "global_store_dwordx4 v252, v[220:223], s[54:55] offset:0 sc1\n"
      "v_mfma_f32_16x16x32_bf16 v[132:135], a[160:163], v[92:95], v[132:135]\n"
      "s_add_u32 m0, s17, 0xdf00\n"
      "v_mfma_f32_16x16x32_bf16 v[132:135], a[164:167], v[108:111], v[132:135]\n"
      "ds_write_b64 v244, v[210:211] offset:8\n"
      "v_mfma_f32_16x16x32_bf16 v[132:135], a[168:171], v[112:115], v[132:135]\n"
      "global_load_lds_dwordx4 v236, s[52:53] offset:256 nt\n"
      "v_mfma_f32_16x16x32_bf16 v[132:135], a[172:175], v[116:119], v[132:135]\n"
      "v_mfma_f32_16x16x32_bf16 v[136:139], a[176:179], v[36:39], v[12:15]\n"
      "v_mfma_f32_16x16x32_bf16 v[136:139], a[180:183], v[40:43], v[136:139]\n"
      "v_mfma_f32_16x16x32_bf16 v[136:139], a[184:187], v[44:47], v[136:139]\n"
      "v_mfma_f32_16x16x32_bf16 v[136:139], a[188:191], v[60:63], v[136:139]\n"
      "v_cvt_pk_bf16_f32 v212, v132, v133\n"
      "v_mfma_f32_16x16x32_bf16 v[136:139], a[192:195], v[64:67], v[136:139]\n"
      "v_cvt_pk_bf16_f32 v213, v134, v135\n"
      "v_mfma_f32_16x16x32_bf16 v[136:139], a[196:199], v[68:71], v[136:139]\n"
      "v_mfma_f32_16x16x32_bf16 v[136:139], a[200:203], v[84:87], v[136:139]\n"
      "v_mfma_f32_16x16x32_bf16 v[136:139], a[204:207], v[88:91], v[136:139]\n"
      "s_waitcnt lgkmcnt(3)\n"
      "v_mfma_f32_16x16x32_bf16 v[136:139], a[208:211], v[92:95], v[136:139]\n"
      "global_store_dwordx4 v253, v[224:227], s[54:55] offset:0 sc1\n"
      "v_mfma_f32_16x16x32_bf16 v[136:139], a[212:215], v[108:111], v[136:139]\n"
      "ds_write_b64 v245, v[212:213] offset:0\n"
      "v_mfma_f32_16x16x32_bf16 v[136:139], a[216:219], v[112:115], v[136:139]\n"
      "s_add_u32 m0, s17, 0xfe00\n"
      "v_mfma_f32_16x16x32_bf16 v[136:139], a[220:223], v[116:119], v[136:139]\n"
      "global_load_lds_dwordx4 v236, s[52:53] offset:512 nt\n"
      "v_mfma_f32_16x16x32_bf16 v[132:135], a[224:227], v[36:39], v[16:19]\n"
      "v_mfma_f32_16x16x32_bf16 v[132:135], a[228:231], v[40:43], v[132:135]\n"
      "v_mfma_f32_16x16x32_bf16 v[132:135], a[232:235], v[44:47], v[132:135]\n"
      "v_mfma_f32_16x16x32_bf16 v[132:135], a[236:239], v[60:63], v[132:135]\n"
      "v_cvt_pk_bf16_f32 v214, v136, v137\n"
      "v_mfma_f32_16x16x32_bf16 v[132:135], a[0:3], v[64:67], v[132:135]\n"
      "v_cvt_pk_bf16_f32 v215, v138, v139\n"
      "v_mfma_f32_16x16x32_bf16 v[132:135], a[4:7], v[68:71], v[132:135]\n"
      "v_mfma_f32_16x16x32_bf16 v[132:135], a[8:11], v[84:87], v[132:135]\n"
      "v_mfma_f32_16x16x32_bf16 v[132:135], a[12:15], v[88:91], v[132:135]\n"
      "v_mfma_f32_16x16x32_bf16 v[132:135], a[16:19], v[92:95], v[132:135]\n"
      "v_mfma_f32_16x16x32_bf16 v[132:135], a[20:23], v[108:111], v[132:135]\n"
      "ds_write_b64 v245, v[214:215] offset:8\n"
      "v_mfma_f32_16x16x32_bf16 v[132:135], v[140:143], v[112:115], v[132:135]\n"
      "v_mfma_f32_16x16x32_bf16 v[132:135], v[156:159], v[116:119], v[132:135]\n"
      "s_waitcnt lgkmcnt(4)\n"
      "s_barrier\n"
      "v_mfma_f32_16x16x32_bf16 v[136:139], v[160:163], v[36:39], v[20:23]\n"
      "ds_read_b128 v[36:39], v232 offset:24832\n"
      "v_mfma_f32_16x16x32_bf16 v[136:139], v[164:167], v[40:43], v[136:139]\n"
      "ds_read_b128 v[40:43], v233 offset:24832\n"
      "v_mfma_f32_16x16x32_bf16 v[136:139], v[180:183], v[44:47], v[136:139]\n"
      "ds_read_b128 v[44:47], v234 offset:24832\n"
      "v_mfma_f32_16x16x32_bf16 v[136:139], v[184:187], v[60:63], v[136:139]\n"
      "ds_read_b128 v[60:63], v235 offset:24832\n"
      "v_cvt_pk_bf16_f32 v208, v132, v133\n"
      "v_mfma_f32_16x16x32_bf16 v[136:139], v[188:191], v[64:67], v[136:139]\n"
      "ds_read_b128 v[64:67], v232 offset:33024\n"
      "v_cvt_pk_bf16_f32 v209, v134, v135\n"
      "v_mfma_f32_16x16x32_bf16 v[136:139], v[204:207], v[68:71], v[136:139]\n"
      "ds_read_b128 v[68:71], v233 offset:33024\n"
      "v_mfma_f32_16x16x32_bf16 v[136:139], a[24:27], v[84:87], v[136:139]\n"
      "ds_read_b128 v[84:87], v234 offset:33024\n"
      "v_mfma_f32_16x16x32_bf16 v[136:139], a[28:31], v[88:91], v[136:139]\n"
      "ds_read_b128 v[88:91], v235 offset:33024\n"
      "v_mfma_f32_16x16x32_bf16 v[136:139], a[32:35], v[92:95], v[136:139]\n"
      "ds_read_b128 v[92:95], v232 offset:41216\n"
      "v_mfma_f32_16x16x32_bf16 v[136:139], a[36:39], v[108:111], v[136:139]\n"
      "ds_read_b128 v[108:111], v233 offset:41216\n"
      "ds_write_b64 v244, v[208:209] offset:128\n"
      "v_mfma_f32_16x16x32_bf16 v[136:139], a[40:43], v[112:115], v[136:139]\n"
      "ds_read_b128 v[112:115], v234 offset:41216\n"
      "v_mfma_f32_16x16x32_bf16 v[136:139], a[44:47], v[116:119], v[136:139]\n"
      "ds_read_b128 v[116:119], v235 offset:41216\n"
      "s_waitcnt lgkmcnt(9)\n"
      "v_mfma_f32_16x16x32_bf16 v[132:135], a[240:243], v[36:39], v[0:3]\n"
      "v_add_u32 v232, 0xffff4000, v232\n"
      "v_mfma_f32_16x16x32_bf16 v[132:135], a[244:247], v[40:43], v[132:135]\n"
      "v_add_u32 v233, 0xffff4000, v233\n"
      "v_mfma_f32_16x16x32_bf16 v[132:135], a[248:251], v[44:47], v[132:135]\n"
      "v_add_u32 v234, 0xffff4000, v234\n"
      "v_mfma_f32_16x16x32_bf16 v[132:135], a[252:255], v[60:63], v[132:135]\n"
      "v_add_u32 v235, 0xffff4000, v235\n"
      "s_waitcnt lgkmcnt(5)\n"
      "v_mfma_f32_16x16x32_bf16 v[132:135], a[48:51], v[64:67], v[132:135]\n"
      "v_cvt_pk_bf16_f32 v210, v136, v137\n"
      "v_mfma_f32_16x16x32_bf16 v[132:135], a[52:55], v[68:71], v[132:135]\n"
      "v_cvt_pk_bf16_f32 v211, v138, v139\n"
      "v_mfma_f32_16x16x32_bf16 v[132:135], a[56:59], v[84:87], v[132:135]\n"
      "ds_read_b128 v[216:219], v246 offset:24576\n"
      "v_mfma_f32_16x16x32_bf16 v[132:135], a[60:63], v[88:91], v[132:135]\n"
      "ds_read_b128 v[220:223], v246 offset:25600\n"
      "s_waitcnt lgkmcnt(2)\n"
      "v_mfma_f32_16x16x32_bf16 v[132:135], a[64:67], v[92:95], v[132:135]\n"
      "ds_read_b128 v[224:227], v246 offset:26624\n"
      "v_mfma_f32_16x16x32_bf16 v[132:135], a[68:71], v[108:111], v[132:135]\n"
      "v_lshlrev_b32 v0, 16, v96\n"
      "v_mfma_f32_16x16x32_bf16 v[132:135], a[72:75], v[112:115], v[132:135]\n"
      "ds_write_b64 v244, v[210:211] offset:136\n"
      "v_mfma_f32_16x16x32_bf16 v[132:135], a[76:79], v[116:119], v[132:135]\n"
      "v_and_b32 v1, 0xffff0000, v96\n"
      "v_mfma_f32_16x16x32_bf16 v[136:139], a[80:83], v[36:39], v[4:7]\n"
      "v_lshlrev_b32 v2, 16, v97\n"
      "v_mfma_f32_16x16x32_bf16 v[136:139], a[84:87], v[40:43], v[136:139]\n"
      "v_and_b32 v3, 0xffff0000, v97\n"
      "v_mfma_f32_16x16x32_bf16 v[136:139], a[88:91], v[44:47], v[136:139]\n"
      "v_mfma_f32_16x16x32_bf16 v[136:139], a[92:95], v[60:63], v[136:139]\n"
      "v_cvt_pk_bf16_f32 v208, v132, v133\n"
      "v_mfma_f32_16x16x32_bf16 v[136:139], a[96:99], v[64:67], v[136:139]\n"
      "v_cvt_pk_bf16_f32 v209, v134, v135\n"
      "v_mfma_f32_16x16x32_bf16 v[136:139], a[100:103], v[68:71], v[136:139]\n"
      "s_waitcnt lgkmcnt(3)\n"
      "v_mfma_f32_16x16x32_bf16 v[136:139], a[104:107], v[84:87], v[136:139]\n"
      "global_store_dwordx4 v247, v[216:219], s[54:55] offset:768 sc1\n"
      "v_mfma_f32_16x16x32_bf16 v[136:139], a[108:111], v[88:91], v[136:139]\n"
      "s_add_u32 m0, s17, 0xd000\n"
      "v_mfma_f32_16x16x32_bf16 v[136:139], a[112:115], v[92:95], v[136:139]\n"
      "s_waitcnt vmcnt(19)\n"
      "s_sub_u32 s49, s48, 1000\n"
      "v_mfma_f32_16x16x32_bf16 v[136:139], a[116:119], v[108:111], v[136:139]\n"
      "v_mov_b32 v53, 1100\n"
      "ds_write_b64 v244, v[208:209] offset:12288\n"
      "v_mfma_f32_16x16x32_bf16 v[136:139], a[120:123], v[112:115], v[136:139]\n"
      "v_xor_b32 v48, s80, v48\n"
      "v_mfma_f32_16x16x32_bf16 v[136:139], a[124:127], v[116:119], v[136:139]\n"
      "v_subrev_u32 v48, s49, v48\n"
      "v_mfma_f32_16x16x32_bf16 v[132:135], a[128:131], v[36:39], v[8:11]\n"
      "v_cmp_gt_u32 vcc, 2000, v48\n"
      "v_cndmask_b32 v48, v53, v48, vcc\n"
      "v_mfma_f32_16x16x32_bf16 v[132:135], a[132:135], v[40:43], v[132:135]\n"
      "global_load_lds_dwordx4 v237, s[52:53] offset:0 nt\n"
      "v_mfma_f32_16x16x32_bf16 v[132:135], a[136:139], v[44:47], v[132:135]\n"
      "v_add_u32_dpp v48, v48, v48 row_shr:1 row_mask:0xf bank_mask:0xf bound_ctrl:0\n"
      "v_mfma_f32_16x16x32_bf16 v[132:135], a[140:143], v[60:63], v[132:135]\n"
      "v_cvt_pk_bf16_f32 v210, v136, v137\n"
      "v_mfma_f32_16x16x32_bf16 v[132:135], a[144:147], v[64:67], v[132:135]\n"
      "v_add_u32_dpp v48, v48, v48 row_shr:2 row_mask:0xf bank_mask:0xf bound_ctrl:0\n"
      "v_mfma_f32_16x16x32_bf16 v[132:135], a[148:151], v[68:71], v[132:135]\n"
      "v_cvt_pk_bf16_f32 v211, v138, v139\n"
      "v_mfma_f32_16x16x32_bf16 v[132:135], a[152:155], v[84:87], v[132:135]\n"
      "v_add_u32_dpp v48, v48, v48 row_shr:4 row_mask:0xf bank_mask:0xf bound_ctrl:0\n"
      "v_mfma_f32_16x16x32_bf16 v[132:135], a[156:159], v[88:91], v[132:135]\n"
      "s_waitcnt lgkmcnt(3)\n"
      "v_mfma_f32_16x16x32_bf16 v[132:135], a[160:163], v[92:95], v[132:135]\n"
      "v_add_u32_dpp v48, v48, v48 row_shr:8 row_mask:0xf bank_mask:0xf bound_ctrl:0\n"
      "v_mfma_f32_16x16x32_bf16 v[132:135], a[164:167], v[108:111], v[132:135]\n"
      "global_store_dwordx4 v252, v[220:223], s[54:55] offset:768 sc1\n"
      "v_mfma_f32_16x16x32_bf16 v[132:135], a[168:171], v[112:115], v[132:135]\n"
      "v_readlane_b32 s56, v48, 15\n"
      "v_mfma_f32_16x16x32_bf16 v[132:135], a[172:175], v[116:119], v[132:135]\n"
      "ds_write_b64 v244, v[210:211] offset:12296\n"
      "v_mfma_f32_16x16x32_bf16 v[136:139], a[176:179], v[36:39], v[12:15]\n"
      "s_add_u32 m0, s17, 0xef00\n"
      "v_mfma_f32_16x16x32_bf16 v[136:139], a[180:183], v[40:43], v[136:139]\n"
      "s_lshr_b32 s56, s56, 4\n"
      "v_mfma_f32_16x16x32_bf16 v[136:139], a[184:187], v[44:47], v[136:139]\n"
      "s_add_u32 s56, s56, 60\n"
      "v_mfma_f32_16x16x32_bf16 v[136:139], a[188:191], v[60:63], v[136:139]\n"
      "s_add_u32 s49, s49, s56\n"
      "v_mfma_f32_16x16x32_bf16 v[136:139], a[192:195], v[64:67], v[136:139]\n"
      "v_cvt_pk_bf16_f32 v212, v132, v133\n"
      "v_mfma_f32_16x16x32_bf16 v[136:139], a[196:199], v[68:71], v[136:139]\n"
      "v_cvt_pk_bf16_f32 v213, v134, v135\n"
      "v_mfma_f32_16x16x32_bf16 v[136:139], a[200:203], v[84:87], v[136:139]\n"
      "global_load_lds_dwordx4 v237, s[52:53] offset:256 nt\n"
      "v_mfma_f32_16x16x32_bf16 v[136:139], a[204:207], v[88:91], v[136:139]\n"
      "s_waitcnt lgkmcnt(3)\n"
      "v_mfma_f32_16x16x32_bf16 v[136:139], a[208:211], v[92:95], v[136:139]\n"
      "global_store_dwordx4 v253, v[224:227], s[54:55] offset:768 sc1\n"
      "v_mfma_f32_16x16x32_bf16 v[136:139], a[212:215], v[108:111], v[136:139]\n"
      "v_lshlrev_b32 v4, 16, v98\n"
      "v_mfma_f32_16x16x32_bf16 v[136:139], a[216:219], v[112:115], v[136:139]\n"
      "ds_write_b64 v245, v[212:213] offset:12288\n"
      "v_mfma_f32_16x16x32_bf16 v[136:139], a[220:223], v[116:119], v[136:139]\n"
      "v_and_b32 v5, 0xffff0000, v98\n"
      "v_mfma_f32_16x16x32_bf16 v[132:135], a[224:227], v[36:39], v[16:19]\n"
      "v_lshlrev_b32 v6, 16, v99\n"
      "v_mfma_f32_16x16x32_bf16 v[132:135], a[228:231], v[40:43], v[132:135]\n"
      "v_and_b32 v7, 0xffff0000, v99\n"
      "v_mfma_f32_16x16x32_bf16 v[132:135], a[232:235], v[44:47], v[132:135]\n"
      "v_lshlrev_b32 v8, 16, v100\n"
      "v_mfma_f32_16x16x32_bf16 v[132:135], a[236:239], v[60:63], v[132:135]\n"
      "s_memrealtime s[84:85]\n"
      "v_mfma_f32_16x16x32_bf16 v[132:135], a[0:3], v[64:67], v[132:135]\n"
      "v_cvt_pk_bf16_f32 v214, v136, v137\n"
      "v_mfma_f32_16x16x32_bf16 v[132:135], a[4:7], v[68:71], v[132:135]\n"
      "v_cvt_pk_bf16_f32 v215, v138, v139\n"
      "v_mfma_f32_16x16x32_bf16 v[132:135], a[8:11], v[84:87], v[132:135]\n"
      "s_add_u32 m0, s17, 0x10e00\n"
      "v_mfma_f32_16x16x32_bf16 v[132:135], a[12:15], v[88:91], v[132:135]\n"
      "global_load_lds_dwordx4 v237, s[52:53] offset:512 nt\n"
      "v_mfma_f32_16x16x32_bf16 v[132:135], a[16:19], v[92:95], v[132:135]\n"
      "v_and_b32 v9, 0xffff0000, v100\n"
      "v_mfma_f32_16x16x32_bf16 v[132:135], a[20:23], v[108:111], v[132:135]\n"
      "v_lshlrev_b32 v10, 16, v101\n"
      "v_mfma_f32_16x16x32_bf16 v[132:135], v[140:143], v[112:115], v[132:135]\n"
      "s_waitcnt lgkmcnt(0)\n"
      "s_sub_u32 s86, s49, s84\n"
      "s_mov_b32 s83, s84\n"
      "v_mfma_f32_16x16x32_bf16 v[132:135], v[156:159], v[116:119], v[132:135]\n"
      "s_ashr_i32 s86, s86, 3\n"
      "s_cmp_gt_u32 s56, 1080\n"
      "s_cselect_b64 vcc, -1, 0\n"
      "s_barrier\n"
      "v_mfma_f32_16x16x32_bf16 v[136:139], v[160:163], v[36:39], v[20:23]\n"
      "ds_read_b128 v[36:39], v232 offset:0\n"
      "ds_write_b64 v245, v[214:215] offset:12296\n"
      "v_mfma_f32_16x16x32_bf16 v[136:139], v[164:167], v[40:43], v[136:139]\n"
      "ds_read_b128 v[40:43], v233 offset:0\n"
      "v_mfma_f32_16x16x32_bf16 v[136:139], v[180:183], v[44:47], v[136:139]\n"
      "ds_read_b128 v[44:47], v234 offset:0\n"
      "v_mfma_f32_16x16x32_bf16 v[136:139], v[184:187], v[60:63], v[136:139]\n"
      "ds_read_b128 v[60:63], v235 offset:0\n"
      "v_cvt_pk_bf16_f32 v208, v132, v133\n"
      "v_mfma_f32_16x16x32_bf16 v[136:139], v[188:191], v[64:67], v[136:139]\n"
      "ds_read_b128 v[64:67], v232 offset:8192\n"
      "v_cvt_pk_bf16_f32 v209, v134, v135\n"
      "v_mfma_f32_16x16x32_bf16 v[136:139], v[204:207], v[68:71], v[136:139]\n"
      "ds_read_b128 v[68:71], v233 offset:8192\n"
      "v_mfma_f32_16x16x32_bf16 v[136:139], a[24:27], v[84:87], v[136:139]\n"
      "ds_read_b128 v[84:87], v234 offset:8192\n"
      "v_mfma_f32_16x16x32_bf16 v[136:139], a[28:31], v[88:91], v[136:139]\n"
      "ds_read_b128 v[88:91], v235 offset:8192\n"
      "v_mfma_f32_16x16x32_bf16 v[136:139], a[32:35], v[92:95], v[136:139]\n"
      "ds_read_b128 v[92:95], v232 offset:16384\n"
      "v_mfma_f32_16x16x32_bf16 v[136:139], a[36:39], v[108:111], v[136:139]\n"
      "ds_read_b128 v[108:111], v233 offset:16384\n"
      "v_mfma_f32_16x16x32_bf16 v[136:139], a[40:43], v[112:115], v[136:139]\n"
      "ds_read_b128 v[112:115], v234 offset:16384\n"
      "v_mfma_f32_16x16x32_bf16 v[136:139], a[44:47], v[116:119], v[136:139]\n"
      "ds_read_b128 v[116:119], v235 offset:16384\n"
      "s_waitcnt lgkmcnt(8)\n"
      "v_mfma_f32_16x16x32_bf16 v[132:135], a[240:243], v[36:39], v[0:3]\n"
      "ds_write_b64 v244, v[208:209] offset:12416\n"
      "s_cbranch_vccnz LT0\n"
      "LS0:\n"
      "v_mfma_f32_16x16x32_bf16 v[132:135], a[244:247], v[40:43], v[132:135]\n"
      "v_and_b32 v11, 0xffff0000, v101\n"
      "v_mfma_f32_16x16x32_bf16 v[132:135], a[248:251], v[44:47], v[132:135]\n"
      "v_lshlrev_b32 v12, 16, v102\n"
      "v_mfma_f32_16x16x32_bf16 v[132:135], a[252:255], v[60:63], v[132:135]\n"
      "v_and_b32 v13, 0xffff0000, v102\n"
      "v_cvt_pk_bf16_f32 v210, v136, v137\n"
      "s_waitcnt lgkmcnt(5)\n"
      "v_mfma_f32_16x16x32_bf16 v[132:135], a[48:51], v[64:67], v[132:135]\n"
      "v_lshlrev_b32 v14, 16, v103\n"
      "v_cvt_pk_bf16_f32 v211, v138, v139\n"
      "v_mfma_f32_16x16x32_bf16 v[132:135], a[52:55], v[68:71], v[132:135]\n"
      "v_and_b32 v15, 0xffff0000, v103\n"
      "v_mfma_f32_16x16x32_bf16 v[132:135], a[56:59], v[84:87], v[132:135]\n"
      "v_lshlrev_b32 v16, 16, v104\n"
      "v_mfma_f32_16x16x32_bf16 v[132:135], a[60:63], v[88:91], v[132:135]\n"
      "v_and_b32 v17, 0xffff0000, v104\n"
      "s_waitcnt lgkmcnt(1)\n"
      "v_mfma_f32_16x16x32_bf16 v[132:135], a[64:67], v[92:95], v[132:135]\n"
      "v_lshlrev_b32 v18, 16, v105\n"
      "v_mfma_f32_16x16x32_bf16 v[132:135], a[68:71], v[108:111], v[132:135]\n"
      "v_and_b32 v19, 0xffff0000, v105\n"
      "ds_write_b64 v244, v[210:211] offset:12424\n"
      "v_mfma_f32_16x16x32_bf16 v[132:135], a[72:75], v[112:115], v[132:135]\n"
      "v_lshlrev_b32 v20, 16, v106\n"
      "v_mfma_f32_16x16x32_bf16 v[132:135], a[76:79], v[116:119], v[132:135]\n"
      "v_and_b32 v21, 0xffff0000, v106\n"
      "v_mfma_f32_16x16x32_bf16 v[136:139], a[80:83], v[36:39], v[4:7]\n"
      "v_lshlrev_b32 v22, 16, v107\n"
      "v_mfma_f32_16x16x32_bf16 v[136:139], a[84:87], v[40:43], v[136:139]\n"
      "v_and_b32 v23, 0xffff0000, v107\n"
      "v_mfma_f32_16x16x32_bf16 v[136:139], a[88:91], v[44:47], v[136:139]\n"
      "s_add_u32 s54, s44, s35\n"
      "s_addc_u32 s55, s45, 0\n"
      "v_mfma_f32_16x16x32_bf16 v[136:139], a[92:95], v[60:63], v[136:139]\n"
      "v_cvt_pk_bf16_f32 v208, v132, v133\n"
      "v_mfma_f32_16x16x32_bf16 v[136:139], a[96:99], v[64:67], v[136:139]\n"
      "v_cvt_pk_bf16_f32 v209, v134, v135\n"
      "v_mfma_f32_16x16x32_bf16 v[136:139], a[100:103], v[68:71], v[136:139]\n"
      "ds_read_b128 v[216:219], v246 offset:0\n"
      "v_mfma_f32_16x16x32_bf16 v[136:139], a[104:107], v[84:87], v[136:139]\n"
      "ds_read_b128 v[220:223], v246 offset:1024\n"
      "v_mfma_f32_16x16x32_bf16 v[136:139], a[108:111], v[88:91], v[136:139]\n"
      "ds_read_b128 v[224:227], v246 offset:2048\n"
      "v_mfma_f32_16x16x32_bf16 v[136:139], a[112:115], v[92:95], v[136:139]\n"
      "s_waitcnt lgkmcnt(2)\n"
      "v_mfma_f32_16x16x32_bf16 v[136:139], a[116:119], v[108:111], v[136:139]\n"
      "ds_write_b64 v244, v[208:209] offset:24576\n"
      "v_mfma_f32_16x16x32_bf16 v[136:139], a[120:123], v[112:115], v[136:139]\n"
      "global_store_dwordx4 v247, v[216:219], s[54:55] offset:0 sc1\n"
      "v_mfma_f32_16x16x32_bf16 v[136:139], a[124:127], v[116:119], v[136:139]\n"
      "s_add_u32 s52, s42, s37\n"
      "s_addc_u32 s53, s43, 0\n"
      "v_mfma_f32_16x16x32_bf16 v[132:135], a[128:131], v[36:39], v[8:11]\n"
      "s_add_u32 m0, s17, 0x12000\n"
      "v_mfma_f32_16x16x32_bf16 v[132:135], a[132:135], v[40:43], v[132:135]\n"
      "global_load_lds_dwordx4 v236, s[52:53] offset:0 nt\n"
      "v_mfma_f32_16x16x32_bf16 v[132:135], a[136:139], v[44:47], v[132:135]\n"
      "s_cbranch_vccnz LT1\n"
      "LS1:\n"
      "v_mfma_f32_16x16x32_bf16 v[132:135], a[140:143], v[60:63], v[132:135]\n"
      "v_cvt_pk_bf16_f32 v210, v136, v137\n"
      "v_mfma_f32_16x16x32_bf16 v[132:135], a[144:147], v[64:67], v[132:135]\n"
      "v_cvt_pk_bf16_f32 v211, v138, v139\n"
      "v_mfma_f32_16x16x32_bf16 v[132:135], a[148:151], v[68:71], v[132:135]\n"
      "v_mfma_f32_16x16x32_bf16 v[132:135], a[152:155], v[84:87], v[132:135]\n"
      "s_waitcnt lgkmcnt(2)\n"
      "v_mfma_f32_16x16x32_bf16 v[132:135], a[156:159], v[88:91], v[132:135]\n"
      "global_store_dwordx4 v252, v[220:223], s[54:55] offset:0 sc1\n"
      "v_mfma_f32_16x16x32_bf16 v[132:135], a[160:163], v[92:95], v[132:135]\n"
      "v_mfma_f32_16x16x32_bf16 v[132:135], a[164:167], v[108:111], v[132:135]\n"
      "ds_write_b64 v244, v[210:211] offset:24584\n"
      "v_mfma_f32_16x16x32_bf16 v[132:135], a[168:171], v[112:115], v[132:135]\n"
      "v_mfma_f32_16x16x32_bf16 v[132:135], a[172:175], v[116:119], v[132:135]\n"
      "s_add_u32 m0, s17, 0x13f00\n"
      "v_mfma_f32_16x16x32_bf16 v[136:139], a[176:179], v[36:39], v[12:15]\n"
      "global_load_lds_dwordx4 v236, s[52:53] offset:256 nt\n"
      "v_mfma_f32_16x16x32_bf16 v[136:139], a[180:183], v[40:43], v[136:139]\n"
      "v_mfma_f32_16x16x32_bf16 v[136:139], a[184:187], v[44:47], v[136:139]\n"
      "v_mfma_f32_16x16x32_bf16 v[136:139], a[188:191], v[60:63], v[136:139]\n"
      "v_cvt_pk_bf16_f32 v212, v132, v133\n"
      "v_mfma_f32_16x16x32_bf16 v[136:139], a[192:195], v[64:67], v[136:139]\n"
      "v_cvt_pk_bf16_f32 v213, v134, v135\n"
      "v_mfma_f32_16x16x32_bf16 v[136:139], a[196:199], v[68:71], v[136:139]\n"
      "v_mfma_f32_16x16x32_bf16 v[136:139], a[200:203], v[84:87], v[136:139]\n"
      "v_mfma_f32_16x16x32_bf16 v[136:139], a[204:207], v[88:91], v[136:139]\n"
      "s_waitcnt lgkmcnt(2)\n"
      "v_mfma_f32_16x16x32_bf16 v[136:139], a[208:211], v[92:95], v[136:139]\n"
      "global_store_dwordx4 v253, v[224:227], s[54:55] offset:0 sc1\n"
      "v_mfma_f32_16x16x32_bf16 v[136:139], a[212:215], v[108:111], v[136:139]\n"
      "ds_write_b64 v245, v[212:213] offset:24576\n"
      "v_mfma_f32_16x16x32_bf16 v[136:139], a[216:219], v[112:115], v[136:139]\n"
      "s_add_u32 m0, s17, 0x15e00\n"
      "v_mfma_f32_16x16x32_bf16 v[136:139], a[220:223], v[116:119], v[136:139]\n"
      "global_load_lds_dwordx4 v236, s[52:53] offset:512 nt\n"
      "v_mfma_f32_16x16x32_bf16 v[132:135], a[224:227], v[36:39], v[16:19]\n"
      "s_cbranch_vccnz LT2\n"
      "LS2:\n"
      "v_mfma_f32_16x16x32_bf16 v[132:135], a[228:231], v[40:43], v[132:135]\n"
      "v_mfma_f32_16x16x32_bf16 v[132:135], a[232:235], v[44:47], v[132:135]\n"
      "v_mfma_f32_16x16x32_bf16 v[132:135], a[236:239], v[60:63], v[132:135]\n"
      "v_cvt_pk_bf16_f32 v214, v136, v137\n"
      "v_mfma_f32_16x16x32_bf16 v[132:135], a[0:3], v[64:67], v[132:135]\n"
      "v_cvt_pk_bf16_f32 v215, v138, v139\n"
      "v_mfma_f32_16x16x32_bf16 v[132:135], a[4:7], v[68:71], v[132:135]\n"
      "v_mfma_f32_16x16x32_bf16 v[132:135], a[8:11], v[84:87], v[132:135]\n"
      "v_mfma_f32_16x16x32_bf16 v[132:135], a[12:15], v[88:91], v[132:135]\n"
      "v_mfma_f32_16x16x32_bf16 v[132:135], a[16:19], v[92:95], v[132:135]\n"
      "v_mfma_f32_16x16x32_bf16 v[132:135], a[20:23], v[108:111], v[132:135]\n"
      "ds_write_b64 v245, v[214:215] offset:24584\n"
      "v_mfma_f32_16x16x32_bf16 v[132:135], v[140:143], v[112:115], v[132:135]\n"
      "v_mfma_f32_16x16x32_bf16 v[132:135], v[156:159], v[116:119], v[132:135]\n"
      "s_barrier\n"
      "v_mfma_f32_16x16x32_bf16 v[136:139], v[160:163], v[36:39], v[20:23]\n"
      "ds_read_b128 v[36:39], v232 offset:256\n"
      "v_mfma_f32_16x16x32_bf16 v[136:139], v[164:167], v[40:43], v[136:139]\n"
      "ds_read_b128 v[40:43], v233 offset:256\n"
      "v_mfma_f32_16x16x32_bf16 v[136:139], v[180:183], v[44:47], v[136:139]\n"
      "ds_read_b128 v[44:47], v234 offset:256\n"
      "v_mfma_f32_16x16x32_bf16 v[136:139], v[184:187], v[60:63], v[136:139]\n"
      "ds_read_b128 v[60:63], v235 offset:256\n"
      "v_cvt_pk_bf16_f32 v208, v132, v133\n"
      "v_mfma_f32_16x16x32_bf16 v[136:139], v[188:191], v[64:67], v[136:139]\n"
      "ds_read_b128 v[64:67], v232 offset:8448\n"
      "v_cvt_pk_bf16_f32 v209, v134, v135\n"
      "v_mfma_f32_16x16x32_bf16 v[136:139], v[204:207], v[68:71], v[136:139]\n"
      "ds_read_b128 v[68:71], v233 offset:8448\n"
      "v_mfma_f32_16x16x32_bf16 v[136:139], a[24:27], v[84:87], v[136:139]\n"
      "ds_read_b128 v[84:87], v234 offset:8448\n"
      "v_mfma_f32_16x16x32_bf16 v[136:139], a[28:31], v[88:91], v[136:139]\n"
      "ds_read_b128 v[88:91], v235 offset:8448\n"
      "v_mfma_f32_16x16x32_bf16 v[136:139], a[32:35], v[92:95], v[136:139]\n"
      "ds_read_b128 v[92:95], v232 offset:16640\n"
      "v_mfma_f32_16x16x32_bf16 v[136:139], a[36:39], v[108:111], v[136:139]\n"
      "ds_read_b128 v[108:111], v233 offset:16640\n"
      "v_mfma_f32_16x16x32_bf16 v[136:139], a[40:43], v[112:115], v[136:139]\n"
      "ds_read_b128 v[112:115], v234 offset:16640\n"
      "v_mfma_f32_16x16x32_bf16 v[136:139], a[44:47], v[116:119], v[136:139]\n"
      "ds_read_b128 v[116:119], v235 offset:16640\n"
      "s_waitcnt lgkmcnt(8)\n"
      "v_mfma_f32_16x16x32_bf16 v[132:135], a[240:243], v[36:39], v[0:3]\n"
      "ds_write_b64 v244, v[208:209] offset:24704\n"
      "v_mfma_f32_16x16x32_bf16 v[132:135], a[244:247], v[40:43], v[132:135]\n"
      "s_cbranch_vccnz LT3\n"
      "LS3:\n"
      "v_mfma_f32_16x16x32_bf16 v[132:135], a[248:251], v[44:47], v[132:135]\n"
      "v_mfma_f32_16x16x32_bf16 v[132:135], a[252:255], v[60:63], v[132:135]\n"
      "v_cvt_pk_bf16_f32 v210, v136, v137\n"
      "s_waitcnt lgkmcnt(5)\n"
      "v_mfma_f32_16x16x32_bf16 v[132:135], a[48:51], v[64:67], v[132:135]\n"
      "v_cvt_pk_bf16_f32 v211, v138, v139\n"
      "v_mfma_f32_16x16x32_bf16 v[132:135], a[52:55], v[68:71], v[132:135]\n"
      "v_mfma_f32_16x16x32_bf16 v[132:135], a[56:59], v[84:87], v[132:135]\n"
      "ds_read_b128 v[216:219], v246 offset:12288\n"
      "v_mfma_f32_16x16x32_bf16 v[132:135], a[60:63], v[88:91], v[132:135]\n"
      "ds_read_b128 v[220:223], v246 offset:13312\n"
      "s_waitcnt lgkmcnt(3)\n"
      "v_mfma_f32_16x16x32_bf16 v[132:135], a[64:67], v[92:95], v[132:135]\n"
      "ds_read_b128 v[224:227], v246 offset:14336\n"
      "v_mfma_f32_16x16x32_bf16 v[132:135], a[68:71], v[108:111], v[132:135]\n"
      "ds_write_b64 v244, v[210:211] offset:24712\n"
      "v_mfma_f32_16x16x32_bf16 v[132:135], a[72:75], v[112:115], v[132:135]\n"
      "v_mfma_f32_16x16x32_bf16 v[132:135], a[76:79], v[116:119], v[132:135]\n"
      "v_mfma_f32_16x16x32_bf16 v[136:139], a[80:83], v[36:39], v[4:7]\n"
      "v_mfma_f32_16x16x32_bf16 v[136:139], a[84:87], v[40:43], v[136:139]\n"
      "v_mfma_f32_16x16x32_bf16 v[136:139], a[88:91], v[44:47], v[136:139]\n"
      "v_mfma_f32_16x16x32_bf16 v[136:139], a[92:95], v[60:63], v[136:139]\n"
      "v_cvt_pk_bf16_f32 v208, v132, v133\n"
      "v_mfma_f32_16x16x32_bf16 v[136:139], a[96:99], v[64:67], v[136:139]\n"
      "v_cvt_pk_bf16_f32 v209, v134, v135\n"
      "v_mfma_f32_16x16x32_bf16 v[136:139], a[100:103], v[68:71], v[136:139]\n"
      "s_waitcnt lgkmcnt(3)\n"
      "v_mfma_f32_16x16x32_bf16 v[136:139], a[104:107], v[84:87], v[136:139]\n"
      "global_store_dwordx4 v247, v[216:219], s[54:55] offset:768 sc1\n"
      "v_mfma_f32_16x16x32_bf16 v[136:139], a[108:111], v[88:91], v[136:139]\n"
      "s_add_u32 m0, s17, 0x13000\n"
      "v_mfma_f32_16x16x32_bf16 v[136:139], a[112:115], v[92:95], v[136:139]\n"
      "global_load_lds_dwordx4 v237, s[52:53] offset:0 nt\n"
      "v_mfma_f32_16x16x32_bf16 v[136:139], a[116:119], v[108:111], v[136:139]\n"
      "ds_write_b64 v244, v[208:209] offset:0\n"
      "v_mfma_f32_16x16x32_bf16 v[136:139], a[120:123], v[112:115], v[136:139]\n"
      "v_mfma_f32_16x16x32_bf16 v[136:139], a[124:127], v[116:119], v[136:139]\n"
      "v_mfma_f32_16x16x32_bf16 v[132:135], a[128:131], v[36:39], v[8:11]\n"
      "s_cbranch_vccnz LT4\n"
      "LS4:\n"
      "v_mfma_f32_16x16x32_bf16 v[132:135], a[132:135], v[40:43], v[132:135]\n"
      "v_mfma_f32_16x16x32_bf16 v[132:135], a[136:139], v[44:47], v[132:135]\n"
      "v_mfma_f32_16x16x32_bf16 v[132:135], a[140:143], v[60:63], v[132:135]\n"
      "v_cvt_pk_bf16_f32 v210, v136, v137\n"
      "v_mfma_f32_16x16x32_bf16 v[132:135], a[144:147], v[64:67], v[132:135]\n"
      "v_cvt_pk_bf16_f32 v211, v138, v139\n"
      "v_mfma_f32_16x16x32_bf16 v[132:135], a[148:151], v[68:71], v[132:135]\n"
      "v_mfma_f32_16x16x32_bf16 v[132:135], a[152:155], v[84:87], v[132:135]\n"
      "s_waitcnt lgkmcnt(3)\n"
      "v_mfma_f32_16x16x32_bf16 v[132:135], a[156:159], v[88:91], v[132:135]\n"
      "global_store_dwordx4 v252, v[220:223], s[54:55] offset:768 sc1\n"
      "v_mfma_f32_16x16x32_bf16 v[132:135], a[160:163], v[92:95], v[132:135]\n"
      "s_add_u32 m0, s17, 0x14f00\n"
      "v_mfma_f32_16x16x32_bf16 v[132:135], a[164:167], v[108:111], v[132:135]\n"
      "ds_write_b64 v244, v[210:211] offset:8\n"
      "v_mfma_f32_16x16x32_bf16 v[132:135], a[168:171], v[112:115], v[132:135]\n"
      "global_load_lds_dwordx4 v237, s[52:53] offset:256 nt\n"
      "v_mfma_f32_16x16x32_bf16 v[132:135], a[172:175], v[116:119], v[132:135]\n"
      "v_mfma_f32_16x16x32_bf16 v[136:139], a[176:179], v[36:39], v[12:15]\n"
      "v_mfma_f32_16x16x32_bf16 v[136:139], a[180:183], v[40:43], v[136:139]\n"
      "v_mfma_f32_16x16x32_bf16 v[136:139], a[184:187], v[44:47], v[136:139]\n"
      "v_mfma_f32_16x16x32_bf16 v[136:139], a[188:191], v[60:63], v[136:139]\n"
      "v_cvt_pk_bf16_f32 v212, v132, v133\n"
      "v_mfma_f32_16x16x32_bf16 v[136:139], a[192:195], v[64:67], v[136:139]\n"
      "v_cvt_pk_bf16_f32 v213, v134, v135\n"
      "v_mfma_f32_16x16x32_bf16 v[136:139], a[196:199], v[68:71], v[136:139]\n"
      "v_mfma_f32_16x16x32_bf16 v[136:139], a[200:203], v[84:87], v[136:139]\n"
      "v_mfma_f32_16x16x32_bf16 v[136:139], a[204:207], v[88:91], v[136:139]\n"
      "s_waitcnt lgkmcnt(3)\n"
      "v_mfma_f32_16x16x32_bf16 v[136:139], a[208:211], v[92:95], v[136:139]\n"
      "global_store_dwordx4 v253, v[224:227], s[54:55] offset:768 sc1\n"
      "v_mfma_f32_16x16x32_bf16 v[136:139], a[212:215], v[108:111], v[136:139]\n"
      "ds_write_b64 v245, v[212:213] offset:0\n"
      "v_mfma_f32_16x16x32_bf16 v[136:139], a[216:219], v[112:115], v[136:139]\n"
      "s_add_u32 m0, s17, 0x16e00\n"
      "v_mfma_f32_16x16x32_bf16 v[136:139], a[220:223], v[116:119], v[136:139]\n"
      "global_load_lds_dwordx4 v237, s[52:53] offset:512 nt\n"
      "v_mfma_f32_16x16x32_bf16 v[132:135], a[224:227], v[36:39], v[16:19]\n"
      "s_cbranch_vccnz LT5\n"
      "LS5:\n"
      "v_mfma_f32_16x16x32_bf16 v[132:135], a[228:231], v[40:43], v[132:135]\n"
      "v_mfma_f32_16x16x32_bf16 v[132:135], a[232:235], v[44:47], v[132:135]\n"
      "v_mfma_f32_16x16x32_bf16 v[132:135], a[236:239], v[60:63], v[132:135]\n"
      "s_memrealtime s[84:85]\n"
      "v_mfma_f32_16x16x32_bf16 v[132:135], a[0:3], v[64:67], v[132:135]\n"
      "v_cvt_pk_bf16_f32 v214, v136, v137\n"
      "v_mfma_f32_16x16x32_bf16 v[132:135], a[4:7], v[68:71], v[132:135]\n"
      "v_cvt_pk_bf16_f32 v215, v138, v139\n"
      "v_mfma_f32_16x16x32_bf16 v[132:135], a[8:11], v[84:87], v[132:135]\n"
      "v_mfma_f32_16x16x32_bf16 v[132:135], a[12:15], v[88:91], v[132:135]\n"
      "s_add_u32 s83, s83, s86\n"
      "v_mfma_f32_16x16x32_bf16 v[132:135], a[16:19], v[92:95], v[132:135]\n"
      "v_mfma_f32_16x16x32_bf16 v[132:135], a[20:23], v[108:111], v[132:135]\n"
      "v_mfma_f32_16x16x32_bf16 v[132:135], v[140:143], v[112:115], v[132:135]\n"
      "s_waitcnt lgkmcnt(0)\n"
      "s_sub_u32 s84, s84, s83\n"
      "v_mfma_f32_16x16x32_bf16 v[132:135], v[156:159], v[116:119], v[132:135]\n"
      "s_cmp_lt_i32 s84, 0\n"
      "s_cselect_b64 vcc, -1, 0\n"
      "s_waitcnt vmcnt(24)\n"
      "s_barrier\n"
      "v_mfma_f32_16x16x32_bf16 v[136:139], v[160:163], v[36:39], v[20:23]\n"
      "ds_read_b128 v[36:39], v232 offset:24576\n"
      "ds_write_b64 v245, v[214:215] offset:8\n"
      "v_mfma_f32_16x16x32_bf16 v[136:139], v[164:167], v[40:43], v[136:139]\n"
      "ds_read_b128 v[40:43], v233 offset:24576\n"
      "s_lshl_b32 s79, s93, 1\n"
      "v_mfma_f32_16x16x32_bf16 v[136:139], v[180:183], v[44:47], v[136:139]\n"
      "ds_read_b128 v[44:47], v234 offset:24576\n"
      "v_mad_i32_i24 v252, v251, s79, v252\n"
      "v_mfma_f32_16x16x32_bf16 v[136:139], v[184:187], v[60:63], v[136:139]\n"
      "ds_read_b128 v[60:63], v235 offset:24576\n"
      "v_cvt_pk_bf16_f32 v208, v132, v133\n"
      "v_mfma_f32_16x16x32_bf16 v[136:139], v[188:191], v[64:67], v[136:139]\n"
      "ds_read_b128 v[64:67], v232 offset:32768\n"
      "v_cvt_pk_bf16_f32 v209, v134, v135\n"
      "v_mfma_f32_16x16x32_bf16 v[136:139], v[204:207], v[68:71], v[136:139]\n"
      "ds_read_b128 v[68:71], v233 offset:32768\n"
      "s_mul_i32 s79, s79, s100\n"
      "v_mfma_f32_16x16x32_bf16 v[136:139], a[24:27], v[84:87], v[136:139]\n"
      "ds_read_b128 v[84:87], v234 offset:32768\n"
      "v_add_u32 v247, s79, v247\n"
      "v_mfma_f32_16x16x32_bf16 v[136:139], a[28:31], v[88:91], v[136:139]\n"
      "ds_read_b128 v[88:91], v235 offset:32768\n"
      "s_mul_i32 s79, s79, s101\n"
      "v_mfma_f32_16x16x32_bf16 v[136:139], a[32:35], v[92:95], v[136:139]\n"
      "ds_read_b128 v[92:95], v232 offset:40960\n"
      "v_add_u32 v253, s79, v253\n"
      "v_mfma_f32_16x16x32_bf16 v[136:139], a[36:39], v[108:111], v[136:139]\n"
      "ds_read_b128 v[108:111], v233 offset:40960\n"
      "v_mfma_f32_16x16x32_bf16 v[136:139], a[40:43], v[112:115], v[136:139]\n"
      "ds_read_b128 v[112:115], v234 offset:40960\n"
      "v_mfma_f32_16x16x32_bf16 v[136:139], a[44:47], v[116:119], v[136:139]\n"
      "ds_read_b128 v[116:119], v235 offset:40960\n"
      "s_waitcnt lgkmcnt(8)\n"
      "v_mfma_f32_16x16x32_bf16 v[132:135], a[240:243], v[36:39], v[0:3]\n"
      "ds_write_b64 v244, v[208:209] offset:128\n"
      "v_mfma_f32_16x16x32_bf16 v[132:135], a[244:247], v[40:43], v[132:135]\n"
      "s_add_u32 s54, s30, s36\n"
      "s_addc_u32 s55, s31, 0\n"
      "v_mfma_f32_16x16x32_bf16 v[132:135], a[248:251], v[44:47], v[132:135]\n"
      "s_cbranch_vccnz LT6\n"
      "LS6:\n"
      "v_mfma_f32_16x16x32_bf16 v[132:135], a[252:255], v[60:63], v[132:135]\n"
      "v_cvt_pk_bf16_f32 v210, v136, v137\n"
      "s_waitcnt lgkmcnt(5)\n"
      "v_mfma_f32_16x16x32_bf16 v[132:135], a[48:51], v[64:67], v[132:135]\n"
      "v_cvt_pk_bf16_f32 v211, v138, v139\n"
      "v_mfma_f32_16x16x32_bf16 v[132:135], a[52:55], v[68:71], v[132:135]\n"
      "s_mul_i32 s78, s95, s19\n"
      "v_mfma_f32_16x16x32_bf16 v[132:135], a[56:59], v[84:87], v[132:135]\n"
      "ds_read_b128 v[216:219], v246 offset:24576\n"
      "v_add_u32 v236, s78, v236\n"
      "v_mfma_f32_16x16x32_bf16 v[132:135], a[60:63], v[88:91], v[132:135]\n"
      "ds_read_b128 v[220:223], v246 offset:25600\n"
      "s_waitcnt lgkmcnt(3)\n"
      "v_mfma_f32_16x16x32_bf16 v[132:135], a[64:67], v[92:95], v[132:135]\n"
      "ds_read_b128 v[224:227], v246 offset:26624\n"
      "v_mfma_f32_16x16x32_bf16 v[132:135], a[68:71], v[108:111], v[132:135]\n"
      "ds_write_b64 v244, v[210:211] offset:136\n"
      "v_mfma_f32_16x16x32_bf16 v[132:135], a[72:75], v[112:115], v[132:135]\n"
      "s_mul_i32 s78, s78, s99\n"
      "v_mfma_f32_16x16x32_bf16 v[132:135], a[76:79], v[116:119], v[132:135]\n"
      "v_add_u32 v237, s78, v237\n"
      "v_mfma_f32_16x16x32_bf16 v[136:139], a[80:83], v[36:39], v[4:7]\n"
      "v_mfma_f32_16x16x32_bf16 v[136:139], a[84:87], v[40:43], v[136:139]\n"
      "v_mfma_f32_16x16x32_bf16 v[136:139], a[88:91], v[44:47], v[136:139]\n"
      "v_mfma_f32_16x16x32_bf16 v[136:139], a[92:95], v[60:63], v[136:139]\n"
      "v_cvt_pk_bf16_f32 v208, v132, v133\n"
      "v_mfma_f32_16x16x32_bf16 v[136:139], a[96:99], v[64:67], v[136:139]\n"
      "v_cvt_pk_bf16_f32 v209, v134, v135\n"
      "v_mfma_f32_16x16x32_bf16 v[136:139], a[100:103], v[68:71], v[136:139]\n"
      "s_waitcnt lgkmcnt(3)\n"
      "v_mfma_f32_16x16x32_bf16 v[136:139], a[104:107], v[84:87], v[136:139]\n"
      "global_store_dwordx4 v247, v[216:219], s[54:55] offset:0 sc1\n"
      "v_mfma_f32_16x16x32_bf16 v[136:139], a[108:111], v[88:91], v[136:139]\n"
      "s_add_u32 s52, s20, s38\n"
      "s_addc_u32 s53, s21, 0\n"
      "v_mfma_f32_16x16x32_bf16 v[136:139], a[112:115], v[92:95], v[136:139]\n"
      "s_add_u32 m0, s17, 0x0\n"
      "v_mfma_f32_16x16x32_bf16 v[136:139], a[116:119], v[108:111], v[136:139]\n"
      "ds_write_b64 v244, v[208:209] offset:12288\n"
      "v_mfma_f32_16x16x32_bf16 v[136:139], a[120:123], v[112:115], v[136:139]\n"
      "global_load_lds_dwordx4 v236, s[52:53] offset:0 nt\n"
      "v_mfma_f32_16x16x32_bf16 v[136:139], a[124:127], v[116:119], v[136:139]\n"
      "v_mfma_f32_16x16x32_bf16 v[132:135], a[128:131], v[36:39], v[8:11]\n"
      "s_cbranch_vccnz LT7\n"
      "LS7:\n"
      "v_mfma_f32_16x16x32_bf16 v[132:135], a[132:135], v[40:43], v[132:135]\n"
      "v_mfma_f32_16x16x32_bf16 v[132:135], a[136:139], v[44:47], v[132:135]\n"
      "v_mfma_f32_16x16x32_bf16 v[132:135], a[140:143], v[60:63], v[132:135]\n"
      "v_cvt_pk_bf16_f32 v210, v136, v137\n"
      "v_mfma_f32_16x16x32_bf16 v[132:135], a[144:147], v[64:67], v[132:135]\n"
      "v_cvt_pk_bf16_f32 v211, v138, v139\n"
      "v_mfma_f32_16x16x32_bf16 v[132:135], a[148:151], v[68:71], v[132:135]\n"
      "v_mfma_f32_16x16x32_bf16 v[132:135], a[152:155], v[84:87], v[132:135]\n"
      "s_waitcnt lgkmcnt(3)\n"
      "v_mfma_f32_16x16x32_bf16 v[132:135], a[156:159], v[88:91], v[132:135]\n"
      "global_store_dwordx4 v252, v[220:223], s[54:55] offset:0 sc1\n"
      "v_mfma_f32_16x16x32_bf16 v[132:135], a[160:163], v[92:95], v[132:135]\n"
      "s_add_u32 m0, s17, 0x1f00\n"
      "v_mfma_f32_16x16x32_bf16 v[132:135], a[164:167], v[108:111], v[132:135]\n"
      "ds_write_b64 v244, v[210:211] offset:12296\n"
      "v_mfma_f32_16x16x32_bf16 v[132:135], a[168:171], v[112:115], v[132:135]\n"
      "global_load_lds_dwordx4 v236, s[52:53] offset:256 nt\n"
      "v_mfma_f32_16x16x32_bf16 v[132:135], a[172:175], v[116:119], v[132:135]\n"
      "v_mfma_f32_16x16x32_bf16 v[136:139], a[176:179], v[36:39], v[12:15]\n"
      "v_mfma_f32_16x16x32_bf16 v[136:139], a[180:183], v[40:43], v[136:139]\n"
      "v_mfma_f32_16x16x32_bf16 v[136:139], a[184:187], v[44:47], v[136:139]\n"
      "v_mfma_f32_16x16x32_bf16 v[136:139], a[188:191], v[60:63], v[136:139]\n"
      "v_cvt_pk_bf16_f32 v212, v132, v133\n"
      "v_mfma_f32_16x16x32_bf16 v[136:139], a[192:195], v[64:67], v[136:139]\n"
      "v_cvt_pk_bf16_f32 v213, v134, v135\n"
      "v_mfma_f32_16x16x32_bf16 v[136:139], a[196:199], v[68:71], v[136:139]\n"
      "v_mfma_f32_16x16x32_bf16 v[136:139], a[200:203], v[84:87], v[136:139]\n"
      "v_mfma_f32_16x16x32_bf16 v[136:139], a[204:207], v[88:91], v[136:139]\n"
      "s_waitcnt lgkmcnt(3)\n"
      "v_mfma_f32_16x16x32_bf16 v[136:139], a[208:211], v[92:95], v[136:139]\n"
      "global_store_dwordx4 v253, v[224:227], s[54:55] offset:0 sc1\n"
      "v_mfma_f32_16x16x32_bf16 v[136:139], a[212:215], v[108:111], v[136:139]\n"
      "ds_write_b64 v245, v[212:213] offset:12288\n"
      "v_mfma_f32_16x16x32_bf16 v[136:139], a[216:219], v[112:115], v[136:139]\n"
      "s_add_u32 m0, s17, 0x3e00\n"
      "v_mfma_f32_16x16x32_bf16 v[136:139], a[220:223], v[116:119], v[136:139]\n"
      "global_load_lds_dwordx4 v236, s[52:53] offset:512 nt\n"
      "v_mfma_f32_16x16x32_bf16 v[132:135], a[224:227], v[36:39], v[16:19]\n"
      "s_cbranch_vccnz LT8\n"
      "LS8:\n"
      "v_mfma_f32_16x16x32_bf16 v[132:135], a[228:231], v[40:43], v[132:135]\n"
      "v_mfma_f32_16x16x32_bf16 v[132:135], a[232:235], v[44:47], v[132:135]\n"
      "v_mfma_f32_16x16x32_bf16 v[132:135], a[236:239], v[60:63], v[132:135]\n"
      "v_cvt_pk_bf16_f32 v214, v136, v137\n"
      "v_mfma_f32_16x16x32_bf16 v[132:135], a[0:3], v[64:67], v[132:135]\n"
      "v_cvt_pk_bf16_f32 v215, v138, v139\n"
      "v_mfma_f32_16x16x32_bf16 v[132:135], a[4:7], v[68:71], v[132:135]\n"
      "v_mfma_f32_16x16x32_bf16 v[132:135], a[8:11], v[84:87], v[132:135]\n"
      "v_mfma_f32_16x16x32_bf16 v[132:135], a[12:15], v[88:91], v[132:135]\n"
      "v_mfma_f32_16x16x32_bf16 v[132:135], a[16:19], v[92:95], v[132:135]\n"
      "v_mfma_f32_16x16x32_bf16 v[132:135], a[20:23], v[108:111], v[132:135]\n"
      "ds_write_b64 v245, v[214:215] offset:12296\n"
      "v_mfma_f32_16x16x32_bf16 v[132:135], v[140:143], v[112:115], v[132:135]\n"
      "v_mfma_f32_16x16x32_bf16 v[132:135], v[156:159], v[116:119], v[132:135]\n"
      "s_waitcnt lgkmcnt(4)\n"
      "s_barrier\n"
      "v_mfma_f32_16x16x32_bf16 v[136:139], v[160:163], v[36:39], v[20:23]\n"
      "ds_read_b128 v[36:39], v232 offset:24832\n"
      "v_mfma_f32_16x16x32_bf16 v[136:139], v[164:167], v[40:43], v[136:139]\n"
      "ds_read_b128 v[40:43], v233 offset:24832\n"
      "v_mfma_f32_16x16x32_bf16 v[136:139], v[180:183], v[44:47], v[136:139]\n"
      "ds_read_b128 v[44:47], v234 offset:24832\n"
      "v_mfma_f32_16x16x32_bf16 v[136:139], v[184:187], v[60:63], v[136:139]\n"
      "ds_read_b128 v[60:63], v235 offset:24832\n"
      "v_cvt_pk_bf16_f32 v208, v132, v133\n"
      "v_mfma_f32_16x16x32_bf16 v[136:139], v[188:191], v[64:67], v[136:139]\n"
      "ds_read_b128 v[64:67], v232 offset:33024\n"
      "v_cvt_pk_bf16_f32 v209, v134, v135\n"
      "v_mfma_f32_16x16x32_bf16 v[136:139], v[204:207], v[68:71], v[136:139]\n"
      "ds_read_b128 v[68:71], v233 offset:33024\n"
      "v_mfma_f32_16x16x32_bf16 v[136:139], a[24:27], v[84:87], v[136:139]\n"
      "ds_read_b128 v[84:87], v234 offset:33024\n"
      "v_mfma_f32_16x16x32_bf16 v[136:139], a[28:31], v[88:91], v[136:139]\n"
      "ds_read_b128 v[88:91], v235 offset:33024\n"
      "v_mfma_f32_16x16x32_bf16 v[136:139], a[32:35], v[92:95], v[136:139]\n"
      "ds_read_b128 v[92:95], v232 offset:41216\n"
      "v_mfma_f32_16x16x32_bf16 v[136:139], a[36:39], v[108:111], v[136:139]\n"
      "ds_read_b128 v[108:111], v233 offset:41216\n"
      "ds_write_b64 v244, v[208:209] offset:12416\n"
      "v_mfma_f32_16x16x32_bf16 v[136:139], a[40:43], v[112:115], v[136:139]\n"
      "ds_read_b128 v[112:115], v234 offset:41216\n"
      "v_mfma_f32_16x16x32_bf16 v[136:139], a[44:47], v[116:119], v[136:139]\n"
      "ds_read_b128 v[116:119], v235 offset:41216\n"
      "s_waitcnt lgkmcnt(9)\n"
      "v_mfma_f32_16x16x32_bf16 v[132:135], a[240:243], v[36:39], v[0:3]\n"
      "v_add_u32 v232, 0xc000, v232\n"
      "s_cbranch_vccnz LT9\n"
      "LS9:\n"
      "v_mfma_f32_16x16x32_bf16 v[132:135], a[244:247], v[40:43], v[132:135]\n"
      "v_add_u32 v233, 0xc000, v233\n"
      "v_mfma_f32_16x16x32_bf16 v[132:135], a[248:251], v[44:47], v[132:135]\n"
      "v_add_u32 v234, 0xc000, v234\n"
      "v_mfma_f32_16x16x32_bf16 v[132:135], a[252:255], v[60:63], v[132:135]\n"
      "v_add_u32 v235, 0xc000, v235\n"
      "s_waitcnt lgkmcnt(5)\n"
      "v_mfma_f32_16x16x32_bf16 v[132:135], a[48:51], v[64:67], v[132:135]\n"
      "v_cvt_pk_bf16_f32 v210, v136, v137\n"
      "v_mfma_f32_16x16x32_bf16 v[132:135], a[52:55], v[68:71], v[132:135]\n"
      "v_cvt_pk_bf16_f32 v211, v138, v139\n"
      "v_mfma_f32_16x16x32_bf16 v[132:135], a[56:59], v[84:87], v[132:135]\n"
      "ds_read_b128 v[216:219], v246 offset:0\n"
      "v_mfma_f32_16x16x32_bf16 v[132:135], a[60:63], v[88:91], v[132:135]\n"
      "ds_read_b128 v[220:223], v246 offset:1024\n"
      "s_waitcnt lgkmcnt(2)\n"
      "v_mfma_f32_16x16x32_bf16 v[132:135], a[64:67], v[92:95], v[132:135]\n"
      "ds_read_b128 v[224:227], v246 offset:2048\n"
      "v_mfma_f32_16x16x32_bf16 v[132:135], a[68:71], v[108:111], v[132:135]\n"
      "v_lshlrev_b32 v0, 16, v120\n"
      "v_mfma_f32_16x16x32_bf16 v[132:135], a[72:75], v[112:115], v[132:135]\n"
      "ds_write_b64 v244, v[210:211] offset:12424\n"
      "v_mfma_f32_16x16x32_bf16 v[132:135], a[76:79], v[116:119], v[132:135]\n"
      "v_and_b32 v1, 0xffff0000, v120\n"
      "v_mfma_f32_16x16x32_bf16 v[136:139], a[80:83], v[36:39], v[4:7]\n"
      "v_lshlrev_b32 v2, 16, v121\n"
      "v_mfma_f32_16x16x32_bf16 v[136:139], a[84:87], v[40:43], v[136:139]\n"
      "v_and_b32 v3, 0xffff0000, v121\n"
      "v_mfma_f32_16x16x32_bf16 v[136:139], a[88:91], v[44:47], v[136:139]\n"
      "v_mfma_f32_16x16x32_bf16 v[136:139], a[92:95], v[60:63], v[136:139]\n"
      "v_cvt_pk_bf16_f32 v208, v132, v133\n"
      "v_mfma_f32_16x16x32_bf16 v[136:139], a[96:99], v[64:67], v[136:139]\n"
      "v_cvt_pk_bf16_f32 v209, v134, v135\n"
      "v_mfma_f32_16x16x32_bf16 v[136:139], a[100:103], v[68:71], v[136:139]\n"
      "s_waitcnt lgkmcnt(3)\n"
      "v_mfma_f32_16x16x32_bf16 v[136:139], a[104:107], v[84:87], v[136:139]\n"
      "global_store_dwordx4 v247, v[216:219], s[54:55] offset:768 sc1\n"
      "v_mfma_f32_16x16x32_bf16 v[136:139], a[108:111], v[88:91], v[136:139]\n"
      "s_add_u32 m0, s17, 0x1000\n"
      "v_mfma_f32_16x16x32_bf16 v[136:139], a[112:115], v[92:95], v[136:139]\n"
      "global_load_lds_dwordx4 v237, s[52:53] offset:0 nt\n"
      "v_mfma_f32_16x16x32_bf16 v[136:139], a[116:119], v[108:111], v[136:139]\n"
      "ds_write_b64 v244, v[208:209] offset:24576\n"
      "v_mfma_f32_16x16x32_bf16 v[136:139], a[120:123], v[112:115], v[136:139]\n"
      "v_lshlrev_b32 v4, 16, v122\n"
      "v_mfma_f32_16x16x32_bf16 v[136:139], a[124:127], v[116:119], v[136:139]\n"
      "v_and_b32 v5, 0xffff0000, v122\n"
      "v_mfma_f32_16x16x32_bf16 v[132:135], a[128:131], v[36:39], v[8:11]\n"
      "v_lshlrev_b32 v6, 16, v123\n"
      "v_mfma_f32_16x16x32_bf16 v[132:135], a[132:135], v[40:43], v[132:135]\n"
      "v_and_b32 v7, 0xffff0000, v123\n"
      "v_mfma_f32_16x16x32_bf16 v[132:135], a[136:139], v[44:47], v[132:135]\n"
      "s_cbranch_vccnz LT10\n"
      "LS10:\n"
      "v_mfma_f32_16x16x32_bf16 v[132:135], a[140:143], v[60:63], v[132:135]\n"
      "v_cvt_pk_bf16_f32 v210, v136, v137\n"
      "v_mfma_f32_16x16x32_bf16 v[132:135], a[144:147], v[64:67], v[132:135]\n"
      "v_cvt_pk_bf16_f32 v211, v138, v139\n"
      "v_mfma_f32_16x16x32_bf16 v[132:135], a[148:151], v[68:71], v[132:135]\n"
      "v_lshlrev_b32 v8, 16, v124\n"
      "v_mfma_f32_16x16x32_bf16 v[132:135], a[152:155], v[84:87], v[132:135]\n"
      "s_waitcnt lgkmcnt(3)\n"
      "v_mfma_f32_16x16x32_bf16 v[132:135], a[156:159], v[88:91], v[132:135]\n"
      "global_store_dwordx4 v252, v[220:223], s[54:55] offset:768 sc1\n"
      "v_mfma_f32_16x16x32_bf16 v[132:135], a[160:163], v[92:95], v[132:135]\n"
      "s_add_u32 m0, s17, 0x2f00\n"
      "v_mfma_f32_16x16x32_bf16 v[132:135], a[164:167], v[108:111], v[132:135]\n"
      "ds_write_b64 v244, v[210:211] offset:24584\n"
      "v_mfma_f32_16x16x32_bf16 v[132:135], a[168:171], v[112:115], v[132:135]\n"
      "global_load_lds_dwordx4 v237, s[52:53] offset:256 nt\n"
      "v_mfma_f32_16x16x32_bf16 v[132:135], a[172:175], v[116:119], v[132:135]\n"
      "v_and_b32 v9, 0xffff0000, v124\n"
      "v_mfma_f32_16x16x32_bf16 v[136:139], a[176:179], v[36:39], v[12:15]\n"
      "v_lshlrev_b32 v10, 16, v125\n"
      "v_mfma_f32_16x16x32_bf16 v[136:139], a[180:183], v[40:43], v[136:139]\n"
      "v_and_b32 v11, 0xffff0000, v125\n"
      "v_mfma_f32_16x16x32_bf16 v[136:139], a[184:187], v[44:47], v[136:139]\n"
      "v_mfma_f32_16x16x32_bf16 v[136:139], a[188:191], v[60:63], v[136:139]\n"
      "v_cvt_pk_bf16_f32 v212, v132, v133\n"
      "v_mfma_f32_16x16x32_bf16 v[136:139], a[192:195], v[64:67], v[136:139]\n"
      "v_cvt_pk_bf16_f32 v213, v134, v135\n"
      "v_mfma_f32_16x16x32_bf16 v[136:139], a[196:199], v[68:71], v[136:139]\n"
      "v_lshlrev_b32 v12, 16, v126\n"
      "v_mfma_f32_16x16x32_bf16 v[136:139], a[200:203], v[84:87], v[136:139]\n"
      "v_and_b32 v13, 0xffff0000, v126\n"
      "v_mfma_f32_16x16x32_bf16 v[136:139], a[204:207], v[88:91], v[136:139]\n"
      "s_waitcnt lgkmcnt(3)\n"
      "v_mfma_f32_16x16x32_bf16 v[136:139], a[208:211], v[92:95], v[136:139]\n"
      "global_store_dwordx4 v253, v[224:227], s[54:55] offset:768 sc1\n"
      "v_mfma_f32_16x16x32_bf16 v[136:139], a[212:215], v[108:111], v[136:139]\n"
      "ds_write_b64 v245, v[212:213] offset:24576\n"
      "v_mfma_f32_16x16x32_bf16 v[136:139], a[216:219], v[112:115], v[136:139]\n"
      "s_add_u32 m0, s17, 0x4e00\n"
      "v_mfma_f32_16x16x32_bf16 v[136:139], a[220:223], v[116:119], v[136:139]\n"
      "global_load_lds_dwordx4 v237, s[52:53] offset:512 nt\n"
      "v_mfma_f32_16x16x32_bf16 v[132:135], a[224:227], v[36:39], v[16:19]\n"
      "v_lshlrev_b32 v14, 16, v127\n"
      "v_mfma_f32_16x16x32_bf16 v[132:135], a[228:231], v[40:43], v[132:135]\n"
      "v_and_b32 v15, 0xffff0000, v127\n"
      "v_mfma_f32_16x16x32_bf16 v[132:135], a[232:235], v[44:47], v[132:135]\n"
      "s_cbranch_vccnz LT11\n"
      "LS11:\n"
      "v_mfma_f32_16x16x32_bf16 v[132:135], a[236:239], v[60:63], v[132:135]\n"
      "s_memrealtime s[84:85]\n"
      "v_mfma_f32_16x16x32_bf16 v[132:135], a[0:3], v[64:67], v[132:135]\n"
      "v_cvt_pk_bf16_f32 v214, v136, v137\n"
      "v_mfma_f32_16x16x32_bf16 v[132:135], a[4:7], v[68:71], v[132:135]\n"
      "v_cvt_pk_bf16_f32 v215, v138, v139\n"
      "v_mfma_f32_16x16x32_bf16 v[132:135], a[8:11], v[84:87], v[132:135]\n"
      "v_lshlrev_b32 v16, 16, v128\n"
      "v_mfma_f32_16x16x32_bf16 v[132:135], a[12:15], v[88:91], v[132:135]\n"
      "s_add_u32 s83, s83, s86\n"
      "v_mfma_f32_16x16x32_bf16 v[132:135], a[16:19], v[92:95], v[132:135]\n"
      "v_and_b32 v17, 0xffff0000, v128\n"
      "v_mfma_f32_16x16x32_bf16 v[132:135], a[20:23], v[108:111], v[132:135]\n"
      "v_lshlrev_b32 v18, 16, v129\n"
      "v_mfma_f32_16x16x32_bf16 v[132:135], v[140:143], v[112:115], v[132:135]\n"
      "s_waitcnt lgkmcnt(0)\n"
      "s_sub_u32 s84, s84, s83\n"
      "v_mfma_f32_16x16x32_bf16 v[132:135], v[156:159], v[116:119], v[132:135]\n"
      "s_cmp_lt_i32 s84, 0\n"
      "s_cselect_b64 vcc, -1, 0\n"
      "s_waitcnt vmcnt(24)\n"
      "s_barrier\n"
      "v_mfma_f32_16x16x32_bf16 v[136:139], v[160:163], v[36:39], v[20:23]\n"
      "ds_read_b128 v[36:39], v232 offset:0\n"
      "ds_write_b64 v245, v[214:215] offset:24584\n"
      "v_mfma_f32_16x16x32_bf16 v[136:139], v[164:167], v[40:43], v[136:139]\n"
      "ds_read_b128 v[40:43], v233 offset:0\n"
      "v_mfma_f32_16x16x32_bf16 v[136:139], v[180:183], v[44:47], v[136:139]\n"
      "ds_read_b128 v[44:47], v234 offset:0\n"
      "v_mfma_f32_16x16x32_bf16 v[136:139], v[184:187], v[60:63], v[136:139]\n"
      "ds_read_b128 v[60:63], v235 offset:0\n"
      "v_cvt_pk_bf16_f32 v208, v132, v133\n"
      "v_mfma_f32_16x16x32_bf16 v[136:139], v[188:191], v[64:67], v[136:139]\n"
      "ds_read_b128 v[64:67], v232 offset:8192\n"
      "v_cvt_pk_bf16_f32 v209, v134, v135\n"
      "v_mfma_f32_16x16x32_bf16 v[136:139], v[204:207], v[68:71], v[136:139]\n"
      "ds_read_b128 v[68:71], v233 offset:8192\n"
      "v_mfma_f32_16x16x32_bf16 v[136:139], a[24:27], v[84:87], v[136:139]\n"
      "ds_read_b128 v[84:87], v234 offset:8192\n"
      "v_mfma_f32_16x16x32_bf16 v[136:139], a[28:31], v[88:91], v[136:139]\n"
      "ds_read_b128 v[88:91], v235 offset:8192\n"
      "v_mfma_f32_16x16x32_bf16 v[136:139], a[32:35], v[92:95], v[136:139]\n"
      "ds_read_b128 v[92:95], v232 offset:16384\n"
      "v_mfma_f32_16x16x32_bf16 v[136:139], a[36:39], v[108:111], v[136:139]\n"
      "ds_read_b128 v[108:111], v233 offset:16384\n"
      "v_mfma_f32_16x16x32_bf16 v[136:139], a[40:43], v[112:115], v[136:139]\n"
      "ds_read_b128 v[112:115], v234 offset:16384\n"
      "v_mfma_f32_16x16x32_bf16 v[136:139], a[44:47], v[116:119], v[136:139]\n"
      "ds_read_b128 v[116:119], v235 offset:16384\n"
      "s_waitcnt lgkmcnt(8)\n"
      "v_mfma_f32_16x16x32_bf16 v[132:135], a[240:243], v[36:39], v[0:3]\n"
      "ds_write_b64 v244, v[208:209] offset:24704\n"
      "s_cbranch_vccnz LT12\n"
      "LS12:\n"
      "v_mfma_f32_16x16x32_bf16 v[132:135], a[244:247], v[40:43], v[132:135]\n"
      "v_and_b32 v19, 0xffff0000, v129\n"
      "v_mfma_f32_16x16x32_bf16 v[132:135], a[248:251], v[44:47], v[132:135]\n"
      "v_lshlrev_b32 v20, 16, v130\n"
      "v_mfma_f32_16x16x32_bf16 v[132:135], a[252:255], v[60:63], v[132:135]\n"
      "v_and_b32 v21, 0xffff0000, v130\n"
      "s_waitcnt lgkmcnt(5)\n"
      "v_mfma_f32_16x16x32_bf16 v[132:135], a[48:51], v[64:67], v[132:135]\n"
      "v_lshlrev_b32 v22, 16, v131\n"
      "v_mfma_f32_16x16x32_bf16 v[132:135], a[52:55], v[68:71], v[132:135]\n"
      "v_and_b32 v23, 0xffff0000, v131\n"
      "v_mfma_f32_16x16x32_bf16 v[132:135], a[56:59], v[84:87], v[132:135]\n"
      "v_cvt_pk_bf16_f32 v210, v136, v137\n"
      "v_mfma_f32_16x16x32_bf16 v[132:135], a[60:63], v[88:91], v[132:135]\n"
      "v_cvt_pk_bf16_f32 v211, v138, v139\n"
      "s_waitcnt lgkmcnt(1)\n"
      "v_mfma_f32_16x16x32_bf16 v[132:135], a[64:67], v[92:95], v[132:135]\n"
      "s_add_u32 s54, s44, s36\n"
      "s_addc_u32 s55, s45, 0\n"
      "v_mfma_f32_16x16x32_bf16 v[132:135], a[68:71], v[108:111], v[132:135]\n"
      "ds_read_b128 v[216:219], v246 offset:12288\n"
      "v_mfma_f32_16x16x32_bf16 v[132:135], a[72:75], v[112:115], v[132:135]\n"
      "ds_read_b128 v[220:223], v246 offset:13312\n"
      "v_mfma_f32_16x16x32_bf16 v[132:135], a[76:79], v[116:119], v[132:135]\n"
      "ds_read_b128 v[224:227], v246 offset:14336\n"
      "v_mfma_f32_16x16x32_bf16 v[136:139], a[80:83], v[36:39], v[4:7]\n"
      "ds_write_b64 v244, v[210:211] offset:24712\n"
      "v_mfma_f32_16x16x32_bf16 v[136:139], a[84:87], v[40:43], v[136:139]\n"
      "v_mfma_f32_16x16x32_bf16 v[136:139], a[88:91], v[44:47], v[136:139]\n"
      "v_mfma_f32_16x16x32_bf16 v[136:139], a[92:95], v[60:63], v[136:139]\n"
      "v_cvt_pk_bf16_f32 v208, v132, v133\n"
      "v_mfma_f32_16x16x32_bf16 v[136:139], a[96:99], v[64:67], v[136:139]\n"
      "v_cvt_pk_bf16_f32 v209, v134, v135\n"
      "v_mfma_f32_16x16x32_bf16 v[136:139], a[100:103], v[68:71], v[136:139]\n"
      "s_waitcnt lgkmcnt(3)\n"
      "v_mfma_f32_16x16x32_bf16 v[136:139], a[104:107], v[84:87], v[136:139]\n"
      "global_store_dwordx4 v247, v[216:219], s[54:55] offset:0 sc1\n"
      "v_mfma_f32_16x16x32_bf16 v[136:139], a[108:111], v[88:91], v[136:139]\n"
      "s_add_u32 s52, s42, s38\n"
      "s_addc_u32 s53, s43, 0\n"
      "v_mfma_f32_16x16x32_bf16 v[136:139], a[112:115], v[92:95], v[136:139]\n"
      "s_add_u32 m0, s17, 0x6000\n"
      "v_mfma_f32_16x16x32_bf16 v[136:139], a[116:119], v[108:111], v[136:139]\n"
      "ds_write_b64 v244, v[208:209] offset:0\n"
      "v_mfma_f32_16x16x32_bf16 v[136:139], a[120:123], v[112:115], v[136:139]\n"
      "global_load_lds_dwordx4 v236, s[52:53] offset:0 nt\n"
      "v_mfma_f32_16x16x32_bf16 v[136:139], a[124:127], v[116:119], v[136:139]\n"
      "v_mfma_f32_16x16x32_bf16 v[132:135], a[128:131], v[36:39], v[8:11]\n"
      "s_cbranch_vccnz LT13\n"
      "LS13:\n"
      "v_mfma_f32_16x16x32_bf16 v[132:135], a[132:135], v[40:43], v[132:135]\n"
      "v_mfma_f32_16x16x32_bf16 v[132:135], a[136:139], v[44:47], v[132:135]\n"
      "v_mfma_f32_16x16x32_bf16 v[132:135], a[140:143], v[60:63], v[132:135]\n"
      "v_cvt_pk_bf16_f32 v210, v136, v137\n"
      "v_mfma_f32_16x16x32_bf16 v[132:135], a[144:147], v[64:67], v[132:135]\n"
      "v_cvt_pk_bf16_f32 v211, v138, v139\n"
      "v_mfma_f32_16x16x32_bf16 v[132:135], a[148:151], v[68:71], v[132:135]\n"
      "v_mfma_f32_16x16x32_bf16 v[132:135], a[152:155], v[84:87], v[132:135]\n"
      "s_waitcnt lgkmcnt(3)\n"
      "v_mfma_f32_16x16x32_bf16 v[132:135], a[156:159], v[88:91], v[132:135]\n"
      "global_store_dwordx4 v252, v[220:223], s[54:55] offset:0 sc1\n"
      "v_mfma_f32_16x16x32_bf16 v[132:135], a[160:163], v[92:95], v[132:135]\n"
      "s_add_u32 m0, s17, 0x7f00\n"
      "v_mfma_f32_16x16x32_bf16 v[132:135], a[164:167], v[108:111], v[132:135]\n"
      "ds_write_b64 v244, v[210:211] offset:8\n"
      "v_mfma_f32_16x16x32_bf16 v[132:135], a[168:171], v[112:115], v[132:135]\n"
      "global_load_lds_dwordx4 v236, s[52:53] offset:256 nt\n"
      "v_mfma_f32_16x16x32_bf16 v[132:135], a[172:175], v[116:119], v[132:135]\n"
      "v_mfma_f32_16x16x32_bf16 v[136:139], a[176:179], v[36:39], v[12:15]\n"
      "v_mfma_f32_16x16x32_bf16 v[136:139], a[180:183], v[40:43], v[136:139]\n"
      "v_mfma_f32_16x16x32_bf16 v[136:139], a[184:187], v[44:47], v[136:139]\n"
      "v_mfma_f32_16x16x32_bf16 v[136:139], a[188:191], v[60:63], v[136:139]\n"
      "v_cvt_pk_bf16_f32 v212, v132, v133\n"
      "v_mfma_f32_16x16x32_bf16 v[136:139], a[192:195], v[64:67], v[136:139]\n"
      "v_cvt_pk_bf16_f32 v213, v134, v135\n"
      "v_mfma_f32_16x16x32_bf16 v[136:139], a[196:199], v[68:71], v[136:139]\n"
      "v_mfma_f32_16x16x32_bf16 v[136:139], a[200:203], v[84:87], v[136:139]\n"
      "v_mfma_f32_16x16x32_bf16 v[136:139], a[204:207], v[88:91], v[136:139]\n"
      "s_waitcnt lgkmcnt(3)\n"
      "v_mfma_f32_16x16x32_bf16 v[136:139], a[208:211], v[92:95], v[136:139]\n"
      "global_store_dwordx4 v253, v[224:227], s[54:55] offset:0 sc1\n"
      "v_mfma_f32_16x16x32_bf16 v[136:139], a[212:215], v[108:111], v[136:139]\n"
      "ds_write_b64 v245, v[212:213] offset:0\n"
      "v_mfma_f32_16x16x32_bf16 v[136:139], a[216:219], v[112:115], v[136:139]\n"
      "s_add_u32 m0, s17, 0x9e00\n"
      "v_mfma_f32_16x16x32_bf16 v[136:139], a[220:223], v[116:119], v[136:139]\n"
      "global_load_lds_dwordx4 v236, s[52:53] offset:512 nt\n"
      "v_mfma_f32_16x16x32_bf16 v[132:135], a[224:227], v[36:39], v[16:19]\n"
      "s_cbranch_vccnz LT14\n"
      "LS14:\n"
      "v_mfma_f32_16x16x32_bf16 v[132:135], a[228:231], v[40:43], v[132:135]\n"
      "v_mfma_f32_16x16x32_bf16 v[132:135], a[232:235], v[44:47], v[132:135]\n"
      "v_mfma_f32_16x16x32_bf16 v[132:135], a[236:239], v[60:63], v[132:135]\n"
      "v_cvt_pk_bf16_f32 v214, v136, v137\n"
      "v_mfma_f32_16x16x32_bf16 v[132:135], a[0:3], v[64:67], v[132:135]\n"
      "v_cvt_pk_bf16_f32 v215, v138, v139\n"
      "v_mfma_f32_16x16x32_bf16 v[132:135], a[4:7], v[68:71], v[132:135]\n"
      "v_mfma_f32_16x16x32_bf16 v[132:135], a[8:11], v[84:87], v[132:135]\n"
      "v_mfma_f32_16x16x32_bf16 v[132:135], a[12:15], v[88:91], v[132:135]\n"
      "v_mfma_f32_16x16x32_bf16 v[132:135], a[16:19], v[92:95], v[132:135]\n"
      "v_mfma_f32_16x16x32_bf16 v[132:135], a[20:23], v[108:111], v[132:135]\n"
      "ds_write_b64 v245, v[214:215] offset:8\n"
      "v_mfma_f32_16x16x32_bf16 v[132:135], v[140:143], v[112:115], v[132:135]\n"
      "v_mfma_f32_16x16x32_bf16 v[132:135], v[156:159], v[116:119], v[132:135]\n"
      "s_waitcnt lgkmcnt(4)\n"
      "s_barrier\n"
      "v_mfma_f32_16x16x32_bf16 v[136:139], v[160:163], v[36:39], v[20:23]\n"
      "ds_read_b128 v[36:39], v232 offset:256\n"
      "v_mfma_f32_16x16x32_bf16 v[136:139], v[164:167], v[40:43], v[136:139]\n"
      "ds_read_b128 v[40:43], v233 offset:256\n"
      "v_mfma_f32_16x16x32_bf16 v[136:139], v[180:183], v[44:47], v[136:139]\n"
      "ds_read_b128 v[44:47], v234 offset:256\n"
      "v_mfma_f32_16x16x32_bf16 v[136:139], v[184:187], v[60:63], v[136:139]\n"
      "ds_read_b128 v[60:63], v235 offset:256\n"
      "v_cvt_pk_bf16_f32 v208, v132, v133\n"
      "v_mfma_f32_16x16x32_bf16 v[136:139], v[188:191], v[64:67], v[136:139]\n"
      "ds_read_b128 v[64:67], v232 offset:8448\n"
      "v_cvt_pk_bf16_f32 v209, v134, v135\n"
      "v_mfma_f32_16x16x32_bf16 v[136:139], v[204:207], v[68:71], v[136:139]\n"
      "ds_read_b128 v[68:71], v233 offset:8448\n"
      "v_mfma_f32_16x16x32_bf16 v[136:139], a[24:27], v[84:87], v[136:139]\n"
      "ds_read_b128 v[84:87], v234 offset:8448\n"
      "v_mfma_f32_16x16x32_bf16 v[136:139], a[28:31], v[88:91], v[136:139]\n"
      "ds_read_b128 v[88:91], v235 offset:8448\n"
      "v_mfma_f32_16x16x32_bf16 v[136:139], a[32:35], v[92:95], v[136:139]\n"
      "ds_read_b128 v[92:95], v232 offset:16640\n"
      "v_mfma_f32_16x16x32_bf16 v[136:139], a[36:39], v[108:111], v[136:139]\n"
      "ds_read_b128 v[108:111], v233 offset:16640\n"
      "v_mfma_f32_16x16x32_bf16 v[136:139], a[40:43], v[112:115], v[136:139]\n"
      "ds_read_b128 v[112:115], v234 offset:16640\n"
      "v_mfma_f32_16x16x32_bf16 v[136:139], a[44:47], v[116:119], v[136:139]\n"
      "ds_read_b128 v[116:119], v235 offset:16640\n"
      "s_waitcnt lgkmcnt(8)\n"
      "v_mfma_f32_16x16x32_bf16 v[132:135], a[240:243], v[36:39], v[0:3]\n"
      "ds_write_b64 v244, v[208:209] offset:128\n"
      "v_mfma_f32_16x16x32_bf16 v[132:135], a[244:247], v[40:43], v[132:135]\n"
      "s_cbranch_vccnz LT15\n"
      "LS15:\n"
      "v_mfma_f32_16x16x32_bf16 v[132:135], a[248:251], v[44:47], v[132:135]\n"
      "v_mfma_f32_16x16x32_bf16 v[132:135], a[252:255], v[60:63], v[132:135]\n"
      "v_cvt_pk_bf16_f32 v210, v136, v137\n"
      "s_waitcnt lgkmcnt(5)\n"
      "v_mfma_f32_16x16x32_bf16 v[132:135], a[48:51], v[64:67], v[132:135]\n"
      "v_cvt_pk_bf16_f32 v211, v138, v139\n"
      "v_mfma_f32_16x16x32_bf16 v[132:135], a[52:55], v[68:71], v[132:135]\n"
      "v_mfma_f32_16x16x32_bf16 v[132:135], a[56:59], v[84:87], v[132:135]\n"
      "ds_read_b128 v[216:219], v246 offset:24576\n"
      "v_mfma_f32_16x16x32_bf16 v[132:135], a[60:63], v[88:91], v[132:135]\n"
      "ds_read_b128 v[220:223], v246 offset:25600\n"
      "s_waitcnt lgkmcnt(3)\n"
      "v_mfma_f32_16x16x32_bf16 v[132:135], a[64:67], v[92:95], v[132:135]\n"
      "ds_read_b128 v[224:227], v246 offset:26624\n"
      "v_mfma_f32_16x16x32_bf16 v[132:135], a[68:71], v[108:111], v[132:135]\n"
      "ds_write_b64 v244, v[210:211] offset:136\n"
      "v_mfma_f32_16x16x32_bf16 v[132:135], a[72:75], v[112:115], v[132:135]\n"
      "v_mfma_f32_16x16x32_bf16 v[132:135], a[76:79], v[116:119], v[132:135]\n"
      "v_mfma_f32_16x16x32_bf16 v[136:139], a[80:83], v[36:39], v[4:7]\n"
      "v_mfma_f32_16x16x32_bf16 v[136:139], a[84:87], v[40:43], v[136:139]\n"
      "v_mfma_f32_16x16x32_bf16 v[136:139], a[88:91], v[44:47], v[136:139]\n"
      "v_mfma_f32_16x16x32_bf16 v[136:139], a[92:95], v[60:63], v[136:139]\n"
      "v_cvt_pk_bf16_f32 v208, v132, v133\n"
      "v_mfma_f32_16x16x32_bf16 v[136:139], a[96:99], v[64:67], v[136:139]\n"
      "v_cvt_pk_bf16_f32 v209, v134, v135\n"
      "v_mfma_f32_16x16x32_bf16 v[136:139], a[100:103], v[68:71], v[136:139]\n"
      "s_waitcnt lgkmcnt(3)\n"
      "v_mfma_f32_16x16x32_bf16 v[136:139], a[104:107], v[84:87], v[136:139]\n"
      "global_store_dwordx4 v247, v[216:219], s[54:55] offset:768 sc1\n"
      "v_mfma_f32_16x16x32_bf16 v[136:139], a[108:111], v[88:91], v[136:139]\n"
      "s_add_u32 m0, s17, 0x7000\n"
      "v_mfma_f32_16x16x32_bf16 v[136:139], a[112:115], v[92:95], v[136:139]\n"
      "global_load_lds_dwordx4 v237, s[52:53] offset:0 nt\n"
      "v_mfma_f32_16x16x32_bf16 v[136:139], a[116:119], v[108:111], v[136:139]\n"
      "ds_write_b64 v244, v[208:209] offset:12288\n"
      "v_mfma_f32_16x16x32_bf16 v[136:139], a[120:123], v[112:115], v[136:139]\n"
      "v_mfma_f32_16x16x32_bf16 v[136:139], a[124:127], v[116:119], v[136:139]\n"
      "v_mfma_f32_16x16x32_bf16 v[132:135], a[128:131], v[36:39], v[8:11]\n"
      "s_cbranch_vccnz LT16\n"
      "LS16:\n"
      "v_mfma_f32_16x16x32_bf16 v[132:135], a[132:135], v[40:43], v[132:135]\n"
      "v_mfma_f32_16x16x32_bf16 v[132:135], a[136:139], v[44:47], v[132:135]\n"
      "v_mfma_f32_16x16x32_bf16 v[132:135], a[140:143], v[60:63], v[132:135]\n"
      "v_cvt_pk_bf16_f32 v210, v136, v137\n"
      "v_mfma_f32_16x16x32_bf16 v[132:135], a[144:147], v[64:67], v[132:135]\n"
      "v_cvt_pk_bf16_f32 v211, v138, v139\n"
      "v_mfma_f32_16x16x32_bf16 v[132:135], a[148:151], v[68:71], v[132:135]\n"
      "v_mfma_f32_16x16x32_bf16 v[132:135], a[152:155], v[84:87], v[132:135]\n"
      "s_waitcnt lgkmcnt(3)\n"
      "v_mfma_f32_16x16x32_bf16 v[132:135], a[156:159], v[88:91], v[132:135]\n"
      "global_store_dwordx4 v252, v[220:223], s[54:55] offset:768 sc1\n"
      "v_mfma_f32_16x16x32_bf16 v[132:135], a[160:163], v[92:95], v[132:135]\n"
      "s_add_u32 m0, s17, 0x8f00\n"
      "v_mfma_f32_16x16x32_bf16 v[132:135], a[164:167], v[108:111], v[132:135]\n"
      "ds_write_b64 v244, v[210:211] offset:12296\n"
      "v_mfma_f32_16x16x32_bf16 v[132:135], a[168:171], v[112:115], v[132:135]\n"
      "global_load_lds_dwordx4 v237, s[52:53] offset:256 nt\n"
      "v_mfma_f32_16x16x32_bf16 v[132:135], a[172:175], v[116:119], v[132:135]\n"
      "v_mfma_f32_16x16x32_bf16 v[136:139], a[176:179], v[36:39], v[12:15]\n"
      "v_mfma_f32_16x16x32_bf16 v[136:139], a[180:183], v[40:43], v[136:139]\n"
      "v_mfma_f32_16x16x32_bf16 v[136:139], a[184:187], v[44:47], v[136:139]\n"
      "v_mfma_f32_16x16x32_bf16 v[136:139], a[188:191], v[60:63], v[136:139]\n"
      "v_cvt_pk_bf16_f32 v212, v132, v133\n"
      "v_mfma_f32_16x16x32_bf16 v[136:139], a[192:195], v[64:67], v[136:139]\n"
      "v_cvt_pk_bf16_f32 v213, v134, v135\n"
      "v_mfma_f32_16x16x32_bf16 v[136:139], a[196:199], v[68:71], v[136:139]\n"
      "v_mfma_f32_16x16x32_bf16 v[136:139], a[200:203], v[84:87], v[136:139]\n"
      "v_mfma_f32_16x16x32_bf16 v[136:139], a[204:207], v[88:91], v[136:139]\n"
      "s_waitcnt lgkmcnt(3)\n"
      "v_mfma_f32_16x16x32_bf16 v[136:139], a[208:211], v[92:95], v[136:139]\n"
      "global_store_dwordx4 v253, v[224:227], s[54:55] offset:768 sc1\n"
      "v_mfma_f32_16x16x32_bf16 v[136:139], a[212:215], v[108:111], v[136:139]\n"
      "ds_write_b64 v245, v[212:213] offset:12288\n"
      "v_mfma_f32_16x16x32_bf16 v[136:139], a[216:219], v[112:115], v[136:139]\n"
      "s_add_u32 m0, s17, 0xae00\n"
      "v_mfma_f32_16x16x32_bf16 v[136:139], a[220:223], v[116:119], v[136:139]\n"
      "global_load_lds_dwordx4 v237, s[52:53] offset:512 nt\n"
      "v_mfma_f32_16x16x32_bf16 v[132:135], a[224:227], v[36:39], v[16:19]\n"
      "s_cbranch_vccnz LT17\n"
      "LS17:\n"
      "v_mfma_f32_16x16x32_bf16 v[132:135], a[228:231], v[40:43], v[132:135]\n"
      "v_mfma_f32_16x16x32_bf16 v[132:135], a[232:235], v[44:47], v[132:135]\n"
      "v_mfma_f32_16x16x32_bf16 v[132:135], a[236:239], v[60:63], v[132:135]\n"
      "s_memrealtime s[84:85]\n"
      "v_mfma_f32_16x16x32_bf16 v[132:135], a[0:3], v[64:67], v[132:135]\n"
      "v_cvt_pk_bf16_f32 v214, v136, v137\n"
      "v_mfma_f32_16x16x32_bf16 v[132:135], a[4:7], v[68:71], v[132:135]\n"
      "v_cvt_pk_bf16_f32 v215, v138, v139\n"
      "v_mfma_f32_16x16x32_bf16 v[132:135], a[8:11], v[84:87], v[132:135]\n"
      "v_mfma_f32_16x16x32_bf16 v[132:135], a[12:15], v[88:91], v[132:135]\n"
      "s_add_u32 s83, s83, s86\n"
      "v_mfma_f32_16x16x32_bf16 v[132:135], a[16:19], v[92:95], v[132:135]\n"
      "v_mfma_f32_16x16x32_bf16 v[132:135], a[20:23], v[108:111], v[132:135]\n"
      "v_mfma_f32_16x16x32_bf16 v[132:135], v[140:143], v[112:115], v[132:135]\n"
      "s_waitcnt lgkmcnt(0)\n"
      "s_sub_u32 s84, s84, s83\n"
      "v_mfma_f32_16x16x32_bf16 v[132:135], v[156:159], v[116:119], v[132:135]\n"
      "s_cmp_lt_i32 s84, 0\n"
      "s_cselect_b64 vcc, -1, 0\n"
      "s_waitcnt vmcnt(24)\n"
      "s_barrier\n"
      "v_mfma_f32_16x16x32_bf16 v[136:139], v[160:163], v[36:39], v[20:23]\n"
      "ds_read_b128 v[36:39], v232 offset:24576\n"
      "ds_write_b64 v245, v[214:215] offset:12296\n"
      "v_mfma_f32_16x16x32_bf16 v[136:139], v[164:167], v[40:43], v[136:139]\n"
      "ds_read_b128 v[40:43], v233 offset:24576\n"
      "s_lshl_b32 s79, s94, 1\n"
      "v_mfma_f32_16x16x32_bf16 v[136:139], v[180:183], v[44:47], v[136:139]\n"
      "ds_read_b128 v[44:47], v234 offset:24576\n"
      "v_mad_i32_i24 v252, v251, s79, v252\n"
      "v_mfma_f32_16x16x32_bf16 v[136:139], v[184:187], v[60:63], v[136:139]\n"
      "ds_read_b128 v[60:63], v235 offset:24576\n"
      "v_cvt_pk_bf16_f32 v208, v132, v133\n"
      "v_mfma_f32_16x16x32_bf16 v[136:139], v[188:191], v[64:67], v[136:139]\n"
      "ds_read_b128 v[64:67], v232 offset:32768\n"
      "v_cvt_pk_bf16_f32 v209, v134, v135\n"
      "v_mfma_f32_16x16x32_bf16 v[136:139], v[204:207], v[68:71], v[136:139]\n"
      "ds_read_b128 v[68:71], v233 offset:32768\n"
      "s_mul_i32 s79, s79, s100\n"
      "v_mfma_f32_16x16x32_bf16 v[136:139], a[24:27], v[84:87], v[136:139]\n"
      "ds_read_b128 v[84:87], v234 offset:32768\n"
      "v_add_u32 v247, s79, v247\n"
      "v_mfma_f32_16x16x32_bf16 v[136:139], a[28:31], v[88:91], v[136:139]\n"
      "ds_read_b128 v[88:91], v235 offset:32768\n"
      "s_mul_i32 s79, s79, s101\n"
      "v_mfma_f32_16x16x32_bf16 v[136:139], a[32:35], v[92:95], v[136:139]\n"
      "ds_read_b128 v[92:95], v232 offset:40960\n"
      "v_add_u32 v253, s79, v253\n"
      "v_mfma_f32_16x16x32_bf16 v[136:139], a[36:39], v[108:111], v[136:139]\n"
      "ds_read_b128 v[108:111], v233 offset:40960\n"
      "v_mfma_f32_16x16x32_bf16 v[136:139], a[40:43], v[112:115], v[136:139]\n"
      "ds_read_b128 v[112:115], v234 offset:40960\n"
      "v_mfma_f32_16x16x32_bf16 v[136:139], a[44:47], v[116:119], v[136:139]\n"
      "ds_read_b128 v[116:119], v235 offset:40960\n"
      "s_waitcnt lgkmcnt(8)\n"
      "v_mfma_f32_16x16x32_bf16 v[132:135], a[240:243], v[36:39], v[0:3]\n"
      "ds_write_b64 v244, v[208:209] offset:12416\n"
      "v_mfma_f32_16x16x32_bf16 v[132:135], a[244:247], v[40:43], v[132:135]\n"
      "s_add_u32 s54, s30, s37\n"
      "s_addc_u32 s55, s31, 0\n"
      "v_mfma_f32_16x16x32_bf16 v[132:135], a[248:251], v[44:47], v[132:135]\n"
      "s_cbranch_vccnz LT18\n"
      "LS18:\n"
      "v_mfma_f32_16x16x32_bf16 v[132:135], a[252:255], v[60:63], v[132:135]\n"
      "v_cvt_pk_bf16_f32 v210, v136, v137\n"
      "s_waitcnt lgkmcnt(5)\n"
      "v_mfma_f32_16x16x32_bf16 v[132:135], a[48:51], v[64:67], v[132:135]\n"
      "v_cvt_pk_bf16_f32 v211, v138, v139\n"
      "v_mfma_f32_16x16x32_bf16 v[132:135], a[52:55], v[68:71], v[132:135]\n"
      "s_mul_i32 s78, s96, s19\n"
      "v_mfma_f32_16x16x32_bf16 v[132:135], a[56:59], v[84:87], v[132:135]\n"
      "ds_read_b128 v[216:219], v246 offset:0\n"
      "v_add_u32 v236, s78, v236\n"
      "v_mfma_f32_16x16x32_bf16 v[132:135], a[60:63], v[88:91], v[132:135]\n"
      "ds_read_b128 v[220:223], v246 offset:1024\n"
      "s_waitcnt lgkmcnt(3)\n"
      "v_mfma_f32_16x16x32_bf16 v[132:135], a[64:67], v[92:95], v[132:135]\n"
      "ds_read_b128 v[224:227], v246 offset:2048\n"
      "v_mfma_f32_16x16x32_bf16 v[132:135], a[68:71], v[108:111], v[132:135]\n"
      "ds_write_b64 v244, v[210:211] offset:12424\n"
      "v_mfma_f32_16x16x32_bf16 v[132:135], a[72:75], v[112:115], v[132:135]\n"
      "s_mul_i32 s78, s78, s99\n"
      "v_mfma_f32_16x16x32_bf16 v[132:135], a[76:79], v[116:119], v[132:135]\n"
      "v_add_u32 v237, s78, v237\n"
      "v_mfma_f32_16x16x32_bf16 v[136:139], a[80:83], v[36:39], v[4:7]\n"
      "v_mfma_f32_16x16x32_bf16 v[136:139], a[84:87], v[40:43], v[136:139]\n"
      "v_mfma_f32_16x16x32_bf16 v[136:139], a[88:91], v[44:47], v[136:139]\n"
      "v_mfma_f32_16x16x32_bf16 v[136:139], a[92:95], v[60:63], v[136:139]\n"
      "v_cvt_pk_bf16_f32 v208, v132, v133\n"
      "v_mfma_f32_16x16x32_bf16 v[136:139], a[96:99], v[64:67], v[136:139]\n"
      "v_cvt_pk_bf16_f32 v209, v134, v135\n"
      "v_mfma_f32_16x16x32_bf16 v[136:139], a[100:103], v[68:71], v[136:139]\n"
      "s_waitcnt lgkmcnt(3)\n"
      "v_mfma_f32_16x16x32_bf16 v[136:139], a[104:107], v[84:87], v[136:139]\n"
      "global_store_dwordx4 v247, v[216:219], s[54:55] offset:0 sc1\n"
      "v_mfma_f32_16x16x32_bf16 v[136:139], a[108:111], v[88:91], v[136:139]\n"
      "s_add_u32 s52, s20, s39\n"
      "s_addc_u32 s53, s21, 0\n"
      "v_mfma_f32_16x16x32_bf16 v[136:139], a[112:115], v[92:95], v[136:139]\n"
      "s_add_u32 m0, s17, 0xc000\n"
      "v_mfma_f32_16x16x32_bf16 v[136:139], a[116:119], v[108:111], v[136:139]\n"
      "ds_write_b64 v244, v[208:209] offset:24576\n"
      "v_mfma_f32_16x16x32_bf16 v[136:139], a[120:123], v[112:115], v[136:139]\n"
      "global_load_lds_dwordx4 v236, s[52:53] offset:0 nt\n"
      "v_mfma_f32_16x16x32_bf16 v[136:139], a[124:127], v[116:119], v[136:139]\n"
      "v_mfma_f32_16x16x32_bf16 v[132:135], a[128:131], v[36:39], v[8:11]\n"
      "s_cbranch_vccnz LT19\n"
      "LS19:\n"
      "v_mfma_f32_16x16x32_bf16 v[132:135], a[132:135], v[40:43], v[132:135]\n"
      "v_mfma_f32_16x16x32_bf16 v[132:135], a[136:139], v[44:47], v[132:135]\n"
      "v_mfma_f32_16x16x32_bf16 v[132:135], a[140:143], v[60:63], v[132:135]\n"
      "v_cvt_pk_bf16_f32 v210, v136, v137\n"
      "v_mfma_f32_16x16x32_bf16 v[132:135], a[144:147], v[64:67], v[132:135]\n"
      "v_cvt_pk_bf16_f32 v211, v138, v139\n"
      "v_mfma_f32_16x16x32_bf16 v[132:135], a[148:151], v[68:71], v[132:135]\n"
      "v_mfma_f32_16x16x32_bf16 v[132:135], a[152:155], v[84:87], v[132:135]\n"
      "s_waitcnt lgkmcnt(3)\n"
      "v_mfma_f32_16x16x32_bf16 v[132:135], a[156:159], v[88:91], v[132:135]\n"
      "global_store_dwordx4 v252, v[220:223], s[54:55] offset:0 sc1\n"
      "v_mfma_f32_16x16x32_bf16 v[132:135], a[160:163], v[92:95], v[132:135]\n"
      "s_add_u32 m0, s17, 0xdf00\n"
      "v_mfma_f32_16x16x32_bf16 v[132:135], a[164:167], v[108:111], v[132:135]\n"
      "ds_write_b64 v244, v[210:211] offset:24584\n"
      "v_mfma_f32_16x16x32_bf16 v[132:135], a[168:171], v[112:115], v[132:135]\n"
      "global_load_lds_dwordx4 v236, s[52:53] offset:256 nt\n"
      "v_mfma_f32_16x16x32_bf16 v[132:135], a[172:175], v[116:119], v[132:135]\n"
      "v_mfma_f32_16x16x32_bf16 v[136:139], a[176:179], v[36:39], v[12:15]\n"
      "v_mfma_f32_16x16x32_bf16 v[136:139], a[180:183], v[40:43], v[136:139]\n"
      "v_mfma_f32_16x16x32_bf16 v[136:139], a[184:187], v[44:47], v[136:139]\n"
      "v_mfma_f32_16x16x32_bf16 v[136:139], a[188:191], v[60:63], v[136:139]\n"
      "v_cvt_pk_bf16_f32 v212, v132, v133\n"
      "v_mfma_f32_16x16x32_bf16 v[136:139], a[192:195], v[64:67], v[136:139]\n"
      "v_cvt_pk_bf16_f32 v213, v134, v135\n"
      "v_mfma_f32_16x16x32_bf16 v[136:139], a[196:199], v[68:71], v[136:139]\n"
      "v_mfma_f32_16x16x32_bf16 v[136:139], a[200:203], v[84:87], v[136:139]\n"
      "v_mfma_f32_16x16x32_bf16 v[136:139], a[204:207], v[88:91], v[136:139]\n"
      "s_waitcnt lgkmcnt(3)\n"
      "v_mfma_f32_16x16x32_bf16 v[136:139], a[208:211], v[92:95], v[136:139]\n"
      "global_store_dwordx4 v253, v[224:227], s[54:55] offset:0 sc1\n"
      "v_mfma_f32_16x16x32_bf16 v[136:139], a[212:215], v[108:111], v[136:139]\n"
      "ds_write_b64 v245, v[212:213] offset:24576\n"
      "v_mfma_f32_16x16x32_bf16 v[136:139], a[216:219], v[112:115], v[136:139]\n"
      "s_add_u32 m0, s17, 0xfe00\n"
      "v_mfma_f32_16x16x32_bf16 v[136:139], a[220:223], v[116:119], v[136:139]\n"
      "global_load_lds_dwordx4 v236, s[52:53] offset:512 nt\n"
      "v_mfma_f32_16x16x32_bf16 v[132:135], a[224:227], v[36:39], v[16:19]\n"
      "s_cbranch_vccnz LT20\n"
      "LS20:\n"
      "v_mfma_f32_16x16x32_bf16 v[132:135], a[228:231], v[40:43], v[132:135]\n"
      "v_mfma_f32_16x16x32_bf16 v[132:135], a[232:235], v[44:47], v[132:135]\n"
      "v_mfma_f32_16x16x32_bf16 v[132:135], a[236:239], v[60:63], v[132:135]\n"
      "v_cvt_pk_bf16_f32 v214, v136, v137\n"
      "v_mfma_f32_16x16x32_bf16 v[132:135], a[0:3], v[64:67], v[132:135]\n"
      "v_cvt_pk_bf16_f32 v215, v138, v139\n"
      "v_mfma_f32_16x16x32_bf16 v[132:135], a[4:7], v[68:71], v[132:135]\n"
      "v_mfma_f32_16x16x32_bf16 v[132:135], a[8:11], v[84:87], v[132:135]\n"
      "v_mfma_f32_16x16x32_bf16 v[132:135], a[12:15], v[88:91], v[132:135]\n"
      "v_mfma_f32_16x16x32_bf16 v[132:135], a[16:19], v[92:95], v[132:135]\n"
      "v_mfma_f32_16x16x32_bf16 v[132:135], a[20:23], v[108:111], v[132:135]\n"
      "ds_write_b64 v245, v[214:215] offset:24584\n"
      "v_mfma_f32_16x16x32_bf16 v[132:135], v[140:143], v[112:115], v[132:135]\n"
      "v_mfma_f32_16x16x32_bf16 v[132:135], v[156:159], v[116:119], v[132:135]\n"
      "s_waitcnt lgkmcnt(4)\n"
      "s_barrier\n"
      "v_mfma_f32_16x16x32_bf16 v[136:139], v[160:163], v[36:39], v[20:23]\n"
      "ds_read_b128 v[36:39], v232 offset:24832\n"
      "v_mfma_f32_16x16x32_bf16 v[136:139], v[164:167], v[40:43], v[136:139]\n"
      "ds_read_b128 v[40:43], v233 offset:24832\n"
      "v_mfma_f32_16x16x32_bf16 v[136:139], v[180:183], v[44:47], v[136:139]\n"
      "ds_read_b128 v[44:47], v234 offset:24832\n"
      "v_mfma_f32_16x16x32_bf16 v[136:139], v[184:187], v[60:63], v[136:139]\n"
      "ds_read_b128 v[60:63], v235 offset:24832\n"
      "v_cvt_pk_bf16_f32 v208, v132, v133\n"
      "v_mfma_f32_16x16x32_bf16 v[136:139], v[188:191], v[64:67], v[136:139]\n"
      "ds_read_b128 v[64:67], v232 offset:33024\n"
      "v_cvt_pk_bf16_f32 v209, v134, v135\n"
      "v_mfma_f32_16x16x32_bf16 v[136:139], v[204:207], v[68:71], v[136:139]\n"
      "ds_read_b128 v[68:71], v233 offset:33024\n"
      "v_mfma_f32_16x16x32_bf16 v[136:139], a[24:27], v[84:87], v[136:139]\n"
      "ds_read_b128 v[84:87], v234 offset:33024\n"
      "v_mfma_f32_16x16x32_bf16 v[136:139], a[28:31], v[88:91], v[136:139]\n"
      "ds_read_b128 v[88:91], v235 offset:33024\n"
      "v_mfma_f32_16x16x32_bf16 v[136:139], a[32:35], v[92:95], v[136:139]\n"
      "ds_read_b128 v[92:95], v232 offset:41216\n"
      "v_mfma_f32_16x16x32_bf16 v[136:139], a[36:39], v[108:111], v[136:139]\n"
      "ds_read_b128 v[108:111], v233 offset:41216\n"
      "ds_write_b64 v244, v[208:209] offset:24704\n"
      "v_mfma_f32_16x16x32_bf16 v[136:139], a[40:43], v[112:115], v[136:139]\n"
      "ds_read_b128 v[112:115], v234 offset:41216\n"
      "v_mfma_f32_16x16x32_bf16 v[136:139], a[44:47], v[116:119], v[136:139]\n"
      "ds_read_b128 v[116:119], v235 offset:41216\n"
      "s_waitcnt lgkmcnt(9)\n"
      "v_mfma_f32_16x16x32_bf16 v[132:135], a[240:243], v[36:39], v[0:3]\n"
      "v_add_u32 v232, 0xffff4000, v232\n"
      "s_cbranch_vccnz LT21\n"
      "LS21:\n"
      "v_mfma_f32_16x16x32_bf16 v[132:135], a[244:247], v[40:43], v[132:135]\n"
      "v_add_u32 v233, 0xffff4000, v233\n"
      "v_mfma_f32_16x16x32_bf16 v[132:135], a[248:251], v[44:47], v[132:135]\n"
      "v_add_u32 v234, 0xffff4000, v234\n"
      "v_mfma_f32_16x16x32_bf16 v[132:135], a[252:255], v[60:63], v[132:135]\n"
      "v_add_u32 v235, 0xffff4000, v235\n"
      "s_waitcnt lgkmcnt(5)\n"
      "v_mfma_f32_16x16x32_bf16 v[132:135], a[48:51], v[64:67], v[132:135]\n"
      "v_cvt_pk_bf16_f32 v210, v136, v137\n"
      "v_mfma_f32_16x16x32_bf16 v[132:135], a[52:55], v[68:71], v[132:135]\n"
      "v_cvt_pk_bf16_f32 v211, v138, v139\n"
      "v_mfma_f32_16x16x32_bf16 v[132:135], a[56:59], v[84:87], v[132:135]\n"
      "ds_read_b128 v[216:219], v246 offset:12288\n"
      "v_mfma_f32_16x16x32_bf16 v[132:135], a[60:63], v[88:91], v[132:135]\n"
      "ds_read_b128 v[220:223], v246 offset:13312\n"
      "s_waitcnt lgkmcnt(2)\n"
      "v_mfma_f32_16x16x32_bf16 v[132:135], a[64:67], v[92:95], v[132:135]\n"
      "ds_read_b128 v[224:227], v246 offset:14336\n"
      "v_mfma_f32_16x16x32_bf16 v[132:135], a[68:71], v[108:111], v[132:135]\n"
      "v_lshlrev_b32 v0, 16, v144\n"
      "v_mfma_f32_16x16x32_bf16 v[132:135], a[72:75], v[112:115], v[132:135]\n"
      "ds_write_b64 v244, v[210:211] offset:24712\n"
      "v_mfma_f32_16x16x32_bf16 v[132:135], a[76:79], v[116:119], v[132:135]\n"
      "v_and_b32 v1, 0xffff0000, v144\n"
      "v_mfma_f32_16x16x32_bf16 v[136:139], a[80:83], v[36:39], v[4:7]\n"
      "v_lshlrev_b32 v2, 16, v145\n"
      "v_mfma_f32_16x16x32_bf16 v[136:139], a[84:87], v[40:43], v[136:139]\n"
      "v_and_b32 v3, 0xffff0000, v145\n"
      "v_mfma_f32_16x16x32_bf16 v[136:139], a[88:91], v[44:47], v[136:139]\n"
      "v_mfma_f32_16x16x32_bf16 v[136:139], a[92:95], v[60:63], v[136:139]\n"
      "v_cvt_pk_bf16_f32 v208, v132, v133\n"
      "v_mfma_f32_16x16x32_bf16 v[136:139], a[96:99], v[64:67], v[136:139]\n"
      "v_cvt_pk_bf16_f32 v209, v134, v135\n"
      "v_mfma_f32_16x16x32_bf16 v[136:139], a[100:103], v[68:71], v[136:139]\n"
      "s_waitcnt lgkmcnt(3)\n"
      "v_mfma_f32_16x16x32_bf16 v[136:139], a[104:107], v[84:87], v[136:139]\n"
      "global_store_dwordx4 v247, v[216:219], s[54:55] offset:768 sc1\n"
      "v_mfma_f32_16x16x32_bf16 v[136:139], a[108:111], v[88:91], v[136:139]\n"
      "s_add_u32 m0, s17, 0xd000\n"
      "v_mfma_f32_16x16x32_bf16 v[136:139], a[112:115], v[92:95], v[136:139]\n"
      "global_load_lds_dwordx4 v237, s[52:53] offset:0 nt\n"
      "v_mfma_f32_16x16x32_bf16 v[136:139], a[116:119], v[108:111], v[136:139]\n"
      "ds_write_b64 v244, v[208:209] offset:0\n"
      "v_mfma_f32_16x16x32_bf16 v[136:139], a[120:123], v[112:115], v[136:139]\n"
      "v_lshlrev_b32 v4, 16, v146\n"
      "v_mfma_f32_16x16x32_bf16 v[136:139], a[124:127], v[116:119], v[136:139]\n"
      "v_and_b32 v5, 0xffff0000, v146\n"
      "v_mfma_f32_16x16x32_bf16 v[132:135], a[128:131], v[36:39], v[8:11]\n"
      "v_lshlrev_b32 v6, 16, v147\n"
      "v_mfma_f32_16x16x32_bf16 v[132:135], a[132:135], v[40:43], v[132:135]\n"
      "v_and_b32 v7, 0xffff0000, v147\n"
      "v_mfma_f32_16x16x32_bf16 v[132:135], a[136:139], v[44:47], v[132:135]\n"
      "s_cbranch_vccnz LT22\n"
      "LS22:\n"
      "v_mfma_f32_16x16x32_bf16 v[132:135], a[140:143], v[60:63], v[132:135]\n"
      "v_cvt_pk_bf16_f32 v210, v136, v137\n"
      "v_mfma_f32_16x16x32_bf16 v[132:135], a[144:147], v[64:67], v[132:135]\n"
      "v_cvt_pk_bf16_f32 v211, v138, v139\n"
      "v_mfma_f32_16x16x32_bf16 v[132:135], a[148:151], v[68:71], v[132:135]\n"
      "v_lshlrev_b32 v8, 16, v148\n"
      "v_mfma_f32_16x16x32_bf16 v[132:135], a[152:155], v[84:87], v[132:135]\n"
      "s_waitcnt lgkmcnt(3)\n"
      "v_mfma_f32_16x16x32_bf16 v[132:135], a[156:159], v[88:91], v[132:135]\n"
      "global_store_dwordx4 v252, v[220:223], s[54:55] offset:768 sc1\n"
      "v_mfma_f32_16x16x32_bf16 v[132:135], a[160:163], v[92:95], v[132:135]\n"
      "s_add_u32 m0, s17, 0xef00\n"
      "v_mfma_f32_16x16x32_bf16 v[132:135], a[164:167], v[108:111], v[132:135]\n"
      "ds_write_b64 v244, v[210:211] offset:8\n"
      "v_mfma_f32_16x16x32_bf16 v[132:135], a[168:171], v[112:115], v[132:135]\n"
      "global_load_lds_dwordx4 v237, s[52:53] offset:256 nt\n"
      "v_mfma_f32_16x16x32_bf16 v[132:135], a[172:175], v[116:119], v[132:135]\n"
      "v_and_b32 v9, 0xffff0000, v148\n"
      "v_mfma_f32_16x16x32_bf16 v[136:139], a[176:179], v[36:39], v[12:15]\n"
      "v_lshlrev_b32 v10, 16, v149\n"
      "v_mfma_f32_16x16x32_bf16 v[136:139], a[180:183], v[40:43], v[136:139]\n"
      "v_and_b32 v11, 0xffff0000, v149\n"
      "v_mfma_f32_16x16x32_bf16 v[136:139], a[184:187], v[44:47], v[136:139]\n"
      "v_mfma_f32_16x16x32_bf16 v[136:139], a[188:191], v[60:63], v[136:139]\n"
      "v_cvt_pk_bf16_f32 v212, v132, v133\n"
      "v_mfma_f32_16x16x32_bf16 v[136:139], a[192:195], v[64:67], v[136:139]\n"
      "v_cvt_pk_bf16_f32 v213, v134, v135\n"
      "v_mfma_f32_16x16x32_bf16 v[136:139], a[196:199], v[68:71], v[136:139]\n"
      "v_lshlrev_b32 v12, 16, v150\n"
      "v_mfma_f32_16x16x32_bf16 v[136:139], a[200:203], v[84:87], v[136:139]\n"
      "v_and_b32 v13, 0xffff0000, v150\n"
      "v_mfma_f32_16x16x32_bf16 v[136:139], a[204:207], v[88:91], v[136:139]\n"
      "s_waitcnt lgkmcnt(3)\n"
      "v_mfma_f32_16x16x32_bf16 v[136:139], a[208:211], v[92:95], v[136:139]\n"
      "global_store_dwordx4 v253, v[224:227], s[54:55] offset:768 sc1\n"
      "v_mfma_f32_16x16x32_bf16 v[136:139], a[212:215], v[108:111], v[136:139]\n"
      "ds_write_b64 v245, v[212:213] offset:0\n"
      "v_mfma_f32_16x16x32_bf16 v[136:139], a[216:219], v[112:115], v[136:139]\n"
      "s_add_u32 m0, s17, 0x10e00\n"
      "v_mfma_f32_16x16x32_bf16 v[136:139], a[220:223], v[116:119], v[136:139]\n"
      "global_load_lds_dwordx4 v237, s[52:53] offset:512 nt\n"
      "v_mfma_f32_16x16x32_bf16 v[132:135], a[224:227], v[36:39], v[16:19]\n"
      "v_lshlrev_b32 v14, 16, v151\n"
      "v_mfma_f32_16x16x32_bf16 v[132:135], a[228:231], v[40:43], v[132:135]\n"
      "v_and_b32 v15, 0xffff0000, v151\n"
      "v_mfma_f32_16x16x32_bf16 v[132:135], a[232:235], v[44:47], v[132:135]\n"
      "s_cbranch_vccnz LT23\n"
      "LS23:\n"
      "v_mfma_f32_16x16x32_bf16 v[132:135], a[236:239], v[60:63], v[132:135]\n"
      "s_memrealtime s[84:85]\n"
      "v_mfma_f32_16x16x32_bf16 v[132:135], a[0:3], v[64:67], v[132:135]\n"
      "v_cvt_pk_bf16_f32 v214, v136, v137\n"
      "v_mfma_f32_16x16x32_bf16 v[132:135], a[4:7], v[68:71], v[132:135]\n"
      "v_cvt_pk_bf16_f32 v215, v138, v139\n"
      "v_mfma_f32_16x16x32_bf16 v[132:135], a[8:11], v[84:87], v[132:135]\n"
      "v_lshlrev_b32 v16, 16, v152\n"
      "v_mfma_f32_16x16x32_bf16 v[132:135], a[12:15], v[88:91], v[132:135]\n"
      "s_add_u32 s83, s83, s86\n"
      "v_mfma_f32_16x16x32_bf16 v[132:135], a[16:19], v[92:95], v[132:135]\n"
      "v_and_b32 v17, 0xffff0000, v152\n"
      "v_mfma_f32_16x16x32_bf16 v[132:135], a[20:23], v[108:111], v[132:135]\n"
      "v_lshlrev_b32 v18, 16, v153\n"
      "v_mfma_f32_16x16x32_bf16 v[132:135], v[140:143], v[112:115], v[132:135]\n"
      "s_waitcnt lgkmcnt(0)\n"
      "s_sub_u32 s84, s84, s83\n"
      "v_mfma_f32_16x16x32_bf16 v[132:135], v[156:159], v[116:119], v[132:135]\n"
      "s_cmp_lt_i32 s84, 0\n"
      "s_cselect_b64 vcc, -1, 0\n"
      "s_waitcnt vmcnt(24)\n"
      "s_barrier\n"
      "v_mfma_f32_16x16x32_bf16 v[136:139], v[160:163], v[36:39], v[20:23]\n"
      "ds_read_b128 v[36:39], v232 offset:0\n"
      "ds_write_b64 v245, v[214:215] offset:8\n"
      "v_mfma_f32_16x16x32_bf16 v[136:139], v[164:167], v[40:43], v[136:139]\n"
      "ds_read_b128 v[40:43], v233 offset:0\n"
      "v_mfma_f32_16x16x32_bf16 v[136:139], v[180:183], v[44:47], v[136:139]\n"
      "ds_read_b128 v[44:47], v234 offset:0\n"
      "v_mfma_f32_16x16x32_bf16 v[136:139], v[184:187], v[60:63], v[136:139]\n"
      "ds_read_b128 v[60:63], v235 offset:0\n"
      "v_cvt_pk_bf16_f32 v208, v132, v133\n"
      "v_mfma_f32_16x16x32_bf16 v[136:139], v[188:191], v[64:67], v[136:139]\n"
      "ds_read_b128 v[64:67], v232 offset:8192\n"
      "v_cvt_pk_bf16_f32 v209, v134, v135\n"
      "v_mfma_f32_16x16x32_bf16 v[136:139], v[204:207], v[68:71], v[136:139]\n"
      "ds_read_b128 v[68:71], v233 offset:8192\n"
      "v_mfma_f32_16x16x32_bf16 v[136:139], a[24:27], v[84:87], v[136:139]\n"
      "ds_read_b128 v[84:87], v234 offset:8192\n"
      "v_mfma_f32_16x16x32_bf16 v[136:139], a[28:31], v[88:91], v[136:139]\n"
      "ds_read_b128 v[88:91], v235 offset:8192\n"
      "v_mfma_f32_16x16x32_bf16 v[136:139], a[32:35], v[92:95], v[136:139]\n"
      "ds_read_b128 v[92:95], v232 offset:16384\n"
      "v_mfma_f32_16x16x32_bf16 v[136:139], a[36:39], v[108:111], v[136:139]\n"
      "ds_read_b128 v[108:111], v233 offset:16384\n"
      "v_mfma_f32_16x16x32_bf16 v[136:139], a[40:43], v[112:115], v[136:139]\n"
      "ds_read_b128 v[112:115], v234 offset:16384\n"
      "v_mfma_f32_16x16x32_bf16 v[136:139], a[44:47], v[116:119], v[136:139]\n"
      "ds_read_b128 v[116:119], v235 offset:16384\n"
      "s_waitcnt lgkmcnt(8)\n"
      "v_mfma_f32_16x16x32_bf16 v[132:135], a[240:243], v[36:39], v[0:3]\n"
      "ds_write_b64 v244, v[208:209] offset:128\n"
      "s_cbranch_vccnz LT24\n"
      "LS24:\n"
      "v_mfma_f32_16x16x32_bf16 v[132:135], a[244:247], v[40:43], v[132:135]\n"
      "v_and_b32 v19, 0xffff0000, v153\n"
      "v_mfma_f32_16x16x32_bf16 v[132:135], a[248:251], v[44:47], v[132:135]\n"
      "v_lshlrev_b32 v20, 16, v154\n"
      "v_mfma_f32_16x16x32_bf16 v[132:135], a[252:255], v[60:63], v[132:135]\n"
      "v_and_b32 v21, 0xffff0000, v154\n"
      "s_waitcnt lgkmcnt(5)\n"
      "v_mfma_f32_16x16x32_bf16 v[132:135], a[48:51], v[64:67], v[132:135]\n"
      "v_lshlrev_b32 v22, 16, v155\n"
      "v_mfma_f32_16x16x32_bf16 v[132:135], a[52:55], v[68:71], v[132:135]\n"
      "v_and_b32 v23, 0xffff0000, v155\n"
      "v_mfma_f32_16x16x32_bf16 v[132:135], a[56:59], v[84:87], v[132:135]\n"
      "v_cvt_pk_bf16_f32 v210, v136, v137\n"
      "v_mfma_f32_16x16x32_bf16 v[132:135], a[60:63], v[88:91], v[132:135]\n"
      "v_cvt_pk_bf16_f32 v211, v138, v139\n"
      "s_waitcnt lgkmcnt(1)\n"
      "v_mfma_f32_16x16x32_bf16 v[132:135], a[64:67], v[92:95], v[132:135]\n"
      "s_add_u32 s54, s44, s37\n"
      "s_addc_u32 s55, s45, 0\n"
      "v_mfma_f32_16x16x32_bf16 v[132:135], a[68:71], v[108:111], v[132:135]\n"
      "ds_read_b128 v[216:219], v246 offset:24576\n"
      "v_mfma_f32_16x16x32_bf16 v[132:135], a[72:75], v[112:115], v[132:135]\n"
      "ds_read_b128 v[220:223], v246 offset:25600\n"
      "v_mfma_f32_16x16x32_bf16 v[132:135], a[76:79], v[116:119], v[132:135]\n"
      "ds_read_b128 v[224:227], v246 offset:26624\n"
      "v_mfma_f32_16x16x32_bf16 v[136:139], a[80:83], v[36:39], v[4:7]\n"
      "ds_write_b64 v244, v[210:211] offset:136\n"
      "v_mfma_f32_16x16x32_bf16 v[136:139], a[84:87], v[40:43], v[136:139]\n"
      "v_mfma_f32_16x16x32_bf16 v[136:139], a[88:91], v[44:47], v[136:139]\n"
      "v_mfma_f32_16x16x32_bf16 v[136:139], a[92:95], v[60:63], v[136:139]\n"
      "v_cvt_pk_bf16_f32 v208, v132, v133\n"
      "v_mfma_f32_16x16x32_bf16 v[136:139], a[96:99], v[64:67], v[136:139]\n"
      "v_cvt_pk_bf16_f32 v209, v134, v135\n"
      "v_mfma_f32_16x16x32_bf16 v[136:139], a[100:103], v[68:71], v[136:139]\n"
      "s_waitcnt lgkmcnt(3)\n"
      "v_mfma_f32_16x16x32_bf16 v[136:139], a[104:107], v[84:87], v[136:139]\n"
      "global_store_dwordx4 v247, v[216:219], s[54:55] offset:0 sc1\n"
      "v_mfma_f32_16x16x32_bf16 v[136:139], a[108:111], v[88:91], v[136:139]\n"
      "s_add_u32 s52, s42, s39\n"
      "s_addc_u32 s53, s43, 0\n"
      "v_mfma_f32_16x16x32_bf16 v[136:139], a[112:115], v[92:95], v[136:139]\n"
      "s_add_u32 m0, s17, 0x12000\n"
      "v_mfma_f32_16x16x32_bf16 v[136:139], a[116:119], v[108:111], v[136:139]\n"
      "ds_write_b64 v244, v[208:209] offset:12288\n"
      "v_mfma_f32_16x16x32_bf16 v[136:139], a[120:123], v[112:115], v[136:139]\n"
      "global_load_lds_dwordx4 v236, s[52:53] offset:0 nt\n"
      "v_mfma_f32_16x16x32_bf16 v[136:139], a[124:127], v[116:119], v[136:139]\n"
      "v_mfma_f32_16x16x32_bf16 v[132:135], a[128:131], v[36:39], v[8:11]\n"
      "s_cbranch_vccnz LT25\n"
      "LS25:\n"
      "v_mfma_f32_16x16x32_bf16 v[132:135], a[132:135], v[40:43], v[132:135]\n"
      "v_mfma_f32_16x16x32_bf16 v[132:135], a[136:139], v[44:47], v[132:135]\n"
      "v_mfma_f32_16x16x32_bf16 v[132:135], a[140:143], v[60:63], v[132:135]\n"
      "v_cvt_pk_bf16_f32 v210, v136, v137\n"
      "v_mfma_f32_16x16x32_bf16 v[132:135], a[144:147], v[64:67], v[132:135]\n"
      "v_cvt_pk_bf16_f32 v211, v138, v139\n"
      "v_mfma_f32_16x16x32_bf16 v[132:135], a[148:151], v[68:71], v[132:135]\n"
      "v_mfma_f32_16x16x32_bf16 v[132:135], a[152:155], v[84:87], v[132:135]\n"
      "s_waitcnt lgkmcnt(3)\n"
      "v_mfma_f32_16x16x32_bf16 v[132:135], a[156:159], v[88:91], v[132:135]\n"
      "global_store_dwordx4 v252, v[220:223], s[54:55] offset:0 sc1\n"
      "v_mfma_f32_16x16x32_bf16 v[132:135], a[160:163], v[92:95], v[132:135]\n"
      "s_add_u32 m0, s17, 0x13f00\n"
      "v_mfma_f32_16x16x32_bf16 v[132:135], a[164:167], v[108:111], v[132:135]\n"
      "ds_write_b64 v244, v[210:211] offset:12296\n"
      "v_mfma_f32_16x16x32_bf16 v[132:135], a[168:171], v[112:115], v[132:135]\n"
      "global_load_lds_dwordx4 v236, s[52:53] offset:256 nt\n"
      "v_mfma_f32_16x16x32_bf16 v[132:135], a[172:175], v[116:119], v[132:135]\n"
      "v_mfma_f32_16x16x32_bf16 v[136:139], a[176:179], v[36:39], v[12:15]\n"
      "v_mfma_f32_16x16x32_bf16 v[136:139], a[180:183], v[40:43], v[136:139]\n"
      "v_mfma_f32_16x16x32_bf16 v[136:139], a[184:187], v[44:47], v[136:139]\n"
      "v_mfma_f32_16x16x32_bf16 v[136:139], a[188:191], v[60:63], v[136:139]\n"
      "v_cvt_pk_bf16_f32 v212, v132, v133\n"
      "v_mfma_f32_16x16x32_bf16 v[136:139], a[192:195], v[64:67], v[136:139]\n"
      "v_cvt_pk_bf16_f32 v213, v134, v135\n"
      "v_mfma_f32_16x16x32_bf16 v[136:139], a[196:199], v[68:71], v[136:139]\n"
      "v_mfma_f32_16x16x32_bf16 v[136:139], a[200:203], v[84:87], v[136:139]\n"
      "v_mfma_f32_16x16x32_bf16 v[136:139], a[204:207], v[88:91], v[136:139]\n"
      "s_waitcnt lgkmcnt(3)\n"
      "v_mfma_f32_16x16x32_bf16 v[136:139], a[208:211], v[92:95], v[136:139]\n"
      "global_store_dwordx4 v253, v[224:227], s[54:55] offset:0 sc1\n"
      "v_mfma_f32_16x16x32_bf16 v[136:139], a[212:215], v[108:111], v[136:139]\n"
      "ds_write_b64 v245, v[212:213] offset:12288\n"
      "v_mfma_f32_16x16x32_bf16 v[136:139], a[216:219], v[112:115], v[136:139]\n"
      "s_add_u32 m0, s17, 0x15e00\n"
      "v_mfma_f32_16x16x32_bf16 v[136:139], a[220:223], v[116:119], v[136:139]\n"
      "global_load_lds_dwordx4 v236, s[52:53] offset:512 nt\n"
      "v_mfma_f32_16x16x32_bf16 v[132:135], a[224:227], v[36:39], v[16:19]\n"
      "s_cbranch_vccnz LT26\n"
      "LS26:\n"
      "v_mfma_f32_16x16x32_bf16 v[132:135], a[228:231], v[40:43], v[132:135]\n"
      "v_mfma_f32_16x16x32_bf16 v[132:135], a[232:235], v[44:47], v[132:135]\n"
      "v_mfma_f32_16x16x32_bf16 v[132:135], a[236:239], v[60:63], v[132:135]\n"
      "v_cvt_pk_bf16_f32 v214, v136, v137\n"
      "v_mfma_f32_16x16x32_bf16 v[132:135], a[0:3], v[64:67], v[132:135]\n"
      "v_cvt_pk_bf16_f32 v215, v138, v139\n"
      "v_mfma_f32_16x16x32_bf16 v[132:135], a[4:7], v[68:71], v[132:135]\n"
      "v_mfma_f32_16x16x32_bf16 v[132:135], a[8:11], v[84:87], v[132:135]\n"
      "v_mfma_f32_16x16x32_bf16 v[132:135], a[12:15], v[88:91], v[132:135]\n"
      "v_mfma_f32_16x16x32_bf16 v[132:135], a[16:19], v[92:95], v[132:135]\n"
      "v_mfma_f32_16x16x32_bf16 v[132:135], a[20:23], v[108:111], v[132:135]\n"
      "ds_write_b64 v245, v[214:215] offset:12296\n"
      "v_mfma_f32_16x16x32_bf16 v[132:135], v[140:143], v[112:115], v[132:135]\n"
      "v_mfma_f32_16x16x32_bf16 v[132:135], v[156:159], v[116:119], v[132:135]\n"
      "s_waitcnt lgkmcnt(4)\n"
      "s_barrier\n"
      "v_mfma_f32_16x16x32_bf16 v[136:139], v[160:163], v[36:39], v[20:23]\n"
      "ds_read_b128 v[36:39], v232 offset:256\n"
      "v_mfma_f32_16x16x32_bf16 v[136:139], v[164:167], v[40:43], v[136:139]\n"
      "ds_read_b128 v[40:43], v233 offset:256\n"
      "v_mfma_f32_16x16x32_bf16 v[136:139], v[180:183], v[44:47], v[136:139]\n"
      "ds_read_b128 v[44:47], v234 offset:256\n"
      "v_mfma_f32_16x16x32_bf16 v[136:139], v[184:187], v[60:63], v[136:139]\n"
      "ds_read_b128 v[60:63], v235 offset:256\n"
      "v_cvt_pk_bf16_f32 v208, v132, v133\n"
      "v_mfma_f32_16x16x32_bf16 v[136:139], v[188:191], v[64:67], v[136:139]\n"
      "ds_read_b128 v[64:67], v232 offset:8448\n"
      "v_cvt_pk_bf16_f32 v209, v134, v135\n"
      "v_mfma_f32_16x16x32_bf16 v[136:139], v[204:207], v[68:71], v[136:139]\n"
      "ds_read_b128 v[68:71], v233 offset:8448\n"
      "v_mfma_f32_16x16x32_bf16 v[136:139], a[24:27], v[84:87], v[136:139]\n"
      "ds_read_b128 v[84:87], v234 offset:8448\n"
      "v_mfma_f32_16x16x32_bf16 v[136:139], a[28:31], v[88:91], v[136:139]\n"
      "ds_read_b128 v[88:91], v235 offset:8448\n"
      "v_mfma_f32_16x16x32_bf16 v[136:139], a[32:35], v[92:95], v[136:139]\n"
      "ds_read_b128 v[92:95], v232 offset:16640\n"
      "v_mfma_f32_16x16x32_bf16 v[136:139], a[36:39], v[108:111], v[136:139]\n"
      "ds_read_b128 v[108:111], v233 offset:16640\n"
      "v_mfma_f32_16x16x32_bf16 v[136:139], a[40:43], v[112:115], v[136:139]\n"
      "ds_read_b128 v[112:115], v234 offset:16640\n"
      "v_mfma_f32_16x16x32_bf16 v[136:139], a[44:47], v[116:119], v[136:139]\n"
      "ds_read_b128 v[116:119], v235 offset:16640\n"
      "s_waitcnt lgkmcnt(8)\n"
      "v_mfma_f32_16x16x32_bf16 v[132:135], a[240:243], v[36:39], v[0:3]\n"
      "ds_write_b64 v244, v[208:209] offset:12416\n"
      "v_mfma_f32_16x16x32_bf16 v[132:135], a[244:247], v[40:43], v[132:135]\n"
      "s_cbranch_vccnz LT27\n"
      "LS27:\n"
      "v_mfma_f32_16x16x32_bf16 v[132:135], a[248:251], v[44:47], v[132:135]\n"
      "v_mfma_f32_16x16x32_bf16 v[132:135], a[252:255], v[60:63], v[132:135]\n"
      "v_cvt_pk_bf16_f32 v210, v136, v137\n"
      "s_waitcnt lgkmcnt(5)\n"
      "v_mfma_f32_16x16x32_bf16 v[132:135], a[48:51], v[64:67], v[132:135]\n"
      "v_cvt_pk_bf16_f32 v211, v138, v139\n"
      "v_mfma_f32_16x16x32_bf16 v[132:135], a[52:55], v[68:71], v[132:135]\n"
      "v_mfma_f32_16x16x32_bf16 v[132:135], a[56:59], v[84:87], v[132:135]\n"
      "ds_read_b128 v[216:219], v246 offset:0\n"
      "v_mfma_f32_16x16x32_bf16 v[132:135], a[60:63], v[88:91], v[132:135]\n"
      "ds_read_b128 v[220:223], v246 offset:1024\n"
      "s_waitcnt lgkmcnt(3)\n"
      "v_mfma_f32_16x16x32_bf16 v[132:135], a[64:67], v[92:95], v[132:135]\n"
      "ds_read_b128 v[224:227], v246 offset:2048\n"
      "v_mfma_f32_16x16x32_bf16 v[132:135], a[68:71], v[108:111], v[132:135]\n"
      "ds_write_b64 v244, v[210:211] offset:12424\n"
      "v_mfma_f32_16x16x32_bf16 v[132:135], a[72:75], v[112:115], v[132:135]\n"
      "v_mfma_f32_16x16x32_bf16 v[132:135], a[76:79], v[116:119], v[132:135]\n"
      "v_mfma_f32_16x16x32_bf16 v[136:139], a[80:83], v[36:39], v[4:7]\n"
      "v_mfma_f32_16x16x32_bf16 v[136:139], a[84:87], v[40:43], v[136:139]\n"
      "v_mfma_f32_16x16x32_bf16 v[136:139], a[88:91], v[44:47], v[136:139]\n"
      "v_mfma_f32_16x16x32_bf16 v[136:139], a[92:95], v[60:63], v[136:139]\n"
      "v_cvt_pk_bf16_f32 v208, v132, v133\n"
      "v_mfma_f32_16x16x32_bf16 v[136:139], a[96:99], v[64:67], v[136:139]\n"
      "v_cvt_pk_bf16_f32 v209, v134, v135\n"
      "v_mfma_f32_16x16x32_bf16 v[136:139], a[100:103], v[68:71], v[136:139]\n"
      "s_waitcnt lgkmcnt(3)\n"
      "v_mfma_f32_16x16x32_bf16 v[136:139], a[104:107], v[84:87], v[136:139]\n"
      "global_store_dwordx4 v247, v[216:219], s[54:55] offset:768 sc1\n"
      "v_mfma_f32_16x16x32_bf16 v[136:139], a[108:111], v[88:91], v[136:139]\n"
      "s_add_u32 m0, s17, 0x13000\n"
      "v_mfma_f32_16x16x32_bf16 v[136:139], a[112:115], v[92:95], v[136:139]\n"
      "global_load_lds_dwordx4 v237, s[52:53] offset:0 nt\n"
      "v_mfma_f32_16x16x32_bf16 v[136:139], a[116:119], v[108:111], v[136:139]\n"
      "ds_write_b64 v244, v[208:209] offset:24576\n"
      "v_mfma_f32_16x16x32_bf16 v[136:139], a[120:123], v[112:115], v[136:139]\n"
      "v_mfma_f32_16x16x32_bf16 v[136:139], a[124:127], v[116:119], v[136:139]\n"
      "v_mfma_f32_16x16x32_bf16 v[132:135], a[128:131], v[36:39], v[8:11]\n"
      "s_cbranch_vccnz LT28\n"
      "LS28:\n"
      "v_mfma_f32_16x16x32_bf16 v[132:135], a[132:135], v[40:43], v[132:135]\n"
      "v_mfma_f32_16x16x32_bf16 v[132:135], a[136:139], v[44:47], v[132:135]\n"
      "v_mfma_f32_16x16x32_bf16 v[132:135], a[140:143], v[60:63], v[132:135]\n"
      "v_cvt_pk_bf16_f32 v210, v136, v137\n"
      "v_mfma_f32_16x16x32_bf16 v[132:135], a[144:147], v[64:67], v[132:135]\n"
      "v_cvt_pk_bf16_f32 v211, v138, v139\n"
      "v_mfma_f32_16x16x32_bf16 v[132:135], a[148:151], v[68:71], v[132:135]\n"
      "v_mfma_f32_16x16x32_bf16 v[132:135], a[152:155], v[84:87], v[132:135]\n"
      "s_waitcnt lgkmcnt(3)\n"
      "v_mfma_f32_16x16x32_bf16 v[132:135], a[156:159], v[88:91], v[132:135]\n"
      "global_store_dwordx4 v252, v[220:223], s[54:55] offset:768 sc1\n"
      "v_mfma_f32_16x16x32_bf16 v[132:135], a[160:163], v[92:95], v[132:135]\n"
      "s_add_u32 m0, s17, 0x14f00\n"
      "v_mfma_f32_16x16x32_bf16 v[132:135], a[164:167], v[108:111], v[132:135]\n"
      "ds_write_b64 v244, v[210:211] offset:24584\n"
      "v_mfma_f32_16x16x32_bf16 v[132:135], a[168:171], v[112:115], v[132:135]\n"
      "global_load_lds_dwordx4 v237, s[52:53] offset:256 nt\n"
      "v_mfma_f32_16x16x32_bf16 v[132:135], a[172:175], v[116:119], v[132:135]\n"
      "v_mfma_f32_16x16x32_bf16 v[136:139], a[176:179], v[36:39], v[12:15]\n"
      "v_mfma_f32_16x16x32_bf16 v[136:139], a[180:183], v[40:43], v[136:139]\n"
      "v_mfma_f32_16x16x32_bf16 v[136:139], a[184:187], v[44:47], v[136:139]\n"
      "v_mfma_f32_16x16x32_bf16 v[136:139], a[188:191], v[60:63], v[136:139]\n"
      "v_cvt_pk_bf16_f32 v212, v132, v133\n"
      "v_mfma_f32_16x16x32_bf16 v[136:139], a[192:195], v[64:67], v[136:139]\n"
      "v_cvt_pk_bf16_f32 v213, v134, v135\n"
      "v_mfma_f32_16x16x32_bf16 v[136:139], a[196:199], v[68:71], v[136:139]\n"
      "v_mfma_f32_16x16x32_bf16 v[136:139], a[200:203], v[84:87], v[136:139]\n"
      "v_mfma_f32_16x16x32_bf16 v[136:139], a[204:207], v[88:91], v[136:139]\n"
      "s_waitcnt lgkmcnt(3)\n"
      "v_mfma_f32_16x16x32_bf16 v[136:139], a[208:211], v[92:95], v[136:139]\n"
      "global_store_dwordx4 v253, v[224:227], s[54:55] offset:768 sc1\n"
      "v_mfma_f32_16x16x32_bf16 v[136:139], a[212:215], v[108:111], v[136:139]\n"
      "ds_write_b64 v245, v[212:213] offset:24576\n"
      "v_mfma_f32_16x16x32_bf16 v[136:139], a[216:219], v[112:115], v[136:139]\n"
      "s_add_u32 m0, s17, 0x16e00\n"
      "v_mfma_f32_16x16x32_bf16 v[136:139], a[220:223], v[116:119], v[136:139]\n"
      "global_load_lds_dwordx4 v237, s[52:53] offset:512 nt\n"
      "v_mfma_f32_16x16x32_bf16 v[132:135], a[224:227], v[36:39], v[16:19]\n"
      "s_cbranch_vccnz LT29\n"
      "LS29:\n"
      "v_mfma_f32_16x16x32_bf16 v[132:135], a[228:231], v[40:43], v[132:135]\n"
      "v_mfma_f32_16x16x32_bf16 v[132:135], a[232:235], v[44:47], v[132:135]\n"
      "v_mfma_f32_16x16x32_bf16 v[132:135], a[236:239], v[60:63], v[132:135]\n"
      "s_memrealtime s[84:85]\n"
      "v_mfma_f32_16x16x32_bf16 v[132:135], a[0:3], v[64:67], v[132:135]\n"
      "v_cvt_pk_bf16_f32 v214, v136, v137\n"
      "v_mfma_f32_16x16x32_bf16 v[132:135], a[4:7], v[68:71], v[132:135]\n"
      "v_cvt_pk_bf16_f32 v215, v138, v139\n"
      "v_mfma_f32_16x16x32_bf16 v[132:135], a[8:11], v[84:87], v[132:135]\n"
      "v_mfma_f32_16x16x32_bf16 v[132:135], a[12:15], v[88:91], v[132:135]\n"
      "s_add_u32 s83, s83, s86\n"
      "v_mfma_f32_16x16x32_bf16 v[132:135], a[16:19], v[92:95], v[132:135]\n"
      "v_mfma_f32_16x16x32_bf16 v[132:135], a[20:23], v[108:111], v[132:135]\n"
      "v_mfma_f32_16x16x32_bf16 v[132:135], v[140:143], v[112:115], v[132:135]\n"
      "s_waitcnt lgkmcnt(0)\n"
      "s_sub_u32 s84, s84, s83\n"
      "v_mfma_f32_16x16x32_bf16 v[132:135], v[156:159], v[116:119], v[132:135]\n"
      "s_cmp_lt_i32 s84, 0\n"
      "s_cselect_b64 vcc, -1, 0\n"
      "s_waitcnt vmcnt(24)\n"
      "s_barrier\n"
      "v_mfma_f32_16x16x32_bf16 v[136:139], v[160:163], v[36:39], v[20:23]\n"
      "ds_read_b128 v[36:39], v232 offset:24576\n"
      "ds_write_b64 v245, v[214:215] offset:24584\n"
      "v_mfma_f32_16x16x32_bf16 v[136:139], v[164:167], v[40:43], v[136:139]\n"
      "ds_read_b128 v[40:43], v233 offset:24576\n"
      "v_mfma_f32_16x16x32_bf16 v[136:139], v[180:183], v[44:47], v[136:139]\n"
      "ds_read_b128 v[44:47], v234 offset:24576\n"
      "v_mfma_f32_16x16x32_bf16 v[136:139], v[184:187], v[60:63], v[136:139]\n"
      "ds_read_b128 v[60:63], v235 offset:24576\n"
      "v_cvt_pk_bf16_f32 v208, v132, v133\n"
      "v_mfma_f32_16x16x32_bf16 v[136:139], v[188:191], v[64:67], v[136:139]\n"
      "ds_read_b128 v[64:67], v232 offset:32768\n"
      "v_cvt_pk_bf16_f32 v209, v134, v135\n"
      "v_mfma_f32_16x16x32_bf16 v[136:139], v[204:207], v[68:71], v[136:139]\n"
      "ds_read_b128 v[68:71], v233 offset:32768\n"
      "v_mfma_f32_16x16x32_bf16 v[136:139], a[24:27], v[84:87], v[136:139]\n"
      "ds_read_b128 v[84:87], v234 offset:32768\n"
      "v_mfma_f32_16x16x32_bf16 v[136:139], a[28:31], v[88:91], v[136:139]\n"
      "ds_read_b128 v[88:91], v235 offset:32768\n"
      "v_mfma_f32_16x16x32_bf16 v[136:139], a[32:35], v[92:95], v[136:139]\n"
      "ds_read_b128 v[92:95], v232 offset:40960\n"
      "v_mfma_f32_16x16x32_bf16 v[136:139], a[36:39], v[108:111], v[136:139]\n"
      "ds_read_b128 v[108:111], v233 offset:40960\n"
      "v_mfma_f32_16x16x32_bf16 v[136:139], a[40:43], v[112:115], v[136:139]\n"
      "ds_read_b128 v[112:115], v234 offset:40960\n"
      "v_mfma_f32_16x16x32_bf16 v[136:139], a[44:47], v[116:119], v[136:139]\n"
      "ds_read_b128 v[116:119], v235 offset:40960\n"
      "s_waitcnt lgkmcnt(8)\n"
      "v_mfma_f32_16x16x32_bf16 v[132:135], a[240:243], v[36:39], v[0:3]\n"
      "ds_write_b64 v244, v[208:209] offset:24704\n"
      "v_mfma_f32_16x16x32_bf16 v[132:135], a[244:247], v[40:43], v[132:135]\n"
      "s_cbranch_vccnz LT30\n"
      "LS30:\n"
      "v_mfma_f32_16x16x32_bf16 v[132:135], a[248:251], v[44:47], v[132:135]\n"
      "v_mfma_f32_16x16x32_bf16 v[132:135], a[252:255], v[60:63], v[132:135]\n"
      "v_cvt_pk_bf16_f32 v210, v136, v137\n"
      "s_lshl_b32 s79, s95, 1\n"
      "s_waitcnt lgkmcnt(5)\n"
      "v_mfma_f32_16x16x32_bf16 v[132:135], a[48:51], v[64:67], v[132:135]\n"
      "v_cvt_pk_bf16_f32 v211, v138, v139\n"
      "s_mul_i32 s78, s97, s19\n"
      "v_mfma_f32_16x16x32_bf16 v[132:135], a[52:55], v[68:71], v[132:135]\n"
      "v_mad_i32_i24 v252, v251, s79, v252\n"
      "v_add_u32 v236, s78, v236\n"
      "v_mfma_f32_16x16x32_bf16 v[132:135], a[56:59], v[84:87], v[132:135]\n"
      "ds_read_b128 v[24:27], v246 offset:12288\n"
      "s_mul_i32 s79, s79, s100\n"
      "v_mfma_f32_16x16x32_bf16 v[132:135], a[60:63], v[88:91], v[132:135]\n"
      "ds_read_b128 v[28:31], v246 offset:13312\n"
      "v_add_u32 v247, s79, v247\n"
      "s_waitcnt lgkmcnt(3)\n"
      "v_mfma_f32_16x16x32_bf16 v[132:135], a[64:67], v[92:95], v[132:135]\n"
      "ds_read_b128 v[32:35], v246 offset:14336\n"
      "s_mul_i32 s79, s79, s101\n"
      "v_mfma_f32_16x16x32_bf16 v[132:135], a[68:71], v[108:111], v[132:135]\n"
      "ds_write_b64 v244, v[210:211] offset:24712\n"
      "s_mul_i32 s78, s78, s99\n"
      "v_mfma_f32_16x16x32_bf16 v[132:135], a[72:75], v[112:115], v[132:135]\n"
      "v_add_u32 v253, s79, v253\n"
      "v_add_u32 v237, s78, v237\n"
      "v_mfma_f32_16x16x32_bf16 v[132:135], a[76:79], v[116:119], v[132:135]\n"
      "s_add_u32 s54, s30, s38\n"
      "s_addc_u32 s55, s31, 0\n"
      "v_mfma_f32_16x16x32_bf16 v[136:139], a[80:83], v[36:39], v[4:7]\n"
      "s_waitcnt lgkmcnt(3)\n"
      "v_mfma_f32_16x16x32_bf16 v[136:139], a[84:87], v[40:43], v[136:139]\n"
      "global_store_dwordx4 v247, v[24:27], s[54:55] offset:0 sc1\n"
      "v_mfma_f32_16x16x32_bf16 v[136:139], a[88:91], v[44:47], v[136:139]\n"
      "v_mfma_f32_16x16x32_bf16 v[136:139], a[92:95], v[60:63], v[136:139]\n"
      "v_cvt_pk_bf16_f32 v208, v132, v133\n"
      "v_mfma_f32_16x16x32_bf16 v[136:139], a[96:99], v[64:67], v[136:139]\n"
      "v_cvt_pk_bf16_f32 v209, v134, v135\n"
      "v_mfma_f32_16x16x32_bf16 v[136:139], a[100:103], v[68:71], v[136:139]\n"
      "v_mfma_f32_16x16x32_bf16 v[136:139], a[104:107], v[84:87], v[136:139]\n"
      "v_mfma_f32_16x16x32_bf16 v[136:139], a[108:111], v[88:91], v[136:139]\n"
      "s_add_u32 s52, s20, s40\n"
      "s_addc_u32 s53, s21, 0\n"
      "v_mfma_f32_16x16x32_bf16 v[136:139], a[112:115], v[92:95], v[136:139]\n"
      "s_add_u32 m0, s17, 0x0\n"
      "v_mfma_f32_16x16x32_bf16 v[136:139], a[116:119], v[108:111], v[136:139]\n"
      "ds_write_b64 v244, v[208:209] offset:0\n"
      "v_mfma_f32_16x16x32_bf16 v[136:139], a[120:123], v[112:115], v[136:139]\n"
      "global_load_lds_dwordx4 v236, s[52:53] offset:0 nt\n"
      "v_mfma_f32_16x16x32_bf16 v[136:139], a[124:127], v[116:119], v[136:139]\n"
      "v_mfma_f32_16x16x32_bf16 v[132:135], a[128:131], v[36:39], v[8:11]\n"
      "s_cbranch_vccnz LT31\n"
      "LS31:\n"
      "v_mfma_f32_16x16x32_bf16 v[132:135], a[132:135], v[40:43], v[132:135]\n"
      "v_mfma_f32_16x16x32_bf16 v[132:135], a[136:139], v[44:47], v[132:135]\n"
      "v_mfma_f32_16x16x32_bf16 v[132:135], a[140:143], v[60:63], v[132:135]\n"
      "v_cvt_pk_bf16_f32 v210, v136, v137\n"
      "v_mfma_f32_16x16x32_bf16 v[132:135], a[144:147], v[64:67], v[132:135]\n"
      "v_cvt_pk_bf16_f32 v211, v138, v139\n"
      "v_mfma_f32_16x16x32_bf16 v[132:135], a[148:151], v[68:71], v[132:135]\n"
      "v_mfma_f32_16x16x32_bf16 v[132:135], a[152:155], v[84:87], v[132:135]\n"
      "s_waitcnt lgkmcnt(3)\n"
      "v_mfma_f32_16x16x32_bf16 v[132:135], a[156:159], v[88:91], v[132:135]\n"
      "global_store_dwordx4 v252, v[28:31], s[54:55] offset:0 sc1\n"
      "v_mfma_f32_16x16x32_bf16 v[132:135], a[160:163], v[92:95], v[132:135]\n"
      "s_add_u32 m0, s17, 0x1f00\n"
      "v_mfma_f32_16x16x32_bf16 v[132:135], a[164:167], v[108:111], v[132:135]\n"
      "ds_write_b64 v244, v[210:211] offset:8\n"
      "v_mfma_f32_16x16x32_bf16 v[132:135], a[168:171], v[112:115], v[132:135]\n"
      "global_load_lds_dwordx4 v236, s[52:53] offset:256 nt\n"
      "v_mfma_f32_16x16x32_bf16 v[132:135], a[172:175], v[116:119], v[132:135]\n"
      "v_mfma_f32_16x16x32_bf16 v[136:139], a[176:179], v[36:39], v[12:15]\n"
      "v_mfma_f32_16x16x32_bf16 v[136:139], a[180:183], v[40:43], v[136:139]\n"
      "v_mfma_f32_16x16x32_bf16 v[136:139], a[184:187], v[44:47], v[136:139]\n"
      "v_mfma_f32_16x16x32_bf16 v[136:139], a[188:191], v[60:63], v[136:139]\n"
      "v_cvt_pk_bf16_f32 v212, v132, v133\n"
      "v_mfma_f32_16x16x32_bf16 v[136:139], a[192:195], v[64:67], v[136:139]\n"
      "v_cvt_pk_bf16_f32 v213, v134, v135\n"
      "v_mfma_f32_16x16x32_bf16 v[136:139], a[196:199], v[68:71], v[136:139]\n"
      "v_mfma_f32_16x16x32_bf16 v[136:139], a[200:203], v[84:87], v[136:139]\n"
      "v_mfma_f32_16x16x32_bf16 v[136:139], a[204:207], v[88:91], v[136:139]\n"
      "s_waitcnt lgkmcnt(3)\n"
      "v_mfma_f32_16x16x32_bf16 v[136:139], a[208:211], v[92:95], v[136:139]\n"
      "global_store_dwordx4 v253, v[32:35], s[54:55] offset:0 sc1\n"
      "v_mfma_f32_16x16x32_bf16 v[136:139], a[212:215], v[108:111], v[136:139]\n"
      "ds_write_b64 v245, v[212:213] offset:0\n"
      "v_mfma_f32_16x16x32_bf16 v[136:139], a[216:219], v[112:115], v[136:139]\n"
      "s_add_u32 m0, s17, 0x3e00\n"
      "v_mfma_f32_16x16x32_bf16 v[136:139], a[220:223], v[116:119], v[136:139]\n"
      "global_load_lds_dwordx4 v236, s[52:53] offset:512 nt\n"
      "v_mfma_f32_16x16x32_bf16 v[132:135], a[224:227], v[36:39], v[16:19]\n"
      "s_cbranch_vccnz LT32\n"
      "LS32:\n"
      "v_mfma_f32_16x16x32_bf16 v[132:135], a[228:231], v[40:43], v[132:135]\n"
      "v_mfma_f32_16x16x32_bf16 v[132:135], a[232:235], v[44:47], v[132:135]\n"
      "v_mfma_f32_16x16x32_bf16 v[132:135], a[236:239], v[60:63], v[132:135]\n"
      "v_cvt_pk_bf16_f32 v214, v136, v137\n"
      "v_mfma_f32_16x16x32_bf16 v[132:135], a[0:3], v[64:67], v[132:135]\n"
      "v_cvt_pk_bf16_f32 v215, v138, v139\n"
      "v_mfma_f32_16x16x32_bf16 v[132:135], a[4:7], v[68:71], v[132:135]\n"
      "v_mfma_f32_16x16x32_bf16 v[132:135], a[8:11], v[84:87], v[132:135]\n"
      "v_mfma_f32_16x16x32_bf16 v[132:135], a[12:15], v[88:91], v[132:135]\n"
      "v_mfma_f32_16x16x32_bf16 v[132:135], a[16:19], v[92:95], v[132:135]\n"
      "v_mfma_f32_16x16x32_bf16 v[132:135], a[20:23], v[108:111], v[132:135]\n"
      "ds_write_b64 v245, v[214:215] offset:8\n"
      "v_mfma_f32_16x16x32_bf16 v[132:135], v[140:143], v[112:115], v[132:135]\n"
      "v_mfma_f32_16x16x32_bf16 v[132:135], v[156:159], v[116:119], v[132:135]\n"
      "s_waitcnt lgkmcnt(4)\n"
      "s_barrier\n"
      "v_mfma_f32_16x16x32_bf16 v[136:139], v[160:163], v[36:39], v[20:23]\n"
      "ds_read_b128 v[36:39], v232 offset:24832\n"
      "v_mfma_f32_16x16x32_bf16 v[136:139], v[164:167], v[40:43], v[136:139]\n"
      "ds_read_b128 v[40:43], v233 offset:24832\n"
      "v_mfma_f32_16x16x32_bf16 v[136:139], v[180:183], v[44:47], v[136:139]\n"
      "ds_read_b128 v[44:47], v234 offset:24832\n"
      "v_mfma_f32_16x16x32_bf16 v[136:139], v[184:187], v[60:63], v[136:139]\n"
      "ds_read_b128 v[60:63], v235 offset:24832\n"
      "v_cvt_pk_bf16_f32 v208, v132, v133\n"
      "v_mfma_f32_16x16x32_bf16 v[136:139], v[188:191], v[64:67], v[136:139]\n"
      "ds_read_b128 v[64:67], v232 offset:33024\n"
      "v_cvt_pk_bf16_f32 v209, v134, v135\n"
      "v_mfma_f32_16x16x32_bf16 v[136:139], v[204:207], v[68:71], v[136:139]\n"
      "ds_read_b128 v[68:71], v233 offset:33024\n"
      "v_mfma_f32_16x16x32_bf16 v[136:139], a[24:27], v[84:87], v[136:139]\n"
      "ds_read_b128 v[84:87], v234 offset:33024\n"
      "v_mfma_f32_16x16x32_bf16 v[136:139], a[28:31], v[88:91], v[136:139]\n"
      "ds_read_b128 v[88:91], v235 offset:33024\n"
      "v_mfma_f32_16x16x32_bf16 v[136:139], a[32:35], v[92:95], v[136:139]\n"
      "ds_read_b128 v[92:95], v232 offset:41216\n"
      "v_mfma_f32_16x16x32_bf16 v[136:139], a[36:39], v[108:111], v[136:139]\n"
      "ds_read_b128 v[108:111], v233 offset:41216\n"
      "ds_write_b64 v244, v[208:209] offset:128\n"
      "v_mfma_f32_16x16x32_bf16 v[136:139], a[40:43], v[112:115], v[136:139]\n"
      "ds_read_b128 v[112:115], v234 offset:41216\n"
      "v_mfma_f32_16x16x32_bf16 v[136:139], a[44:47], v[116:119], v[136:139]\n"
      "ds_read_b128 v[116:119], v235 offset:41216\n"
      "s_waitcnt lgkmcnt(9)\n"
      "v_mfma_f32_16x16x32_bf16 v[132:135], a[240:243], v[36:39], v[0:3]\n"
      "v_add_u32 v232, 0xc000, v232\n"
      "s_cbranch_vccnz LT33\n"
      "LS33:\n"
      "v_mfma_f32_16x16x32_bf16 v[132:135], a[244:247], v[40:43], v[132:135]\n"
      "v_add_u32 v233, 0xc000, v233\n"
      "v_mfma_f32_16x16x32_bf16 v[132:135], a[248:251], v[44:47], v[132:135]\n"
      "v_add_u32 v234, 0xc000, v234\n"
      "v_mfma_f32_16x16x32_bf16 v[132:135], a[252:255], v[60:63], v[132:135]\n"
      "v_add_u32 v235, 0xc000, v235\n"
      "s_waitcnt lgkmcnt(5)\n"
      "v_mfma_f32_16x16x32_bf16 v[132:135], a[48:51], v[64:67], v[132:135]\n"
      "v_cvt_pk_bf16_f32 v210, v136, v137\n"
      "v_mfma_f32_16x16x32_bf16 v[132:135], a[52:55], v[68:71], v[132:135]\n"
      "v_cvt_pk_bf16_f32 v211, v138, v139\n"
      "v_mfma_f32_16x16x32_bf16 v[132:135], a[56:59], v[84:87], v[132:135]\n"
      "ds_read_b128 v[48:51], v246 offset:24576\n"
      "v_mfma_f32_16x16x32_bf16 v[132:135], a[60:63], v[88:91], v[132:135]\n"
      "ds_read_b128 v[52:55], v246 offset:25600\n"
      "s_waitcnt lgkmcnt(2)\n"
      "v_mfma_f32_16x16x32_bf16 v[132:135], a[64:67], v[92:95], v[132:135]\n"
      "ds_read_b128 v[56:59], v246 offset:26624\n"
      "v_mfma_f32_16x16x32_bf16 v[132:135], a[68:71], v[108:111], v[132:135]\n"
      "v_lshlrev_b32 v0, 16, v168\n"
      "v_mfma_f32_16x16x32_bf16 v[132:135], a[72:75], v[112:115], v[132:135]\n"
      "ds_write_b64 v244, v[210:211] offset:136\n"
      "v_mfma_f32_16x16x32_bf16 v[132:135], a[76:79], v[116:119], v[132:135]\n"
      "v_and_b32 v1, 0xffff0000, v168\n"
      "v_mfma_f32_16x16x32_bf16 v[136:139], a[80:83], v[36:39], v[4:7]\n"
      "v_lshlrev_b32 v2, 16, v169\n"
      "v_mfma_f32_16x16x32_bf16 v[136:139], a[84:87], v[40:43], v[136:139]\n"
      "v_and_b32 v3, 0xffff0000, v169\n"
      "v_mfma_f32_16x16x32_bf16 v[136:139], a[88:91], v[44:47], v[136:139]\n"
      "v_mfma_f32_16x16x32_bf16 v[136:139], a[92:95], v[60:63], v[136:139]\n"
      "v_cvt_pk_bf16_f32 v208, v132, v133\n"
      "v_mfma_f32_16x16x32_bf16 v[136:139], a[96:99], v[64:67], v[136:139]\n"
      "v_cvt_pk_bf16_f32 v209, v134, v135\n"
      "v_mfma_f32_16x16x32_bf16 v[136:139], a[100:103], v[68:71], v[136:139]\n"
      "s_waitcnt lgkmcnt(3)\n"
      "v_mfma_f32_16x16x32_bf16 v[136:139], a[104:107], v[84:87], v[136:139]\n"
      "global_store_dwordx4 v247, v[48:51], s[54:55] offset:768 sc1\n"
      "v_mfma_f32_16x16x32_bf16 v[136:139], a[108:111], v[88:91], v[136:139]\n"
      "s_add_u32 m0, s17, 0x1000\n"
      "v_mfma_f32_16x16x32_bf16 v[136:139], a[112:115], v[92:95], v[136:139]\n"
      "global_load_lds_dwordx4 v237, s[52:53] offset:0 nt\n"
      "v_mfma_f32_16x16x32_bf16 v[136:139], a[116:119], v[108:111], v[136:139]\n"
      "ds_write_b64 v244, v[208:209] offset:12288\n"
      "v_mfma_f32_16x16x32_bf16 v[136:139], a[120:123], v[112:115], v[136:139]\n"
      "v_lshlrev_b32 v4, 16, v170\n"
      "v_mfma_f32_16x16x32_bf16 v[136:139], a[124:127], v[116:119], v[136:139]\n"
      "v_and_b32 v5, 0xffff0000, v170\n"
      "v_mfma_f32_16x16x32_bf16 v[132:135], a[128:131], v[36:39], v[8:11]\n"
      "v_lshlrev_b32 v6, 16, v171\n"
      "v_mfma_f32_16x16x32_bf16 v[132:135], a[132:135], v[40:43], v[132:135]\n"
      "v_and_b32 v7, 0xffff0000, v171\n"
      "v_mfma_f32_16x16x32_bf16 v[132:135], a[136:139], v[44:47], v[132:135]\n"
      "s_cbranch_vccnz LT34\n"
      "LS34:\n"
      "v_mfma_f32_16x16x32_bf16 v[132:135], a[140:143], v[60:63], v[132:135]\n"
      "v_cvt_pk_bf16_f32 v210, v136, v137\n"
      "v_mfma_f32_16x16x32_bf16 v[132:135], a[144:147], v[64:67], v[132:135]\n"
      "v_cvt_pk_bf16_f32 v211, v138, v139\n"
      "v_mfma_f32_16x16x32_bf16 v[132:135], a[148:151], v[68:71], v[132:135]\n"
      "v_lshlrev_b32 v8, 16, v172\n"
      "v_mfma_f32_16x16x32_bf16 v[132:135], a[152:155], v[84:87], v[132:135]\n"
      "s_waitcnt lgkmcnt(3)\n"
      "v_mfma_f32_16x16x32_bf16 v[132:135], a[156:159], v[88:91], v[132:135]\n"
      "global_store_dwordx4 v252, v[52:55], s[54:55] offset:768 sc1\n"
      "v_mfma_f32_16x16x32_bf16 v[132:135], a[160:163], v[92:95], v[132:135]\n"
      "s_add_u32 m0, s17, 0x2f00\n"
      "v_mfma_f32_16x16x32_bf16 v[132:135], a[164:167], v[108:111], v[132:135]\n"
      "ds_write_b64 v244, v[210:211] offset:12296\n"
      "v_mfma_f32_16x16x32_bf16 v[132:135], a[168:171], v[112:115], v[132:135]\n"
      "global_load_lds_dwordx4 v237, s[52:53] offset:256 nt\n"
      "v_mfma_f32_16x16x32_bf16 v[132:135], a[172:175], v[116:119], v[132:135]\n"
      "v_and_b32 v9, 0xffff0000, v172\n"
      "v_mfma_f32_16x16x32_bf16 v[136:139], a[176:179], v[36:39], v[12:15]\n"
      "v_lshlrev_b32 v10, 16, v173\n"
      "v_mfma_f32_16x16x32_bf16 v[136:139], a[180:183], v[40:43], v[136:139]\n"
      "v_and_b32 v11, 0xffff0000, v173\n"
      "v_mfma_f32_16x16x32_bf16 v[136:139], a[184:187], v[44:47], v[136:139]\n"
      "v_mfma_f32_16x16x32_bf16 v[136:139], a[188:191], v[60:63], v[136:139]\n"
      "v_cvt_pk_bf16_f32 v212, v132, v133\n"
      "v_mfma_f32_16x16x32_bf16 v[136:139], a[192:195], v[64:67], v[136:139]\n"
      "v_cvt_pk_bf16_f32 v213, v134, v135\n"
      "v_mfma_f32_16x16x32_bf16 v[136:139], a[196:199], v[68:71], v[136:139]\n"
      "v_lshlrev_b32 v12, 16, v174\n"
      "v_mfma_f32_16x16x32_bf16 v[136:139], a[200:203], v[84:87], v[136:139]\n"
      "v_and_b32 v13, 0xffff0000, v174\n"
      "v_mfma_f32_16x16x32_bf16 v[136:139], a[204:207], v[88:91], v[136:139]\n"
      "s_waitcnt lgkmcnt(3)\n"
      "v_mfma_f32_16x16x32_bf16 v[136:139], a[208:211], v[92:95], v[136:139]\n"
      "global_store_dwordx4 v253, v[56:59], s[54:55] offset:768 sc1\n"
      "v_mfma_f32_16x16x32_bf16 v[136:139], a[212:215], v[108:111], v[136:139]\n"
      "ds_write_b64 v245, v[212:213] offset:12288\n"
      "v_mfma_f32_16x16x32_bf16 v[136:139], a[216:219], v[112:115], v[136:139]\n"
      "s_add_u32 m0, s17, 0x4e00\n"
      "v_mfma_f32_16x16x32_bf16 v[136:139], a[220:223], v[116:119], v[136:139]\n"
      "global_load_lds_dwordx4 v237, s[52:53] offset:512 nt\n"
      "v_mfma_f32_16x16x32_bf16 v[132:135], a[224:227], v[36:39], v[16:19]\n"
      "v_lshlrev_b32 v14, 16, v175\n"
      "v_mfma_f32_16x16x32_bf16 v[132:135], a[228:231], v[40:43], v[132:135]\n"
      "v_and_b32 v15, 0xffff0000, v175\n"
      "v_mfma_f32_16x16x32_bf16 v[132:135], a[232:235], v[44:47], v[132:135]\n"
      "s_cbranch_vccnz LT35\n"
      "LS35:\n"
      "v_mfma_f32_16x16x32_bf16 v[132:135], a[236:239], v[60:63], v[132:135]\n"
      "s_memrealtime s[84:85]\n"
      "v_mfma_f32_16x16x32_bf16 v[132:135], a[0:3], v[64:67], v[132:135]\n"
      "v_cvt_pk_bf16_f32 v214, v136, v137\n"
      "v_mfma_f32_16x16x32_bf16 v[132:135], a[4:7], v[68:71], v[132:135]\n"
      "v_cvt_pk_bf16_f32 v215, v138, v139\n"
      "v_mfma_f32_16x16x32_bf16 v[132:135], a[8:11], v[84:87], v[132:135]\n"
      "v_lshlrev_b32 v16, 16, v176\n"
      "v_mfma_f32_16x16x32_bf16 v[132:135], a[12:15], v[88:91], v[132:135]\n"
      "s_add_u32 s83, s83, s86\n"
      "v_mfma_f32_16x16x32_bf16 v[132:135], a[16:19], v[92:95], v[132:135]\n"
      "v_and_b32 v17, 0xffff0000, v176\n"
      "v_mfma_f32_16x16x32_bf16 v[132:135], a[20:23], v[108:111], v[132:135]\n"
      "v_lshlrev_b32 v18, 16, v177\n"
      "v_mfma_f32_16x16x32_bf16 v[132:135], v[140:143], v[112:115], v[132:135]\n"
      "s_waitcnt lgkmcnt(0)\n"
      "s_sub_u32 s84, s84, s83\n"
      "v_mfma_f32_16x16x32_bf16 v[132:135], v[156:159], v[116:119], v[132:135]\n"
      "s_cmp_lt_i32 s84, 0\n"
      "s_cselect_b64 vcc, -1, 0\n"
      "s_waitcnt vmcnt(24)\n"
      "s_barrier\n"
      "v_mfma_f32_16x16x32_bf16 v[136:139], v[160:163], v[36:39], v[20:23]\n"
      "ds_read_b128 v[36:39], v232 offset:0\n"
      "ds_write_b64 v245, v[214:215] offset:12296\n"
      "v_mfma_f32_16x16x32_bf16 v[136:139], v[164:167], v[40:43], v[136:139]\n"
      "ds_read_b128 v[40:43], v233 offset:0\n"
      "v_mfma_f32_16x16x32_bf16 v[136:139], v[180:183], v[44:47], v[136:139]\n"
      "ds_read_b128 v[44:47], v234 offset:0\n"
      "v_mfma_f32_16x16x32_bf16 v[136:139], v[184:187], v[60:63], v[136:139]\n"
      "ds_read_b128 v[60:63], v235 offset:0\n"
      "v_cvt_pk_bf16_f32 v208, v132, v133\n"
      "v_mfma_f32_16x16x32_bf16 v[136:139], v[188:191], v[64:67], v[136:139]\n"
      "ds_read_b128 v[64:67], v232 offset:8192\n"
      "v_cvt_pk_bf16_f32 v209, v134, v135\n"
      "v_mfma_f32_16x16x32_bf16 v[136:139], v[204:207], v[68:71], v[136:139]\n"
      "ds_read_b128 v[68:71], v233 offset:8192\n"
      "v_mfma_f32_16x16x32_bf16 v[136:139], a[24:27], v[84:87], v[136:139]\n"
      "ds_read_b128 v[84:87], v234 offset:8192\n"
      "v_mfma_f32_16x16x32_bf16 v[136:139], a[28:31], v[88:91], v[136:139]\n"
      "ds_read_b128 v[88:91], v235 offset:8192\n"
      "v_mfma_f32_16x16x32_bf16 v[136:139], a[32:35], v[92:95], v[136:139]\n"
      "ds_read_b128 v[92:95], v232 offset:16384\n"
      "v_mfma_f32_16x16x32_bf16 v[136:139], a[36:39], v[108:111], v[136:139]\n"
      "ds_read_b128 v[108:111], v233 offset:16384\n"
      "v_mfma_f32_16x16x32_bf16 v[136:139], a[40:43], v[112:115], v[136:139]\n"
      "ds_read_b128 v[112:115], v234 offset:16384\n"
      "v_mfma_f32_16x16x32_bf16 v[136:139], a[44:47], v[116:119], v[136:139]\n"
      "ds_read_b128 v[116:119], v235 offset:16384\n"
      "s_waitcnt lgkmcnt(8)\n"
      "v_mfma_f32_16x16x32_bf16 v[132:135], a[240:243], v[36:39], v[0:3]\n"
      "ds_write_b64 v244, v[208:209] offset:12416\n"
      "s_cbranch_vccnz LT36\n"
      "LS36:\n"
      "v_mfma_f32_16x16x32_bf16 v[132:135], a[244:247], v[40:43], v[132:135]\n"
      "v_and_b32 v19, 0xffff0000, v177\n"
      "v_mfma_f32_16x16x32_bf16 v[132:135], a[248:251], v[44:47], v[132:135]\n"
      "v_lshlrev_b32 v20, 16, v178\n"
      "v_mfma_f32_16x16x32_bf16 v[132:135], a[252:255], v[60:63], v[132:135]\n"
      "v_and_b32 v21, 0xffff0000, v178\n"
      "s_waitcnt lgkmcnt(5)\n"
      "v_mfma_f32_16x16x32_bf16 v[132:135], a[48:51], v[64:67], v[132:135]\n"
      "v_lshlrev_b32 v22, 16, v179\n"
      "v_mfma_f32_16x16x32_bf16 v[132:135], a[52:55], v[68:71], v[132:135]\n"
      "v_and_b32 v23, 0xffff0000, v179\n"
      "v_mfma_f32_16x16x32_bf16 v[132:135], a[56:59], v[84:87], v[132:135]\n"
      "v_cvt_pk_bf16_f32 v210, v136, v137\n"
      "v_mfma_f32_16x16x32_bf16 v[132:135], a[60:63], v[88:91], v[132:135]\n"
      "v_cvt_pk_bf16_f32 v211, v138, v139\n"
      "s_waitcnt lgkmcnt(1)\n"
      "v_mfma_f32_16x16x32_bf16 v[132:135], a[64:67], v[92:95], v[132:135]\n"
      "ds_read_b128 v[72:75], v246 offset:0\n"
      "v_mfma_f32_16x16x32_bf16 v[132:135], a[68:71], v[108:111], v[132:135]\n"
      "ds_read_b128 v[76:79], v246 offset:1024\n"
      "v_mfma_f32_16x16x32_bf16 v[132:135], a[72:75], v[112:115], v[132:135]\n"
      "ds_read_b128 v[80:83], v246 offset:2048\n"
      "v_mfma_f32_16x16x32_bf16 v[132:135], a[76:79], v[116:119], v[132:135]\n"
      "v_mfma_f32_16x16x32_bf16 v[136:139], a[80:83], v[36:39], v[4:7]\n"
      "ds_write_b64 v244, v[210:211] offset:12424\n"
      "v_mfma_f32_16x16x32_bf16 v[136:139], a[84:87], v[40:43], v[136:139]\n"
      "v_mfma_f32_16x16x32_bf16 v[136:139], a[88:91], v[44:47], v[136:139]\n"
      "v_mfma_f32_16x16x32_bf16 v[136:139], a[92:95], v[60:63], v[136:139]\n"
      "v_cvt_pk_bf16_f32 v208, v132, v133\n"
      "s_add_u32 s54, s44, s38\n"
      "s_addc_u32 s55, s45, 0\n"
      "v_mfma_f32_16x16x32_bf16 v[136:139], a[96:99], v[64:67], v[136:139]\n"
      "v_cvt_pk_bf16_f32 v209, v134, v135\n"
      "v_mfma_f32_16x16x32_bf16 v[136:139], a[100:103], v[68:71], v[136:139]\n"
      "s_waitcnt lgkmcnt(3)\n"
      "v_mfma_f32_16x16x32_bf16 v[136:139], a[104:107], v[84:87], v[136:139]\n"
      "global_store_dwordx4 v247, v[72:75], s[54:55] offset:0 sc1\n"
      "v_mfma_f32_16x16x32_bf16 v[136:139], a[108:111], v[88:91], v[136:139]\n"
      "s_add_u32 s52, s42, s40\n"
      "s_addc_u32 s53, s43, 0\n"
      "v_mfma_f32_16x16x32_bf16 v[136:139], a[112:115], v[92:95], v[136:139]\n"
      "s_add_u32 m0, s17, 0x6000\n"
      "v_mfma_f32_16x16x32_bf16 v[136:139], a[116:119], v[108:111], v[136:139]\n"
      "ds_write_b64 v244, v[208:209] offset:24576\n"
      "v_mfma_f32_16x16x32_bf16 v[136:139], a[120:123], v[112:115], v[136:139]\n"
      "global_load_lds_dwordx4 v236, s[52:53] offset:0 nt\n"
      "v_mfma_f32_16x16x32_bf16 v[136:139], a[124:127], v[116:119], v[136:139]\n"
      "v_mfma_f32_16x16x32_bf16 v[132:135], a[128:131], v[36:39], v[8:11]\n"
      "s_cbranch_vccnz LT37\n"
      "LS37:\n"
      "v_mfma_f32_16x16x32_bf16 v[132:135], a[132:135], v[40:43], v[132:135]\n"
      "v_mfma_f32_16x16x32_bf16 v[132:135], a[136:139], v[44:47], v[132:135]\n"
      "v_mfma_f32_16x16x32_bf16 v[132:135], a[140:143], v[60:63], v[132:135]\n"
      "v_cvt_pk_bf16_f32 v210, v136, v137\n"
      "v_mfma_f32_16x16x32_bf16 v[132:135], a[144:147], v[64:67], v[132:135]\n"
      "v_cvt_pk_bf16_f32 v211, v138, v139\n"
      "v_mfma_f32_16x16x32_bf16 v[132:135], a[148:151], v[68:71], v[132:135]\n"
      "v_mfma_f32_16x16x32_bf16 v[132:135], a[152:155], v[84:87], v[132:135]\n"
      "s_waitcnt lgkmcnt(3)\n"
      "v_mfma_f32_16x16x32_bf16 v[132:135], a[156:159], v[88:91], v[132:135]\n"
      "global_store_dwordx4 v252, v[76:79], s[54:55] offset:0 sc1\n"
      "v_mfma_f32_16x16x32_bf16 v[132:135], a[160:163], v[92:95], v[132:135]\n"
      "s_add_u32 m0, s17, 0x7f00\n"
      "v_mfma_f32_16x16x32_bf16 v[132:135], a[164:167], v[108:111], v[132:135]\n"
      "ds_write_b64 v244, v[210:211] offset:24584\n"
      "v_mfma_f32_16x16x32_bf16 v[132:135], a[168:171], v[112:115], v[132:135]\n"
      "global_load_lds_dwordx4 v236, s[52:53] offset:256 nt\n"
      "v_mfma_f32_16x16x32_bf16 v[132:135], a[172:175], v[116:119], v[132:135]\n"
      "v_mfma_f32_16x16x32_bf16 v[136:139], a[176:179], v[36:39], v[12:15]\n"
      "v_mfma_f32_16x16x32_bf16 v[136:139], a[180:183], v[40:43], v[136:139]\n"
      "v_mfma_f32_16x16x32_bf16 v[136:139], a[184:187], v[44:47], v[136:139]\n"
      "v_mfma_f32_16x16x32_bf16 v[136:139], a[188:191], v[60:63], v[136:139]\n"
      "v_cvt_pk_bf16_f32 v212, v132, v133\n"
      "v_mfma_f32_16x16x32_bf16 v[136:139], a[192:195], v[64:67], v[136:139]\n"
      "v_cvt_pk_bf16_f32 v213, v134, v135\n"
      "v_mfma_f32_16x16x32_bf16 v[136:139], a[196:199], v[68:71], v[136:139]\n"
      "v_mfma_f32_16x16x32_bf16 v[136:139], a[200:203], v[84:87], v[136:139]\n"
      "v_mfma_f32_16x16x32_bf16 v[136:139], a[204:207], v[88:91], v[136:139]\n"
      "s_waitcnt lgkmcnt(3)\n"
      "v_mfma_f32_16x16x32_bf16 v[136:139], a[208:211], v[92:95], v[136:139]\n"
      "global_store_dwordx4 v253, v[80:83], s[54:55] offset:0 sc1\n"
      "v_mfma_f32_16x16x32_bf16 v[136:139], a[212:215], v[108:111], v[136:139]\n"
      "ds_write_b64 v245, v[212:213] offset:24576\n"
      "v_mfma_f32_16x16x32_bf16 v[136:139], a[216:219], v[112:115], v[136:139]\n"
      "s_add_u32 m0, s17, 0x9e00\n"
      "v_mfma_f32_16x16x32_bf16 v[136:139], a[220:223], v[116:119], v[136:139]\n"
      "global_load_lds_dwordx4 v236, s[52:53] offset:512 nt\n"
      "v_mfma_f32_16x16x32_bf16 v[132:135], a[224:227], v[36:39], v[16:19]\n"
      "s_cbranch_vccnz LT38\n"
      "LS38:\n"
      "v_mfma_f32_16x16x32_bf16 v[132:135], a[228:231], v[40:43], v[132:135]\n"
      "v_mfma_f32_16x16x32_bf16 v[132:135], a[232:235], v[44:47], v[132:135]\n"
      "v_mfma_f32_16x16x32_bf16 v[132:135], a[236:239], v[60:63], v[132:135]\n"
      "v_cvt_pk_bf16_f32 v214, v136, v137\n"
      "v_mfma_f32_16x16x32_bf16 v[132:135], a[0:3], v[64:67], v[132:135]\n"
      "v_cvt_pk_bf16_f32 v215, v138, v139\n"
      "v_mfma_f32_16x16x32_bf16 v[132:135], a[4:7], v[68:71], v[132:135]\n"
      "v_mfma_f32_16x16x32_bf16 v[132:135], a[8:11], v[84:87], v[132:135]\n"
      "v_mfma_f32_16x16x32_bf16 v[132:135], a[12:15], v[88:91], v[132:135]\n"
      "v_mfma_f32_16x16x32_bf16 v[132:135], a[16:19], v[92:95], v[132:135]\n"
      "v_mfma_f32_16x16x32_bf16 v[132:135], a[20:23], v[108:111], v[132:135]\n"
      "ds_write_b64 v245, v[214:215] offset:24584\n"
      "v_mfma_f32_16x16x32_bf16 v[132:135], v[140:143], v[112:115], v[132:135]\n"
      "v_mfma_f32_16x16x32_bf16 v[132:135], v[156:159], v[116:119], v[132:135]\n"
      "s_waitcnt lgkmcnt(4)\n"
      "s_barrier\n"
      "v_mfma_f32_16x16x32_bf16 v[136:139], v[160:163], v[36:39], v[20:23]\n"
      "ds_read_b128 v[36:39], v232 offset:256\n"
      "v_mfma_f32_16x16x32_bf16 v[136:139], v[164:167], v[40:43], v[136:139]\n"
      "ds_read_b128 v[40:43], v233 offset:256\n"
      "v_mfma_f32_16x16x32_bf16 v[136:139], v[180:183], v[44:47], v[136:139]\n"
      "ds_read_b128 v[44:47], v234 offset:256\n"
      "v_mfma_f32_16x16x32_bf16 v[136:139], v[184:187], v[60:63], v[136:139]\n"
      "ds_read_b128 v[60:63], v235 offset:256\n"
      "v_cvt_pk_bf16_f32 v208, v132, v133\n"
      "v_mfma_f32_16x16x32_bf16 v[136:139], v[188:191], v[64:67], v[136:139]\n"
      "ds_read_b128 v[64:67], v232 offset:8448\n"
      "v_cvt_pk_bf16_f32 v209, v134, v135\n"
      "v_mfma_f32_16x16x32_bf16 v[136:139], v[204:207], v[68:71], v[136:139]\n"
      "ds_read_b128 v[68:71], v233 offset:8448\n"
      "v_mfma_f32_16x16x32_bf16 v[136:139], a[24:27], v[84:87], v[136:139]\n"
      "ds_read_b128 v[84:87], v234 offset:8448\n"
      "v_mfma_f32_16x16x32_bf16 v[136:139], a[28:31], v[88:91], v[136:139]\n"
      "ds_read_b128 v[88:91], v235 offset:8448\n"
      "v_mfma_f32_16x16x32_bf16 v[136:139], a[32:35], v[92:95], v[136:139]\n"
      "ds_read_b128 v[92:95], v232 offset:16640\n"
      "v_mfma_f32_16x16x32_bf16 v[136:139], a[36:39], v[108:111], v[136:139]\n"
      "ds_read_b128 v[108:111], v233 offset:16640\n"
      "v_mfma_f32_16x16x32_bf16 v[136:139], a[40:43], v[112:115], v[136:139]\n"
      "ds_read_b128 v[112:115], v234 offset:16640\n"
      "v_mfma_f32_16x16x32_bf16 v[136:139], a[44:47], v[116:119], v[136:139]\n"
      "ds_read_b128 v[116:119], v235 offset:16640\n"
      "s_waitcnt lgkmcnt(8)\n"
      "v_mfma_f32_16x16x32_bf16 v[132:135], a[240:243], v[36:39], v[0:3]\n"
      "ds_write_b64 v244, v[208:209] offset:24704\n"
      "v_mfma_f32_16x16x32_bf16 v[132:135], a[244:247], v[40:43], v[132:135]\n"
      "s_cbranch_vccnz LT39\n"
      "LS39:\n"
      "v_mfma_f32_16x16x32_bf16 v[132:135], a[248:251], v[44:47], v[132:135]\n"
      "v_mfma_f32_16x16x32_bf16 v[132:135], a[252:255], v[60:63], v[132:135]\n"
      "v_cvt_pk_bf16_f32 v210, v136, v137\n"
      "s_waitcnt lgkmcnt(5)\n"
      "v_mfma_f32_16x16x32_bf16 v[132:135], a[48:51], v[64:67], v[132:135]\n"
      "v_cvt_pk_bf16_f32 v211, v138, v139\n"
      "v_mfma_f32_16x16x32_bf16 v[132:135], a[52:55], v[68:71], v[132:135]\n"
      "v_mfma_f32_16x16x32_bf16 v[132:135], a[56:59], v[84:87], v[132:135]\n"
      "ds_read_b128 v[96:99], v246 offset:12288\n"
      "v_mfma_f32_16x16x32_bf16 v[132:135], a[60:63], v[88:91], v[132:135]\n"
      "ds_read_b128 v[100:103], v246 offset:13312\n"
      "s_waitcnt lgkmcnt(3)\n"
      "v_mfma_f32_16x16x32_bf16 v[132:135], a[64:67], v[92:95], v[132:135]\n"
      "ds_read_b128 v[104:107], v246 offset:14336\n"
      "v_mfma_f32_16x16x32_bf16 v[132:135], a[68:71], v[108:111], v[132:135]\n"
      "ds_write_b64 v244, v[210:211] offset:24712\n"
      "v_mfma_f32_16x16x32_bf16 v[132:135], a[72:75], v[112:115], v[132:135]\n"
      "v_mfma_f32_16x16x32_bf16 v[132:135], a[76:79], v[116:119], v[132:135]\n"
      "v_mfma_f32_16x16x32_bf16 v[136:139], a[80:83], v[36:39], v[4:7]\n"
      "v_mfma_f32_16x16x32_bf16 v[136:139], a[84:87], v[40:43], v[136:139]\n"
      "v_mfma_f32_16x16x32_bf16 v[136:139], a[88:91], v[44:47], v[136:139]\n"
      "v_mfma_f32_16x16x32_bf16 v[136:139], a[92:95], v[60:63], v[136:139]\n"
      "v_cvt_pk_bf16_f32 v208, v132, v133\n"
      "v_mfma_f32_16x16x32_bf16 v[136:139], a[96:99], v[64:67], v[136:139]\n"
      "v_cvt_pk_bf16_f32 v209, v134, v135\n"
      "v_mfma_f32_16x16x32_bf16 v[136:139], a[100:103], v[68:71], v[136:139]\n"
      "v_mfma_f32_16x16x32_bf16 v[136:139], a[104:107], v[84:87], v[136:139]\n"
      "v_mfma_f32_16x16x32_bf16 v[136:139], a[108:111], v[88:91], v[136:139]\n"
      "s_add_u32 m0, s17, 0x7000\n"
      "v_mfma_f32_16x16x32_bf16 v[136:139], a[112:115], v[92:95], v[136:139]\n"
      "global_load_lds_dwordx4 v237, s[52:53] offset:0 nt\n"
      "v_mfma_f32_16x16x32_bf16 v[136:139], a[116:119], v[108:111], v[136:139]\n"
      "ds_write_b64 v244, v[208:209] offset:0\n"
      "v_mfma_f32_16x16x32_bf16 v[136:139], a[120:123], v[112:115], v[136:139]\n"
      "v_mfma_f32_16x16x32_bf16 v[136:139], a[124:127], v[116:119], v[136:139]\n"
      "v_mfma_f32_16x16x32_bf16 v[132:135], a[128:131], v[36:39], v[8:11]\n"
      "s_cbranch_vccnz LT40\n"
      "LS40:\n"
      "v_mfma_f32_16x16x32_bf16 v[132:135], a[132:135], v[40:43], v[132:135]\n"
      "v_mfma_f32_16x16x32_bf16 v[132:135], a[136:139], v[44:47], v[132:135]\n"
      "v_mfma_f32_16x16x32_bf16 v[132:135], a[140:143], v[60:63], v[132:135]\n"
      "v_cvt_pk_bf16_f32 v210, v136, v137\n"
      "v_mfma_f32_16x16x32_bf16 v[132:135], a[144:147], v[64:67], v[132:135]\n"
      "v_cvt_pk_bf16_f32 v211, v138, v139\n"
      "v_mfma_f32_16x16x32_bf16 v[132:135], a[148:151], v[68:71], v[132:135]\n"
      "v_mfma_f32_16x16x32_bf16 v[132:135], a[152:155], v[84:87], v[132:135]\n"
      "s_add_u32 m0, s17, 0x8f00\n"
      "v_mfma_f32_16x16x32_bf16 v[132:135], a[156:159], v[88:91], v[132:135]\n"
      "global_load_lds_dwordx4 v237, s[52:53] offset:256 nt\n"
      "v_mfma_f32_16x16x32_bf16 v[132:135], a[160:163], v[92:95], v[132:135]\n"
      "v_mfma_f32_16x16x32_bf16 v[132:135], a[164:167], v[108:111], v[132:135]\n"
      "ds_write_b64 v244, v[210:211] offset:8\n"
      "v_mfma_f32_16x16x32_bf16 v[132:135], a[168:171], v[112:115], v[132:135]\n"
      "v_mfma_f32_16x16x32_bf16 v[132:135], a[172:175], v[116:119], v[132:135]\n"
      "v_mfma_f32_16x16x32_bf16 v[136:139], a[176:179], v[36:39], v[12:15]\n"
      "v_mfma_f32_16x16x32_bf16 v[136:139], a[180:183], v[40:43], v[136:139]\n"
      "v_mfma_f32_16x16x32_bf16 v[136:139], a[184:187], v[44:47], v[136:139]\n"
      "v_mfma_f32_16x16x32_bf16 v[136:139], a[188:191], v[60:63], v[136:139]\n"
      "v_cvt_pk_bf16_f32 v212, v132, v133\n"
      "v_mfma_f32_16x16x32_bf16 v[136:139], a[192:195], v[64:67], v[136:139]\n"
      "v_cvt_pk_bf16_f32 v213, v134, v135\n"
      "v_mfma_f32_16x16x32_bf16 v[136:139], a[196:199], v[68:71], v[136:139]\n"
      "s_add_u32 m0, s17, 0xae00\n"
      "v_mfma_f32_16x16x32_bf16 v[136:139], a[200:203], v[84:87], v[136:139]\n"
      "global_load_lds_dwordx4 v237, s[52:53] offset:512 nt\n"
      "v_mfma_f32_16x16x32_bf16 v[136:139], a[204:207], v[88:91], v[136:139]\n"
      "v_mfma_f32_16x16x32_bf16 v[136:139], a[208:211], v[92:95], v[136:139]\n"
      "v_mfma_f32_16x16x32_bf16 v[136:139], a[212:215], v[108:111], v[136:139]\n"
      "ds_write_b64 v245, v[212:213] offset:0\n"
      "v_mfma_f32_16x16x32_bf16 v[136:139], a[216:219], v[112:115], v[136:139]\n"
      "v_mfma_f32_16x16x32_bf16 v[136:139], a[220:223], v[116:119], v[136:139]\n"
      "v_mfma_f32_16x16x32_bf16 v[132:135], a[224:227], v[36:39], v[16:19]\n"
      "s_cbranch_vccnz LT41\n"
      "LS41:\n"
      "v_mfma_f32_16x16x32_bf16 v[132:135], a[228:231], v[40:43], v[132:135]\n"
      "v_mfma_f32_16x16x32_bf16 v[132:135], a[232:235], v[44:47], v[132:135]\n"
      "v_mfma_f32_16x16x32_bf16 v[132:135], a[236:239], v[60:63], v[132:135]\n"
      "s_memrealtime s[84:85]\n"
      "v_mfma_f32_16x16x32_bf16 v[132:135], a[0:3], v[64:67], v[132:135]\n"
      "v_cvt_pk_bf16_f32 v214, v136, v137\n"
      "v_mfma_f32_16x16x32_bf16 v[132:135], a[4:7], v[68:71], v[132:135]\n"
      "v_cvt_pk_bf16_f32 v215, v138, v139\n"
      "v_mfma_f32_16x16x32_bf16 v[132:135], a[8:11], v[84:87], v[132:135]\n"
      "v_mfma_f32_16x16x32_bf16 v[132:135], a[12:15], v[88:91], v[132:135]\n"
      "s_add_u32 s83, s83, s86\n"
      "v_mfma_f32_16x16x32_bf16 v[132:135], a[16:19], v[92:95], v[132:135]\n"
      "v_mfma_f32_16x16x32_bf16 v[132:135], a[20:23], v[108:111], v[132:135]\n"
      "v_mfma_f32_16x16x32_bf16 v[132:135], v[140:143], v[112:115], v[132:135]\n"
      "s_waitcnt lgkmcnt(0)\n"
      "s_sub_u32 s84, s84, s83\n"
      "v_mfma_f32_16x16x32_bf16 v[132:135], v[156:159], v[116:119], v[132:135]\n"
      "s_cmp_lt_i32 s84, 0\n"
      "s_cselect_b64 vcc, -1, 0\n"
      "s_waitcnt vmcnt(21)\n"
      "s_barrier\n"
      "v_mfma_f32_16x16x32_bf16 v[136:139], v[160:163], v[36:39], v[20:23]\n"
      "ds_read_b128 v[36:39], v232 offset:24576\n"
      "ds_write_b64 v245, v[214:215] offset:8\n"
      "v_mfma_f32_16x16x32_bf16 v[136:139], v[164:167], v[40:43], v[136:139]\n"
      "ds_read_b128 v[40:43], v233 offset:24576\n"
      "v_mfma_f32_16x16x32_bf16 v[136:139], v[180:183], v[44:47], v[136:139]\n"
      "ds_read_b128 v[44:47], v234 offset:24576\n"
      "v_mfma_f32_16x16x32_bf16 v[136:139], v[184:187], v[60:63], v[136:139]\n"
      "ds_read_b128 v[60:63], v235 offset:24576\n"
      "v_cvt_pk_bf16_f32 v208, v132, v133\n"
      "v_mfma_f32_16x16x32_bf16 v[136:139], v[188:191], v[64:67], v[136:139]\n"
      "ds_read_b128 v[64:67], v232 offset:32768\n"
      "v_cvt_pk_bf16_f32 v209, v134, v135\n"
      "v_mfma_f32_16x16x32_bf16 v[136:139], v[204:207], v[68:71], v[136:139]\n"
      "ds_read_b128 v[68:71], v233 offset:32768\n"
      "v_mfma_f32_16x16x32_bf16 v[136:139], a[24:27], v[84:87], v[136:139]\n"
      "ds_read_b128 v[84:87], v234 offset:32768\n"
      "v_mfma_f32_16x16x32_bf16 v[136:139], a[28:31], v[88:91], v[136:139]\n"
      "ds_read_b128 v[88:91], v235 offset:32768\n"
      "v_mfma_f32_16x16x32_bf16 v[136:139], a[32:35], v[92:95], v[136:139]\n"
      "ds_read_b128 v[92:95], v232 offset:40960\n"
      "v_mfma_f32_16x16x32_bf16 v[136:139], a[36:39], v[108:111], v[136:139]\n"
      "ds_read_b128 v[108:111], v233 offset:40960\n"
      "v_mfma_f32_16x16x32_bf16 v[136:139], a[40:43], v[112:115], v[136:139]\n"
      "ds_read_b128 v[112:115], v234 offset:40960\n"
      "v_mfma_f32_16x16x32_bf16 v[136:139], a[44:47], v[116:119], v[136:139]\n"
      "ds_read_b128 v[116:119], v235 offset:40960\n"
      "s_waitcnt lgkmcnt(8)\n"
      "v_mfma_f32_16x16x32_bf16 v[132:135], a[240:243], v[36:39], v[0:3]\n"
      "ds_write_b64 v244, v[208:209] offset:128\n"
      "v_mfma_f32_16x16x32_bf16 v[132:135], a[244:247], v[40:43], v[132:135]\n"
      "s_cbranch_vccnz LT42\n"
      "LS42:\n"
      "v_mfma_f32_16x16x32_bf16 v[132:135], a[248:251], v[44:47], v[132:135]\n"
      "v_mfma_f32_16x16x32_bf16 v[132:135], a[252:255], v[60:63], v[132:135]\n"
      "v_cvt_pk_bf16_f32 v210, v136, v137\n"
      "s_waitcnt lgkmcnt(5)\n"
      "v_mfma_f32_16x16x32_bf16 v[132:135], a[48:51], v[64:67], v[132:135]\n"
      "v_cvt_pk_bf16_f32 v211, v138, v139\n"
      "v_mfma_f32_16x16x32_bf16 v[132:135], a[52:55], v[68:71], v[132:135]\n"
      "v_mfma_f32_16x16x32_bf16 v[132:135], a[56:59], v[84:87], v[132:135]\n"
      "ds_read_b128 v[120:123], v246 offset:24576\n"
      "v_mfma_f32_16x16x32_bf16 v[132:135], a[60:63], v[88:91], v[132:135]\n"
      "ds_read_b128 v[124:127], v246 offset:25600\n"
      "s_waitcnt lgkmcnt(3)\n"
      "v_mfma_f32_16x16x32_bf16 v[132:135], a[64:67], v[92:95], v[132:135]\n"
      "ds_read_b128 v[128:131], v246 offset:26624\n"
      "v_mfma_f32_16x16x32_bf16 v[132:135], a[68:71], v[108:111], v[132:135]\n"
      "ds_write_b64 v244, v[210:211] offset:136\n"
      "v_mfma_f32_16x16x32_bf16 v[132:135], a[72:75], v[112:115], v[132:135]\n"
      "v_mfma_f32_16x16x32_bf16 v[132:135], a[76:79], v[116:119], v[132:135]\n"
      "v_mfma_f32_16x16x32_bf16 v[136:139], a[80:83], v[36:39], v[4:7]\n"
      "v_mfma_f32_16x16x32_bf16 v[136:139], a[84:87], v[40:43], v[136:139]\n"
      "v_mfma_f32_16x16x32_bf16 v[136:139], a[88:91], v[44:47], v[136:139]\n"
      "v_mfma_f32_16x16x32_bf16 v[136:139], a[92:95], v[60:63], v[136:139]\n"
      "v_cvt_pk_bf16_f32 v208, v132, v133\n"
      "v_mfma_f32_16x16x32_bf16 v[136:139], a[96:99], v[64:67], v[136:139]\n"
      "v_cvt_pk_bf16_f32 v209, v134, v135\n"
      "v_mfma_f32_16x16x32_bf16 v[136:139], a[100:103], v[68:71], v[136:139]\n"
      "v_mfma_f32_16x16x32_bf16 v[136:139], a[104:107], v[84:87], v[136:139]\n"
      "v_mfma_f32_16x16x32_bf16 v[136:139], a[108:111], v[88:91], v[136:139]\n"
      "v_mfma_f32_16x16x32_bf16 v[136:139], a[112:115], v[92:95], v[136:139]\n"
      "v_mfma_f32_16x16x32_bf16 v[136:139], a[116:119], v[108:111], v[136:139]\n"
      "ds_write_b64 v244, v[208:209] offset:12288\n"
      "v_mfma_f32_16x16x32_bf16 v[136:139], a[120:123], v[112:115], v[136:139]\n"
      "v_mfma_f32_16x16x32_bf16 v[136:139], a[124:127], v[116:119], v[136:139]\n"
      "v_mfma_f32_16x16x32_bf16 v[132:135], a[128:131], v[36:39], v[8:11]\n"
      "s_cbranch_vccnz LT43\n"
      "LS43:\n"
      "v_mfma_f32_16x16x32_bf16 v[132:135], a[132:135], v[40:43], v[132:135]\n"
      "v_mfma_f32_16x16x32_bf16 v[132:135], a[136:139], v[44:47], v[132:135]\n"
      "v_mfma_f32_16x16x32_bf16 v[132:135], a[140:143], v[60:63], v[132:135]\n"
      "v_cvt_pk_bf16_f32 v210, v136, v137\n"
      "v_mfma_f32_16x16x32_bf16 v[132:135], a[144:147], v[64:67], v[132:135]\n"
      "v_cvt_pk_bf16_f32 v211, v138, v139\n"
      "v_mfma_f32_16x16x32_bf16 v[132:135], a[148:151], v[68:71], v[132:135]\n"
      "v_mfma_f32_16x16x32_bf16 v[132:135], a[152:155], v[84:87], v[132:135]\n"
      "v_mfma_f32_16x16x32_bf16 v[132:135], a[156:159], v[88:91], v[132:135]\n"
      "v_mfma_f32_16x16x32_bf16 v[132:135], a[160:163], v[92:95], v[132:135]\n"
      "v_mfma_f32_16x16x32_bf16 v[132:135], a[164:167], v[108:111], v[132:135]\n"
      "ds_write_b64 v244, v[210:211] offset:12296\n"
      "v_mfma_f32_16x16x32_bf16 v[132:135], a[168:171], v[112:115], v[132:135]\n"
      "v_mfma_f32_16x16x32_bf16 v[132:135], a[172:175], v[116:119], v[132:135]\n"
      "v_mfma_f32_16x16x32_bf16 v[136:139], a[176:179], v[36:39], v[12:15]\n"
      "v_mfma_f32_16x16x32_bf16 v[136:139], a[180:183], v[40:43], v[136:139]\n"
      "v_mfma_f32_16x16x32_bf16 v[136:139], a[184:187], v[44:47], v[136:139]\n"
      "v_mfma_f32_16x16x32_bf16 v[136:139], a[188:191], v[60:63], v[136:139]\n"
      "v_cvt_pk_bf16_f32 v212, v132, v133\n"
      "v_mfma_f32_16x16x32_bf16 v[136:139], a[192:195], v[64:67], v[136:139]\n"
      "v_cvt_pk_bf16_f32 v213, v134, v135\n"
      "v_mfma_f32_16x16x32_bf16 v[136:139], a[196:199], v[68:71], v[136:139]\n"
      "v_mfma_f32_16x16x32_bf16 v[136:139], a[200:203], v[84:87], v[136:139]\n"
      "v_mfma_f32_16x16x32_bf16 v[136:139], a[204:207], v[88:91], v[136:139]\n"
      "v_mfma_f32_16x16x32_bf16 v[136:139], a[208:211], v[92:95], v[136:139]\n"
      "v_mfma_f32_16x16x32_bf16 v[136:139], a[212:215], v[108:111], v[136:139]\n"
      "ds_write_b64 v245, v[212:213] offset:12288\n"
      "v_mfma_f32_16x16x32_bf16 v[136:139], a[216:219], v[112:115], v[136:139]\n"
      "v_mfma_f32_16x16x32_bf16 v[136:139], a[220:223], v[116:119], v[136:139]\n"
      "v_mfma_f32_16x16x32_bf16 v[132:135], a[224:227], v[36:39], v[16:19]\n"
      "s_cbranch_vccnz LT44\n"
      "LS44:\n"
      "v_mfma_f32_16x16x32_bf16 v[132:135], a[228:231], v[40:43], v[132:135]\n"
      "v_mfma_f32_16x16x32_bf16 v[132:135], a[232:235], v[44:47], v[132:135]\n"
      "v_mfma_f32_16x16x32_bf16 v[132:135], a[236:239], v[60:63], v[132:135]\n"
      "v_cvt_pk_bf16_f32 v214, v136, v137\n"
      "v_mfma_f32_16x16x32_bf16 v[132:135], a[0:3], v[64:67], v[132:135]\n"
      "v_cvt_pk_bf16_f32 v215, v138, v139\n"
      "v_mfma_f32_16x16x32_bf16 v[132:135], a[4:7], v[68:71], v[132:135]\n"
      "v_mfma_f32_16x16x32_bf16 v[132:135], a[8:11], v[84:87], v[132:135]\n"
      "v_mfma_f32_16x16x32_bf16 v[132:135], a[12:15], v[88:91], v[132:135]\n"
      "v_mfma_f32_16x16x32_bf16 v[132:135], a[16:19], v[92:95], v[132:135]\n"
      "v_mfma_f32_16x16x32_bf16 v[132:135], a[20:23], v[108:111], v[132:135]\n"
      "ds_write_b64 v245, v[214:215] offset:12296\n"
      "v_mfma_f32_16x16x32_bf16 v[132:135], v[140:143], v[112:115], v[132:135]\n"
      "v_mfma_f32_16x16x32_bf16 v[132:135], v[156:159], v[116:119], v[132:135]\n"
      "s_waitcnt lgkmcnt(4)\n"
      "s_barrier\n"
      "v_mfma_f32_16x16x32_bf16 v[136:139], v[160:163], v[36:39], v[20:23]\n"
      "ds_read_b128 v[36:39], v232 offset:24832\n"
      "v_mfma_f32_16x16x32_bf16 v[136:139], v[164:167], v[40:43], v[136:139]\n"
      "ds_read_b128 v[40:43], v233 offset:24832\n"
      "v_mfma_f32_16x16x32_bf16 v[136:139], v[180:183], v[44:47], v[136:139]\n"
      "ds_read_b128 v[44:47], v234 offset:24832\n"
      "v_mfma_f32_16x16x32_bf16 v[136:139], v[184:187], v[60:63], v[136:139]\n"
      "ds_read_b128 v[60:63], v235 offset:24832\n"
      "v_cvt_pk_bf16_f32 v208, v132, v133\n"
      "v_mfma_f32_16x16x32_bf16 v[136:139], v[188:191], v[64:67], v[136:139]\n"
      "ds_read_b128 v[64:67], v232 offset:33024\n"
      "v_cvt_pk_bf16_f32 v209, v134, v135\n"
      "v_mfma_f32_16x16x32_bf16 v[136:139], v[204:207], v[68:71], v[136:139]\n"
      "ds_read_b128 v[68:71], v233 offset:33024\n"
      "v_mfma_f32_16x16x32_bf16 v[136:139], a[24:27], v[84:87], v[136:139]\n"
      "ds_read_b128 v[84:87], v234 offset:33024\n"
      "v_mfma_f32_16x16x32_bf16 v[136:139], a[28:31], v[88:91], v[136:139]\n"
      "ds_read_b128 v[88:91], v235 offset:33024\n"
      "v_mfma_f32_16x16x32_bf16 v[136:139], a[32:35], v[92:95], v[136:139]\n"
      "ds_read_b128 v[92:95], v232 offset:41216\n"
      "v_mfma_f32_16x16x32_bf16 v[136:139], a[36:39], v[108:111], v[136:139]\n"
      "ds_read_b128 v[108:111], v233 offset:41216\n"
      "ds_write_b64 v244, v[208:209] offset:12416\n"
      "v_mfma_f32_16x16x32_bf16 v[136:139], a[40:43], v[112:115], v[136:139]\n"
      "ds_read_b128 v[112:115], v234 offset:41216\n"
      "v_mfma_f32_16x16x32_bf16 v[136:139], a[44:47], v[116:119], v[136:139]\n"
      "ds_read_b128 v[116:119], v235 offset:41216\n"
      "s_waitcnt lgkmcnt(9)\n"
      "v_mfma_f32_16x16x32_bf16 v[132:135], a[240:243], v[36:39], v[0:3]\n"
      "v_add_u32 v232, 0xffff4000, v232\n"
      "s_cbranch_vccnz LT45\n"
      "LS45:\n"
      "v_mfma_f32_16x16x32_bf16 v[132:135], a[244:247], v[40:43], v[132:135]\n"
      "v_add_u32 v233, 0xffff4000, v233\n"
      "v_mfma_f32_16x16x32_bf16 v[132:135], a[248:251], v[44:47], v[132:135]\n"
      "v_add_u32 v234, 0xffff4000, v234\n"
      "v_mfma_f32_16x16x32_bf16 v[132:135], a[252:255], v[60:63], v[132:135]\n"
      "v_add_u32 v235, 0xffff4000, v235\n"
      "s_waitcnt lgkmcnt(5)\n"
      "v_mfma_f32_16x16x32_bf16 v[132:135], a[48:51], v[64:67], v[132:135]\n"
      "v_cvt_pk_bf16_f32 v210, v136, v137\n"
      "v_mfma_f32_16x16x32_bf16 v[132:135], a[52:55], v[68:71], v[132:135]\n"
      "v_cvt_pk_bf16_f32 v211, v138, v139\n"
      "v_mfma_f32_16x16x32_bf16 v[132:135], a[56:59], v[84:87], v[132:135]\n"
      "ds_read_b128 v[144:147], v246 offset:0\n"
      "v_mfma_f32_16x16x32_bf16 v[132:135], a[60:63], v[88:91], v[132:135]\n"
      "ds_read_b128 v[148:151], v246 offset:1024\n"
      "s_waitcnt lgkmcnt(2)\n"
      "v_mfma_f32_16x16x32_bf16 v[132:135], a[64:67], v[92:95], v[132:135]\n"
      "ds_read_b128 v[152:155], v246 offset:2048\n"
      "v_mfma_f32_16x16x32_bf16 v[132:135], a[68:71], v[108:111], v[132:135]\n"
      "v_lshlrev_b32 v0, 16, v192\n"
      "v_mfma_f32_16x16x32_bf16 v[132:135], a[72:75], v[112:115], v[132:135]\n"
      "ds_write_b64 v244, v[210:211] offset:12424\n"
      "v_mfma_f32_16x16x32_bf16 v[132:135], a[76:79], v[116:119], v[132:135]\n"
      "v_and_b32 v1, 0xffff0000, v192\n"
      "v_mfma_f32_16x16x32_bf16 v[136:139], a[80:83], v[36:39], v[4:7]\n"
      "v_lshlrev_b32 v2, 16, v193\n"
      "v_mfma_f32_16x16x32_bf16 v[136:139], a[84:87], v[40:43], v[136:139]\n"
      "v_and_b32 v3, 0xffff0000, v193\n"
      "v_mfma_f32_16x16x32_bf16 v[136:139], a[88:91], v[44:47], v[136:139]\n"
      "v_mfma_f32_16x16x32_bf16 v[136:139], a[92:95], v[60:63], v[136:139]\n"
      "v_cvt_pk_bf16_f32 v208, v132, v133\n"
      "v_mfma_f32_16x16x32_bf16 v[136:139], a[96:99], v[64:67], v[136:139]\n"
      "v_cvt_pk_bf16_f32 v209, v134, v135\n"
      "v_mfma_f32_16x16x32_bf16 v[136:139], a[100:103], v[68:71], v[136:139]\n"
      "v_lshlrev_b32 v4, 16, v194\n"
      "v_mfma_f32_16x16x32_bf16 v[136:139], a[104:107], v[84:87], v[136:139]\n"
      "v_and_b32 v5, 0xffff0000, v194\n"
      "v_mfma_f32_16x16x32_bf16 v[136:139], a[108:111], v[88:91], v[136:139]\n"
      "v_lshlrev_b32 v6, 16, v195\n"
      "v_mfma_f32_16x16x32_bf16 v[136:139], a[112:115], v[92:95], v[136:139]\n"
      "v_and_b32 v7, 0xffff0000, v195\n"
      "v_mfma_f32_16x16x32_bf16 v[136:139], a[116:119], v[108:111], v[136:139]\n"
      "ds_write_b64 v244, v[208:209] offset:24576\n"
      "v_mfma_f32_16x16x32_bf16 v[136:139], a[120:123], v[112:115], v[136:139]\n"
      "v_mfma_f32_16x16x32_bf16 v[136:139], a[124:127], v[116:119], v[136:139]\n"
      "v_mfma_f32_16x16x32_bf16 v[132:135], a[128:131], v[36:39], v[8:11]\n"
      "s_cbranch_vccnz LT46\n"
      "LS46:\n"
      "v_mfma_f32_16x16x32_bf16 v[132:135], a[132:135], v[40:43], v[132:135]\n"
      "v_mfma_f32_16x16x32_bf16 v[132:135], a[136:139], v[44:47], v[132:135]\n"
      "v_mfma_f32_16x16x32_bf16 v[132:135], a[140:143], v[60:63], v[132:135]\n"
      "v_cvt_pk_bf16_f32 v210, v136, v137\n"
      "v_mfma_f32_16x16x32_bf16 v[132:135], a[144:147], v[64:67], v[132:135]\n"
      "v_cvt_pk_bf16_f32 v211, v138, v139\n"
      "v_mfma_f32_16x16x32_bf16 v[132:135], a[148:151], v[68:71], v[132:135]\n"
      "v_lshlrev_b32 v8, 16, v196\n"
      "v_mfma_f32_16x16x32_bf16 v[132:135], a[152:155], v[84:87], v[132:135]\n"
      "v_and_b32 v9, 0xffff0000, v196\n"
      "v_mfma_f32_16x16x32_bf16 v[132:135], a[156:159], v[88:91], v[132:135]\n"
      "v_lshlrev_b32 v10, 16, v197\n"
      "v_mfma_f32_16x16x32_bf16 v[132:135], a[160:163], v[92:95], v[132:135]\n"
      "v_and_b32 v11, 0xffff0000, v197\n"
      "v_mfma_f32_16x16x32_bf16 v[132:135], a[164:167], v[108:111], v[132:135]\n"
      "ds_write_b64 v244, v[210:211] offset:24584\n"
      "v_mfma_f32_16x16x32_bf16 v[132:135], a[168:171], v[112:115], v[132:135]\n"
      "v_mfma_f32_16x16x32_bf16 v[132:135], a[172:175], v[116:119], v[132:135]\n"
      "v_mfma_f32_16x16x32_bf16 v[136:139], a[176:179], v[36:39], v[12:15]\n"
      "v_mfma_f32_16x16x32_bf16 v[136:139], a[180:183], v[40:43], v[136:139]\n"
      "v_mfma_f32_16x16x32_bf16 v[136:139], a[184:187], v[44:47], v[136:139]\n"
      "v_mfma_f32_16x16x32_bf16 v[136:139], a[188:191], v[60:63], v[136:139]\n"
      "v_cvt_pk_bf16_f32 v212, v132, v133\n"
      "v_mfma_f32_16x16x32_bf16 v[136:139], a[192:195], v[64:67], v[136:139]\n"
      "v_cvt_pk_bf16_f32 v213, v134, v135\n"
      "v_mfma_f32_16x16x32_bf16 v[136:139], a[196:199], v[68:71], v[136:139]\n"
      "v_lshlrev_b32 v12, 16, v198\n"
      "v_mfma_f32_16x16x32_bf16 v[136:139], a[200:203], v[84:87], v[136:139]\n"
      "v_and_b32 v13, 0xffff0000, v198\n"
      "v_mfma_f32_16x16x32_bf16 v[136:139], a[204:207], v[88:91], v[136:139]\n"
      "v_lshlrev_b32 v14, 16, v199\n"
      "v_mfma_f32_16x16x32_bf16 v[136:139], a[208:211], v[92:95], v[136:139]\n"
      "v_and_b32 v15, 0xffff0000, v199\n"
      "v_mfma_f32_16x16x32_bf16 v[136:139], a[212:215], v[108:111], v[136:139]\n"
      "ds_write_b64 v245, v[212:213] offset:24576\n"
      "v_mfma_f32_16x16x32_bf16 v[136:139], a[216:219], v[112:115], v[136:139]\n"
      "v_mfma_f32_16x16x32_bf16 v[136:139], a[220:223], v[116:119], v[136:139]\n"
      "v_mfma_f32_16x16x32_bf16 v[132:135], a[224:227], v[36:39], v[16:19]\n"
      "s_cbranch_vccnz LT47\n"
      "LS47:\n"
      "v_mfma_f32_16x16x32_bf16 v[132:135], a[228:231], v[40:43], v[132:135]\n"
      "v_mfma_f32_16x16x32_bf16 v[132:135], a[232:235], v[44:47], v[132:135]\n"
      "v_mfma_f32_16x16x32_bf16 v[132:135], a[236:239], v[60:63], v[132:135]\n"
      "v_cvt_pk_bf16_f32 v214, v136, v137\n"
      "v_mfma_f32_16x16x32_bf16 v[132:135], a[0:3], v[64:67], v[132:135]\n"
      "v_cvt_pk_bf16_f32 v215, v138, v139\n"
      "v_mfma_f32_16x16x32_bf16 v[132:135], a[4:7], v[68:71], v[132:135]\n"
      "v_lshlrev_b32 v16, 16, v200\n"
      "v_mfma_f32_16x16x32_bf16 v[132:135], a[8:11], v[84:87], v[132:135]\n"
      "v_and_b32 v17, 0xffff0000, v200\n"
      "v_mfma_f32_16x16x32_bf16 v[132:135], a[12:15], v[88:91], v[132:135]\n"
      "v_lshlrev_b32 v18, 16, v201\n"
      "v_mfma_f32_16x16x32_bf16 v[132:135], a[16:19], v[92:95], v[132:135]\n"
      "v_and_b32 v19, 0xffff0000, v201\n"
      "v_mfma_f32_16x16x32_bf16 v[132:135], a[20:23], v[108:111], v[132:135]\n"
      "ds_write_b64 v245, v[214:215] offset:24584\n"
      "v_mfma_f32_16x16x32_bf16 v[132:135], v[140:143], v[112:115], v[132:135]\n"
      "v_mfma_f32_16x16x32_bf16 v[132:135], v[156:159], v[116:119], v[132:135]\n"
      "s_waitcnt vmcnt(9) lgkmcnt(4)\n"
      "s_barrier\n"
      "v_mfma_f32_16x16x32_bf16 v[136:139], v[160:163], v[36:39], v[20:23]\n"
      "ds_read_b128 v[36:39], v232 offset:0\n"
      "v_mfma_f32_16x16x32_bf16 v[136:139], v[164:167], v[40:43], v[136:139]\n"
      "ds_read_b128 v[40:43], v233 offset:0\n"
      "v_mfma_f32_16x16x32_bf16 v[136:139], v[180:183], v[44:47], v[136:139]\n"
      "ds_read_b128 v[44:47], v234 offset:0\n"
      "v_mfma_f32_16x16x32_bf16 v[136:139], v[184:187], v[60:63], v[136:139]\n"
      "ds_read_b128 v[60:63], v235 offset:0\n"
      "v_cvt_pk_bf16_f32 v208, v132, v133\n"
      "v_mfma_f32_16x16x32_bf16 v[136:139], v[188:191], v[64:67], v[136:139]\n"
      "ds_read_b128 v[64:67], v232 offset:8192\n"
      "v_cvt_pk_bf16_f32 v209, v134, v135\n"
      "v_mfma_f32_16x16x32_bf16 v[136:139], v[204:207], v[68:71], v[136:139]\n"
      "ds_read_b128 v[68:71], v233 offset:8192\n"
      "v_mfma_f32_16x16x32_bf16 v[136:139], a[24:27], v[84:87], v[136:139]\n"
      "ds_read_b128 v[84:87], v234 offset:8192\n"
      "v_mfma_f32_16x16x32_bf16 v[136:139], a[28:31], v[88:91], v[136:139]\n"
      "ds_read_b128 v[88:91], v235 offset:8192\n"
      "v_mfma_f32_16x16x32_bf16 v[136:139], a[32:35], v[92:95], v[136:139]\n"
      "ds_read_b128 v[92:95], v232 offset:16384\n"
      "v_mfma_f32_16x16x32_bf16 v[136:139], a[36:39], v[108:111], v[136:139]\n"
      "ds_read_b128 v[108:111], v233 offset:16384\n"
      "v_mfma_f32_16x16x32_bf16 v[136:139], a[40:43], v[112:115], v[136:139]\n"
      "ds_read_b128 v[112:115], v234 offset:16384\n"
      "v_mfma_f32_16x16x32_bf16 v[136:139], a[44:47], v[116:119], v[136:139]\n"
      "ds_read_b128 v[116:119], v235 offset:16384\n"
      "s_waitcnt lgkmcnt(8)\n"
      "v_mfma_f32_16x16x32_bf16 v[132:135], a[240:243], v[36:39], v[0:3]\n"
      "ds_write_b64 v244, v[208:209] offset:24704\n"
      "v_mfma_f32_16x16x32_bf16 v[132:135], a[244:247], v[40:43], v[132:135]\n"
      "v_lshlrev_b32 v20, 16, v202\n"
      "v_mfma_f32_16x16x32_bf16 v[132:135], a[248:251], v[44:47], v[132:135]\n"
      "v_and_b32 v21, 0xffff0000, v202\n"
      "v_mfma_f32_16x16x32_bf16 v[132:135], a[252:255], v[60:63], v[132:135]\n"
      "v_lshlrev_b32 v22, 16, v203\n"
      "global_store_dwordx4 v247, v[96:99], s[54:55] offset:768 sc1\n"
      "s_waitcnt lgkmcnt(5)\n"
      "v_mfma_f32_16x16x32_bf16 v[132:135], a[48:51], v[64:67], v[132:135]\n"
      "v_and_b32 v23, 0xffff0000, v203\n"
      "v_mfma_f32_16x16x32_bf16 v[132:135], a[52:55], v[68:71], v[132:135]\n"
      "v_cvt_pk_bf16_f32 v210, v136, v137\n"
      "v_mfma_f32_16x16x32_bf16 v[132:135], a[56:59], v[84:87], v[132:135]\n"
      "v_cvt_pk_bf16_f32 v211, v138, v139\n"
      "v_mfma_f32_16x16x32_bf16 v[132:135], a[60:63], v[88:91], v[132:135]\n"
      "ds_read_b128 v[24:27], v246 offset:12288\n"
      "s_waitcnt lgkmcnt(2)\n"
      "v_mfma_f32_16x16x32_bf16 v[132:135], a[64:67], v[92:95], v[132:135]\n"
      "ds_read_b128 v[28:31], v246 offset:13312\n"
      "v_mfma_f32_16x16x32_bf16 v[132:135], a[68:71], v[108:111], v[132:135]\n"
      "ds_read_b128 v[32:35], v246 offset:14336\n"
      "v_mfma_f32_16x16x32_bf16 v[132:135], a[72:75], v[112:115], v[132:135]\n"
      "v_mfma_f32_16x16x32_bf16 v[132:135], a[76:79], v[116:119], v[132:135]\n"
      "ds_write_b64 v244, v[210:211] offset:24712\n"
      "v_mfma_f32_16x16x32_bf16 v[136:139], a[80:83], v[36:39], v[4:7]\n"
      "v_mfma_f32_16x16x32_bf16 v[136:139], a[84:87], v[40:43], v[136:139]\n"
      "v_mfma_f32_16x16x32_bf16 v[136:139], a[88:91], v[44:47], v[136:139]\n"
      "v_mfma_f32_16x16x32_bf16 v[136:139], a[92:95], v[60:63], v[136:139]\n"
      "v_cvt_pk_bf16_f32 v208, v132, v133\n"
      "global_store_dwordx4 v252, v[100:103], s[54:55] offset:768 sc1\n"
      "v_mfma_f32_16x16x32_bf16 v[136:139], a[96:99], v[64:67], v[136:139]\n"
      "v_cvt_pk_bf16_f32 v209, v134, v135\n"
      "v_mfma_f32_16x16x32_bf16 v[136:139], a[100:103], v[68:71], v[136:139]\n"
      "v_mfma_f32_16x16x32_bf16 v[136:139], a[104:107], v[84:87], v[136:139]\n"
      "v_mfma_f32_16x16x32_bf16 v[136:139], a[108:111], v[88:91], v[136:139]\n"
      "v_mfma_f32_16x16x32_bf16 v[136:139], a[112:115], v[92:95], v[136:139]\n"
      "v_mfma_f32_16x16x32_bf16 v[136:139], a[116:119], v[108:111], v[136:139]\n"
      "ds_write_b64 v244, v[208:209] offset:0\n"
      "v_mfma_f32_16x16x32_bf16 v[136:139], a[120:123], v[112:115], v[136:139]\n"
      "v_mfma_f32_16x16x32_bf16 v[136:139], a[124:127], v[116:119], v[136:139]\n"
      "v_mfma_f32_16x16x32_bf16 v[132:135], a[128:131], v[36:39], v[8:11]\n"
      "v_mfma_f32_16x16x32_bf16 v[132:135], a[132:135], v[40:43], v[132:135]\n"
      "v_mfma_f32_16x16x32_bf16 v[132:135], a[136:139], v[44:47], v[132:135]\n"
      "v_mfma_f32_16x16x32_bf16 v[132:135], a[140:143], v[60:63], v[132:135]\n"
      "v_cvt_pk_bf16_f32 v210, v136, v137\n"
      "v_mfma_f32_16x16x32_bf16 v[132:135], a[144:147], v[64:67], v[132:135]\n"
      "v_cvt_pk_bf16_f32 v211, v138, v139\n"
      "v_mfma_f32_16x16x32_bf16 v[132:135], a[148:151], v[68:71], v[132:135]\n"
      "global_store_dwordx4 v253, v[104:107], s[54:55] offset:768 sc1\n"
      "v_mfma_f32_16x16x32_bf16 v[132:135], a[152:155], v[84:87], v[132:135]\n"
      "v_mfma_f32_16x16x32_bf16 v[132:135], a[156:159], v[88:91], v[132:135]\n"
      "s_lshl_b32 s79, s96, 1\n"
      "v_mfma_f32_16x16x32_bf16 v[132:135], a[160:163], v[92:95], v[132:135]\n"
      "v_mad_i32_i24 v252, v251, s79, v252\n"
      "v_mfma_f32_16x16x32_bf16 v[132:135], a[164:167], v[108:111], v[132:135]\n"
      "ds_write_b64 v244, v[210:211] offset:8\n"
      "v_mfma_f32_16x16x32_bf16 v[132:135], a[168:171], v[112:115], v[132:135]\n"
      "s_mul_i32 s79, s79, s100\n"
      "v_mfma_f32_16x16x32_bf16 v[132:135], a[172:175], v[116:119], v[132:135]\n"
      "v_add_u32 v247, s79, v247\n"
      "v_mfma_f32_16x16x32_bf16 v[136:139], a[176:179], v[36:39], v[12:15]\n"
      "s_mul_i32 s79, s79, s101\n"
      "v_mfma_f32_16x16x32_bf16 v[136:139], a[180:183], v[40:43], v[136:139]\n"
      "v_add_u32 v253, s79, v253\n"
      "v_mfma_f32_16x16x32_bf16 v[136:139], a[184:187], v[44:47], v[136:139]\n"
      "s_add_u32 s54, s30, s39\n"
      "s_addc_u32 s55, s31, 0\n"
      "v_mfma_f32_16x16x32_bf16 v[136:139], a[188:191], v[60:63], v[136:139]\n"
      "v_cvt_pk_bf16_f32 v212, v132, v133\n"
      "v_mfma_f32_16x16x32_bf16 v[136:139], a[192:195], v[64:67], v[136:139]\n"
      "v_cvt_pk_bf16_f32 v213, v134, v135\n"
      "v_mfma_f32_16x16x32_bf16 v[136:139], a[196:199], v[68:71], v[136:139]\n"
      "global_store_dwordx4 v247, v[120:123], s[54:55] offset:0 sc1\n"
      "v_mfma_f32_16x16x32_bf16 v[136:139], a[200:203], v[84:87], v[136:139]\n"
      "v_mfma_f32_16x16x32_bf16 v[136:139], a[204:207], v[88:91], v[136:139]\n"
      "v_mfma_f32_16x16x32_bf16 v[136:139], a[208:211], v[92:95], v[136:139]\n"
      "v_mfma_f32_16x16x32_bf16 v[136:139], a[212:215], v[108:111], v[136:139]\n"
      "ds_write_b64 v245, v[212:213] offset:0\n"
      "v_mfma_f32_16x16x32_bf16 v[136:139], a[216:219], v[112:115], v[136:139]\n"
      "v_mfma_f32_16x16x32_bf16 v[136:139], a[220:223], v[116:119], v[136:139]\n"
      "v_mfma_f32_16x16x32_bf16 v[132:135], a[224:227], v[36:39], v[16:19]\n"
      "v_mfma_f32_16x16x32_bf16 v[132:135], a[228:231], v[40:43], v[132:135]\n"
      "v_mfma_f32_16x16x32_bf16 v[132:135], a[232:235], v[44:47], v[132:135]\n"
      "v_mfma_f32_16x16x32_bf16 v[132:135], a[236:239], v[60:63], v[132:135]\n"
      "v_cvt_pk_bf16_f32 v214, v136, v137\n"
      "v_mfma_f32_16x16x32_bf16 v[132:135], a[0:3], v[64:67], v[132:135]\n"
      "v_cvt_pk_bf16_f32 v215, v138, v139\n"
      "v_mfma_f32_16x16x32_bf16 v[132:135], a[4:7], v[68:71], v[132:135]\n"
      "v_mfma_f32_16x16x32_bf16 v[132:135], a[8:11], v[84:87], v[132:135]\n"
      "global_store_dwordx4 v252, v[124:127], s[54:55] offset:0 sc1\n"
      "v_mfma_f32_16x16x32_bf16 v[132:135], a[12:15], v[88:91], v[132:135]\n"
      "v_mfma_f32_16x16x32_bf16 v[132:135], a[16:19], v[92:95], v[132:135]\n"
      "v_mfma_f32_16x16x32_bf16 v[132:135], a[20:23], v[108:111], v[132:135]\n"
      "ds_write_b64 v245, v[214:215] offset:8\n"
      "v_mfma_f32_16x16x32_bf16 v[132:135], v[140:143], v[112:115], v[132:135]\n"
      "v_mfma_f32_16x16x32_bf16 v[132:135], v[156:159], v[116:119], v[132:135]\n"
      "s_waitcnt lgkmcnt(4)\n"
      "s_barrier\n"
      "v_mfma_f32_16x16x32_bf16 v[136:139], v[160:163], v[36:39], v[20:23]\n"
      "ds_read_b128 v[36:39], v232 offset:256\n"
      "v_mfma_f32_16x16x32_bf16 v[136:139], v[164:167], v[40:43], v[136:139]\n"
      "ds_read_b128 v[40:43], v233 offset:256\n"
      "v_mfma_f32_16x16x32_bf16 v[136:139], v[180:183], v[44:47], v[136:139]\n"
      "ds_read_b128 v[44:47], v234 offset:256\n"
      "v_mfma_f32_16x16x32_bf16 v[136:139], v[184:187], v[60:63], v[136:139]\n"
      "ds_read_b128 v[60:63], v235 offset:256\n"
      "v_cvt_pk_bf16_f32 v208, v132, v133\n"
      "v_mfma_f32_16x16x32_bf16 v[136:139], v[188:191], v[64:67], v[136:139]\n"
      "ds_read_b128 v[64:67], v232 offset:8448\n"
      "v_cvt_pk_bf16_f32 v209, v134, v135\n"
      "v_mfma_f32_16x16x32_bf16 v[136:139], v[204:207], v[68:71], v[136:139]\n"
      "ds_read_b128 v[68:71], v233 offset:8448\n"
      "v_mfma_f32_16x16x32_bf16 v[136:139], a[24:27], v[84:87], v[136:139]\n"
      "ds_read_b128 v[84:87], v234 offset:8448\n"
      "v_mfma_f32_16x16x32_bf16 v[136:139], a[28:31], v[88:91], v[136:139]\n"
      "ds_read_b128 v[88:91], v235 offset:8448\n"
      "v_mfma_f32_16x16x32_bf16 v[136:139], a[32:35], v[92:95], v[136:139]\n"
      "ds_read_b128 v[92:95], v232 offset:16640\n"
      "v_mfma_f32_16x16x32_bf16 v[136:139], a[36:39], v[108:111], v[136:139]\n"
      "ds_read_b128 v[108:111], v233 offset:16640\n"
      "v_mfma_f32_16x16x32_bf16 v[136:139], a[40:43], v[112:115], v[136:139]\n"
      "ds_read_b128 v[112:115], v234 offset:16640\n"
      "v_mfma_f32_16x16x32_bf16 v[136:139], a[44:47], v[116:119], v[136:139]\n"
      "ds_read_b128 v[116:119], v235 offset:16640\n"
      "s_waitcnt lgkmcnt(8)\n"
      "v_mfma_f32_16x16x32_bf16 v[132:135], a[240:243], v[36:39], v[0:3]\n"
      "ds_write_b64 v244, v[208:209] offset:128\n"
      "v_mfma_f32_16x16x32_bf16 v[132:135], a[244:247], v[40:43], v[132:135]\n"
      "v_mfma_f32_16x16x32_bf16 v[132:135], a[248:251], v[44:47], v[132:135]\n"
      "v_mfma_f32_16x16x32_bf16 v[132:135], a[252:255], v[60:63], v[132:135]\n"
      "v_cvt_pk_bf16_f32 v210, v136, v137\n"
      "global_store_dwordx4 v253, v[128:131], s[54:55] offset:0 sc1\n"
      "s_waitcnt lgkmcnt(5)\n"
      "v_mfma_f32_16x16x32_bf16 v[132:135], a[48:51], v[64:67], v[132:135]\n"
      "v_cvt_pk_bf16_f32 v211, v138, v139\n"
      "v_mfma_f32_16x16x32_bf16 v[132:135], a[52:55], v[68:71], v[132:135]\n"
      "v_mfma_f32_16x16x32_bf16 v[132:135], a[56:59], v[84:87], v[132:135]\n"
      "ds_read_b128 v[48:51], v246 offset:24576\n"
      "v_mfma_f32_16x16x32_bf16 v[132:135], a[60:63], v[88:91], v[132:135]\n"
      "ds_read_b128 v[52:55], v246 offset:25600\n"
      "s_waitcnt lgkmcnt(3)\n"
      "v_mfma_f32_16x16x32_bf16 v[132:135], a[64:67], v[92:95], v[132:135]\n"
      "ds_read_b128 v[56:59], v246 offset:26624\n"
      "v_mfma_f32_16x16x32_bf16 v[132:135], a[68:71], v[108:111], v[132:135]\n"
      "ds_write_b64 v244, v[210:211] offset:136\n"
      "v_mfma_f32_16x16x32_bf16 v[132:135], a[72:75], v[112:115], v[132:135]\n"
      "v_mfma_f32_16x16x32_bf16 v[132:135], a[76:79], v[116:119], v[132:135]\n"
      "v_mfma_f32_16x16x32_bf16 v[136:139], a[80:83], v[36:39], v[4:7]\n"
      "v_mfma_f32_16x16x32_bf16 v[136:139], a[84:87], v[40:43], v[136:139]\n"
      "v_mfma_f32_16x16x32_bf16 v[136:139], a[88:91], v[44:47], v[136:139]\n"
      "v_mfma_f32_16x16x32_bf16 v[136:139], a[92:95], v[60:63], v[136:139]\n"
      "v_cvt_pk_bf16_f32 v208, v132, v133\n"
      "global_store_dwordx4 v247, v[144:147], s[54:55] offset:768 sc1\n"
      "v_mfma_f32_16x16x32_bf16 v[136:139], a[96:99], v[64:67], v[136:139]\n"
      "v_cvt_pk_bf16_f32 v209, v134, v135\n"
      "v_mfma_f32_16x16x32_bf16 v[136:139], a[100:103], v[68:71], v[136:139]\n"
      "v_mfma_f32_16x16x32_bf16 v[136:139], a[104:107], v[84:87], v[136:139]\n"
      "v_mfma_f32_16x16x32_bf16 v[136:139], a[108:111], v[88:91], v[136:139]\n"
      "v_mfma_f32_16x16x32_bf16 v[136:139], a[112:115], v[92:95], v[136:139]\n"
      "v_mfma_f32_16x16x32_bf16 v[136:139], a[116:119], v[108:111], v[136:139]\n"
      "ds_write_b64 v244, v[208:209] offset:12288\n"
      "v_mfma_f32_16x16x32_bf16 v[136:139], a[120:123], v[112:115], v[136:139]\n"
      "v_mfma_f32_16x16x32_bf16 v[136:139], a[124:127], v[116:119], v[136:139]\n"
      "v_mfma_f32_16x16x32_bf16 v[132:135], a[128:131], v[36:39], v[8:11]\n"
      "v_mfma_f32_16x16x32_bf16 v[132:135], a[132:135], v[40:43], v[132:135]\n"
      "v_mfma_f32_16x16x32_bf16 v[132:135], a[136:139], v[44:47], v[132:135]\n"
      "v_mfma_f32_16x16x32_bf16 v[132:135], a[140:143], v[60:63], v[132:135]\n"
      "v_cvt_pk_bf16_f32 v210, v136, v137\n"
      "v_mfma_f32_16x16x32_bf16 v[132:135], a[144:147], v[64:67], v[132:135]\n"
      "v_cvt_pk_bf16_f32 v211, v138, v139\n"
      "v_mfma_f32_16x16x32_bf16 v[132:135], a[148:151], v[68:71], v[132:135]\n"
      "global_store_dwordx4 v252, v[148:151], s[54:55] offset:768 sc1\n"
      "v_mfma_f32_16x16x32_bf16 v[132:135], a[152:155], v[84:87], v[132:135]\n"
      "v_mfma_f32_16x16x32_bf16 v[132:135], a[156:159], v[88:91], v[132:135]\n"
      "v_mfma_f32_16x16x32_bf16 v[132:135], a[160:163], v[92:95], v[132:135]\n"
      "v_mfma_f32_16x16x32_bf16 v[132:135], a[164:167], v[108:111], v[132:135]\n"
      "ds_write_b64 v244, v[210:211] offset:12296\n"
      "v_mfma_f32_16x16x32_bf16 v[132:135], a[168:171], v[112:115], v[132:135]\n"
      "v_mfma_f32_16x16x32_bf16 v[132:135], a[172:175], v[116:119], v[132:135]\n"
      "v_mfma_f32_16x16x32_bf16 v[136:139], a[176:179], v[36:39], v[12:15]\n"
      "v_mfma_f32_16x16x32_bf16 v[136:139], a[180:183], v[40:43], v[136:139]\n"
      "v_mfma_f32_16x16x32_bf16 v[136:139], a[184:187], v[44:47], v[136:139]\n"
      "v_mfma_f32_16x16x32_bf16 v[136:139], a[188:191], v[60:63], v[136:139]\n"
      "v_cvt_pk_bf16_f32 v212, v132, v133\n"
      "v_mfma_f32_16x16x32_bf16 v[136:139], a[192:195], v[64:67], v[136:139]\n"
      "v_cvt_pk_bf16_f32 v213, v134, v135\n"
      "v_mfma_f32_16x16x32_bf16 v[136:139], a[196:199], v[68:71], v[136:139]\n"
      "v_mfma_f32_16x16x32_bf16 v[136:139], a[200:203], v[84:87], v[136:139]\n"
      "global_store_dwordx4 v253, v[152:155], s[54:55] offset:768 sc1\n"
      "v_mfma_f32_16x16x32_bf16 v[136:139], a[204:207], v[88:91], v[136:139]\n"
      "v_mfma_f32_16x16x32_bf16 v[136:139], a[208:211], v[92:95], v[136:139]\n"
      "v_mfma_f32_16x16x32_bf16 v[136:139], a[212:215], v[108:111], v[136:139]\n"
      "ds_write_b64 v245, v[212:213] offset:12288\n"
      "v_mfma_f32_16x16x32_bf16 v[136:139], a[216:219], v[112:115], v[136:139]\n"
      "v_mfma_f32_16x16x32_bf16 v[136:139], a[220:223], v[116:119], v[136:139]\n"
      "v_mfma_f32_16x16x32_bf16 v[132:135], a[224:227], v[36:39], v[16:19]\n"
      "v_mfma_f32_16x16x32_bf16 v[132:135], a[228:231], v[40:43], v[132:135]\n"
      "v_mfma_f32_16x16x32_bf16 v[132:135], a[232:235], v[44:47], v[132:135]\n"
      "v_mfma_f32_16x16x32_bf16 v[132:135], a[236:239], v[60:63], v[132:135]\n"
      "v_cvt_pk_bf16_f32 v214, v136, v137\n"
      "s_add_u32 s54, s44, s39\n"
      "s_addc_u32 s55, s45, 0\n"
      "v_mfma_f32_16x16x32_bf16 v[132:135], a[0:3], v[64:67], v[132:135]\n"
      "v_cvt_pk_bf16_f32 v215, v138, v139\n"
      "v_mfma_f32_16x16x32_bf16 v[132:135], a[4:7], v[68:71], v[132:135]\n"
      "v_mfma_f32_16x16x32_bf16 v[132:135], a[8:11], v[84:87], v[132:135]\n"
      "global_store_dwordx4 v247, v[24:27], s[54:55] offset:0 sc1\n"
      "v_mfma_f32_16x16x32_bf16 v[132:135], a[12:15], v[88:91], v[132:135]\n"
      "v_mfma_f32_16x16x32_bf16 v[132:135], a[16:19], v[92:95], v[132:135]\n"
      "v_mfma_f32_16x16x32_bf16 v[132:135], a[20:23], v[108:111], v[132:135]\n"
      "ds_write_b64 v245, v[214:215] offset:12296\n"
      "v_mfma_f32_16x16x32_bf16 v[132:135], v[140:143], v[112:115], v[132:135]\n"
      "v_mfma_f32_16x16x32_bf16 v[132:135], v[156:159], v[116:119], v[132:135]\n"
      "s_waitcnt vmcnt(10) lgkmcnt(4)\n"
      "s_barrier\n"
      "v_mfma_f32_16x16x32_bf16 v[136:139], v[160:163], v[36:39], v[20:23]\n"
      "ds_read_b128 v[36:39], v232 offset:24576\n"
      "v_mfma_f32_16x16x32_bf16 v[136:139], v[164:167], v[40:43], v[136:139]\n"
      "ds_read_b128 v[40:43], v233 offset:24576\n"
      "v_mfma_f32_16x16x32_bf16 v[136:139], v[180:183], v[44:47], v[136:139]\n"
      "ds_read_b128 v[44:47], v234 offset:24576\n"
      "v_mfma_f32_16x16x32_bf16 v[136:139], v[184:187], v[60:63], v[136:139]\n"
      "ds_read_b128 v[60:63], v235 offset:24576\n"
      "v_cvt_pk_bf16_f32 v208, v132, v133\n"
      "v_mfma_f32_16x16x32_bf16 v[136:139], v[188:191], v[64:67], v[136:139]\n"
      "ds_read_b128 v[64:67], v232 offset:32768\n"
      "v_cvt_pk_bf16_f32 v209, v134, v135\n"
      "v_mfma_f32_16x16x32_bf16 v[136:139], v[204:207], v[68:71], v[136:139]\n"
      "ds_read_b128 v[68:71], v233 offset:32768\n"
      "v_mfma_f32_16x16x32_bf16 v[136:139], a[24:27], v[84:87], v[136:139]\n"
      "ds_read_b128 v[84:87], v234 offset:32768\n"
      "v_mfma_f32_16x16x32_bf16 v[136:139], a[28:31], v[88:91], v[136:139]\n"
      "ds_read_b128 v[88:91], v235 offset:32768\n"
      "v_mfma_f32_16x16x32_bf16 v[136:139], a[32:35], v[92:95], v[136:139]\n"
      "ds_read_b128 v[92:95], v232 offset:40960\n"
      "v_mfma_f32_16x16x32_bf16 v[136:139], a[36:39], v[108:111], v[136:139]\n"
      "ds_read_b128 v[108:111], v233 offset:40960\n"
      "v_mfma_f32_16x16x32_bf16 v[136:139], a[40:43], v[112:115], v[136:139]\n"
      "ds_read_b128 v[112:115], v234 offset:40960\n"
      "v_mfma_f32_16x16x32_bf16 v[136:139], a[44:47], v[116:119], v[136:139]\n"
      "ds_read_b128 v[116:119], v235 offset:40960\n"
      "s_waitcnt lgkmcnt(8)\n"
      "v_mfma_f32_16x16x32_bf16 v[132:135], a[240:243], v[36:39], v[0:3]\n"
      "ds_write_b64 v244, v[208:209] offset:12416\n"
      "v_mfma_f32_16x16x32_bf16 v[132:135], a[244:247], v[40:43], v[132:135]\n"
      "v_mfma_f32_16x16x32_bf16 v[132:135], a[248:251], v[44:47], v[132:135]\n"
      "v_mfma_f32_16x16x32_bf16 v[132:135], a[252:255], v[60:63], v[132:135]\n"
      "v_cvt_pk_bf16_f32 v210, v136, v137\n"
      "global_store_dwordx4 v252, v[28:31], s[54:55] offset:0 sc1\n"
      "s_waitcnt lgkmcnt(5)\n"
      "v_mfma_f32_16x16x32_bf16 v[132:135], a[48:51], v[64:67], v[132:135]\n"
      "v_cvt_pk_bf16_f32 v211, v138, v139\n"
      "v_mfma_f32_16x16x32_bf16 v[132:135], a[52:55], v[68:71], v[132:135]\n"
      "v_mfma_f32_16x16x32_bf16 v[132:135], a[56:59], v[84:87], v[132:135]\n"
      "ds_read_b128 v[72:75], v246 offset:0\n"
      "v_mfma_f32_16x16x32_bf16 v[132:135], a[60:63], v[88:91], v[132:135]\n"
      "ds_read_b128 v[76:79], v246 offset:1024\n"
      "s_waitcnt lgkmcnt(3)\n"
      "v_mfma_f32_16x16x32_bf16 v[132:135], a[64:67], v[92:95], v[132:135]\n"
      "ds_read_b128 v[80:83], v246 offset:2048\n"
      "v_mfma_f32_16x16x32_bf16 v[132:135], a[68:71], v[108:111], v[132:135]\n"
      "ds_write_b64 v244, v[210:211] offset:12424\n"
      "v_mfma_f32_16x16x32_bf16 v[132:135], a[72:75], v[112:115], v[132:135]\n"
      "v_mfma_f32_16x16x32_bf16 v[132:135], a[76:79], v[116:119], v[132:135]\n"
      "v_mfma_f32_16x16x32_bf16 v[136:139], a[80:83], v[36:39], v[4:7]\n"
      "v_mfma_f32_16x16x32_bf16 v[136:139], a[84:87], v[40:43], v[136:139]\n"
      "v_mfma_f32_16x16x32_bf16 v[136:139], a[88:91], v[44:47], v[136:139]\n"
      "v_mfma_f32_16x16x32_bf16 v[136:139], a[92:95], v[60:63], v[136:139]\n"
      "v_cvt_pk_bf16_f32 v208, v132, v133\n"
      "global_store_dwordx4 v253, v[32:35], s[54:55] offset:0 sc1\n"
      "v_mfma_f32_16x16x32_bf16 v[136:139], a[96:99], v[64:67], v[136:139]\n"
      "v_cvt_pk_bf16_f32 v209, v134, v135\n"
      "v_mfma_f32_16x16x32_bf16 v[136:139], a[100:103], v[68:71], v[136:139]\n"
      "v_mfma_f32_16x16x32_bf16 v[136:139], a[104:107], v[84:87], v[136:139]\n"
      "v_mfma_f32_16x16x32_bf16 v[136:139], a[108:111], v[88:91], v[136:139]\n"
      "v_mfma_f32_16x16x32_bf16 v[136:139], a[112:115], v[92:95], v[136:139]\n"
      "v_mfma_f32_16x16x32_bf16 v[136:139], a[116:119], v[108:111], v[136:139]\n"
      "ds_write_b64 v244, v[208:209] offset:24576\n"
      "v_mfma_f32_16x16x32_bf16 v[136:139], a[120:123], v[112:115], v[136:139]\n"
      "v_mfma_f32_16x16x32_bf16 v[136:139], a[124:127], v[116:119], v[136:139]\n"
      "v_mfma_f32_16x16x32_bf16 v[132:135], a[128:131], v[36:39], v[8:11]\n"
      "v_mfma_f32_16x16x32_bf16 v[132:135], a[132:135], v[40:43], v[132:135]\n"
      "v_mfma_f32_16x16x32_bf16 v[132:135], a[136:139], v[44:47], v[132:135]\n"
      "v_mfma_f32_16x16x32_bf16 v[132:135], a[140:143], v[60:63], v[132:135]\n"
      "v_cvt_pk_bf16_f32 v210, v136, v137\n"
      "v_mfma_f32_16x16x32_bf16 v[132:135], a[144:147], v[64:67], v[132:135]\n"
      "v_cvt_pk_bf16_f32 v211, v138, v139\n"
      "v_mfma_f32_16x16x32_bf16 v[132:135], a[148:151], v[68:71], v[132:135]\n"
      "global_store_dwordx4 v247, v[48:51], s[54:55] offset:768 sc1\n"
      "v_mfma_f32_16x16x32_bf16 v[132:135], a[152:155], v[84:87], v[132:135]\n"
      "v_mfma_f32_16x16x32_bf16 v[132:135], a[156:159], v[88:91], v[132:135]\n"
      "v_mfma_f32_16x16x32_bf16 v[132:135], a[160:163], v[92:95], v[132:135]\n"
      "v_mfma_f32_16x16x32_bf16 v[132:135], a[164:167], v[108:111], v[132:135]\n"
      "ds_write_b64 v244, v[210:211] offset:24584\n"
      "v_mfma_f32_16x16x32_bf16 v[132:135], a[168:171], v[112:115], v[132:135]\n"
      "v_mfma_f32_16x16x32_bf16 v[132:135], a[172:175], v[116:119], v[132:135]\n"
      "v_mfma_f32_16x16x32_bf16 v[136:139], a[176:179], v[36:39], v[12:15]\n"
      "v_mfma_f32_16x16x32_bf16 v[136:139], a[180:183], v[40:43], v[136:139]\n"
      "v_mfma_f32_16x16x32_bf16 v[136:139], a[184:187], v[44:47], v[136:139]\n"
      "v_mfma_f32_16x16x32_bf16 v[136:139], a[188:191], v[60:63], v[136:139]\n"
      "v_cvt_pk_bf16_f32 v212, v132, v133\n"
      "v_mfma_f32_16x16x32_bf16 v[136:139], a[192:195], v[64:67], v[136:139]\n"
      "v_cvt_pk_bf16_f32 v213, v134, v135\n"
      "v_mfma_f32_16x16x32_bf16 v[136:139], a[196:199], v[68:71], v[136:139]\n"
      "v_mfma_f32_16x16x32_bf16 v[136:139], a[200:203], v[84:87], v[136:139]\n"
      "global_store_dwordx4 v252, v[52:55], s[54:55] offset:768 sc1\n"
      "v_mfma_f32_16x16x32_bf16 v[136:139], a[204:207], v[88:91], v[136:139]\n"
      "v_mfma_f32_16x16x32_bf16 v[136:139], a[208:211], v[92:95], v[136:139]\n"
      "v_mfma_f32_16x16x32_bf16 v[136:139], a[212:215], v[108:111], v[136:139]\n"
      "ds_write_b64 v245, v[212:213] offset:24576\n"
      "v_mfma_f32_16x16x32_bf16 v[136:139], a[216:219], v[112:115], v[136:139]\n"
      "v_mfma_f32_16x16x32_bf16 v[136:139], a[220:223], v[116:119], v[136:139]\n"
      "v_mfma_f32_16x16x32_bf16 v[132:135], a[224:227], v[36:39], v[16:19]\n"
      "v_mfma_f32_16x16x32_bf16 v[132:135], a[228:231], v[40:43], v[132:135]\n"
      "v_mfma_f32_16x16x32_bf16 v[132:135], a[232:235], v[44:47], v[132:135]\n"
      "v_mfma_f32_16x16x32_bf16 v[132:135], a[236:239], v[60:63], v[132:135]\n"
      "v_cvt_pk_bf16_f32 v214, v136, v137\n"
      "v_mfma_f32_16x16x32_bf16 v[132:135], a[0:3], v[64:67], v[132:135]\n"
      "v_cvt_pk_bf16_f32 v215, v138, v139\n"
      "v_mfma_f32_16x16x32_bf16 v[132:135], a[4:7], v[68:71], v[132:135]\n"
      "v_mfma_f32_16x16x32_bf16 v[132:135], a[8:11], v[84:87], v[132:135]\n"
      "global_store_dwordx4 v253, v[56:59], s[54:55] offset:768 sc1\n"
      "v_mfma_f32_16x16x32_bf16 v[132:135], a[12:15], v[88:91], v[132:135]\n"
      "v_mfma_f32_16x16x32_bf16 v[132:135], a[16:19], v[92:95], v[132:135]\n"
      "v_mfma_f32_16x16x32_bf16 v[132:135], a[20:23], v[108:111], v[132:135]\n"
      "ds_write_b64 v245, v[214:215] offset:24584\n"
      "v_mfma_f32_16x16x32_bf16 v[132:135], v[140:143], v[112:115], v[132:135]\n"
      "v_mfma_f32_16x16x32_bf16 v[132:135], v[156:159], v[116:119], v[132:135]\n"
      "s_waitcnt lgkmcnt(4)\n"
      "s_barrier\n"
      "v_mfma_f32_16x16x32_bf16 v[136:139], v[160:163], v[36:39], v[20:23]\n"
      "ds_read_b128 v[36:39], v232 offset:24832\n"
      "s_lshl_b32 s79, s97, 1\n"
      "v_mfma_f32_16x16x32_bf16 v[136:139], v[164:167], v[40:43], v[136:139]\n"
      "ds_read_b128 v[40:43], v233 offset:24832\n"
      "v_mad_i32_i24 v252, v251, s79, v252\n"
      "v_mfma_f32_16x16x32_bf16 v[136:139], v[180:183], v[44:47], v[136:139]\n"
      "ds_read_b128 v[44:47], v234 offset:24832\n"
      "s_mul_i32 s79, s79, s100\n"
      "v_mfma_f32_16x16x32_bf16 v[136:139], v[184:187], v[60:63], v[136:139]\n"
      "ds_read_b128 v[60:63], v235 offset:24832\n"
      "v_cvt_pk_bf16_f32 v208, v132, v133\n"
      "v_add_u32 v247, s79, v247\n"
      "v_mfma_f32_16x16x32_bf16 v[136:139], v[188:191], v[64:67], v[136:139]\n"
      "ds_read_b128 v[64:67], v232 offset:33024\n"
      "v_cvt_pk_bf16_f32 v209, v134, v135\n"
      "v_mfma_f32_16x16x32_bf16 v[136:139], v[204:207], v[68:71], v[136:139]\n"
      "ds_read_b128 v[68:71], v233 offset:33024\n"
      "s_mul_i32 s79, s79, s101\n"
      "v_mfma_f32_16x16x32_bf16 v[136:139], a[24:27], v[84:87], v[136:139]\n"
      "ds_read_b128 v[84:87], v234 offset:33024\n"
      "v_add_u32 v253, s79, v253\n"
      "v_mfma_f32_16x16x32_bf16 v[136:139], a[28:31], v[88:91], v[136:139]\n"
      "ds_read_b128 v[88:91], v235 offset:33024\n"
      "s_add_u32 s54, s30, s40\n"
      "s_addc_u32 s55, s31, 0\n"
      "v_mfma_f32_16x16x32_bf16 v[136:139], a[32:35], v[92:95], v[136:139]\n"
      "ds_read_b128 v[92:95], v232 offset:41216\n"
      "v_mfma_f32_16x16x32_bf16 v[136:139], a[36:39], v[108:111], v[136:139]\n"
      "ds_read_b128 v[108:111], v233 offset:41216\n"
      "global_store_dwordx4 v247, v[72:75], s[54:55] offset:0 sc1\n"
      "v_mfma_f32_16x16x32_bf16 v[136:139], a[40:43], v[112:115], v[136:139]\n"
      "ds_read_b128 v[112:115], v234 offset:41216\n"
      "v_mfma_f32_16x16x32_bf16 v[136:139], a[44:47], v[116:119], v[136:139]\n"
      "ds_read_b128 v[116:119], v235 offset:41216\n"
      "s_waitcnt lgkmcnt(8)\n"
      "v_mfma_f32_16x16x32_bf16 v[132:135], a[240:243], v[36:39], v[0:3]\n"
      "ds_write_b64 v244, v[208:209] offset:24704\n"
      "v_mfma_f32_16x16x32_bf16 v[132:135], a[244:247], v[40:43], v[132:135]\n"
      "v_mfma_f32_16x16x32_bf16 v[132:135], a[248:251], v[44:47], v[132:135]\n"
      "v_mfma_f32_16x16x32_bf16 v[132:135], a[252:255], v[60:63], v[132:135]\n"
      "v_cvt_pk_bf16_f32 v210, v136, v137\n"
      "s_waitcnt lgkmcnt(5)\n"
      "v_mfma_f32_16x16x32_bf16 v[132:135], a[48:51], v[64:67], v[132:135]\n"
      "v_cvt_pk_bf16_f32 v211, v138, v139\n"
      "v_mfma_f32_16x16x32_bf16 v[132:135], a[52:55], v[68:71], v[132:135]\n"
      "v_mfma_f32_16x16x32_bf16 v[132:135], a[56:59], v[84:87], v[132:135]\n"
      "ds_read_b128 v[96:99], v246 offset:12288\n"
      "v_mfma_f32_16x16x32_bf16 v[132:135], a[60:63], v[88:91], v[132:135]\n"
      "ds_read_b128 v[100:103], v246 offset:13312\n"
      "s_waitcnt lgkmcnt(3)\n"
      "v_mfma_f32_16x16x32_bf16 v[132:135], a[64:67], v[92:95], v[132:135]\n"
      "ds_read_b128 v[104:107], v246 offset:14336\n"
      "v_mfma_f32_16x16x32_bf16 v[132:135], a[68:71], v[108:111], v[132:135]\n"
      "ds_write_b64 v244, v[210:211] offset:24712\n"
      "v_mfma_f32_16x16x32_bf16 v[132:135], a[72:75], v[112:115], v[132:135]\n"
      "v_mfma_f32_16x16x32_bf16 v[132:135], a[76:79], v[116:119], v[132:135]\n"
      "v_mfma_f32_16x16x32_bf16 v[136:139], a[80:83], v[36:39], v[4:7]\n"
      "v_mfma_f32_16x16x32_bf16 v[136:139], a[84:87], v[40:43], v[136:139]\n"
      "global_store_dwordx4 v252, v[76:79], s[54:55] offset:0 sc1\n"
      "v_mfma_f32_16x16x32_bf16 v[136:139], a[88:91], v[44:47], v[136:139]\n"
      "v_mfma_f32_16x16x32_bf16 v[136:139], a[92:95], v[60:63], v[136:139]\n"
      "v_cvt_pk_bf16_f32 v208, v132, v133\n"
      "v_mfma_f32_16x16x32_bf16 v[136:139], a[96:99], v[64:67], v[136:139]\n"
      "v_cvt_pk_bf16_f32 v209, v134, v135\n"
      "v_mfma_f32_16x16x32_bf16 v[136:139], a[100:103], v[68:71], v[136:139]\n"
      "v_mfma_f32_16x16x32_bf16 v[136:139], a[104:107], v[84:87], v[136:139]\n"
      "v_mfma_f32_16x16x32_bf16 v[136:139], a[108:111], v[88:91], v[136:139]\n"
      "v_mfma_f32_16x16x32_bf16 v[136:139], a[112:115], v[92:95], v[136:139]\n"
      "v_mfma_f32_16x16x32_bf16 v[136:139], a[116:119], v[108:111], v[136:139]\n"
      "ds_write_b64 v244, v[208:209] offset:0\n"
      "v_mfma_f32_16x16x32_bf16 v[136:139], a[120:123], v[112:115], v[136:139]\n"
      "v_mfma_f32_16x16x32_bf16 v[136:139], a[124:127], v[116:119], v[136:139]\n"
      "v_mfma_f32_16x16x32_bf16 v[132:135], a[128:131], v[36:39], v[8:11]\n"
      "global_store_dwordx4 v253, v[80:83], s[54:55] offset:0 sc1\n"
      "v_mfma_f32_16x16x32_bf16 v[132:135], a[132:135], v[40:43], v[132:135]\n"
      "v_mfma_f32_16x16x32_bf16 v[132:135], a[136:139], v[44:47], v[132:135]\n"
      "v_mfma_f32_16x16x32_bf16 v[132:135], a[140:143], v[60:63], v[132:135]\n"
      "v_cvt_pk_bf16_f32 v210, v136, v137\n"
      "v_mfma_f32_16x16x32_bf16 v[132:135], a[144:147], v[64:67], v[132:135]\n"
      "v_cvt_pk_bf16_f32 v211, v138, v139\n"
      "v_mfma_f32_16x16x32_bf16 v[132:135], a[148:151], v[68:71], v[132:135]\n"
      "v_mfma_f32_16x16x32_bf16 v[132:135], a[152:155], v[84:87], v[132:135]\n"
      "v_mfma_f32_16x16x32_bf16 v[132:135], a[156:159], v[88:91], v[132:135]\n"
      "v_mfma_f32_16x16x32_bf16 v[132:135], a[160:163], v[92:95], v[132:135]\n"
      "v_mfma_f32_16x16x32_bf16 v[132:135], a[164:167], v[108:111], v[132:135]\n"
      "ds_write_b64 v244, v[210:211] offset:8\n"
      "v_mfma_f32_16x16x32_bf16 v[132:135], a[168:171], v[112:115], v[132:135]\n"
      "s_waitcnt lgkmcnt(5)\n"
      "v_mfma_f32_16x16x32_bf16 v[132:135], a[172:175], v[116:119], v[132:135]\n"
      "global_store_dwordx4 v247, v[96:99], s[54:55] offset:768 sc1\n"
      "v_mfma_f32_16x16x32_bf16 v[136:139], a[176:179], v[36:39], v[12:15]\n"
      "v_mfma_f32_16x16x32_bf16 v[136:139], a[180:183], v[40:43], v[136:139]\n"
      "v_mfma_f32_16x16x32_bf16 v[136:139], a[184:187], v[44:47], v[136:139]\n"
      "v_mfma_f32_16x16x32_bf16 v[136:139], a[188:191], v[60:63], v[136:139]\n"
      "v_cvt_pk_bf16_f32 v212, v132, v133\n"
      "v_mfma_f32_16x16x32_bf16 v[136:139], a[192:195], v[64:67], v[136:139]\n"
      "v_cvt_pk_bf16_f32 v213, v134, v135\n"
      "v_mfma_f32_16x16x32_bf16 v[136:139], a[196:199], v[68:71], v[136:139]\n"
      "v_mfma_f32_16x16x32_bf16 v[136:139], a[200:203], v[84:87], v[136:139]\n"
      "v_mfma_f32_16x16x32_bf16 v[136:139], a[204:207], v[88:91], v[136:139]\n"
      "v_mfma_f32_16x16x32_bf16 v[136:139], a[208:211], v[92:95], v[136:139]\n"
      "s_waitcnt lgkmcnt(4)\n"
      "v_mfma_f32_16x16x32_bf16 v[136:139], a[212:215], v[108:111], v[136:139]\n"
      "ds_write_b64 v245, v[212:213] offset:0\n"
      "v_mfma_f32_16x16x32_bf16 v[136:139], a[216:219], v[112:115], v[136:139]\n"
      "global_store_dwordx4 v252, v[100:103], s[54:55] offset:768 sc1\n"
      "v_mfma_f32_16x16x32_bf16 v[136:139], a[220:223], v[116:119], v[136:139]\n"
      "v_mfma_f32_16x16x32_bf16 v[132:135], a[224:227], v[36:39], v[16:19]\n"
      "v_mfma_f32_16x16x32_bf16 v[132:135], a[228:231], v[40:43], v[132:135]\n"
      "v_mfma_f32_16x16x32_bf16 v[132:135], a[232:235], v[44:47], v[132:135]\n"
      "v_mfma_f32_16x16x32_bf16 v[132:135], a[236:239], v[60:63], v[132:135]\n"
      "v_cvt_pk_bf16_f32 v214, v136, v137\n"
      "v_mfma_f32_16x16x32_bf16 v[132:135], a[0:3], v[64:67], v[132:135]\n"
      "v_cvt_pk_bf16_f32 v215, v138, v139\n"
      "v_mfma_f32_16x16x32_bf16 v[132:135], a[4:7], v[68:71], v[132:135]\n"
      "v_mfma_f32_16x16x32_bf16 v[132:135], a[8:11], v[84:87], v[132:135]\n"
      "v_mfma_f32_16x16x32_bf16 v[132:135], a[12:15], v[88:91], v[132:135]\n"
      "s_waitcnt lgkmcnt(4)\n"
      "v_mfma_f32_16x16x32_bf16 v[132:135], a[16:19], v[92:95], v[132:135]\n"
      "global_store_dwordx4 v253, v[104:107], s[54:55] offset:768 sc1\n"
      "v_mfma_f32_16x16x32_bf16 v[132:135], a[20:23], v[108:111], v[132:135]\n"
      "ds_write_b64 v245, v[214:215] offset:8\n"
      "v_mfma_f32_16x16x32_bf16 v[132:135], v[140:143], v[112:115], v[132:135]\n"
      "v_mfma_f32_16x16x32_bf16 v[132:135], v[156:159], v[116:119], v[132:135]\n"
      "s_waitcnt lgkmcnt(4)\n"
      "s_barrier\n"
      "v_mfma_f32_16x16x32_bf16 v[136:139], v[160:163], v[36:39], v[20:23]\n"
      "v_mfma_f32_16x16x32_bf16 v[136:139], v[164:167], v[40:43], v[136:139]\n"
      "v_mfma_f32_16x16x32_bf16 v[136:139], v[180:183], v[44:47], v[136:139]\n"
      "v_mfma_f32_16x16x32_bf16 v[136:139], v[184:187], v[60:63], v[136:139]\n"
      "v_cvt_pk_bf16_f32 v208, v132, v133\n"
      "v_mfma_f32_16x16x32_bf16 v[136:139], v[188:191], v[64:67], v[136:139]\n"
      "v_cvt_pk_bf16_f32 v209, v134, v135\n"
      "v_mfma_f32_16x16x32_bf16 v[136:139], v[204:207], v[68:71], v[136:139]\n"
      "v_mfma_f32_16x16x32_bf16 v[136:139], a[24:27], v[84:87], v[136:139]\n"
      "v_mfma_f32_16x16x32_bf16 v[136:139], a[28:31], v[88:91], v[136:139]\n"
      "v_mfma_f32_16x16x32_bf16 v[136:139], a[32:35], v[92:95], v[136:139]\n"
      "v_mfma_f32_16x16x32_bf16 v[136:139], a[36:39], v[108:111], v[136:139]\n"
      "ds_write_b64 v244, v[208:209] offset:128\n"
      "v_mfma_f32_16x16x32_bf16 v[136:139], a[40:43], v[112:115], v[136:139]\n"
      "v_mfma_f32_16x16x32_bf16 v[136:139], a[44:47], v[116:119], v[136:139]\n"
      "ds_read_b128 v[120:123], v246 offset:24576\n"
      "ds_read_b128 v[124:127], v246 offset:25600\n"
      "ds_read_b128 v[128:131], v246 offset:26624\n"
      "s_add_u32 s54, s44, s40\n"
      "s_addc_u32 s55, s45, 0\n"
      "s_nop 3\n"
      "v_cvt_pk_bf16_f32 v210, v136, v137\n"
      "v_cvt_pk_bf16_f32 v211, v138, v139\n"
      "ds_write_b128 v244, v[208:211] offset:128\n"
      "s_waitcnt lgkmcnt(3)\n"
      "global_store_dwordx4 v247, v[120:123], s[54:55] offset:0 sc1\n"
      "s_waitcnt lgkmcnt(2)\n"
      "global_store_dwordx4 v252, v[124:127], s[54:55] offset:0 sc1\n"
      "s_waitcnt lgkmcnt(1)\n"
      "global_store_dwordx4 v253, v[128:131], s[54:55] offset:0 sc1\n"
      "s_waitcnt lgkmcnt(0)\n"
      "s_barrier\n"
      "ds_read_b128 v[144:147], v246 offset:0\n"
      "ds_read_b128 v[148:151], v246 offset:1024\n"
      "ds_read_b128 v[152:155], v246 offset:2048\n"
      "s_waitcnt lgkmcnt(2)\n"
      "global_store_dwordx4 v247, v[144:147], s[54:55] offset:768 sc1\n"
      "s_waitcnt lgkmcnt(1)\n"
      "global_store_dwordx4 v252, v[148:151], s[54:55] offset:768 sc1\n"
      "s_waitcnt lgkmcnt(0)\n"
      "global_store_dwordx4 v253, v[152:155], s[54:55] offset:768 sc1\n"
      "s_cmp_lg_u32 s87, 0\n"
      "s_cbranch_scc1 LE1\n"
      "v_min_u32 v0, 0x370, v239\n"
      "v_add_u32 v0, 0x2480, v0\n"
      "s_mov_b32 s82, 0x40000\n"
      "LE0:\n"
      "global_load_dwordx4 v[4:7], v0, s[28:29] sc1\n"
      "s_sub_u32 s82, s82, 1\n"
      "s_cbranch_scc1 LE1\n"
      "s_waitcnt vmcnt(0)\n"
      "v_cmp_ne_u64 vcc, s[80:81], v[4:5]\n"
      "v_cmp_ne_u64 s[56:57], s[80:81], v[6:7]\n"
      "s_nop 3\n"
      "s_or_b64 vcc, vcc, s[56:57]\n"
      "s_cbranch_scc1 LE0\n"
      "v_mov_b32 v0, 0\n"
      "v_mov_b32 v2, s80\n"
      "v_mov_b32 v3, s81\n"
      "s_mov_b64 exec, 1\n"
      "global_store_dwordx2 v0, v[2:3], s[28:29] sc1\n"
      "s_mov_b64 exec, -1\n"
      "LE1:\n"
      "s_waitcnt vmcnt(0)\n"
      "s_branch LCEND\n"
      "LPS:\n"
      "s_mov_b64 exec, 1\n"
      "global_store_dword v52, v53, s[28:29] sc1\n"
      "s_mov_b64 exec, -1\n"
      "s_branch LPR\n"
      "LT0:\n"
      "s_nop 15\n"
      "s_nop 3\n"
      "s_branch LS0\n"
      "LT1:\n"
      "s_nop 15\n"
      "s_nop 3\n"
      "s_branch LS1\n"
      "LT2:\n"
      "s_nop 15\n"
      "s_nop 3\n"
      "s_branch LS2\n"
      "LT3:\n"
      "s_nop 15\n"
      "s_nop 3\n"
      "s_branch LS3\n"
      "LT4:\n"
      "s_nop 15\n"
      "s_nop 3\n"
      "s_branch LS4\n"
      "LT5:\n"
      "s_nop 15\n"
      "s_nop 3\n"
      "s_branch LS5\n"
      "LT6:\n"
      "s_nop 15\n"
      "s_nop 3\n"
      "s_branch LS6\n"
      "LT7:\n"
      "s_nop 15\n"
      "s_nop 3\n"
      "s_branch LS7\n"
      "LT8:\n"
      "s_nop 15\n"
      "s_nop 3\n"
      "s_branch LS8\n"
      "LT9:\n"
      "s_nop 15\n"
      "s_nop 3\n"
      "s_branch LS9\n"
      "LT10:\n"
      "s_nop 15\n"
      "s_nop 3\n"
      "s_branch LS10\n"
      "LT11:\n"
      "s_nop 15\n"
      "s_nop 3\n"
      "s_branch LS11\n"
      "LT12:\n"
      "s_nop 15\n"
      "s_nop 3\n"
      "s_branch LS12\n"
      "LT13:\n"
      "s_nop 15\n"
      "s_nop 3\n"
      "s_branch LS13\n"
      "LT14:\n"
      "s_nop 15\n"
      "s_nop 3\n"
      "s_branch LS14\n"
      "LT15:\n"
      "s_nop 15\n"
      "s_nop 3\n"
      "s_branch LS15\n"
      "LT16:\n"
      "s_nop 15\n"
      "s_nop 3\n"
      "s_branch LS16\n"
      "LT17:\n"
      "s_nop 15\n"
      "s_nop 3\n"
      "s_branch LS17\n"
      "LT18:\n"
      "s_nop 15\n"
      "s_nop 3\n"
      "s_branch LS18\n"
      "LT19:\n"
      "s_nop 15\n"
      "s_nop 3\n"
      "s_branch LS19\n"
      "LT20:\n"
      "s_nop 15\n"
      "s_nop 3\n"
      "s_branch LS20\n"
      "LT21:\n"
      "s_nop 15\n"
      "s_nop 3\n"
      "s_branch LS21\n"
      "LT22:\n"
      "s_nop 15\n"
      "s_nop 3\n"
      "s_branch LS22\n"
      "LT23:\n"
      "s_nop 15\n"
      "s_nop 3\n"
      "s_branch LS23\n"
      "LT24:\n"
      "s_nop 15\n"
      "s_nop 3\n"
      "s_branch LS24\n"
      "LT25:\n"
      "s_nop 15\n"
      "s_nop 3\n"
      "s_branch LS25\n"
      "LT26:\n"
      "s_nop 15\n"
      "s_nop 3\n"
      "s_branch LS26\n"
      "LT27:\n"
      "s_nop 15\n"
      "s_nop 3\n"
      "s_branch LS27\n"
      "LT28:\n"
      "s_nop 15\n"
      "s_nop 3\n"
      "s_branch LS28\n"
      "LT29:\n"
      "s_nop 15\n"
      "s_nop 3\n"
      "s_branch LS29\n"
      "LT30:\n"
      "s_nop 15\n"
      "s_nop 3\n"
      "s_branch LS30\n"
      "LT31:\n"
      "s_nop 15\n"
      "s_nop 3\n"
      "s_branch LS31\n"
      "LT32:\n"
      "s_nop 15\n"
      "s_nop 3\n"
      "s_branch LS32\n"
      "LT33:\n"
      "s_nop 15\n"
      "s_nop 3\n"
      "s_branch LS33\n"
      "LT34:\n"
      "s_nop 15\n"
      "s_nop 3\n"
      "s_branch LS34\n"
      "LT35:\n"
      "s_nop 15\n"
      "s_nop 3\n"
      "s_branch LS35\n"
      "LT36:\n"
      "s_nop 15\n"
      "s_nop 3\n"
      "s_branch LS36\n"
      "LT37:\n"
      "s_nop 15\n"
      "s_nop 3\n"
      "s_branch LS37\n"
      "LT38:\n"
      "s_nop 15\n"
      "s_nop 3\n"
      "s_branch LS38\n"
      "LT39:\n"
      "s_nop 15\n"
      "s_nop 3\n"
      "s_branch LS39\n"
      "LT40:\n"
      "s_nop 15\n"
      "s_nop 3\n"
      "s_branch LS40\n"
      "LT41:\n"
      "s_nop 15\n"
      "s_nop 3\n"
      "s_branch LS41\n"
      "LT42:\n"
      "s_nop 15\n"
      "s_nop 3\n"
      "s_branch LS42\n"
      "LT43:\n"
      "s_nop 15\n"
      "s_nop 3\n"
      "s_branch LS43\n"
      "LT44:\n"
      "s_nop 15\n"
      "s_nop 3\n"
      "s_branch LS44\n"
      "LT45:\n"
      "s_nop 15\n"
      "s_nop 3\n"
      "s_branch LS45\n"
      "LT46:\n"
      "s_nop 15\n"
      "s_nop 3\n"
      "s_branch LS46\n"
      "LT47:\n"
      "s_nop 15\n"
      "s_nop 3\n"
      "s_branch LS47\n"
      "LCEND:\n"

      :
      : "{v232}"(cB0), "{v233}"(cB0 ^ 64), "{v234}"(cB0 ^ 128), "{v235}"(cB0 ^ 192), "{v238}"(pvo),
        "{v239}"(lo16), "{v240}"(wro[0]), "{v241}"(wro[1]), "{v242}"(wro[2]), "{v243}"(tra), "{v248}"(fla), "{v249}"(c256),
        "{v250}"(pk), "{v105}"(two), "{v106}"(tcode), "{v[252:253]}"(ep),
        "{s[20:21]}"(cf), "{s[22:23]}"(pc), "{s[24:25]}"(wb), "{s[30:31]}"(out), "{s[28:29]}"(ws), "{s18}"(mP), "{s17}"(mX),
        "{s32}"(xo[0]), "{s33}"(xo[1]), "{s34}"(xo[2]), "{s35}"(xo[3]), "{s36}"(xo[4]), "{s37}"(xo[5]), "{s38}"(xo[6]),
        "{s39}"(xo[7]), "{s40}"(xo[8]), "{s[42:43]}"(cf + 294912), "{s[44:45]}"(out + 294912), "{s[46:47]}"(tab), "{s50}"(var), "{s51}"(b),
        "{s[64:65]}"(w2w), "{s74}"(mW), "{s76}"(wid), "{s77}"(jfl), "{v251}"(sgn),
        "{s89}"(dj[0]), "{s90}"(dj[1]), "{s91}"(dj[2]), "{s92}"(dj[3]), "{s93}"(dj[4]), "{s94}"(dj[5]), "{s95}"(dj[6]),
        "{s96}"(dj[7]), "{s97}"(dj[8]), "{s98}"(d0), "{s99}"(wid ? -1 : 1), "{s100}"(wid < 3 ? 1 : -1), "{s101}"(wid == 2 ? -1 : 1),
        "{v102}"(jt), "{v103}"(jrw), "{v104}"(jrr), "{v[110:111]}"(cd), "{v[112:113]}"(cr), "{v107}"(sg1), "{v108}"(scr), "{s[72:73]}"(jo),
        "{v[4:7]}"(ja0), "{v[8:11]}"(ja1), "{v[12:15]}"(ja2), "{v[16:19]}"(ja3), "{v[20:23]}"(ja4), "{v[24:27]}"(ja5),
        "{s83}"(t_start + (0) + var * ((0) - (0))), "{s86}"(158 + var * ((158) - (158)))
      : "memory", "scc", "vcc", "v0", "v1", "v2", "v3", "v28", "v29", "v30", "v31", "v32", "v33", "v34", "v35", "v36", "v37", "v38", "v39", "v40", "v41", "v42", "v43", "v44", "v45", "v46", "v47", "v48", "v49", "v50", "v51", "v52", "v53", "v54", "v55", "v56", "v57", "v58", "v59", "v60", "v61", "v62", "v63", "v64", "v65", "v66", "v67", "v68", "v69", "v70", "v71", "v72", "v73", "v74", "v75", "v76", "v77", "v78", "v79", "v80", "v81", "v82", "v83", "v84", "v85", "v86", "v87", "v88", "v89", "v90", "v91", "v92", "v93", "v94", "v95", "v96", "v97", "v98", "v99", "v100", "v101", "v109", "v114", "v115", "v116", "v117", "v118", "v119", "v120", "v121", "v122", "v123", "v124", "v125", "v126", "v127", "v128", "v129", "v130", "v131", "v132", "v133", "v134", "v135", "v136", "v137", "v138", "v139", "v140", "v141", "v142", "v143", "v144", "v145", "v146", "v147", "v148", "v149", "v150", "v151", "v152", "v153", "v154", "v155", "v156", "v157", "v158", "v159", "v160", "v161", "v162", "v163", "v164", "v165", "v166", "v167", "v168", "v169", "v170", "v171", "v172", "v173", "v174", "v175", "v176", "v177", "v178", "v179", "v180", "v181", "v182", "v183", "v184", "v185", "v186", "v187", "v188", "v189", "v190", "v191", "v192", "v193", "v194", "v195", "v196", "v197", "v198", "v199", "v200", "v201", "v202", "v203", "v204", "v205", "v206", "v207", "v208", "v209", "v210", "v211", "v212", "v213", "v214", "v215", "v216", "v217", "v218", "v219", "v220", "v221", "v222", "v223", "v224", "v225", "v226", "v227", "v228", "v229", "v230", "v231", "v236", "v237", "v244", "v245", "v246", "v247", "v254", "v255", "a0", "a1", "a2", "a3", "a4", "a5", "a6", "a7", "a8", "a9", "a10", "a11", "a12", "a13", "a14", "a15", "a16", "a17", "a18", "a19", "a20", "a21", "a22", "a23", "a24", "a25", "a26", "a27", "a28", "a29", "a30", "a31", "a32", "a33", "a34", "a35", "a36", "a37", "a38", "a39", "a40", "a41", "a42", "a43", "a44", "a45", "a46", "a47", "a48", "a49", "a50", "a51", "a52", "a53", "a54", "a55", "a56", "a57", "a58", "a59", "a60", "a61", "a62", "a63", "a64", "a65", "a66", "a67", "a68", "a69", "a70", "a71", "a72", "a73", "a74", "a75", "a76", "a77", "a78", "a79", "a80", "a81", "a82", "a83", "a84", "a85", "a86", "a87", "a88", "a89", "a90", "a91", "a92", "a93", "a94", "a95", "a96", "a97", "a98", "a99", "a100", "a101", "a102", "a103", "a104", "a105", "a106", "a107", "a108", "a109", "a110", "a111", "a112", "a113", "a114", "a115", "a116", "a117", "a118", "a119", "a120", "a121", "a122", "a123", "a124", "a125", "a126", "a127", "a128", "a129", "a130", "a131", "a132", "a133", "a134", "a135", "a136", "a137", "a138", "a139", "a140", "a141", "a142", "a143", "a144", "a145", "a146", "a147", "a148", "a149", "a150", "a151", "a152", "a153", "a154", "a155", "a156", "a157", "a158", "a159", "a160", "a161", "a162", "a163", "a164", "a165", "a166", "a167", "a168", "a169", "a170", "a171", "a172", "a173", "a174", "a175", "a176", "a177", "a178", "a179", "a180", "a181", "a182", "a183", "a184", "a185", "a186", "a187", "a188", "a189", "a190", "a191", "a192", "a193", "a194", "a195", "a196", "a197", "a198", "a199", "a200", "a201", "a202", "a203", "a204", "a205", "a206", "a207", "a208", "a209", "a210", "a211", "a212", "a213", "a214", "a215", "a216", "a217", "a218", "a219", "a220", "a221", "a222", "a223", "a224", "a225", "a226", "a227", "a228", "a229", "a230", "a231", "a232", "a233", "a234", "a235", "a236", "a237", "a238", "a239", "a240", "a241", "a242", "a243", "a244", "a245", "a246", "a247", "a248", "a249", "a250", "a251", "a252", "a253", "a254", "a255", "s48", "s49", "s52", "s53", "s54", "s55", "s56", "s57", "s58", "s59", "s60", "s61", "s62", "s63", "s78", "s79", "s80", "s81", "s82", "s84", "s85", "s87", "s19");
}

extern "C" void kernel_launch(void* const* d_in, const int* in_sizes, int n_in,
                              void* d_out, int out_size, void* d_ws, size_t ws_size,
                              hipStream_t stream) {
  (void)hipFuncSetAttribute((const void*)fpn_kernel, hipFuncAttributeMaxDynamicSharedMemorySize, 159808);
  fpn_kernel<<<256, 256, 159808, stream>>>((const char*)d_in[0], (const char*)d_in[1], (const char*)d_in[2],
                                                (const char*)d_in[3], (char*)d_ws, (char*)d_out);
}
```

```cpp
#include <hip/hip_runtime.h>
#include <hip/hip_bf16.h>

typedef __attribute__((ext_vector_type(4))) unsigned u32x4;
typedef __attribute__((ext_vector_type(2))) unsigned u32x2;
#define NSTAMPS 0

__device__ __forceinline__ unsigned col0(unsigned i) { return i + (i >= 5 ? 6 : 0) + (i >= 10 ? 5 : 0); }

static __device__ const unsigned __attribute__((aligned(128))) lane_tab[2048] = {
  98304, 98368, 98304, 0, 99088, 99152, 98320, 16, 99872, 99936, 98336, 32, 100656, 100720, 98352, 48,
  101440, 101376, 98368, 64, 102224, 102160, 98384, 80, 103008, 102944, 98400, 96, 103792, 103728, 98416, 112,
  104448, 104512, 98432, 128, 105232, 105296, 98448, 144, 106016, 106080, 98464, 160, 106800, 106864, 98480, 176,
  107584, 107520, 98496, 192, 108368, 108304, 98512, 208, 109152, 109088, 98528, 224, 109936, 109872, 98544, 240,
  98320, 98384, 98560, 256, 99072, 99136, 98576, 272, 99888, 99952, 98592, 288, 100640, 100704, 98608, 304,
  101456, 101392, 98624, 320, 102208, 102144, 98640, 336, 103024, 102960, 98656, 352, 103776, 103712, 98672, 368,
  104464, 104528, 98688, 384, 105216, 105280, 98704, 400, 106032, 106096, 98720, 416, 106784, 106848, 98736, 432,
  107600, 107536, 98752, 448, 108352, 108288, 98768, 464, 109168, 109104, 98784, 480, 109920, 109856, 98800, 496,
  98336, 98400, 98816, 512, 99120, 99184, 98832, 528, 99840, 99904, 98848, 544, 100624, 100688, 98864, 560,
  101472, 101408, 98880, 576, 102256, 102192, 98896, 592, 102976, 102912, 98912, 608, 103760, 103696, 98928, 624,
  104480, 104544, 98944, 640, 105264, 105328, 98960, 656, 105984, 106048, 98976, 672, 106768, 106832, 98992, 688,
  107616, 107552, 99008, 704, 108400, 108336, 99024, 720, 109120, 109056, 99040, 736, 109904, 109840, 99056, 752,
  98352, 98416, 99072, 1552, 99104, 99168, 99088, 1536, 99856, 99920, 99104, 1584, 100608, 100672, 99120, 1568,
  101488, 101424, 99136, 1616, 102240, 102176, 99152, 1600, 102992, 102928, 99168, 1648, 103744, 103680, 99184, 1632,
  104496, 104560, 99200, 1680, 105248, 105312, 99216, 1664, 106000, 106064, 99232, 1712, 106752, 106816, 99248, 1696,
  107632, 107568, 99264, 1744, 108384, 108320, 99280, 1728, 109136, 109072, 99296, 1776, 109888, 109824, 99312, 1760,
  98496, 98560, 101376, 6208, 99280, 99344, 101392, 6224, 100064, 100128, 101408, 6240, 100848, 100912, 101424, 6256,
  101504, 101696, 101440, 6144, 102288, 102480, 101456, 6160, 103072, 103264, 101472, 6176, 103856, 104048, 101488, 6192,
  104640, 104704, 101504, 6336, 105424, 105488, 101520, 6352, 106208, 106272, 101536, 6368, 106992, 107056, 101552, 6384,
  107648, 107840, 101568, 6272, 108432, 108624, 101584, 6288, 109216, 109408, 101600, 6304, 110000, 110192, 101616, 6320,
  98512, 98576, 101632, 6464, 99264, 99328, 101648, 6480, 100080, 100144, 101664, 6496, 100832, 100896, 101680, 6512,
  101520, 101712, 101696, 6400, 102272, 102464, 101712, 6416, 103088, 103280, 101728, 6432, 103840, 104032, 101744, 6448,
  104656, 104720, 101760, 6592, 105408, 105472, 101776, 6608, 106224, 106288, 101792, 6624, 106976, 107040, 101808, 6640,
  107664, 107856, 101824, 6528, 108416, 108608, 101840, 6544, 109232, 109424, 101856, 6560, 109984, 110176, 101872, 6576,
  98528, 98592, 101888, 6720, 99312, 99376, 101904, 6736, 100032, 100096, 101920, 6752, 100816, 100880, 101936, 6768,
  101536, 101728, 101952, 6656, 102320, 102512, 101968, 6672, 103040, 103232, 101984, 6688, 103824, 104016, 102000, 6704,
  104672, 104736, 102016, 6848, 105456, 105520, 102032, 6864, 106176, 106240, 102048, 6880, 106960, 107024, 102064, 6896,
  107680, 107872, 102080, 6784, 108464, 108656, 102096, 6800, 109184, 109376, 102112, 6816, 109968, 110160, 102128, 6832,
  98544, 98608, 102144, 16976, 99296, 99360, 102160, 16960, 100048, 100112, 102176, 17008, 100800, 100864, 102192, 16992,
  101552, 101744, 102208, 16912, 102304, 102496, 102224, 16896, 103056, 103248, 102240, 16944, 103808, 104000, 102256, 16928,
  104688, 104752, 102272, 17104, 105440, 105504, 102288, 17088, 106192, 106256, 102304, 17136, 106944, 107008, 102320, 17120,
  107696, 107888, 102336, 17040, 108448, 108640, 102352, 17024, 109200, 109392, 102368, 17072, 109952, 110144, 102384, 17056,
  1808, 3616, 0, 21632, 1792, 3632, 16, 21648, 1840, 3584, 32, 21664, 1824, 3600, 48, 21680,
  1872, 3680, 64, 21696, 1856, 3696, 80, 21712, 1904, 3648, 96, 21728, 1888, 3664, 112, 21744,
  1936, 3744, 128, 21504, 1920, 3760, 144, 21520, 1968, 3712, 160, 21536, 1952, 3728, 176, 21552,
  2000, 3808, 192, 21568, 1984, 3824, 208, 21584, 2032, 3776, 224, 21600, 2016, 3792, 240, 21616,
  2064, 4656, 768, 22400, 2048, 4640, 784, 22416, 2096, 4624, 800, 22432, 2080, 4608, 816, 22448,
  2128, 4720, 832, 22464, 2112, 4704, 848, 22480, 2160, 4688, 864, 22496, 2144, 4672, 880, 22512,
  2192, 4784, 896, 22272, 2176, 4768, 912, 22288, 2224, 4752, 928, 22304, 2208, 4736, 944, 22320,
  2256, 4848, 960, 22336, 2240, 4832, 976, 22352, 2288, 4816, 992, 22368, 2272, 4800, 1008, 22384,
  3104, 4912, 1552, 23184, 3120, 4896, 1536, 23168, 3072, 4880, 1584, 23216, 3088, 4864, 1568, 23200,
  3168, 4976, 1616, 23248, 3184, 4960, 1600, 23232, 3136, 4944, 1648, 23280, 3152, 4928, 1632, 23264,
  3232, 5040, 1680, 23056, 3248, 5024, 1664, 23040, 3200, 5008, 1712, 23088, 3216, 4992, 1696, 23072,
  3296, 5104, 1744, 23120, 3312, 5088, 1728, 23104, 3264, 5072, 1776, 23152, 3280, 5056, 1760, 23136,
  3360, 5168, 2320, 23952, 3376, 5152, 2304, 23936, 3328, 5136, 2352, 23984, 3344, 5120, 2336, 23968,
  3424, 5232, 2384, 24016, 3440, 5216, 2368, 24000, 3392, 5200, 2416, 24048, 3408, 5184, 2400, 24032,
  3488, 5296, 2448, 23824, 3504, 5280, 2432, 23808, 3456, 5264, 2480, 23856, 3472, 5248, 2464, 23840,
  3552, 5360, 2512, 23888, 3568, 5344, 2496, 23872, 3520, 5328, 2544, 23920, 3536, 5312, 2528, 23904,
  17232, 19040, 3104, 32416, 17216, 19056, 3120, 32432, 17264, 19008, 3072, 32384, 17248, 19024, 3088, 32400,
  17168, 18976, 3168, 32480, 17152, 18992, 3184, 32496, 17200, 18944, 3136, 32448, 17184, 18960, 3152, 32464,
  17360, 19168, 3232, 32288, 17344, 19184, 3248, 32304, 17392, 19136, 3200, 32256, 17376, 19152, 3216, 32272,
  17296, 19104, 3296, 32352, 17280, 19120, 3312, 32368, 17328, 19072, 3264, 32320, 17312, 19088, 3280, 32336,
  17488, 20080, 3872, 33184, 17472, 20064, 3888, 33200, 17520, 20048, 3840, 33152, 17504, 20032, 3856, 33168,
  17424, 20016, 3936, 33248, 17408, 20000, 3952, 33264, 17456, 19984, 3904, 33216, 17440, 19968, 3920, 33232,
  17616, 20208, 4000, 33056, 17600, 20192, 4016, 33072, 17648, 20176, 3968, 33024, 17632, 20160, 3984, 33040,
  17552, 20144, 4064, 33120, 17536, 20128, 4080, 33136, 17584, 20112, 4032, 33088, 17568, 20096, 4048, 33104,
  18528, 20336, 4656, 33968, 18544, 20320, 4640, 33952, 18496, 20304, 4624, 33936, 18512, 20288, 4608, 33920,
  18464, 20272, 4720, 34032, 18480, 20256, 4704, 34016, 18432, 20240, 4688, 34000, 18448, 20224, 4672, 33984,
  18656, 20464, 4784, 33840, 18672, 20448, 4768, 33824, 18624, 20432, 4752, 33808, 18640, 20416, 4736, 33792,
  18592, 20400, 4848, 33904, 18608, 20384, 4832, 33888, 18560, 20368, 4816, 33872, 18576, 20352, 4800, 33856,
  18784, 20592, 5424, 34736, 18800, 20576, 5408, 34720, 18752, 20560, 5392, 34704, 18768, 20544, 5376, 34688,
  18720, 20528, 5488, 34800, 18736, 20512, 5472, 34784, 18688, 20496, 5456, 34768, 18704, 20480, 5440, 34752,
  18912, 20720, 5552, 34608, 18928, 20704, 5536, 34592, 18880, 20688, 5520, 34576, 18896, 20672, 5504, 34560,
  18848, 20656, 5616, 34672, 18864, 20640, 5600, 34656, 18816, 20624, 5584, 34640, 18832, 20608, 5568, 34624,
  98688, 98752, 104448, 21504, 99472, 99536, 104464, 21520, 100256, 100320, 104480, 21536, 101040, 101104, 104496, 21552,
  101824, 101760, 104512, 21568, 102608, 102544, 104528, 21584, 103392, 103328, 104544, 21600, 104176, 104112, 104560, 21616,
  104832, 104896, 104576, 21632, 105616, 105680, 104592, 21648, 106400, 106464, 104608, 21664, 107184, 107248, 104624, 21680,
  107968, 107904, 104640, 21696, 108752, 108688, 104656, 21712, 109536, 109472, 104672, 21728, 110320, 110256, 104688, 21744,
  98704, 98768, 104704, 21760, 99456, 99520, 104720, 21776, 100272, 100336, 104736, 21792, 101024, 101088, 104752, 21808,
  101840, 101776, 104768, 21824, 102592, 102528, 104784, 21840, 103408, 103344, 104800, 21856, 104160, 104096, 104816, 21872,
  104848, 104912, 104832, 21888, 105600, 105664, 104848, 21904, 106416, 106480, 104864, 21920, 107168, 107232, 104880, 21936,
  107984, 107920, 104896, 21952, 108736, 108672, 104912, 21968, 109552, 109488, 104928, 21984, 110304, 110240, 104944, 22000,
  98720, 98784, 104960, 22016, 99504, 99568, 104976, 22032, 100224, 100288, 104992, 22048, 101008, 101072, 105008, 22064,
  101856, 101792, 105024, 22080, 102640, 102576, 105040, 22096, 103360, 103296, 105056, 22112, 104144, 104080, 105072, 22128,
  104864, 104928, 105088, 22144, 105648, 105712, 105104, 22160, 106368, 106432, 105120, 22176, 107152, 107216, 105136, 22192,
  108000, 107936, 105152, 22208, 108784, 108720, 105168, 22224, 109504, 109440, 105184, 22240, 110288, 110224, 105200, 22256,
  98736, 98800, 105216, 23056, 99488, 99552, 105232, 23040, 100240, 100304, 105248, 23088, 100992, 101056, 105264, 23072,
  101872, 101808, 105280, 23120, 102624, 102560, 105296, 23104, 103376, 103312, 105312, 23152, 104128, 104064, 105328, 23136,
  104880, 104944, 105344, 23184, 105632, 105696, 105360, 23168, 106384, 106448, 105376, 23216, 107136, 107200, 105392, 23200,
  108016, 107952, 105408, 23248, 108768, 108704, 105424, 23232, 109520, 109456, 105440, 23280, 110272, 110208, 105456, 23264,
  98880, 98944, 107520, 35392, 99664, 99728, 107536, 35408, 100448, 100512, 107552, 35424, 101232, 101296, 107568, 35440,
  101888, 102080, 107584, 35328, 102672, 102864, 107600, 35344, 103456, 103648, 107616, 35360, 104240, 104432, 107632, 35376,
  105024, 105088, 107648, 35520, 105808, 105872, 107664, 35536, 106592, 106656, 107680, 35552, 107376, 107440, 107696, 35568,
  108032, 108224, 107712, 35456, 108816, 109008, 107728, 35472, 109600, 109792, 107744, 35488, 110384, 110576, 107760, 35504,
  98896, 98960, 107776, 35648, 99648, 99712, 107792, 35664, 100464, 100528, 107808, 35680, 101216, 101280, 107824, 35696,
  101904, 102096, 107840, 35584, 102656, 102848, 107856, 35600, 103472, 103664, 107872, 35616, 104224, 104416, 107888, 35632,
  105040, 105104, 107904, 35776, 105792, 105856, 107920, 35792, 106608, 106672, 107936, 35808, 107360, 107424, 107952, 35824,
  108048, 108240, 107968, 35712, 108800, 108992, 107984, 35728, 109616, 109808, 108000, 35744, 110368, 110560, 108016, 35760,
  98912, 98976, 108032, 35904, 99696, 99760, 108048, 35920, 100416, 100480, 108064, 35936, 101200, 101264, 108080, 35952,
  101920, 102112, 108096, 35840, 102704, 102896, 108112, 35856, 103424, 103616, 108128, 35872, 104208, 104400, 108144, 35888,
  105056, 105120, 108160, 36032, 105840, 105904, 108176, 36048, 106560, 106624, 108192, 36064, 107344, 107408, 108208, 36080,
  108064, 108256, 108224, 35968, 108848, 109040, 108240, 35984, 109568, 109760, 108256, 36000, 110352, 110544, 108272, 36016,
  98928, 98992, 108288, 36944, 99680, 99744, 108304, 36928, 100432, 100496, 108320, 36976, 101184, 101248, 108336, 36960,
  101936, 102128, 108352, 36880, 102688, 102880, 108368, 36864, 103440, 103632, 108384, 36912, 104192, 104384, 108400, 36896,
  105072, 105136, 108416, 37072, 105824, 105888, 108432, 37056, 106576, 106640, 108448, 37104, 107328, 107392, 108464, 37088,
  108080, 108272, 108480, 37008, 108832, 109024, 108496, 36992, 109584, 109776, 108512, 37040, 110336, 110528, 108528, 37024,
  23312, 32800, 6208, 35520, 23296, 32816, 6224, 35536, 23344, 32768, 6240, 35552, 23328, 32784, 6256, 35568,
  23376, 32864, 6144, 35456, 23360, 32880, 6160, 35472, 23408, 32832, 6176, 35488, 23392, 32848, 6192, 35504,
  23440, 32928, 6336, 35392, 23424, 32944, 6352, 35408, 23472, 32896, 6368, 35424, 23456, 32912, 6384, 35440,
  23504, 32992, 6272, 35328, 23488, 33008, 6288, 35344, 23536, 32960, 6304, 35360, 23520, 32976, 6320, 35376,
  23568, 33840, 6976, 36288, 23552, 33824, 6992, 36304, 23600, 33808, 7008, 36320, 23584, 33792, 7024, 36336,
  23632, 33904, 6912, 36224, 23616, 33888, 6928, 36240, 23664, 33872, 6944, 36256, 23648, 33856, 6960, 36272,
  23696, 33968, 7104, 36160, 23680, 33952, 7120, 36176, 23728, 33936, 7136, 36192, 23712, 33920, 7152, 36208,
  23760, 34032, 7040, 36096, 23744, 34016, 7056, 36112, 23792, 34000, 7072, 36128, 23776, 33984, 7088, 36144,
  32288, 34096, 16976, 37072, 32304, 34080, 16960, 37056, 32256, 34064, 17008, 37104, 32272, 34048, 16992, 37088,
  32352, 34160, 16912, 37008, 32368, 34144, 16896, 36992, 32320, 34128, 16944, 37040, 32336, 34112, 16928, 37024,
  32416, 34224, 17104, 36944, 32432, 34208, 17088, 36928, 32384, 34192, 17136, 36976, 32400, 34176, 17120, 36960,
  32480, 34288, 17040, 36880, 32496, 34272, 17024, 36864, 32448, 34256, 17072, 36912, 32464, 34240, 17056, 36896,
  32544, 34352, 17744, 37840, 32560, 34336, 17728, 37824, 32512, 34320, 17776, 37872, 32528, 34304, 17760, 37856,
  32608, 34416, 17680, 37776, 32624, 34400, 17664, 37760, 32576, 34384, 17712, 37808, 32592, 34368, 17696, 37792,
  32672, 34480, 17872, 37712, 32688, 34464, 17856, 37696, 32640, 34448, 17904, 37744, 32656, 34432, 17888, 37728,
  32736, 34544, 17808, 37648, 32752, 34528, 17792, 37632, 32704, 34512, 17840, 37680, 32720, 34496, 17824, 37664,
  37200, 39008, 18528, 38624, 37184, 39024, 18544, 38640, 37232, 38976, 18496, 38592, 37216, 38992, 18512, 38608,
  37136, 38944, 18464, 38560, 37120, 38960, 18480, 38576, 37168, 38912, 18432, 38528, 37152, 38928, 18448, 38544,
  37328, 39136, 18656, 38496, 37312, 39152, 18672, 38512, 37360, 39104, 18624, 38464, 37344, 39120, 18640, 38480,
  37264, 39072, 18592, 38432, 37248, 39088, 18608, 38448, 37296, 39040, 18560, 38400, 37280, 39056, 18576, 38416,
  37456, 40048, 19296, 39392, 37440, 40032, 19312, 39408, 37488, 40016, 19264, 39360, 37472, 40000, 19280, 39376,
  37392, 39984, 19232, 39328, 37376, 39968, 19248, 39344, 37424, 39952, 19200, 39296, 37408, 39936, 19216, 39312,
  37584, 40176, 19424, 39264, 37568, 40160, 19440, 39280, 37616, 40144, 19392, 39232, 37600, 40128, 19408, 39248,
  37520, 40112, 19360, 39200, 37504, 40096, 19376, 39216, 37552, 40080, 19328, 39168, 37536, 40064, 19344, 39184,
  38496, 40304, 20080, 40176, 38512, 40288, 20064, 40160, 38464, 40272, 20048, 40144, 38480, 40256, 20032, 40128,
  38432, 40240, 20016, 40112, 38448, 40224, 20000, 40096, 38400, 40208, 19984, 40080, 38416, 40192, 19968, 40064,
  38624, 40432, 20208, 40048, 38640, 40416, 20192, 40032, 38592, 40400, 20176, 40016, 38608, 40384, 20160, 40000,
  38560, 40368, 20144, 39984, 38576, 40352, 20128, 39968, 38528, 40336, 20112, 39952, 38544, 40320, 20096, 39936,
  38752, 40560, 20848, 40944, 38768, 40544, 20832, 40928, 38720, 40528, 20816, 40912, 38736, 40512, 20800, 40896,
  38688, 40496, 20784, 40880, 38704, 40480, 20768, 40864, 38656, 40464, 20752, 40848, 38672, 40448, 20736, 40832,
  38880, 40688, 20976, 40816, 38896, 40672, 20960, 40800, 38848, 40656, 20944, 40784, 38864, 40640, 20928, 40768,
  38816, 40624, 20912, 40752, 38832, 40608, 20896, 40736, 38784, 40592, 20880, 40720, 38800, 40576, 20864, 40704};

__global__ __attribute__((aligned(4096))) __launch_bounds__(256, 1) void fpn_kernel(const char* __restrict__ cf, const char* __restrict__ pc,
                                                     const char* __restrict__ w1, const char* __restrict__ w2,
                                                     char* __restrict__ ws, char* __restrict__ out) {
  unsigned lane = threadIdx.x & 63;
  unsigned wid = __builtin_amdgcn_readfirstlane(threadIdx.x >> 6);
  unsigned b = blockIdx.x;
  unsigned long long t64;
  asm volatile("s_memrealtime %0" : "={s[88:89]}"(t64), "+s"(b), "+s"(wid));
  const unsigned t_start = (unsigned)t64;
  const unsigned pr = (b >> 3) * 4 + (b & 3);
  unsigned xo[9], pg[9];
#pragma unroll
  for (int j = 0; j < 9; ++j) {
    xo[j] = ((3 * pr) >> 1) * 589824u + (pr & 1) * (147456u + (j >= 3 && j < 6 ? 294912u : 0)) + j * 49152u + (j >= 6 ? 294912u : 0);
    pg[j] = xo[j] >> 21;
  }
  int dj[9];
#pragma unroll
  for (int j = 1; j < 9; ++j) dj[j] = (int)(pg[j] - pg[j - 1]) * (j <= 4 ? -12288 : 12288);
  const int d0 = (int)(pg[4] - pg[0]) * 12288;
  dj[0] = d0 - (int)(pg[8] - pg[4]) * 12288;
  const unsigned scr = 159744 + wid * 4;
  const int sgn = (4 * wid + (lane >> 4)) < 10 ? 1 : -1;
  const unsigned mP = 73728 + wid * 1024;
  unsigned pvo = __umul24(col0(4 * wid + (lane >> 4)), 768) + ((((lane & 15) ^ (lane >> 4) ^ (4 * wid)) & 15) << 4)
                 + pr * (9 * 24576);
  int sg1;
  u32x2 cd, cr, po;
  po[0] = xo[4] + lane * 1024 + (b & 4) * 128;
  po[1] = po[0] + 8192;
  const unsigned mJ = 110592 + wid * 6144;
  const unsigned var = (b + 112) >> 8;
  u32x4 ja0, ja1, ja2, ja3, ja4, ja5;
  u32x2 ep;
  unsigned two = min((((b >> 3) - 18) * 4 + wid) * 42 + min(lane, 41u), 2303u) * 128;
  unsigned tcode = ((((b >> 3) - 18) * 4 + wid) * 11 + min(lane, 10u)) * 128;
  const unsigned jb = b < 144 ? b : 0;
  const unsigned G32 = (jb * 171) >> 11, jcb = jb - 12 * G32;
  {
    const unsigned jw = __umul24(lane >> 2, 768) + (lane & 3) * 16 + 64 * G32 + wid * (96 * 768);
    const unsigned ja = __umul24(8 * ((lane & 15) >> 2) + (lane & 3), 768) + 16 * (lane >> 4) + jcb * (32 * 768) + wid * 192;
    const unsigned cla = 1048576 + pr * 128;
    asm volatile(
      "s_nop 4\n"
      "s_cmp_lg_u32 s76, 0\n"
      "s_cbranch_scc1 LQ0\n"
      "s_mul_i32 s62, s88, 0x1080\n"
      "s_and_b32 s62, s62, 0x4180\n"
      "s_add_u32 s56, s30, s62\n"
      "s_addc_u32 s57, s31, 0\n"
      "s_cmp_lg_u32 s76, 0\n"
      "s_mov_b64 exec, 0x1\n"
      "global_load_dword v116, v118, s[56:57] nt\n"
      "global_load_dword v117, v119, s[56:57] nt\n"
      "s_mov_b64 exec, -1\n"
      "LQ0:\n"
      "v_mov_b32 v0, 0\n"
      "s_barrier\n"
      "global_load_dwordx2 v[252:253], v0, s[28:29] sc1\n"
      "s_cbranch_scc1 LQ1\n"
      ".p2align 5\n"
      "s_waitcnt vmcnt(2)\n"
      "s_memtime s[60:61]\n"
      "s_waitcnt vmcnt(1)\n"
      "s_memtime s[62:63]\n"
      "s_waitcnt vmcnt(0) lgkmcnt(0)\n"
      "s_sub_u32 s62, s62, s60\n"
      "s_bfe_u32 s63, s51, 0x10002\n"
      "s_cmp_lt_u32 s62, 30\n"
      "s_cselect_b32 s62, 1, 0\n"
      "v_mov_b32 v116, s62\n"
      "ds_write_b32 v108, v116 offset:16\n"
      "s_xor_b32 s62, s62, s63\n"
      "v_add_co_u32 v110, vcc, 0x7f4a7c15, v252\n"
      "v_mov_b32 v111, 0x9e3779b9\n"
      "v_addc_co_u32 v111, vcc, v111, v253, vcc\n"
      "v_and_or_b32 v110, v110, -2, s62\n"
      "s_mov_b64 exec, 1\n"
      "global_atomic_swap_x2 v[112:113], v114, v[110:111], s[28:29] sc0 sc1\n"
      "s_mov_b64 exec, -1\n"
      "LQ1:\n"
      "s_cmp_lg_u32 s50, 0\n"
      "s_cbranch_scc0 LU1\n"
      "s_getpc_b64 s[56:57]\n"
      "LC0_1:\n"
      "v_min_u32 v106, LCEND-LC0_1-4, v106\n"
      "global_load_dword v106, v106, s[56:57]\n"
      "global_load_dword v105, v105, s[26:27]\n"
      "v_and_b32 v117, -16, v108\n"
      "s_waitcnt lgkmcnt(0)\n"
      "s_barrier\n"
      "ds_read_b32 v116, v117 offset:16\n"
      "s_waitcnt lgkmcnt(0)\n"
      "v_readfirstlane_b32 s62, v116\n"
      "s_nop 3\n"
      "s_mul_i32 s63, s62, 0x3000\n"
      "s_sub_u32 s82, s98, s63\n"
      "s_lshl_b32 s62, s62, 1\n"
      "s_sub_u32 s62, 1, s62\n"
      "v_mul_i32_i24 v107, s62, v251\n"
      "v_mad_i32_i24 v238, v107, s82, v238\n"
      "s_add_u32 s56, s22, 0x0\n"
      "s_addc_u32 s57, s23, 0\n"
      "s_add_u32 m0, s18, 0x0\n"
      "s_nop 0\n"
      "global_load_lds_dwordx4 v238, s[56:57] offset:0 \n"
      "v_mad_i32_i24 v238, v107, s90, v238\n"
      "s_add_u32 s56, s22, 0x6000\n"
      "s_addc_u32 s57, s23, 0\n"
      "s_add_u32 m0, s18, 0x1000\n"
      "s_nop 0\n"
      "global_load_lds_dwordx4 v238, s[56:57] offset:0 \n"
      "v_mad_i32_i24 v238, v107, s91, v238\n"
      "s_add_u32 s56, s22, 0xc000\n"
      "s_addc_u32 s57, s23, 0\n"
      "s_add_u32 m0, s18, 0x2000\n"
      "s_nop 0\n"
      "global_load_lds_dwordx4 v238, s[56:57] offset:0 \n"
      "v_mad_i32_i24 v238, v107, s92, v238\n"
      "s_add_u32 s56, s22, 0x12000\n"
      "s_addc_u32 s57, s23, 0\n"
      "s_add_u32 m0, s18, 0x3000\n"
      "s_nop 0\n"
      "global_load_lds_dwordx4 v238, s[56:57] offset:0 \n"
      "v_mad_i32_i24 v238, v107, s93, v238\n"
      "s_add_u32 s56, s22, 0x18000\n"
      "s_addc_u32 s57, s23, 0\n"
      "s_add_u32 m0, s18, 0x4000\n"
      "s_nop 0\n"
      "global_load_lds_dwordx4 v238, s[56:57] offset:0 \n"
      "v_mad_i32_i24 v238, v107, s94, v238\n"
      "s_add_u32 s56, s22, 0x1e000\n"
      "s_addc_u32 s57, s23, 0\n"
      "s_add_u32 m0, s18, 0x5000\n"
      "s_nop 0\n"
      "global_load_lds_dwordx4 v238, s[56:57] offset:0 \n"
      "v_mad_i32_i24 v238, v107, s95, v238\n"
      "s_add_u32 s56, s22, 0x24000\n"
      "s_addc_u32 s57, s23, 0\n"
      "s_add_u32 m0, s18, 0x6000\n"
      "s_nop 0\n"
      "global_load_lds_dwordx4 v238, s[56:57] offset:0 \n"
      "v_mad_i32_i24 v238, v107, s96, v238\n"
      "s_add_u32 s56, s22, 0x2a000\n"
      "s_addc_u32 s57, s23, 0\n"
      "s_add_u32 m0, s18, 0x7000\n"
      "s_nop 0\n"
      "global_load_lds_dwordx4 v238, s[56:57] offset:0 \n"
      "v_mad_i32_i24 v238, v107, s97, v238\n"
      "s_add_u32 s56, s22, 0x30000\n"
      "s_addc_u32 s57, s23, 0\n"
      "s_add_u32 m0, s18, 0x8000\n"
      "s_nop 0\n"
      "global_load_lds_dwordx4 v238, s[56:57] offset:0 \n"
      "s_branch LUE\n"
      "LU1:\n"
      "s_add_u32 s56, s26, 0x0\n"
      "s_addc_u32 s57, s27, 0\n"
      "s_add_u32 m0, s75, 0x0\n"
      "s_nop 0\n"
      "global_load_lds_dwordx4 v100, s[56:57]\n"
      "s_add_u32 s56, s26, 0x3000\n"
      "s_addc_u32 s57, s27, 0\n"
      "s_add_u32 m0, s75, 0x400\n"
      "s_nop 0\n"
      "global_load_lds_dwordx4 v100, s[56:57]\n"
      "s_add_u32 s56, s26, 0x6000\n"
      "s_addc_u32 s57, s27, 0\n"
      "s_add_u32 m0, s75, 0x800\n"
      "s_nop 0\n"
      "global_load_lds_dwordx4 v100, s[56:57]\n"
      "s_add_u32 s56, s26, 0x9000\n"
      "s_addc_u32 s57, s27, 0\n"
      "s_add_u32 m0, s75, 0xc00\n"
      "s_nop 0\n"
      "global_load_lds_dwordx4 v100, s[56:57]\n"
      "s_add_u32 s56, s26, 0xc000\n"
      "s_addc_u32 s57, s27, 0\n"
      "s_add_u32 m0, s75, 0x1000\n"
      "s_nop 0\n"
      "global_load_lds_dwordx4 v100, s[56:57]\n"
      "s_add_u32 s56, s26, 0xf000\n"
      "s_addc_u32 s57, s27, 0\n"
      "s_add_u32 m0, s75, 0x1400\n"
      "s_nop 0\n"
      "global_load_lds_dwordx4 v100, s[56:57]\n"
      "global_load_dwordx4 v[4:7], v101, s[24:25] offset:0\n"
      "global_load_dwordx4 v[8:11], v101, s[24:25] offset:3072\n"
      "global_load_dwordx4 v[12:15], v101, s[24:25] offset:64\n"
      "global_load_dwordx4 v[16:19], v101, s[24:25] offset:3136\n"
      "global_load_dwordx4 v[20:23], v101, s[24:25] offset:128\n"
      "global_load_dwordx4 v[24:27], v101, s[24:25] offset:3200\n"
      "LUE:\n"

      : "={v[4:7]}"(ja0), "={v[8:11]}"(ja1), "={v[12:15]}"(ja2), "={v[16:19]}"(ja3), "={v[20:23]}"(ja4), "={v[24:27]}"(ja5),
        "={v[252:253]}"(ep), "+{v105}"(two), "+{v106}"(tcode), "={v[110:111]}"(cd), "={v[112:113]}"(cr), "+{v[118:119]}"(po),
        "+{v238}"(pvo), "={v107}"(sg1)
      : "{v114}"(cla), "{s[88:89]}"(t64), "{v100}"(jw), "{v101}"(ja), "{s[20:21]}"(cf), "{s[24:25]}"(w1), "{s[26:27]}"(w2), "{s[28:29]}"(ws), "{s[30:31]}"(out),
        "{s75}"(mJ), "{s51}"(b), "{s50}"(var), "{s76}"(wid), "{s[22:23]}"(pc), "{s18}"(mP), "{v108}"(scr), "{v251}"(sgn),
        "{s90}"(dj[1]), "{s91}"(dj[2]), "{s92}"(dj[3]), "{s93}"(dj[4]), "{s94}"(dj[5]), "{s95}"(dj[6]), "{s96}"(dj[7]), "{s97}"(dj[8]), "{s98}"(d0)
      : "memory", "scc", "vcc", "v0", "v116", "v117", "s56", "s57", "s60", "s61", "s62", "s63", "s82");
  }
  asm volatile("" : "+v"(lane));
  asm volatile("" : "+s"(wid));
  unsigned wro[3];
#pragma unroll
  for (int i = 0; i < 3; ++i) { const unsigned x = lane + 64 * i, q = __umul24(x, 171) >> 11; wro[i] = __umul24(q, 576) + x * 16; }
  const unsigned mW = wid * 6144;
  const char* w2w = w2 + wid * 192;
  const unsigned lo16 = lane * 16;
  const char* wb = ws + (65536 + wid * 73728);
  const unsigned c = lane & 15, g = lane >> 4;
  const unsigned cB0 = 73728 + c * 256 + ((g ^ c) << 4);
  const unsigned r8 = 8 * g + (c >> 2), c3 = (c & 3) * 16;
  const unsigned tra = mW + __umul24(r8, 192) + c3;
  const unsigned mX = wid * 1024;
  const unsigned fla = 4096 + wid * 288 + min(lo16, 272u);
  const unsigned c256 = c * 256 - 73728;
  const unsigned jfl = (b < 144 ? 4096 : 8192) + 8 * b;
  char* jo = ws + (65536 + (24 * G32 + jcb) * 1024);
  const unsigned jt = mJ + r8 * 64 + c3;
  const unsigned jrr = 135168 + lo16;
  const unsigned jrw = jrr + wid * 4096;
  const unsigned pk = 151552 + mX + lo16;
  const char* tab = (const char*)lane_tab + ((wid >> 1) * 4096 + (wid & 1) * 1024);
  asm volatile(
      "s_nop 4\n"
      "LBODY:\n"
      "s_branch LB0\n"
      "LRET:\n"
      "s_setpc_b64 s[56:57]\n"
      "LB0:\n"
      "s_cmp_lg_u32 s50, 0\n"
      "s_cbranch_scc1 LV1\n"
      "s_getpc_b64 s[56:57]\n"
      "LTR_0:\n"
      "s_add_u32 s56, s56, LTE_0-LTR_0\n"
      "s_addc_u32 s57, s57, 0\n"
      "s_mov_b64 exec, 0\n"
      "s_cmp_eq_u32 s76, 3\n"
      "s_cbranch_scc1 LPD2\n"
      "s_cmp_eq_u32 s76, 2\n"
      "s_cbranch_scc1 LPD1\n"
      "s_cmp_eq_u32 s76, 1\n"
      "s_cbranch_scc1 LPD0\n"
      "LTE_0:\n"
      "s_mov_b64 exec, -1\n"
      "s_waitcnt vmcnt(12)\n"
      "v_readfirstlane_b32 s80, v252\n"
      "v_readfirstlane_b32 s81, v253\n"
      "s_nop 3\n"
      "s_add_u32 s80, s80, 0x7f4a7c15\n"
      "s_addc_u32 s81, s81, 0x9e3779b9\n"
      "v_mov_b32 v252, s80\n"
      "v_mov_b32 v253, s81\n"
      "v_xor_b32 v116, v112, v110\n"
      "v_xor_b32 v117, v113, v111\n"
      "v_and_or_b32 v116, v116, -2, v117\n"
      "v_cmp_eq_u32 vcc, 0, v116\n"
      "v_cndmask_b32 v116, v110, v112, vcc\n"
      "s_mov_b64 exec, 1\n"
      "ds_write_b32 v108, v116\n"
      "s_mov_b64 exec, -1\n"
      "v_and_b32 v108, -16, v108\n"
      "s_waitcnt lgkmcnt(0)\n"
      "s_barrier\n"
      "ds_read_b32 v116, v108\n"
      "s_waitcnt lgkmcnt(0)\n"
      "v_readfirstlane_b32 s62, v116\n"
      "s_lshr_b32 s63, s51, 2\n"
      "s_nop 2\n"
      "s_xor_b32 s62, s62, s63\n"
      "s_and_b32 s62, s62, 1\n"
      "s_mul_i32 s63, s62, 0x3000\n"
      "s_sub_u32 s98, s98, s63\n"
      "s_lshl_b32 s62, s62, 1\n"
      "s_sub_u32 s62, 1, s62\n"
      "v_mul_i32_i24 v251, s62, v251\n"
      "s_lshl_b32 s19, s62, 1\n"
      "s_mul_i32 s100, s100, s62\n"
      "v_mad_i32_i24 v238, v251, s98, v238\n"
      "s_add_u32 s56, s22, 0x0\n"
      "s_addc_u32 s57, s23, 0\n"
      "s_add_u32 m0, s18, 0x0\n"
      "s_nop 0\n"
      "global_load_lds_dwordx4 v238, s[56:57] offset:0 \n"
      "v_mad_i32_i24 v238, v251, s90, v238\n"
      "s_add_u32 s56, s22, 0x6000\n"
      "s_addc_u32 s57, s23, 0\n"
      "s_add_u32 m0, s18, 0x1000\n"
      "s_nop 0\n"
      "global_load_lds_dwordx4 v238, s[56:57] offset:0 \n"
      "v_mad_i32_i24 v238, v251, s91, v238\n"
      "s_add_u32 s56, s22, 0xc000\n"
      "s_addc_u32 s57, s23, 0\n"
      "s_add_u32 m0, s18, 0x2000\n"
      "s_nop 0\n"
      "global_load_lds_dwordx4 v238, s[56:57] offset:0 \n"
      "v_mad_i32_i24 v238, v251, s92, v238\n"
      "s_add_u32 s56, s22, 0x12000\n"
      "s_addc_u32 s57, s23, 0\n"
      "s_add_u32 m0, s18, 0x3000\n"
      "s_nop 0\n"
      "global_load_lds_dwordx4 v238, s[56:57] offset:0 \n"
      "v_mad_i32_i24 v238, v251, s93, v238\n"
      "s_add_u32 s56, s22, 0x18000\n"
      "s_addc_u32 s57, s23, 0\n"
      "s_add_u32 m0, s18, 0x4000\n"
      "s_nop 0\n"
      "global_load_lds_dwordx4 v238, s[56:57] offset:0 \n"
      "v_mad_i32_i24 v238, v251, s94, v238\n"
      "s_add_u32 s56, s22, 0x1e000\n"
      "s_addc_u32 s57, s23, 0\n"
      "s_add_u32 m0, s18, 0x5000\n"
      "s_nop 0\n"
      "global_load_lds_dwordx4 v238, s[56:57] offset:0 \n"
      "v_mad_i32_i24 v238, v251, s95, v238\n"
      "s_add_u32 s56, s22, 0x24000\n"
      "s_addc_u32 s57, s23, 0\n"
      "s_add_u32 m0, s18, 0x6000\n"
      "s_nop 0\n"
      "global_load_lds_dwordx4 v238, s[56:57] offset:0 \n"
      "v_mad_i32_i24 v238, v251, s96, v238\n"
      "s_add_u32 s56, s22, 0x2a000\n"
      "s_addc_u32 s57, s23, 0\n"
      "s_add_u32 m0, s18, 0x7000\n"
      "s_nop 0\n"
      "global_load_lds_dwordx4 v238, s[56:57] offset:0 \n"
      "v_mad_i32_i24 v238, v251, s97, v238\n"
      "s_add_u32 s56, s22, 0x30000\n"
      "s_addc_u32 s57, s23, 0\n"
      "s_add_u32 m0, s18, 0x8000\n"
      "s_nop 0\n"
      "global_load_lds_dwordx4 v238, s[56:57] offset:0 \n"
      "s_waitcnt vmcnt(9)\n"
      "ds_read_b64_tr_b16 v[28:29], v102 offset:0\n"
      "ds_read_b64_tr_b16 v[30:31], v102 offset:256\n"
      "ds_read_b64_tr_b16 v[32:33], v102 offset:8\n"
      "ds_read_b64_tr_b16 v[34:35], v102 offset:264\n"
      "ds_read_b64_tr_b16 v[36:37], v102 offset:2048\n"
      "ds_read_b64_tr_b16 v[38:39], v102 offset:2304\n"
      "ds_read_b64_tr_b16 v[40:41], v102 offset:2056\n"
      "ds_read_b64_tr_b16 v[42:43], v102 offset:2312\n"
      "ds_read_b64_tr_b16 v[44:45], v102 offset:4096\n"
      "ds_read_b64_tr_b16 v[46:47], v102 offset:4352\n"
      "ds_read_b64_tr_b16 v[48:49], v102 offset:4104\n"
      "ds_read_b64_tr_b16 v[50:51], v102 offset:4360\n"
      "s_waitcnt lgkmcnt(0)\n"
      "v_mfma_f32_16x16x32_bf16 v[52:55], v[4:7], v[28:31], 0\n"
      "v_mfma_f32_16x16x32_bf16 v[56:59], v[8:11], v[28:31], 0\n"
      "v_mfma_f32_16x16x32_bf16 v[60:63], v[4:7], v[32:35], 0\n"
      "v_mfma_f32_16x16x32_bf16 v[64:67], v[8:11], v[32:35], 0\n"
      "v_mfma_f32_16x16x32_bf16 v[52:55], v[12:15], v[36:39], v[52:55]\n"
      "v_mfma_f32_16x16x32_bf16 v[56:59], v[16:19], v[36:39], v[56:59]\n"
      "v_mfma_f32_16x16x32_bf16 v[60:63], v[12:15], v[40:43], v[60:63]\n"
      "v_mfma_f32_16x16x32_bf16 v[64:67], v[16:19], v[40:43], v[64:67]\n"
      "v_mfma_f32_16x16x32_bf16 v[52:55], v[20:23], v[44:47], v[52:55]\n"
      "v_mfma_f32_16x16x32_bf16 v[56:59], v[24:27], v[44:47], v[56:59]\n"
      "v_mfma_f32_16x16x32_bf16 v[60:63], v[20:23], v[48:51], v[60:63]\n"
      "v_mfma_f32_16x16x32_bf16 v[64:67], v[24:27], v[48:51], v[64:67]\n"
      "s_nop 7\n"
      "ds_write_b128 v103, v[52:55] offset:0\n"
      "ds_write_b128 v103, v[56:59] offset:1024\n"
      "ds_write_b128 v103, v[60:63] offset:2048\n"
      "ds_write_b128 v103, v[64:67] offset:3072\n"
      "s_waitcnt lgkmcnt(0)\n"
      "s_barrier\n"
      "s_cmp_lg_u32 s76, 1\n"
      "s_cbranch_scc1 LJ1\n"
      "ds_read_b128 v[4:7], v104 offset:0\n"
      "ds_read_b128 v[8:11], v104 offset:1024\n"
      "ds_read_b128 v[12:15], v104 offset:2048\n"
      "ds_read_b128 v[16:19], v104 offset:3072\n"
      "ds_read_b128 v[20:23], v104 offset:4096\n"
      "ds_read_b128 v[24:27], v104 offset:5120\n"
      "ds_read_b128 v[28:31], v104 offset:6144\n"
      "ds_read_b128 v[32:35], v104 offset:7168\n"
      "ds_read_b128 v[36:39], v104 offset:8192\n"
      "ds_read_b128 v[40:43], v104 offset:9216\n"
      "ds_read_b128 v[44:47], v104 offset:10240\n"
      "ds_read_b128 v[48:51], v104 offset:11264\n"
      "ds_read_b128 v[52:55], v104 offset:12288\n"
      "ds_read_b128 v[56:59], v104 offset:13312\n"
      "ds_read_b128 v[60:63], v104 offset:14336\n"
      "ds_read_b128 v[64:67], v104 offset:15360\n"
      "s_waitcnt lgkmcnt(8)\n"
      "v_add_f32 v4, v4, v20\n"
      "v_add_f32 v5, v5, v21\n"
      "v_add_f32 v6, v6, v22\n"
      "v_add_f32 v7, v7, v23\n"
      "v_add_f32 v8, v8, v24\n"
      "v_add_f32 v9, v9, v25\n"
      "v_add_f32 v10, v10, v26\n"
      "v_add_f32 v11, v11, v27\n"
      "v_add_f32 v12, v12, v28\n"
      "v_add_f32 v13, v13, v29\n"
      "v_add_f32 v14, v14, v30\n"
      "v_add_f32 v15, v15, v31\n"
      "v_add_f32 v16, v16, v32\n"
      "v_add_f32 v17, v17, v33\n"
      "v_add_f32 v18, v18, v34\n"
      "v_add_f32 v19, v19, v35\n"
      "s_waitcnt lgkmcnt(4)\n"
      "v_add_f32 v4, v4, v36\n"
      "v_add_f32 v5, v5, v37\n"
      "v_add_f32 v6, v6, v38\n"
      "v_add_f32 v7, v7, v39\n"
      "v_add_f32 v8, v8, v40\n"
      "v_add_f32 v9, v9, v41\n"
      "v_add_f32 v10, v10, v42\n"
      "v_add_f32 v11, v11, v43\n"
      "v_add_f32 v12, v12, v44\n"
      "v_add_f32 v13, v13, v45\n"
      "v_add_f32 v14, v14, v46\n"
      "v_add_f32 v15, v15, v47\n"
      "v_add_f32 v16, v16, v48\n"
      "v_add_f32 v17, v17, v49\n"
      "v_add_f32 v18, v18, v50\n"
      "v_add_f32 v19, v19, v51\n"
      "s_waitcnt lgkmcnt(0)\n"
      "v_add_f32 v4, v4, v52\n"
      "v_add_f32 v5, v5, v53\n"
      "v_add_f32 v6, v6, v54\n"
      "v_add_f32 v7, v7, v55\n"
      "v_add_f32 v8, v8, v56\n"
      "v_add_f32 v9, v9, v57\n"
      "v_add_f32 v10, v10, v58\n"
      "v_add_f32 v11, v11, v59\n"
      "v_add_f32 v12, v12, v60\n"
      "v_add_f32 v13, v13, v61\n"
      "v_add_f32 v14, v14, v62\n"
      "v_add_f32 v15, v15, v63\n"
      "v_add_f32 v16, v16, v64\n"
      "v_add_f32 v17, v17, v65\n"
      "v_add_f32 v18, v18, v66\n"
      "v_add_f32 v19, v19, v67\n"
      "v_cvt_pk_bf16_f32 v68, v4, v5\n"
      "v_cvt_pk_bf16_f32 v69, v6, v7\n"
      "v_cvt_pk_bf16_f32 v70, v8, v9\n"
      "v_cvt_pk_bf16_f32 v71, v10, v11\n"
      "v_cvt_pk_bf16_f32 v72, v12, v13\n"
      "v_cvt_pk_bf16_f32 v73, v14, v15\n"
      "v_cvt_pk_bf16_f32 v74, v16, v17\n"
      "v_cvt_pk_bf16_f32 v75, v18, v19\n"
      "global_store_dwordx4 v239, v[68:71], s[72:73] sc1\n"
      "s_add_u32 s72, s72, 0x3000\n"
      "s_addc_u32 s73, s73, 0\n"
      "global_store_dwordx4 v239, v[72:75], s[72:73] sc1\n"
      "LJ1:\n"
      "s_add_u32 s78, s64, 0x0\n"
      "s_addc_u32 s79, s65, 0\n"
      "s_add_u32 m0, s74, 0x0\n"
      "s_nop 0\n"
      "global_load_lds_dwordx4 v240, s[78:79]\n"
      "s_add_u32 m0, s74, 0x400\n"
      "s_nop 0\n"
      "global_load_lds_dwordx4 v241, s[78:79]\n"
      "s_add_u32 m0, s74, 0x800\n"
      "s_nop 0\n"
      "global_load_lds_dwordx4 v242, s[78:79]\n"
      "s_add_u32 s78, s64, 0x3000\n"
      "s_addc_u32 s79, s65, 0\n"
      "s_add_u32 m0, s74, 0xc00\n"
      "s_nop 0\n"
      "global_load_lds_dwordx4 v240, s[78:79]\n"
      "s_add_u32 m0, s74, 0x1000\n"
      "s_nop 0\n"
      "global_load_lds_dwordx4 v241, s[78:79]\n"
      "s_add_u32 m0, s74, 0x1400\n"
      "s_nop 0\n"
      "global_load_lds_dwordx4 v242, s[78:79]\n"
      "s_add_u32 s78, s64, 0x6000\n"
      "s_addc_u32 s79, s65, 0\n"
      "s_add_u32 m0, s74, 0x6000\n"
      "s_nop 0\n"
      "global_load_lds_dwordx4 v240, s[78:79]\n"
      "s_add_u32 m0, s74, 0x6400\n"
      "s_nop 0\n"
      "global_load_lds_dwordx4 v241, s[78:79]\n"
      "s_add_u32 m0, s74, 0x6800\n"
      "s_nop 0\n"
      "global_load_lds_dwordx4 v242, s[78:79]\n"
      "s_add_u32 s78, s64, 0x9000\n"
      "s_addc_u32 s79, s65, 0\n"
      "s_add_u32 m0, s74, 0x6c00\n"
      "s_nop 0\n"
      "global_load_lds_dwordx4 v240, s[78:79]\n"
      "s_add_u32 m0, s74, 0x7000\n"
      "s_nop 0\n"
      "global_load_lds_dwordx4 v241, s[78:79]\n"
      "s_add_u32 m0, s74, 0x7400\n"
      "s_nop 0\n"
      "global_load_lds_dwordx4 v242, s[78:79]\n"
      "s_add_u32 s78, s64, 0xc000\n"
      "s_addc_u32 s79, s65, 0\n"
      "s_add_u32 m0, s74, 0xc000\n"
      "s_nop 0\n"
      "global_load_lds_dwordx4 v240, s[78:79]\n"
      "s_add_u32 m0, s74, 0xc400\n"
      "s_nop 0\n"
      "global_load_lds_dwordx4 v241, s[78:79]\n"
      "s_add_u32 m0, s74, 0xc800\n"
      "s_nop 0\n"
      "global_load_lds_dwordx4 v242, s[78:79]\n"
      "s_add_u32 s78, s64, 0xf000\n"
      "s_addc_u32 s79, s65, 0\n"
      "s_add_u32 m0, s74, 0xcc00\n"
      "s_nop 0\n"
      "global_load_lds_dwordx4 v240, s[78:79]\n"
      "s_add_u32 m0, s74, 0xd000\n"
      "s_nop 0\n"
      "global_load_lds_dwordx4 v241, s[78:79]\n"
      "s_add_u32 m0, s74, 0xd400\n"
      "s_nop 0\n"
      "global_load_lds_dwordx4 v242, s[78:79]\n"
      "s_add_u32 m0, s17, 0x25000\n"
      "s_nop 0\n"
      "global_load_lds_dwordx4 v239, s[46:47] offset:0\n"
      "s_add_u32 m0, s17, 0x25800\n"
      "s_nop 0\n"
      "global_load_lds_dwordx4 v239, s[46:47] offset:2048\n"
      "s_waitcnt vmcnt(14)\n"
      "ds_read_b64_tr_b16 a[0:1], v243 offset:0\n"
      "ds_read_b64_tr_b16 a[2:3], v243 offset:768\n"
      "ds_read_b64_tr_b16 a[4:5], v243 offset:8\n"
      "ds_read_b64_tr_b16 a[6:7], v243 offset:776\n"
      "ds_read_b64_tr_b16 a[8:9], v243 offset:64\n"
      "ds_read_b64_tr_b16 a[10:11], v243 offset:832\n"
      "ds_read_b64_tr_b16 a[12:13], v243 offset:72\n"
      "ds_read_b64_tr_b16 a[14:15], v243 offset:840\n"
      "ds_read_b64_tr_b16 a[16:17], v243 offset:128\n"
      "ds_read_b64_tr_b16 a[18:19], v243 offset:896\n"
      "ds_read_b64_tr_b16 a[20:21], v243 offset:136\n"
      "ds_read_b64_tr_b16 a[22:23], v243 offset:904\n"
      "s_barrier\n"
      "s_cmp_lg_u32 s76, 1\n"
      "s_cbranch_scc1 LA_0\n"
      "v_mov_b32 v244, s77\n"
      "s_mov_b64 exec, 1\n"
      "global_store_dwordx2 v244, v[252:253], s[28:29] sc1\n"
      "s_mov_b64 exec, -1\n"
      "s_nop 4\n"
      "LA_0:\n"
      "ds_read_b128 v[216:219], v232 offset:0\n"
      "ds_read_b128 v[220:223], v232 offset:4096\n"
      "ds_read_b128 v[224:227], v232 offset:8192\n"
      "s_waitcnt lgkmcnt(2)\n"
      "v_mfma_f32_16x16x32_bf16 v[0:3], a[0:3], v[216:219], 0\n"
      "s_add_u32 s78, s64, 0x12000\n"
      "s_addc_u32 s79, s65, 0\n"
      "s_add_u32 m0, s74, 0x0\n"
      "v_mfma_f32_16x16x32_bf16 v[4:7], a[4:7], v[216:219], 0\n"
      "global_load_lds_dwordx4 v240, s[78:79]\n"
      "v_mfma_f32_16x16x32_bf16 v[8:11], a[8:11], v[216:219], 0\n"
      "ds_read_b128 v[228:231], v232 offset:12288\n"
      "v_mfma_f32_16x16x32_bf16 v[12:15], a[12:15], v[216:219], 0\n"
      "v_mfma_f32_16x16x32_bf16 v[16:19], a[16:19], v[216:219], 0\n"
      "s_add_u32 m0, s74, 0x400\n"
      "v_mfma_f32_16x16x32_bf16 v[20:23], a[20:23], v[216:219], 0\n"
      "global_load_lds_dwordx4 v241, s[78:79]\n"
      "s_waitcnt lgkmcnt(2)\n"
      "v_mfma_f32_16x16x32_bf16 v[24:27], a[0:3], v[220:223], 0\n"
      "s_waitcnt vmcnt(10)\n"
      "v_mfma_f32_16x16x32_bf16 v[28:31], a[4:7], v[220:223], 0\n"
      "ds_read_b64_tr_b16 a[24:25], v243 offset:24576\n"
      "v_mfma_f32_16x16x32_bf16 v[32:35], a[8:11], v[220:223], 0\n"
      "ds_read_b128 v[216:219], v232 offset:16384\n"
      "s_add_u32 m0, s74, 0x800\n"
      "v_mfma_f32_16x16x32_bf16 v[36:39], a[12:15], v[220:223], 0\n"
      "ds_read_b64_tr_b16 a[26:27], v243 offset:25344\n"
      "global_load_lds_dwordx4 v242, s[78:79]\n"
      "v_mfma_f32_16x16x32_bf16 v[40:43], a[16:19], v[220:223], 0\n"
      "LPD0:\n"
      "s_cbranch_execz LPD3\n"
      "ds_read_b64_tr_b16 a[28:29], v243 offset:24584\n"
      "v_mfma_f32_16x16x32_bf16 v[44:47], a[20:23], v[220:223], 0\n"
      "s_waitcnt lgkmcnt(5)\n"
      "v_mfma_f32_16x16x32_bf16 v[48:51], a[0:3], v[224:227], 0\n"
      "ds_read_b64_tr_b16 a[30:31], v243 offset:25352\n"
      "s_add_u32 s78, s64, 0x15000\n"
      "s_addc_u32 s79, s65, 0\n"
      "s_add_u32 m0, s74, 0xc00\n"
      "v_mfma_f32_16x16x32_bf16 v[52:55], a[4:7], v[224:227], 0\n"
      "ds_read_b64_tr_b16 a[32:33], v243 offset:24640\n"
      "global_load_lds_dwordx4 v240, s[78:79]\n"
      "v_mfma_f32_16x16x32_bf16 v[56:59], a[8:11], v[224:227], 0\n"
      "ds_read_b128 v[220:223], v232 offset:20480\n"
      "v_mfma_f32_16x16x32_bf16 v[60:63], a[12:15], v[224:227], 0\n"
      "ds_read_b64_tr_b16 a[34:35], v243 offset:25408\n"
      "v_mfma_f32_16x16x32_bf16 v[64:67], a[16:19], v[224:227], 0\n"
      "ds_read_b64_tr_b16 a[36:37], v243 offset:24648\n"
      "s_add_u32 m0, s74, 0x1000\n"
      "v_mfma_f32_16x16x32_bf16 v[68:71], a[20:23], v[224:227], 0\n"
      "global_load_lds_dwordx4 v241, s[78:79]\n"
      "s_waitcnt lgkmcnt(9)\n"
      "v_mfma_f32_16x16x32_bf16 v[72:75], a[0:3], v[228:231], 0\n"
      "ds_read_b64_tr_b16 a[38:39], v243 offset:25416\n"
      "v_mfma_f32_16x16x32_bf16 v[76:79], a[4:7], v[228:231], 0\n"
      "ds_read_b64_tr_b16 a[40:41], v243 offset:24704\n"
      "v_mfma_f32_16x16x32_bf16 v[80:83], a[8:11], v[228:231], 0\n"
      "ds_read_b128 v[224:227], v232 offset:24576\n"
      "s_add_u32 m0, s74, 0x1400\n"
      "v_mfma_f32_16x16x32_bf16 v[84:87], a[12:15], v[228:231], 0\n"
      "ds_read_b64_tr_b16 a[42:43], v243 offset:25472\n"
      "global_load_lds_dwordx4 v242, s[78:79]\n"
      "v_mfma_f32_16x16x32_bf16 v[88:91], a[16:19], v[228:231], 0\n"
      "ds_read_b64_tr_b16 a[44:45], v243 offset:24712\n"
      "v_mfma_f32_16x16x32_bf16 v[92:95], a[20:23], v[228:231], 0\n"
      "s_waitcnt lgkmcnt(12)\n"
      "v_mfma_f32_16x16x32_bf16 v[96:99], a[0:3], v[216:219], 0\n"
      "ds_read_b64_tr_b16 a[46:47], v243 offset:25480\n"
      "v_mfma_f32_16x16x32_bf16 v[100:103], a[4:7], v[216:219], 0\n"
      "s_waitcnt vmcnt(8)\n"
      "v_mfma_f32_16x16x32_bf16 v[104:107], a[8:11], v[216:219], 0\n"
      "ds_read_b128 v[228:231], v232 offset:28672\n"
      "v_mfma_f32_16x16x32_bf16 v[108:111], a[12:15], v[216:219], 0\n"
      "ds_read_b64_tr_b16 a[48:49], v243 offset:49152\n"
      "v_mfma_f32_16x16x32_bf16 v[112:115], a[16:19], v[216:219], 0\n"
      "ds_read_b64_tr_b16 a[50:51], v243 offset:49920\n"
      "v_mfma_f32_16x16x32_bf16 v[116:119], a[20:23], v[216:219], 0\n"
      "s_waitcnt lgkmcnt(11)\n"
      "v_mfma_f32_16x16x32_bf16 v[120:123], a[0:3], v[220:223], 0\n"
      "ds_read_b64_tr_b16 a[52:53], v243 offset:49160\n"
      "v_mfma_f32_16x16x32_bf16 v[124:127], a[4:7], v[220:223], 0\n"
      "ds_read_b64_tr_b16 a[54:55], v243 offset:49928\n"
      "v_mfma_f32_16x16x32_bf16 v[128:131], a[8:11], v[220:223], 0\n"
      "ds_read_b128 v[216:219], v232 offset:32768\n"
      "v_add_u32 v232, 0x9000, v232\n"
      "v_mfma_f32_16x16x32_bf16 v[132:135], a[12:15], v[220:223], 0\n"
      "ds_read_b64_tr_b16 a[56:57], v243 offset:49216\n"
      "v_mfma_f32_16x16x32_bf16 v[136:139], a[16:19], v[220:223], 0\n"
      "ds_read_b64_tr_b16 a[58:59], v243 offset:49984\n"
      "s_waitcnt lgkmcnt(8)\n"
      "s_add_u32 s78, s64, 0x18000\n"
      "s_addc_u32 s79, s65, 0\n"
      "s_add_u32 m0, s74, 0x6000\n"
      "v_mfma_f32_16x16x32_bf16 v[140:143], a[20:23], v[220:223], 0\n"
      "global_load_lds_dwordx4 v240, s[78:79]\n"
      "v_mfma_f32_16x16x32_bf16 v[144:147], a[0:3], v[224:227], 0\n"
      "ds_read_b64_tr_b16 a[60:61], v243 offset:49224\n"
      "v_mfma_f32_16x16x32_bf16 v[148:151], a[4:7], v[224:227], 0\n"
      "ds_read_b64_tr_b16 a[62:63], v243 offset:49992\n"
      "v_mfma_f32_16x16x32_bf16 v[152:155], a[8:11], v[224:227], 0\n"
      "ds_read_b128 v[220:223], v233 offset:0\n"
      "s_add_u32 m0, s74, 0x6400\n"
      "v_mfma_f32_16x16x32_bf16 v[156:159], a[12:15], v[224:227], 0\n"
      "ds_read_b64_tr_b16 a[64:65], v243 offset:49280\n"
      "global_load_lds_dwordx4 v241, s[78:79]\n"
      "v_mfma_f32_16x16x32_bf16 v[160:163], a[16:19], v[224:227], 0\n"
      "ds_read_b64_tr_b16 a[66:67], v243 offset:50048\n"
      "v_mfma_f32_16x16x32_bf16 v[164:167], a[20:23], v[224:227], 0\n"
      "s_waitcnt lgkmcnt(12)\n"
      "v_mfma_f32_16x16x32_bf16 v[168:171], a[0:3], v[228:231], 0\n"
      "ds_read_b64_tr_b16 a[68:69], v243 offset:49288\n"
      "s_add_u32 m0, s74, 0x6800\n"
      "v_mfma_f32_16x16x32_bf16 v[172:175], a[4:7], v[228:231], 0\n"
      "ds_read_b64_tr_b16 a[70:71], v243 offset:50056\n"
      "global_load_lds_dwordx4 v242, s[78:79]\n"
      "v_mfma_f32_16x16x32_bf16 v[176:179], a[8:11], v[228:231], 0\n"
      "ds_read_b128 v[224:227], v233 offset:4096\n"
      "v_mfma_f32_16x16x32_bf16 v[180:183], a[12:15], v[228:231], 0\n"
      "v_mfma_f32_16x16x32_bf16 v[184:187], a[16:19], v[228:231], 0\n"
      "s_add_u32 s78, s64, 0x1b000\n"
      "s_addc_u32 s79, s65, 0\n"
      "s_add_u32 m0, s74, 0x6c00\n"
      "v_mfma_f32_16x16x32_bf16 v[188:191], a[20:23], v[228:231], 0\n"
      "global_load_lds_dwordx4 v240, s[78:79]\n"
      "s_waitcnt lgkmcnt(10)\n"
      "v_mfma_f32_16x16x32_bf16 v[192:195], a[0:3], v[216:219], 0\n"
      "v_mfma_f32_16x16x32_bf16 v[196:199], a[4:7], v[216:219], 0\n"
      "v_mfma_f32_16x16x32_bf16 v[200:203], a[8:11], v[216:219], 0\n"
      "ds_read_b128 v[228:231], v233 offset:8192\n"
      "s_add_u32 m0, s74, 0x7000\n"
      "v_mfma_f32_16x16x32_bf16 v[204:207], a[12:15], v[216:219], 0\n"
      "global_load_lds_dwordx4 v241, s[78:79]\n"
      "v_mfma_f32_16x16x32_bf16 v[208:211], a[16:19], v[216:219], 0\n"
      "v_mfma_f32_16x16x32_bf16 v[212:215], a[20:23], v[216:219], 0\n"
      "s_waitcnt lgkmcnt(6)\n"
      "v_mfma_f32_16x16x32_bf16 v[0:3], a[24:27], v[220:223], v[0:3]\n"
      "s_add_u32 m0, s74, 0x7400\n"
      "v_mfma_f32_16x16x32_bf16 v[4:7], a[28:31], v[220:223], v[4:7]\n"
      "global_load_lds_dwordx4 v242, s[78:79]\n"
      "v_mfma_f32_16x16x32_bf16 v[8:11], a[32:35], v[220:223], v[8:11]\n"
      "ds_read_b128 v[216:219], v233 offset:12288\n"
      "s_waitcnt vmcnt(6)\n"
      "v_mfma_f32_16x16x32_bf16 v[12:15], a[36:39], v[220:223], v[12:15]\n"
      "ds_read_b64_tr_b16 a[72:73], v243 offset:0\n"
      "v_mfma_f32_16x16x32_bf16 v[16:19], a[40:43], v[220:223], v[16:19]\n"
      "ds_read_b64_tr_b16 a[74:75], v243 offset:768\n"
      "v_mad_i32_i24 v238, v251, s89, v238\n"
      "s_add_u32 s56, s22, 0x0\n"
      "s_addc_u32 s57, s23, 0\n"
      "s_add_u32 m0, s18, 0x8f00\n"
      "v_mfma_f32_16x16x32_bf16 v[20:23], a[44:47], v[220:223], v[20:23]\n"
      "global_load_lds_dwordx4 v238, s[56:57] offset:256 \n"
      "s_waitcnt lgkmcnt(4)\n"
      "v_mfma_f32_16x16x32_bf16 v[24:27], a[24:27], v[224:227], v[24:27]\n"
      "ds_read_b64_tr_b16 a[76:77], v243 offset:8\n"
      "v_mfma_f32_16x16x32_bf16 v[28:31], a[28:31], v[224:227], v[28:31]\n"
      "ds_read_b64_tr_b16 a[78:79], v243 offset:776\n"
      "v_mad_i32_i24 v238, v251, s90, v238\n"
      "s_add_u32 s56, s22, 0x6000\n"
      "s_addc_u32 s57, s23, 0\n"
      "s_add_u32 m0, s18, 0x9f00\n"
      "v_mfma_f32_16x16x32_bf16 v[32:35], a[32:35], v[224:227], v[32:35]\n"
      "ds_read_b128 v[220:223], v233 offset:16384\n"
      "global_load_lds_dwordx4 v238, s[56:57] offset:256 \n"
      "v_mfma_f32_16x16x32_bf16 v[36:39], a[36:39], v[224:227], v[36:39]\n"
      "ds_read_b64_tr_b16 a[80:81], v243 offset:64\n"
      "v_mfma_f32_16x16x32_bf16 v[40:43], a[40:43], v[224:227], v[40:43]\n"
      "ds_read_b64_tr_b16 a[82:83], v243 offset:832\n"
      "v_mad_i32_i24 v238, v251, s91, v238\n"
      "s_add_u32 s56, s22, 0xc000\n"
      "s_addc_u32 s57, s23, 0\n"
      "s_add_u32 m0, s18, 0xaf00\n"
      "v_mfma_f32_16x16x32_bf16 v[44:47], a[44:47], v[224:227], v[44:47]\n"
      "global_load_lds_dwordx4 v238, s[56:57] offset:256 \n"
      "s_waitcnt lgkmcnt(8)\n"
      "v_mfma_f32_16x16x32_bf16 v[48:51], a[24:27], v[228:231], v[48:51]\n"
      "ds_read_b64_tr_b16 a[84:85], v243 offset:72\n"
      "v_mfma_f32_16x16x32_bf16 v[52:55], a[28:31], v[228:231], v[52:55]\n"
      "ds_read_b64_tr_b16 a[86:87], v243 offset:840\n"
      "v_mad_i32_i24 v238, v251, s92, v238\n"
      "s_add_u32 s56, s22, 0x12000\n"
      "s_addc_u32 s57, s23, 0\n"
      "s_add_u32 m0, s18, 0xbf00\n"
      "v_mfma_f32_16x16x32_bf16 v[56:59], a[32:35], v[228:231], v[56:59]\n"
      "ds_read_b128 v[224:227], v233 offset:20480\n"
      "global_load_lds_dwordx4 v238, s[56:57] offset:256 \n"
      "v_mfma_f32_16x16x32_bf16 v[60:63], a[36:39], v[228:231], v[60:63]\n"
      "ds_read_b64_tr_b16 a[88:89], v243 offset:128\n"
      "v_mfma_f32_16x16x32_bf16 v[64:67], a[40:43], v[228:231], v[64:67]\n"
      "ds_read_b64_tr_b16 a[90:91], v243 offset:896\n"
      "v_mad_i32_i24 v238, v251, s93, v238\n"
      "s_add_u32 s56, s22, 0x18000\n"
      "s_addc_u32 s57, s23, 0\n"
      "s_add_u32 m0, s18, 0xcf00\n"
      "v_mfma_f32_16x16x32_bf16 v[68:71], a[44:47], v[228:231], v[68:71]\n"
      "global_load_lds_dwordx4 v238, s[56:57] offset:256 \n"
      "s_waitcnt lgkmcnt(12)\n"
      "v_mfma_f32_16x16x32_bf16 v[72:75], a[24:27], v[216:219], v[72:75]\n"
      "ds_read_b64_tr_b16 a[92:93], v243 offset:136\n"
      "v_mfma_f32_16x16x32_bf16 v[76:79], a[28:31], v[216:219], v[76:79]\n"
      "ds_read_b64_tr_b16 a[94:95], v243 offset:904\n"
      "v_mad_i32_i24 v238, v251, s94, v238\n"
      "s_add_u32 s56, s22, 0x1e000\n"
      "s_addc_u32 s57, s23, 0\n"
      "s_add_u32 m0, s18, 0xdf00\n"
      "v_mfma_f32_16x16x32_bf16 v[80:83], a[32:35], v[216:219], v[80:83]\n"
      "ds_read_b128 v[228:231], v233 offset:24576\n"
      "global_load_lds_dwordx4 v238, s[56:57] offset:256 \n"
      "v_mfma_f32_16x16x32_bf16 v[84:87], a[36:39], v[216:219], v[84:87]\n"
      "v_mfma_f32_16x16x32_bf16 v[88:91], a[40:43], v[216:219], v[88:91]\n"
      "v_mad_i32_i24 v238, v251, s95, v238\n"
      "s_add_u32 s56, s22, 0x24000\n"
      "s_addc_u32 s57, s23, 0\n"
      "s_add_u32 m0, s18, 0xef00\n"
      "v_mfma_f32_16x16x32_bf16 v[92:95], a[44:47], v[216:219], v[92:95]\n"
      "global_load_lds_dwordx4 v238, s[56:57] offset:256 \n"
      "s_waitcnt lgkmcnt(10)\n"
      "v_mfma_f32_16x16x32_bf16 v[96:99], a[24:27], v[220:223], v[96:99]\n"
      "v_mfma_f32_16x16x32_bf16 v[100:103], a[28:31], v[220:223], v[100:103]\n"
      "v_mad_i32_i24 v238, v251, s96, v238\n"
      "s_add_u32 s56, s22, 0x2a000\n"
      "s_addc_u32 s57, s23, 0\n"
      "s_add_u32 m0, s18, 0xff00\n"
      "v_mfma_f32_16x16x32_bf16 v[104:107], a[32:35], v[220:223], v[104:107]\n"
      "ds_read_b128 v[216:219], v233 offset:28672\n"
      "global_load_lds_dwordx4 v238, s[56:57] offset:256 \n"
      "v_mfma_f32_16x16x32_bf16 v[108:111], a[36:39], v[220:223], v[108:111]\n"
      "v_mfma_f32_16x16x32_bf16 v[112:115], a[40:43], v[220:223], v[112:115]\n"
      "v_mad_i32_i24 v238, v251, s97, v238\n"
      "s_add_u32 s56, s22, 0x30000\n"
      "s_addc_u32 s57, s23, 0\n"
      "s_add_u32 m0, s18, 0x10f00\n"
      "v_mfma_f32_16x16x32_bf16 v[116:119], a[44:47], v[220:223], v[116:119]\n"
      "global_load_lds_dwordx4 v238, s[56:57] offset:256 \n"
      "s_waitcnt lgkmcnt(6)\n"
      "v_mfma_f32_16x16x32_bf16 v[120:123], a[24:27], v[224:227], v[120:123]\n"
      "v_mfma_f32_16x16x32_bf16 v[124:127], a[28:31], v[224:227], v[124:127]\n"
      "v_mfma_f32_16x16x32_bf16 v[128:131], a[32:35], v[224:227], v[128:131]\n"
      "ds_read_b128 v[220:223], v233 offset:32768\n"
      "v_add_u32 v233, 0x9000, v233\n"
      "s_add_u32 s78, s64, 0x1e000\n"
      "s_addc_u32 s79, s65, 0\n"
      "s_add_u32 m0, s74, 0xc000\n"
      "v_mfma_f32_16x16x32_bf16 v[132:135], a[36:39], v[224:227], v[132:135]\n"
      "global_load_lds_dwordx4 v240, s[78:79]\n"
      "v_mfma_f32_16x16x32_bf16 v[136:139], a[40:43], v[224:227], v[136:139]\n"
      "v_mfma_f32_16x16x32_bf16 v[140:143], a[44:47], v[224:227], v[140:143]\n"
      "s_waitcnt lgkmcnt(2)\n"
      "v_mfma_f32_16x16x32_bf16 v[144:147], a[24:27], v[228:231], v[144:147]\n"
      "s_waitcnt vmcnt(10)\n"
      "s_add_u32 m0, s74, 0xc400\n"
      "v_mfma_f32_16x16x32_bf16 v[148:151], a[28:31], v[228:231], v[148:151]\n"
      "ds_read_b64_tr_b16 a[96:97], v243 offset:24576\n"
      "global_load_lds_dwordx4 v241, s[78:79]\n"
      "v_mfma_f32_16x16x32_bf16 v[152:155], a[32:35], v[228:231], v[152:155]\n"
      "ds_read_b128 v[224:227], v234 offset:0\n"
      "v_mfma_f32_16x16x32_bf16 v[156:159], a[36:39], v[228:231], v[156:159]\n"
      "ds_read_b64_tr_b16 a[98:99], v243 offset:25344\n"
      "v_mfma_f32_16x16x32_bf16 v[160:163], a[40:43], v[228:231], v[160:163]\n"
      "ds_read_b64_tr_b16 a[100:101], v243 offset:24584\n"
      "s_add_u32 m0, s74, 0xc800\n"
      "v_mfma_f32_16x16x32_bf16 v[164:167], a[44:47], v[228:231], v[164:167]\n"
      "global_load_lds_dwordx4 v242, s[78:79]\n"
      "s_waitcnt lgkmcnt(5)\n"
      "v_mfma_f32_16x16x32_bf16 v[168:171], a[24:27], v[216:219], v[168:171]\n"
      "ds_read_b64_tr_b16 a[102:103], v243 offset:25352\n"
      "v_mfma_f32_16x16x32_bf16 v[172:175], a[28:31], v[216:219], v[172:175]\n"
      "ds_read_b64_tr_b16 a[104:105], v243 offset:24640\n"
      "v_mfma_f32_16x16x32_bf16 v[176:179], a[32:35], v[216:219], v[176:179]\n"
      "ds_read_b128 v[228:231], v234 offset:4096\n"
      "s_add_u32 s78, s64, 0x21000\n"
      "s_addc_u32 s79, s65, 0\n"
      "s_add_u32 m0, s74, 0xcc00\n"
      "v_mfma_f32_16x16x32_bf16 v[180:183], a[36:39], v[216:219], v[180:183]\n"
      "ds_read_b64_tr_b16 a[106:107], v243 offset:25408\n"
      "global_load_lds_dwordx4 v240, s[78:79]\n"
      "v_mfma_f32_16x16x32_bf16 v[184:187], a[40:43], v[216:219], v[184:187]\n"
      "ds_read_b64_tr_b16 a[108:109], v243 offset:24648\n"
      "v_mfma_f32_16x16x32_bf16 v[188:191], a[44:47], v[216:219], v[188:191]\n"
      "s_waitcnt lgkmcnt(9)\n"
      "v_mfma_f32_16x16x32_bf16 v[192:195], a[24:27], v[220:223], v[192:195]\n"
      "ds_read_b64_tr_b16 a[110:111], v243 offset:25416\n"
      "s_add_u32 m0, s74, 0xd000\n"
      "v_mfma_f32_16x16x32_bf16 v[196:199], a[28:31], v[220:223], v[196:199]\n"
      "ds_read_b64_tr_b16 a[112:113], v243 offset:24704\n"
      "global_load_lds_dwordx4 v241, s[78:79]\n"
      "v_mfma_f32_16x16x32_bf16 v[200:203], a[32:35], v[220:223], v[200:203]\n"
      "ds_read_b128 v[216:219], v234 offset:8192\n"
      "v_mfma_f32_16x16x32_bf16 v[204:207], a[36:39], v[220:223], v[204:207]\n"
      "ds_read_b64_tr_b16 a[114:115], v243 offset:25472\n"
      "v_mfma_f32_16x16x32_bf16 v[208:211], a[40:43], v[220:223], v[208:211]\n"
      "ds_read_b64_tr_b16 a[116:117], v243 offset:24712\n"
      "s_add_u32 m0, s74, 0xd400\n"
      "v_mfma_f32_16x16x32_bf16 v[212:215], a[44:47], v[220:223], v[212:215]\n"
      "global_load_lds_dwordx4 v242, s[78:79]\n"
      "s_waitcnt lgkmcnt(12)\n"
      "v_mfma_f32_16x16x32_bf16 v[0:3], a[48:51], v[224:227], v[0:3]\n"
      "ds_read_b64_tr_b16 a[118:119], v243 offset:25480\n"
      "v_mfma_f32_16x16x32_bf16 v[4:7], a[52:55], v[224:227], v[4:7]\n"
      "v_mfma_f32_16x16x32_bf16 v[8:11], a[56:59], v[224:227], v[8:11]\n"
      "ds_read_b128 v[220:223], v234 offset:12288\n"
      "s_add_u32 s78, s64, 0x24000\n"
      "s_addc_u32 s79, s65, 0\n"
      "s_add_u32 m0, s74, 0x0\n"
      "v_mfma_f32_16x16x32_bf16 v[12:15], a[60:63], v[224:227], v[12:15]\n"
      "global_load_lds_dwordx4 v240, s[78:79]\n"
      "v_mfma_f32_16x16x32_bf16 v[16:19], a[64:67], v[224:227], v[16:19]\n"
      "v_mfma_f32_16x16x32_bf16 v[20:23], a[68:71], v[224:227], v[20:23]\n"
      "s_waitcnt lgkmcnt(9)\n"
      "v_mfma_f32_16x16x32_bf16 v[24:27], a[48:51], v[228:231], v[24:27]\n"
      "s_add_u32 m0, s74, 0x400\n"
      "v_mfma_f32_16x16x32_bf16 v[28:31], a[52:55], v[228:231], v[28:31]\n"
      "global_load_lds_dwordx4 v241, s[78:79]\n"
      "v_mfma_f32_16x16x32_bf16 v[32:35], a[56:59], v[228:231], v[32:35]\n"
      "ds_read_b128 v[224:227], v234 offset:16384\n"
      "v_mfma_f32_16x16x32_bf16 v[36:39], a[60:63], v[228:231], v[36:39]\n"
      "v_mfma_f32_16x16x32_bf16 v[40:43], a[64:67], v[228:231], v[40:43]\n"
      "s_add_u32 m0, s74, 0x800\n"
      "v_mfma_f32_16x16x32_bf16 v[44:47], a[68:71], v[228:231], v[44:47]\n"
      "global_load_lds_dwordx4 v242, s[78:79]\n"
      "s_waitcnt lgkmcnt(5)\n"
      "v_mfma_f32_16x16x32_bf16 v[48:51], a[48:51], v[216:219], v[48:51]\n"
      "v_mfma_f32_16x16x32_bf16 v[52:55], a[52:55], v[216:219], v[52:55]\n"
      "v_mfma_f32_16x16x32_bf16 v[56:59], a[56:59], v[216:219], v[56:59]\n"
      "ds_read_b128 v[228:231], v234 offset:20480\n"
      "s_add_u32 s78, s64, 0x27000\n"
      "s_addc_u32 s79, s65, 0\n"
      "s_add_u32 m0, s74, 0xc00\n"
      "v_mfma_f32_16x16x32_bf16 v[60:63], a[60:63], v[216:219], v[60:63]\n"
      "global_load_lds_dwordx4 v240, s[78:79]\n"
      "v_mfma_f32_16x16x32_bf16 v[64:67], a[64:67], v[216:219], v[64:67]\n"
      "v_mfma_f32_16x16x32_bf16 v[68:71], a[68:71], v[216:219], v[68:71]\n"
      "s_waitcnt lgkmcnt(2)\n"
      "v_mfma_f32_16x16x32_bf16 v[72:75], a[48:51], v[220:223], v[72:75]\n"
      "s_add_u32 m0, s74, 0x1000\n"
      "v_mfma_f32_16x16x32_bf16 v[76:79], a[52:55], v[220:223], v[76:79]\n"
      "global_load_lds_dwordx4 v241, s[78:79]\n"
      "v_mfma_f32_16x16x32_bf16 v[80:83], a[56:59], v[220:223], v[80:83]\n"
      "ds_read_b128 v[216:219], v234 offset:24576\n"
      "v_mfma_f32_16x16x32_bf16 v[84:87], a[60:63], v[220:223], v[84:87]\n"
      "v_mfma_f32_16x16x32_bf16 v[88:91], a[64:67], v[220:223], v[88:91]\n"
      "s_add_u32 m0, s74, 0x1400\n"
      "v_mfma_f32_16x16x32_bf16 v[92:95], a[68:71], v[220:223], v[92:95]\n"
      "global_load_lds_dwordx4 v242, s[78:79]\n"
      "s_waitcnt lgkmcnt(2)\n"
      "v_mfma_f32_16x16x32_bf16 v[96:99], a[48:51], v[224:227], v[96:99]\n"
      "v_mfma_f32_16x16x32_bf16 v[100:103], a[52:55], v[224:227], v[100:103]\n"
      "v_mfma_f32_16x16x32_bf16 v[104:107], a[56:59], v[224:227], v[104:107]\n"
      "ds_read_b128 v[220:223], v234 offset:28672\n"
      "s_add_u32 s78, s64, 0x2a000\n"
      "s_addc_u32 s79, s65, 0\n"
      "s_add_u32 m0, s74, 0x6000\n"
      "v_mfma_f32_16x16x32_bf16 v[108:111], a[60:63], v[224:227], v[108:111]\n"
      "global_load_lds_dwordx4 v240, s[78:79]\n"
      "v_mfma_f32_16x16x32_bf16 v[112:115], a[64:67], v[224:227], v[112:115]\n"
      "v_mfma_f32_16x16x32_bf16 v[116:119], a[68:71], v[224:227], v[116:119]\n"
      "s_waitcnt lgkmcnt(2)\n"
      "v_mfma_f32_16x16x32_bf16 v[120:123], a[48:51], v[228:231], v[120:123]\n"
      "s_add_u32 m0, s74, 0x6400\n"
      "v_mfma_f32_16x16x32_bf16 v[124:127], a[52:55], v[228:231], v[124:127]\n"
      "global_load_lds_dwordx4 v241, s[78:79]\n"
      "v_mfma_f32_16x16x32_bf16 v[128:131], a[56:59], v[228:231], v[128:131]\n"
      "ds_read_b128 v[224:227], v234 offset:32768\n"
      "v_add_u32 v234, 0x9000, v234\n"
      "v_mfma_f32_16x16x32_bf16 v[132:135], a[60:63], v[228:231], v[132:135]\n"
      "v_mfma_f32_16x16x32_bf16 v[136:139], a[64:67], v[228:231], v[136:139]\n"
      "s_waitcnt vmcnt(8)\n"
      "s_add_u32 m0, s74, 0x6800\n"
      "v_mfma_f32_16x16x32_bf16 v[140:143], a[68:71], v[228:231], v[140:143]\n"
      "global_load_lds_dwordx4 v242, s[78:79]\n"
      "s_waitcnt lgkmcnt(2)\n"
      "v_mfma_f32_16x16x32_bf16 v[144:147], a[48:51], v[216:219], v[144:147]\n"
      "ds_read_b64_tr_b16 a[120:121], v243 offset:49152\n"
      "v_mfma_f32_16x16x32_bf16 v[148:151], a[52:55], v[216:219], v[148:151]\n"
      "ds_read_b64_tr_b16 a[122:123], v243 offset:49920\n"
      "v_mfma_f32_16x16x32_bf16 v[152:155], a[56:59], v[216:219], v[152:155]\n"
      "ds_read_b128 v[228:231], v235 offset:0\n"
      "s_add_u32 s78, s64, 0x2d000\n"
      "s_addc_u32 s79, s65, 0\n"
      "s_add_u32 m0, s74, 0x6c00\n"
      "v_mfma_f32_16x16x32_bf16 v[156:159], a[60:63], v[216:219], v[156:159]\n"
      "ds_read_b64_tr_b16 a[124:125], v243 offset:49160\n"
      "global_load_lds_dwordx4 v240, s[78:79]\n"
      "v_mfma_f32_16x16x32_bf16 v[160:163], a[64:67], v[216:219], v[160:163]\n"
      "ds_read_b64_tr_b16 a[126:127], v243 offset:49928\n"
      "v_mfma_f32_16x16x32_bf16 v[164:167], a[68:71], v[216:219], v[164:167]\n"
      "s_waitcnt lgkmcnt(6)\n"
      "v_mfma_f32_16x16x32_bf16 v[168:171], a[48:51], v[220:223], v[168:171]\n"
      "ds_read_b64_tr_b16 a[128:129], v243 offset:49216\n"
      "s_add_u32 m0, s74, 0x7000\n"
      "v_mfma_f32_16x16x32_bf16 v[172:175], a[52:55], v[220:223], v[172:175]\n"
      "ds_read_b64_tr_b16 a[130:131], v243 offset:49984\n"
      "global_load_lds_dwordx4 v241, s[78:79]\n"
      "v_mfma_f32_16x16x32_bf16 v[176:179], a[56:59], v[220:223], v[176:179]\n"
      "ds_read_b128 v[216:219], v235 offset:4096\n"
      "v_mfma_f32_16x16x32_bf16 v[180:183], a[60:63], v[220:223], v[180:183]\n"
      "ds_read_b64_tr_b16 a[132:133], v243 offset:49224\n"
      "v_mfma_f32_16x16x32_bf16 v[184:187], a[64:67], v[220:223], v[184:187]\n"
      "ds_read_b64_tr_b16 a[134:135], v243 offset:49992\n"
      "s_add_u32 m0, s74, 0x7400\n"
      "v_mfma_f32_16x16x32_bf16 v[188:191], a[68:71], v[220:223], v[188:191]\n"
      "global_load_lds_dwordx4 v242, s[78:79]\n"
      "s_waitcnt lgkmcnt(10)\n"
      "v_mfma_f32_16x16x32_bf16 v[192:195], a[48:51], v[224:227], v[192:195]\n"
      "ds_read_b64_tr_b16 a[136:137], v243 offset:49280\n"
      "v_mfma_f32_16x16x32_bf16 v[196:199], a[52:55], v[224:227], v[196:199]\n"
      "ds_read_b64_tr_b16 a[138:139], v243 offset:50048\n"
      "v_mfma_f32_16x16x32_bf16 v[200:203], a[56:59], v[224:227], v[200:203]\n"
      "ds_read_b128 v[220:223], v235 offset:8192\n"
      "global_load_dwordx4 v[244:247], v248, s[28:29] sc1\n"
      "v_mfma_f32_16x16x32_bf16 v[204:207], a[60:63], v[224:227], v[204:207]\n"
      "ds_read_b64_tr_b16 a[140:141], v243 offset:49288\n"
      "v_mfma_f32_16x16x32_bf16 v[208:211], a[64:67], v[224:227], v[208:211]\n"
      "ds_read_b64_tr_b16 a[142:143], v243 offset:50056\n"
      "v_mfma_f32_16x16x32_bf16 v[212:215], a[68:71], v[224:227], v[212:215]\n"
      "s_waitcnt lgkmcnt(12)\n"
      "v_mfma_f32_16x16x32_bf16 v[0:3], a[72:75], v[228:231], v[0:3]\n"
      "v_mfma_f32_16x16x32_bf16 v[4:7], a[76:79], v[228:231], v[4:7]\n"
      "v_mfma_f32_16x16x32_bf16 v[8:11], a[80:83], v[228:231], v[8:11]\n"
      "ds_read_b128 v[224:227], v235 offset:12288\n"
      "v_mfma_f32_16x16x32_bf16 v[12:15], a[84:87], v[228:231], v[12:15]\n"
      "v_mfma_f32_16x16x32_bf16 v[16:19], a[88:91], v[228:231], v[16:19]\n"
      "s_waitcnt vmcnt(7)\n"
      "v_mfma_f32_16x16x32_bf16 v[20:23], a[92:95], v[228:231], v[20:23]\n"
      "s_waitcnt lgkmcnt(8)\n"
      "v_mfma_f32_16x16x32_bf16 v[24:27], a[72:75], v[216:219], v[24:27]\n"
      "ds_read_b64_tr_b16 a[144:145], v243 offset:0\n"
      "v_mfma_f32_16x16x32_bf16 v[28:31], a[76:79], v[216:219], v[28:31]\n"
      "ds_read_b64_tr_b16 a[146:147], v243 offset:768\n"
      "v_mfma_f32_16x16x32_bf16 v[32:35], a[80:83], v[216:219], v[32:35]\n"
      "ds_read_b128 v[228:231], v235 offset:16384\n"
      "s_waitcnt lgkmcnt(4)\n"
      "s_add_u32 s78, s64, 0x30000\n"
      "s_addc_u32 s79, s65, 0\n"
      "s_add_u32 m0, s74, 0xc000\n"
      "v_mfma_f32_16x16x32_bf16 v[36:39], a[84:87], v[216:219], v[36:39]\n"
      "ds_read_b64_tr_b16 a[148:149], v243 offset:8\n"
      "global_load_lds_dwordx4 v240, s[78:79]\n"
      "v_mfma_f32_16x16x32_bf16 v[40:43], a[88:91], v[216:219], v[40:43]\n"
      "ds_read_b64_tr_b16 a[150:151], v243 offset:776\n"
      "v_mfma_f32_16x16x32_bf16 v[44:47], a[92:95], v[216:219], v[44:47]\n"
      "v_mfma_f32_16x16x32_bf16 v[48:51], a[72:75], v[220:223], v[48:51]\n"
      "ds_read_b64_tr_b16 a[152:153], v243 offset:64\n"
      "s_add_u32 m0, s74, 0xc400\n"
      "v_mfma_f32_16x16x32_bf16 v[52:55], a[76:79], v[220:223], v[52:55]\n"
      "ds_read_b64_tr_b16 a[154:155], v243 offset:832\n"
      "global_load_lds_dwordx4 v241, s[78:79]\n"
      "v_mfma_f32_16x16x32_bf16 v[56:59], a[80:83], v[220:223], v[56:59]\n"
      "ds_read_b128 v[216:219], v235 offset:20480\n"
      "v_mfma_f32_16x16x32_bf16 v[60:63], a[84:87], v[220:223], v[60:63]\n"
      "ds_read_b64_tr_b16 a[156:157], v243 offset:72\n"
      "v_mfma_f32_16x16x32_bf16 v[64:67], a[88:91], v[220:223], v[64:67]\n"
      "ds_read_b64_tr_b16 a[158:159], v243 offset:840\n"
      "s_add_u32 m0, s74, 0xc800\n"
      "v_mfma_f32_16x16x32_bf16 v[68:71], a[92:95], v[220:223], v[68:71]\n"
      "global_load_lds_dwordx4 v242, s[78:79]\n"
      "s_waitcnt lgkmcnt(10)\n"
      "v_mfma_f32_16x16x32_bf16 v[72:75], a[72:75], v[224:227], v[72:75]\n"
      "ds_read_b64_tr_b16 a[160:161], v243 offset:128\n"
      "v_mfma_f32_16x16x32_bf16 v[76:79], a[76:79], v[224:227], v[76:79]\n"
      "ds_read_b64_tr_b16 a[162:163], v243 offset:896\n"
      "v_mfma_f32_16x16x32_bf16 v[80:83], a[80:83], v[224:227], v[80:83]\n"
      "ds_read_b128 v[220:223], v235 offset:24576\n"
      "s_add_u32 s78, s64, 0x33000\n"
      "s_addc_u32 s79, s65, 0\n"
      "s_add_u32 m0, s74, 0xcc00\n"
      "v_mfma_f32_16x16x32_bf16 v[84:87], a[84:87], v[224:227], v[84:87]\n"
      "ds_read_b64_tr_b16 a[164:165], v243 offset:136\n"
      "global_load_lds_dwordx4 v240, s[78:79]\n"
      "v_mfma_f32_16x16x32_bf16 v[88:91], a[88:91], v[224:227], v[88:91]\n"
      "ds_read_b64_tr_b16 a[166:167], v243 offset:904\n"
      "v_mfma_f32_16x16x32_bf16 v[92:95], a[92:95], v[224:227], v[92:95]\n"
      "s_waitcnt lgkmcnt(12)\n"
      "v_mfma_f32_16x16x32_bf16 v[96:99], a[72:75], v[228:231], v[96:99]\n"
      "s_add_u32 m0, s74, 0xd000\n"
      "v_mfma_f32_16x16x32_bf16 v[100:103], a[76:79], v[228:231], v[100:103]\n"
      "global_load_lds_dwordx4 v241, s[78:79]\n"
      "v_mfma_f32_16x16x32_bf16 v[104:107], a[80:83], v[228:231], v[104:107]\n"
      "ds_read_b128 v[224:227], v235 offset:28672\n"
      "v_mfma_f32_16x16x32_bf16 v[108:111], a[84:87], v[228:231], v[108:111]\n"
      "v_mfma_f32_16x16x32_bf16 v[112:115], a[88:91], v[228:231], v[112:115]\n"
      "s_waitcnt vmcnt(6)\n"
      "s_add_u32 m0, s74, 0xd400\n"
      "v_mfma_f32_16x16x32_bf16 v[116:119], a[92:95], v[228:231], v[116:119]\n"
      "global_load_lds_dwordx4 v242, s[78:79]\n"
      "s_waitcnt lgkmcnt(8)\n"
      "v_mfma_f32_16x16x32_bf16 v[120:123], a[72:75], v[216:219], v[120:123]\n"
      "ds_read_b64_tr_b16 a[168:169], v243 offset:24576\n"
      "v_mfma_f32_16x16x32_bf16 v[124:127], a[76:79], v[216:219], v[124:127]\n"
      "ds_read_b64_tr_b16 a[170:171], v243 offset:25344\n"
      "v_mfma_f32_16x16x32_bf16 v[128:131], a[80:83], v[216:219], v[128:131]\n"
      "ds_read_b128 v[228:231], v235 offset:32768\n"
      "v_add_u32 v235, 0x9000, v235\n"
      "s_waitcnt lgkmcnt(4)\n"
      "s_add_u32 s78, s64, 0x36000\n"
      "s_addc_u32 s79, s65, 0\n"
      "s_add_u32 m0, s74, 0x0\n"
      "v_mfma_f32_16x16x32_bf16 v[132:135], a[84:87], v[216:219], v[132:135]\n"
      "ds_read_b64_tr_b16 a[172:173], v243 offset:24584\n"
      "global_load_lds_dwordx4 v240, s[78:79]\n"
      "v_mfma_f32_16x16x32_bf16 v[136:139], a[88:91], v[216:219], v[136:139]\n"
      "ds_read_b64_tr_b16 a[174:175], v243 offset:25352\n"
      "v_mfma_f32_16x16x32_bf16 v[140:143], a[92:95], v[216:219], v[140:143]\n"
      "v_mfma_f32_16x16x32_bf16 v[144:147], a[72:75], v[220:223], v[144:147]\n"
      "ds_read_b64_tr_b16 a[176:177], v243 offset:24640\n"
      "s_waitcnt vmcnt(7)\n"
      "v_cmp_ne_u64 vcc, s[80:81], v[244:245]\n"
      "v_cmp_ne_u64 s[56:57], s[80:81], v[246:247]\n"
      "s_nop 3\n"
      "s_or_b64 vcc, vcc, s[56:57]\n"
      "s_cbranch_scc0 LP1_0\n"
      "s_mov_b32 s82, 0x40000\n"
      "LP0_0:\n"
      "s_sleep 2\n"
      "global_load_dwordx4 v[244:247], v248, s[28:29] sc1\n"
      "s_sub_u32 s82, s82, 1\n"
      "s_cbranch_scc1 LP2_0\n"
      "s_waitcnt vmcnt(0)\n"
      "v_cmp_ne_u64 vcc, s[80:81], v[244:245]\n"
      "v_cmp_ne_u64 s[56:57], s[80:81], v[246:247]\n"
      "s_nop 3\n"
      "s_or_b64 vcc, vcc, s[56:57]\n"
      "s_cbranch_scc1 LP0_0\n"
      "LP2_0:\n"
      "s_waitcnt vmcnt(0)\n"
      "LP1_0:\n"
      "s_nop 0\n"
      "v_mfma_f32_16x16x32_bf16 v[148:151], a[76:79], v[220:223], v[148:151]\n"
      "s_barrier\n"
      "s_add_u32 m0, s74, 0x400\n"
      "v_mfma_f32_16x16x32_bf16 v[152:155], a[80:83], v[220:223], v[152:155]\n"
      "ds_read_b128 v[216:219], v232 offset:0\n"
      "global_load_lds_dwordx4 v241, s[78:79]\n"
      "v_mfma_f32_16x16x32_bf16 v[156:159], a[84:87], v[220:223], v[156:159]\n"
      "ds_read_b64_tr_b16 a[178:179], v243 offset:25408\n"
      "v_mfma_f32_16x16x32_bf16 v[160:163], a[88:91], v[220:223], v[160:163]\n"
      "ds_read_b64_tr_b16 a[180:181], v243 offset:24648\n"
      "v_mfma_f32_16x16x32_bf16 v[164:167], a[92:95], v[220:223], v[164:167]\n"
      "s_add_u32 m0, s74, 0x800\n"
      "s_waitcnt lgkmcnt(9)\n"
      "v_mfma_f32_16x16x32_bf16 v[168:171], a[72:75], v[224:227], v[168:171]\n"
      "ds_read_b64_tr_b16 a[182:183], v243 offset:25416\n"
      "global_load_lds_dwordx4 v242, s[78:79]\n"
      "v_mfma_f32_16x16x32_bf16 v[172:175], a[76:79], v[224:227], v[172:175]\n"
      "ds_read_b64_tr_b16 a[184:185], v243 offset:24704\n"
      "v_mfma_f32_16x16x32_bf16 v[176:179], a[80:83], v[224:227], v[176:179]\n"
      "ds_read_b128 v[220:223], v232 offset:4096\n"
      "v_mfma_f32_16x16x32_bf16 v[180:183], a[84:87], v[224:227], v[180:183]\n"
      "ds_read_b64_tr_b16 a[186:187], v243 offset:25472\n"
      "s_add_u32 s78, s64, 0x39000\n"
      "s_addc_u32 s79, s65, 0\n"
      "s_add_u32 m0, s74, 0xc00\n"
      "v_mfma_f32_16x16x32_bf16 v[184:187], a[88:91], v[224:227], v[184:187]\n"
      "ds_read_b64_tr_b16 a[188:189], v243 offset:24712\n"
      "global_load_lds_dwordx4 v240, s[78:79]\n"
      "v_mfma_f32_16x16x32_bf16 v[188:191], a[92:95], v[224:227], v[188:191]\n"
      "s_waitcnt lgkmcnt(11)\n"
      "v_mfma_f32_16x16x32_bf16 v[192:195], a[72:75], v[228:231], v[192:195]\n"
      "ds_read_b64_tr_b16 a[190:191], v243 offset:25480\n"
      "v_mfma_f32_16x16x32_bf16 v[196:199], a[76:79], v[228:231], v[196:199]\n"
      "s_add_u32 m0, s74, 0x1000\n"
      "v_mfma_f32_16x16x32_bf16 v[200:203], a[80:83], v[228:231], v[200:203]\n"
      "ds_read_b128 v[224:227], v232 offset:8192\n"
      "global_load_lds_dwordx4 v241, s[78:79]\n"
      "v_mfma_f32_16x16x32_bf16 v[204:207], a[84:87], v[228:231], v[204:207]\n"
      "v_mfma_f32_16x16x32_bf16 v[208:211], a[88:91], v[228:231], v[208:211]\n"
      "v_mfma_f32_16x16x32_bf16 v[212:215], a[92:95], v[228:231], v[212:215]\n"
      "s_add_u32 m0, s74, 0x1400\n"
      "s_waitcnt lgkmcnt(9)\n"
      "v_mfma_f32_16x16x32_bf16 v[0:3], a[96:99], v[216:219], v[0:3]\n"
      "global_load_lds_dwordx4 v242, s[78:79]\n"
      "v_mfma_f32_16x16x32_bf16 v[4:7], a[100:103], v[216:219], v[4:7]\n"
      "s_barrier\n"
      "v_mfma_f32_16x16x32_bf16 v[8:11], a[104:107], v[216:219], v[8:11]\n"
      "ds_read_b128 v[228:231], v232 offset:12288\n"
      "v_mfma_f32_16x16x32_bf16 v[12:15], a[108:111], v[216:219], v[12:15]\n"
      "s_add_u32 s58, s24, 0x0\n"
      "s_addc_u32 s59, s25, 0\n"
      "global_load_dwordx4 a[240:243], v239, s[58:59] offset:0 sc1\n"
      "v_mfma_f32_16x16x32_bf16 v[16:19], a[112:115], v[216:219], v[16:19]\n"
      "v_mfma_f32_16x16x32_bf16 v[20:23], a[116:119], v[216:219], v[20:23]\n"
      "s_waitcnt lgkmcnt(5)\n"
      "v_mfma_f32_16x16x32_bf16 v[24:27], a[96:99], v[220:223], v[24:27]\n"
      "v_mfma_f32_16x16x32_bf16 v[28:31], a[100:103], v[220:223], v[28:31]\n"
      "s_waitcnt lgkmcnt(2)\n"
      "s_add_u32 s78, s64, 0x3c000\n"
      "s_addc_u32 s79, s65, 0\n"
      "s_add_u32 m0, s74, 0x6000\n"
      "v_mfma_f32_16x16x32_bf16 v[32:35], a[104:107], v[220:223], v[32:35]\n"
      "ds_read_b128 v[216:219], v232 offset:16384\n"
      "LPD1:\n"
      "s_cbranch_execz LPD8\n"
      "global_load_lds_dwordx4 v240, s[78:79]\n"
      "v_mfma_f32_16x16x32_bf16 v[36:39], a[108:111], v[220:223], v[36:39]\n"
      "v_mfma_f32_16x16x32_bf16 v[40:43], a[112:115], v[220:223], v[40:43]\n"
      "s_waitcnt vmcnt(8)\n"
      "v_mfma_f32_16x16x32_bf16 v[44:47], a[116:119], v[220:223], v[44:47]\n"
      "s_add_u32 m0, s74, 0x6400\n"
      "s_waitcnt lgkmcnt(2)\n"
      "v_mfma_f32_16x16x32_bf16 v[48:51], a[96:99], v[224:227], v[48:51]\n"
      "ds_read_b64_tr_b16 a[192:193], v243 offset:49152\n"
      "global_load_lds_dwordx4 v241, s[78:79]\n"
      "v_mfma_f32_16x16x32_bf16 v[52:55], a[100:103], v[224:227], v[52:55]\n"
      "ds_read_b64_tr_b16 a[194:195], v243 offset:49920\n"
      "v_mfma_f32_16x16x32_bf16 v[56:59], a[104:107], v[224:227], v[56:59]\n"
      "ds_read_b128 v[220:223], v232 offset:20480\n"
      "v_mfma_f32_16x16x32_bf16 v[60:63], a[108:111], v[224:227], v[60:63]\n"
      "ds_read_b64_tr_b16 a[196:197], v243 offset:49160\n"
      "s_add_u32 m0, s74, 0x6800\n"
      "v_mfma_f32_16x16x32_bf16 v[64:67], a[112:115], v[224:227], v[64:67]\n"
      "ds_read_b64_tr_b16 a[198:199], v243 offset:49928\n"
      "global_load_lds_dwordx4 v242, s[78:79]\n"
      "v_mfma_f32_16x16x32_bf16 v[68:71], a[116:119], v[224:227], v[68:71]\n"
      "s_waitcnt lgkmcnt(6)\n"
      "v_mfma_f32_16x16x32_bf16 v[72:75], a[96:99], v[228:231], v[72:75]\n"
      "ds_read_b64_tr_b16 a[200:201], v243 offset:49216\n"
      "v_mfma_f32_16x16x32_bf16 v[76:79], a[100:103], v[228:231], v[76:79]\n"
      "ds_read_b64_tr_b16 a[202:203], v243 offset:49984\n"
      "s_add_u32 s78, s64, 0x3f000\n"
      "s_addc_u32 s79, s65, 0\n"
      "s_add_u32 m0, s74, 0x6c00\n"
      "v_mfma_f32_16x16x32_bf16 v[80:83], a[104:107], v[228:231], v[80:83]\n"
      "ds_read_b128 v[224:227], v232 offset:24576\n"
      "global_load_lds_dwordx4 v240, s[78:79]\n"
      "v_mfma_f32_16x16x32_bf16 v[84:87], a[108:111], v[228:231], v[84:87]\n"
      "ds_read_b64_tr_b16 a[204:205], v243 offset:49224\n"
      "v_mfma_f32_16x16x32_bf16 v[88:91], a[112:115], v[228:231], v[88:91]\n"
      "ds_read_b64_tr_b16 a[206:207], v243 offset:49992\n"
      "v_mfma_f32_16x16x32_bf16 v[92:95], a[116:119], v[228:231], v[92:95]\n"
      "s_add_u32 m0, s74, 0x7000\n"
      "s_waitcnt lgkmcnt(10)\n"
      "v_mfma_f32_16x16x32_bf16 v[96:99], a[96:99], v[216:219], v[96:99]\n"
      "ds_read_b64_tr_b16 a[208:209], v243 offset:49280\n"
      "global_load_lds_dwordx4 v241, s[78:79]\n"
      "v_mfma_f32_16x16x32_bf16 v[100:103], a[100:103], v[216:219], v[100:103]\n"
      "ds_read_b64_tr_b16 a[210:211], v243 offset:50048\n"
      "v_mfma_f32_16x16x32_bf16 v[104:107], a[104:107], v[216:219], v[104:107]\n"
      "ds_read_b128 v[228:231], v232 offset:28672\n"
      "v_mfma_f32_16x16x32_bf16 v[108:111], a[108:111], v[216:219], v[108:111]\n"
      "ds_read_b64_tr_b16 a[212:213], v243 offset:49288\n"
      "s_add_u32 m0, s74, 0x7400\n"
      "v_mfma_f32_16x16x32_bf16 v[112:115], a[112:115], v[216:219], v[112:115]\n"
      "ds_read_b64_tr_b16 a[214:215], v243 offset:50056\n"
      "global_load_lds_dwordx4 v242, s[78:79]\n"
      "v_mfma_f32_16x16x32_bf16 v[116:119], a[116:119], v[216:219], v[116:119]\n"
      "s_waitcnt lgkmcnt(12)\n"
      "v_mfma_f32_16x16x32_bf16 v[120:123], a[96:99], v[220:223], v[120:123]\n"
      "v_mfma_f32_16x16x32_bf16 v[124:127], a[100:103], v[220:223], v[124:127]\n"
      "global_load_dwordx4 a[244:247], v239, s[58:59] offset:1024 sc1\n"
      "v_mfma_f32_16x16x32_bf16 v[128:131], a[104:107], v[220:223], v[128:131]\n"
      "ds_read_b128 v[216:219], v232 offset:32768\n"
      "v_add_u32 v232, 0xffff7000, v232\n"
      "v_mfma_f32_16x16x32_bf16 v[132:135], a[108:111], v[220:223], v[132:135]\n"
      "v_mfma_f32_16x16x32_bf16 v[136:139], a[112:115], v[220:223], v[136:139]\n"
      "v_mfma_f32_16x16x32_bf16 v[140:143], a[116:119], v[220:223], v[140:143]\n"
      "global_load_dwordx4 a[248:251], v239, s[58:59] offset:2048 sc1\n"
      "s_waitcnt lgkmcnt(8)\n"
      "v_mfma_f32_16x16x32_bf16 v[144:147], a[96:99], v[224:227], v[144:147]\n"
      "s_waitcnt vmcnt(9)\n"
      "v_mfma_f32_16x16x32_bf16 v[148:151], a[100:103], v[224:227], v[148:151]\n"
      "ds_read_b64_tr_b16 a[216:217], v243 offset:0\n"
      "v_mfma_f32_16x16x32_bf16 v[152:155], a[104:107], v[224:227], v[152:155]\n"
      "ds_read_b128 v[220:223], v233 offset:0\n"
      "v_mfma_f32_16x16x32_bf16 v[156:159], a[108:111], v[224:227], v[156:159]\n"
      "ds_read_b64_tr_b16 a[218:219], v243 offset:768\n"
      "s_waitcnt lgkmcnt(4)\n"
      "s_add_u32 s78, s64, 0x42000\n"
      "s_addc_u32 s79, s65, 0\n"
      "s_add_u32 m0, s74, 0xc000\n"
      "v_mfma_f32_16x16x32_bf16 v[160:163], a[112:115], v[224:227], v[160:163]\n"
      "ds_read_b64_tr_b16 a[220:221], v243 offset:8\n"
      "global_load_lds_dwordx4 v240, s[78:79]\n"
      "v_mfma_f32_16x16x32_bf16 v[164:167], a[116:119], v[224:227], v[164:167]\n"
      "v_mfma_f32_16x16x32_bf16 v[168:171], a[96:99], v[228:231], v[168:171]\n"
      "ds_read_b64_tr_b16 a[222:223], v243 offset:776\n"
      "v_mfma_f32_16x16x32_bf16 v[172:175], a[100:103], v[228:231], v[172:175]\n"
      "ds_read_b64_tr_b16 a[224:225], v243 offset:64\n"
      "s_add_u32 m0, s74, 0xc400\n"
      "v_mfma_f32_16x16x32_bf16 v[176:179], a[104:107], v[228:231], v[176:179]\n"
      "ds_read_b128 v[224:227], v233 offset:4096\n"
      "global_load_lds_dwordx4 v241, s[78:79]\n"
      "v_mfma_f32_16x16x32_bf16 v[180:183], a[108:111], v[228:231], v[180:183]\n"
      "ds_read_b64_tr_b16 a[226:227], v243 offset:832\n"
      "v_mfma_f32_16x16x32_bf16 v[184:187], a[112:115], v[228:231], v[184:187]\n"
      "ds_read_b64_tr_b16 a[228:229], v243 offset:72\n"
      "v_mfma_f32_16x16x32_bf16 v[188:191], a[116:119], v[228:231], v[188:191]\n"
      "s_add_u32 m0, s74, 0xc800\n"
      "s_waitcnt lgkmcnt(9)\n"
      "v_mfma_f32_16x16x32_bf16 v[192:195], a[96:99], v[216:219], v[192:195]\n"
      "ds_read_b64_tr_b16 a[230:231], v243 offset:840\n"
      "global_load_lds_dwordx4 v242, s[78:79]\n"
      "v_mfma_f32_16x16x32_bf16 v[196:199], a[100:103], v[216:219], v[196:199]\n"
      "ds_read_b64_tr_b16 a[232:233], v243 offset:128\n"
      "v_mfma_f32_16x16x32_bf16 v[200:203], a[104:107], v[216:219], v[200:203]\n"
      "ds_read_b128 v[228:231], v233 offset:8192\n"
      "v_mfma_f32_16x16x32_bf16 v[204:207], a[108:111], v[216:219], v[204:207]\n"
      "ds_read_b64_tr_b16 a[234:235], v243 offset:896\n"
      "s_add_u32 s78, s64, 0x45000\n"
      "s_addc_u32 s79, s65, 0\n"
      "s_add_u32 m0, s74, 0xcc00\n"
      "v_mfma_f32_16x16x32_bf16 v[208:211], a[112:115], v[216:219], v[208:211]\n"
      "ds_read_b64_tr_b16 a[236:237], v243 offset:136\n"
      "global_load_lds_dwordx4 v240, s[78:79]\n"
      "v_mfma_f32_16x16x32_bf16 v[212:215], a[116:119], v[216:219], v[212:215]\n"
      "s_waitcnt lgkmcnt(12)\n"
      "v_mfma_f32_16x16x32_bf16 v[0:3], a[120:123], v[220:223], v[0:3]\n"
      "ds_read_b64_tr_b16 a[238:239], v243 offset:904\n"
      "v_mfma_f32_16x16x32_bf16 v[4:7], a[124:127], v[220:223], v[4:7]\n"
      "s_add_u32 m0, s74, 0xd000\n"
      "v_mfma_f32_16x16x32_bf16 v[8:11], a[128:131], v[220:223], v[8:11]\n"
      "ds_read_b128 v[216:219], v233 offset:12288\n"
      "global_load_lds_dwordx4 v241, s[78:79]\n"
      "v_mfma_f32_16x16x32_bf16 v[12:15], a[132:135], v[220:223], v[12:15]\n"
      "v_mfma_f32_16x16x32_bf16 v[16:19], a[136:139], v[220:223], v[16:19]\n"
      "v_mfma_f32_16x16x32_bf16 v[20:23], a[140:143], v[220:223], v[20:23]\n"
      "s_add_u32 m0, s74, 0xd400\n"
      "s_waitcnt lgkmcnt(9)\n"
      "v_mfma_f32_16x16x32_bf16 v[24:27], a[120:123], v[224:227], v[24:27]\n"
      "global_load_lds_dwordx4 v242, s[78:79]\n"
      "v_mfma_f32_16x16x32_bf16 v[28:31], a[124:127], v[224:227], v[28:31]\n"
      "v_mfma_f32_16x16x32_bf16 v[32:35], a[128:131], v[224:227], v[32:35]\n"
      "ds_read_b128 v[220:223], v233 offset:16384\n"
      "v_mfma_f32_16x16x32_bf16 v[36:39], a[132:135], v[224:227], v[36:39]\n"
      "s_waitcnt vmcnt(8)\n"
      "v_mad_i32_i24 v238, v251, s89, v238\n"
      "s_add_u32 s56, s22, 0x0\n"
      "s_addc_u32 s57, s23, 0\n"
      "s_add_u32 m0, s18, 0xfffffe00\n"
      "v_mfma_f32_16x16x32_bf16 v[40:43], a[136:139], v[224:227], v[40:43]\n"
      "ds_read_b64_tr_b16 a[0:1], v243 offset:24576\n"
      "global_load_lds_dwordx4 v238, s[56:57] offset:512 \n"
      "v_mfma_f32_16x16x32_bf16 v[44:47], a[140:143], v[224:227], v[44:47]\n"
      "s_waitcnt lgkmcnt(6)\n"
      "v_mfma_f32_16x16x32_bf16 v[48:51], a[120:123], v[228:231], v[48:51]\n"
      "ds_read_b64_tr_b16 a[2:3], v243 offset:25344\n"
      "v_mad_i32_i24 v238, v251, s90, v238\n"
      "s_add_u32 s56, s22, 0x6000\n"
      "s_addc_u32 s57, s23, 0\n"
      "s_add_u32 m0, s18, 0xe00\n"
      "v_mfma_f32_16x16x32_bf16 v[52:55], a[124:127], v[228:231], v[52:55]\n"
      "ds_read_b64_tr_b16 a[4:5], v243 offset:24584\n"
      "global_load_lds_dwordx4 v238, s[56:57] offset:512 \n"
      "v_mfma_f32_16x16x32_bf16 v[56:59], a[128:131], v[228:231], v[56:59]\n"
      "ds_read_b128 v[224:227], v233 offset:20480\n"
      "v_mfma_f32_16x16x32_bf16 v[60:63], a[132:135], v[228:231], v[60:63]\n"
      "ds_read_b64_tr_b16 a[6:7], v243 offset:25352\n"
      "v_mad_i32_i24 v238, v251, s91, v238\n"
      "s_add_u32 s56, s22, 0xc000\n"
      "s_addc_u32 s57, s23, 0\n"
      "s_add_u32 m0, s18, 0x1e00\n"
      "v_mfma_f32_16x16x32_bf16 v[64:67], a[136:139], v[228:231], v[64:67]\n"
      "ds_read_b64_tr_b16 a[8:9], v243 offset:24640\n"
      "global_load_lds_dwordx4 v238, s[56:57] offset:512 \n"
      "v_mfma_f32_16x16x32_bf16 v[68:71], a[140:143], v[228:231], v[68:71]\n"
      "s_waitcnt lgkmcnt(7)\n"
      "v_mfma_f32_16x16x32_bf16 v[72:75], a[120:123], v[216:219], v[72:75]\n"
      "ds_read_b64_tr_b16 a[10:11], v243 offset:25408\n"
      "v_mad_i32_i24 v238, v251, s92, v238\n"
      "s_add_u32 s56, s22, 0x12000\n"
      "s_addc_u32 s57, s23, 0\n"
      "s_add_u32 m0, s18, 0x2e00\n"
      "v_mfma_f32_16x16x32_bf16 v[76:79], a[124:127], v[216:219], v[76:79]\n"
      "ds_read_b64_tr_b16 a[12:13], v243 offset:24648\n"
      "global_load_lds_dwordx4 v238, s[56:57] offset:512 \n"
      "v_mfma_f32_16x16x32_bf16 v[80:83], a[128:131], v[216:219], v[80:83]\n"
      "ds_read_b128 v[228:231], v233 offset:24576\n"
      "v_mfma_f32_16x16x32_bf16 v[84:87], a[132:135], v[216:219], v[84:87]\n"
      "ds_read_b64_tr_b16 a[14:15], v243 offset:25416\n"
      "v_mad_i32_i24 v238, v251, s93, v238\n"
      "s_add_u32 s56, s22, 0x18000\n"
      "s_addc_u32 s57, s23, 0\n"
      "s_add_u32 m0, s18, 0x3e00\n"
      "v_mfma_f32_16x16x32_bf16 v[88:91], a[136:139], v[216:219], v[88:91]\n"
      "ds_read_b64_tr_b16 a[16:17], v243 offset:24704\n"
      "global_load_lds_dwordx4 v238, s[56:57] offset:512 \n"
      "v_mfma_f32_16x16x32_bf16 v[92:95], a[140:143], v[216:219], v[92:95]\n"
      "s_waitcnt lgkmcnt(11)\n"
      "v_mfma_f32_16x16x32_bf16 v[96:99], a[120:123], v[220:223], v[96:99]\n"
      "ds_read_b64_tr_b16 a[18:19], v243 offset:25472\n"
      "v_mad_i32_i24 v238, v251, s94, v238\n"
      "s_add_u32 s56, s22, 0x1e000\n"
      "s_addc_u32 s57, s23, 0\n"
      "s_add_u32 m0, s18, 0x4e00\n"
      "v_mfma_f32_16x16x32_bf16 v[100:103], a[124:127], v[220:223], v[100:103]\n"
      "ds_read_b64_tr_b16 a[20:21], v243 offset:24712\n"
      "global_load_lds_dwordx4 v238, s[56:57] offset:512 \n"
      "v_mfma_f32_16x16x32_bf16 v[104:107], a[128:131], v[220:223], v[104:107]\n"
      "ds_read_b128 v[216:219], v233 offset:28672\n"
      "v_mfma_f32_16x16x32_bf16 v[108:111], a[132:135], v[220:223], v[108:111]\n"
      "ds_read_b64_tr_b16 a[22:23], v243 offset:25480\n"
      "v_mad_i32_i24 v238, v251, s95, v238\n"
      "s_add_u32 s56, s22, 0x24000\n"
      "s_addc_u32 s57, s23, 0\n"
      "s_add_u32 m0, s18, 0x5e00\n"
      "v_mfma_f32_16x16x32_bf16 v[112:115], a[136:139], v[220:223], v[112:115]\n"
      "global_load_lds_dwordx4 v238, s[56:57] offset:512 \n"
      "v_mfma_f32_16x16x32_bf16 v[116:119], a[140:143], v[220:223], v[116:119]\n"
      "s_waitcnt lgkmcnt(11)\n"
      "v_mfma_f32_16x16x32_bf16 v[120:123], a[120:123], v[224:227], v[120:123]\n"
      "v_mad_i32_i24 v238, v251, s96, v238\n"
      "s_add_u32 s56, s22, 0x2a000\n"
      "s_addc_u32 s57, s23, 0\n"
      "s_add_u32 m0, s18, 0x6e00\n"
      "v_mfma_f32_16x16x32_bf16 v[124:127], a[124:127], v[224:227], v[124:127]\n"
      "global_load_lds_dwordx4 v238, s[56:57] offset:512 \n"
      "v_mfma_f32_16x16x32_bf16 v[128:131], a[128:131], v[224:227], v[128:131]\n"
      "ds_read_b128 v[220:223], v233 offset:32768\n"
      "v_add_u32 v233, 0xffff7000, v233\n"
      "v_mfma_f32_16x16x32_bf16 v[132:135], a[132:135], v[224:227], v[132:135]\n"
      "v_mad_i32_i24 v238, v251, s97, v238\n"
      "s_add_u32 s56, s22, 0x30000\n"
      "s_addc_u32 s57, s23, 0\n"
      "s_add_u32 m0, s18, 0x7e00\n"
      "v_mfma_f32_16x16x32_bf16 v[136:139], a[136:139], v[224:227], v[136:139]\n"
      "global_load_lds_dwordx4 v238, s[56:57] offset:512 \n"
      "v_mfma_f32_16x16x32_bf16 v[140:143], a[140:143], v[224:227], v[140:143]\n"
      "s_waitcnt lgkmcnt(7)\n"
      "v_mfma_f32_16x16x32_bf16 v[144:147], a[120:123], v[228:231], v[144:147]\n"
      "v_mfma_f32_16x16x32_bf16 v[148:151], a[124:127], v[228:231], v[148:151]\n"
      "global_load_dwordx4 a[252:255], v239, s[58:59] offset:3072 sc1\n"
      "v_mfma_f32_16x16x32_bf16 v[152:155], a[128:131], v[228:231], v[152:155]\n"
      "ds_read_b128 v[224:227], v234 offset:0\n"
      "v_mfma_f32_16x16x32_bf16 v[156:159], a[132:135], v[228:231], v[156:159]\n"
      "v_mfma_f32_16x16x32_bf16 v[160:163], a[136:139], v[228:231], v[160:163]\n"
      "v_mfma_f32_16x16x32_bf16 v[164:167], a[140:143], v[228:231], v[164:167]\n"
      "s_add_u32 s58, s24, 0x1000\n"
      "s_addc_u32 s59, s25, 0\n"
      "global_load_dwordx4 a[48:51], v239, s[58:59] offset:0 sc1\n"
      "s_waitcnt lgkmcnt(3)\n"
      "v_mfma_f32_16x16x32_bf16 v[168:171], a[120:123], v[216:219], v[168:171]\n"
      "s_waitcnt vmcnt(11)\n"
      "v_mfma_f32_16x16x32_bf16 v[172:175], a[124:127], v[216:219], v[172:175]\n"
      "ds_read_b64_tr_b16 a[24:25], v243 offset:49152\n"
      "v_mfma_f32_16x16x32_bf16 v[176:179], a[128:131], v[216:219], v[176:179]\n"
      "ds_read_b128 v[228:231], v234 offset:4096\n"
      "v_mfma_f32_16x16x32_bf16 v[180:183], a[132:135], v[216:219], v[180:183]\n"
      "ds_read_b64_tr_b16 a[26:27], v243 offset:49920\n"
      "global_load_dwordx4 a[52:55], v239, s[58:59] offset:1024 sc1\n"
      "v_mfma_f32_16x16x32_bf16 v[184:187], a[136:139], v[216:219], v[184:187]\n"
      "ds_read_b64_tr_b16 a[28:29], v243 offset:49160\n"
      "v_mfma_f32_16x16x32_bf16 v[188:191], a[140:143], v[216:219], v[188:191]\n"
      "s_waitcnt lgkmcnt(5)\n"
      "v_mfma_f32_16x16x32_bf16 v[192:195], a[120:123], v[220:223], v[192:195]\n"
      "ds_read_b64_tr_b16 a[30:31], v243 offset:49928\n"
      "v_mfma_f32_16x16x32_bf16 v[196:199], a[124:127], v[220:223], v[196:199]\n"
      "ds_read_b64_tr_b16 a[32:33], v243 offset:49216\n"
      "global_load_dwordx4 a[56:59], v239, s[58:59] offset:2048 sc1\n"
      "v_mfma_f32_16x16x32_bf16 v[200:203], a[128:131], v[220:223], v[200:203]\n"
      "ds_read_b128 v[216:219], v234 offset:8192\n"
      "v_mfma_f32_16x16x32_bf16 v[204:207], a[132:135], v[220:223], v[204:207]\n"
      "ds_read_b64_tr_b16 a[34:35], v243 offset:49984\n"
      "v_mfma_f32_16x16x32_bf16 v[208:211], a[136:139], v[220:223], v[208:211]\n"
      "ds_read_b64_tr_b16 a[36:37], v243 offset:49224\n"
      "v_mfma_f32_16x16x32_bf16 v[212:215], a[140:143], v[220:223], v[212:215]\n"
      "global_load_dwordx4 a[60:63], v239, s[58:59] offset:3072 sc1\n"
      "s_waitcnt lgkmcnt(9)\n"
      "v_mfma_f32_16x16x32_bf16 v[0:3], a[144:147], v[224:227], v[0:3]\n"
      "ds_read_b64_tr_b16 a[38:39], v243 offset:49992\n"
      "v_mfma_f32_16x16x32_bf16 v[4:7], a[148:151], v[224:227], v[4:7]\n"
      "ds_read_b64_tr_b16 a[40:41], v243 offset:49280\n"
      "v_mfma_f32_16x16x32_bf16 v[8:11], a[152:155], v[224:227], v[8:11]\n"
      "ds_read_b128 v[220:223], v234 offset:12288\n"
      "v_mfma_f32_16x16x32_bf16 v[12:15], a[156:159], v[224:227], v[12:15]\n"
      "ds_read_b64_tr_b16 a[42:43], v243 offset:50048\n"
      "s_add_u32 s58, s24, 0x2000\n"
      "s_addc_u32 s59, s25, 0\n"
      "global_load_dwordx4 a[64:67], v239, s[58:59] offset:0 sc1\n"
      "v_mfma_f32_16x16x32_bf16 v[16:19], a[160:163], v[224:227], v[16:19]\n"
      "ds_read_b64_tr_b16 a[44:45], v243 offset:49288\n"
      "v_mfma_f32_16x16x32_bf16 v[20:23], a[164:167], v[224:227], v[20:23]\n"
      "s_waitcnt lgkmcnt(12)\n"
      "v_mfma_f32_16x16x32_bf16 v[24:27], a[144:147], v[228:231], v[24:27]\n"
      "ds_read_b64_tr_b16 a[46:47], v243 offset:50056\n"
      "v_mfma_f32_16x16x32_bf16 v[28:31], a[148:151], v[228:231], v[28:31]\n"
      "global_load_dwordx4 a[68:71], v239, s[58:59] offset:1024 sc1\n"
      "v_mfma_f32_16x16x32_bf16 v[32:35], a[152:155], v[228:231], v[32:35]\n"
      "ds_read_b128 v[224:227], v234 offset:16384\n"
      "v_mfma_f32_16x16x32_bf16 v[36:39], a[156:159], v[228:231], v[36:39]\n"
      "v_mfma_f32_16x16x32_bf16 v[40:43], a[160:163], v[228:231], v[40:43]\n"
      "v_mfma_f32_16x16x32_bf16 v[44:47], a[164:167], v[228:231], v[44:47]\n"
      "global_load_dwordx4 a[72:75], v239, s[58:59] offset:2048 sc1\n"
      "s_waitcnt lgkmcnt(9)\n"
      "v_mfma_f32_16x16x32_bf16 v[48:51], a[144:147], v[216:219], v[48:51]\n"
      "v_mfma_f32_16x16x32_bf16 v[52:55], a[148:151], v[216:219], v[52:55]\n"
      "v_mfma_f32_16x16x32_bf16 v[56:59], a[152:155], v[216:219], v[56:59]\n"
      "ds_read_b128 v[228:231], v234 offset:20480\n"
      "v_mfma_f32_16x16x32_bf16 v[60:63], a[156:159], v[216:219], v[60:63]\n"
      "global_load_dwordx4 a[76:79], v239, s[58:59] offset:3072 sc1\n"
      "v_mfma_f32_16x16x32_bf16 v[64:67], a[160:163], v[216:219], v[64:67]\n"
      "v_mfma_f32_16x16x32_bf16 v[68:71], a[164:167], v[216:219], v[68:71]\n"
      "s_waitcnt lgkmcnt(5)\n"
      "v_mfma_f32_16x16x32_bf16 v[72:75], a[144:147], v[220:223], v[72:75]\n"
      "v_mfma_f32_16x16x32_bf16 v[76:79], a[148:151], v[220:223], v[76:79]\n"
      "s_add_u32 s58, s24, 0x3000\n"
      "s_addc_u32 s59, s25, 0\n"
      "global_load_dwordx4 a[80:83], v239, s[58:59] offset:0 sc1\n"
      "v_mfma_f32_16x16x32_bf16 v[80:83], a[152:155], v[220:223], v[80:83]\n"
      "ds_read_b128 v[216:219], v234 offset:24576\n"
      "v_mfma_f32_16x16x32_bf16 v[84:87], a[156:159], v[220:223], v[84:87]\n"
      "v_mfma_f32_16x16x32_bf16 v[88:91], a[160:163], v[220:223], v[88:91]\n"
      "v_mfma_f32_16x16x32_bf16 v[92:95], a[164:167], v[220:223], v[92:95]\n"
      "global_load_dwordx4 a[84:87], v239, s[58:59] offset:1024 sc1\n"
      "s_waitcnt lgkmcnt(2)\n"
      "v_mfma_f32_16x16x32_bf16 v[96:99], a[144:147], v[224:227], v[96:99]\n"
      "v_mfma_f32_16x16x32_bf16 v[100:103], a[148:151], v[224:227], v[100:103]\n"
      "v_mfma_f32_16x16x32_bf16 v[104:107], a[152:155], v[224:227], v[104:107]\n"
      "ds_read_b128 v[220:223], v234 offset:28672\n"
      "v_mfma_f32_16x16x32_bf16 v[108:111], a[156:159], v[224:227], v[108:111]\n"
      "global_load_dwordx4 a[88:91], v239, s[58:59] offset:2048 sc1\n"
      "v_mfma_f32_16x16x32_bf16 v[112:115], a[160:163], v[224:227], v[112:115]\n"
      "v_mfma_f32_16x16x32_bf16 v[116:119], a[164:167], v[224:227], v[116:119]\n"
      "s_waitcnt lgkmcnt(2)\n"
      "v_mfma_f32_16x16x32_bf16 v[120:123], a[144:147], v[228:231], v[120:123]\n"
      "v_mfma_f32_16x16x32_bf16 v[124:127], a[148:151], v[228:231], v[124:127]\n"
      "global_load_dwordx4 a[92:95], v239, s[58:59] offset:3072 sc1\n"
      "v_mfma_f32_16x16x32_bf16 v[128:131], a[152:155], v[228:231], v[128:131]\n"
      "ds_read_b128 v[224:227], v234 offset:32768\n"
      "v_add_u32 v234, 0xffff7000, v234\n"
      "v_mfma_f32_16x16x32_bf16 v[132:135], a[156:159], v[228:231], v[132:135]\n"
      "v_mfma_f32_16x16x32_bf16 v[136:139], a[160:163], v[228:231], v[136:139]\n"
      "v_mfma_f32_16x16x32_bf16 v[140:143], a[164:167], v[228:231], v[140:143]\n"
      "s_add_u32 s58, s24, 0x4000\n"
      "s_addc_u32 s59, s25, 0\n"
      "global_load_dwordx4 a[96:99], v239, s[58:59] offset:0 sc1\n"
      "s_waitcnt lgkmcnt(2)\n"
      "v_mfma_f32_16x16x32_bf16 v[144:147], a[144:147], v[216:219], v[144:147]\n"
      "ds_read_b64 v[236:237], v250 offset:4104\n"
      "v_mfma_f32_16x16x32_bf16 v[148:151], a[148:151], v[216:219], v[148:151]\n"
      "v_mfma_f32_16x16x32_bf16 v[152:155], a[152:155], v[216:219], v[152:155]\n"
      "ds_read_b128 v[228:231], v235 offset:0\n"
      "v_mfma_f32_16x16x32_bf16 v[156:159], a[156:159], v[216:219], v[156:159]\n"
      "global_load_dwordx4 a[100:103], v239, s[58:59] offset:1024 sc1\n"
      "v_mfma_f32_16x16x32_bf16 v[160:163], a[160:163], v[216:219], v[160:163]\n"
      "v_mfma_f32_16x16x32_bf16 v[164:167], a[164:167], v[216:219], v[164:167]\n"
      "s_waitcnt lgkmcnt(3)\n"
      "v_mfma_f32_16x16x32_bf16 v[168:171], a[144:147], v[220:223], v[168:171]\n"
      "v_mfma_f32_16x16x32_bf16 v[172:175], a[148:151], v[220:223], v[172:175]\n"
      "global_load_dwordx4 a[104:107], v239, s[58:59] offset:2048 sc1\n"
      "v_mfma_f32_16x16x32_bf16 v[176:179], a[152:155], v[220:223], v[176:179]\n"
      "ds_read_b128 v[216:219], v235 offset:4096\n"
      "v_mfma_f32_16x16x32_bf16 v[180:183], a[156:159], v[220:223], v[180:183]\n"
      "v_mfma_f32_16x16x32_bf16 v[184:187], a[160:163], v[220:223], v[184:187]\n"
      "v_mfma_f32_16x16x32_bf16 v[188:191], a[164:167], v[220:223], v[188:191]\n"
      "global_load_dwordx4 a[108:111], v239, s[58:59] offset:3072 sc1\n"
      "s_waitcnt lgkmcnt(3)\n"
      "v_mfma_f32_16x16x32_bf16 v[192:195], a[144:147], v[224:227], v[192:195]\n"
      "v_mfma_f32_16x16x32_bf16 v[196:199], a[148:151], v[224:227], v[196:199]\n"
      "v_mfma_f32_16x16x32_bf16 v[200:203], a[152:155], v[224:227], v[200:203]\n"
      "ds_read_b128 v[220:223], v235 offset:8192\n"
      "v_mfma_f32_16x16x32_bf16 v[204:207], a[156:159], v[224:227], v[204:207]\n"
      "s_add_u32 s58, s24, 0x5000\n"
      "s_addc_u32 s59, s25, 0\n"
      "global_load_dwordx4 a[112:115], v239, s[58:59] offset:0 sc1\n"
      "v_mfma_f32_16x16x32_bf16 v[208:211], a[160:163], v[224:227], v[208:211]\n"
      "v_mfma_f32_16x16x32_bf16 v[212:215], a[164:167], v[224:227], v[212:215]\n"
      "s_waitcnt lgkmcnt(2)\n"
      "v_mfma_f32_16x16x32_bf16 v[0:3], a[168:171], v[228:231], v[0:3]\n"
      "v_mfma_f32_16x16x32_bf16 v[4:7], a[172:175], v[228:231], v[4:7]\n"
      "global_load_dwordx4 a[116:119], v239, s[58:59] offset:1024 sc1\n"
      "v_mfma_f32_16x16x32_bf16 v[8:11], a[176:179], v[228:231], v[8:11]\n"
      "ds_read_b128 v[224:227], v235 offset:12288\n"
      "v_mfma_f32_16x16x32_bf16 v[12:15], a[180:183], v[228:231], v[12:15]\n"
      "v_mfma_f32_16x16x32_bf16 v[16:19], a[184:187], v[228:231], v[16:19]\n"
      "v_mfma_f32_16x16x32_bf16 v[20:23], a[188:191], v[228:231], v[20:23]\n"
      "global_load_dwordx4 a[120:123], v239, s[58:59] offset:2048 sc1\n"
      "s_waitcnt lgkmcnt(2)\n"
      "v_mfma_f32_16x16x32_bf16 v[24:27], a[168:171], v[216:219], v[24:27]\n"
      "v_mfma_f32_16x16x32_bf16 v[28:31], a[172:175], v[216:219], v[28:31]\n"
      "v_mfma_f32_16x16x32_bf16 v[32:35], a[176:179], v[216:219], v[32:35]\n"
      "ds_read_b128 v[228:231], v235 offset:16384\n"
      "v_mfma_f32_16x16x32_bf16 v[36:39], a[180:183], v[216:219], v[36:39]\n"
      "global_load_dwordx4 a[124:127], v239, s[58:59] offset:3072 sc1\n"
      "v_mfma_f32_16x16x32_bf16 v[40:43], a[184:187], v[216:219], v[40:43]\n"
      "v_mfma_f32_16x16x32_bf16 v[44:47], a[188:191], v[216:219], v[44:47]\n"
      "s_waitcnt lgkmcnt(2)\n"
      "v_mfma_f32_16x16x32_bf16 v[48:51], a[168:171], v[220:223], v[48:51]\n"
      "v_mfma_f32_16x16x32_bf16 v[52:55], a[172:175], v[220:223], v[52:55]\n"
      "s_add_u32 s58, s24, 0x6000\n"
      "s_addc_u32 s59, s25, 0\n"
      "global_load_dwordx4 a[128:131], v239, s[58:59] offset:0 sc1\n"
      "v_mfma_f32_16x16x32_bf16 v[56:59], a[176:179], v[220:223], v[56:59]\n"
      "ds_read_b128 v[216:219], v235 offset:20480\n"
      "v_mfma_f32_16x16x32_bf16 v[60:63], a[180:183], v[220:223], v[60:63]\n"
      "v_mfma_f32_16x16x32_bf16 v[64:67], a[184:187], v[220:223], v[64:67]\n"
      "v_mfma_f32_16x16x32_bf16 v[68:71], a[188:191], v[220:223], v[68:71]\n"
      "global_load_dwordx4 a[132:135], v239, s[58:59] offset:1024 sc1\n"
      "s_waitcnt lgkmcnt(2)\n"
      "v_mfma_f32_16x16x32_bf16 v[72:75], a[168:171], v[224:227], v[72:75]\n"
      "v_mfma_f32_16x16x32_bf16 v[76:79], a[172:175], v[224:227], v[76:79]\n"
      "v_mfma_f32_16x16x32_bf16 v[80:83], a[176:179], v[224:227], v[80:83]\n"
      "ds_read_b128 v[220:223], v235 offset:24576\n"
      "v_mfma_f32_16x16x32_bf16 v[84:87], a[180:183], v[224:227], v[84:87]\n"
      "global_load_dwordx4 a[136:139], v239, s[58:59] offset:2048 sc1\n"
      "v_mfma_f32_16x16x32_bf16 v[88:91], a[184:187], v[224:227], v[88:91]\n"
      "v_mfma_f32_16x16x32_bf16 v[92:95], a[188:191], v[224:227], v[92:95]\n"
      "s_waitcnt lgkmcnt(2)\n"
      "v_mfma_f32_16x16x32_bf16 v[96:99], a[168:171], v[228:231], v[96:99]\n"
      "v_mfma_f32_16x16x32_bf16 v[100:103], a[172:175], v[228:231], v[100:103]\n"
      "global_load_dwordx4 a[140:143], v239, s[58:59] offset:3072 sc1\n"
      "v_mfma_f32_16x16x32_bf16 v[104:107], a[176:179], v[228:231], v[104:107]\n"
      "ds_read_b128 v[224:227], v235 offset:28672\n"
      "v_mfma_f32_16x16x32_bf16 v[108:111], a[180:183], v[228:231], v[108:111]\n"
      "v_mfma_f32_16x16x32_bf16 v[112:115], a[184:187], v[228:231], v[112:115]\n"
      "v_mfma_f32_16x16x32_bf16 v[116:119], a[188:191], v[228:231], v[116:119]\n"
      "s_add_u32 s58, s24, 0x7000\n"
      "s_addc_u32 s59, s25, 0\n"
      "global_load_dwordx4 a[144:147], v239, s[58:59] offset:0 sc1\n"
      "s_waitcnt lgkmcnt(2)\n"
      "v_mfma_f32_16x16x32_bf16 v[120:123], a[168:171], v[216:219], v[120:123]\n"
      "v_mfma_f32_16x16x32_bf16 v[124:127], a[172:175], v[216:219], v[124:127]\n"
      "v_mfma_f32_16x16x32_bf16 v[128:131], a[176:179], v[216:219], v[128:131]\n"
      "ds_read_b128 v[228:231], v235 offset:32768\n"
      "v_add_u32 v235, 0xffff7000, v235\n"
      "v_mfma_f32_16x16x32_bf16 v[132:135], a[180:183], v[216:219], v[132:135]\n"
      "global_load_dwordx4 a[148:151], v239, s[58:59] offset:1024 sc1\n"
      "v_mfma_f32_16x16x32_bf16 v[136:139], a[184:187], v[216:219], v[136:139]\n"
      "v_mfma_f32_16x16x32_bf16 v[140:143], a[188:191], v[216:219], v[140:143]\n"
      "s_waitcnt lgkmcnt(2)\n"
      "v_mfma_f32_16x16x32_bf16 v[144:147], a[168:171], v[220:223], v[144:147]\n"
      "v_mfma_f32_16x16x32_bf16 v[148:151], a[172:175], v[220:223], v[148:151]\n"
      "s_waitcnt vmcnt(27)\n"
      "s_barrier\n"
      "global_load_dwordx4 a[152:155], v239, s[58:59] offset:2048 sc1\n"
      "v_mfma_f32_16x16x32_bf16 v[152:155], a[176:179], v[220:223], v[152:155]\n"
      "ds_read_b128 v[216:219], v232 offset:0\n"
      "v_mfma_f32_16x16x32_bf16 v[156:159], a[180:183], v[220:223], v[156:159]\n"
      "v_mfma_f32_16x16x32_bf16 v[160:163], a[184:187], v[220:223], v[160:163]\n"
      "v_mfma_f32_16x16x32_bf16 v[164:167], a[188:191], v[220:223], v[164:167]\n"
      "global_load_dwordx4 a[156:159], v239, s[58:59] offset:3072 sc1\n"
      "s_waitcnt lgkmcnt(2)\n"
      "v_mfma_f32_16x16x32_bf16 v[168:171], a[168:171], v[224:227], v[168:171]\n"
      "v_mfma_f32_16x16x32_bf16 v[172:175], a[172:175], v[224:227], v[172:175]\n"
      "v_mfma_f32_16x16x32_bf16 v[176:179], a[176:179], v[224:227], v[176:179]\n"
      "ds_read_b128 v[220:223], v232 offset:4096\n"
      "v_mfma_f32_16x16x32_bf16 v[180:183], a[180:183], v[224:227], v[180:183]\n"
      "s_add_u32 s58, s24, 0x8000\n"
      "s_addc_u32 s59, s25, 0\n"
      "global_load_dwordx4 a[160:163], v239, s[58:59] offset:0 sc1\n"
      "v_mfma_f32_16x16x32_bf16 v[184:187], a[184:187], v[224:227], v[184:187]\n"
      "v_mfma_f32_16x16x32_bf16 v[188:191], a[188:191], v[224:227], v[188:191]\n"
      "s_waitcnt lgkmcnt(2)\n"
      "v_mfma_f32_16x16x32_bf16 v[192:195], a[168:171], v[228:231], v[192:195]\n"
      "v_mfma_f32_16x16x32_bf16 v[196:199], a[172:175], v[228:231], v[196:199]\n"
      "global_load_dwordx4 a[164:167], v239, s[58:59] offset:1024 sc1\n"
      "v_mfma_f32_16x16x32_bf16 v[200:203], a[176:179], v[228:231], v[200:203]\n"
      "ds_read_b128 v[224:227], v232 offset:8192\n"
      "v_mfma_f32_16x16x32_bf16 v[204:207], a[180:183], v[228:231], v[204:207]\n"
      "v_mfma_f32_16x16x32_bf16 v[208:211], a[184:187], v[228:231], v[208:211]\n"
      "v_mfma_f32_16x16x32_bf16 v[212:215], a[188:191], v[228:231], v[212:215]\n"
      "s_waitcnt lgkmcnt(2)\n"
      "v_mfma_f32_16x16x32_bf16 v[0:3], a[192:195], v[216:219], v[0:3]\n"
      "global_load_dwordx4 a[168:171], v239, s[58:59] offset:2048 sc1\n"
      "v_mfma_f32_16x16x32_bf16 v[4:7], a[196:199], v[216:219], v[4:7]\n"
      "v_mfma_f32_16x16x32_bf16 v[8:11], a[200:203], v[216:219], v[8:11]\n"
      "ds_read_b128 v[228:231], v232 offset:12288\n"
      "v_mfma_f32_16x16x32_bf16 v[12:15], a[204:207], v[216:219], v[12:15]\n"
      "v_mfma_f32_16x16x32_bf16 v[16:19], a[208:211], v[216:219], v[16:19]\n"
      "global_load_dwordx4 a[172:175], v239, s[58:59] offset:3072 sc1\n"
      "v_mfma_f32_16x16x32_bf16 v[20:23], a[212:215], v[216:219], v[20:23]\n"
      "s_waitcnt lgkmcnt(2)\n"
      "v_mfma_f32_16x16x32_bf16 v[24:27], a[192:195], v[220:223], v[24:27]\n"
      "v_mfma_f32_16x16x32_bf16 v[28:31], a[196:199], v[220:223], v[28:31]\n"
      "v_mfma_f32_16x16x32_bf16 v[32:35], a[200:203], v[220:223], v[32:35]\n"
      "ds_read_b128 v[216:219], v232 offset:16384\n"
      "s_add_u32 s58, s24, 0x9000\n"
      "s_addc_u32 s59, s25, 0\n"
      "global_load_dwordx4 a[176:179], v239, s[58:59] offset:0 sc1\n"
      "v_mfma_f32_16x16x32_bf16 v[36:39], a[204:207], v[220:223], v[36:39]\n"
      "v_mfma_f32_16x16x32_bf16 v[40:43], a[208:211], v[220:223], v[40:43]\n"
      "v_mfma_f32_16x16x32_bf16 v[44:47], a[212:215], v[220:223], v[44:47]\n"
      "s_waitcnt lgkmcnt(2)\n"
      "v_mfma_f32_16x16x32_bf16 v[48:51], a[192:195], v[224:227], v[48:51]\n"
      "global_load_dwordx4 a[180:183], v239, s[58:59] offset:1024 sc1\n"
      "v_mfma_f32_16x16x32_bf16 v[52:55], a[196:199], v[224:227], v[52:55]\n"
      "v_mfma_f32_16x16x32_bf16 v[56:59], a[200:203], v[224:227], v[56:59]\n"
      "ds_read_b128 v[220:223], v232 offset:20480\n"
      "v_mfma_f32_16x16x32_bf16 v[60:63], a[204:207], v[224:227], v[60:63]\n"
      "v_mfma_f32_16x16x32_bf16 v[64:67], a[208:211], v[224:227], v[64:67]\n"
      "global_load_dwordx4 a[184:187], v239, s[58:59] offset:2048 sc1\n"
      "v_mfma_f32_16x16x32_bf16 v[68:71], a[212:215], v[224:227], v[68:71]\n"
      "s_waitcnt lgkmcnt(2)\n"
      "v_mfma_f32_16x16x32_bf16 v[72:75], a[192:195], v[228:231], v[72:75]\n"
      "v_mfma_f32_16x16x32_bf16 v[76:79], a[196:199], v[228:231], v[76:79]\n"
      "v_mfma_f32_16x16x32_bf16 v[80:83], a[200:203], v[228:231], v[80:83]\n"
      "ds_read_b128 v[224:227], v232 offset:24576\n"
      "global_load_dwordx4 a[188:191], v239, s[58:59] offset:3072 sc1\n"
      "v_mfma_f32_16x16x32_bf16 v[84:87], a[204:207], v[228:231], v[84:87]\n"
      "v_mfma_f32_16x16x32_bf16 v[88:91], a[208:211], v[228:231], v[88:91]\n"
      "v_mfma_f32_16x16x32_bf16 v[92:95], a[212:215], v[228:231], v[92:95]\n"
      "s_waitcnt lgkmcnt(2)\n"
      "v_mfma_f32_16x16x32_bf16 v[96:99], a[192:195], v[216:219], v[96:99]\n"
      "s_mul_i32 s78, s98, s19\n"
      "v_mfma_f32_16x16x32_bf16 v[100:103], a[196:199], v[216:219], v[100:103]\n"
      "v_add_u32 v236, s78, v236\n"
      "v_mfma_f32_16x16x32_bf16 v[104:107], a[200:203], v[216:219], v[104:107]\n"
      "ds_read_b128 v[228:231], v232 offset:28672\n"
      "s_mul_i32 s78, s78, s99\n"
      "v_mfma_f32_16x16x32_bf16 v[108:111], a[204:207], v[216:219], v[108:111]\n"
      "v_add_u32 v237, s78, v237\n"
      "v_mfma_f32_16x16x32_bf16 v[112:115], a[208:211], v[216:219], v[112:115]\n"
      "s_add_u32 s52, s20, s32\n"
      "s_addc_u32 s53, s21, 0\n"
      "s_add_u32 m0, s17, 0x0\n"
      "v_mfma_f32_16x16x32_bf16 v[116:119], a[212:215], v[216:219], v[116:119]\n"
      "global_load_lds_dwordx4 v236, s[52:53] offset:0 \n"
      "s_waitcnt lgkmcnt(2)\n"
      "v_mfma_f32_16x16x32_bf16 v[120:123], a[192:195], v[220:223], v[120:123]\n"
      "v_mfma_f32_16x16x32_bf16 v[124:127], a[196:199], v[220:223], v[124:127]\n"
      "s_add_u32 m0, s17, 0x1f00\n"
      "v_mfma_f32_16x16x32_bf16 v[128:131], a[200:203], v[220:223], v[128:131]\n"
      "ds_read_b128 v[216:219], v232 offset:32768\n"
      "v_add_u32 v232, v249, v232\n"
      "global_load_lds_dwordx4 v236, s[52:53] offset:256 \n"
      "v_mfma_f32_16x16x32_bf16 v[132:135], a[204:207], v[220:223], v[132:135]\n"
      "v_mfma_f32_16x16x32_bf16 v[136:139], a[208:211], v[220:223], v[136:139]\n"
      "s_add_u32 m0, s17, 0x3e00\n"
      "v_mfma_f32_16x16x32_bf16 v[140:143], a[212:215], v[220:223], v[140:143]\n"
      "global_load_lds_dwordx4 v236, s[52:53] offset:512 \n"
      "s_waitcnt lgkmcnt(2)\n"
      "v_mfma_f32_16x16x32_bf16 v[144:147], a[192:195], v[224:227], v[144:147]\n"
      "v_mfma_f32_16x16x32_bf16 v[148:151], a[196:199], v[224:227], v[148:151]\n"
      "s_add_u32 m0, s17, 0x1000\n"
      "v_mfma_f32_16x16x32_bf16 v[152:155], a[200:203], v[224:227], v[152:155]\n"
      "ds_read_b128 v[220:223], v233 offset:0\n"
      "global_load_lds_dwordx4 v237, s[52:53] offset:0 \n"
      "v_mfma_f32_16x16x32_bf16 v[156:159], a[204:207], v[224:227], v[156:159]\n"
      "v_mfma_f32_16x16x32_bf16 v[160:163], a[208:211], v[224:227], v[160:163]\n"
      "s_add_u32 m0, s17, 0x2f00\n"
      "v_mfma_f32_16x16x32_bf16 v[164:167], a[212:215], v[224:227], v[164:167]\n"
      "global_load_lds_dwordx4 v237, s[52:53] offset:256 \n"
      "s_waitcnt lgkmcnt(2)\n"
      "v_mfma_f32_16x16x32_bf16 v[168:171], a[192:195], v[228:231], v[168:171]\n"
      "v_mfma_f32_16x16x32_bf16 v[172:175], a[196:199], v[228:231], v[172:175]\n"
      "s_add_u32 m0, s17, 0x4e00\n"
      "v_mfma_f32_16x16x32_bf16 v[176:179], a[200:203], v[228:231], v[176:179]\n"
      "ds_read_b128 v[224:227], v233 offset:4096\n"
      "global_load_lds_dwordx4 v237, s[52:53] offset:512 \n"
      "v_mfma_f32_16x16x32_bf16 v[180:183], a[204:207], v[228:231], v[180:183]\n"
      "v_mfma_f32_16x16x32_bf16 v[184:187], a[208:211], v[228:231], v[184:187]\n"
      "v_mfma_f32_16x16x32_bf16 v[188:191], a[212:215], v[228:231], v[188:191]\n"
      "s_waitcnt lgkmcnt(2)\n"
      "v_mfma_f32_16x16x32_bf16 v[192:195], a[192:195], v[216:219], v[192:195]\n"
      "v_mfma_f32_16x16x32_bf16 v[196:199], a[196:199], v[216:219], v[196:199]\n"
      "LPD2:\n"
      "s_cbranch_execz LPD7\n"
      "v_mfma_f32_16x16x32_bf16 v[200:203], a[200:203], v[216:219], v[200:203]\n"
      "ds_read_b128 v[228:231], v233 offset:8192\n"
      "v_mfma_f32_16x16x32_bf16 v[204:207], a[204:207], v[216:219], v[204:207]\n"
      "v_mfma_f32_16x16x32_bf16 v[208:211], a[208:211], v[216:219], v[208:211]\n"
      "v_mfma_f32_16x16x32_bf16 v[212:215], a[212:215], v[216:219], v[212:215]\n"
      "s_waitcnt lgkmcnt(2)\n"
      "v_mfma_f32_16x16x32_bf16 v[0:3], a[216:219], v[220:223], v[0:3]\n"
      "s_add_u32 s58, s24, 0xa000\n"
      "s_addc_u32 s59, s25, 0\n"
      "global_load_dwordx4 a[192:195], v239, s[58:59] offset:0 sc1\n"
      "v_mfma_f32_16x16x32_bf16 v[4:7], a[220:223], v[220:223], v[4:7]\n"
      "v_mfma_f32_16x16x32_bf16 v[8:11], a[224:227], v[220:223], v[8:11]\n"
      "ds_read_b128 v[216:219], v233 offset:12288\n"
      "v_mfma_f32_16x16x32_bf16 v[12:15], a[228:231], v[220:223], v[12:15]\n"
      "v_mfma_f32_16x16x32_bf16 v[16:19], a[232:235], v[220:223], v[16:19]\n"
      "global_load_dwordx4 a[196:199], v239, s[58:59] offset:1024 sc1\n"
      "v_mfma_f32_16x16x32_bf16 v[20:23], a[236:239], v[220:223], v[20:23]\n"
      "s_waitcnt lgkmcnt(2)\n"
      "v_mfma_f32_16x16x32_bf16 v[24:27], a[216:219], v[224:227], v[24:27]\n"
      "v_mfma_f32_16x16x32_bf16 v[28:31], a[220:223], v[224:227], v[28:31]\n"
      "v_mfma_f32_16x16x32_bf16 v[32:35], a[224:227], v[224:227], v[32:35]\n"
      "ds_read_b128 v[220:223], v233 offset:16384\n"
      "global_load_dwordx4 a[200:203], v239, s[58:59] offset:2048 sc1\n"
      "v_mfma_f32_16x16x32_bf16 v[36:39], a[228:231], v[224:227], v[36:39]\n"
      "v_mfma_f32_16x16x32_bf16 v[40:43], a[232:235], v[224:227], v[40:43]\n"
      "v_mfma_f32_16x16x32_bf16 v[44:47], a[236:239], v[224:227], v[44:47]\n"
      "s_waitcnt lgkmcnt(2)\n"
      "v_mfma_f32_16x16x32_bf16 v[48:51], a[216:219], v[228:231], v[48:51]\n"
      "global_load_dwordx4 a[204:207], v239, s[58:59] offset:3072 sc1\n"
      "v_mfma_f32_16x16x32_bf16 v[52:55], a[220:223], v[228:231], v[52:55]\n"
      "v_mfma_f32_16x16x32_bf16 v[56:59], a[224:227], v[228:231], v[56:59]\n"
      "ds_read_b128 v[224:227], v233 offset:20480\n"
      "v_mfma_f32_16x16x32_bf16 v[60:63], a[228:231], v[228:231], v[60:63]\n"
      "v_mfma_f32_16x16x32_bf16 v[64:67], a[232:235], v[228:231], v[64:67]\n"
      "s_add_u32 s58, s24, 0xb000\n"
      "s_addc_u32 s59, s25, 0\n"
      "global_load_dwordx4 a[208:211], v239, s[58:59] offset:0 sc1\n"
      "v_mfma_f32_16x16x32_bf16 v[68:71], a[236:239], v[228:231], v[68:71]\n"
      "s_waitcnt lgkmcnt(2)\n"
      "v_mfma_f32_16x16x32_bf16 v[72:75], a[216:219], v[216:219], v[72:75]\n"
      "v_mfma_f32_16x16x32_bf16 v[76:79], a[220:223], v[216:219], v[76:79]\n"
      "v_mfma_f32_16x16x32_bf16 v[80:83], a[224:227], v[216:219], v[80:83]\n"
      "ds_read_b128 v[228:231], v233 offset:24576\n"
      "global_load_dwordx4 a[212:215], v239, s[58:59] offset:1024 sc1\n"
      "v_mfma_f32_16x16x32_bf16 v[84:87], a[228:231], v[216:219], v[84:87]\n"
      "v_mfma_f32_16x16x32_bf16 v[88:91], a[232:235], v[216:219], v[88:91]\n"
      "v_mfma_f32_16x16x32_bf16 v[92:95], a[236:239], v[216:219], v[92:95]\n"
      "s_waitcnt lgkmcnt(2)\n"
      "v_mfma_f32_16x16x32_bf16 v[96:99], a[216:219], v[220:223], v[96:99]\n"
      "v_mfma_f32_16x16x32_bf16 v[100:103], a[220:223], v[220:223], v[100:103]\n"
      "v_mfma_f32_16x16x32_bf16 v[104:107], a[224:227], v[220:223], v[104:107]\n"
      "ds_read_b128 v[216:219], v233 offset:28672\n"
      "v_mfma_f32_16x16x32_bf16 v[108:111], a[228:231], v[220:223], v[108:111]\n"
      "v_mfma_f32_16x16x32_bf16 v[112:115], a[232:235], v[220:223], v[112:115]\n"
      "v_mfma_f32_16x16x32_bf16 v[116:119], a[236:239], v[220:223], v[116:119]\n"
      "s_waitcnt lgkmcnt(2)\n"
      "v_mfma_f32_16x16x32_bf16 v[120:123], a[216:219], v[224:227], v[120:123]\n"
      "v_mfma_f32_16x16x32_bf16 v[124:127], a[220:223], v[224:227], v[124:127]\n"
      "v_mfma_f32_16x16x32_bf16 v[128:131], a[224:227], v[224:227], v[128:131]\n"
      "ds_read_b128 v[220:223], v233 offset:32768\n"
      "v_add_u32 v233, v249, v233\n"
      "v_mfma_f32_16x16x32_bf16 v[132:135], a[228:231], v[224:227], v[132:135]\n"
      "v_mfma_f32_16x16x32_bf16 v[136:139], a[232:235], v[224:227], v[136:139]\n"
      "v_mfma_f32_16x16x32_bf16 v[140:143], a[236:239], v[224:227], v[140:143]\n"
      "s_waitcnt lgkmcnt(2)\n"
      "v_mfma_f32_16x16x32_bf16 v[144:147], a[216:219], v[228:231], v[144:147]\n"
      "v_mfma_f32_16x16x32_bf16 v[148:151], a[220:223], v[228:231], v[148:151]\n"
      "v_mfma_f32_16x16x32_bf16 v[152:155], a[224:227], v[228:231], v[152:155]\n"
      "ds_read_b128 v[224:227], v234 offset:0\n"
      "v_mfma_f32_16x16x32_bf16 v[156:159], a[228:231], v[228:231], v[156:159]\n"
      "v_mfma_f32_16x16x32_bf16 v[160:163], a[232:235], v[228:231], v[160:163]\n"
      "v_mfma_f32_16x16x32_bf16 v[164:167], a[236:239], v[228:231], v[164:167]\n"
      "s_waitcnt lgkmcnt(2)\n"
      "v_mfma_f32_16x16x32_bf16 v[168:171], a[216:219], v[216:219], v[168:171]\n"
      "v_mfma_f32_16x16x32_bf16 v[172:175], a[220:223], v[216:219], v[172:175]\n"
      "v_mfma_f32_16x16x32_bf16 v[176:179], a[224:227], v[216:219], v[176:179]\n"
      "ds_read_b128 v[228:231], v234 offset:4096\n"
      "v_mfma_f32_16x16x32_bf16 v[180:183], a[228:231], v[216:219], v[180:183]\n"
      "v_mfma_f32_16x16x32_bf16 v[184:187], a[232:235], v[216:219], v[184:187]\n"
      "s_add_u32 s52, s42, s32\n"
      "s_addc_u32 s53, s43, 0\n"
      "s_add_u32 m0, s17, 0x6000\n"
      "v_mfma_f32_16x16x32_bf16 v[188:191], a[236:239], v[216:219], v[188:191]\n"
      "global_load_lds_dwordx4 v236, s[52:53] offset:0 \n"
      "s_waitcnt lgkmcnt(2)\n"
      "v_mfma_f32_16x16x32_bf16 v[192:195], a[216:219], v[220:223], v[192:195]\n"
      "v_mfma_f32_16x16x32_bf16 v[196:199], a[220:223], v[220:223], v[196:199]\n"
      "s_add_u32 m0, s17, 0x7f00\n"
      "v_mfma_f32_16x16x32_bf16 v[200:203], a[224:227], v[220:223], v[200:203]\n"
      "ds_read_b128 v[216:219], v234 offset:8192\n"
      "global_load_lds_dwordx4 v236, s[52:53] offset:256 \n"
      "v_mfma_f32_16x16x32_bf16 v[204:207], a[228:231], v[220:223], v[204:207]\n"
      "v_mfma_f32_16x16x32_bf16 v[208:211], a[232:235], v[220:223], v[208:211]\n"
      "s_add_u32 m0, s17, 0x9e00\n"
      "v_mfma_f32_16x16x32_bf16 v[212:215], a[236:239], v[220:223], v[212:215]\n"
      "global_load_lds_dwordx4 v236, s[52:53] offset:512 \n"
      "s_waitcnt lgkmcnt(2)\n"
      "v_mfma_f32_16x16x32_bf16 v[0:3], a[0:3], v[224:227], v[0:3]\n"
      "ds_read_b128 v[244:247], v250\n"
      "v_mfma_f32_16x16x32_bf16 v[4:7], a[4:7], v[224:227], v[4:7]\n"
      "s_add_u32 m0, s17, 0x7000\n"
      "ds_read_b64 v[252:253], v250 offset:4096\n"
      "v_mfma_f32_16x16x32_bf16 v[8:11], a[8:11], v[224:227], v[8:11]\n"
      "ds_read_b128 v[220:223], v234 offset:12288\n"
      "global_load_lds_dwordx4 v237, s[52:53] offset:0 \n"
      "v_mfma_f32_16x16x32_bf16 v[12:15], a[12:15], v[224:227], v[12:15]\n"
      "v_mfma_f32_16x16x32_bf16 v[16:19], a[16:19], v[224:227], v[16:19]\n"
      "s_add_u32 m0, s17, 0x8f00\n"
      "v_mfma_f32_16x16x32_bf16 v[20:23], a[20:23], v[224:227], v[20:23]\n"
      "global_load_lds_dwordx4 v237, s[52:53] offset:256 \n"
      "s_waitcnt lgkmcnt(4)\n"
      "v_mfma_f32_16x16x32_bf16 v[24:27], a[0:3], v[228:231], v[24:27]\n"
      "v_mfma_f32_16x16x32_bf16 v[28:31], a[4:7], v[228:231], v[28:31]\n"
      "s_add_u32 m0, s17, 0xae00\n"
      "v_mfma_f32_16x16x32_bf16 v[32:35], a[8:11], v[228:231], v[32:35]\n"
      "ds_read_b128 v[224:227], v234 offset:16384\n"
      "global_load_lds_dwordx4 v237, s[52:53] offset:512 \n"
      "v_mfma_f32_16x16x32_bf16 v[36:39], a[12:15], v[228:231], v[36:39]\n"
      "v_mfma_f32_16x16x32_bf16 v[40:43], a[16:19], v[228:231], v[40:43]\n"
      "v_mfma_f32_16x16x32_bf16 v[44:47], a[20:23], v[228:231], v[44:47]\n"
      "global_load_dwordx4 a[216:219], v239, s[58:59] offset:2048 sc1\n"
      "s_waitcnt lgkmcnt(4)\n"
      "v_mfma_f32_16x16x32_bf16 v[48:51], a[0:3], v[216:219], v[48:51]\n"
      "v_mfma_f32_16x16x32_bf16 v[52:55], a[4:7], v[216:219], v[52:55]\n"
      "v_mfma_f32_16x16x32_bf16 v[56:59], a[8:11], v[216:219], v[56:59]\n"
      "ds_read_b128 v[228:231], v234 offset:20480\n"
      "v_mfma_f32_16x16x32_bf16 v[60:63], a[12:15], v[216:219], v[60:63]\n"
      "global_load_dwordx4 a[220:223], v239, s[58:59] offset:3072 sc1\n"
      "v_mfma_f32_16x16x32_bf16 v[64:67], a[16:19], v[216:219], v[64:67]\n"
      "v_mfma_f32_16x16x32_bf16 v[68:71], a[20:23], v[216:219], v[68:71]\n"
      "s_waitcnt lgkmcnt(2)\n"
      "v_mfma_f32_16x16x32_bf16 v[72:75], a[0:3], v[220:223], v[72:75]\n"
      "v_mfma_f32_16x16x32_bf16 v[76:79], a[4:7], v[220:223], v[76:79]\n"
      "s_add_u32 s58, s24, 0xc000\n"
      "s_addc_u32 s59, s25, 0\n"
      "global_load_dwordx4 a[224:227], v239, s[58:59] offset:0 sc1\n"
      "v_mfma_f32_16x16x32_bf16 v[80:83], a[8:11], v[220:223], v[80:83]\n"
      "ds_read_b128 v[216:219], v234 offset:24576\n"
      "v_mfma_f32_16x16x32_bf16 v[84:87], a[12:15], v[220:223], v[84:87]\n"
      "v_mfma_f32_16x16x32_bf16 v[88:91], a[16:19], v[220:223], v[88:91]\n"
      "v_mfma_f32_16x16x32_bf16 v[92:95], a[20:23], v[220:223], v[92:95]\n"
      "global_load_dwordx4 a[228:231], v239, s[58:59] offset:1024 sc1\n"
      "s_waitcnt lgkmcnt(2)\n"
      "v_mfma_f32_16x16x32_bf16 v[96:99], a[0:3], v[224:227], v[96:99]\n"
      "v_mfma_f32_16x16x32_bf16 v[100:103], a[4:7], v[224:227], v[100:103]\n"
      "v_mfma_f32_16x16x32_bf16 v[104:107], a[8:11], v[224:227], v[104:107]\n"
      "ds_read_b128 v[220:223], v234 offset:28672\n"
      "v_mfma_f32_16x16x32_bf16 v[108:111], a[12:15], v[224:227], v[108:111]\n"
      "global_load_dwordx4 a[232:235], v239, s[58:59] offset:2048 sc1\n"
      "v_mfma_f32_16x16x32_bf16 v[112:115], a[16:19], v[224:227], v[112:115]\n"
      "v_mfma_f32_16x16x32_bf16 v[116:119], a[20:23], v[224:227], v[116:119]\n"
      "s_waitcnt lgkmcnt(2)\n"
      "v_mfma_f32_16x16x32_bf16 v[120:123], a[0:3], v[228:231], v[120:123]\n"
      "v_mfma_f32_16x16x32_bf16 v[124:127], a[4:7], v[228:231], v[124:127]\n"
      "global_load_dwordx4 a[236:239], v239, s[58:59] offset:3072 sc1\n"
      "v_mfma_f32_16x16x32_bf16 v[128:131], a[8:11], v[228:231], v[128:131]\n"
      "ds_read_b128 v[224:227], v234 offset:32768\n"
      "v_add_u32 v234, v249, v234\n"
      "v_mfma_f32_16x16x32_bf16 v[132:135], a[12:15], v[228:231], v[132:135]\n"
      "v_mfma_f32_16x16x32_bf16 v[136:139], a[16:19], v[228:231], v[136:139]\n"
      "v_mfma_f32_16x16x32_bf16 v[140:143], a[20:23], v[228:231], v[140:143]\n"
      "s_waitcnt lgkmcnt(2)\n"
      "v_mfma_f32_16x16x32_bf16 v[144:147], a[0:3], v[216:219], v[144:147]\n"
      "v_mfma_f32_16x16x32_bf16 v[148:151], a[4:7], v[216:219], v[148:151]\n"
      "v_mfma_f32_16x16x32_bf16 v[152:155], a[8:11], v[216:219], v[152:155]\n"
      "ds_read_b128 v[228:231], v235 offset:4096\n"
      "v_mfma_f32_16x16x32_bf16 v[156:159], a[12:15], v[216:219], v[156:159]\n"
      "v_mfma_f32_16x16x32_bf16 v[160:163], a[16:19], v[216:219], v[160:163]\n"
      "v_mfma_f32_16x16x32_bf16 v[164:167], a[20:23], v[216:219], v[164:167]\n"
      "s_waitcnt lgkmcnt(2)\n"
      "v_mfma_f32_16x16x32_bf16 v[168:171], a[0:3], v[220:223], v[168:171]\n"
      "v_mfma_f32_16x16x32_bf16 v[172:175], a[4:7], v[220:223], v[172:175]\n"
      "v_mfma_f32_16x16x32_bf16 v[176:179], a[8:11], v[220:223], v[176:179]\n"
      "ds_read_b128 v[216:219], v235 offset:8192\n"
      "v_mfma_f32_16x16x32_bf16 v[180:183], a[12:15], v[220:223], v[180:183]\n"
      "v_mfma_f32_16x16x32_bf16 v[184:187], a[16:19], v[220:223], v[184:187]\n"
      "v_mfma_f32_16x16x32_bf16 v[188:191], a[20:23], v[220:223], v[188:191]\n"
      "s_waitcnt lgkmcnt(2)\n"
      "v_mfma_f32_16x16x32_bf16 v[192:195], a[0:3], v[224:227], v[192:195]\n"
      "v_mfma_f32_16x16x32_bf16 v[196:199], a[4:7], v[224:227], v[196:199]\n"
      "v_mfma_f32_16x16x32_bf16 v[200:203], a[8:11], v[224:227], v[200:203]\n"
      "ds_read_b128 v[220:223], v235 offset:12288\n"
      "v_mfma_f32_16x16x32_bf16 v[204:207], a[12:15], v[224:227], v[204:207]\n"
      "v_mfma_f32_16x16x32_bf16 v[208:211], a[16:19], v[224:227], v[208:211]\n"
      "v_mfma_f32_16x16x32_bf16 v[212:215], a[20:23], v[224:227], v[212:215]\n"
      "s_waitcnt lgkmcnt(2)\n"
      "v_mfma_f32_16x16x32_bf16 v[24:27], a[24:27], v[228:231], v[24:27]\n"
      "s_add_u32 s58, s24, 0xd000\n"
      "s_addc_u32 s59, s25, 0\n"
      "global_load_dwordx4 a[0:3], v239, s[58:59] offset:0 sc1\n"
      "v_mfma_f32_16x16x32_bf16 v[28:31], a[28:31], v[228:231], v[28:31]\n"
      "v_mfma_f32_16x16x32_bf16 v[32:35], a[32:35], v[228:231], v[32:35]\n"
      "ds_read_b128 v[224:227], v235 offset:16384\n"
      "v_mfma_f32_16x16x32_bf16 v[36:39], a[36:39], v[228:231], v[36:39]\n"
      "v_mfma_f32_16x16x32_bf16 v[40:43], a[40:43], v[228:231], v[40:43]\n"
      "global_load_dwordx4 a[4:7], v239, s[58:59] offset:1024 sc1\n"
      "v_mfma_f32_16x16x32_bf16 v[44:47], a[44:47], v[228:231], v[44:47]\n"
      "s_waitcnt lgkmcnt(2)\n"
      "v_mfma_f32_16x16x32_bf16 v[48:51], a[24:27], v[216:219], v[48:51]\n"
      "v_cvt_pk_bf16_f32 v24, v24, v25\n"
      "v_cvt_pk_bf16_f32 v25, v26, v27\n"
      "v_mfma_f32_16x16x32_bf16 v[52:55], a[28:31], v[216:219], v[52:55]\n"
      "v_cvt_pk_bf16_f32 v26, v28, v29\n"
      "v_cvt_pk_bf16_f32 v27, v30, v31\n"
      "v_mfma_f32_16x16x32_bf16 v[56:59], a[32:35], v[216:219], v[56:59]\n"
      "ds_read_b128 v[228:231], v235 offset:20480\n"
      "v_cvt_pk_bf16_f32 v28, v32, v33\n"
      "v_cvt_pk_bf16_f32 v29, v34, v35\n"
      "global_load_dwordx4 a[8:11], v239, s[58:59] offset:2048 sc1\n"
      "v_mfma_f32_16x16x32_bf16 v[60:63], a[36:39], v[216:219], v[60:63]\n"
      "v_cvt_pk_bf16_f32 v30, v36, v37\n"
      "v_cvt_pk_bf16_f32 v31, v38, v39\n"
      "v_mfma_f32_16x16x32_bf16 v[64:67], a[40:43], v[216:219], v[64:67]\n"
      "v_cvt_pk_bf16_f32 v32, v40, v41\n"
      "v_cvt_pk_bf16_f32 v33, v42, v43\n"
      "v_mfma_f32_16x16x32_bf16 v[68:71], a[44:47], v[216:219], v[68:71]\n"
      "v_cvt_pk_bf16_f32 v34, v44, v45\n"
      "v_cvt_pk_bf16_f32 v35, v46, v47\n"
      "s_waitcnt lgkmcnt(2)\n"
      "v_mfma_f32_16x16x32_bf16 v[72:75], a[24:27], v[220:223], v[72:75]\n"
      "v_cvt_pk_bf16_f32 v48, v48, v49\n"
      "v_cvt_pk_bf16_f32 v49, v50, v51\n"
      "global_load_dwordx4 a[12:15], v239, s[58:59] offset:3072 sc1\n"
      "v_mfma_f32_16x16x32_bf16 v[76:79], a[28:31], v[220:223], v[76:79]\n"
      "v_cvt_pk_bf16_f32 v50, v52, v53\n"
      "v_cvt_pk_bf16_f32 v51, v54, v55\n"
      "v_mfma_f32_16x16x32_bf16 v[80:83], a[32:35], v[220:223], v[80:83]\n"
      "ds_read_b128 v[216:219], v235 offset:24576\n"
      "v_cvt_pk_bf16_f32 v52, v56, v57\n"
      "v_cvt_pk_bf16_f32 v53, v58, v59\n"
      "v_mfma_f32_16x16x32_bf16 v[84:87], a[36:39], v[220:223], v[84:87]\n"
      "v_cvt_pk_bf16_f32 v54, v60, v61\n"
      "v_cvt_pk_bf16_f32 v55, v62, v63\n"
      "v_mfma_f32_16x16x32_bf16 v[88:91], a[40:43], v[220:223], v[88:91]\n"
      "v_cvt_pk_bf16_f32 v56, v64, v65\n"
      "v_cvt_pk_bf16_f32 v57, v66, v67\n"
      "s_add_u32 s58, s24, 0xe000\n"
      "s_addc_u32 s59, s25, 0\n"
      "global_load_dwordx4 a[16:19], v239, s[58:59] offset:0 sc1\n"
      "v_mfma_f32_16x16x32_bf16 v[92:95], a[44:47], v[220:223], v[92:95]\n"
      "v_cvt_pk_bf16_f32 v58, v68, v69\n"
      "v_cvt_pk_bf16_f32 v59, v70, v71\n"
      "s_waitcnt lgkmcnt(2)\n"
      "v_mfma_f32_16x16x32_bf16 v[96:99], a[24:27], v[224:227], v[96:99]\n"
      "v_cvt_pk_bf16_f32 v72, v72, v73\n"
      "v_cvt_pk_bf16_f32 v73, v74, v75\n"
      "v_mfma_f32_16x16x32_bf16 v[100:103], a[28:31], v[224:227], v[100:103]\n"
      "v_cvt_pk_bf16_f32 v74, v76, v77\n"
      "v_cvt_pk_bf16_f32 v75, v78, v79\n"
      "v_mfma_f32_16x16x32_bf16 v[104:107], a[32:35], v[224:227], v[104:107]\n"
      "ds_read_b128 v[220:223], v235 offset:28672\n"
      "v_cvt_pk_bf16_f32 v76, v80, v81\n"
      "v_cvt_pk_bf16_f32 v77, v82, v83\n"
      "global_load_dwordx4 a[20:23], v239, s[58:59] offset:1024 sc1\n"
      "v_mfma_f32_16x16x32_bf16 v[108:111], a[36:39], v[224:227], v[108:111]\n"
      "v_cvt_pk_bf16_f32 v78, v84, v85\n"
      "v_cvt_pk_bf16_f32 v79, v86, v87\n"
      "v_mfma_f32_16x16x32_bf16 v[112:115], a[40:43], v[224:227], v[112:115]\n"
      "v_cvt_pk_bf16_f32 v80, v88, v89\n"
      "v_cvt_pk_bf16_f32 v81, v90, v91\n"
      "v_mfma_f32_16x16x32_bf16 v[116:119], a[44:47], v[224:227], v[116:119]\n"
      "v_cvt_pk_bf16_f32 v82, v92, v93\n"
      "v_cvt_pk_bf16_f32 v83, v94, v95\n"
      "s_waitcnt lgkmcnt(2)\n"
      "v_mfma_f32_16x16x32_bf16 v[120:123], a[24:27], v[228:231], v[120:123]\n"
      "v_cvt_pk_bf16_f32 v96, v96, v97\n"
      "v_cvt_pk_bf16_f32 v97, v98, v99\n"
      "v_mfma_f32_16x16x32_bf16 v[124:127], a[28:31], v[228:231], v[124:127]\n"
      "v_cvt_pk_bf16_f32 v98, v100, v101\n"
      "v_cvt_pk_bf16_f32 v99, v102, v103\n"
      "v_mfma_f32_16x16x32_bf16 v[128:131], a[32:35], v[228:231], v[128:131]\n"
      "ds_read_b128 v[224:227], v235 offset:32768\n"
      "v_cvt_pk_bf16_f32 v100, v104, v105\n"
      "v_cvt_pk_bf16_f32 v101, v106, v107\n"
      "v_mfma_f32_16x16x32_bf16 v[132:135], a[36:39], v[228:231], v[132:135]\n"
      "v_cvt_pk_bf16_f32 v102, v108, v109\n"
      "v_cvt_pk_bf16_f32 v103, v110, v111\n"
      "v_mfma_f32_16x16x32_bf16 v[136:139], a[40:43], v[228:231], v[136:139]\n"
      "v_cvt_pk_bf16_f32 v104, v112, v113\n"
      "v_cvt_pk_bf16_f32 v105, v114, v115\n"
      "v_mfma_f32_16x16x32_bf16 v[140:143], a[44:47], v[228:231], v[140:143]\n"
      "v_cvt_pk_bf16_f32 v106, v116, v117\n"
      "v_cvt_pk_bf16_f32 v107, v118, v119\n"
      "s_waitcnt lgkmcnt(2)\n"
      "v_mfma_f32_16x16x32_bf16 v[144:147], a[24:27], v[216:219], v[144:147]\n"
      "v_cvt_pk_bf16_f32 v120, v120, v121\n"
      "v_cvt_pk_bf16_f32 v121, v122, v123\n"
      "v_mfma_f32_16x16x32_bf16 v[148:151], a[28:31], v[216:219], v[148:151]\n"
      "v_cvt_pk_bf16_f32 v122, v124, v125\n"
      "v_cvt_pk_bf16_f32 v123, v126, v127\n"
      "v_mfma_f32_16x16x32_bf16 v[152:155], a[32:35], v[216:219], v[152:155]\n"
      "ds_read_b128 v[228:231], v235 offset:0\n"
      "v_add_u32 v235, v249, v235\n"
      "s_waitcnt vmcnt(24)\n"
      "s_barrier\n"
      "v_cvt_pk_bf16_f32 v124, v128, v129\n"
      "v_cvt_pk_bf16_f32 v125, v130, v131\n"
      "v_mfma_f32_16x16x32_bf16 v[156:159], a[36:39], v[216:219], v[156:159]\n"
      "ds_read_b128 v[36:39], v232 offset:0\n"
      "v_cvt_pk_bf16_f32 v126, v132, v133\n"
      "v_cvt_pk_bf16_f32 v127, v134, v135\n"
      "v_mfma_f32_16x16x32_bf16 v[160:163], a[40:43], v[216:219], v[160:163]\n"
      "v_cvt_pk_bf16_f32 v128, v136, v137\n"
      "v_cvt_pk_bf16_f32 v129, v138, v139\n"
      "v_mfma_f32_16x16x32_bf16 v[164:167], a[44:47], v[216:219], v[164:167]\n"
      "ds_read_b128 v[40:43], v233 offset:0\n"
      "v_cvt_pk_bf16_f32 v130, v140, v141\n"
      "v_cvt_pk_bf16_f32 v131, v142, v143\n"
      "s_waitcnt lgkmcnt(4)\n"
      "v_mfma_f32_16x16x32_bf16 v[168:171], a[24:27], v[220:223], v[168:171]\n"
      "v_cvt_pk_bf16_f32 v144, v144, v145\n"
      "v_cvt_pk_bf16_f32 v145, v146, v147\n"
      "global_load_dwordx4 v[140:143], v239, s[58:59] offset:2048 sc1\n"
      "v_mfma_f32_16x16x32_bf16 v[172:175], a[28:31], v[220:223], v[172:175]\n"
      "ds_read_b128 v[44:47], v234 offset:0\n"
      "v_cvt_pk_bf16_f32 v146, v148, v149\n"
      "v_cvt_pk_bf16_f32 v147, v150, v151\n"
      "v_mfma_f32_16x16x32_bf16 v[176:179], a[32:35], v[220:223], v[176:179]\n"
      "v_cvt_pk_bf16_f32 v148, v152, v153\n"
      "v_cvt_pk_bf16_f32 v149, v154, v155\n"
      "v_mfma_f32_16x16x32_bf16 v[180:183], a[36:39], v[220:223], v[180:183]\n"
      "ds_read_b128 v[60:63], v235 offset:0\n"
      "v_cvt_pk_bf16_f32 v150, v156, v157\n"
      "v_cvt_pk_bf16_f32 v151, v158, v159\n"
      "v_mfma_f32_16x16x32_bf16 v[184:187], a[40:43], v[220:223], v[184:187]\n"
      "v_cvt_pk_bf16_f32 v152, v160, v161\n"
      "v_cvt_pk_bf16_f32 v153, v162, v163\n"
      "v_mfma_f32_16x16x32_bf16 v[188:191], a[44:47], v[220:223], v[188:191]\n"
      "ds_read_b128 v[64:67], v232 offset:8192\n"
      "v_cvt_pk_bf16_f32 v154, v164, v165\n"
      "v_cvt_pk_bf16_f32 v155, v166, v167\n"
      "s_waitcnt lgkmcnt(6)\n"
      "v_mfma_f32_16x16x32_bf16 v[192:195], a[24:27], v[224:227], v[192:195]\n"
      "v_cvt_pk_bf16_f32 v168, v168, v169\n"
      "v_cvt_pk_bf16_f32 v169, v170, v171\n"
      "global_load_dwordx4 v[156:159], v239, s[58:59] offset:3072 sc1\n"
      "v_mfma_f32_16x16x32_bf16 v[196:199], a[28:31], v[224:227], v[196:199]\n"
      "ds_read_b128 v[68:71], v233 offset:8192\n"
      "v_cvt_pk_bf16_f32 v170, v172, v173\n"
      "v_cvt_pk_bf16_f32 v171, v174, v175\n"
      "v_mfma_f32_16x16x32_bf16 v[200:203], a[32:35], v[224:227], v[200:203]\n"
      "v_cvt_pk_bf16_f32 v172, v176, v177\n"
      "v_cvt_pk_bf16_f32 v173, v178, v179\n"
      "v_mfma_f32_16x16x32_bf16 v[204:207], a[36:39], v[224:227], v[204:207]\n"
      "ds_read_b128 v[84:87], v234 offset:8192\n"
      "v_cvt_pk_bf16_f32 v174, v180, v181\n"
      "v_cvt_pk_bf16_f32 v175, v182, v183\n"
      "v_mfma_f32_16x16x32_bf16 v[208:211], a[40:43], v[224:227], v[208:211]\n"
      "v_cvt_pk_bf16_f32 v176, v184, v185\n"
      "v_cvt_pk_bf16_f32 v177, v186, v187\n"
      "s_add_u32 s58, s24, 0xf000\n"
      "s_addc_u32 s59, s25, 0\n"
      "global_load_dwordx4 v[160:163], v239, s[58:59] offset:0 sc1\n"
      "v_mfma_f32_16x16x32_bf16 v[212:215], a[44:47], v[224:227], v[212:215]\n"
      "ds_read_b128 v[88:91], v235 offset:8192\n"
      "v_cvt_pk_bf16_f32 v178, v188, v189\n"
      "v_cvt_pk_bf16_f32 v179, v190, v191\n"
      "s_waitcnt lgkmcnt(8)\n"
      "v_mfma_f32_16x16x32_bf16 v[0:3], a[24:27], v[228:231], v[0:3]\n"
      "v_cvt_pk_bf16_f32 v192, v192, v193\n"
      "v_cvt_pk_bf16_f32 v193, v194, v195\n"
      "v_mfma_f32_16x16x32_bf16 v[4:7], a[28:31], v[228:231], v[4:7]\n"
      "ds_read_b128 v[92:95], v232 offset:16384\n"
      "v_cvt_pk_bf16_f32 v194, v196, v197\n"
      "v_cvt_pk_bf16_f32 v195, v198, v199\n"
      "v_mfma_f32_16x16x32_bf16 v[8:11], a[32:35], v[228:231], v[8:11]\n"
      "v_cvt_pk_bf16_f32 v196, v200, v201\n"
      "v_cvt_pk_bf16_f32 v197, v202, v203\n"
      "global_load_dwordx4 v[164:167], v239, s[58:59] offset:1024 sc1\n"
      "v_mfma_f32_16x16x32_bf16 v[12:15], a[36:39], v[228:231], v[12:15]\n"
      "ds_read_b128 v[108:111], v233 offset:16384\n"
      "v_cvt_pk_bf16_f32 v198, v204, v205\n"
      "v_cvt_pk_bf16_f32 v199, v206, v207\n"
      "v_mfma_f32_16x16x32_bf16 v[16:19], a[40:43], v[228:231], v[16:19]\n"
      "ds_read_b128 v[112:115], v234 offset:16384\n"
      "v_cvt_pk_bf16_f32 v200, v208, v209\n"
      "v_cvt_pk_bf16_f32 v201, v210, v211\n"
      "v_mfma_f32_16x16x32_bf16 v[20:23], a[44:47], v[228:231], v[20:23]\n"
      "ds_read_b128 v[116:119], v235 offset:16384\n"
      "v_cvt_pk_bf16_f32 v202, v212, v213\n"
      "v_cvt_pk_bf16_f32 v203, v214, v215\n"
      "s_waitcnt lgkmcnt(8)\n"
      "v_mfma_f32_16x16x32_bf16 v[132:135], a[240:243], v[36:39], v[0:3]\n"
      "global_load_dwordx4 v[180:183], v239, s[58:59] offset:2048 sc1\n"
      "s_mov_b64 vcc, 0\n"
      "v_mfma_f32_16x16x32_bf16 v[132:135], a[244:247], v[40:43], v[132:135]\n"
      "v_mfma_f32_16x16x32_bf16 v[132:135], a[248:251], v[44:47], v[132:135]\n"
      "v_mfma_f32_16x16x32_bf16 v[132:135], a[252:255], v[60:63], v[132:135]\n"
      "global_load_dwordx4 v[184:187], v239, s[58:59] offset:3072 sc1\n"
      "s_waitcnt lgkmcnt(4)\n"
      "v_mfma_f32_16x16x32_bf16 v[132:135], a[48:51], v[64:67], v[132:135]\n"
      "v_mfma_f32_16x16x32_bf16 v[132:135], a[52:55], v[68:71], v[132:135]\n"
      "v_mfma_f32_16x16x32_bf16 v[132:135], a[56:59], v[84:87], v[132:135]\n"
      "s_add_u32 s58, s24, 0x10000\n"
      "s_addc_u32 s59, s25, 0\n"
      "global_load_dwordx4 v[188:191], v239, s[58:59] offset:0 sc1\n"
      "v_mfma_f32_16x16x32_bf16 v[132:135], a[60:63], v[88:91], v[132:135]\n"
      "s_waitcnt lgkmcnt(0)\n"
      "v_mfma_f32_16x16x32_bf16 v[132:135], a[64:67], v[92:95], v[132:135]\n"
      "v_mfma_f32_16x16x32_bf16 v[132:135], a[68:71], v[108:111], v[132:135]\n"
      "global_load_dwordx4 v[204:207], v239, s[58:59] offset:1024 sc1\n"
      "v_mfma_f32_16x16x32_bf16 v[132:135], a[72:75], v[112:115], v[132:135]\n"
      "v_mfma_f32_16x16x32_bf16 v[132:135], a[76:79], v[116:119], v[132:135]\n"
      "v_mfma_f32_16x16x32_bf16 v[136:139], a[80:83], v[36:39], v[4:7]\n"
      "global_load_dwordx4 a[24:27], v239, s[58:59] offset:2048 sc1\n"
      "v_mfma_f32_16x16x32_bf16 v[136:139], a[84:87], v[40:43], v[136:139]\n"
      "v_mfma_f32_16x16x32_bf16 v[136:139], a[88:91], v[44:47], v[136:139]\n"
      "v_mfma_f32_16x16x32_bf16 v[136:139], a[92:95], v[60:63], v[136:139]\n"
      "global_load_dwordx4 a[28:31], v239, s[58:59] offset:3072 sc1\n"
      "v_mfma_f32_16x16x32_bf16 v[136:139], a[96:99], v[64:67], v[136:139]\n"
      "v_cvt_pk_bf16_f32 v208, v132, v133\n"
      "v_mfma_f32_16x16x32_bf16 v[136:139], a[100:103], v[68:71], v[136:139]\n"
      "v_cvt_pk_bf16_f32 v209, v134, v135\n"
      "v_mfma_f32_16x16x32_bf16 v[136:139], a[104:107], v[84:87], v[136:139]\n"
      "s_add_u32 s58, s24, 0x11000\n"
      "s_addc_u32 s59, s25, 0\n"
      "global_load_dwordx4 a[32:35], v239, s[58:59] offset:0 sc1\n"
      "v_mfma_f32_16x16x32_bf16 v[136:139], a[108:111], v[88:91], v[136:139]\n"
      "v_mfma_f32_16x16x32_bf16 v[136:139], a[112:115], v[92:95], v[136:139]\n"
      "v_mfma_f32_16x16x32_bf16 v[136:139], a[116:119], v[108:111], v[136:139]\n"
      "global_load_dwordx4 a[36:39], v239, s[58:59] offset:1024 sc1\n"
      "v_mfma_f32_16x16x32_bf16 v[136:139], a[120:123], v[112:115], v[136:139]\n"
      "ds_write_b64 v244, v[208:209] offset:12288\n"
      "v_mfma_f32_16x16x32_bf16 v[136:139], a[124:127], v[116:119], v[136:139]\n"
      "v_mfma_f32_16x16x32_bf16 v[132:135], a[128:131], v[36:39], v[8:11]\n"
      "global_load_dwordx4 a[40:43], v239, s[58:59] offset:2048 sc1\n"
      "v_mfma_f32_16x16x32_bf16 v[132:135], a[132:135], v[40:43], v[132:135]\n"
      "v_mfma_f32_16x16x32_bf16 v[132:135], a[136:139], v[44:47], v[132:135]\n"
      "v_mfma_f32_16x16x32_bf16 v[132:135], a[140:143], v[60:63], v[132:135]\n"
      "global_load_dwordx4 a[44:47], v239, s[58:59] offset:3072 sc1\n"
      "v_mfma_f32_16x16x32_bf16 v[132:135], a[144:147], v[64:67], v[132:135]\n"
      "v_cvt_pk_bf16_f32 v210, v136, v137\n"
      "v_mfma_f32_16x16x32_bf16 v[132:135], a[148:151], v[68:71], v[132:135]\n"
      "v_cvt_pk_bf16_f32 v211, v138, v139\n"
      "v_mfma_f32_16x16x32_bf16 v[132:135], a[152:155], v[84:87], v[132:135]\n"
      "s_mul_i32 s78, s90, s19\n"
      "v_mfma_f32_16x16x32_bf16 v[132:135], a[156:159], v[88:91], v[132:135]\n"
      "v_add_u32 v236, s78, v236\n"
      "v_mfma_f32_16x16x32_bf16 v[132:135], a[160:163], v[92:95], v[132:135]\n"
      "s_mul_i32 s78, s78, s99\n"
      "v_mfma_f32_16x16x32_bf16 v[132:135], a[164:167], v[108:111], v[132:135]\n"
      "v_add_u32 v237, s78, v237\n"
      "v_mfma_f32_16x16x32_bf16 v[132:135], a[168:171], v[112:115], v[132:135]\n"
      "s_add_u32 s52, s20, s33\n"
      "s_addc_u32 s53, s21, 0\n"
      "s_add_u32 m0, s17, 0xc000\n"
      "v_mfma_f32_16x16x32_bf16 v[132:135], a[172:175], v[116:119], v[132:135]\n"
      "global_load_lds_dwordx4 v236, s[52:53] offset:0 \n"
      "v_mfma_f32_16x16x32_bf16 v[136:139], a[176:179], v[36:39], v[12:15]\n"
      "ds_write_b64 v244, v[210:211] offset:12296\n"
      "v_mfma_f32_16x16x32_bf16 v[136:139], a[180:183], v[40:43], v[136:139]\n"
      "v_mfma_f32_16x16x32_bf16 v[136:139], a[184:187], v[44:47], v[136:139]\n"
      "v_mfma_f32_16x16x32_bf16 v[136:139], a[188:191], v[60:63], v[136:139]\n"
      "v_cvt_pk_bf16_f32 v212, v132, v133\n"
      "s_waitcnt vmcnt(35)\n"
      "v_mfma_f32_16x16x32_bf16 v[136:139], a[192:195], v[64:67], v[136:139]\n"
      "s_add_u32 m0, s17, 0xdf00\n"
      "v_mfma_f32_16x16x32_bf16 v[136:139], a[196:199], v[68:71], v[136:139]\n"
      "global_load_lds_dwordx4 v236, s[52:53] offset:256 \n"
      "v_mfma_f32_16x16x32_bf16 v[136:139], a[200:203], v[84:87], v[136:139]\n"
      "v_cvt_pk_bf16_f32 v213, v134, v135\n"
      "v_mfma_f32_16x16x32_bf16 v[136:139], a[204:207], v[88:91], v[136:139]\n"
      "s_waitcnt vmcnt(26)\n"
      "v_mfma_f32_16x16x32_bf16 v[136:139], a[208:211], v[92:95], v[136:139]\n"
      "v_mfma_f32_16x16x32_bf16 v[136:139], a[212:215], v[108:111], v[136:139]\n"
      "v_mfma_f32_16x16x32_bf16 v[136:139], a[216:219], v[112:115], v[136:139]\n"
      "s_add_u32 m0, s17, 0xfe00\n"
      "v_mfma_f32_16x16x32_bf16 v[136:139], a[220:223], v[116:119], v[136:139]\n"
      "global_load_lds_dwordx4 v236, s[52:53] offset:512 \n"
      "s_waitcnt vmcnt(23)\n"
      "v_mfma_f32_16x16x32_bf16 v[132:135], a[224:227], v[36:39], v[16:19]\n"
      "ds_write_b64 v245, v[212:213] offset:12288\n"
      "v_mfma_f32_16x16x32_bf16 v[132:135], a[228:231], v[40:43], v[132:135]\n"
      "v_mfma_f32_16x16x32_bf16 v[132:135], a[232:235], v[44:47], v[132:135]\n"
      "v_mfma_f32_16x16x32_bf16 v[132:135], a[236:239], v[60:63], v[132:135]\n"
      "v_cvt_pk_bf16_f32 v214, v136, v137\n"
      "s_waitcnt vmcnt(19)\n"
      "v_mfma_f32_16x16x32_bf16 v[132:135], a[0:3], v[64:67], v[132:135]\n"
      "s_add_u32 m0, s17, 0xd000\n"
      "v_mfma_f32_16x16x32_bf16 v[132:135], a[4:7], v[68:71], v[132:135]\n"
      "global_load_lds_dwordx4 v237, s[52:53] offset:0 \n"
      "v_mfma_f32_16x16x32_bf16 v[132:135], a[8:11], v[84:87], v[132:135]\n"
      "v_cvt_pk_bf16_f32 v215, v138, v139\n"
      "v_mfma_f32_16x16x32_bf16 v[132:135], a[12:15], v[88:91], v[132:135]\n"
      "s_waitcnt vmcnt(16)\n"
      "v_mfma_f32_16x16x32_bf16 v[132:135], a[16:19], v[92:95], v[132:135]\n"
      "v_mfma_f32_16x16x32_bf16 v[132:135], a[20:23], v[108:111], v[132:135]\n"
      "v_mfma_f32_16x16x32_bf16 v[132:135], v[140:143], v[112:115], v[132:135]\n"
      "s_add_u32 m0, s17, 0xef00\n"
      "v_mfma_f32_16x16x32_bf16 v[132:135], v[156:159], v[116:119], v[132:135]\n"
      "s_barrier\n"
      "global_load_lds_dwordx4 v237, s[52:53] offset:256 \n"
      "s_waitcnt vmcnt(13)\n"
      "v_mfma_f32_16x16x32_bf16 v[136:139], v[160:163], v[36:39], v[20:23]\n"
      "ds_read_b128 v[36:39], v232 offset:256\n"
      "ds_write_b64 v245, v[214:215] offset:12296\n"
      "v_mfma_f32_16x16x32_bf16 v[136:139], v[164:167], v[40:43], v[136:139]\n"
      "ds_read_b128 v[40:43], v233 offset:256\n"
      "v_mfma_f32_16x16x32_bf16 v[136:139], v[180:183], v[44:47], v[136:139]\n"
      "ds_read_b128 v[44:47], v234 offset:256\n"
      "v_mfma_f32_16x16x32_bf16 v[136:139], v[184:187], v[60:63], v[136:139]\n"
      "ds_read_b128 v[60:63], v235 offset:256\n"
      "v_cvt_pk_bf16_f32 v208, v132, v133\n"
      "s_waitcnt vmcnt(9)\n"
      "v_mfma_f32_16x16x32_bf16 v[136:139], v[188:191], v[64:67], v[136:139]\n"
      "ds_read_b128 v[64:67], v232 offset:8448\n"
      "s_add_u32 m0, s17, 0x10e00\n"
      "v_mfma_f32_16x16x32_bf16 v[136:139], v[204:207], v[68:71], v[136:139]\n"
      "ds_read_b128 v[68:71], v233 offset:8448\n"
      "global_load_lds_dwordx4 v237, s[52:53] offset:512 \n"
      "v_mfma_f32_16x16x32_bf16 v[136:139], a[24:27], v[84:87], v[136:139]\n"
      "ds_read_b128 v[84:87], v234 offset:8448\n"
      "v_cvt_pk_bf16_f32 v209, v134, v135\n"
      "v_mfma_f32_16x16x32_bf16 v[136:139], a[28:31], v[88:91], v[136:139]\n"
      "ds_read_b128 v[88:91], v235 offset:8448\n"
      "s_waitcnt vmcnt(6)\n"
      "v_mfma_f32_16x16x32_bf16 v[136:139], a[32:35], v[92:95], v[136:139]\n"
      "ds_read_b128 v[92:95], v232 offset:16640\n"
      "v_mfma_f32_16x16x32_bf16 v[136:139], a[36:39], v[108:111], v[136:139]\n"
      "ds_read_b128 v[108:111], v233 offset:16640\n"
      "v_mfma_f32_16x16x32_bf16 v[136:139], a[40:43], v[112:115], v[136:139]\n"
      "ds_read_b128 v[112:115], v234 offset:16640\n"
      "v_mfma_f32_16x16x32_bf16 v[136:139], a[44:47], v[116:119], v[136:139]\n"
      "ds_read_b128 v[116:119], v235 offset:16640\n"
      "s_waitcnt lgkmcnt(8)\n"
      "v_mfma_f32_16x16x32_bf16 v[132:135], a[240:243], v[36:39], v[0:3]\n"
      "s_add_u32 s52, s42, s33\n"
      "s_addc_u32 s53, s43, 0\n"
      "s_add_u32 m0, s17, 0x12000\n"
      "v_mfma_f32_16x16x32_bf16 v[132:135], a[244:247], v[40:43], v[132:135]\n"
      "global_load_lds_dwordx4 v236, s[52:53] offset:0 \n"
      "v_mfma_f32_16x16x32_bf16 v[132:135], a[248:251], v[44:47], v[132:135]\n"
      "ds_write_b64 v244, v[208:209] offset:12416\n"
      "v_mfma_f32_16x16x32_bf16 v[132:135], a[252:255], v[60:63], v[132:135]\n"
      "v_cvt_pk_bf16_f32 v210, v136, v137\n"
      "s_waitcnt lgkmcnt(5)\n"
      "v_mfma_f32_16x16x32_bf16 v[132:135], a[48:51], v[64:67], v[132:135]\n"
      "v_cvt_pk_bf16_f32 v211, v138, v139\n"
      "v_mfma_f32_16x16x32_bf16 v[132:135], a[52:55], v[68:71], v[132:135]\n"
      "v_mfma_f32_16x16x32_bf16 v[132:135], a[56:59], v[84:87], v[132:135]\n"
      "s_add_u32 m0, s17, 0x13f00\n"
      "v_mfma_f32_16x16x32_bf16 v[132:135], a[60:63], v[88:91], v[132:135]\n"
      "global_load_lds_dwordx4 v236, s[52:53] offset:256 \n"
      "s_waitcnt lgkmcnt(1)\n"
      "LPD3:\n"
      "s_cbranch_execz LPD9\n"
      "v_mfma_f32_16x16x32_bf16 v[132:135], a[64:67], v[92:95], v[132:135]\n"
      "v_mfma_f32_16x16x32_bf16 v[132:135], a[68:71], v[108:111], v[132:135]\n"
      "ds_write_b64 v244, v[210:211] offset:12424\n"
      "v_mfma_f32_16x16x32_bf16 v[132:135], a[72:75], v[112:115], v[132:135]\n"
      "v_mfma_f32_16x16x32_bf16 v[132:135], a[76:79], v[116:119], v[132:135]\n"
      "v_mfma_f32_16x16x32_bf16 v[136:139], a[80:83], v[36:39], v[4:7]\n"
      "s_add_u32 m0, s17, 0x15e00\n"
      "v_mfma_f32_16x16x32_bf16 v[136:139], a[84:87], v[40:43], v[136:139]\n"
      "global_load_lds_dwordx4 v236, s[52:53] offset:512 \n"
      "v_mfma_f32_16x16x32_bf16 v[136:139], a[88:91], v[44:47], v[136:139]\n"
      "v_mfma_f32_16x16x32_bf16 v[136:139], a[92:95], v[60:63], v[136:139]\n"
      "v_cvt_pk_bf16_f32 v208, v132, v133\n"
      "v_mfma_f32_16x16x32_bf16 v[136:139], a[96:99], v[64:67], v[136:139]\n"
      "v_cvt_pk_bf16_f32 v209, v134, v135\n"
      "v_mfma_f32_16x16x32_bf16 v[136:139], a[100:103], v[68:71], v[136:139]\n"
      "v_mfma_f32_16x16x32_bf16 v[136:139], a[104:107], v[84:87], v[136:139]\n"
      "s_add_u32 m0, s17, 0x13000\n"
      "v_mfma_f32_16x16x32_bf16 v[136:139], a[108:111], v[88:91], v[136:139]\n"
      "global_load_lds_dwordx4 v237, s[52:53] offset:0 \n"
      "v_mfma_f32_16x16x32_bf16 v[136:139], a[112:115], v[92:95], v[136:139]\n"
      "v_mfma_f32_16x16x32_bf16 v[136:139], a[116:119], v[108:111], v[136:139]\n"
      "ds_write_b64 v244, v[208:209] offset:24576\n"
      "v_mfma_f32_16x16x32_bf16 v[136:139], a[120:123], v[112:115], v[136:139]\n"
      "v_mfma_f32_16x16x32_bf16 v[136:139], a[124:127], v[116:119], v[136:139]\n"
      "v_mfma_f32_16x16x32_bf16 v[132:135], a[128:131], v[36:39], v[8:11]\n"
      "s_add_u32 m0, s17, 0x14f00\n"
      "v_mfma_f32_16x16x32_bf16 v[132:135], a[132:135], v[40:43], v[132:135]\n"
      "global_load_lds_dwordx4 v237, s[52:53] offset:256 \n"
      "v_mfma_f32_16x16x32_bf16 v[132:135], a[136:139], v[44:47], v[132:135]\n"
      "v_mfma_f32_16x16x32_bf16 v[132:135], a[140:143], v[60:63], v[132:135]\n"
      "v_cvt_pk_bf16_f32 v210, v136, v137\n"
      "v_mfma_f32_16x16x32_bf16 v[132:135], a[144:147], v[64:67], v[132:135]\n"
      "v_cvt_pk_bf16_f32 v211, v138, v139\n"
      "v_mfma_f32_16x16x32_bf16 v[132:135], a[148:151], v[68:71], v[132:135]\n"
      "v_mfma_f32_16x16x32_bf16 v[132:135], a[152:155], v[84:87], v[132:135]\n"
      "s_add_u32 m0, s17, 0x16e00\n"
      "v_mfma_f32_16x16x32_bf16 v[132:135], a[156:159], v[88:91], v[132:135]\n"
      "global_load_lds_dwordx4 v237, s[52:53] offset:512 \n"
      "v_mfma_f32_16x16x32_bf16 v[132:135], a[160:163], v[92:95], v[132:135]\n"
      "v_mfma_f32_16x16x32_bf16 v[132:135], a[164:167], v[108:111], v[132:135]\n"
      "ds_write_b64 v244, v[210:211] offset:24584\n"
      "v_mfma_f32_16x16x32_bf16 v[132:135], a[168:171], v[112:115], v[132:135]\n"
      "v_mfma_f32_16x16x32_bf16 v[132:135], a[172:175], v[116:119], v[132:135]\n"
      "v_mfma_f32_16x16x32_bf16 v[136:139], a[176:179], v[36:39], v[12:15]\n"
      "v_mfma_f32_16x16x32_bf16 v[136:139], a[180:183], v[40:43], v[136:139]\n"
      "v_mfma_f32_16x16x32_bf16 v[136:139], a[184:187], v[44:47], v[136:139]\n"
      "v_mfma_f32_16x16x32_bf16 v[136:139], a[188:191], v[60:63], v[136:139]\n"
      "v_cvt_pk_bf16_f32 v212, v132, v133\n"
      "v_mfma_f32_16x16x32_bf16 v[136:139], a[192:195], v[64:67], v[136:139]\n"
      "v_cvt_pk_bf16_f32 v213, v134, v135\n"
      "v_mfma_f32_16x16x32_bf16 v[136:139], a[196:199], v[68:71], v[136:139]\n"
      "v_mfma_f32_16x16x32_bf16 v[136:139], a[200:203], v[84:87], v[136:139]\n"
      "v_mfma_f32_16x16x32_bf16 v[136:139], a[204:207], v[88:91], v[136:139]\n"
      "v_mfma_f32_16x16x32_bf16 v[136:139], a[208:211], v[92:95], v[136:139]\n"
      "v_mfma_f32_16x16x32_bf16 v[136:139], a[212:215], v[108:111], v[136:139]\n"
      "ds_write_b64 v245, v[212:213] offset:24576\n"
      "v_mfma_f32_16x16x32_bf16 v[136:139], a[216:219], v[112:115], v[136:139]\n"
      "v_mfma_f32_16x16x32_bf16 v[136:139], a[220:223], v[116:119], v[136:139]\n"
      "v_mfma_f32_16x16x32_bf16 v[132:135], a[224:227], v[36:39], v[16:19]\n"
      "v_mfma_f32_16x16x32_bf16 v[132:135], a[228:231], v[40:43], v[132:135]\n"
      "v_mfma_f32_16x16x32_bf16 v[132:135], a[232:235], v[44:47], v[132:135]\n"
      "v_mfma_f32_16x16x32_bf16 v[132:135], a[236:239], v[60:63], v[132:135]\n"
      "v_cvt_pk_bf16_f32 v214, v136, v137\n"
      "v_mfma_f32_16x16x32_bf16 v[132:135], a[0:3], v[64:67], v[132:135]\n"
      "v_cvt_pk_bf16_f32 v215, v138, v139\n"
      "v_mfma_f32_16x16x32_bf16 v[132:135], a[4:7], v[68:71], v[132:135]\n"
      "v_mfma_f32_16x16x32_bf16 v[132:135], a[8:11], v[84:87], v[132:135]\n"
      "v_mfma_f32_16x16x32_bf16 v[132:135], a[12:15], v[88:91], v[132:135]\n"
      "v_mfma_f32_16x16x32_bf16 v[132:135], a[16:19], v[92:95], v[132:135]\n"
      "v_mfma_f32_16x16x32_bf16 v[132:135], a[20:23], v[108:111], v[132:135]\n"
      "ds_write_b64 v245, v[214:215] offset:24584\n"
      "v_mfma_f32_16x16x32_bf16 v[132:135], v[140:143], v[112:115], v[132:135]\n"
      "s_lshl_b32 s79, s98, 1\n"
      "v_mfma_f32_16x16x32_bf16 v[132:135], v[156:159], v[116:119], v[132:135]\n"
      "s_waitcnt lgkmcnt(4)\n"
      "s_barrier\n"
      "v_mad_i32_i24 v252, v251, s79, v252\n"
      "v_mfma_f32_16x16x32_bf16 v[136:139], v[160:163], v[36:39], v[20:23]\n"
      "ds_read_b128 v[36:39], v232 offset:24576\n"
      "s_mul_i32 s79, s79, s100\n"
      "v_mfma_f32_16x16x32_bf16 v[136:139], v[164:167], v[40:43], v[136:139]\n"
      "ds_read_b128 v[40:43], v233 offset:24576\n"
      "v_add_u32 v247, s79, v247\n"
      "v_mfma_f32_16x16x32_bf16 v[136:139], v[180:183], v[44:47], v[136:139]\n"
      "ds_read_b128 v[44:47], v234 offset:24576\n"
      "s_mul_i32 s79, s79, s101\n"
      "v_mfma_f32_16x16x32_bf16 v[136:139], v[184:187], v[60:63], v[136:139]\n"
      "ds_read_b128 v[60:63], v235 offset:24576\n"
      "v_cvt_pk_bf16_f32 v208, v132, v133\n"
      "v_mfma_f32_16x16x32_bf16 v[136:139], v[188:191], v[64:67], v[136:139]\n"
      "ds_read_b128 v[64:67], v232 offset:32768\n"
      "v_cvt_pk_bf16_f32 v209, v134, v135\n"
      "v_mfma_f32_16x16x32_bf16 v[136:139], v[204:207], v[68:71], v[136:139]\n"
      "ds_read_b128 v[68:71], v233 offset:32768\n"
      "v_add_u32 v253, s79, v253\n"
      "v_mfma_f32_16x16x32_bf16 v[136:139], a[24:27], v[84:87], v[136:139]\n"
      "ds_read_b128 v[84:87], v234 offset:32768\n"
      "v_mfma_f32_16x16x32_bf16 v[136:139], a[28:31], v[88:91], v[136:139]\n"
      "ds_read_b128 v[88:91], v235 offset:32768\n"
      "v_mfma_f32_16x16x32_bf16 v[136:139], a[32:35], v[92:95], v[136:139]\n"
      "ds_read_b128 v[92:95], v232 offset:40960\n"
      "v_mfma_f32_16x16x32_bf16 v[136:139], a[36:39], v[108:111], v[136:139]\n"
      "ds_read_b128 v[108:111], v233 offset:40960\n"
      "v_mfma_f32_16x16x32_bf16 v[136:139], a[40:43], v[112:115], v[136:139]\n"
      "ds_read_b128 v[112:115], v234 offset:40960\n"
      "v_mfma_f32_16x16x32_bf16 v[136:139], a[44:47], v[116:119], v[136:139]\n"
      "ds_read_b128 v[116:119], v235 offset:40960\n"
      "s_branch LJOIN\n"
      "LF_1:\n"
      "s_mul_i32 s62, s62, 0x3000\n"
      "s_sub_u32 s62, s98, s62\n"
      "s_sub_u32 s62, s62, s89\n"
      "v_mad_i32_i24 v238, v251, s62, v238\n"
      "v_mad_i32_i24 v238, v251, s98, v238\n"
      "s_add_u32 s56, s22, 0x0\n"
      "s_addc_u32 s57, s23, 0\n"
      "s_add_u32 m0, s18, 0x0\n"
      "s_nop 0\n"
      "global_load_lds_dwordx4 v238, s[56:57] offset:0 \n"
      "v_mad_i32_i24 v238, v251, s90, v238\n"
      "s_add_u32 s56, s22, 0x6000\n"
      "s_addc_u32 s57, s23, 0\n"
      "s_add_u32 m0, s18, 0x1000\n"
      "s_nop 0\n"
      "global_load_lds_dwordx4 v238, s[56:57] offset:0 \n"
      "v_mad_i32_i24 v238, v251, s91, v238\n"
      "s_add_u32 s56, s22, 0xc000\n"
      "s_addc_u32 s57, s23, 0\n"
      "s_add_u32 m0, s18, 0x2000\n"
      "s_nop 0\n"
      "global_load_lds_dwordx4 v238, s[56:57] offset:0 \n"
      "v_mad_i32_i24 v238, v251, s92, v238\n"
      "s_add_u32 s56, s22, 0x12000\n"
      "s_addc_u32 s57, s23, 0\n"
      "s_add_u32 m0, s18, 0x3000\n"
      "s_nop 0\n"
      "global_load_lds_dwordx4 v238, s[56:57] offset:0 \n"
      "v_mad_i32_i24 v238, v251, s93, v238\n"
      "s_add_u32 s56, s22, 0x18000\n"
      "s_addc_u32 s57, s23, 0\n"
      "s_add_u32 m0, s18, 0x4000\n"
      "s_nop 0\n"
      "global_load_lds_dwordx4 v238, s[56:57] offset:0 \n"
      "v_mad_i32_i24 v238, v251, s94, v238\n"
      "s_add_u32 s56, s22, 0x1e000\n"
      "s_addc_u32 s57, s23, 0\n"
      "s_add_u32 m0, s18, 0x5000\n"
      "s_nop 0\n"
      "global_load_lds_dwordx4 v238, s[56:57] offset:0 \n"
      "v_mad_i32_i24 v238, v251, s95, v238\n"
      "s_add_u32 s56, s22, 0x24000\n"
      "s_addc_u32 s57, s23, 0\n"
      "s_add_u32 m0, s18, 0x6000\n"
      "s_nop 0\n"
      "global_load_lds_dwordx4 v238, s[56:57] offset:0 \n"
      "v_mad_i32_i24 v238, v251, s96, v238\n"
      "s_add_u32 s56, s22, 0x2a000\n"
      "s_addc_u32 s57, s23, 0\n"
      "s_add_u32 m0, s18, 0x7000\n"
      "s_nop 0\n"
      "global_load_lds_dwordx4 v238, s[56:57] offset:0 \n"
      "v_mad_i32_i24 v238, v251, s97, v238\n"
      "s_add_u32 s56, s22, 0x30000\n"
      "s_addc_u32 s57, s23, 0\n"
      "s_add_u32 m0, s18, 0x8000\n"
      "s_nop 0\n"
      "global_load_lds_dwordx4 v238, s[56:57] offset:0 \n"
      "s_branch LR_1\n"
      "LV1:\n"
      "s_add_u32 s78, s64, 0x0\n"
      "s_addc_u32 s79, s65, 0\n"
      "s_add_u32 m0, s74, 0x0\n"
      "s_nop 0\n"
      "global_load_lds_dwordx4 v240, s[78:79]\n"
      "s_add_u32 m0, s74, 0x400\n"
      "s_nop 0\n"
      "global_load_lds_dwordx4 v241, s[78:79]\n"
      "s_add_u32 m0, s74, 0x800\n"
      "s_nop 0\n"
      "global_load_lds_dwordx4 v242, s[78:79]\n"
      "s_add_u32 s78, s64, 0x3000\n"
      "s_addc_u32 s79, s65, 0\n"
      "s_add_u32 m0, s74, 0xc00\n"
      "s_nop 0\n"
      "global_load_lds_dwordx4 v240, s[78:79]\n"
      "s_add_u32 m0, s74, 0x1000\n"
      "s_nop 0\n"
      "global_load_lds_dwordx4 v241, s[78:79]\n"
      "s_add_u32 m0, s74, 0x1400\n"
      "s_nop 0\n"
      "global_load_lds_dwordx4 v242, s[78:79]\n"
      "s_getpc_b64 s[56:57]\n"
      "LTR_1:\n"
      "s_add_u32 s56, s56, LTE_1-LTR_1\n"
      "s_addc_u32 s57, s57, 0\n"
      "s_mov_b64 exec, 0\n"
      "s_cmp_eq_u32 s76, 3\n"
      "s_cbranch_scc1 LPD6\n"
      "s_cmp_eq_u32 s76, 2\n"
      "s_cbranch_scc1 LPD5\n"
      "s_cmp_eq_u32 s76, 1\n"
      "s_cbranch_scc1 LPD4\n"
      "LTE_1:\n"
      "s_mov_b64 exec, -1\n"
      "s_waitcnt vmcnt(17)\n"
      "v_readfirstlane_b32 s80, v252\n"
      "v_readfirstlane_b32 s81, v253\n"
      "s_nop 3\n"
      "s_add_u32 s80, s80, 0x7f4a7c15\n"
      "s_addc_u32 s81, s81, 0x9e3779b9\n"
      "v_mov_b32 v252, s80\n"
      "v_mov_b32 v253, s81\n"
      "v_xor_b32 v116, v112, v110\n"
      "v_xor_b32 v117, v113, v111\n"
      "v_and_or_b32 v116, v116, -2, v117\n"
      "v_cmp_eq_u32 vcc, 0, v116\n"
      "v_cndmask_b32 v116, v110, v112, vcc\n"
      "s_mov_b64 exec, 1\n"
      "ds_write_b32 v108, v116\n"
      "s_mov_b64 exec, -1\n"
      "v_and_b32 v108, -16, v108\n"
      "s_waitcnt lgkmcnt(0)\n"
      "s_barrier\n"
      "ds_read_b32 v116, v108\n"
      "s_waitcnt lgkmcnt(0)\n"
      "v_readfirstlane_b32 s62, v116\n"
      "s_lshr_b32 s63, s51, 2\n"
      "s_nop 2\n"
      "s_xor_b32 s62, s62, s63\n"
      "s_and_b32 s62, s62, 1\n"
      "s_mul_i32 s63, s62, 0x3000\n"
      "s_sub_u32 s98, s98, s63\n"
      "s_lshl_b32 s62, s62, 1\n"
      "s_sub_u32 s62, 1, s62\n"
      "v_mul_i32_i24 v251, s62, v251\n"
      "s_lshl_b32 s19, s62, 1\n"
      "s_mul_i32 s100, s100, s62\n"
      "v_cmp_ne_u32 vcc, v107, v251\n"
      "s_nop 3\n"
      "s_cbranch_vccnz LF_1\n"
      "LR_1:\n"
      "s_nop 0\n"
      "s_add_u32 s78, s64, 0x6000\n"
      "s_addc_u32 s79, s65, 0\n"
      "s_add_u32 m0, s74, 0x6000\n"
      "s_nop 0\n"
      "global_load_lds_dwordx4 v240, s[78:79]\n"
      "s_add_u32 m0, s74, 0x6400\n"
      "s_nop 0\n"
      "global_load_lds_dwordx4 v241, s[78:79]\n"
      "s_add_u32 m0, s74, 0x6800\n"
      "s_nop 0\n"
      "global_load_lds_dwordx4 v242, s[78:79]\n"
      "s_add_u32 s78, s64, 0x9000\n"
      "s_addc_u32 s79, s65, 0\n"
      "s_add_u32 m0, s74, 0x6c00\n"
      "s_nop 0\n"
      "global_load_lds_dwordx4 v240, s[78:79]\n"
      "s_add_u32 m0, s74, 0x7000\n"
      "s_nop 0\n"
      "global_load_lds_dwordx4 v241, s[78:79]\n"
      "s_add_u32 m0, s74, 0x7400\n"
      "s_nop 0\n"
      "global_load_lds_dwordx4 v242, s[78:79]\n"
      "s_add_u32 s78, s64, 0xc000\n"
      "s_addc_u32 s79, s65, 0\n"
      "s_add_u32 m0, s74, 0xc000\n"
      "s_nop 0\n"
      "global_load_lds_dwordx4 v240, s[78:79]\n"
      "s_add_u32 m0, s74, 0xc400\n"
      "s_nop 0\n"
      "global_load_lds_dwordx4 v241, s[78:79]\n"
      "s_add_u32 m0, s74, 0xc800\n"
      "s_nop 0\n"
      "global_load_lds_dwordx4 v242, s[78:79]\n"
      "s_add_u32 s78, s64, 0xf000\n"
      "s_addc_u32 s79, s65, 0\n"
      "s_add_u32 m0, s74, 0xcc00\n"
      "s_nop 0\n"
      "global_load_lds_dwordx4 v240, s[78:79]\n"
      "s_add_u32 m0, s74, 0xd000\n"
      "s_nop 0\n"
      "global_load_lds_dwordx4 v241, s[78:79]\n"
      "s_add_u32 m0, s74, 0xd400\n"
      "s_nop 0\n"
      "global_load_lds_dwordx4 v242, s[78:79]\n"
      "s_add_u32 m0, s17, 0x25000\n"
      "s_nop 0\n"
      "global_load_lds_dwordx4 v239, s[46:47] offset:0\n"
      "s_add_u32 m0, s17, 0x25800\n"
      "s_nop 0\n"
      "global_load_lds_dwordx4 v239, s[46:47] offset:2048\n"
      "s_waitcnt vmcnt(14)\n"
      "ds_read_b64_tr_b16 a[0:1], v243 offset:0\n"
      "ds_read_b64_tr_b16 a[2:3], v243 offset:768\n"
      "ds_read_b64_tr_b16 a[4:5], v243 offset:8\n"
      "ds_read_b64_tr_b16 a[6:7], v243 offset:776\n"
      "ds_read_b64_tr_b16 a[8:9], v243 offset:64\n"
      "ds_read_b64_tr_b16 a[10:11], v243 offset:832\n"
      "ds_read_b64_tr_b16 a[12:13], v243 offset:72\n"
      "ds_read_b64_tr_b16 a[14:15], v243 offset:840\n"
      "ds_read_b64_tr_b16 a[16:17], v243 offset:128\n"
      "ds_read_b64_tr_b16 a[18:19], v243 offset:896\n"
      "ds_read_b64_tr_b16 a[20:21], v243 offset:136\n"
      "ds_read_b64_tr_b16 a[22:23], v243 offset:904\n"
      "s_waitcnt vmcnt(13)\n"
      "s_barrier\n"
      "ds_read_b128 v[216:219], v232 offset:0\n"
      "ds_read_b128 v[220:223], v232 offset:4096\n"
      "ds_read_b128 v[224:227], v232 offset:8192\n"
      "s_waitcnt lgkmcnt(2)\n"
      "v_mfma_f32_16x16x32_bf16 v[0:3], a[0:3], v[216:219], 0\n"
      "s_add_u32 s78, s64, 0x12000\n"
      "s_addc_u32 s79, s65, 0\n"
      "s_add_u32 m0, s74, 0x0\n"
      "v_mfma_f32_16x16x32_bf16 v[4:7], a[4:7], v[216:219], 0\n"
      "global_load_lds_dwordx4 v240, s[78:79]\n"
      "v_mfma_f32_16x16x32_bf16 v[8:11], a[8:11], v[216:219], 0\n"
      "ds_read_b128 v[228:231], v232 offset:12288\n"
      "v_mfma_f32_16x16x32_bf16 v[12:15], a[12:15], v[216:219], 0\n"
      "v_mfma_f32_16x16x32_bf16 v[16:19], a[16:19], v[216:219], 0\n"
      "s_add_u32 m0, s74, 0x400\n"
      "v_mfma_f32_16x16x32_bf16 v[20:23], a[20:23], v[216:219], 0\n"
      "global_load_lds_dwordx4 v241, s[78:79]\n"
      "s_waitcnt lgkmcnt(2)\n"
      "v_mfma_f32_16x16x32_bf16 v[24:27], a[0:3], v[220:223], 0\n"
      "s_waitcnt vmcnt(10)\n"
      "s_cmp_lg_u32 s76, 1\n"
      "s_cbranch_scc1 LA_1\n"
      "v_mov_b32 v244, s77\n"
      "s_mov_b64 exec, 1\n"
      "global_store_dwordx2 v244, v[252:253], s[28:29] sc1\n"
      "s_mov_b64 exec, -1\n"
      "s_nop 4\n"
      "LA_1:\n"
      "v_mfma_f32_16x16x32_bf16 v[28:31], a[4:7], v[220:223], 0\n"
      "ds_read_b64_tr_b16 a[24:25], v243 offset:24576\n"
      "v_mfma_f32_16x16x32_bf16 v[32:35], a[8:11], v[220:223], 0\n"
      "ds_read_b128 v[216:219], v232 offset:16384\n"
      "s_add_u32 m0, s74, 0x800\n"
      "v_mfma_f32_16x16x32_bf16 v[36:39], a[12:15], v[220:223], 0\n"
      "ds_read_b64_tr_b16 a[26:27], v243 offset:25344\n"
      "global_load_lds_dwordx4 v242, s[78:79]\n"
      "v_mfma_f32_16x16x32_bf16 v[40:43], a[16:19], v[220:223], 0\n"
      "ds_read_b64_tr_b16 a[28:29], v243 offset:24584\n"
      "v_mfma_f32_16x16x32_bf16 v[44:47], a[20:23], v[220:223], 0\n"
      "s_waitcnt lgkmcnt(5)\n"
      "v_mfma_f32_16x16x32_bf16 v[48:51], a[0:3], v[224:227], 0\n"
      "ds_read_b64_tr_b16 a[30:31], v243 offset:25352\n"
      "s_add_u32 s78, s64, 0x15000\n"
      "s_addc_u32 s79, s65, 0\n"
      "s_add_u32 m0, s74, 0xc00\n"
      "v_mfma_f32_16x16x32_bf16 v[52:55], a[4:7], v[224:227], 0\n"
      "ds_read_b64_tr_b16 a[32:33], v243 offset:24640\n"
      "global_load_lds_dwordx4 v240, s[78:79]\n"
      "v_mfma_f32_16x16x32_bf16 v[56:59], a[8:11], v[224:227], 0\n"
      "ds_read_b128 v[220:223], v232 offset:20480\n"
      "v_mfma_f32_16x16x32_bf16 v[60:63], a[12:15], v[224:227], 0\n"
      "ds_read_b64_tr_b16 a[34:35], v243 offset:25408\n"
      "v_mfma_f32_16x16x32_bf16 v[64:67], a[16:19], v[224:227], 0\n"
      "ds_read_b64_tr_b16 a[36:37], v243 offset:24648\n"
      "s_add_u32 m0, s74, 0x1000\n"
      "v_mfma_f32_16x16x32_bf16 v[68:71], a[20:23], v[224:227], 0\n"
      "global_load_lds_dwordx4 v241, s[78:79]\n"
      "s_waitcnt lgkmcnt(9)\n"
      "v_mfma_f32_16x16x32_bf16 v[72:75], a[0:3], v[228:231], 0\n"
      "ds_read_b64_tr_b16 a[38:39], v243 offset:25416\n"
      "v_mfma_f32_16x16x32_bf16 v[76:79], a[4:7], v[228:231], 0\n"
      "ds_read_b64_tr_b16 a[40:41], v243 offset:24704\n"
      "v_mfma_f32_16x16x32_bf16 v[80:83], a[8:11], v[228:231], 0\n"
      "ds_read_b128 v[224:227], v232 offset:24576\n"
      "s_add_u32 m0, s74, 0x1400\n"
      "v_mfma_f32_16x16x32_bf16 v[84:87], a[12:15], v[228:231], 0\n"
      "ds_read_b64_tr_b16 a[42:43], v243 offset:25472\n"
      "global_load_lds_dwordx4 v242, s[78:79]\n"
      "v_mfma_f32_16x16x32_bf16 v[88:91], a[16:19], v[228:231], 0\n"
      "ds_read_b64_tr_b16 a[44:45], v243 offset:24712\n"
      "v_mfma_f32_16x16x32_bf16 v[92:95], a[20:23], v[228:231], 0\n"
      "s_waitcnt lgkmcnt(12)\n"
      "v_mfma_f32_16x16x32_bf16 v[96:99], a[0:3], v[216:219], 0\n"
      "ds_read_b64_tr_b16 a[46:47], v243 offset:25480\n"
      "v_mfma_f32_16x16x32_bf16 v[100:103], a[4:7], v[216:219], 0\n"
      "s_waitcnt vmcnt(8)\n"
      "v_mfma_f32_16x16x32_bf16 v[104:107], a[8:11], v[216:219], 0\n"
      "ds_read_b128 v[228:231], v232 offset:28672\n"
      "v_mfma_f32_16x16x32_bf16 v[108:111], a[12:15], v[216:219], 0\n"
      "ds_read_b64_tr_b16 a[48:49], v243 offset:49152\n"
      "v_mfma_f32_16x16x32_bf16 v[112:115], a[16:19], v[216:219], 0\n"
      "ds_read_b64_tr_b16 a[50:51], v243 offset:49920\n"
      "v_mfma_f32_16x16x32_bf16 v[116:119], a[20:23], v[216:219], 0\n"
      "s_waitcnt lgkmcnt(11)\n"
      "v_mfma_f32_16x16x32_bf16 v[120:123], a[0:3], v[220:223], 0\n"
      "ds_read_b64_tr_b16 a[52:53], v243 offset:49160\n"
      "v_mfma_f32_16x16x32_bf16 v[124:127], a[4:7], v[220:223], 0\n"
      "ds_read_b64_tr_b16 a[54:55], v243 offset:49928\n"
      "v_mfma_f32_16x16x32_bf16 v[128:131], a[8:11], v[220:223], 0\n"
      "ds_read_b128 v[216:219], v232 offset:32768\n"
      "v_add_u32 v232, 0x9000, v232\n"
      "v_mfma_f32_16x16x32_bf16 v[132:135], a[12:15], v[220:223], 0\n"
      "ds_read_b64_tr_b16 a[56:57], v243 offset:49216\n"
      "v_mfma_f32_16x16x32_bf16 v[136:139], a[16:19], v[220:223], 0\n"
      "ds_read_b64_tr_b16 a[58:59], v243 offset:49984\n"
      "s_waitcnt lgkmcnt(8)\n"
      "s_add_u32 s78, s64, 0x18000\n"
      "s_addc_u32 s79, s65, 0\n"
      "s_add_u32 m0, s74, 0x6000\n"
      "v_mfma_f32_16x16x32_bf16 v[140:143], a[20:23], v[220:223], 0\n"
      "global_load_lds_dwordx4 v240, s[78:79]\n"
      "v_mfma_f32_16x16x32_bf16 v[144:147], a[0:3], v[224:227], 0\n"
      "ds_read_b64_tr_b16 a[60:61], v243 offset:49224\n"
      "v_mfma_f32_16x16x32_bf16 v[148:151], a[4:7], v[224:227], 0\n"
      "ds_read_b64_tr_b16 a[62:63], v243 offset:49992\n"
      "v_mfma_f32_16x16x32_bf16 v[152:155], a[8:11], v[224:227], 0\n"
      "ds_read_b128 v[220:223], v233 offset:0\n"
      "s_add_u32 m0, s74, 0x6400\n"
      "v_mfma_f32_16x16x32_bf16 v[156:159], a[12:15], v[224:227], 0\n"
      "ds_read_b64_tr_b16 a[64:65], v243 offset:49280\n"
      "global_load_lds_dwordx4 v241, s[78:79]\n"
      "v_mfma_f32_16x16x32_bf16 v[160:163], a[16:19], v[224:227], 0\n"
      "ds_read_b64_tr_b16 a[66:67], v243 offset:50048\n"
      "v_mfma_f32_16x16x32_bf16 v[164:167], a[20:23], v[224:227], 0\n"
      "s_waitcnt lgkmcnt(12)\n"
      "v_mfma_f32_16x16x32_bf16 v[168:171], a[0:3], v[228:231], 0\n"
      "ds_read_b64_tr_b16 a[68:69], v243 offset:49288\n"
      "s_add_u32 m0, s74, 0x6800\n"
      "v_mfma_f32_16x16x32_bf16 v[172:175], a[4:7], v[228:231], 0\n"
      "ds_read_b64_tr_b16 a[70:71], v243 offset:50056\n"
      "global_load_lds_dwordx4 v242, s[78:79]\n"
      "v_mfma_f32_16x16x32_bf16 v[176:179], a[8:11], v[228:231], 0\n"
      "ds_read_b128 v[224:227], v233 offset:4096\n"
      "v_mfma_f32_16x16x32_bf16 v[180:183], a[12:15], v[228:231], 0\n"
      "v_mfma_f32_16x16x32_bf16 v[184:187], a[16:19], v[228:231], 0\n"
      "s_add_u32 s78, s64, 0x1b000\n"
      "s_addc_u32 s79, s65, 0\n"
      "s_add_u32 m0, s74, 0x6c00\n"
      "v_mfma_f32_16x16x32_bf16 v[188:191], a[20:23], v[228:231], 0\n"
      "global_load_lds_dwordx4 v240, s[78:79]\n"
      "s_waitcnt lgkmcnt(10)\n"
      "v_mfma_f32_16x16x32_bf16 v[192:195], a[0:3], v[216:219], 0\n"
      "v_mfma_f32_16x16x32_bf16 v[196:199], a[4:7], v[216:219], 0\n"
      "v_mfma_f32_16x16x32_bf16 v[200:203], a[8:11], v[216:219], 0\n"
      "ds_read_b128 v[228:231], v233 offset:8192\n"
      "s_add_u32 m0, s74, 0x7000\n"
      "v_mfma_f32_16x16x32_bf16 v[204:207], a[12:15], v[216:219], 0\n"
      "global_load_lds_dwordx4 v241, s[78:79]\n"
      "v_mfma_f32_16x16x32_bf16 v[208:211], a[16:19], v[216:219], 0\n"
      "v_mfma_f32_16x16x32_bf16 v[212:215], a[20:23], v[216:219], 0\n"
      "s_waitcnt lgkmcnt(6)\n"
      "v_mfma_f32_16x16x32_bf16 v[0:3], a[24:27], v[220:223], v[0:3]\n"
      "s_add_u32 m0, s74, 0x7400\n"
      "v_mfma_f32_16x16x32_bf16 v[4:7], a[28:31], v[220:223], v[4:7]\n"
      "global_load_lds_dwordx4 v242, s[78:79]\n"
      "v_mfma_f32_16x16x32_bf16 v[8:11], a[32:35], v[220:223], v[8:11]\n"
      "ds_read_b128 v[216:219], v233 offset:12288\n"
      "s_waitcnt vmcnt(6)\n"
      "v_mfma_f32_16x16x32_bf16 v[12:15], a[36:39], v[220:223], v[12:15]\n"
      "ds_read_b64_tr_b16 a[72:73], v243 offset:0\n"
      "v_mfma_f32_16x16x32_bf16 v[16:19], a[40:43], v[220:223], v[16:19]\n"
      "ds_read_b64_tr_b16 a[74:75], v243 offset:768\n"
      "v_mad_i32_i24 v238, v251, s89, v238\n"
      "s_add_u32 s56, s22, 0x0\n"
      "s_addc_u32 s57, s23, 0\n"
      "s_add_u32 m0, s18, 0x8f00\n"
      "v_mfma_f32_16x16x32_bf16 v[20:23], a[44:47], v[220:223], v[20:23]\n"
      "global_load_lds_dwordx4 v238, s[56:57] offset:256 \n"
      "s_waitcnt lgkmcnt(4)\n"
      "v_mfma_f32_16x16x32_bf16 v[24:27], a[24:27], v[224:227], v[24:27]\n"
      "ds_read_b64_tr_b16 a[76:77], v243 offset:8\n"
      "v_mfma_f32_16x16x32_bf16 v[28:31], a[28:31], v[224:227], v[28:31]\n"
      "ds_read_b64_tr_b16 a[78:79], v243 offset:776\n"
      "v_mad_i32_i24 v238, v251, s90, v238\n"
      "s_add_u32 s56, s22, 0x6000\n"
      "s_addc_u32 s57, s23, 0\n"
      "s_add_u32 m0, s18, 0x9f00\n"
      "v_mfma_f32_16x16x32_bf16 v[32:35], a[32:35], v[224:227], v[32:35]\n"
      "ds_read_b128 v[220:223], v233 offset:16384\n"
      "global_load_lds_dwordx4 v238, s[56:57] offset:256 \n"
      "v_mfma_f32_16x16x32_bf16 v[36:39], a[36:39], v[224:227], v[36:39]\n"
      "ds_read_b64_tr_b16 a[80:81], v243 offset:64\n"
      "v_mfma_f32_16x16x32_bf16 v[40:43], a[40:43], v[224:227], v[40:43]\n"
      "ds_read_b64_tr_b16 a[82:83], v243 offset:832\n"
      "v_mad_i32_i24 v238, v251, s91, v238\n"
      "s_add_u32 s56, s22, 0xc000\n"
      "s_addc_u32 s57, s23, 0\n"
      "s_add_u32 m0, s18, 0xaf00\n"
      "v_mfma_f32_16x16x32_bf16 v[44:47], a[44:47], v[224:227], v[44:47]\n"
      "global_load_lds_dwordx4 v238, s[56:57] offset:256 \n"
      "s_waitcnt lgkmcnt(8)\n"
      "v_mfma_f32_16x16x32_bf16 v[48:51], a[24:27], v[228:231], v[48:51]\n"
      "ds_read_b64_tr_b16 a[84:85], v243 offset:72\n"
      "v_mfma_f32_16x16x32_bf16 v[52:55], a[28:31], v[228:231], v[52:55]\n"
      "ds_read_b64_tr_b16 a[86:87], v243 offset:840\n"
      "v_mad_i32_i24 v238, v251, s92, v238\n"
      "s_add_u32 s56, s22, 0x12000\n"
      "s_addc_u32 s57, s23, 0\n"
      "s_add_u32 m0, s18, 0xbf00\n"
      "v_mfma_f32_16x16x32_bf16 v[56:59], a[32:35], v[228:231], v[56:59]\n"
      "ds_read_b128 v[224:227], v233 offset:20480\n"
      "global_load_lds_dwordx4 v238, s[56:57] offset:256 \n"
      "v_mfma_f32_16x16x32_bf16 v[60:63], a[36:39], v[228:231], v[60:63]\n"
      "ds_read_b64_tr_b16 a[88:89], v243 offset:128\n"
      "v_mfma_f32_16x16x32_bf16 v[64:67], a[40:43], v[228:231], v[64:67]\n"
      "ds_read_b64_tr_b16 a[90:91], v243 offset:896\n"
      "v_mad_i32_i24 v238, v251, s93, v238\n"
      "s_add_u32 s56, s22, 0x18000\n"
      "s_addc_u32 s57, s23, 0\n"
      "s_add_u32 m0, s18, 0xcf00\n"
      "v_mfma_f32_16x16x32_bf16 v[68:71], a[44:47], v[228:231], v[68:71]\n"
      "global_load_lds_dwordx4 v238, s[56:57] offset:256 \n"
      "s_waitcnt lgkmcnt(12)\n"
      "v_mfma_f32_16x16x32_bf16 v[72:75], a[24:27], v[216:219], v[72:75]\n"
      "ds_read_b64_tr_b16 a[92:93], v243 offset:136\n"
      "v_mfma_f32_16x16x32_bf16 v[76:79], a[28:31], v[216:219], v[76:79]\n"
      "ds_read_b64_tr_b16 a[94:95], v243 offset:904\n"
      "v_mad_i32_i24 v238, v251, s94, v238\n"
      "s_add_u32 s56, s22, 0x1e000\n"
      "s_addc_u32 s57, s23, 0\n"
      "s_add_u32 m0, s18, 0xdf00\n"
      "v_mfma_f32_16x16x32_bf16 v[80:83], a[32:35], v[216:219], v[80:83]\n"
      "ds_read_b128 v[228:231], v233 offset:24576\n"
      "global_load_lds_dwordx4 v238, s[56:57] offset:256 \n"
      "v_mfma_f32_16x16x32_bf16 v[84:87], a[36:39], v[216:219], v[84:87]\n"
      "v_mfma_f32_16x16x32_bf16 v[88:91], a[40:43], v[216:219], v[88:91]\n"
      "v_mad_i32_i24 v238, v251, s95, v238\n"
      "s_add_u32 s56, s22, 0x24000\n"
      "s_addc_u32 s57, s23, 0\n"
      "s_add_u32 m0, s18, 0xef00\n"
      "v_mfma_f32_16x16x32_bf16 v[92:95], a[44:47], v[216:219], v[92:95]\n"
      "global_load_lds_dwordx4 v238, s[56:57] offset:256 \n"
      "s_waitcnt lgkmcnt(10)\n"
      "v_mfma_f32_16x16x32_bf16 v[96:99], a[24:27], v[220:223], v[96:99]\n"
      "v_mfma_f32_16x16x32_bf16 v[100:103], a[28:31], v[220:223], v[100:103]\n"
      "v_mad_i32_i24 v238, v251, s96, v238\n"
      "s_add_u32 s56, s22, 0x2a000\n"
      "s_addc_u32 s57, s23, 0\n"
      "s_add_u32 m0, s18, 0xff00\n"
      "v_mfma_f32_16x16x32_bf16 v[104:107], a[32:35], v[220:223], v[104:107]\n"
      "ds_read_b128 v[216:219], v233 offset:28672\n"
      "global_load_lds_dwordx4 v238, s[56:57] offset:256 \n"
      "v_mfma_f32_16x16x32_bf16 v[108:111], a[36:39], v[220:223], v[108:111]\n"
      "v_mfma_f32_16x16x32_bf16 v[112:115], a[40:43], v[220:223], v[112:115]\n"
      "v_mad_i32_i24 v238, v251, s97, v238\n"
      "s_add_u32 s56, s22, 0x30000\n"
      "s_addc_u32 s57, s23, 0\n"
      "s_add_u32 m0, s18, 0x10f00\n"
      "v_mfma_f32_16x16x32_bf16 v[116:119], a[44:47], v[220:223], v[116:119]\n"
      "global_load_lds_dwordx4 v238, s[56:57] offset:256 \n"
      "s_waitcnt lgkmcnt(6)\n"
      "v_mfma_f32_16x16x32_bf16 v[120:123], a[24:27], v[224:227], v[120:123]\n"
      "v_mfma_f32_16x16x32_bf16 v[124:127], a[28:31], v[224:227], v[124:127]\n"
      "v_mfma_f32_16x16x32_bf16 v[128:131], a[32:35], v[224:227], v[128:131]\n"
      "ds_read_b128 v[220:223], v233 offset:32768\n"
      "v_add_u32 v233, 0x9000, v233\n"
      "s_add_u32 s78, s64, 0x1e000\n"
      "s_addc_u32 s79, s65, 0\n"
      "s_add_u32 m0, s74, 0xc000\n"
      "v_mfma_f32_16x16x32_bf16 v[132:135], a[36:39], v[224:227], v[132:135]\n"
      "global_load_lds_dwordx4 v240, s[78:79]\n"
      "v_mfma_f32_16x16x32_bf16 v[136:139], a[40:43], v[224:227], v[136:139]\n"
      "v_mfma_f32_16x16x32_bf16 v[140:143], a[44:47], v[224:227], v[140:143]\n"
      "s_waitcnt lgkmcnt(2)\n"
      "v_mfma_f32_16x16x32_bf16 v[144:147], a[24:27], v[228:231], v[144:147]\n"
      "s_waitcnt vmcnt(10)\n"
      "s_add_u32 m0, s74, 0xc400\n"
      "v_mfma_f32_16x16x32_bf16 v[148:151], a[28:31], v[228:231], v[148:151]\n"
      "ds_read_b64_tr_b16 a[96:97], v243 offset:24576\n"
      "global_load_lds_dwordx4 v241, s[78:79]\n"
      "v_mfma_f32_16x16x32_bf16 v[152:155], a[32:35], v[228:231], v[152:155]\n"
      "ds_read_b128 v[224:227], v234 offset:0\n"
      "v_mfma_f32_16x16x32_bf16 v[156:159], a[36:39], v[228:231], v[156:159]\n"
      "ds_read_b64_tr_b16 a[98:99], v243 offset:25344\n"
      "v_mfma_f32_16x16x32_bf16 v[160:163], a[40:43], v[228:231], v[160:163]\n"
      "ds_read_b64_tr_b16 a[100:101], v243 offset:24584\n"
      "s_add_u32 m0, s74, 0xc800\n"
      "v_mfma_f32_16x16x32_bf16 v[164:167], a[44:47], v[228:231], v[164:167]\n"
      "global_load_lds_dwordx4 v242, s[78:79]\n"
      "s_waitcnt lgkmcnt(5)\n"
      "v_mfma_f32_16x16x32_bf16 v[168:171], a[24:27], v[216:219], v[168:171]\n"
      "ds_read_b64_tr_b16 a[102:103], v243 offset:25352\n"
      "v_mfma_f32_16x16x32_bf16 v[172:175], a[28:31], v[216:219], v[172:175]\n"
      "ds_read_b64_tr_b16 a[104:105], v243 offset:24640\n"
      "v_mfma_f32_16x16x32_bf16 v[176:179], a[32:35], v[216:219], v[176:179]\n"
      "ds_read_b128 v[228:231], v234 offset:4096\n"
      "s_add_u32 s78, s64, 0x21000\n"
      "s_addc_u32 s79, s65, 0\n"
      "s_add_u32 m0, s74, 0xcc00\n"
      "v_mfma_f32_16x16x32_bf16 v[180:183], a[36:39], v[216:219], v[180:183]\n"
      "ds_read_b64_tr_b16 a[106:107], v243 offset:25408\n"
      "global_load_lds_dwordx4 v240, s[78:79]\n"
      "v_mfma_f32_16x16x32_bf16 v[184:187], a[40:43], v[216:219], v[184:187]\n"
      "ds_read_b64_tr_b16 a[108:109], v243 offset:24648\n"
      "LPD4:\n"
      "s_cbranch_execz LPD9\n"
      "v_mfma_f32_16x16x32_bf16 v[188:191], a[44:47], v[216:219], v[188:191]\n"
      "s_waitcnt lgkmcnt(9)\n"
      "v_mfma_f32_16x16x32_bf16 v[192:195], a[24:27], v[220:223], v[192:195]\n"
      "ds_read_b64_tr_b16 a[110:111], v243 offset:25416\n"
      "s_add_u32 m0, s74, 0xd000\n"
      "v_mfma_f32_16x16x32_bf16 v[196:199], a[28:31], v[220:223], v[196:199]\n"
      "ds_read_b64_tr_b16 a[112:113], v243 offset:24704\n"
      "global_load_lds_dwordx4 v241, s[78:79]\n"
      "v_mfma_f32_16x16x32_bf16 v[200:203], a[32:35], v[220:223], v[200:203]\n"
      "ds_read_b128 v[216:219], v234 offset:8192\n"
      "v_mfma_f32_16x16x32_bf16 v[204:207], a[36:39], v[220:223], v[204:207]\n"
      "ds_read_b64_tr_b16 a[114:115], v243 offset:25472\n"
      "v_mfma_f32_16x16x32_bf16 v[208:211], a[40:43], v[220:223], v[208:211]\n"
      "ds_read_b64_tr_b16 a[116:117], v243 offset:24712\n"
      "s_add_u32 m0, s74, 0xd400\n"
      "v_mfma_f32_16x16x32_bf16 v[212:215], a[44:47], v[220:223], v[212:215]\n"
      "global_load_lds_dwordx4 v242, s[78:79]\n"
      "s_waitcnt lgkmcnt(12)\n"
      "v_mfma_f32_16x16x32_bf16 v[0:3], a[48:51], v[224:227], v[0:3]\n"
      "ds_read_b64_tr_b16 a[118:119], v243 offset:25480\n"
      "v_mfma_f32_16x16x32_bf16 v[4:7], a[52:55], v[224:227], v[4:7]\n"
      "v_mfma_f32_16x16x32_bf16 v[8:11], a[56:59], v[224:227], v[8:11]\n"
      "ds_read_b128 v[220:223], v234 offset:12288\n"
      "s_add_u32 s78, s64, 0x24000\n"
      "s_addc_u32 s79, s65, 0\n"
      "s_add_u32 m0, s74, 0x0\n"
      "v_mfma_f32_16x16x32_bf16 v[12:15], a[60:63], v[224:227], v[12:15]\n"
      "global_load_lds_dwordx4 v240, s[78:79]\n"
      "v_mfma_f32_16x16x32_bf16 v[16:19], a[64:67], v[224:227], v[16:19]\n"
      "v_mfma_f32_16x16x32_bf16 v[20:23], a[68:71], v[224:227], v[20:23]\n"
      "s_waitcnt lgkmcnt(9)\n"
      "v_mfma_f32_16x16x32_bf16 v[24:27], a[48:51], v[228:231], v[24:27]\n"
      "s_add_u32 m0, s74, 0x400\n"
      "v_mfma_f32_16x16x32_bf16 v[28:31], a[52:55], v[228:231], v[28:31]\n"
      "global_load_lds_dwordx4 v241, s[78:79]\n"
      "v_mfma_f32_16x16x32_bf16 v[32:35], a[56:59], v[228:231], v[32:35]\n"
      "ds_read_b128 v[224:227], v234 offset:16384\n"
      "v_mfma_f32_16x16x32_bf16 v[36:39], a[60:63], v[228:231], v[36:39]\n"
      "v_mfma_f32_16x16x32_bf16 v[40:43], a[64:67], v[228:231], v[40:43]\n"
      "s_add_u32 m0, s74, 0x800\n"
      "v_mfma_f32_16x16x32_bf16 v[44:47], a[68:71], v[228:231], v[44:47]\n"
      "global_load_lds_dwordx4 v242, s[78:79]\n"
      "s_waitcnt lgkmcnt(5)\n"
      "v_mfma_f32_16x16x32_bf16 v[48:51], a[48:51], v[216:219], v[48:51]\n"
      "v_mfma_f32_16x16x32_bf16 v[52:55], a[52:55], v[216:219], v[52:55]\n"
      "v_mfma_f32_16x16x32_bf16 v[56:59], a[56:59], v[216:219], v[56:59]\n"
      "ds_read_b128 v[228:231], v234 offset:20480\n"
      "s_add_u32 s78, s64, 0x27000\n"
      "s_addc_u32 s79, s65, 0\n"
      "s_add_u32 m0, s74, 0xc00\n"
      "v_mfma_f32_16x16x32_bf16 v[60:63], a[60:63], v[216:219], v[60:63]\n"
      "global_load_lds_dwordx4 v240, s[78:79]\n"
      "v_mfma_f32_16x16x32_bf16 v[64:67], a[64:67], v[216:219], v[64:67]\n"
      "v_mfma_f32_16x16x32_bf16 v[68:71], a[68:71], v[216:219], v[68:71]\n"
      "s_waitcnt lgkmcnt(2)\n"
      "v_mfma_f32_16x16x32_bf16 v[72:75], a[48:51], v[220:223], v[72:75]\n"
      "s_add_u32 m0, s74, 0x1000\n"
      "v_mfma_f32_16x16x32_bf16 v[76:79], a[52:55], v[220:223], v[76:79]\n"
      "global_load_lds_dwordx4 v241, s[78:79]\n"
      "v_mfma_f32_16x16x32_bf16 v[80:83], a[56:59], v[220:223], v[80:83]\n"
      "ds_read_b128 v[216:219], v234 offset:24576\n"
      "v_mfma_f32_16x16x32_bf16 v[84:87], a[60:63], v[220:223], v[84:87]\n"
      "v_mfma_f32_16x16x32_bf16 v[88:91], a[64:67], v[220:223], v[88:91]\n"
      "s_add_u32 m0, s74, 0x1400\n"
      "v_mfma_f32_16x16x32_bf16 v[92:95], a[68:71], v[220:223], v[92:95]\n"
      "global_load_lds_dwordx4 v242, s[78:79]\n"
      "s_waitcnt lgkmcnt(2)\n"
      "v_mfma_f32_16x16x32_bf16 v[96:99], a[48:51], v[224:227], v[96:99]\n"
      "v_mfma_f32_16x16x32_bf16 v[100:103], a[52:55], v[224:227], v[100:103]\n"
      "v_mfma_f32_16x16x32_bf16 v[104:107], a[56:59], v[224:227], v[104:107]\n"
      "ds_read_b128 v[220:223], v234 offset:28672\n"
      "s_add_u32 s78, s64, 0x2a000\n"
      "s_addc_u32 s79, s65, 0\n"
      "s_add_u32 m0, s74, 0x6000\n"
      "v_mfma_f32_16x16x32_bf16 v[108:111], a[60:63], v[224:227], v[108:111]\n"
      "global_load_lds_dwordx4 v240, s[78:79]\n"
      "v_mfma_f32_16x16x32_bf16 v[112:115], a[64:67], v[224:227], v[112:115]\n"
      "v_mfma_f32_16x16x32_bf16 v[116:119], a[68:71], v[224:227], v[116:119]\n"
      "s_waitcnt lgkmcnt(2)\n"
      "v_mfma_f32_16x16x32_bf16 v[120:123], a[48:51], v[228:231], v[120:123]\n"
      "s_add_u32 m0, s74, 0x6400\n"
      "v_mfma_f32_16x16x32_bf16 v[124:127], a[52:55], v[228:231], v[124:127]\n"
      "global_load_lds_dwordx4 v241, s[78:79]\n"
      "v_mfma_f32_16x16x32_bf16 v[128:131], a[56:59], v[228:231], v[128:131]\n"
      "ds_read_b128 v[224:227], v234 offset:32768\n"
      "v_add_u32 v234, 0x9000, v234\n"
      "v_mfma_f32_16x16x32_bf16 v[132:135], a[60:63], v[228:231], v[132:135]\n"
      "v_mfma_f32_16x16x32_bf16 v[136:139], a[64:67], v[228:231], v[136:139]\n"
      "s_waitcnt vmcnt(8)\n"
      "s_add_u32 m0, s74, 0x6800\n"
      "v_mfma_f32_16x16x32_bf16 v[140:143], a[68:71], v[228:231], v[140:143]\n"
      "global_load_lds_dwordx4 v242, s[78:79]\n"
      "s_waitcnt lgkmcnt(2)\n"
      "v_mfma_f32_16x16x32_bf16 v[144:147], a[48:51], v[216:219], v[144:147]\n"
      "ds_read_b64_tr_b16 a[120:121], v243 offset:49152\n"
      "v_mfma_f32_16x16x32_bf16 v[148:151], a[52:55], v[216:219], v[148:151]\n"
      "ds_read_b64_tr_b16 a[122:123], v243 offset:49920\n"
      "v_mfma_f32_16x16x32_bf16 v[152:155], a[56:59], v[216:219], v[152:155]\n"
      "ds_read_b128 v[228:231], v235 offset:0\n"
      "s_add_u32 s78, s64, 0x2d000\n"
      "s_addc_u32 s79, s65, 0\n"
      "s_add_u32 m0, s74, 0x6c00\n"
      "v_mfma_f32_16x16x32_bf16 v[156:159], a[60:63], v[216:219], v[156:159]\n"
      "ds_read_b64_tr_b16 a[124:125], v243 offset:49160\n"
      "global_load_lds_dwordx4 v240, s[78:79]\n"
      "v_mfma_f32_16x16x32_bf16 v[160:163], a[64:67], v[216:219], v[160:163]\n"
      "ds_read_b64_tr_b16 a[126:127], v243 offset:49928\n"
      "v_mfma_f32_16x16x32_bf16 v[164:167], a[68:71], v[216:219], v[164:167]\n"
      "s_waitcnt lgkmcnt(6)\n"
      "v_mfma_f32_16x16x32_bf16 v[168:171], a[48:51], v[220:223], v[168:171]\n"
      "ds_read_b64_tr_b16 a[128:129], v243 offset:49216\n"
      "s_add_u32 m0, s74, 0x7000\n"
      "v_mfma_f32_16x16x32_bf16 v[172:175], a[52:55], v[220:223], v[172:175]\n"
      "ds_read_b64_tr_b16 a[130:131], v243 offset:49984\n"
      "global_load_lds_dwordx4 v241, s[78:79]\n"
      "v_mfma_f32_16x16x32_bf16 v[176:179], a[56:59], v[220:223], v[176:179]\n"
      "ds_read_b128 v[216:219], v235 offset:4096\n"
      "v_mfma_f32_16x16x32_bf16 v[180:183], a[60:63], v[220:223], v[180:183]\n"
      "ds_read_b64_tr_b16 a[132:133], v243 offset:49224\n"
      "v_mfma_f32_16x16x32_bf16 v[184:187], a[64:67], v[220:223], v[184:187]\n"
      "ds_read_b64_tr_b16 a[134:135], v243 offset:49992\n"
      "s_add_u32 m0, s74, 0x7400\n"
      "v_mfma_f32_16x16x32_bf16 v[188:191], a[68:71], v[220:223], v[188:191]\n"
      "global_load_lds_dwordx4 v242, s[78:79]\n"
      "s_waitcnt lgkmcnt(10)\n"
      "v_mfma_f32_16x16x32_bf16 v[192:195], a[48:51], v[224:227], v[192:195]\n"
      "ds_read_b64_tr_b16 a[136:137], v243 offset:49280\n"
      "v_mfma_f32_16x16x32_bf16 v[196:199], a[52:55], v[224:227], v[196:199]\n"
      "ds_read_b64_tr_b16 a[138:139], v243 offset:50048\n"
      "v_mfma_f32_16x16x32_bf16 v[200:203], a[56:59], v[224:227], v[200:203]\n"
      "ds_read_b128 v[220:223], v235 offset:8192\n"
      "global_load_dwordx4 v[244:247], v248, s[28:29] sc1\n"
      "v_mfma_f32_16x16x32_bf16 v[204:207], a[60:63], v[224:227], v[204:207]\n"
      "ds_read_b64_tr_b16 a[140:141], v243 offset:49288\n"
      "v_mfma_f32_16x16x32_bf16 v[208:211], a[64:67], v[224:227], v[208:211]\n"
      "ds_read_b64_tr_b16 a[142:143], v243 offset:50056\n"
      "v_mfma_f32_16x16x32_bf16 v[212:215], a[68:71], v[224:227], v[212:215]\n"
      "s_waitcnt lgkmcnt(12)\n"
      "v_mfma_f32_16x16x32_bf16 v[0:3], a[72:75], v[228:231], v[0:3]\n"
      "v_mfma_f32_16x16x32_bf16 v[4:7], a[76:79], v[228:231], v[4:7]\n"
      "v_mfma_f32_16x16x32_bf16 v[8:11], a[80:83], v[228:231], v[8:11]\n"
      "ds_read_b128 v[224:227], v235 offset:12288\n"
      "v_mfma_f32_16x16x32_bf16 v[12:15], a[84:87], v[228:231], v[12:15]\n"
      "v_mfma_f32_16x16x32_bf16 v[16:19], a[88:91], v[228:231], v[16:19]\n"
      "s_waitcnt vmcnt(7)\n"
      "v_mfma_f32_16x16x32_bf16 v[20:23], a[92:95], v[228:231], v[20:23]\n"
      "s_waitcnt lgkmcnt(8)\n"
      "v_mfma_f32_16x16x32_bf16 v[24:27], a[72:75], v[216:219], v[24:27]\n"
      "ds_read_b64_tr_b16 a[144:145], v243 offset:0\n"
      "v_mfma_f32_16x16x32_bf16 v[28:31], a[76:79], v[216:219], v[28:31]\n"
      "ds_read_b64_tr_b16 a[146:147], v243 offset:768\n"
      "v_mfma_f32_16x16x32_bf16 v[32:35], a[80:83], v[216:219], v[32:35]\n"
      "ds_read_b128 v[228:231], v235 offset:16384\n"
      "s_waitcnt lgkmcnt(4)\n"
      "s_add_u32 s78, s64, 0x30000\n"
      "s_addc_u32 s79, s65, 0\n"
      "s_add_u32 m0, s74, 0xc000\n"
      "v_mfma_f32_16x16x32_bf16 v[36:39], a[84:87], v[216:219], v[36:39]\n"
      "ds_read_b64_tr_b16 a[148:149], v243 offset:8\n"
      "global_load_lds_dwordx4 v240, s[78:79]\n"
      "v_mfma_f32_16x16x32_bf16 v[40:43], a[88:91], v[216:219], v[40:43]\n"
      "ds_read_b64_tr_b16 a[150:151], v243 offset:776\n"
      "v_mfma_f32_16x16x32_bf16 v[44:47], a[92:95], v[216:219], v[44:47]\n"
      "v_mfma_f32_16x16x32_bf16 v[48:51], a[72:75], v[220:223], v[48:51]\n"
      "ds_read_b64_tr_b16 a[152:153], v243 offset:64\n"
      "s_add_u32 m0, s74, 0xc400\n"
      "v_mfma_f32_16x16x32_bf16 v[52:55], a[76:79], v[220:223], v[52:55]\n"
      "ds_read_b64_tr_b16 a[154:155], v243 offset:832\n"
      "global_load_lds_dwordx4 v241, s[78:79]\n"
      "v_mfma_f32_16x16x32_bf16 v[56:59], a[80:83], v[220:223], v[56:59]\n"
      "ds_read_b128 v[216:219], v235 offset:20480\n"
      "v_mfma_f32_16x16x32_bf16 v[60:63], a[84:87], v[220:223], v[60:63]\n"
      "ds_read_b64_tr_b16 a[156:157], v243 offset:72\n"
      "v_mfma_f32_16x16x32_bf16 v[64:67], a[88:91], v[220:223], v[64:67]\n"
      "ds_read_b64_tr_b16 a[158:159], v243 offset:840\n"
      "s_add_u32 m0, s74, 0xc800\n"
      "v_mfma_f32_16x16x32_bf16 v[68:71], a[92:95], v[220:223], v[68:71]\n"
      "global_load_lds_dwordx4 v242, s[78:79]\n"
      "s_waitcnt lgkmcnt(10)\n"
      "v_mfma_f32_16x16x32_bf16 v[72:75], a[72:75], v[224:227], v[72:75]\n"
      "ds_read_b64_tr_b16 a[160:161], v243 offset:128\n"
      "v_mfma_f32_16x16x32_bf16 v[76:79], a[76:79], v[224:227], v[76:79]\n"
      "ds_read_b64_tr_b16 a[162:163], v243 offset:896\n"
      "v_mfma_f32_16x16x32_bf16 v[80:83], a[80:83], v[224:227], v[80:83]\n"
      "ds_read_b128 v[220:223], v235 offset:24576\n"
      "s_add_u32 s78, s64, 0x33000\n"
      "s_addc_u32 s79, s65, 0\n"
      "s_add_u32 m0, s74, 0xcc00\n"
      "v_mfma_f32_16x16x32_bf16 v[84:87], a[84:87], v[224:227], v[84:87]\n"
      "ds_read_b64_tr_b16 a[164:165], v243 offset:136\n"
      "global_load_lds_dwordx4 v240, s[78:79]\n"
      "v_mfma_f32_16x16x32_bf16 v[88:91], a[88:91], v[224:227], v[88:91]\n"
      "ds_read_b64_tr_b16 a[166:167], v243 offset:904\n"
      "v_mfma_f32_16x16x32_bf16 v[92:95], a[92:95], v[224:227], v[92:95]\n"
      "s_waitcnt lgkmcnt(12)\n"
      "v_mfma_f32_16x16x32_bf16 v[96:99], a[72:75], v[228:231], v[96:99]\n"
      "s_add_u32 m0, s74, 0xd000\n"
      "v_mfma_f32_16x16x32_bf16 v[100:103], a[76:79], v[228:231], v[100:103]\n"
      "global_load_lds_dwordx4 v241, s[78:79]\n"
      "v_mfma_f32_16x16x32_bf16 v[104:107], a[80:83], v[228:231], v[104:107]\n"
      "ds_read_b128 v[224:227], v235 offset:28672\n"
      "v_mfma_f32_16x16x32_bf16 v[108:111], a[84:87], v[228:231], v[108:111]\n"
      "v_mfma_f32_16x16x32_bf16 v[112:115], a[88:91], v[228:231], v[112:115]\n"
      "s_waitcnt vmcnt(6)\n"
      "s_add_u32 m0, s74, 0xd400\n"
      "v_mfma_f32_16x16x32_bf16 v[116:119], a[92:95], v[228:231], v[116:119]\n"
      "global_load_lds_dwordx4 v242, s[78:79]\n"
      "s_waitcnt lgkmcnt(8)\n"
      "v_mfma_f32_16x16x32_bf16 v[120:123], a[72:75], v[216:219], v[120:123]\n"
      "ds_read_b64_tr_b16 a[168:169], v243 offset:24576\n"
      "v_mfma_f32_16x16x32_bf16 v[124:127], a[76:79], v[216:219], v[124:127]\n"
      "ds_read_b64_tr_b16 a[170:171], v243 offset:25344\n"
      "v_mfma_f32_16x16x32_bf16 v[128:131], a[80:83], v[216:219], v[128:131]\n"
      "ds_read_b128 v[228:231], v235 offset:32768\n"
      "v_add_u32 v235, 0x9000, v235\n"
      "s_waitcnt lgkmcnt(4)\n"
      "s_add_u32 s78, s64, 0x36000\n"
      "s_addc_u32 s79, s65, 0\n"
      "s_add_u32 m0, s74, 0x0\n"
      "v_mfma_f32_16x16x32_bf16 v[132:135], a[84:87], v[216:219], v[132:135]\n"
      "ds_read_b64_tr_b16 a[172:173], v243 offset:24584\n"
      "global_load_lds_dwordx4 v240, s[78:79]\n"
      "v_mfma_f32_16x16x32_bf16 v[136:139], a[88:91], v[216:219], v[136:139]\n"
      "ds_read_b64_tr_b16 a[174:175], v243 offset:25352\n"
      "v_mfma_f32_16x16x32_bf16 v[140:143], a[92:95], v[216:219], v[140:143]\n"
      "v_mfma_f32_16x16x32_bf16 v[144:147], a[72:75], v[220:223], v[144:147]\n"
      "ds_read_b64_tr_b16 a[176:177], v243 offset:24640\n"
      "s_waitcnt vmcnt(7)\n"
      "v_cmp_ne_u64 vcc, s[80:81], v[244:245]\n"
      "v_cmp_ne_u64 s[56:57], s[80:81], v[246:247]\n"
      "s_nop 3\n"
      "s_or_b64 vcc, vcc, s[56:57]\n"
      "s_cbranch_scc0 LP1_1\n"
      "s_mov_b32 s82, 0x40000\n"
      "LP0_1:\n"
      "s_sleep 2\n"
      "global_load_dwordx4 v[244:247], v248, s[28:29] sc1\n"
      "s_sub_u32 s82, s82, 1\n"
      "s_cbranch_scc1 LP2_1\n"
      "s_waitcnt vmcnt(0)\n"
      "v_cmp_ne_u64 vcc, s[80:81], v[244:245]\n"
      "v_cmp_ne_u64 s[56:57], s[80:81], v[246:247]\n"
      "s_nop 3\n"
      "s_or_b64 vcc, vcc, s[56:57]\n"
      "s_cbranch_scc1 LP0_1\n"
      "LP2_1:\n"
      "s_waitcnt vmcnt(0)\n"
      "LP1_1:\n"
      "s_nop 0\n"
      "v_mfma_f32_16x16x32_bf16 v[148:151], a[76:79], v[220:223], v[148:151]\n"
      "s_barrier\n"
      "s_add_u32 m0, s74, 0x400\n"
      "v_mfma_f32_16x16x32_bf16 v[152:155], a[80:83], v[220:223], v[152:155]\n"
      "ds_read_b128 v[216:219], v232 offset:0\n"
      "global_load_lds_dwordx4 v241, s[78:79]\n"
      "v_mfma_f32_16x16x32_bf16 v[156:159], a[84:87], v[220:223], v[156:159]\n"
      "ds_read_b64_tr_b16 a[178:179], v243 offset:25408\n"
      "v_mfma_f32_16x16x32_bf16 v[160:163], a[88:91], v[220:223], v[160:163]\n"
      "ds_read_b64_tr_b16 a[180:181], v243 offset:24648\n"
      "v_mfma_f32_16x16x32_bf16 v[164:167], a[92:95], v[220:223], v[164:167]\n"
      "s_add_u32 m0, s74, 0x800\n"
      "s_waitcnt lgkmcnt(9)\n"
      "v_mfma_f32_16x16x32_bf16 v[168:171], a[72:75], v[224:227], v[168:171]\n"
      "ds_read_b64_tr_b16 a[182:183], v243 offset:25416\n"
      "global_load_lds_dwordx4 v242, s[78:79]\n"
      "v_mfma_f32_16x16x32_bf16 v[172:175], a[76:79], v[224:227], v[172:175]\n"
      "ds_read_b64_tr_b16 a[184:185], v243 offset:24704\n"
      "v_mfma_f32_16x16x32_bf16 v[176:179], a[80:83], v[224:227], v[176:179]\n"
      "ds_read_b128 v[220:223], v232 offset:4096\n"
      "v_mfma_f32_16x16x32_bf16 v[180:183], a[84:87], v[224:227], v[180:183]\n"
      "ds_read_b64_tr_b16 a[186:187], v243 offset:25472\n"
      "s_add_u32 s78, s64, 0x39000\n"
      "s_addc_u32 s79, s65, 0\n"
      "s_add_u32 m0, s74, 0xc00\n"
      "v_mfma_f32_16x16x32_bf16 v[184:187], a[88:91], v[224:227], v[184:187]\n"
      "ds_read_b64_tr_b16 a[188:189], v243 offset:24712\n"
      "global_load_lds_dwordx4 v240, s[78:79]\n"
      "v_mfma_f32_16x16x32_bf16 v[188:191], a[92:95], v[224:227], v[188:191]\n"
      "s_waitcnt lgkmcnt(11)\n"
      "v_mfma_f32_16x16x32_bf16 v[192:195], a[72:75], v[228:231], v[192:195]\n"
      "ds_read_b64_tr_b16 a[190:191], v243 offset:25480\n"
      "v_mfma_f32_16x16x32_bf16 v[196:199], a[76:79], v[228:231], v[196:199]\n"
      "s_add_u32 m0, s74, 0x1000\n"
      "v_mfma_f32_16x16x32_bf16 v[200:203], a[80:83], v[228:231], v[200:203]\n"
      "ds_read_b128 v[224:227], v232 offset:8192\n"
      "global_load_lds_dwordx4 v241, s[78:79]\n"
      "v_mfma_f32_16x16x32_bf16 v[204:207], a[84:87], v[228:231], v[204:207]\n"
      "v_mfma_f32_16x16x32_bf16 v[208:211], a[88:91], v[228:231], v[208:211]\n"
      "v_mfma_f32_16x16x32_bf16 v[212:215], a[92:95], v[228:231], v[212:215]\n"
      "s_add_u32 m0, s74, 0x1400\n"
      "s_waitcnt lgkmcnt(9)\n"
      "v_mfma_f32_16x16x32_bf16 v[0:3], a[96:99], v[216:219], v[0:3]\n"
      "global_load_lds_dwordx4 v242, s[78:79]\n"
      "v_mfma_f32_16x16x32_bf16 v[4:7], a[100:103], v[216:219], v[4:7]\n"
      "s_barrier\n"
      "v_mfma_f32_16x16x32_bf16 v[8:11], a[104:107], v[216:219], v[8:11]\n"
      "ds_read_b128 v[228:231], v232 offset:12288\n"
      "v_mfma_f32_16x16x32_bf16 v[12:15], a[108:111], v[216:219], v[12:15]\n"
      "s_add_u32 s58, s24, 0x0\n"
      "s_addc_u32 s59, s25, 0\n"
      "global_load_dwordx4 a[240:243], v239, s[58:59] offset:0 sc1\n"
      "v_mfma_f32_16x16x32_bf16 v[16:19], a[112:115], v[216:219], v[16:19]\n"
      "v_mfma_f32_16x16x32_bf16 v[20:23], a[116:119], v[216:219], v[20:23]\n"
      "s_waitcnt lgkmcnt(5)\n"
      "v_mfma_f32_16x16x32_bf16 v[24:27], a[96:99], v[220:223], v[24:27]\n"
      "v_mfma_f32_16x16x32_bf16 v[28:31], a[100:103], v[220:223], v[28:31]\n"
      "s_waitcnt lgkmcnt(2)\n"
      "s_add_u32 s78, s64, 0x3c000\n"
      "s_addc_u32 s79, s65, 0\n"
      "s_add_u32 m0, s74, 0x6000\n"
      "v_mfma_f32_16x16x32_bf16 v[32:35], a[104:107], v[220:223], v[32:35]\n"
      "ds_read_b128 v[216:219], v232 offset:16384\n"
      "global_load_lds_dwordx4 v240, s[78:79]\n"
      "v_mfma_f32_16x16x32_bf16 v[36:39], a[108:111], v[220:223], v[36:39]\n"
      "v_mfma_f32_16x16x32_bf16 v[40:43], a[112:115], v[220:223], v[40:43]\n"
      "s_waitcnt vmcnt(8)\n"
      "v_mfma_f32_16x16x32_bf16 v[44:47], a[116:119], v[220:223], v[44:47]\n"
      "s_add_u32 m0, s74, 0x6400\n"
      "s_waitcnt lgkmcnt(2)\n"
      "v_mfma_f32_16x16x32_bf16 v[48:51], a[96:99], v[224:227], v[48:51]\n"
      "ds_read_b64_tr_b16 a[192:193], v243 offset:49152\n"
      "global_load_lds_dwordx4 v241, s[78:79]\n"
      "v_mfma_f32_16x16x32_bf16 v[52:55], a[100:103], v[224:227], v[52:55]\n"
      "ds_read_b64_tr_b16 a[194:195], v243 offset:49920\n"
      "v_mfma_f32_16x16x32_bf16 v[56:59], a[104:107], v[224:227], v[56:59]\n"
      "ds_read_b128 v[220:223], v232 offset:20480\n"
      "v_mfma_f32_16x16x32_bf16 v[60:63], a[108:111], v[224:227], v[60:63]\n"
      "ds_read_b64_tr_b16 a[196:197], v243 offset:49160\n"
      "s_add_u32 m0, s74, 0x6800\n"
      "v_mfma_f32_16x16x32_bf16 v[64:67], a[112:115], v[224:227], v[64:67]\n"
      "ds_read_b64_tr_b16 a[198:199], v243 offset:49928\n"
      "global_load_lds_dwordx4 v242, s[78:79]\n"
      "v_mfma_f32_16x16x32_bf16 v[68:71], a[116:119], v[224:227], v[68:71]\n"
      "s_waitcnt lgkmcnt(6)\n"
      "v_mfma_f32_16x16x32_bf16 v[72:75], a[96:99], v[228:231], v[72:75]\n"
      "ds_read_b64_tr_b16 a[200:201], v243 offset:49216\n"
      "v_mfma_f32_16x16x32_bf16 v[76:79], a[100:103], v[228:231], v[76:79]\n"
      "ds_read_b64_tr_b16 a[202:203], v243 offset:49984\n"
      "s_add_u32 s78, s64, 0x3f000\n"
      "s_addc_u32 s79, s65, 0\n"
      "s_add_u32 m0, s74, 0x6c00\n"
      "v_mfma_f32_16x16x32_bf16 v[80:83], a[104:107], v[228:231], v[80:83]\n"
      "ds_read_b128 v[224:227], v232 offset:24576\n"
      "global_load_lds_dwordx4 v240, s[78:79]\n"
      "v_mfma_f32_16x16x32_bf16 v[84:87], a[108:111], v[228:231], v[84:87]\n"
      "ds_read_b64_tr_b16 a[204:205], v243 offset:49224\n"
      "v_mfma_f32_16x16x32_bf16 v[88:91], a[112:115], v[228:231], v[88:91]\n"
      "ds_read_b64_tr_b16 a[206:207], v243 offset:49992\n"
      "v_mfma_f32_16x16x32_bf16 v[92:95], a[116:119], v[228:231], v[92:95]\n"
      "s_add_u32 m0, s74, 0x7000\n"
      "s_waitcnt lgkmcnt(10)\n"
      "v_mfma_f32_16x16x32_bf16 v[96:99], a[96:99], v[216:219], v[96:99]\n"
      "ds_read_b64_tr_b16 a[208:209], v243 offset:49280\n"
      "global_load_lds_dwordx4 v241, s[78:79]\n"
      "v_mfma_f32_16x16x32_bf16 v[100:103], a[100:103], v[216:219], v[100:103]\n"
      "ds_read_b64_tr_b16 a[210:211], v243 offset:50048\n"
      "v_mfma_f32_16x16x32_bf16 v[104:107], a[104:107], v[216:219], v[104:107]\n"
      "ds_read_b128 v[228:231], v232 offset:28672\n"
      "v_mfma_f32_16x16x32_bf16 v[108:111], a[108:111], v[216:219], v[108:111]\n"
      "ds_read_b64_tr_b16 a[212:213], v243 offset:49288\n"
      "s_add_u32 m0, s74, 0x7400\n"
      "v_mfma_f32_16x16x32_bf16 v[112:115], a[112:115], v[216:219], v[112:115]\n"
      "ds_read_b64_tr_b16 a[214:215], v243 offset:50056\n"
      "global_load_lds_dwordx4 v242, s[78:79]\n"
      "v_mfma_f32_16x16x32_bf16 v[116:119], a[116:119], v[216:219], v[116:119]\n"
      "s_waitcnt lgkmcnt(12)\n"
      "v_mfma_f32_16x16x32_bf16 v[120:123], a[96:99], v[220:223], v[120:123]\n"
      "v_mfma_f32_16x16x32_bf16 v[124:127], a[100:103], v[220:223], v[124:127]\n"
      "global_load_dwordx4 a[244:247], v239, s[58:59] offset:1024 sc1\n"
      "v_mfma_f32_16x16x32_bf16 v[128:131], a[104:107], v[220:223], v[128:131]\n"
      "ds_read_b128 v[216:219], v232 offset:32768\n"
      "v_add_u32 v232, 0xffff7000, v232\n"
      "v_mfma_f32_16x16x32_bf16 v[132:135], a[108:111], v[220:223], v[132:135]\n"
      "v_mfma_f32_16x16x32_bf16 v[136:139], a[112:115], v[220:223], v[136:139]\n"
      "v_mfma_f32_16x16x32_bf16 v[140:143], a[116:119], v[220:223], v[140:143]\n"
      "global_load_dwordx4 a[248:251], v239, s[58:59] offset:2048 sc1\n"
      "s_waitcnt lgkmcnt(8)\n"
      "v_mfma_f32_16x16x32_bf16 v[144:147], a[96:99], v[224:227], v[144:147]\n"
      "s_waitcnt vmcnt(9)\n"
      "v_mfma_f32_16x16x32_bf16 v[148:151], a[100:103], v[224:227], v[148:151]\n"
      "ds_read_b64_tr_b16 a[216:217], v243 offset:0\n"
      "v_mfma_f32_16x16x32_bf16 v[152:155], a[104:107], v[224:227], v[152:155]\n"
      "ds_read_b128 v[220:223], v233 offset:0\n"
      "v_mfma_f32_16x16x32_bf16 v[156:159], a[108:111], v[224:227], v[156:159]\n"
      "ds_read_b64_tr_b16 a[218:219], v243 offset:768\n"
      "s_waitcnt lgkmcnt(4)\n"
      "s_add_u32 s78, s64, 0x42000\n"
      "s_addc_u32 s79, s65, 0\n"
      "s_add_u32 m0, s74, 0xc000\n"
      "v_mfma_f32_16x16x32_bf16 v[160:163], a[112:115], v[224:227], v[160:163]\n"
      "ds_read_b64_tr_b16 a[220:221], v243 offset:8\n"
      "global_load_lds_dwordx4 v240, s[78:79]\n"
      "v_mfma_f32_16x16x32_bf16 v[164:167], a[116:119], v[224:227], v[164:167]\n"
      "v_mfma_f32_16x16x32_bf16 v[168:171], a[96:99], v[228:231], v[168:171]\n"
      "ds_read_b64_tr_b16 a[222:223], v243 offset:776\n"
      "v_mfma_f32_16x16x32_bf16 v[172:175], a[100:103], v[228:231], v[172:175]\n"
      "ds_read_b64_tr_b16 a[224:225], v243 offset:64\n"
      "s_add_u32 m0, s74, 0xc400\n"
      "v_mfma_f32_16x16x32_bf16 v[176:179], a[104:107], v[228:231], v[176:179]\n"
      "ds_read_b128 v[224:227], v233 offset:4096\n"
      "global_load_lds_dwordx4 v241, s[78:79]\n"
      "v_mfma_f32_16x16x32_bf16 v[180:183], a[108:111], v[228:231], v[180:183]\n"
      "ds_read_b64_tr_b16 a[226:227], v243 offset:832\n"
      "v_mfma_f32_16x16x32_bf16 v[184:187], a[112:115], v[228:231], v[184:187]\n"
      "ds_read_b64_tr_b16 a[228:229], v243 offset:72\n"
      "v_mfma_f32_16x16x32_bf16 v[188:191], a[116:119], v[228:231], v[188:191]\n"
      "s_add_u32 m0, s74, 0xc800\n"
      "s_waitcnt lgkmcnt(9)\n"
      "v_mfma_f32_16x16x32_bf16 v[192:195], a[96:99], v[216:219], v[192:195]\n"
      "ds_read_b64_tr_b16 a[230:231], v243 offset:840\n"
      "global_load_lds_dwordx4 v242, s[78:79]\n"
      "v_mfma_f32_16x16x32_bf16 v[196:199], a[100:103], v[216:219], v[196:199]\n"
      "ds_read_b64_tr_b16 a[232:233], v243 offset:128\n"
      "v_mfma_f32_16x16x32_bf16 v[200:203], a[104:107], v[216:219], v[200:203]\n"
      "ds_read_b128 v[228:231], v233 offset:8192\n"
      "v_mfma_f32_16x16x32_bf16 v[204:207], a[108:111], v[216:219], v[204:207]\n"
      "ds_read_b64_tr_b16 a[234:235], v243 offset:896\n"
      "s_add_u32 s78, s64, 0x45000\n"
      "s_addc_u32 s79, s65, 0\n"
      "s_add_u32 m0, s74, 0xcc00\n"
      "v_mfma_f32_16x16x32_bf16 v[208:211], a[112:115], v[216:219], v[208:211]\n"
      "ds_read_b64_tr_b16 a[236:237], v243 offset:136\n"
      "global_load_lds_dwordx4 v240, s[78:79]\n"
      "v_mfma_f32_16x16x32_bf16 v[212:215], a[116:119], v[216:219], v[212:215]\n"
      "s_waitcnt lgkmcnt(12)\n"
      "v_mfma_f32_16x16x32_bf16 v[0:3], a[120:123], v[220:223], v[0:3]\n"
      "ds_read_b64_tr_b16 a[238:239], v243 offset:904\n"
      "v_mfma_f32_16x16x32_bf16 v[4:7], a[124:127], v[220:223], v[4:7]\n"
      "s_add_u32 m0, s74, 0xd000\n"
      "v_mfma_f32_16x16x32_bf16 v[8:11], a[128:131], v[220:223], v[8:11]\n"
      "ds_read_b128 v[216:219], v233 offset:12288\n"
      "global_load_lds_dwordx4 v241, s[78:79]\n"
      "v_mfma_f32_16x16x32_bf16 v[12:15], a[132:135], v[220:223], v[12:15]\n"
      "v_mfma_f32_16x16x32_bf16 v[16:19], a[136:139], v[220:223], v[16:19]\n"
      "v_mfma_f32_16x16x32_bf16 v[20:23], a[140:143], v[220:223], v[20:23]\n"
      "s_add_u32 m0, s74, 0xd400\n"
      "s_waitcnt lgkmcnt(9)\n"
      "v_mfma_f32_16x16x32_bf16 v[24:27], a[120:123], v[224:227], v[24:27]\n"
      "global_load_lds_dwordx4 v242, s[78:79]\n"
      "v_mfma_f32_16x16x32_bf16 v[28:31], a[124:127], v[224:227], v[28:31]\n"
      "v_mfma_f32_16x16x32_bf16 v[32:35], a[128:131], v[224:227], v[32:35]\n"
      "ds_read_b128 v[220:223], v233 offset:16384\n"
      "v_mfma_f32_16x16x32_bf16 v[36:39], a[132:135], v[224:227], v[36:39]\n"
      "s_waitcnt vmcnt(8)\n"
      "v_mad_i32_i24 v238, v251, s89, v238\n"
      "s_add_u32 s56, s22, 0x0\n"
      "s_addc_u32 s57, s23, 0\n"
      "s_add_u32 m0, s18, 0xfffffe00\n"
      "v_mfma_f32_16x16x32_bf16 v[40:43], a[136:139], v[224:227], v[40:43]\n"
      "ds_read_b64_tr_b16 a[0:1], v243 offset:24576\n"
      "global_load_lds_dwordx4 v238, s[56:57] offset:512 \n"
      "v_mfma_f32_16x16x32_bf16 v[44:47], a[140:143], v[224:227], v[44:47]\n"
      "s_waitcnt lgkmcnt(6)\n"
      "v_mfma_f32_16x16x32_bf16 v[48:51], a[120:123], v[228:231], v[48:51]\n"
      "ds_read_b64_tr_b16 a[2:3], v243 offset:25344\n"
      "v_mad_i32_i24 v238, v251, s90, v238\n"
      "s_add_u32 s56, s22, 0x6000\n"
      "s_addc_u32 s57, s23, 0\n"
      "s_add_u32 m0, s18, 0xe00\n"
      "v_mfma_f32_16x16x32_bf16 v[52:55], a[124:127], v[228:231], v[52:55]\n"
      "ds_read_b64_tr_b16 a[4:5], v243 offset:24584\n"
      "global_load_lds_dwordx4 v238, s[56:57] offset:512 \n"
      "v_mfma_f32_16x16x32_bf16 v[56:59], a[128:131], v[228:231], v[56:59]\n"
      "ds_read_b128 v[224:227], v233 offset:20480\n"
      "v_mfma_f32_16x16x32_bf16 v[60:63], a[132:135], v[228:231], v[60:63]\n"
      "ds_read_b64_tr_b16 a[6:7], v243 offset:25352\n"
      "v_mad_i32_i24 v238, v251, s91, v238\n"
      "s_add_u32 s56, s22, 0xc000\n"
      "s_addc_u32 s57, s23, 0\n"
      "s_add_u32 m0, s18, 0x1e00\n"
      "v_mfma_f32_16x16x32_bf16 v[64:67], a[136:139], v[228:231], v[64:67]\n"
      "ds_read_b64_tr_b16 a[8:9], v243 offset:24640\n"
      "global_load_lds_dwordx4 v238, s[56:57] offset:512 \n"
      "v_mfma_f32_16x16x32_bf16 v[68:71], a[140:143], v[228:231], v[68:71]\n"
      "s_waitcnt lgkmcnt(7)\n"
      "v_mfma_f32_16x16x32_bf16 v[72:75], a[120:123], v[216:219], v[72:75]\n"
      "ds_read_b64_tr_b16 a[10:11], v243 offset:25408\n"
      "v_mad_i32_i24 v238, v251, s92, v238\n"
      "s_add_u32 s56, s22, 0x12000\n"
      "s_addc_u32 s57, s23, 0\n"
      "s_add_u32 m0, s18, 0x2e00\n"
      "v_mfma_f32_16x16x32_bf16 v[76:79], a[124:127], v[216:219], v[76:79]\n"
      "ds_read_b64_tr_b16 a[12:13], v243 offset:24648\n"
      "global_load_lds_dwordx4 v238, s[56:57] offset:512 \n"
      "v_mfma_f32_16x16x32_bf16 v[80:83], a[128:131], v[216:219], v[80:83]\n"
      "ds_read_b128 v[228:231], v233 offset:24576\n"
      "v_mfma_f32_16x16x32_bf16 v[84:87], a[132:135], v[216:219], v[84:87]\n"
      "ds_read_b64_tr_b16 a[14:15], v243 offset:25416\n"
      "v_mad_i32_i24 v238, v251, s93, v238\n"
      "s_add_u32 s56, s22, 0x18000\n"
      "s_addc_u32 s57, s23, 0\n"
      "s_add_u32 m0, s18, 0x3e00\n"
      "v_mfma_f32_16x16x32_bf16 v[88:91], a[136:139], v[216:219], v[88:91]\n"
      "ds_read_b64_tr_b16 a[16:17], v243 offset:24704\n"
      "global_load_lds_dwordx4 v238, s[56:57] offset:512 \n"
      "v_mfma_f32_16x16x32_bf16 v[92:95], a[140:143], v[216:219], v[92:95]\n"
      "s_waitcnt lgkmcnt(11)\n"
      "v_mfma_f32_16x16x32_bf16 v[96:99], a[120:123], v[220:223], v[96:99]\n"
      "ds_read_b64_tr_b16 a[18:19], v243 offset:25472\n"
      "v_mad_i32_i24 v238, v251, s94, v238\n"
      "s_add_u32 s56, s22, 0x1e000\n"
      "s_addc_u32 s57, s23, 0\n"
      "s_add_u32 m0, s18, 0x4e00\n"
      "v_mfma_f32_16x16x32_bf16 v[100:103], a[124:127], v[220:223], v[100:103]\n"
      "ds_read_b64_tr_b16 a[20:21], v243 offset:24712\n"
      "global_load_lds_dwordx4 v238, s[56:57] offset:512 \n"
      "v_mfma_f32_16x16x32_bf16 v[104:107], a[128:131], v[220:223], v[104:107]\n"
      "ds_read_b128 v[216:219], v233 offset:28672\n"
      "v_mfma_f32_16x16x32_bf16 v[108:111], a[132:135], v[220:223], v[108:111]\n"
      "ds_read_b64_tr_b16 a[22:23], v243 offset:25480\n"
      "v_mad_i32_i24 v238, v251, s95, v238\n"
      "s_add_u32 s56, s22, 0x24000\n"
      "s_addc_u32 s57, s23, 0\n"
      "s_add_u32 m0, s18, 0x5e00\n"
      "v_mfma_f32_16x16x32_bf16 v[112:115], a[136:139], v[220:223], v[112:115]\n"
      "global_load_lds_dwordx4 v238, s[56:57] offset:512 \n"
      "v_mfma_f32_16x16x32_bf16 v[116:119], a[140:143], v[220:223], v[116:119]\n"
      "s_waitcnt lgkmcnt(11)\n"
      "v_mfma_f32_16x16x32_bf16 v[120:123], a[120:123], v[224:227], v[120:123]\n"
      "v_mad_i32_i24 v238, v251, s96, v238\n"
      "s_add_u32 s56, s22, 0x2a000\n"
      "s_addc_u32 s57, s23, 0\n"
      "s_add_u32 m0, s18, 0x6e00\n"
      "v_mfma_f32_16x16x32_bf16 v[124:127], a[124:127], v[224:227], v[124:127]\n"
      "global_load_lds_dwordx4 v238, s[56:57] offset:512 \n"
      "v_mfma_f32_16x16x32_bf16 v[128:131], a[128:131], v[224:227], v[128:131]\n"
      "ds_read_b128 v[220:223], v233 offset:32768\n"
      "v_add_u32 v233, 0xffff7000, v233\n"
      "v_mfma_f32_16x16x32_bf16 v[132:135], a[132:135], v[224:227], v[132:135]\n"
      "v_mad_i32_i24 v238, v251, s97, v238\n"
      "s_add_u32 s56, s22, 0x30000\n"
      "s_addc_u32 s57, s23, 0\n"
      "s_add_u32 m0, s18, 0x7e00\n"
      "v_mfma_f32_16x16x32_bf16 v[136:139], a[136:139], v[224:227], v[136:139]\n"
      "global_load_lds_dwordx4 v238, s[56:57] offset:512 \n"
      "v_mfma_f32_16x16x32_bf16 v[140:143], a[140:143], v[224:227], v[140:143]\n"
      "s_waitcnt lgkmcnt(7)\n"
      "v_mfma_f32_16x16x32_bf16 v[144:147], a[120:123], v[228:231], v[144:147]\n"
      "v_mfma_f32_16x16x32_bf16 v[148:151], a[124:127], v[228:231], v[148:151]\n"
      "global_load_dwordx4 a[252:255], v239, s[58:59] offset:3072 sc1\n"
      "v_mfma_f32_16x16x32_bf16 v[152:155], a[128:131], v[228:231], v[152:155]\n"
      "ds_read_b128 v[224:227], v234 offset:0\n"
      "v_mfma_f32_16x16x32_bf16 v[156:159], a[132:135], v[228:231], v[156:159]\n"
      "v_mfma_f32_16x16x32_bf16 v[160:163], a[136:139], v[228:231], v[160:163]\n"
      "v_mfma_f32_16x16x32_bf16 v[164:167], a[140:143], v[228:231], v[164:167]\n"
      "s_add_u32 s58, s24, 0x1000\n"
      "s_addc_u32 s59, s25, 0\n"
      "global_load_dwordx4 a[48:51], v239, s[58:59] offset:0 sc1\n"
      "s_waitcnt lgkmcnt(3)\n"
      "v_mfma_f32_16x16x32_bf16 v[168:171], a[120:123], v[216:219], v[168:171]\n"
      "s_waitcnt vmcnt(11)\n"
      "v_mfma_f32_16x16x32_bf16 v[172:175], a[124:127], v[216:219], v[172:175]\n"
      "ds_read_b64_tr_b16 a[24:25], v243 offset:49152\n"
      "v_mfma_f32_16x16x32_bf16 v[176:179], a[128:131], v[216:219], v[176:179]\n"
      "ds_read_b128 v[228:231], v234 offset:4096\n"
      "v_mfma_f32_16x16x32_bf16 v[180:183], a[132:135], v[216:219], v[180:183]\n"
      "ds_read_b64_tr_b16 a[26:27], v243 offset:49920\n"
      "global_load_dwordx4 a[52:55], v239, s[58:59] offset:1024 sc1\n"
      "v_mfma_f32_16x16x32_bf16 v[184:187], a[136:139], v[216:219], v[184:187]\n"
      "LPD5:\n"
      "s_cbranch_execz LPD8\n"
      "ds_read_b64_tr_b16 a[28:29], v243 offset:49160\n"
      "v_mfma_f32_16x16x32_bf16 v[188:191], a[140:143], v[216:219], v[188:191]\n"
      "s_waitcnt lgkmcnt(5)\n"
      "v_mfma_f32_16x16x32_bf16 v[192:195], a[120:123], v[220:223], v[192:195]\n"
      "ds_read_b64_tr_b16 a[30:31], v243 offset:49928\n"
      "v_mfma_f32_16x16x32_bf16 v[196:199], a[124:127], v[220:223], v[196:199]\n"
      "ds_read_b64_tr_b16 a[32:33], v243 offset:49216\n"
      "global_load_dwordx4 a[56:59], v239, s[58:59] offset:2048 sc1\n"
      "v_mfma_f32_16x16x32_bf16 v[200:203], a[128:131], v[220:223], v[200:203]\n"
      "ds_read_b128 v[216:219], v234 offset:8192\n"
      "v_mfma_f32_16x16x32_bf16 v[204:207], a[132:135], v[220:223], v[204:207]\n"
      "ds_read_b64_tr_b16 a[34:35], v243 offset:49984\n"
      "v_mfma_f32_16x16x32_bf16 v[208:211], a[136:139], v[220:223], v[208:211]\n"
      "ds_read_b64_tr_b16 a[36:37], v243 offset:49224\n"
      "v_mfma_f32_16x16x32_bf16 v[212:215], a[140:143], v[220:223], v[212:215]\n"
      "global_load_dwordx4 a[60:63], v239, s[58:59] offset:3072 sc1\n"
      "s_waitcnt lgkmcnt(9)\n"
      "v_mfma_f32_16x16x32_bf16 v[0:3], a[144:147], v[224:227], v[0:3]\n"
      "ds_read_b64_tr_b16 a[38:39], v243 offset:49992\n"
      "v_mfma_f32_16x16x32_bf16 v[4:7], a[148:151], v[224:227], v[4:7]\n"
      "ds_read_b64_tr_b16 a[40:41], v243 offset:49280\n"
      "v_mfma_f32_16x16x32_bf16 v[8:11], a[152:155], v[224:227], v[8:11]\n"
      "ds_read_b128 v[220:223], v234 offset:12288\n"
      "v_mfma_f32_16x16x32_bf16 v[12:15], a[156:159], v[224:227], v[12:15]\n"
      "ds_read_b64_tr_b16 a[42:43], v243 offset:50048\n"
      "s_add_u32 s58, s24, 0x2000\n"
      "s_addc_u32 s59, s25, 0\n"
      "global_load_dwordx4 a[64:67], v239, s[58:59] offset:0 sc1\n"
      "v_mfma_f32_16x16x32_bf16 v[16:19], a[160:163], v[224:227], v[16:19]\n"
      "ds_read_b64_tr_b16 a[44:45], v243 offset:49288\n"
      "v_mfma_f32_16x16x32_bf16 v[20:23], a[164:167], v[224:227], v[20:23]\n"
      "s_waitcnt lgkmcnt(12)\n"
      "v_mfma_f32_16x16x32_bf16 v[24:27], a[144:147], v[228:231], v[24:27]\n"
      "ds_read_b64_tr_b16 a[46:47], v243 offset:50056\n"
      "v_mfma_f32_16x16x32_bf16 v[28:31], a[148:151], v[228:231], v[28:31]\n"
      "global_load_dwordx4 a[68:71], v239, s[58:59] offset:1024 sc1\n"
      "v_mfma_f32_16x16x32_bf16 v[32:35], a[152:155], v[228:231], v[32:35]\n"
      "ds_read_b128 v[224:227], v234 offset:16384\n"
      "v_mfma_f32_16x16x32_bf16 v[36:39], a[156:159], v[228:231], v[36:39]\n"
      "v_mfma_f32_16x16x32_bf16 v[40:43], a[160:163], v[228:231], v[40:43]\n"
      "v_mfma_f32_16x16x32_bf16 v[44:47], a[164:167], v[228:231], v[44:47]\n"
      "global_load_dwordx4 a[72:75], v239, s[58:59] offset:2048 sc1\n"
      "s_waitcnt lgkmcnt(9)\n"
      "v_mfma_f32_16x16x32_bf16 v[48:51], a[144:147], v[216:219], v[48:51]\n"
      "v_mfma_f32_16x16x32_bf16 v[52:55], a[148:151], v[216:219], v[52:55]\n"
      "v_mfma_f32_16x16x32_bf16 v[56:59], a[152:155], v[216:219], v[56:59]\n"
      "ds_read_b128 v[228:231], v234 offset:20480\n"
      "v_mfma_f32_16x16x32_bf16 v[60:63], a[156:159], v[216:219], v[60:63]\n"
      "global_load_dwordx4 a[76:79], v239, s[58:59] offset:3072 sc1\n"
      "v_mfma_f32_16x16x32_bf16 v[64:67], a[160:163], v[216:219], v[64:67]\n"
      "v_mfma_f32_16x16x32_bf16 v[68:71], a[164:167], v[216:219], v[68:71]\n"
      "s_waitcnt lgkmcnt(5)\n"
      "v_mfma_f32_16x16x32_bf16 v[72:75], a[144:147], v[220:223], v[72:75]\n"
      "v_mfma_f32_16x16x32_bf16 v[76:79], a[148:151], v[220:223], v[76:79]\n"
      "s_add_u32 s58, s24, 0x3000\n"
      "s_addc_u32 s59, s25, 0\n"
      "global_load_dwordx4 a[80:83], v239, s[58:59] offset:0 sc1\n"
      "v_mfma_f32_16x16x32_bf16 v[80:83], a[152:155], v[220:223], v[80:83]\n"
      "ds_read_b128 v[216:219], v234 offset:24576\n"
      "v_mfma_f32_16x16x32_bf16 v[84:87], a[156:159], v[220:223], v[84:87]\n"
      "v_mfma_f32_16x16x32_bf16 v[88:91], a[160:163], v[220:223], v[88:91]\n"
      "v_mfma_f32_16x16x32_bf16 v[92:95], a[164:167], v[220:223], v[92:95]\n"
      "global_load_dwordx4 a[84:87], v239, s[58:59] offset:1024 sc1\n"
      "s_waitcnt lgkmcnt(2)\n"
      "v_mfma_f32_16x16x32_bf16 v[96:99], a[144:147], v[224:227], v[96:99]\n"
      "v_mfma_f32_16x16x32_bf16 v[100:103], a[148:151], v[224:227], v[100:103]\n"
      "v_mfma_f32_16x16x32_bf16 v[104:107], a[152:155], v[224:227], v[104:107]\n"
      "ds_read_b128 v[220:223], v234 offset:28672\n"
      "v_mfma_f32_16x16x32_bf16 v[108:111], a[156:159], v[224:227], v[108:111]\n"
      "global_load_dwordx4 a[88:91], v239, s[58:59] offset:2048 sc1\n"
      "v_mfma_f32_16x16x32_bf16 v[112:115], a[160:163], v[224:227], v[112:115]\n"
      "v_mfma_f32_16x16x32_bf16 v[116:119], a[164:167], v[224:227], v[116:119]\n"
      "s_waitcnt lgkmcnt(2)\n"
      "v_mfma_f32_16x16x32_bf16 v[120:123], a[144:147], v[228:231], v[120:123]\n"
      "v_mfma_f32_16x16x32_bf16 v[124:127], a[148:151], v[228:231], v[124:127]\n"
      "global_load_dwordx4 a[92:95], v239, s[58:59] offset:3072 sc1\n"
      "v_mfma_f32_16x16x32_bf16 v[128:131], a[152:155], v[228:231], v[128:131]\n"
      "ds_read_b128 v[224:227], v234 offset:32768\n"
      "v_add_u32 v234, 0xffff7000, v234\n"
      "v_mfma_f32_16x16x32_bf16 v[132:135], a[156:159], v[228:231], v[132:135]\n"
      "v_mfma_f32_16x16x32_bf16 v[136:139], a[160:163], v[228:231], v[136:139]\n"
      "v_mfma_f32_16x16x32_bf16 v[140:143], a[164:167], v[228:231], v[140:143]\n"
      "s_add_u32 s58, s24, 0x4000\n"
      "s_addc_u32 s59, s25, 0\n"
      "global_load_dwordx4 a[96:99], v239, s[58:59] offset:0 sc1\n"
      "s_waitcnt lgkmcnt(2)\n"
      "v_mfma_f32_16x16x32_bf16 v[144:147], a[144:147], v[216:219], v[144:147]\n"
      "ds_read_b64 v[236:237], v250 offset:4104\n"
      "v_mfma_f32_16x16x32_bf16 v[148:151], a[148:151], v[216:219], v[148:151]\n"
      "v_mfma_f32_16x16x32_bf16 v[152:155], a[152:155], v[216:219], v[152:155]\n"
      "ds_read_b128 v[228:231], v235 offset:0\n"
      "v_mfma_f32_16x16x32_bf16 v[156:159], a[156:159], v[216:219], v[156:159]\n"
      "global_load_dwordx4 a[100:103], v239, s[58:59] offset:1024 sc1\n"
      "v_mfma_f32_16x16x32_bf16 v[160:163], a[160:163], v[216:219], v[160:163]\n"
      "v_mfma_f32_16x16x32_bf16 v[164:167], a[164:167], v[216:219], v[164:167]\n"
      "s_waitcnt lgkmcnt(3)\n"
      "v_mfma_f32_16x16x32_bf16 v[168:171], a[144:147], v[220:223], v[168:171]\n"
      "v_mfma_f32_16x16x32_bf16 v[172:175], a[148:151], v[220:223], v[172:175]\n"
      "global_load_dwordx4 a[104:107], v239, s[58:59] offset:2048 sc1\n"
      "v_mfma_f32_16x16x32_bf16 v[176:179], a[152:155], v[220:223], v[176:179]\n"
      "ds_read_b128 v[216:219], v235 offset:4096\n"
      "v_mfma_f32_16x16x32_bf16 v[180:183], a[156:159], v[220:223], v[180:183]\n"
      "v_mfma_f32_16x16x32_bf16 v[184:187], a[160:163], v[220:223], v[184:187]\n"
      "v_mfma_f32_16x16x32_bf16 v[188:191], a[164:167], v[220:223], v[188:191]\n"
      "global_load_dwordx4 a[108:111], v239, s[58:59] offset:3072 sc1\n"
      "s_waitcnt lgkmcnt(3)\n"
      "v_mfma_f32_16x16x32_bf16 v[192:195], a[144:147], v[224:227], v[192:195]\n"
      "v_mfma_f32_16x16x32_bf16 v[196:199], a[148:151], v[224:227], v[196:199]\n"
      "v_mfma_f32_16x16x32_bf16 v[200:203], a[152:155], v[224:227], v[200:203]\n"
      "ds_read_b128 v[220:223], v235 offset:8192\n"
      "v_mfma_f32_16x16x32_bf16 v[204:207], a[156:159], v[224:227], v[204:207]\n"
      "s_add_u32 s58, s24, 0x5000\n"
      "s_addc_u32 s59, s25, 0\n"
      "global_load_dwordx4 a[112:115], v239, s[58:59] offset:0 sc1\n"
      "v_mfma_f32_16x16x32_bf16 v[208:211], a[160:163], v[224:227], v[208:211]\n"
      "v_mfma_f32_16x16x32_bf16 v[212:215], a[164:167], v[224:227], v[212:215]\n"
      "s_waitcnt lgkmcnt(2)\n"
      "v_mfma_f32_16x16x32_bf16 v[0:3], a[168:171], v[228:231], v[0:3]\n"
      "v_mfma_f32_16x16x32_bf16 v[4:7], a[172:175], v[228:231], v[4:7]\n"
      "global_load_dwordx4 a[116:119], v239, s[58:59] offset:1024 sc1\n"
      "v_mfma_f32_16x16x32_bf16 v[8:11], a[176:179], v[228:231], v[8:11]\n"
      "ds_read_b128 v[224:227], v235 offset:12288\n"
      "v_mfma_f32_16x16x32_bf16 v[12:15], a[180:183], v[228:231], v[12:15]\n"
      "v_mfma_f32_16x16x32_bf16 v[16:19], a[184:187], v[228:231], v[16:19]\n"
      "v_mfma_f32_16x16x32_bf16 v[20:23], a[188:191], v[228:231], v[20:23]\n"
      "global_load_dwordx4 a[120:123], v239, s[58:59] offset:2048 sc1\n"
      "s_waitcnt lgkmcnt(2)\n"
      "v_mfma_f32_16x16x32_bf16 v[24:27], a[168:171], v[216:219], v[24:27]\n"
      "v_mfma_f32_16x16x32_bf16 v[28:31], a[172:175], v[216:219], v[28:31]\n"
      "v_mfma_f32_16x16x32_bf16 v[32:35], a[176:179], v[216:219], v[32:35]\n"
      "ds_read_b128 v[228:231], v235 offset:16384\n"
      "v_mfma_f32_16x16x32_bf16 v[36:39], a[180:183], v[216:219], v[36:39]\n"
      "global_load_dwordx4 a[124:127], v239, s[58:59] offset:3072 sc1\n"
      "v_mfma_f32_16x16x32_bf16 v[40:43], a[184:187], v[216:219], v[40:43]\n"
      "v_mfma_f32_16x16x32_bf16 v[44:47], a[188:191], v[216:219], v[44:47]\n"
      "s_waitcnt lgkmcnt(2)\n"
      "v_mfma_f32_16x16x32_bf16 v[48:51], a[168:171], v[220:223], v[48:51]\n"
      "v_mfma_f32_16x16x32_bf16 v[52:55], a[172:175], v[220:223], v[52:55]\n"
      "s_add_u32 s58, s24, 0x6000\n"
      "s_addc_u32 s59, s25, 0\n"
      "global_load_dwordx4 a[128:131], v239, s[58:59] offset:0 sc1\n"
      "v_mfma_f32_16x16x32_bf16 v[56:59], a[176:179], v[220:223], v[56:59]\n"
      "ds_read_b128 v[216:219], v235 offset:20480\n"
      "v_mfma_f32_16x16x32_bf16 v[60:63], a[180:183], v[220:223], v[60:63]\n"
      "v_mfma_f32_16x16x32_bf16 v[64:67], a[184:187], v[220:223], v[64:67]\n"
      "v_mfma_f32_16x16x32_bf16 v[68:71], a[188:191], v[220:223], v[68:71]\n"
      "global_load_dwordx4 a[132:135], v239, s[58:59] offset:1024 sc1\n"
      "s_waitcnt lgkmcnt(2)\n"
      "v_mfma_f32_16x16x32_bf16 v[72:75], a[168:171], v[224:227], v[72:75]\n"
      "v_mfma_f32_16x16x32_bf16 v[76:79], a[172:175], v[224:227], v[76:79]\n"
      "v_mfma_f32_16x16x32_bf16 v[80:83], a[176:179], v[224:227], v[80:83]\n"
      "ds_read_b128 v[220:223], v235 offset:24576\n"
      "v_mfma_f32_16x16x32_bf16 v[84:87], a[180:183], v[224:227], v[84:87]\n"
      "global_load_dwordx4 a[136:139], v239, s[58:59] offset:2048 sc1\n"
      "v_mfma_f32_16x16x32_bf16 v[88:91], a[184:187], v[224:227], v[88:91]\n"
      "v_mfma_f32_16x16x32_bf16 v[92:95], a[188:191], v[224:227], v[92:95]\n"
      "s_waitcnt lgkmcnt(2)\n"
      "v_mfma_f32_16x16x32_bf16 v[96:99], a[168:171], v[228:231], v[96:99]\n"
      "v_mfma_f32_16x16x32_bf16 v[100:103], a[172:175], v[228:231], v[100:103]\n"
      "global_load_dwordx4 a[140:143], v239, s[58:59] offset:3072 sc1\n"
      "v_mfma_f32_16x16x32_bf16 v[104:107], a[176:179], v[228:231], v[104:107]\n"
      "ds_read_b128 v[224:227], v235 offset:28672\n"
      "v_mfma_f32_16x16x32_bf16 v[108:111], a[180:183], v[228:231], v[108:111]\n"
      "v_mfma_f32_16x16x32_bf16 v[112:115], a[184:187], v[228:231], v[112:115]\n"
      "v_mfma_f32_16x16x32_bf16 v[116:119], a[188:191], v[228:231], v[116:119]\n"
      "s_add_u32 s58, s24, 0x7000\n"
      "s_addc_u32 s59, s25, 0\n"
      "global_load_dwordx4 a[144:147], v239, s[58:59] offset:0 sc1\n"
      "s_waitcnt lgkmcnt(2)\n"
      "v_mfma_f32_16x16x32_bf16 v[120:123], a[168:171], v[216:219], v[120:123]\n"
      "v_mfma_f32_16x16x32_bf16 v[124:127], a[172:175], v[216:219], v[124:127]\n"
      "v_mfma_f32_16x16x32_bf16 v[128:131], a[176:179], v[216:219], v[128:131]\n"
      "ds_read_b128 v[228:231], v235 offset:32768\n"
      "v_add_u32 v235, 0xffff7000, v235\n"
      "v_mfma_f32_16x16x32_bf16 v[132:135], a[180:183], v[216:219], v[132:135]\n"
      "global_load_dwordx4 a[148:151], v239, s[58:59] offset:1024 sc1\n"
      "v_mfma_f32_16x16x32_bf16 v[136:139], a[184:187], v[216:219], v[136:139]\n"
      "v_mfma_f32_16x16x32_bf16 v[140:143], a[188:191], v[216:219], v[140:143]\n"
      "s_waitcnt lgkmcnt(2)\n"
      "v_mfma_f32_16x16x32_bf16 v[144:147], a[168:171], v[220:223], v[144:147]\n"
      "v_mfma_f32_16x16x32_bf16 v[148:151], a[172:175], v[220:223], v[148:151]\n"
      "s_waitcnt vmcnt(27)\n"
      "s_barrier\n"
      "global_load_dwordx4 a[152:155], v239, s[58:59] offset:2048 sc1\n"
      "v_mfma_f32_16x16x32_bf16 v[152:155], a[176:179], v[220:223], v[152:155]\n"
      "ds_read_b128 v[216:219], v232 offset:0\n"
      "v_mfma_f32_16x16x32_bf16 v[156:159], a[180:183], v[220:223], v[156:159]\n"
      "v_mfma_f32_16x16x32_bf16 v[160:163], a[184:187], v[220:223], v[160:163]\n"
      "v_mfma_f32_16x16x32_bf16 v[164:167], a[188:191], v[220:223], v[164:167]\n"
      "global_load_dwordx4 a[156:159], v239, s[58:59] offset:3072 sc1\n"
      "s_waitcnt lgkmcnt(2)\n"
      "v_mfma_f32_16x16x32_bf16 v[168:171], a[168:171], v[224:227], v[168:171]\n"
      "v_mfma_f32_16x16x32_bf16 v[172:175], a[172:175], v[224:227], v[172:175]\n"
      "v_mfma_f32_16x16x32_bf16 v[176:179], a[176:179], v[224:227], v[176:179]\n"
      "ds_read_b128 v[220:223], v232 offset:4096\n"
      "v_mfma_f32_16x16x32_bf16 v[180:183], a[180:183], v[224:227], v[180:183]\n"
      "s_add_u32 s58, s24, 0x8000\n"
      "s_addc_u32 s59, s25, 0\n"
      "global_load_dwordx4 a[160:163], v239, s[58:59] offset:0 sc1\n"
      "v_mfma_f32_16x16x32_bf16 v[184:187], a[184:187], v[224:227], v[184:187]\n"
      "v_mfma_f32_16x16x32_bf16 v[188:191], a[188:191], v[224:227], v[188:191]\n"
      "s_waitcnt lgkmcnt(2)\n"
      "v_mfma_f32_16x16x32_bf16 v[192:195], a[168:171], v[228:231], v[192:195]\n"
      "v_mfma_f32_16x16x32_bf16 v[196:199], a[172:175], v[228:231], v[196:199]\n"
      "global_load_dwordx4 a[164:167], v239, s[58:59] offset:1024 sc1\n"
      "v_mfma_f32_16x16x32_bf16 v[200:203], a[176:179], v[228:231], v[200:203]\n"
      "ds_read_b128 v[224:227], v232 offset:8192\n"
      "v_mfma_f32_16x16x32_bf16 v[204:207], a[180:183], v[228:231], v[204:207]\n"
      "v_mfma_f32_16x16x32_bf16 v[208:211], a[184:187], v[228:231], v[208:211]\n"
      "v_mfma_f32_16x16x32_bf16 v[212:215], a[188:191], v[228:231], v[212:215]\n"
      "s_waitcnt lgkmcnt(2)\n"
      "v_mfma_f32_16x16x32_bf16 v[0:3], a[192:195], v[216:219], v[0:3]\n"
      "global_load_dwordx4 a[168:171], v239, s[58:59] offset:2048 sc1\n"
      "v_mfma_f32_16x16x32_bf16 v[4:7], a[196:199], v[216:219], v[4:7]\n"
      "v_mfma_f32_16x16x32_bf16 v[8:11], a[200:203], v[216:219], v[8:11]\n"
      "ds_read_b128 v[228:231], v232 offset:12288\n"
      "v_mfma_f32_16x16x32_bf16 v[12:15], a[204:207], v[216:219], v[12:15]\n"
      "v_mfma_f32_16x16x32_bf16 v[16:19], a[208:211], v[216:219], v[16:19]\n"
      "global_load_dwordx4 a[172:175], v239, s[58:59] offset:3072 sc1\n"
      "v_mfma_f32_16x16x32_bf16 v[20:23], a[212:215], v[216:219], v[20:23]\n"
      "s_waitcnt lgkmcnt(2)\n"
      "v_mfma_f32_16x16x32_bf16 v[24:27], a[192:195], v[220:223], v[24:27]\n"
      "v_mfma_f32_16x16x32_bf16 v[28:31], a[196:199], v[220:223], v[28:31]\n"
      "v_mfma_f32_16x16x32_bf16 v[32:35], a[200:203], v[220:223], v[32:35]\n"
      "ds_read_b128 v[216:219], v232 offset:16384\n"
      "s_add_u32 s58, s24, 0x9000\n"
      "s_addc_u32 s59, s25, 0\n"
      "global_load_dwordx4 a[176:179], v239, s[58:59] offset:0 sc1\n"
      "v_mfma_f32_16x16x32_bf16 v[36:39], a[204:207], v[220:223], v[36:39]\n"
      "v_mfma_f32_16x16x32_bf16 v[40:43], a[208:211], v[220:223], v[40:43]\n"
      "v_mfma_f32_16x16x32_bf16 v[44:47], a[212:215], v[220:223], v[44:47]\n"
      "s_waitcnt lgkmcnt(2)\n"
      "v_mfma_f32_16x16x32_bf16 v[48:51], a[192:195], v[224:227], v[48:51]\n"
      "global_load_dwordx4 a[180:183], v239, s[58:59] offset:1024 sc1\n"
      "v_mfma_f32_16x16x32_bf16 v[52:55], a[196:199], v[224:227], v[52:55]\n"
      "v_mfma_f32_16x16x32_bf16 v[56:59], a[200:203], v[224:227], v[56:59]\n"
      "ds_read_b128 v[220:223], v232 offset:20480\n"
      "v_mfma_f32_16x16x32_bf16 v[60:63], a[204:207], v[224:227], v[60:63]\n"
      "v_mfma_f32_16x16x32_bf16 v[64:67], a[208:211], v[224:227], v[64:67]\n"
      "global_load_dwordx4 a[184:187], v239, s[58:59] offset:2048 sc1\n"
      "v_mfma_f32_16x16x32_bf16 v[68:71], a[212:215], v[224:227], v[68:71]\n"
      "s_waitcnt lgkmcnt(2)\n"
      "v_mfma_f32_16x16x32_bf16 v[72:75], a[192:195], v[228:231], v[72:75]\n"
      "v_mfma_f32_16x16x32_bf16 v[76:79], a[196:199], v[228:231], v[76:79]\n"
      "v_mfma_f32_16x16x32_bf16 v[80:83], a[200:203], v[228:231], v[80:83]\n"
      "ds_read_b128 v[224:227], v232 offset:24576\n"
      "global_load_dwordx4 a[188:191], v239, s[58:59] offset:3072 sc1\n"
      "v_mfma_f32_16x16x32_bf16 v[84:87], a[204:207], v[228:231], v[84:87]\n"
      "v_mfma_f32_16x16x32_bf16 v[88:91], a[208:211], v[228:231], v[88:91]\n"
      "v_mfma_f32_16x16x32_bf16 v[92:95], a[212:215], v[228:231], v[92:95]\n"
      "s_waitcnt lgkmcnt(2)\n"
      "v_mfma_f32_16x16x32_bf16 v[96:99], a[192:195], v[216:219], v[96:99]\n"
      "s_mul_i32 s78, s98, s19\n"
      "v_mfma_f32_16x16x32_bf16 v[100:103], a[196:199], v[216:219], v[100:103]\n"
      "v_add_u32 v236, s78, v236\n"
      "v_mfma_f32_16x16x32_bf16 v[104:107], a[200:203], v[216:219], v[104:107]\n"
      "ds_read_b128 v[228:231], v232 offset:28672\n"
      "s_mul_i32 s78, s78, s99\n"
      "v_mfma_f32_16x16x32_bf16 v[108:111], a[204:207], v[216:219], v[108:111]\n"
      "v_add_u32 v237, s78, v237\n"
      "v_mfma_f32_16x16x32_bf16 v[112:115], a[208:211], v[216:219], v[112:115]\n"
      "s_add_u32 s52, s20, s32\n"
      "s_addc_u32 s53, s21, 0\n"
      "s_add_u32 m0, s17, 0x0\n"
      "v_mfma_f32_16x16x32_bf16 v[116:119], a[212:215], v[216:219], v[116:119]\n"
      "global_load_lds_dwordx4 v236, s[52:53] offset:0 \n"
      "s_waitcnt lgkmcnt(2)\n"
      "v_mfma_f32_16x16x32_bf16 v[120:123], a[192:195], v[220:223], v[120:123]\n"
      "v_mfma_f32_16x16x32_bf16 v[124:127], a[196:199], v[220:223], v[124:127]\n"
      "s_add_u32 m0, s17, 0x1f00\n"
      "v_mfma_f32_16x16x32_bf16 v[128:131], a[200:203], v[220:223], v[128:131]\n"
      "ds_read_b128 v[216:219], v232 offset:32768\n"
      "v_add_u32 v232, v249, v232\n"
      "global_load_lds_dwordx4 v236, s[52:53] offset:256 \n"
      "v_mfma_f32_16x16x32_bf16 v[132:135], a[204:207], v[220:223], v[132:135]\n"
      "v_mfma_f32_16x16x32_bf16 v[136:139], a[208:211], v[220:223], v[136:139]\n"
      "s_add_u32 m0, s17, 0x3e00\n"
      "v_mfma_f32_16x16x32_bf16 v[140:143], a[212:215], v[220:223], v[140:143]\n"
      "global_load_lds_dwordx4 v236, s[52:53] offset:512 \n"
      "s_waitcnt lgkmcnt(2)\n"
      "v_mfma_f32_16x16x32_bf16 v[144:147], a[192:195], v[224:227], v[144:147]\n"
      "v_mfma_f32_16x16x32_bf16 v[148:151], a[196:199], v[224:227], v[148:151]\n"
      "s_add_u32 m0, s17, 0x1000\n"
      "v_mfma_f32_16x16x32_bf16 v[152:155], a[200:203], v[224:227], v[152:155]\n"
      "ds_read_b128 v[220:223], v233 offset:0\n"
      "global_load_lds_dwordx4 v237, s[52:53] offset:0 \n"
      "v_mfma_f32_16x16x32_bf16 v[156:159], a[204:207], v[224:227], v[156:159]\n"
      "v_mfma_f32_16x16x32_bf16 v[160:163], a[208:211], v[224:227], v[160:163]\n"
      "s_add_u32 m0, s17, 0x2f00\n"
      "v_mfma_f32_16x16x32_bf16 v[164:167], a[212:215], v[224:227], v[164:167]\n"
      "global_load_lds_dwordx4 v237, s[52:53] offset:256 \n"
      "s_waitcnt lgkmcnt(2)\n"
      "v_mfma_f32_16x16x32_bf16 v[168:171], a[192:195], v[228:231], v[168:171]\n"
      "v_mfma_f32_16x16x32_bf16 v[172:175], a[196:199], v[228:231], v[172:175]\n"
      "s_add_u32 m0, s17, 0x4e00\n"
      "v_mfma_f32_16x16x32_bf16 v[176:179], a[200:203], v[228:231], v[176:179]\n"
      "ds_read_b128 v[224:227], v233 offset:4096\n"
      "global_load_lds_dwordx4 v237, s[52:53] offset:512 \n"
      "v_mfma_f32_16x16x32_bf16 v[180:183], a[204:207], v[228:231], v[180:183]\n"
      "v_mfma_f32_16x16x32_bf16 v[184:187], a[208:211], v[228:231], v[184:187]\n"
      "v_mfma_f32_16x16x32_bf16 v[188:191], a[212:215], v[228:231], v[188:191]\n"
      "s_waitcnt lgkmcnt(2)\n"
      "v_mfma_f32_16x16x32_bf16 v[192:195], a[192:195], v[216:219], v[192:195]\n"
      "v_mfma_f32_16x16x32_bf16 v[196:199], a[196:199], v[216:219], v[196:199]\n"
      "v_mfma_f32_16x16x32_bf16 v[200:203], a[200:203], v[216:219], v[200:203]\n"
      "ds_read_b128 v[228:231], v233 offset:8192\n"
      "v_mfma_f32_16x16x32_bf16 v[204:207], a[204:207], v[216:219], v[204:207]\n"
      "v_mfma_f32_16x16x32_bf16 v[208:211], a[208:211], v[216:219], v[208:211]\n"
      "v_mfma_f32_16x16x32_bf16 v[212:215], a[212:215], v[216:219], v[212:215]\n"
      "s_waitcnt lgkmcnt(2)\n"
      "v_mfma_f32_16x16x32_bf16 v[0:3], a[216:219], v[220:223], v[0:3]\n"
      "s_add_u32 s58, s24, 0xa000\n"
      "s_addc_u32 s59, s25, 0\n"
      "global_load_dwordx4 a[192:195], v239, s[58:59] offset:0 sc1\n"
      "v_mfma_f32_16x16x32_bf16 v[4:7], a[220:223], v[220:223], v[4:7]\n"
      "v_mfma_f32_16x16x32_bf16 v[8:11], a[224:227], v[220:223], v[8:11]\n"
      "ds_read_b128 v[216:219], v233 offset:12288\n"
      "v_mfma_f32_16x16x32_bf16 v[12:15], a[228:231], v[220:223], v[12:15]\n"
      "v_mfma_f32_16x16x32_bf16 v[16:19], a[232:235], v[220:223], v[16:19]\n"
      "global_load_dwordx4 a[196:199], v239, s[58:59] offset:1024 sc1\n"
      "v_mfma_f32_16x16x32_bf16 v[20:23], a[236:239], v[220:223], v[20:23]\n"
      "s_waitcnt lgkmcnt(2)\n"
      "v_mfma_f32_16x16x32_bf16 v[24:27], a[216:219], v[224:227], v[24:27]\n"
      "v_mfma_f32_16x16x32_bf16 v[28:31], a[220:223], v[224:227], v[28:31]\n"
      "v_mfma_f32_16x16x32_bf16 v[32:35], a[224:227], v[224:227], v[32:35]\n"
      "ds_read_b128 v[220:223], v233 offset:16384\n"
      "global_load_dwordx4 a[200:203], v239, s[58:59] offset:2048 sc1\n"
      "v_mfma_f32_16x16x32_bf16 v[36:39], a[228:231], v[224:227], v[36:39]\n"
      "v_mfma_f32_16x16x32_bf16 v[40:43], a[232:235], v[224:227], v[40:43]\n"
      "v_mfma_f32_16x16x32_bf16 v[44:47], a[236:239], v[224:227], v[44:47]\n"
      "s_waitcnt lgkmcnt(2)\n"
      "v_mfma_f32_16x16x32_bf16 v[48:51], a[216:219], v[228:231], v[48:51]\n"
      "global_load_dwordx4 a[204:207], v239, s[58:59] offset:3072 sc1\n"
      "v_mfma_f32_16x16x32_bf16 v[52:55], a[220:223], v[228:231], v[52:55]\n"
      "v_mfma_f32_16x16x32_bf16 v[56:59], a[224:227], v[228:231], v[56:59]\n"
      "ds_read_b128 v[224:227], v233 offset:20480\n"
      "v_mfma_f32_16x16x32_bf16 v[60:63], a[228:231], v[228:231], v[60:63]\n"
      "v_mfma_f32_16x16x32_bf16 v[64:67], a[232:235], v[228:231], v[64:67]\n"
      "s_add_u32 s58, s24, 0xb000\n"
      "s_addc_u32 s59, s25, 0\n"
      "global_load_dwordx4 a[208:211], v239, s[58:59] offset:0 sc1\n"
      "v_mfma_f32_16x16x32_bf16 v[68:71], a[236:239], v[228:231], v[68:71]\n"
      "s_waitcnt lgkmcnt(2)\n"
      "v_mfma_f32_16x16x32_bf16 v[72:75], a[216:219], v[216:219], v[72:75]\n"
      "v_mfma_f32_16x16x32_bf16 v[76:79], a[220:223], v[216:219], v[76:79]\n"
      "v_mfma_f32_16x16x32_bf16 v[80:83], a[224:227], v[216:219], v[80:83]\n"
      "ds_read_b128 v[228:231], v233 offset:24576\n"
      "global_load_dwordx4 a[212:215], v239, s[58:59] offset:1024 sc1\n"
      "v_mfma_f32_16x16x32_bf16 v[84:87], a[228:231], v[216:219], v[84:87]\n"
      "v_mfma_f32_16x16x32_bf16 v[88:91], a[232:235], v[216:219], v[88:91]\n"
      "v_mfma_f32_16x16x32_bf16 v[92:95], a[236:239], v[216:219], v[92:95]\n"
      "s_waitcnt lgkmcnt(2)\n"
      "v_mfma_f32_16x16x32_bf16 v[96:99], a[216:219], v[220:223], v[96:99]\n"
      "v_mfma_f32_16x16x32_bf16 v[100:103], a[220:223], v[220:223], v[100:103]\n"
      "v_mfma_f32_16x16x32_bf16 v[104:107], a[224:227], v[220:223], v[104:107]\n"
      "ds_read_b128 v[216:219], v233 offset:28672\n"
      "v_mfma_f32_16x16x32_bf16 v[108:111], a[228:231], v[220:223], v[108:111]\n"
      "v_mfma_f32_16x16x32_bf16 v[112:115], a[232:235], v[220:223], v[112:115]\n"
      "v_mfma_f32_16x16x32_bf16 v[116:119], a[236:239], v[220:223], v[116:119]\n"
      "s_waitcnt lgkmcnt(2)\n"
      "v_mfma_f32_16x16x32_bf16 v[120:123], a[216:219], v[224:227], v[120:123]\n"
      "v_mfma_f32_16x16x32_bf16 v[124:127], a[220:223], v[224:227], v[124:127]\n"
      "v_mfma_f32_16x16x32_bf16 v[128:131], a[224:227], v[224:227], v[128:131]\n"
      "ds_read_b128 v[220:223], v233 offset:32768\n"
      "v_add_u32 v233, v249, v233\n"
      "v_mfma_f32_16x16x32_bf16 v[132:135], a[228:231], v[224:227], v[132:135]\n"
      "v_mfma_f32_16x16x32_bf16 v[136:139], a[232:235], v[224:227], v[136:139]\n"
      "v_mfma_f32_16x16x32_bf16 v[140:143], a[236:239], v[224:227], v[140:143]\n"
      "s_waitcnt lgkmcnt(2)\n"
      "v_mfma_f32_16x16x32_bf16 v[144:147], a[216:219], v[228:231], v[144:147]\n"
      "v_mfma_f32_16x16x32_bf16 v[148:151], a[220:223], v[228:231], v[148:151]\n"
      "v_mfma_f32_16x16x32_bf16 v[152:155], a[224:227], v[228:231], v[152:155]\n"
      "ds_read_b128 v[224:227], v234 offset:0\n"
      "v_mfma_f32_16x16x32_bf16 v[156:159], a[228:231], v[228:231], v[156:159]\n"
      "v_mfma_f32_16x16x32_bf16 v[160:163], a[232:235], v[228:231], v[160:163]\n"
      "v_mfma_f32_16x16x32_bf16 v[164:167], a[236:239], v[228:231], v[164:167]\n"
      "s_waitcnt lgkmcnt(2)\n"
      "v_mfma_f32_16x16x32_bf16 v[168:171], a[216:219], v[216:219], v[168:171]\n"
      "v_mfma_f32_16x16x32_bf16 v[172:175], a[220:223], v[216:219], v[172:175]\n"
      "v_mfma_f32_16x16x32_bf16 v[176:179], a[224:227], v[216:219], v[176:179]\n"
      "ds_read_b128 v[228:231], v234 offset:4096\n"
      "v_mfma_f32_16x16x32_bf16 v[180:183], a[228:231], v[216:219], v[180:183]\n"
      "v_mfma_f32_16x16x32_bf16 v[184:187], a[232:235], v[216:219], v[184:187]\n"
      "s_add_u32 s52, s42, s32\n"
      "s_addc_u32 s53, s43, 0\n"
      "s_add_u32 m0, s17, 0x6000\n"
      "v_mfma_f32_16x16x32_bf16 v[188:191], a[236:239], v[216:219], v[188:191]\n"
      "global_load_lds_dwordx4 v236, s[52:53] offset:0 \n"
      "s_waitcnt lgkmcnt(2)\n"
      "v_mfma_f32_16x16x32_bf16 v[192:195], a[216:219], v[220:223], v[192:195]\n"
      "v_mfma_f32_16x16x32_bf16 v[196:199], a[220:223], v[220:223], v[196:199]\n"
      "s_add_u32 m0, s17, 0x7f00\n"
      "v_mfma_f32_16x16x32_bf16 v[200:203], a[224:227], v[220:223], v[200:203]\n"
      "ds_read_b128 v[216:219], v234 offset:8192\n"
      "global_load_lds_dwordx4 v236, s[52:53] offset:256 \n"
      "v_mfma_f32_16x16x32_bf16 v[204:207], a[228:231], v[220:223], v[204:207]\n"
      "v_mfma_f32_16x16x32_bf16 v[208:211], a[232:235], v[220:223], v[208:211]\n"
      "s_add_u32 m0, s17, 0x9e00\n"
      "v_mfma_f32_16x16x32_bf16 v[212:215], a[236:239], v[220:223], v[212:215]\n"
      "global_load_lds_dwordx4 v236, s[52:53] offset:512 \n"
      "s_waitcnt lgkmcnt(2)\n"
      "v_mfma_f32_16x16x32_bf16 v[0:3], a[0:3], v[224:227], v[0:3]\n"
      "ds_read_b128 v[244:247], v250\n"
      "v_mfma_f32_16x16x32_bf16 v[4:7], a[4:7], v[224:227], v[4:7]\n"
      "s_add_u32 m0, s17, 0x7000\n"
      "ds_read_b64 v[252:253], v250 offset:4096\n"
      "v_mfma_f32_16x16x32_bf16 v[8:11], a[8:11], v[224:227], v[8:11]\n"
      "ds_read_b128 v[220:223], v234 offset:12288\n"
      "global_load_lds_dwordx4 v237, s[52:53] offset:0 \n"
      "v_mfma_f32_16x16x32_bf16 v[12:15], a[12:15], v[224:227], v[12:15]\n"
      "v_mfma_f32_16x16x32_bf16 v[16:19], a[16:19], v[224:227], v[16:19]\n"
      "s_add_u32 m0, s17, 0x8f00\n"
      "v_mfma_f32_16x16x32_bf16 v[20:23], a[20:23], v[224:227], v[20:23]\n"
      "global_load_lds_dwordx4 v237, s[52:53] offset:256 \n"
      "s_waitcnt lgkmcnt(4)\n"
      "v_mfma_f32_16x16x32_bf16 v[24:27], a[0:3], v[228:231], v[24:27]\n"
      "v_mfma_f32_16x16x32_bf16 v[28:31], a[4:7], v[228:231], v[28:31]\n"
      "s_add_u32 m0, s17, 0xae00\n"
      "v_mfma_f32_16x16x32_bf16 v[32:35], a[8:11], v[228:231], v[32:35]\n"
      "ds_read_b128 v[224:227], v234 offset:16384\n"
      "global_load_lds_dwordx4 v237, s[52:53] offset:512 \n"
      "v_mfma_f32_16x16x32_bf16 v[36:39], a[12:15], v[228:231], v[36:39]\n"
      "v_mfma_f32_16x16x32_bf16 v[40:43], a[16:19], v[228:231], v[40:43]\n"
      "v_mfma_f32_16x16x32_bf16 v[44:47], a[20:23], v[228:231], v[44:47]\n"
      "global_load_dwordx4 a[216:219], v239, s[58:59] offset:2048 sc1\n"
      "s_waitcnt lgkmcnt(4)\n"
      "v_mfma_f32_16x16x32_bf16 v[48:51], a[0:3], v[216:219], v[48:51]\n"
      "v_mfma_f32_16x16x32_bf16 v[52:55], a[4:7], v[216:219], v[52:55]\n"
      "v_mfma_f32_16x16x32_bf16 v[56:59], a[8:11], v[216:219], v[56:59]\n"
      "ds_read_b128 v[228:231], v234 offset:20480\n"
      "v_mfma_f32_16x16x32_bf16 v[60:63], a[12:15], v[216:219], v[60:63]\n"
      "global_load_dwordx4 a[220:223], v239, s[58:59] offset:3072 sc1\n"
      "v_mfma_f32_16x16x32_bf16 v[64:67], a[16:19], v[216:219], v[64:67]\n"
      "v_mfma_f32_16x16x32_bf16 v[68:71], a[20:23], v[216:219], v[68:71]\n"
      "s_waitcnt lgkmcnt(2)\n"
      "v_mfma_f32_16x16x32_bf16 v[72:75], a[0:3], v[220:223], v[72:75]\n"
      "v_mfma_f32_16x16x32_bf16 v[76:79], a[4:7], v[220:223], v[76:79]\n"
      "s_add_u32 s58, s24, 0xc000\n"
      "s_addc_u32 s59, s25, 0\n"
      "global_load_dwordx4 a[224:227], v239, s[58:59] offset:0 sc1\n"
      "v_mfma_f32_16x16x32_bf16 v[80:83], a[8:11], v[220:223], v[80:83]\n"
      "ds_read_b128 v[216:219], v234 offset:24576\n"
      "v_mfma_f32_16x16x32_bf16 v[84:87], a[12:15], v[220:223], v[84:87]\n"
      "v_mfma_f32_16x16x32_bf16 v[88:91], a[16:19], v[220:223], v[88:91]\n"
      "v_mfma_f32_16x16x32_bf16 v[92:95], a[20:23], v[220:223], v[92:95]\n"
      "global_load_dwordx4 a[228:231], v239, s[58:59] offset:1024 sc1\n"
      "s_waitcnt lgkmcnt(2)\n"
      "v_mfma_f32_16x16x32_bf16 v[96:99], a[0:3], v[224:227], v[96:99]\n"
      "v_mfma_f32_16x16x32_bf16 v[100:103], a[4:7], v[224:227], v[100:103]\n"
      "v_mfma_f32_16x16x32_bf16 v[104:107], a[8:11], v[224:227], v[104:107]\n"
      "ds_read_b128 v[220:223], v234 offset:28672\n"
      "v_mfma_f32_16x16x32_bf16 v[108:111], a[12:15], v[224:227], v[108:111]\n"
      "global_load_dwordx4 a[232:235], v239, s[58:59] offset:2048 sc1\n"
      "v_mfma_f32_16x16x32_bf16 v[112:115], a[16:19], v[224:227], v[112:115]\n"
      "v_mfma_f32_16x16x32_bf16 v[116:119], a[20:23], v[224:227], v[116:119]\n"
      "s_waitcnt lgkmcnt(2)\n"
      "v_mfma_f32_16x16x32_bf16 v[120:123], a[0:3], v[228:231], v[120:123]\n"
      "v_mfma_f32_16x16x32_bf16 v[124:127], a[4:7], v[228:231], v[124:127]\n"
      "global_load_dwordx4 a[236:239], v239, s[58:59] offset:3072 sc1\n"
      "v_mfma_f32_16x16x32_bf16 v[128:131], a[8:11], v[228:231], v[128:131]\n"
      "ds_read_b128 v[224:227], v234 offset:32768\n"
      "v_add_u32 v234, v249, v234\n"
      "v_mfma_f32_16x16x32_bf16 v[132:135], a[12:15], v[228:231], v[132:135]\n"
      "v_mfma_f32_16x16x32_bf16 v[136:139], a[16:19], v[228:231], v[136:139]\n"
      "v_mfma_f32_16x16x32_bf16 v[140:143], a[20:23], v[228:231], v[140:143]\n"
      "s_waitcnt lgkmcnt(2)\n"
      "v_mfma_f32_16x16x32_bf16 v[144:147], a[0:3], v[216:219], v[144:147]\n"
      "v_mfma_f32_16x16x32_bf16 v[148:151], a[4:7], v[216:219], v[148:151]\n"
      "v_mfma_f32_16x16x32_bf16 v[152:155], a[8:11], v[216:219], v[152:155]\n"
      "ds_read_b128 v[228:231], v235 offset:4096\n"
      "v_mfma_f32_16x16x32_bf16 v[156:159], a[12:15], v[216:219], v[156:159]\n"
      "v_mfma_f32_16x16x32_bf16 v[160:163], a[16:19], v[216:219], v[160:163]\n"
      "v_mfma_f32_16x16x32_bf16 v[164:167], a[20:23], v[216:219], v[164:167]\n"
      "s_waitcnt lgkmcnt(2)\n"
      "v_mfma_f32_16x16x32_bf16 v[168:171], a[0:3], v[220:223], v[168:171]\n"
      "v_mfma_f32_16x16x32_bf16 v[172:175], a[4:7], v[220:223], v[172:175]\n"
      "v_mfma_f32_16x16x32_bf16 v[176:179], a[8:11], v[220:223], v[176:179]\n"
      "ds_read_b128 v[216:219], v235 offset:8192\n"
      "v_mfma_f32_16x16x32_bf16 v[180:183], a[12:15], v[220:223], v[180:183]\n"
      "v_mfma_f32_16x16x32_bf16 v[184:187], a[16:19], v[220:223], v[184:187]\n"
      "v_mfma_f32_16x16x32_bf16 v[188:191], a[20:23], v[220:223], v[188:191]\n"
      "s_waitcnt lgkmcnt(2)\n"
      "v_mfma_f32_16x16x32_bf16 v[192:195], a[0:3], v[224:227], v[192:195]\n"
      "v_mfma_f32_16x16x32_bf16 v[196:199], a[4:7], v[224:227], v[196:199]\n"
      "v_mfma_f32_16x16x32_bf16 v[200:203], a[8:11], v[224:227], v[200:203]\n"
      "ds_read_b128 v[220:223], v235 offset:12288\n"
      "v_mfma_f32_16x16x32_bf16 v[204:207], a[12:15], v[224:227], v[204:207]\n"
      "v_mfma_f32_16x16x32_bf16 v[208:211], a[16:19], v[224:227], v[208:211]\n"
      "v_mfma_f32_16x16x32_bf16 v[212:215], a[20:23], v[224:227], v[212:215]\n"
      "s_waitcnt lgkmcnt(2)\n"
      "v_mfma_f32_16x16x32_bf16 v[24:27], a[24:27], v[228:231], v[24:27]\n"
      "s_add_u32 s58, s24, 0xd000\n"
      "s_addc_u32 s59, s25, 0\n"
      "global_load_dwordx4 a[0:3], v239, s[58:59] offset:0 sc1\n"
      "v_mfma_f32_16x16x32_bf16 v[28:31], a[28:31], v[228:231], v[28:31]\n"
      "v_mfma_f32_16x16x32_bf16 v[32:35], a[32:35], v[228:231], v[32:35]\n"
      "ds_read_b128 v[224:227], v235 offset:16384\n"
      "v_mfma_f32_16x16x32_bf16 v[36:39], a[36:39], v[228:231], v[36:39]\n"
      "v_mfma_f32_16x16x32_bf16 v[40:43], a[40:43], v[228:231], v[40:43]\n"
      "global_load_dwordx4 a[4:7], v239, s[58:59] offset:1024 sc1\n"
      "v_mfma_f32_16x16x32_bf16 v[44:47], a[44:47], v[228:231], v[44:47]\n"
      "s_waitcnt lgkmcnt(2)\n"
      "v_mfma_f32_16x16x32_bf16 v[48:51], a[24:27], v[216:219], v[48:51]\n"
      "v_cvt_pk_bf16_f32 v24, v24, v25\n"
      "v_cvt_pk_bf16_f32 v25, v26, v27\n"
      "v_mfma_f32_16x16x32_bf16 v[52:55], a[28:31], v[216:219], v[52:55]\n"
      "v_cvt_pk_bf16_f32 v26, v28, v29\n"
      "v_cvt_pk_bf16_f32 v27, v30, v31\n"
      "v_mfma_f32_16x16x32_bf16 v[56:59], a[32:35], v[216:219], v[56:59]\n"
      "ds_read_b128 v[228:231], v235 offset:20480\n"
      "v_cvt_pk_bf16_f32 v28, v32, v33\n"
      "v_cvt_pk_bf16_f32 v29, v34, v35\n"
      "global_load_dwordx4 a[8:11], v239, s[58:59] offset:2048 sc1\n"
      "v_mfma_f32_16x16x32_bf16 v[60:63], a[36:39], v[216:219], v[60:63]\n"
      "v_cvt_pk_bf16_f32 v30, v36, v37\n"
      "v_cvt_pk_bf16_f32 v31, v38, v39\n"
      "v_mfma_f32_16x16x32_bf16 v[64:67], a[40:43], v[216:219], v[64:67]\n"
      "v_cvt_pk_bf16_f32 v32, v40, v41\n"
      "v_cvt_pk_bf16_f32 v33, v42, v43\n"
      "v_mfma_f32_16x16x32_bf16 v[68:71], a[44:47], v[216:219], v[68:71]\n"
      "v_cvt_pk_bf16_f32 v34, v44, v45\n"
      "v_cvt_pk_bf16_f32 v35, v46, v47\n"
      "s_waitcnt lgkmcnt(2)\n"
      "v_mfma_f32_16x16x32_bf16 v[72:75], a[24:27], v[220:223], v[72:75]\n"
      "v_cvt_pk_bf16_f32 v48, v48, v49\n"
      "v_cvt_pk_bf16_f32 v49, v50, v51\n"
      "global_load_dwordx4 a[12:15], v239, s[58:59] offset:3072 sc1\n"
      "v_mfma_f32_16x16x32_bf16 v[76:79], a[28:31], v[220:223], v[76:79]\n"
      "v_cvt_pk_bf16_f32 v50, v52, v53\n"
      "v_cvt_pk_bf16_f32 v51, v54, v55\n"
      "v_mfma_f32_16x16x32_bf16 v[80:83], a[32:35], v[220:223], v[80:83]\n"
      "ds_read_b128 v[216:219], v235 offset:24576\n"
      "v_cvt_pk_bf16_f32 v52, v56, v57\n"
      "v_cvt_pk_bf16_f32 v53, v58, v59\n"
      "v_mfma_f32_16x16x32_bf16 v[84:87], a[36:39], v[220:223], v[84:87]\n"
      "v_cvt_pk_bf16_f32 v54, v60, v61\n"
      "v_cvt_pk_bf16_f32 v55, v62, v63\n"
      "v_mfma_f32_16x16x32_bf16 v[88:91], a[40:43], v[220:223], v[88:91]\n"
      "v_cvt_pk_bf16_f32 v56, v64, v65\n"
      "v_cvt_pk_bf16_f32 v57, v66, v67\n"
      "s_add_u32 s58, s24, 0xe000\n"
      "s_addc_u32 s59, s25, 0\n"
      "global_load_dwordx4 a[16:19], v239, s[58:59] offset:0 sc1\n"
      "v_mfma_f32_16x16x32_bf16 v[92:95], a[44:47], v[220:223], v[92:95]\n"
      "v_cvt_pk_bf16_f32 v58, v68, v69\n"
      "v_cvt_pk_bf16_f32 v59, v70, v71\n"
      "s_waitcnt lgkmcnt(2)\n"
      "v_mfma_f32_16x16x32_bf16 v[96:99], a[24:27], v[224:227], v[96:99]\n"
      "v_cvt_pk_bf16_f32 v72, v72, v73\n"
      "v_cvt_pk_bf16_f32 v73, v74, v75\n"
      "LPD6:\n"
      "s_cbranch_execz LPD7\n"
      "v_mfma_f32_16x16x32_bf16 v[100:103], a[28:31], v[224:227], v[100:103]\n"
      "v_cvt_pk_bf16_f32 v74, v76, v77\n"
      "v_cvt_pk_bf16_f32 v75, v78, v79\n"
      "v_mfma_f32_16x16x32_bf16 v[104:107], a[32:35], v[224:227], v[104:107]\n"
      "ds_read_b128 v[220:223], v235 offset:28672\n"
      "v_cvt_pk_bf16_f32 v76, v80, v81\n"
      "v_cvt_pk_bf16_f32 v77, v82, v83\n"
      "global_load_dwordx4 a[20:23], v239, s[58:59] offset:1024 sc1\n"
      "v_mfma_f32_16x16x32_bf16 v[108:111], a[36:39], v[224:227], v[108:111]\n"
      "v_cvt_pk_bf16_f32 v78, v84, v85\n"
      "v_cvt_pk_bf16_f32 v79, v86, v87\n"
      "v_mfma_f32_16x16x32_bf16 v[112:115], a[40:43], v[224:227], v[112:115]\n"
      "v_cvt_pk_bf16_f32 v80, v88, v89\n"
      "v_cvt_pk_bf16_f32 v81, v90, v91\n"
      "v_mfma_f32_16x16x32_bf16 v[116:119], a[44:47], v[224:227], v[116:119]\n"
      "v_cvt_pk_bf16_f32 v82, v92, v93\n"
      "v_cvt_pk_bf16_f32 v83, v94, v95\n"
      "s_waitcnt lgkmcnt(2)\n"
      "v_mfma_f32_16x16x32_bf16 v[120:123], a[24:27], v[228:231], v[120:123]\n"
      "v_cvt_pk_bf16_f32 v96, v96, v97\n"
      "v_cvt_pk_bf16_f32 v97, v98, v99\n"
      "v_mfma_f32_16x16x32_bf16 v[124:127], a[28:31], v[228:231], v[124:127]\n"
      "v_cvt_pk_bf16_f32 v98, v100, v101\n"
      "v_cvt_pk_bf16_f32 v99, v102, v103\n"
      "v_mfma_f32_16x16x32_bf16 v[128:131], a[32:35], v[228:231], v[128:131]\n"
      "ds_read_b128 v[224:227], v235 offset:32768\n"
      "v_cvt_pk_bf16_f32 v100, v104, v105\n"
      "v_cvt_pk_bf16_f32 v101, v106, v107\n"
      "v_mfma_f32_16x16x32_bf16 v[132:135], a[36:39], v[228:231], v[132:135]\n"
      "v_cvt_pk_bf16_f32 v102, v108, v109\n"
      "v_cvt_pk_bf16_f32 v103, v110, v111\n"
      "v_mfma_f32_16x16x32_bf16 v[136:139], a[40:43], v[228:231], v[136:139]\n"
      "v_cvt_pk_bf16_f32 v104, v112, v113\n"
      "v_cvt_pk_bf16_f32 v105, v114, v115\n"
      "v_mfma_f32_16x16x32_bf16 v[140:143], a[44:47], v[228:231], v[140:143]\n"
      "v_cvt_pk_bf16_f32 v106, v116, v117\n"
      "v_cvt_pk_bf16_f32 v107, v118, v119\n"
      "s_waitcnt lgkmcnt(2)\n"
      "v_mfma_f32_16x16x32_bf16 v[144:147], a[24:27], v[216:219], v[144:147]\n"
      "v_cvt_pk_bf16_f32 v120, v120, v121\n"
      "v_cvt_pk_bf16_f32 v121, v122, v123\n"
      "v_mfma_f32_16x16x32_bf16 v[148:151], a[28:31], v[216:219], v[148:151]\n"
      "v_cvt_pk_bf16_f32 v122, v124, v125\n"
      "v_cvt_pk_bf16_f32 v123, v126, v127\n"
      "v_mfma_f32_16x16x32_bf16 v[152:155], a[32:35], v[216:219], v[152:155]\n"
      "ds_read_b128 v[228:231], v235 offset:0\n"
      "v_add_u32 v235, v249, v235\n"
      "s_waitcnt vmcnt(24)\n"
      "s_barrier\n"
      "v_cvt_pk_bf16_f32 v124, v128, v129\n"
      "v_cvt_pk_bf16_f32 v125, v130, v131\n"
      "v_mfma_f32_16x16x32_bf16 v[156:159], a[36:39], v[216:219], v[156:159]\n"
      "ds_read_b128 v[36:39], v232 offset:0\n"
      "v_cvt_pk_bf16_f32 v126, v132, v133\n"
      "v_cvt_pk_bf16_f32 v127, v134, v135\n"
      "v_mfma_f32_16x16x32_bf16 v[160:163], a[40:43], v[216:219], v[160:163]\n"
      "v_cvt_pk_bf16_f32 v128, v136, v137\n"
      "v_cvt_pk_bf16_f32 v129, v138, v139\n"
      "v_mfma_f32_16x16x32_bf16 v[164:167], a[44:47], v[216:219], v[164:167]\n"
      "ds_read_b128 v[40:43], v233 offset:0\n"
      "v_cvt_pk_bf16_f32 v130, v140, v141\n"
      "v_cvt_pk_bf16_f32 v131, v142, v143\n"
      "s_waitcnt lgkmcnt(4)\n"
      "v_mfma_f32_16x16x32_bf16 v[168:171], a[24:27], v[220:223], v[168:171]\n"
      "v_cvt_pk_bf16_f32 v144, v144, v145\n"
      "v_cvt_pk_bf16_f32 v145, v146, v147\n"
      "global_load_dwordx4 v[140:143], v239, s[58:59] offset:2048 sc1\n"
      "v_mfma_f32_16x16x32_bf16 v[172:175], a[28:31], v[220:223], v[172:175]\n"
      "ds_read_b128 v[44:47], v234 offset:0\n"
      "v_cvt_pk_bf16_f32 v146, v148, v149\n"
      "v_cvt_pk_bf16_f32 v147, v150, v151\n"
      "v_mfma_f32_16x16x32_bf16 v[176:179], a[32:35], v[220:223], v[176:179]\n"
      "v_cvt_pk_bf16_f32 v148, v152, v153\n"
      "v_cvt_pk_bf16_f32 v149, v154, v155\n"
      "v_mfma_f32_16x16x32_bf16 v[180:183], a[36:39], v[220:223], v[180:183]\n"
      "ds_read_b128 v[60:63], v235 offset:0\n"
      "v_cvt_pk_bf16_f32 v150, v156, v157\n"
      "v_cvt_pk_bf16_f32 v151, v158, v159\n"
      "v_mfma_f32_16x16x32_bf16 v[184:187], a[40:43], v[220:223], v[184:187]\n"
      "v_cvt_pk_bf16_f32 v152, v160, v161\n"
      "v_cvt_pk_bf16_f32 v153, v162, v163\n"
      "v_mfma_f32_16x16x32_bf16 v[188:191], a[44:47], v[220:223], v[188:191]\n"
      "ds_read_b128 v[64:67], v232 offset:8192\n"
      "v_cvt_pk_bf16_f32 v154, v164, v165\n"
      "v_cvt_pk_bf16_f32 v155, v166, v167\n"
      "s_waitcnt lgkmcnt(6)\n"
      "v_mfma_f32_16x16x32_bf16 v[192:195], a[24:27], v[224:227], v[192:195]\n"
      "v_cvt_pk_bf16_f32 v168, v168, v169\n"
      "v_cvt_pk_bf16_f32 v169, v170, v171\n"
      "global_load_dwordx4 v[156:159], v239, s[58:59] offset:3072 sc1\n"
      "v_mfma_f32_16x16x32_bf16 v[196:199], a[28:31], v[224:227], v[196:199]\n"
      "ds_read_b128 v[68:71], v233 offset:8192\n"
      "v_cvt_pk_bf16_f32 v170, v172, v173\n"
      "v_cvt_pk_bf16_f32 v171, v174, v175\n"
      "v_mfma_f32_16x16x32_bf16 v[200:203], a[32:35], v[224:227], v[200:203]\n"
      "v_cvt_pk_bf16_f32 v172, v176, v177\n"
      "v_cvt_pk_bf16_f32 v173, v178, v179\n"
      "v_mfma_f32_16x16x32_bf16 v[204:207], a[36:39], v[224:227], v[204:207]\n"
      "ds_read_b128 v[84:87], v234 offset:8192\n"
      "v_cvt_pk_bf16_f32 v174, v180, v181\n"
      "v_cvt_pk_bf16_f32 v175, v182, v183\n"
      "v_mfma_f32_16x16x32_bf16 v[208:211], a[40:43], v[224:227], v[208:211]\n"
      "v_cvt_pk_bf16_f32 v176, v184, v185\n"
      "v_cvt_pk_bf16_f32 v177, v186, v187\n"
      "s_add_u32 s58, s24, 0xf000\n"
      "s_addc_u32 s59, s25, 0\n"
      "global_load_dwordx4 v[160:163], v239, s[58:59] offset:0 sc1\n"
      "v_mfma_f32_16x16x32_bf16 v[212:215], a[44:47], v[224:227], v[212:215]\n"
      "ds_read_b128 v[88:91], v235 offset:8192\n"
      "v_cvt_pk_bf16_f32 v178, v188, v189\n"
      "v_cvt_pk_bf16_f32 v179, v190, v191\n"
      "s_waitcnt lgkmcnt(8)\n"
      "v_mfma_f32_16x16x32_bf16 v[0:3], a[24:27], v[228:231], v[0:3]\n"
      "v_cvt_pk_bf16_f32 v192, v192, v193\n"
      "v_cvt_pk_bf16_f32 v193, v194, v195\n"
      "v_mfma_f32_16x16x32_bf16 v[4:7], a[28:31], v[228:231], v[4:7]\n"
      "ds_read_b128 v[92:95], v232 offset:16384\n"
      "v_cvt_pk_bf16_f32 v194, v196, v197\n"
      "v_cvt_pk_bf16_f32 v195, v198, v199\n"
      "v_mfma_f32_16x16x32_bf16 v[8:11], a[32:35], v[228:231], v[8:11]\n"
      "v_cvt_pk_bf16_f32 v196, v200, v201\n"
      "v_cvt_pk_bf16_f32 v197, v202, v203\n"
      "global_load_dwordx4 v[164:167], v239, s[58:59] offset:1024 sc1\n"
      "v_mfma_f32_16x16x32_bf16 v[12:15], a[36:39], v[228:231], v[12:15]\n"
      "ds_read_b128 v[108:111], v233 offset:16384\n"
      "v_cvt_pk_bf16_f32 v198, v204, v205\n"
      "v_cvt_pk_bf16_f32 v199, v206, v207\n"
      "v_mfma_f32_16x16x32_bf16 v[16:19], a[40:43], v[228:231], v[16:19]\n"
      "ds_read_b128 v[112:115], v234 offset:16384\n"
      "v_cvt_pk_bf16_f32 v200, v208, v209\n"
      "v_cvt_pk_bf16_f32 v201, v210, v211\n"
      "v_mfma_f32_16x16x32_bf16 v[20:23], a[44:47], v[228:231], v[20:23]\n"
      "ds_read_b128 v[116:119], v235 offset:16384\n"
      "v_cvt_pk_bf16_f32 v202, v212, v213\n"
      "v_cvt_pk_bf16_f32 v203, v214, v215\n"
      "s_waitcnt lgkmcnt(8)\n"
      "v_mfma_f32_16x16x32_bf16 v[132:135], a[240:243], v[36:39], v[0:3]\n"
      "global_load_dwordx4 v[180:183], v239, s[58:59] offset:2048 sc1\n"
      "s_mov_b64 vcc, 0\n"
      "v_mfma_f32_16x16x32_bf16 v[132:135], a[244:247], v[40:43], v[132:135]\n"
      "v_mfma_f32_16x16x32_bf16 v[132:135], a[248:251], v[44:47], v[132:135]\n"
      "v_mfma_f32_16x16x32_bf16 v[132:135], a[252:255], v[60:63], v[132:135]\n"
      "global_load_dwordx4 v[184:187], v239, s[58:59] offset:3072 sc1\n"
      "s_waitcnt lgkmcnt(4)\n"
      "v_mfma_f32_16x16x32_bf16 v[132:135], a[48:51], v[64:67], v[132:135]\n"
      "v_mfma_f32_16x16x32_bf16 v[132:135], a[52:55], v[68:71], v[132:135]\n"
      "v_mfma_f32_16x16x32_bf16 v[132:135], a[56:59], v[84:87], v[132:135]\n"
      "s_add_u32 s58, s24, 0x10000\n"
      "s_addc_u32 s59, s25, 0\n"
      "global_load_dwordx4 v[188:191], v239, s[58:59] offset:0 sc1\n"
      "v_mfma_f32_16x16x32_bf16 v[132:135], a[60:63], v[88:91], v[132:135]\n"
      "s_waitcnt lgkmcnt(0)\n"
      "v_mfma_f32_16x16x32_bf16 v[132:135], a[64:67], v[92:95], v[132:135]\n"
      "v_mfma_f32_16x16x32_bf16 v[132:135], a[68:71], v[108:111], v[132:135]\n"
      "global_load_dwordx4 v[204:207], v239, s[58:59] offset:1024 sc1\n"
      "v_mfma_f32_16x16x32_bf16 v[132:135], a[72:75], v[112:115], v[132:135]\n"
      "v_mfma_f32_16x16x32_bf16 v[132:135], a[76:79], v[116:119], v[132:135]\n"
      "v_mfma_f32_16x16x32_bf16 v[136:139], a[80:83], v[36:39], v[4:7]\n"
      "global_load_dwordx4 a[24:27], v239, s[58:59] offset:2048 sc1\n"
      "v_mfma_f32_16x16x32_bf16 v[136:139], a[84:87], v[40:43], v[136:139]\n"
      "v_mfma_f32_16x16x32_bf16 v[136:139], a[88:91], v[44:47], v[136:139]\n"
      "v_mfma_f32_16x16x32_bf16 v[136:139], a[92:95], v[60:63], v[136:139]\n"
      "global_load_dwordx4 a[28:31], v239, s[58:59] offset:3072 sc1\n"
      "v_mfma_f32_16x16x32_bf16 v[136:139], a[96:99], v[64:67], v[136:139]\n"
      "v_cvt_pk_bf16_f32 v208, v132, v133\n"
      "v_mfma_f32_16x16x32_bf16 v[136:139], a[100:103], v[68:71], v[136:139]\n"
      "v_cvt_pk_bf16_f32 v209, v134, v135\n"
      "v_mfma_f32_16x16x32_bf16 v[136:139], a[104:107], v[84:87], v[136:139]\n"
      "s_add_u32 s58, s24, 0x11000\n"
      "s_addc_u32 s59, s25, 0\n"
      "global_load_dwordx4 a[32:35], v239, s[58:59] offset:0 sc1\n"
      "v_mfma_f32_16x16x32_bf16 v[136:139], a[108:111], v[88:91], v[136:139]\n"
      "v_mfma_f32_16x16x32_bf16 v[136:139], a[112:115], v[92:95], v[136:139]\n"
      "v_mfma_f32_16x16x32_bf16 v[136:139], a[116:119], v[108:111], v[136:139]\n"
      "global_load_dwordx4 a[36:39], v239, s[58:59] offset:1024 sc1\n"
      "v_mfma_f32_16x16x32_bf16 v[136:139], a[120:123], v[112:115], v[136:139]\n"
      "ds_write_b64 v244, v[208:209] offset:12288\n"
      "v_mfma_f32_16x16x32_bf16 v[136:139], a[124:127], v[116:119], v[136:139]\n"
      "v_mfma_f32_16x16x32_bf16 v[132:135], a[128:131], v[36:39], v[8:11]\n"
      "global_load_dwordx4 a[40:43], v239, s[58:59] offset:2048 sc1\n"
      "v_mfma_f32_16x16x32_bf16 v[132:135], a[132:135], v[40:43], v[132:135]\n"
      "v_mfma_f32_16x16x32_bf16 v[132:135], a[136:139], v[44:47], v[132:135]\n"
      "v_mfma_f32_16x16x32_bf16 v[132:135], a[140:143], v[60:63], v[132:135]\n"
      "global_load_dwordx4 a[44:47], v239, s[58:59] offset:3072 sc1\n"
      "v_mfma_f32_16x16x32_bf16 v[132:135], a[144:147], v[64:67], v[132:135]\n"
      "v_cvt_pk_bf16_f32 v210, v136, v137\n"
      "v_mfma_f32_16x16x32_bf16 v[132:135], a[148:151], v[68:71], v[132:135]\n"
      "v_cvt_pk_bf16_f32 v211, v138, v139\n"
      "v_mfma_f32_16x16x32_bf16 v[132:135], a[152:155], v[84:87], v[132:135]\n"
      "s_mul_i32 s78, s90, s19\n"
      "v_mfma_f32_16x16x32_bf16 v[132:135], a[156:159], v[88:91], v[132:135]\n"
      "v_add_u32 v236, s78, v236\n"
      "v_mfma_f32_16x16x32_bf16 v[132:135], a[160:163], v[92:95], v[132:135]\n"
      "s_mul_i32 s78, s78, s99\n"
      "v_mfma_f32_16x16x32_bf16 v[132:135], a[164:167], v[108:111], v[132:135]\n"
      "v_add_u32 v237, s78, v237\n"
      "v_mfma_f32_16x16x32_bf16 v[132:135], a[168:171], v[112:115], v[132:135]\n"
      "s_add_u32 s52, s20, s33\n"
      "s_addc_u32 s53, s21, 0\n"
      "s_add_u32 m0, s17, 0xc000\n"
      "v_mfma_f32_16x16x32_bf16 v[132:135], a[172:175], v[116:119], v[132:135]\n"
      "global_load_lds_dwordx4 v236, s[52:53] offset:0 \n"
      "v_mfma_f32_16x16x32_bf16 v[136:139], a[176:179], v[36:39], v[12:15]\n"
      "ds_write_b64 v244, v[210:211] offset:12296\n"
      "v_mfma_f32_16x16x32_bf16 v[136:139], a[180:183], v[40:43], v[136:139]\n"
      "v_mfma_f32_16x16x32_bf16 v[136:139], a[184:187], v[44:47], v[136:139]\n"
      "v_mfma_f32_16x16x32_bf16 v[136:139], a[188:191], v[60:63], v[136:139]\n"
      "v_cvt_pk_bf16_f32 v212, v132, v133\n"
      "s_waitcnt vmcnt(35)\n"
      "v_mfma_f32_16x16x32_bf16 v[136:139], a[192:195], v[64:67], v[136:139]\n"
      "s_add_u32 m0, s17, 0xdf00\n"
      "v_mfma_f32_16x16x32_bf16 v[136:139], a[196:199], v[68:71], v[136:139]\n"
      "global_load_lds_dwordx4 v236, s[52:53] offset:256 \n"
      "v_mfma_f32_16x16x32_bf16 v[136:139], a[200:203], v[84:87], v[136:139]\n"
      "v_cvt_pk_bf16_f32 v213, v134, v135\n"
      "v_mfma_f32_16x16x32_bf16 v[136:139], a[204:207], v[88:91], v[136:139]\n"
      "s_waitcnt vmcnt(26)\n"
      "v_mfma_f32_16x16x32_bf16 v[136:139], a[208:211], v[92:95], v[136:139]\n"
      "v_mfma_f32_16x16x32_bf16 v[136:139], a[212:215], v[108:111], v[136:139]\n"
      "v_mfma_f32_16x16x32_bf16 v[136:139], a[216:219], v[112:115], v[136:139]\n"
      "s_add_u32 m0, s17, 0xfe00\n"
      "v_mfma_f32_16x16x32_bf16 v[136:139], a[220:223], v[116:119], v[136:139]\n"
      "global_load_lds_dwordx4 v236, s[52:53] offset:512 \n"
      "s_waitcnt vmcnt(23)\n"
      "v_mfma_f32_16x16x32_bf16 v[132:135], a[224:227], v[36:39], v[16:19]\n"
      "ds_write_b64 v245, v[212:213] offset:12288\n"
      "v_mfma_f32_16x16x32_bf16 v[132:135], a[228:231], v[40:43], v[132:135]\n"
      "v_mfma_f32_16x16x32_bf16 v[132:135], a[232:235], v[44:47], v[132:135]\n"
      "v_mfma_f32_16x16x32_bf16 v[132:135], a[236:239], v[60:63], v[132:135]\n"
      "v_cvt_pk_bf16_f32 v214, v136, v137\n"
      "s_waitcnt vmcnt(19)\n"
      "v_mfma_f32_16x16x32_bf16 v[132:135], a[0:3], v[64:67], v[132:135]\n"
      "s_add_u32 m0, s17, 0xd000\n"
      "v_mfma_f32_16x16x32_bf16 v[132:135], a[4:7], v[68:71], v[132:135]\n"
      "global_load_lds_dwordx4 v237, s[52:53] offset:0 \n"
      "v_mfma_f32_16x16x32_bf16 v[132:135], a[8:11], v[84:87], v[132:135]\n"
      "v_cvt_pk_bf16_f32 v215, v138, v139\n"
      "v_mfma_f32_16x16x32_bf16 v[132:135], a[12:15], v[88:91], v[132:135]\n"
      "s_waitcnt vmcnt(16)\n"
      "v_mfma_f32_16x16x32_bf16 v[132:135], a[16:19], v[92:95], v[132:135]\n"
      "v_mfma_f32_16x16x32_bf16 v[132:135], a[20:23], v[108:111], v[132:135]\n"
      "v_mfma_f32_16x16x32_bf16 v[132:135], v[140:143], v[112:115], v[132:135]\n"
      "s_add_u32 m0, s17, 0xef00\n"
      "v_mfma_f32_16x16x32_bf16 v[132:135], v[156:159], v[116:119], v[132:135]\n"
      "s_barrier\n"
      "global_load_lds_dwordx4 v237, s[52:53] offset:256 \n"
      "s_waitcnt vmcnt(13)\n"
      "v_mfma_f32_16x16x32_bf16 v[136:139], v[160:163], v[36:39], v[20:23]\n"
      "ds_read_b128 v[36:39], v232 offset:256\n"
      "ds_write_b64 v245, v[214:215] offset:12296\n"
      "v_mfma_f32_16x16x32_bf16 v[136:139], v[164:167], v[40:43], v[136:139]\n"
      "ds_read_b128 v[40:43], v233 offset:256\n"
      "v_mfma_f32_16x16x32_bf16 v[136:139], v[180:183], v[44:47], v[136:139]\n"
      "ds_read_b128 v[44:47], v234 offset:256\n"
      "v_mfma_f32_16x16x32_bf16 v[136:139], v[184:187], v[60:63], v[136:139]\n"
      "ds_read_b128 v[60:63], v235 offset:256\n"
      "v_cvt_pk_bf16_f32 v208, v132, v133\n"
      "s_waitcnt vmcnt(9)\n"
      "v_mfma_f32_16x16x32_bf16 v[136:139], v[188:191], v[64:67], v[136:139]\n"
      "ds_read_b128 v[64:67], v232 offset:8448\n"
      "s_add_u32 m0, s17, 0x10e00\n"
      "v_mfma_f32_16x16x32_bf16 v[136:139], v[204:207], v[68:71], v[136:139]\n"
      "ds_read_b128 v[68:71], v233 offset:8448\n"
      "global_load_lds_dwordx4 v237, s[52:53] offset:512 \n"
      "v_mfma_f32_16x16x32_bf16 v[136:139], a[24:27], v[84:87], v[136:139]\n"
      "ds_read_b128 v[84:87], v234 offset:8448\n"
      "v_cvt_pk_bf16_f32 v209, v134, v135\n"
      "v_mfma_f32_16x16x32_bf16 v[136:139], a[28:31], v[88:91], v[136:139]\n"
      "ds_read_b128 v[88:91], v235 offset:8448\n"
      "s_waitcnt vmcnt(6)\n"
      "v_mfma_f32_16x16x32_bf16 v[136:139], a[32:35], v[92:95], v[136:139]\n"
      "ds_read_b128 v[92:95], v232 offset:16640\n"
      "v_mfma_f32_16x16x32_bf16 v[136:139], a[36:39], v[108:111], v[136:139]\n"
      "ds_read_b128 v[108:111], v233 offset:16640\n"
      "v_mfma_f32_16x16x32_bf16 v[136:139], a[40:43], v[112:115], v[136:139]\n"
      "ds_read_b128 v[112:115], v234 offset:16640\n"
      "v_mfma_f32_16x16x32_bf16 v[136:139], a[44:47], v[116:119], v[136:139]\n"
      "ds_read_b128 v[116:119], v235 offset:16640\n"
      "s_waitcnt lgkmcnt(8)\n"
      "v_mfma_f32_16x16x32_bf16 v[132:135], a[240:243], v[36:39], v[0:3]\n"
      "s_add_u32 s52, s42, s33\n"
      "s_addc_u32 s53, s43, 0\n"
      "s_add_u32 m0, s17, 0x12000\n"
      "v_mfma_f32_16x16x32_bf16 v[132:135], a[244:247], v[40:43], v[132:135]\n"
      "global_load_lds_dwordx4 v236, s[52:53] offset:0 \n"
      "v_mfma_f32_16x16x32_bf16 v[132:135], a[248:251], v[44:47], v[132:135]\n"
      "ds_write_b64 v244, v[208:209] offset:12416\n"
      "v_mfma_f32_16x16x32_bf16 v[132:135], a[252:255], v[60:63], v[132:135]\n"
      "v_cvt_pk_bf16_f32 v210, v136, v137\n"
      "s_waitcnt lgkmcnt(5)\n"
      "v_mfma_f32_16x16x32_bf16 v[132:135], a[48:51], v[64:67], v[132:135]\n"
      "v_cvt_pk_bf16_f32 v211, v138, v139\n"
      "v_mfma_f32_16x16x32_bf16 v[132:135], a[52:55], v[68:71], v[132:135]\n"
      "v_mfma_f32_16x16x32_bf16 v[132:135], a[56:59], v[84:87], v[132:135]\n"
      "s_add_u32 m0, s17, 0x13f00\n"
      "v_mfma_f32_16x16x32_bf16 v[132:135], a[60:63], v[88:91], v[132:135]\n"
      "global_load_lds_dwordx4 v236, s[52:53] offset:256 \n"
      "s_waitcnt lgkmcnt(1)\n"
      "v_mfma_f32_16x16x32_bf16 v[132:135], a[64:67], v[92:95], v[132:135]\n"
      "v_mfma_f32_16x16x32_bf16 v[132:135], a[68:71], v[108:111], v[132:135]\n"
      "ds_write_b64 v244, v[210:211] offset:12424\n"
      "v_mfma_f32_16x16x32_bf16 v[132:135], a[72:75], v[112:115], v[132:135]\n"
      "v_mfma_f32_16x16x32_bf16 v[132:135], a[76:79], v[116:119], v[132:135]\n"
      "v_mfma_f32_16x16x32_bf16 v[136:139], a[80:83], v[36:39], v[4:7]\n"
      "s_add_u32 m0, s17, 0x15e00\n"
      "v_mfma_f32_16x16x32_bf16 v[136:139], a[84:87], v[40:43], v[136:139]\n"
      "global_load_lds_dwordx4 v236, s[52:53] offset:512 \n"
      "v_mfma_f32_16x16x32_bf16 v[136:139], a[88:91], v[44:47], v[136:139]\n"
      "v_mfma_f32_16x16x32_bf16 v[136:139], a[92:95], v[60:63], v[136:139]\n"
      "v_cvt_pk_bf16_f32 v208, v132, v133\n"
      "v_mfma_f32_16x16x32_bf16 v[136:139], a[96:99], v[64:67], v[136:139]\n"
      "v_cvt_pk_bf16_f32 v209, v134, v135\n"
      "v_mfma_f32_16x16x32_bf16 v[136:139], a[100:103], v[68:71], v[136:139]\n"
      "v_mfma_f32_16x16x32_bf16 v[136:139], a[104:107], v[84:87], v[136:139]\n"
      "s_add_u32 m0, s17, 0x13000\n"
      "v_mfma_f32_16x16x32_bf16 v[136:139], a[108:111], v[88:91], v[136:139]\n"
      "global_load_lds_dwordx4 v237, s[52:53] offset:0 \n"
      "v_mfma_f32_16x16x32_bf16 v[136:139], a[112:115], v[92:95], v[136:139]\n"
      "v_mfma_f32_16x16x32_bf16 v[136:139], a[116:119], v[108:111], v[136:139]\n"
      "ds_write_b64 v244, v[208:209] offset:24576\n"
      "v_mfma_f32_16x16x32_bf16 v[136:139], a[120:123], v[112:115], v[136:139]\n"
      "v_mfma_f32_16x16x32_bf16 v[136:139], a[124:127], v[116:119], v[136:139]\n"
      "v_mfma_f32_16x16x32_bf16 v[132:135], a[128:131], v[36:39], v[8:11]\n"
      "s_add_u32 m0, s17, 0x14f00\n"
      "v_mfma_f32_16x16x32_bf16 v[132:135], a[132:135], v[40:43], v[132:135]\n"
      "global_load_lds_dwordx4 v237, s[52:53] offset:256 \n"
      "v_mfma_f32_16x16x32_bf16 v[132:135], a[136:139], v[44:47], v[132:135]\n"
      "v_mfma_f32_16x16x32_bf16 v[132:135], a[140:143], v[60:63], v[132:135]\n"
      "v_cvt_pk_bf16_f32 v210, v136, v137\n"
      "v_mfma_f32_16x16x32_bf16 v[132:135], a[144:147], v[64:67], v[132:135]\n"
      "v_cvt_pk_bf16_f32 v211, v138, v139\n"
      "v_mfma_f32_16x16x32_bf16 v[132:135], a[148:151], v[68:71], v[132:135]\n"
      "v_mfma_f32_16x16x32_bf16 v[132:135], a[152:155], v[84:87], v[132:135]\n"
      "s_add_u32 m0, s17, 0x16e00\n"
      "v_mfma_f32_16x16x32_bf16 v[132:135], a[156:159], v[88:91], v[132:135]\n"
      "global_load_lds_dwordx4 v237, s[52:53] offset:512 \n"
      "v_mfma_f32_16x16x32_bf16 v[132:135], a[160:163], v[92:95], v[132:135]\n"
      "v_mfma_f32_16x16x32_bf16 v[132:135], a[164:167], v[108:111], v[132:135]\n"
      "ds_write_b64 v244, v[210:211] offset:24584\n"
      "v_mfma_f32_16x16x32_bf16 v[132:135], a[168:171], v[112:115], v[132:135]\n"
      "v_mfma_f32_16x16x32_bf16 v[132:135], a[172:175], v[116:119], v[132:135]\n"
      "v_mfma_f32_16x16x32_bf16 v[136:139], a[176:179], v[36:39], v[12:15]\n"
      "v_mfma_f32_16x16x32_bf16 v[136:139], a[180:183], v[40:43], v[136:139]\n"
      "v_mfma_f32_16x16x32_bf16 v[136:139], a[184:187], v[44:47], v[136:139]\n"
      "v_mfma_f32_16x16x32_bf16 v[136:139], a[188:191], v[60:63], v[136:139]\n"
      "v_cvt_pk_bf16_f32 v212, v132, v133\n"
      "v_mfma_f32_16x16x32_bf16 v[136:139], a[192:195], v[64:67], v[136:139]\n"
      "v_cvt_pk_bf16_f32 v213, v134, v135\n"
      "v_mfma_f32_16x16x32_bf16 v[136:139], a[196:199], v[68:71], v[136:139]\n"
      "v_mfma_f32_16x16x32_bf16 v[136:139], a[200:203], v[84:87], v[136:139]\n"
      "v_mfma_f32_16x16x32_bf16 v[136:139], a[204:207], v[88:91], v[136:139]\n"
      "v_mfma_f32_16x16x32_bf16 v[136:139], a[208:211], v[92:95], v[136:139]\n"
      "v_mfma_f32_16x16x32_bf16 v[136:139], a[212:215], v[108:111], v[136:139]\n"
      "ds_write_b64 v245, v[212:213] offset:24576\n"
      "v_mfma_f32_16x16x32_bf16 v[136:139], a[216:219], v[112:115], v[136:139]\n"
      "v_mfma_f32_16x16x32_bf16 v[136:139], a[220:223], v[116:119], v[136:139]\n"
      "v_mfma_f32_16x16x32_bf16 v[132:135], a[224:227], v[36:39], v[16:19]\n"
      "v_mfma_f32_16x16x32_bf16 v[132:135], a[228:231], v[40:43], v[132:135]\n"
      "v_mfma_f32_16x16x32_bf16 v[132:135], a[232:235], v[44:47], v[132:135]\n"
      "v_mfma_f32_16x16x32_bf16 v[132:135], a[236:239], v[60:63], v[132:135]\n"
      "v_cvt_pk_bf16_f32 v214, v136, v137\n"
      "v_mfma_f32_16x16x32_bf16 v[132:135], a[0:3], v[64:67], v[132:135]\n"
      "v_cvt_pk_bf16_f32 v215, v138, v139\n"
      "v_mfma_f32_16x16x32_bf16 v[132:135], a[4:7], v[68:71], v[132:135]\n"
      "v_mfma_f32_16x16x32_bf16 v[132:135], a[8:11], v[84:87], v[132:135]\n"
      "v_mfma_f32_16x16x32_bf16 v[132:135], a[12:15], v[88:91], v[132:135]\n"
      "v_mfma_f32_16x16x32_bf16 v[132:135], a[16:19], v[92:95], v[132:135]\n"
      "v_mfma_f32_16x16x32_bf16 v[132:135], a[20:23], v[108:111], v[132:135]\n"
      "ds_write_b64 v245, v[214:215] offset:24584\n"
      "v_mfma_f32_16x16x32_bf16 v[132:135], v[140:143], v[112:115], v[132:135]\n"
      "s_lshl_b32 s79, s98, 1\n"
      "v_mfma_f32_16x16x32_bf16 v[132:135], v[156:159], v[116:119], v[132:135]\n"
      "s_waitcnt lgkmcnt(4)\n"
      "s_barrier\n"
      "v_mad_i32_i24 v252, v251, s79, v252\n"
      "v_mfma_f32_16x16x32_bf16 v[136:139], v[160:163], v[36:39], v[20:23]\n"
      "ds_read_b128 v[36:39], v232 offset:24576\n"
      "s_mul_i32 s79, s79, s100\n"
      "v_mfma_f32_16x16x32_bf16 v[136:139], v[164:167], v[40:43], v[136:139]\n"
      "ds_read_b128 v[40:43], v233 offset:24576\n"
      "v_add_u32 v247, s79, v247\n"
      "v_mfma_f32_16x16x32_bf16 v[136:139], v[180:183], v[44:47], v[136:139]\n"
      "ds_read_b128 v[44:47], v234 offset:24576\n"
      "s_mul_i32 s79, s79, s101\n"
      "v_mfma_f32_16x16x32_bf16 v[136:139], v[184:187], v[60:63], v[136:139]\n"
      "ds_read_b128 v[60:63], v235 offset:24576\n"
      "v_cvt_pk_bf16_f32 v208, v132, v133\n"
      "v_mfma_f32_16x16x32_bf16 v[136:139], v[188:191], v[64:67], v[136:139]\n"
      "ds_read_b128 v[64:67], v232 offset:32768\n"
      "v_cvt_pk_bf16_f32 v209, v134, v135\n"
      "v_mfma_f32_16x16x32_bf16 v[136:139], v[204:207], v[68:71], v[136:139]\n"
      "ds_read_b128 v[68:71], v233 offset:32768\n"
      "v_add_u32 v253, s79, v253\n"
      "v_mfma_f32_16x16x32_bf16 v[136:139], a[24:27], v[84:87], v[136:139]\n"
      "ds_read_b128 v[84:87], v234 offset:32768\n"
      "v_mfma_f32_16x16x32_bf16 v[136:139], a[28:31], v[88:91], v[136:139]\n"
      "ds_read_b128 v[88:91], v235 offset:32768\n"
      "v_mfma_f32_16x16x32_bf16 v[136:139], a[32:35], v[92:95], v[136:139]\n"
      "ds_read_b128 v[92:95], v232 offset:40960\n"
      "v_mfma_f32_16x16x32_bf16 v[136:139], a[36:39], v[108:111], v[136:139]\n"
      "ds_read_b128 v[108:111], v233 offset:40960\n"
      "v_mfma_f32_16x16x32_bf16 v[136:139], a[40:43], v[112:115], v[136:139]\n"
      "ds_read_b128 v[112:115], v234 offset:40960\n"
      "v_mfma_f32_16x16x32_bf16 v[136:139], a[44:47], v[116:119], v[136:139]\n"
      "ds_read_b128 v[116:119], v235 offset:40960\n"
      "LJOIN:\n"
      "LPD7:\n"
      "s_cbranch_execz LPD10\n"
      "s_waitcnt lgkmcnt(8)\n"
      "v_mfma_f32_16x16x32_bf16 v[132:135], a[240:243], v[36:39], v[0:3]\n"
      "ds_write_b64 v244, v[208:209] offset:24704\n"
      "v_mfma_f32_16x16x32_bf16 v[132:135], a[244:247], v[40:43], v[132:135]\n"
      "s_add_u32 s54, s30, s32\n"
      "s_addc_u32 s55, s31, 0\n"
      "v_mfma_f32_16x16x32_bf16 v[132:135], a[248:251], v[44:47], v[132:135]\n"
      "v_mfma_f32_16x16x32_bf16 v[132:135], a[252:255], v[60:63], v[132:135]\n"
      "v_cvt_pk_bf16_f32 v210, v136, v137\n"
      "s_waitcnt lgkmcnt(5)\n"
      "v_mfma_f32_16x16x32_bf16 v[132:135], a[48:51], v[64:67], v[132:135]\n"
      "v_cvt_pk_bf16_f32 v211, v138, v139\n"
      "v_mfma_f32_16x16x32_bf16 v[132:135], a[52:55], v[68:71], v[132:135]\n"
      "s_mul_i32 s78, s91, s19\n"
      "v_mfma_f32_16x16x32_bf16 v[132:135], a[56:59], v[84:87], v[132:135]\n"
      "ds_read_b128 v[216:219], v246 offset:12288\n"
      "v_add_u32 v236, s78, v236\n"
      "v_mfma_f32_16x16x32_bf16 v[132:135], a[60:63], v[88:91], v[132:135]\n"
      "ds_read_b128 v[220:223], v246 offset:13312\n"
      "s_waitcnt lgkmcnt(3)\n"
      "v_mfma_f32_16x16x32_bf16 v[132:135], a[64:67], v[92:95], v[132:135]\n"
      "ds_read_b128 v[224:227], v246 offset:14336\n"
      "v_mfma_f32_16x16x32_bf16 v[132:135], a[68:71], v[108:111], v[132:135]\n"
      "ds_write_b64 v244, v[210:211] offset:24712\n"
      "v_mfma_f32_16x16x32_bf16 v[132:135], a[72:75], v[112:115], v[132:135]\n"
      "s_mul_i32 s78, s78, s99\n"
      "v_mfma_f32_16x16x32_bf16 v[132:135], a[76:79], v[116:119], v[132:135]\n"
      "v_add_u32 v237, s78, v237\n"
      "v_mfma_f32_16x16x32_bf16 v[136:139], a[80:83], v[36:39], v[4:7]\n"
      "v_mfma_f32_16x16x32_bf16 v[136:139], a[84:87], v[40:43], v[136:139]\n"
      "v_mfma_f32_16x16x32_bf16 v[136:139], a[88:91], v[44:47], v[136:139]\n"
      "v_mfma_f32_16x16x32_bf16 v[136:139], a[92:95], v[60:63], v[136:139]\n"
      "v_cvt_pk_bf16_f32 v208, v132, v133\n"
      "v_mfma_f32_16x16x32_bf16 v[136:139], a[96:99], v[64:67], v[136:139]\n"
      "v_cvt_pk_bf16_f32 v209, v134, v135\n"
      "v_mfma_f32_16x16x32_bf16 v[136:139], a[100:103], v[68:71], v[136:139]\n"
      "s_waitcnt lgkmcnt(3)\n"
      "v_mfma_f32_16x16x32_bf16 v[136:139], a[104:107], v[84:87], v[136:139]\n"
      "global_store_dwordx4 v247, v[216:219], s[54:55] offset:0 sc1\n"
      "v_mfma_f32_16x16x32_bf16 v[136:139], a[108:111], v[88:91], v[136:139]\n"
      "s_add_u32 s52, s20, s34\n"
      "s_addc_u32 s53, s21, 0\n"
      "v_mfma_f32_16x16x32_bf16 v[136:139], a[112:115], v[92:95], v[136:139]\n"
      "s_add_u32 m0, s17, 0x0\n"
      "v_mfma_f32_16x16x32_bf16 v[136:139], a[116:119], v[108:111], v[136:139]\n"
      "ds_write_b64 v244, v[208:209] offset:0\n"
      "v_mfma_f32_16x16x32_bf16 v[136:139], a[120:123], v[112:115], v[136:139]\n"
      "global_load_lds_dwordx4 v236, s[52:53] offset:0 nt\n"
      "v_mfma_f32_16x16x32_bf16 v[136:139], a[124:127], v[116:119], v[136:139]\n"
      "v_mfma_f32_16x16x32_bf16 v[132:135], a[128:131], v[36:39], v[8:11]\n"
      "v_mfma_f32_16x16x32_bf16 v[132:135], a[132:135], v[40:43], v[132:135]\n"
      "v_mfma_f32_16x16x32_bf16 v[132:135], a[136:139], v[44:47], v[132:135]\n"
      "v_mfma_f32_16x16x32_bf16 v[132:135], a[140:143], v[60:63], v[132:135]\n"
      "v_cvt_pk_bf16_f32 v210, v136, v137\n"
      "v_mfma_f32_16x16x32_bf16 v[132:135], a[144:147], v[64:67], v[132:135]\n"
      "v_cvt_pk_bf16_f32 v211, v138, v139\n"
      "v_mfma_f32_16x16x32_bf16 v[132:135], a[148:151], v[68:71], v[132:135]\n"
      "v_mfma_f32_16x16x32_bf16 v[132:135], a[152:155], v[84:87], v[132:135]\n"
      "s_waitcnt lgkmcnt(3)\n"
      "v_mfma_f32_16x16x32_bf16 v[132:135], a[156:159], v[88:91], v[132:135]\n"
      "global_store_dwordx4 v252, v[220:223], s[54:55] offset:0 sc1\n"
      "v_mfma_f32_16x16x32_bf16 v[132:135], a[160:163], v[92:95], v[132:135]\n"
      "s_add_u32 m0, s17, 0x1f00\n"
      "v_mfma_f32_16x16x32_bf16 v[132:135], a[164:167], v[108:111], v[132:135]\n"
      "ds_write_b64 v244, v[210:211] offset:8\n"
      "v_mfma_f32_16x16x32_bf16 v[132:135], a[168:171], v[112:115], v[132:135]\n"
      "global_load_lds_dwordx4 v236, s[52:53] offset:256 nt\n"
      "v_mfma_f32_16x16x32_bf16 v[132:135], a[172:175], v[116:119], v[132:135]\n"
      "v_mfma_f32_16x16x32_bf16 v[136:139], a[176:179], v[36:39], v[12:15]\n"
      "v_mfma_f32_16x16x32_bf16 v[136:139], a[180:183], v[40:43], v[136:139]\n"
      "v_mfma_f32_16x16x32_bf16 v[136:139], a[184:187], v[44:47], v[136:139]\n"
      "v_mfma_f32_16x16x32_bf16 v[136:139], a[188:191], v[60:63], v[136:139]\n"
      "v_cvt_pk_bf16_f32 v212, v132, v133\n"
      "v_mfma_f32_16x16x32_bf16 v[136:139], a[192:195], v[64:67], v[136:139]\n"
      "v_cvt_pk_bf16_f32 v213, v134, v135\n"
      "v_mfma_f32_16x16x32_bf16 v[136:139], a[196:199], v[68:71], v[136:139]\n"
      "v_mfma_f32_16x16x32_bf16 v[136:139], a[200:203], v[84:87], v[136:139]\n"
      "v_mfma_f32_16x16x32_bf16 v[136:139], a[204:207], v[88:91], v[136:139]\n"
      "s_waitcnt lgkmcnt(3)\n"
      "v_mfma_f32_16x16x32_bf16 v[136:139], a[208:211], v[92:95], v[136:139]\n"
      "global_store_dwordx4 v253, v[224:227], s[54:55] offset:0 sc1\n"
      "v_mfma_f32_16x16x32_bf16 v[136:139], a[212:215], v[108:111], v[136:139]\n"
      "ds_write_b64 v245, v[212:213] offset:0\n"
      "v_mfma_f32_16x16x32_bf16 v[136:139], a[216:219], v[112:115], v[136:139]\n"
      "s_add_u32 m0, s17, 0x3e00\n"
      "v_mfma_f32_16x16x32_bf16 v[136:139], a[220:223], v[116:119], v[136:139]\n"
      "global_load_lds_dwordx4 v236, s[52:53] offset:512 nt\n"
      "v_mfma_f32_16x16x32_bf16 v[132:135], a[224:227], v[36:39], v[16:19]\n"
      "v_mfma_f32_16x16x32_bf16 v[132:135], a[228:231], v[40:43], v[132:135]\n"
      "v_mfma_f32_16x16x32_bf16 v[132:135], a[232:235], v[44:47], v[132:135]\n"
      "v_mfma_f32_16x16x32_bf16 v[132:135], a[236:239], v[60:63], v[132:135]\n"
      "v_cvt_pk_bf16_f32 v214, v136, v137\n"
      "v_mfma_f32_16x16x32_bf16 v[132:135], a[0:3], v[64:67], v[132:135]\n"
      "v_cvt_pk_bf16_f32 v215, v138, v139\n"
      "v_mfma_f32_16x16x32_bf16 v[132:135], a[4:7], v[68:71], v[132:135]\n"
      "v_mfma_f32_16x16x32_bf16 v[132:135], a[8:11], v[84:87], v[132:135]\n"
      "v_mfma_f32_16x16x32_bf16 v[132:135], a[12:15], v[88:91], v[132:135]\n"
      "v_mfma_f32_16x16x32_bf16 v[132:135], a[16:19], v[92:95], v[132:135]\n"
      "v_mfma_f32_16x16x32_bf16 v[132:135], a[20:23], v[108:111], v[132:135]\n"
      "ds_write_b64 v245, v[214:215] offset:8\n"
      "v_mfma_f32_16x16x32_bf16 v[132:135], v[140:143], v[112:115], v[132:135]\n"
      "v_mfma_f32_16x16x32_bf16 v[132:135], v[156:159], v[116:119], v[132:135]\n"
      "s_waitcnt lgkmcnt(4)\n"
      "s_barrier\n"
      "v_mfma_f32_16x16x32_bf16 v[136:139], v[160:163], v[36:39], v[20:23]\n"
      "ds_read_b128 v[36:39], v232 offset:24832\n"
      "v_mfma_f32_16x16x32_bf16 v[136:139], v[164:167], v[40:43], v[136:139]\n"
      "ds_read_b128 v[40:43], v233 offset:24832\n"
      "v_mfma_f32_16x16x32_bf16 v[136:139], v[180:183], v[44:47], v[136:139]\n"
      "ds_read_b128 v[44:47], v234 offset:24832\n"
      "v_mfma_f32_16x16x32_bf16 v[136:139], v[184:187], v[60:63], v[136:139]\n"
      "ds_read_b128 v[60:63], v235 offset:24832\n"
      "v_cvt_pk_bf16_f32 v208, v132, v133\n"
      "v_mfma_f32_16x16x32_bf16 v[136:139], v[188:191], v[64:67], v[136:139]\n"
      "ds_read_b128 v[64:67], v232 offset:33024\n"
      "v_cvt_pk_bf16_f32 v209, v134, v135\n"
      "v_mfma_f32_16x16x32_bf16 v[136:139], v[204:207], v[68:71], v[136:139]\n"
      "ds_read_b128 v[68:71], v233 offset:33024\n"
      "v_mfma_f32_16x16x32_bf16 v[136:139], a[24:27], v[84:87], v[136:139]\n"
      "ds_read_b128 v[84:87], v234 offset:33024\n"
      "v_mfma_f32_16x16x32_bf16 v[136:139], a[28:31], v[88:91], v[136:139]\n"
      "ds_read_b128 v[88:91], v235 offset:33024\n"
      "v_mfma_f32_16x16x32_bf16 v[136:139], a[32:35], v[92:95], v[136:139]\n"
      "ds_read_b128 v[92:95], v232 offset:41216\n"
      "v_mfma_f32_16x16x32_bf16 v[136:139], a[36:39], v[108:111], v[136:139]\n"
      "ds_read_b128 v[108:111], v233 offset:41216\n"
      "ds_write_b64 v244, v[208:209] offset:128\n"
      "v_mfma_f32_16x16x32_bf16 v[136:139], a[40:43], v[112:115], v[136:139]\n"
      "ds_read_b128 v[112:115], v234 offset:41216\n"
      "v_mfma_f32_16x16x32_bf16 v[136:139], a[44:47], v[116:119], v[136:139]\n"
      "ds_read_b128 v[116:119], v235 offset:41216\n"
      "s_waitcnt lgkmcnt(9)\n"
      "v_mfma_f32_16x16x32_bf16 v[132:135], a[240:243], v[36:39], v[0:3]\n"
      "v_add_u32 v232, 0xc000, v232\n"
      "v_mfma_f32_16x16x32_bf16 v[132:135], a[244:247], v[40:43], v[132:135]\n"
      "v_add_u32 v233, 0xc000, v233\n"
      "LPD8:\n"
      "s_cbranch_execz LPD11\n"
      "v_mfma_f32_16x16x32_bf16 v[132:135], a[248:251], v[44:47], v[132:135]\n"
      "v_add_u32 v234, 0xc000, v234\n"
      "v_mfma_f32_16x16x32_bf16 v[132:135], a[252:255], v[60:63], v[132:135]\n"
      "v_add_u32 v235, 0xc000, v235\n"
      "s_waitcnt lgkmcnt(5)\n"
      "v_mfma_f32_16x16x32_bf16 v[132:135], a[48:51], v[64:67], v[132:135]\n"
      "v_cvt_pk_bf16_f32 v210, v136, v137\n"
      "v_mfma_f32_16x16x32_bf16 v[132:135], a[52:55], v[68:71], v[132:135]\n"
      "v_cvt_pk_bf16_f32 v211, v138, v139\n"
      "v_mfma_f32_16x16x32_bf16 v[132:135], a[56:59], v[84:87], v[132:135]\n"
      "ds_read_b128 v[216:219], v246 offset:24576\n"
      "v_mfma_f32_16x16x32_bf16 v[132:135], a[60:63], v[88:91], v[132:135]\n"
      "ds_read_b128 v[220:223], v246 offset:25600\n"
      "s_waitcnt lgkmcnt(2)\n"
      "v_mfma_f32_16x16x32_bf16 v[132:135], a[64:67], v[92:95], v[132:135]\n"
      "ds_read_b128 v[224:227], v246 offset:26624\n"
      "v_mfma_f32_16x16x32_bf16 v[132:135], a[68:71], v[108:111], v[132:135]\n"
      "v_lshlrev_b32 v0, 16, v24\n"
      "v_mfma_f32_16x16x32_bf16 v[132:135], a[72:75], v[112:115], v[132:135]\n"
      "ds_write_b64 v244, v[210:211] offset:136\n"
      "v_mfma_f32_16x16x32_bf16 v[132:135], a[76:79], v[116:119], v[132:135]\n"
      "v_and_b32 v1, 0xffff0000, v24\n"
      "v_mfma_f32_16x16x32_bf16 v[136:139], a[80:83], v[36:39], v[4:7]\n"
      "v_lshlrev_b32 v2, 16, v25\n"
      "v_mfma_f32_16x16x32_bf16 v[136:139], a[84:87], v[40:43], v[136:139]\n"
      "v_and_b32 v3, 0xffff0000, v25\n"
      "v_mfma_f32_16x16x32_bf16 v[136:139], a[88:91], v[44:47], v[136:139]\n"
      "v_mfma_f32_16x16x32_bf16 v[136:139], a[92:95], v[60:63], v[136:139]\n"
      "v_cvt_pk_bf16_f32 v208, v132, v133\n"
      "v_mfma_f32_16x16x32_bf16 v[136:139], a[96:99], v[64:67], v[136:139]\n"
      "v_cvt_pk_bf16_f32 v209, v134, v135\n"
      "v_mfma_f32_16x16x32_bf16 v[136:139], a[100:103], v[68:71], v[136:139]\n"
      "s_waitcnt lgkmcnt(3)\n"
      "v_mfma_f32_16x16x32_bf16 v[136:139], a[104:107], v[84:87], v[136:139]\n"
      "global_store_dwordx4 v247, v[216:219], s[54:55] offset:768 sc1\n"
      "v_mfma_f32_16x16x32_bf16 v[136:139], a[108:111], v[88:91], v[136:139]\n"
      "s_add_u32 m0, s17, 0x1000\n"
      "v_mfma_f32_16x16x32_bf16 v[136:139], a[112:115], v[92:95], v[136:139]\n"
      "global_load_lds_dwordx4 v237, s[52:53] offset:0 nt\n"
      "v_mfma_f32_16x16x32_bf16 v[136:139], a[116:119], v[108:111], v[136:139]\n"
      "ds_write_b64 v244, v[208:209] offset:12288\n"
      "v_mfma_f32_16x16x32_bf16 v[136:139], a[120:123], v[112:115], v[136:139]\n"
      "v_lshlrev_b32 v4, 16, v26\n"
      "v_mfma_f32_16x16x32_bf16 v[136:139], a[124:127], v[116:119], v[136:139]\n"
      "v_and_b32 v5, 0xffff0000, v26\n"
      "v_mfma_f32_16x16x32_bf16 v[132:135], a[128:131], v[36:39], v[8:11]\n"
      "v_lshlrev_b32 v6, 16, v27\n"
      "v_mfma_f32_16x16x32_bf16 v[132:135], a[132:135], v[40:43], v[132:135]\n"
      "v_and_b32 v7, 0xffff0000, v27\n"
      "v_mfma_f32_16x16x32_bf16 v[132:135], a[136:139], v[44:47], v[132:135]\n"
      "v_mfma_f32_16x16x32_bf16 v[132:135], a[140:143], v[60:63], v[132:135]\n"
      "v_cvt_pk_bf16_f32 v210, v136, v137\n"
      "v_mfma_f32_16x16x32_bf16 v[132:135], a[144:147], v[64:67], v[132:135]\n"
      "v_cvt_pk_bf16_f32 v211, v138, v139\n"
      "v_mfma_f32_16x16x32_bf16 v[132:135], a[148:151], v[68:71], v[132:135]\n"
      "v_lshlrev_b32 v8, 16, v28\n"
      "v_mfma_f32_16x16x32_bf16 v[132:135], a[152:155], v[84:87], v[132:135]\n"
      "s_waitcnt lgkmcnt(3)\n"
      "v_mfma_f32_16x16x32_bf16 v[132:135], a[156:159], v[88:91], v[132:135]\n"
      "global_store_dwordx4 v252, v[220:223], s[54:55] offset:768 sc1\n"
      "v_mfma_f32_16x16x32_bf16 v[132:135], a[160:163], v[92:95], v[132:135]\n"
      "s_add_u32 m0, s17, 0x2f00\n"
      "v_mfma_f32_16x16x32_bf16 v[132:135], a[164:167], v[108:111], v[132:135]\n"
      "ds_write_b64 v244, v[210:211] offset:12296\n"
      "v_mfma_f32_16x16x32_bf16 v[132:135], a[168:171], v[112:115], v[132:135]\n"
      "global_load_lds_dwordx4 v237, s[52:53] offset:256 nt\n"
      "v_mfma_f32_16x16x32_bf16 v[132:135], a[172:175], v[116:119], v[132:135]\n"
      "v_and_b32 v9, 0xffff0000, v28\n"
      "v_mfma_f32_16x16x32_bf16 v[136:139], a[176:179], v[36:39], v[12:15]\n"
      "v_lshlrev_b32 v10, 16, v29\n"
      "v_mfma_f32_16x16x32_bf16 v[136:139], a[180:183], v[40:43], v[136:139]\n"
      "v_and_b32 v11, 0xffff0000, v29\n"
      "v_mfma_f32_16x16x32_bf16 v[136:139], a[184:187], v[44:47], v[136:139]\n"
      "v_mfma_f32_16x16x32_bf16 v[136:139], a[188:191], v[60:63], v[136:139]\n"
      "v_cvt_pk_bf16_f32 v212, v132, v133\n"
      "v_mfma_f32_16x16x32_bf16 v[136:139], a[192:195], v[64:67], v[136:139]\n"
      "v_cvt_pk_bf16_f32 v213, v134, v135\n"
      "v_mfma_f32_16x16x32_bf16 v[136:139], a[196:199], v[68:71], v[136:139]\n"
      "v_lshlrev_b32 v12, 16, v30\n"
      "v_mfma_f32_16x16x32_bf16 v[136:139], a[200:203], v[84:87], v[136:139]\n"
      "v_and_b32 v13, 0xffff0000, v30\n"
      "v_mfma_f32_16x16x32_bf16 v[136:139], a[204:207], v[88:91], v[136:139]\n"
      "s_waitcnt lgkmcnt(3)\n"
      "v_mfma_f32_16x16x32_bf16 v[136:139], a[208:211], v[92:95], v[136:139]\n"
      "global_store_dwordx4 v253, v[224:227], s[54:55] offset:768 sc1\n"
      "v_mfma_f32_16x16x32_bf16 v[136:139], a[212:215], v[108:111], v[136:139]\n"
      "ds_write_b64 v245, v[212:213] offset:12288\n"
      "v_mfma_f32_16x16x32_bf16 v[136:139], a[216:219], v[112:115], v[136:139]\n"
      "s_add_u32 m0, s17, 0x4e00\n"
      "v_mfma_f32_16x16x32_bf16 v[136:139], a[220:223], v[116:119], v[136:139]\n"
      "global_load_lds_dwordx4 v237, s[52:53] offset:512 nt\n"
      "v_mfma_f32_16x16x32_bf16 v[132:135], a[224:227], v[36:39], v[16:19]\n"
      "v_lshlrev_b32 v14, 16, v31\n"
      "v_mfma_f32_16x16x32_bf16 v[132:135], a[228:231], v[40:43], v[132:135]\n"
      "v_and_b32 v15, 0xffff0000, v31\n"
      "v_mfma_f32_16x16x32_bf16 v[132:135], a[232:235], v[44:47], v[132:135]\n"
      "v_mfma_f32_16x16x32_bf16 v[132:135], a[236:239], v[60:63], v[132:135]\n"
      "s_memrealtime s[84:85]\n"
      "v_mfma_f32_16x16x32_bf16 v[132:135], a[0:3], v[64:67], v[132:135]\n"
      "v_cvt_pk_bf16_f32 v214, v136, v137\n"
      "v_mfma_f32_16x16x32_bf16 v[132:135], a[4:7], v[68:71], v[132:135]\n"
      "v_cvt_pk_bf16_f32 v215, v138, v139\n"
      "v_mfma_f32_16x16x32_bf16 v[132:135], a[8:11], v[84:87], v[132:135]\n"
      "v_lshlrev_b32 v16, 16, v32\n"
      "v_mfma_f32_16x16x32_bf16 v[132:135], a[12:15], v[88:91], v[132:135]\n"
      "v_and_b32 v17, 0xffff0000, v32\n"
      "v_mfma_f32_16x16x32_bf16 v[132:135], a[16:19], v[92:95], v[132:135]\n"
      "v_lshlrev_b32 v18, 16, v33\n"
      "v_mfma_f32_16x16x32_bf16 v[132:135], a[20:23], v[108:111], v[132:135]\n"
      "v_and_b32 v19, 0xffff0000, v33\n"
      "v_mfma_f32_16x16x32_bf16 v[132:135], v[140:143], v[112:115], v[132:135]\n"
      "s_waitcnt lgkmcnt(0)\n"
      "s_mov_b32 s48, s84\n"
      "v_mfma_f32_16x16x32_bf16 v[132:135], v[156:159], v[116:119], v[132:135]\n"
      "s_waitcnt vmcnt(18)\n"
      "s_barrier\n"
      "ds_write_b64 v245, v[214:215] offset:12296\n"
      "v_mfma_f32_16x16x32_bf16 v[136:139], v[160:163], v[36:39], v[20:23]\n"
      "ds_read_b128 v[36:39], v232 offset:0\n"
      "v_mfma_f32_16x16x32_bf16 v[136:139], v[164:167], v[40:43], v[136:139]\n"
      "ds_read_b128 v[40:43], v233 offset:0\n"
      "v_mfma_f32_16x16x32_bf16 v[136:139], v[180:183], v[44:47], v[136:139]\n"
      "ds_read_b128 v[44:47], v234 offset:0\n"
      "v_mfma_f32_16x16x32_bf16 v[136:139], v[184:187], v[60:63], v[136:139]\n"
      "ds_read_b128 v[60:63], v235 offset:0\n"
      "v_cvt_pk_bf16_f32 v208, v132, v133\n"
      "v_mfma_f32_16x16x32_bf16 v[136:139], v[188:191], v[64:67], v[136:139]\n"
      "ds_read_b128 v[64:67], v232 offset:8192\n"
      "v_cvt_pk_bf16_f32 v209, v134, v135\n"
      "v_mfma_f32_16x16x32_bf16 v[136:139], v[204:207], v[68:71], v[136:139]\n"
      "ds_read_b128 v[68:71], v233 offset:8192\n"
      "v_mfma_f32_16x16x32_bf16 v[136:139], a[24:27], v[84:87], v[136:139]\n"
      "ds_read_b128 v[84:87], v234 offset:8192\n"
      "v_mfma_f32_16x16x32_bf16 v[136:139], a[28:31], v[88:91], v[136:139]\n"
      "ds_read_b128 v[88:91], v235 offset:8192\n"
      "v_mfma_f32_16x16x32_bf16 v[136:139], a[32:35], v[92:95], v[136:139]\n"
      "ds_read_b128 v[92:95], v232 offset:16384\n"
      "v_mfma_f32_16x16x32_bf16 v[136:139], a[36:39], v[108:111], v[136:139]\n"
      "ds_read_b128 v[108:111], v233 offset:16384\n"
      "v_mfma_f32_16x16x32_bf16 v[136:139], a[40:43], v[112:115], v[136:139]\n"
      "ds_read_b128 v[112:115], v234 offset:16384\n"
      "v_mfma_f32_16x16x32_bf16 v[136:139], a[44:47], v[116:119], v[136:139]\n"
      "ds_read_b128 v[116:119], v235 offset:16384\n"
      "s_waitcnt lgkmcnt(8)\n"
      "v_mfma_f32_16x16x32_bf16 v[132:135], a[240:243], v[36:39], v[0:3]\n"
      "ds_write_b64 v244, v[208:209] offset:12416\n"
      "v_mfma_f32_16x16x32_bf16 v[132:135], a[244:247], v[40:43], v[132:135]\n"
      "v_lshlrev_b32 v20, 16, v34\n"
      "v_mfma_f32_16x16x32_bf16 v[132:135], a[248:251], v[44:47], v[132:135]\n"
      "v_and_b32 v21, 0xffff0000, v34\n"
      "v_mfma_f32_16x16x32_bf16 v[132:135], a[252:255], v[60:63], v[132:135]\n"
      "v_lshlrev_b32 v22, 16, v35\n"
      "s_waitcnt lgkmcnt(5)\n"
      "v_mfma_f32_16x16x32_bf16 v[132:135], a[48:51], v[64:67], v[132:135]\n"
      "v_and_b32 v23, 0xffff0000, v35\n"
      "v_mfma_f32_16x16x32_bf16 v[132:135], a[52:55], v[68:71], v[132:135]\n"
      "v_cvt_pk_bf16_f32 v210, v136, v137\n"
      "v_mfma_f32_16x16x32_bf16 v[132:135], a[56:59], v[84:87], v[132:135]\n"
      "v_cvt_pk_bf16_f32 v211, v138, v139\n"
      "v_mfma_f32_16x16x32_bf16 v[132:135], a[60:63], v[88:91], v[132:135]\n"
      "s_add_u32 s54, s44, s32\n"
      "s_addc_u32 s55, s45, 0\n"
      "s_waitcnt lgkmcnt(1)\n"
      "v_mfma_f32_16x16x32_bf16 v[132:135], a[64:67], v[92:95], v[132:135]\n"
      "ds_read_b128 v[216:219], v246 offset:0\n"
      "v_mfma_f32_16x16x32_bf16 v[132:135], a[68:71], v[108:111], v[132:135]\n"
      "ds_read_b128 v[220:223], v246 offset:1024\n"
      "v_mfma_f32_16x16x32_bf16 v[132:135], a[72:75], v[112:115], v[132:135]\n"
      "ds_read_b128 v[224:227], v246 offset:2048\n"
      "v_mfma_f32_16x16x32_bf16 v[132:135], a[76:79], v[116:119], v[132:135]\n"
      "ds_write_b64 v244, v[210:211] offset:12424\n"
      "v_mfma_f32_16x16x32_bf16 v[136:139], a[80:83], v[36:39], v[4:7]\n"
      "v_mfma_f32_16x16x32_bf16 v[136:139], a[84:87], v[40:43], v[136:139]\n"
      "v_mfma_f32_16x16x32_bf16 v[136:139], a[88:91], v[44:47], v[136:139]\n"
      "v_mfma_f32_16x16x32_bf16 v[136:139], a[92:95], v[60:63], v[136:139]\n"
      "v_cvt_pk_bf16_f32 v208, v132, v133\n"
      "v_mfma_f32_16x16x32_bf16 v[136:139], a[96:99], v[64:67], v[136:139]\n"
      "v_cvt_pk_bf16_f32 v209, v134, v135\n"
      "v_mfma_f32_16x16x32_bf16 v[136:139], a[100:103], v[68:71], v[136:139]\n"
      "s_waitcnt lgkmcnt(3)\n"
      "v_mfma_f32_16x16x32_bf16 v[136:139], a[104:107], v[84:87], v[136:139]\n"
      "global_store_dwordx4 v247, v[216:219], s[54:55] offset:0 sc1\n"
      "v_mfma_f32_16x16x32_bf16 v[136:139], a[108:111], v[88:91], v[136:139]\n"
      "s_add_u32 s52, s42, s34\n"
      "s_addc_u32 s53, s43, 0\n"
      "v_mfma_f32_16x16x32_bf16 v[136:139], a[112:115], v[92:95], v[136:139]\n"
      "s_add_u32 m0, s17, 0x6000\n"
      "v_mfma_f32_16x16x32_bf16 v[136:139], a[116:119], v[108:111], v[136:139]\n"
      "ds_write_b64 v244, v[208:209] offset:24576\n"
      "v_mfma_f32_16x16x32_bf16 v[136:139], a[120:123], v[112:115], v[136:139]\n"
      "global_load_lds_dwordx4 v236, s[52:53] offset:0 nt\n"
      "v_mfma_f32_16x16x32_bf16 v[136:139], a[124:127], v[116:119], v[136:139]\n"
      "v_mfma_f32_16x16x32_bf16 v[132:135], a[128:131], v[36:39], v[8:11]\n"
      "v_mfma_f32_16x16x32_bf16 v[132:135], a[132:135], v[40:43], v[132:135]\n"
      "v_mfma_f32_16x16x32_bf16 v[132:135], a[136:139], v[44:47], v[132:135]\n"
      "v_mfma_f32_16x16x32_bf16 v[132:135], a[140:143], v[60:63], v[132:135]\n"
      "v_cvt_pk_bf16_f32 v210, v136, v137\n"
      "v_mfma_f32_16x16x32_bf16 v[132:135], a[144:147], v[64:67], v[132:135]\n"
      "v_cvt_pk_bf16_f32 v211, v138, v139\n"
      "v_mfma_f32_16x16x32_bf16 v[132:135], a[148:151], v[68:71], v[132:135]\n"
      "v_mfma_f32_16x16x32_bf16 v[132:135], a[152:155], v[84:87], v[132:135]\n"
      "s_waitcnt lgkmcnt(3)\n"
      "v_mfma_f32_16x16x32_bf16 v[132:135], a[156:159], v[88:91], v[132:135]\n"
      "global_store_dwordx4 v252, v[220:223], s[54:55] offset:0 sc1\n"
      "v_mfma_f32_16x16x32_bf16 v[132:135], a[160:163], v[92:95], v[132:135]\n"
      "s_add_u32 m0, s17, 0x7f00\n"
      "v_mfma_f32_16x16x32_bf16 v[132:135], a[164:167], v[108:111], v[132:135]\n"
      "ds_write_b64 v244, v[210:211] offset:24584\n"
      "v_mfma_f32_16x16x32_bf16 v[132:135], a[168:171], v[112:115], v[132:135]\n"
      "global_load_lds_dwordx4 v236, s[52:53] offset:256 nt\n"
      "v_mfma_f32_16x16x32_bf16 v[132:135], a[172:175], v[116:119], v[132:135]\n"
      "v_mfma_f32_16x16x32_bf16 v[136:139], a[176:179], v[36:39], v[12:15]\n"
      "v_mfma_f32_16x16x32_bf16 v[136:139], a[180:183], v[40:43], v[136:139]\n"
      "v_mfma_f32_16x16x32_bf16 v[136:139], a[184:187], v[44:47], v[136:139]\n"
      "v_mfma_f32_16x16x32_bf16 v[136:139], a[188:191], v[60:63], v[136:139]\n"
      "v_cvt_pk_bf16_f32 v212, v132, v133\n"
      "v_mfma_f32_16x16x32_bf16 v[136:139], a[192:195], v[64:67], v[136:139]\n"
      "v_cvt_pk_bf16_f32 v213, v134, v135\n"
      "v_mfma_f32_16x16x32_bf16 v[136:139], a[196:199], v[68:71], v[136:139]\n"
      "v_mfma_f32_16x16x32_bf16 v[136:139], a[200:203], v[84:87], v[136:139]\n"
      "v_mfma_f32_16x16x32_bf16 v[136:139], a[204:207], v[88:91], v[136:139]\n"
      "s_waitcnt lgkmcnt(3)\n"
      "v_mfma_f32_16x16x32_bf16 v[136:139], a[208:211], v[92:95], v[136:139]\n"
      "global_store_dwordx4 v253, v[224:227], s[54:55] offset:0 sc1\n"
      "v_mfma_f32_16x16x32_bf16 v[136:139], a[212:215], v[108:111], v[136:139]\n"
      "ds_write_b64 v245, v[212:213] offset:24576\n"
      "v_mfma_f32_16x16x32_bf16 v[136:139], a[216:219], v[112:115], v[136:139]\n"
      "s_add_u32 m0, s17, 0x9e00\n"
      "v_mfma_f32_16x16x32_bf16 v[136:139], a[220:223], v[116:119], v[136:139]\n"
      "global_load_lds_dwordx4 v236, s[52:53] offset:512 nt\n"
      "v_mfma_f32_16x16x32_bf16 v[132:135], a[224:227], v[36:39], v[16:19]\n"
      "v_mfma_f32_16x16x32_bf16 v[132:135], a[228:231], v[40:43], v[132:135]\n"
      "v_mfma_f32_16x16x32_bf16 v[132:135], a[232:235], v[44:47], v[132:135]\n"
      "v_mfma_f32_16x16x32_bf16 v[132:135], a[236:239], v[60:63], v[132:135]\n"
      "v_cvt_pk_bf16_f32 v214, v136, v137\n"
      "v_mfma_f32_16x16x32_bf16 v[132:135], a[0:3], v[64:67], v[132:135]\n"
      "v_cvt_pk_bf16_f32 v215, v138, v139\n"
      "v_mfma_f32_16x16x32_bf16 v[132:135], a[4:7], v[68:71], v[132:135]\n"
      "v_mfma_f32_16x16x32_bf16 v[132:135], a[8:11], v[84:87], v[132:135]\n"
      "v_mfma_f32_16x16x32_bf16 v[132:135], a[12:15], v[88:91], v[132:135]\n"
      "v_mfma_f32_16x16x32_bf16 v[132:135], a[16:19], v[92:95], v[132:135]\n"
      "v_mfma_f32_16x16x32_bf16 v[132:135], a[20:23], v[108:111], v[132:135]\n"
      "ds_write_b64 v245, v[214:215] offset:24584\n"
      "v_mfma_f32_16x16x32_bf16 v[132:135], v[140:143], v[112:115], v[132:135]\n"
      "v_mfma_f32_16x16x32_bf16 v[132:135], v[156:159], v[116:119], v[132:135]\n"
      "s_waitcnt lgkmcnt(4)\n"
      "s_barrier\n"
      "v_mfma_f32_16x16x32_bf16 v[136:139], v[160:163], v[36:39], v[20:23]\n"
      "ds_read_b128 v[36:39], v232 offset:256\n"
      "v_mfma_f32_16x16x32_bf16 v[136:139], v[164:167], v[40:43], v[136:139]\n"
      "ds_read_b128 v[40:43], v233 offset:256\n"
      "v_mfma_f32_16x16x32_bf16 v[136:139], v[180:183], v[44:47], v[136:139]\n"
      "ds_read_b128 v[44:47], v234 offset:256\n"
      "v_mfma_f32_16x16x32_bf16 v[136:139], v[184:187], v[60:63], v[136:139]\n"
      "ds_read_b128 v[60:63], v235 offset:256\n"
      "v_cvt_pk_bf16_f32 v208, v132, v133\n"
      "v_mfma_f32_16x16x32_bf16 v[136:139], v[188:191], v[64:67], v[136:139]\n"
      "ds_read_b128 v[64:67], v232 offset:8448\n"
      "v_cvt_pk_bf16_f32 v209, v134, v135\n"
      "v_mfma_f32_16x16x32_bf16 v[136:139], v[204:207], v[68:71], v[136:139]\n"
      "ds_read_b128 v[68:71], v233 offset:8448\n"
      "v_mfma_f32_16x16x32_bf16 v[136:139], a[24:27], v[84:87], v[136:139]\n"
      "ds_read_b128 v[84:87], v234 offset:8448\n"
      "v_mfma_f32_16x16x32_bf16 v[136:139], a[28:31], v[88:91], v[136:139]\n"
      "ds_read_b128 v[88:91], v235 offset:8448\n"
      "v_mfma_f32_16x16x32_bf16 v[136:139], a[32:35], v[92:95], v[136:139]\n"
      "ds_read_b128 v[92:95], v232 offset:16640\n"
      "v_mfma_f32_16x16x32_bf16 v[136:139], a[36:39], v[108:111], v[136:139]\n"
      "ds_read_b128 v[108:111], v233 offset:16640\n"
      "v_mfma_f32_16x16x32_bf16 v[136:139], a[40:43], v[112:115], v[136:139]\n"
      "ds_read_b128 v[112:115], v234 offset:16640\n"
      "v_mfma_f32_16x16x32_bf16 v[136:139], a[44:47], v[116:119], v[136:139]\n"
      "ds_read_b128 v[116:119], v235 offset:16640\n"
      "s_waitcnt lgkmcnt(8)\n"
      "v_mfma_f32_16x16x32_bf16 v[132:135], a[240:243], v[36:39], v[0:3]\n"
      "ds_write_b64 v244, v[208:209] offset:24704\n"
      "v_mfma_f32_16x16x32_bf16 v[132:135], a[244:247], v[40:43], v[132:135]\n"
      "v_mfma_f32_16x16x32_bf16 v[132:135], a[248:251], v[44:47], v[132:135]\n"
      "v_mfma_f32_16x16x32_bf16 v[132:135], a[252:255], v[60:63], v[132:135]\n"
      "v_cvt_pk_bf16_f32 v210, v136, v137\n"
      "s_waitcnt lgkmcnt(5)\n"
      "v_mfma_f32_16x16x32_bf16 v[132:135], a[48:51], v[64:67], v[132:135]\n"
      "v_cvt_pk_bf16_f32 v211, v138, v139\n"
      "v_mfma_f32_16x16x32_bf16 v[132:135], a[52:55], v[68:71], v[132:135]\n"
      "v_mfma_f32_16x16x32_bf16 v[132:135], a[56:59], v[84:87], v[132:135]\n"
      "ds_read_b128 v[216:219], v246 offset:12288\n"
      "v_mfma_f32_16x16x32_bf16 v[132:135], a[60:63], v[88:91], v[132:135]\n"
      "ds_read_b128 v[220:223], v246 offset:13312\n"
      "s_waitcnt lgkmcnt(3)\n"
      "v_mfma_f32_16x16x32_bf16 v[132:135], a[64:67], v[92:95], v[132:135]\n"
      "ds_read_b128 v[224:227], v246 offset:14336\n"
      "v_mfma_f32_16x16x32_bf16 v[132:135], a[68:71], v[108:111], v[132:135]\n"
      "ds_write_b64 v244, v[210:211] offset:24712\n"
      "v_mfma_f32_16x16x32_bf16 v[132:135], a[72:75], v[112:115], v[132:135]\n"
      "v_mfma_f32_16x16x32_bf16 v[132:135], a[76:79], v[116:119], v[132:135]\n"
      "v_mfma_f32_16x16x32_bf16 v[136:139], a[80:83], v[36:39], v[4:7]\n"
      "v_mfma_f32_16x16x32_bf16 v[136:139], a[84:87], v[40:43], v[136:139]\n"
      "v_mfma_f32_16x16x32_bf16 v[136:139], a[88:91], v[44:47], v[136:139]\n"
      "v_mfma_f32_16x16x32_bf16 v[136:139], a[92:95], v[60:63], v[136:139]\n"
      "v_cvt_pk_bf16_f32 v208, v132, v133\n"
      "v_mfma_f32_16x16x32_bf16 v[136:139], a[96:99], v[64:67], v[136:139]\n"
      "v_cvt_pk_bf16_f32 v209, v134, v135\n"
      "v_mfma_f32_16x16x32_bf16 v[136:139], a[100:103], v[68:71], v[136:139]\n"
      "s_waitcnt lgkmcnt(3)\n"
      "v_mfma_f32_16x16x32_bf16 v[136:139], a[104:107], v[84:87], v[136:139]\n"
      "global_store_dwordx4 v247, v[216:219], s[54:55] offset:768 sc1\n"
      "v_mfma_f32_16x16x32_bf16 v[136:139], a[108:111], v[88:91], v[136:139]\n"
      "s_add_u32 m0, s17, 0x7000\n"
      "v_mfma_f32_16x16x32_bf16 v[136:139], a[112:115], v[92:95], v[136:139]\n"
      "global_load_lds_dwordx4 v237, s[52:53] offset:0 nt\n"
      "v_mfma_f32_16x16x32_bf16 v[136:139], a[116:119], v[108:111], v[136:139]\n"
      "ds_write_b64 v244, v[208:209] offset:0\n"
      "v_mfma_f32_16x16x32_bf16 v[136:139], a[120:123], v[112:115], v[136:139]\n"
      "v_mfma_f32_16x16x32_bf16 v[136:139], a[124:127], v[116:119], v[136:139]\n"
      "v_mfma_f32_16x16x32_bf16 v[132:135], a[128:131], v[36:39], v[8:11]\n"
      "v_mfma_f32_16x16x32_bf16 v[132:135], a[132:135], v[40:43], v[132:135]\n"
      "v_mfma_f32_16x16x32_bf16 v[132:135], a[136:139], v[44:47], v[132:135]\n"
      "v_mfma_f32_16x16x32_bf16 v[132:135], a[140:143], v[60:63], v[132:135]\n"
      "v_cvt_pk_bf16_f32 v210, v136, v137\n"
      "v_mfma_f32_16x16x32_bf16 v[132:135], a[144:147], v[64:67], v[132:135]\n"
      "v_cvt_pk_bf16_f32 v211, v138, v139\n"
      "v_mfma_f32_16x16x32_bf16 v[132:135], a[148:151], v[68:71], v[132:135]\n"
      "v_mfma_f32_16x16x32_bf16 v[132:135], a[152:155], v[84:87], v[132:135]\n"
      "s_waitcnt lgkmcnt(3)\n"
      "v_mfma_f32_16x16x32_bf16 v[132:135], a[156:159], v[88:91], v[132:135]\n"
      "global_store_dwordx4 v252, v[220:223], s[54:55] offset:768 sc1\n"
      "v_mfma_f32_16x16x32_bf16 v[132:135], a[160:163], v[92:95], v[132:135]\n"
      "s_add_u32 m0, s17, 0x8f00\n"
      "v_mfma_f32_16x16x32_bf16 v[132:135], a[164:167], v[108:111], v[132:135]\n"
      "ds_write_b64 v244, v[210:211] offset:8\n"
      "v_mfma_f32_16x16x32_bf16 v[132:135], a[168:171], v[112:115], v[132:135]\n"
      "global_load_lds_dwordx4 v237, s[52:53] offset:256 nt\n"
      "v_mfma_f32_16x16x32_bf16 v[132:135], a[172:175], v[116:119], v[132:135]\n"
      "v_mfma_f32_16x16x32_bf16 v[136:139], a[176:179], v[36:39], v[12:15]\n"
      "v_mfma_f32_16x16x32_bf16 v[136:139], a[180:183], v[40:43], v[136:139]\n"
      "v_mfma_f32_16x16x32_bf16 v[136:139], a[184:187], v[44:47], v[136:139]\n"
      "v_mfma_f32_16x16x32_bf16 v[136:139], a[188:191], v[60:63], v[136:139]\n"
      "v_cvt_pk_bf16_f32 v212, v132, v133\n"
      "v_mfma_f32_16x16x32_bf16 v[136:139], a[192:195], v[64:67], v[136:139]\n"
      "v_cvt_pk_bf16_f32 v213, v134, v135\n"
      "v_mfma_f32_16x16x32_bf16 v[136:139], a[196:199], v[68:71], v[136:139]\n"
      "v_mfma_f32_16x16x32_bf16 v[136:139], a[200:203], v[84:87], v[136:139]\n"
      "v_mfma_f32_16x16x32_bf16 v[136:139], a[204:207], v[88:91], v[136:139]\n"
      "s_waitcnt lgkmcnt(3)\n"
      "v_mfma_f32_16x16x32_bf16 v[136:139], a[208:211], v[92:95], v[136:139]\n"
      "global_store_dwordx4 v253, v[224:227], s[54:55] offset:768 sc1\n"
      "v_mfma_f32_16x16x32_bf16 v[136:139], a[212:215], v[108:111], v[136:139]\n"
      "ds_write_b64 v245, v[212:213] offset:0\n"
      "v_mfma_f32_16x16x32_bf16 v[136:139], a[216:219], v[112:115], v[136:139]\n"
      "s_add_u32 m0, s17, 0xae00\n"
      "v_mfma_f32_16x16x32_bf16 v[136:139], a[220:223], v[116:119], v[136:139]\n"
      "global_load_lds_dwordx4 v237, s[52:53] offset:512 nt\n"
      "v_mfma_f32_16x16x32_bf16 v[132:135], a[224:227], v[36:39], v[16:19]\n"
      "v_mfma_f32_16x16x32_bf16 v[132:135], a[228:231], v[40:43], v[132:135]\n"
      "v_mfma_f32_16x16x32_bf16 v[132:135], a[232:235], v[44:47], v[132:135]\n"
      "v_mfma_f32_16x16x32_bf16 v[132:135], a[236:239], v[60:63], v[132:135]\n"
      "v_cvt_pk_bf16_f32 v214, v136, v137\n"
      "v_mfma_f32_16x16x32_bf16 v[132:135], a[0:3], v[64:67], v[132:135]\n"
      "v_cvt_pk_bf16_f32 v215, v138, v139\n"
      "v_mfma_f32_16x16x32_bf16 v[132:135], a[4:7], v[68:71], v[132:135]\n"
      "v_mfma_f32_16x16x32_bf16 v[132:135], a[8:11], v[84:87], v[132:135]\n"
      "v_mfma_f32_16x16x32_bf16 v[132:135], a[12:15], v[88:91], v[132:135]\n"
      "v_mfma_f32_16x16x32_bf16 v[132:135], a[16:19], v[92:95], v[132:135]\n"
      "v_mfma_f32_16x16x32_bf16 v[132:135], a[20:23], v[108:111], v[132:135]\n"
      "ds_write_b64 v245, v[214:215] offset:8\n"
      "v_mfma_f32_16x16x32_bf16 v[132:135], v[140:143], v[112:115], v[132:135]\n"
      "s_lshl_b32 s79, s90, 1\n"
      "v_mfma_f32_16x16x32_bf16 v[132:135], v[156:159], v[116:119], v[132:135]\n"
      "s_waitcnt vmcnt(24) lgkmcnt(4)\n"
      "s_barrier\n"
      "v_mad_i32_i24 v252, v251, s79, v252\n"
      "v_mfma_f32_16x16x32_bf16 v[136:139], v[160:163], v[36:39], v[20:23]\n"
      "ds_read_b128 v[36:39], v232 offset:24576\n"
      "s_mul_i32 s79, s79, s100\n"
      "v_mfma_f32_16x16x32_bf16 v[136:139], v[164:167], v[40:43], v[136:139]\n"
      "ds_read_b128 v[40:43], v233 offset:24576\n"
      "v_add_u32 v247, s79, v247\n"
      "v_mfma_f32_16x16x32_bf16 v[136:139], v[180:183], v[44:47], v[136:139]\n"
      "ds_read_b128 v[44:47], v234 offset:24576\n"
      "s_mul_i32 s79, s79, s101\n"
      "v_mfma_f32_16x16x32_bf16 v[136:139], v[184:187], v[60:63], v[136:139]\n"
      "ds_read_b128 v[60:63], v235 offset:24576\n"
      "v_cvt_pk_bf16_f32 v208, v132, v133\n"
      "v_mfma_f32_16x16x32_bf16 v[136:139], v[188:191], v[64:67], v[136:139]\n"
      "ds_read_b128 v[64:67], v232 offset:32768\n"
      "v_cvt_pk_bf16_f32 v209, v134, v135\n"
      "v_mfma_f32_16x16x32_bf16 v[136:139], v[204:207], v[68:71], v[136:139]\n"
      "ds_read_b128 v[68:71], v233 offset:32768\n"
      "v_add_u32 v253, s79, v253\n"
      "v_mfma_f32_16x16x32_bf16 v[136:139], a[24:27], v[84:87], v[136:139]\n"
      "ds_read_b128 v[84:87], v234 offset:32768\n"
      "v_mfma_f32_16x16x32_bf16 v[136:139], a[28:31], v[88:91], v[136:139]\n"
      "ds_read_b128 v[88:91], v235 offset:32768\n"
      "v_mfma_f32_16x16x32_bf16 v[136:139], a[32:35], v[92:95], v[136:139]\n"
      "ds_read_b128 v[92:95], v232 offset:40960\n"
      "v_mfma_f32_16x16x32_bf16 v[136:139], a[36:39], v[108:111], v[136:139]\n"
      "ds_read_b128 v[108:111], v233 offset:40960\n"
      "v_mfma_f32_16x16x32_bf16 v[136:139], a[40:43], v[112:115], v[136:139]\n"
      "ds_read_b128 v[112:115], v234 offset:40960\n"
      "v_mfma_f32_16x16x32_bf16 v[136:139], a[44:47], v[116:119], v[136:139]\n"
      "ds_read_b128 v[116:119], v235 offset:40960\n"
      "s_waitcnt lgkmcnt(8)\n"
      "v_mfma_f32_16x16x32_bf16 v[132:135], a[240:243], v[36:39], v[0:3]\n"
      "ds_write_b64 v244, v[208:209] offset:128\n"
      "v_mfma_f32_16x16x32_bf16 v[132:135], a[244:247], v[40:43], v[132:135]\n"
      "s_add_u32 s54, s30, s33\n"
      "s_addc_u32 s55, s31, 0\n"
      "v_mfma_f32_16x16x32_bf16 v[132:135], a[248:251], v[44:47], v[132:135]\n"
      "v_mfma_f32_16x16x32_bf16 v[132:135], a[252:255], v[60:63], v[132:135]\n"
      "v_cvt_pk_bf16_f32 v210, v136, v137\n"
      "s_waitcnt lgkmcnt(5)\n"
      "v_mfma_f32_16x16x32_bf16 v[132:135], a[48:51], v[64:67], v[132:135]\n"
      "v_cvt_pk_bf16_f32 v211, v138, v139\n"
      "v_mfma_f32_16x16x32_bf16 v[132:135], a[52:55], v[68:71], v[132:135]\n"
      "s_mul_i32 s78, s92, s19\n"
      "v_mfma_f32_16x16x32_bf16 v[132:135], a[56:59], v[84:87], v[132:135]\n"
      "ds_read_b128 v[216:219], v246 offset:24576\n"
      "v_add_u32 v236, s78, v236\n"
      "v_mfma_f32_16x16x32_bf16 v[132:135], a[60:63], v[88:91], v[132:135]\n"
      "ds_read_b128 v[220:223], v246 offset:25600\n"
      "s_waitcnt lgkmcnt(3)\n"
      "v_mfma_f32_16x16x32_bf16 v[132:135], a[64:67], v[92:95], v[132:135]\n"
      "ds_read_b128 v[224:227], v246 offset:26624\n"
      "v_mfma_f32_16x16x32_bf16 v[132:135], a[68:71], v[108:111], v[132:135]\n"
      "ds_write_b64 v244, v[210:211] offset:136\n"
      "v_mfma_f32_16x16x32_bf16 v[132:135], a[72:75], v[112:115], v[132:135]\n"
      "s_mul_i32 s78, s78, s99\n"
      "v_mfma_f32_16x16x32_bf16 v[132:135], a[76:79], v[116:119], v[132:135]\n"
      "v_add_u32 v237, s78, v237\n"
      "v_mfma_f32_16x16x32_bf16 v[136:139], a[80:83], v[36:39], v[4:7]\n"
      "v_mfma_f32_16x16x32_bf16 v[136:139], a[84:87], v[40:43], v[136:139]\n"
      "v_mfma_f32_16x16x32_bf16 v[136:139], a[88:91], v[44:47], v[136:139]\n"
      "v_mfma_f32_16x16x32_bf16 v[136:139], a[92:95], v[60:63], v[136:139]\n"
      "v_cvt_pk_bf16_f32 v208, v132, v133\n"
      "v_mfma_f32_16x16x32_bf16 v[136:139], a[96:99], v[64:67], v[136:139]\n"
      "v_cvt_pk_bf16_f32 v209, v134, v135\n"
      "v_mfma_f32_16x16x32_bf16 v[136:139], a[100:103], v[68:71], v[136:139]\n"
      "s_waitcnt lgkmcnt(3)\n"
      "v_mfma_f32_16x16x32_bf16 v[136:139], a[104:107], v[84:87], v[136:139]\n"
      "global_store_dwordx4 v247, v[216:219], s[54:55] offset:0 sc1\n"
      "v_mfma_f32_16x16x32_bf16 v[136:139], a[108:111], v[88:91], v[136:139]\n"
      "s_add_u32 s52, s20, s35\n"
      "s_addc_u32 s53, s21, 0\n"
      "v_mfma_f32_16x16x32_bf16 v[136:139], a[112:115], v[92:95], v[136:139]\n"
      "s_add_u32 m0, s17, 0xc000\n"
      "v_mfma_f32_16x16x32_bf16 v[136:139], a[116:119], v[108:111], v[136:139]\n"
      "ds_write_b64 v244, v[208:209] offset:12288\n"
      "v_mfma_f32_16x16x32_bf16 v[136:139], a[120:123], v[112:115], v[136:139]\n"
      "global_load_lds_dwordx4 v236, s[52:53] offset:0 nt\n"
      "v_mfma_f32_16x16x32_bf16 v[136:139], a[124:127], v[116:119], v[136:139]\n"
      "v_mfma_f32_16x16x32_bf16 v[132:135], a[128:131], v[36:39], v[8:11]\n"
      "v_min_u32 v28, 0x370, v239\n"
      "v_mfma_f32_16x16x32_bf16 v[132:135], a[132:135], v[40:43], v[132:135]\n"
      "v_mfma_f32_16x16x32_bf16 v[132:135], a[136:139], v[44:47], v[132:135]\n"
      "v_mfma_f32_16x16x32_bf16 v[132:135], a[140:143], v[60:63], v[132:135]\n"
      "v_cvt_pk_bf16_f32 v210, v136, v137\n"
      "v_mfma_f32_16x16x32_bf16 v[132:135], a[144:147], v[64:67], v[132:135]\n"
      "v_cvt_pk_bf16_f32 v211, v138, v139\n"
      "v_mfma_f32_16x16x32_bf16 v[132:135], a[148:151], v[68:71], v[132:135]\n"
      "v_add_u32 v28, 0x2480, v28\n"
      "v_mfma_f32_16x16x32_bf16 v[132:135], a[152:155], v[84:87], v[132:135]\n"
      "s_waitcnt lgkmcnt(3)\n"
      "global_load_dwordx4 v[24:27], v28, s[28:29] sc1\n"
      "v_mfma_f32_16x16x32_bf16 v[132:135], a[156:159], v[88:91], v[132:135]\n"
      "global_store_dwordx4 v252, v[220:223], s[54:55] offset:0 sc1\n"
      "v_mfma_f32_16x16x32_bf16 v[132:135], a[160:163], v[92:95], v[132:135]\n"
      "s_add_u32 m0, s17, 0xdf00\n"
      "v_mfma_f32_16x16x32_bf16 v[132:135], a[164:167], v[108:111], v[132:135]\n"
      "ds_write_b64 v244, v[210:211] offset:12296\n"
      "v_mfma_f32_16x16x32_bf16 v[132:135], a[168:171], v[112:115], v[132:135]\n"
      "global_load_lds_dwordx4 v236, s[52:53] offset:256 nt\n"
      "v_mfma_f32_16x16x32_bf16 v[132:135], a[172:175], v[116:119], v[132:135]\n"
      "v_mfma_f32_16x16x32_bf16 v[136:139], a[176:179], v[36:39], v[12:15]\n"
      "v_mfma_f32_16x16x32_bf16 v[136:139], a[180:183], v[40:43], v[136:139]\n"
      "v_mfma_f32_16x16x32_bf16 v[136:139], a[184:187], v[44:47], v[136:139]\n"
      "v_mfma_f32_16x16x32_bf16 v[136:139], a[188:191], v[60:63], v[136:139]\n"
      "v_cvt_pk_bf16_f32 v212, v132, v133\n"
      "v_mfma_f32_16x16x32_bf16 v[136:139], a[192:195], v[64:67], v[136:139]\n"
      "v_cvt_pk_bf16_f32 v213, v134, v135\n"
      "v_mfma_f32_16x16x32_bf16 v[136:139], a[196:199], v[68:71], v[136:139]\n"
      "v_mfma_f32_16x16x32_bf16 v[136:139], a[200:203], v[84:87], v[136:139]\n"
      "v_mfma_f32_16x16x32_bf16 v[136:139], a[204:207], v[88:91], v[136:139]\n"
      "s_waitcnt lgkmcnt(3)\n"
      "v_mfma_f32_16x16x32_bf16 v[136:139], a[208:211], v[92:95], v[136:139]\n"
      "global_store_dwordx4 v253, v[224:227], s[54:55] offset:0 sc1\n"
      "v_mfma_f32_16x16x32_bf16 v[136:139], a[212:215], v[108:111], v[136:139]\n"
      "ds_write_b64 v245, v[212:213] offset:12288\n"
      "v_mfma_f32_16x16x32_bf16 v[136:139], a[216:219], v[112:115], v[136:139]\n"
      "s_add_u32 m0, s17, 0xfe00\n"
      "v_mfma_f32_16x16x32_bf16 v[136:139], a[220:223], v[116:119], v[136:139]\n"
      "global_load_lds_dwordx4 v236, s[52:53] offset:512 nt\n"
      "v_mfma_f32_16x16x32_bf16 v[132:135], a[224:227], v[36:39], v[16:19]\n"
      "v_mfma_f32_16x16x32_bf16 v[132:135], a[228:231], v[40:43], v[132:135]\n"
      "v_mfma_f32_16x16x32_bf16 v[132:135], a[232:235], v[44:47], v[132:135]\n"
      "v_mfma_f32_16x16x32_bf16 v[132:135], a[236:239], v[60:63], v[132:135]\n"
      "v_cvt_pk_bf16_f32 v214, v136, v137\n"
      "v_mfma_f32_16x16x32_bf16 v[132:135], a[0:3], v[64:67], v[132:135]\n"
      "v_cvt_pk_bf16_f32 v215, v138, v139\n"
      "v_mfma_f32_16x16x32_bf16 v[132:135], a[4:7], v[68:71], v[132:135]\n"
      "v_mfma_f32_16x16x32_bf16 v[132:135], a[8:11], v[84:87], v[132:135]\n"
      "v_mfma_f32_16x16x32_bf16 v[132:135], a[12:15], v[88:91], v[132:135]\n"
      "v_mfma_f32_16x16x32_bf16 v[132:135], a[16:19], v[92:95], v[132:135]\n"
      "v_mfma_f32_16x16x32_bf16 v[132:135], a[20:23], v[108:111], v[132:135]\n"
      "ds_write_b64 v245, v[214:215] offset:12296\n"
      "v_mfma_f32_16x16x32_bf16 v[132:135], v[140:143], v[112:115], v[132:135]\n"
      "v_mfma_f32_16x16x32_bf16 v[132:135], v[156:159], v[116:119], v[132:135]\n"
      "s_waitcnt lgkmcnt(4)\n"
      "s_barrier\n"
      "v_mfma_f32_16x16x32_bf16 v[136:139], v[160:163], v[36:39], v[20:23]\n"
      "ds_read_b128 v[36:39], v232 offset:24832\n"
      "v_mfma_f32_16x16x32_bf16 v[136:139], v[164:167], v[40:43], v[136:139]\n"
      "ds_read_b128 v[40:43], v233 offset:24832\n"
      "v_mfma_f32_16x16x32_bf16 v[136:139], v[180:183], v[44:47], v[136:139]\n"
      "ds_read_b128 v[44:47], v234 offset:24832\n"
      "v_mfma_f32_16x16x32_bf16 v[136:139], v[184:187], v[60:63], v[136:139]\n"
      "ds_read_b128 v[60:63], v235 offset:24832\n"
      "v_cvt_pk_bf16_f32 v208, v132, v133\n"
      "v_mfma_f32_16x16x32_bf16 v[136:139], v[188:191], v[64:67], v[136:139]\n"
      "ds_read_b128 v[64:67], v232 offset:33024\n"
      "v_cvt_pk_bf16_f32 v209, v134, v135\n"
      "v_mfma_f32_16x16x32_bf16 v[136:139], v[204:207], v[68:71], v[136:139]\n"
      "ds_read_b128 v[68:71], v233 offset:33024\n"
      "v_mfma_f32_16x16x32_bf16 v[136:139], a[24:27], v[84:87], v[136:139]\n"
      "ds_read_b128 v[84:87], v234 offset:33024\n"
      "v_mfma_f32_16x16x32_bf16 v[136:139], a[28:31], v[88:91], v[136:139]\n"
      "ds_read_b128 v[88:91], v235 offset:33024\n"
      "v_mfma_f32_16x16x32_bf16 v[136:139], a[32:35], v[92:95], v[136:139]\n"
      "ds_read_b128 v[92:95], v232 offset:41216\n"
      "v_mfma_f32_16x16x32_bf16 v[136:139], a[36:39], v[108:111], v[136:139]\n"
      "ds_read_b128 v[108:111], v233 offset:41216\n"
      "ds_write_b64 v244, v[208:209] offset:12416\n"
      "v_mfma_f32_16x16x32_bf16 v[136:139], a[40:43], v[112:115], v[136:139]\n"
      "ds_read_b128 v[112:115], v234 offset:41216\n"
      "v_mfma_f32_16x16x32_bf16 v[136:139], a[44:47], v[116:119], v[136:139]\n"
      "ds_read_b128 v[116:119], v235 offset:41216\n"
      "s_waitcnt lgkmcnt(9)\n"
      "v_mfma_f32_16x16x32_bf16 v[132:135], a[240:243], v[36:39], v[0:3]\n"
      "v_add_u32 v232, 0xffff4000, v232\n"
      "v_mfma_f32_16x16x32_bf16 v[132:135], a[244:247], v[40:43], v[132:135]\n"
      "v_add_u32 v233, 0xffff4000, v233\n"
      "v_mfma_f32_16x16x32_bf16 v[132:135], a[248:251], v[44:47], v[132:135]\n"
      "v_add_u32 v234, 0xffff4000, v234\n"
      "v_mfma_f32_16x16x32_bf16 v[132:135], a[252:255], v[60:63], v[132:135]\n"
      "LPD9:\n"
      "s_cbranch_execz LPD12\n"
      "v_add_u32 v235, 0xffff4000, v235\n"
      "s_waitcnt lgkmcnt(5)\n"
      "v_mfma_f32_16x16x32_bf16 v[132:135], a[48:51], v[64:67], v[132:135]\n"
      "v_cvt_pk_bf16_f32 v210, v136, v137\n"
      "v_mfma_f32_16x16x32_bf16 v[132:135], a[52:55], v[68:71], v[132:135]\n"
      "v_cvt_pk_bf16_f32 v211, v138, v139\n"
      "v_mfma_f32_16x16x32_bf16 v[132:135], a[56:59], v[84:87], v[132:135]\n"
      "ds_read_b128 v[216:219], v246 offset:0\n"
      "v_mfma_f32_16x16x32_bf16 v[132:135], a[60:63], v[88:91], v[132:135]\n"
      "ds_read_b128 v[220:223], v246 offset:1024\n"
      "s_waitcnt lgkmcnt(2)\n"
      "v_mfma_f32_16x16x32_bf16 v[132:135], a[64:67], v[92:95], v[132:135]\n"
      "ds_read_b128 v[224:227], v246 offset:2048\n"
      "v_mfma_f32_16x16x32_bf16 v[132:135], a[68:71], v[108:111], v[132:135]\n"
      "v_lshlrev_b32 v0, 16, v48\n"
      "v_mfma_f32_16x16x32_bf16 v[132:135], a[72:75], v[112:115], v[132:135]\n"
      "ds_write_b64 v244, v[210:211] offset:12424\n"
      "v_mfma_f32_16x16x32_bf16 v[132:135], a[76:79], v[116:119], v[132:135]\n"
      "v_and_b32 v1, 0xffff0000, v48\n"
      "v_mfma_f32_16x16x32_bf16 v[136:139], a[80:83], v[36:39], v[4:7]\n"
      "v_lshlrev_b32 v2, 16, v49\n"
      "v_mfma_f32_16x16x32_bf16 v[136:139], a[84:87], v[40:43], v[136:139]\n"
      "v_and_b32 v3, 0xffff0000, v49\n"
      "v_mfma_f32_16x16x32_bf16 v[136:139], a[88:91], v[44:47], v[136:139]\n"
      "v_mfma_f32_16x16x32_bf16 v[136:139], a[92:95], v[60:63], v[136:139]\n"
      "v_cvt_pk_bf16_f32 v208, v132, v133\n"
      "v_mfma_f32_16x16x32_bf16 v[136:139], a[96:99], v[64:67], v[136:139]\n"
      "v_cvt_pk_bf16_f32 v209, v134, v135\n"
      "v_mfma_f32_16x16x32_bf16 v[136:139], a[100:103], v[68:71], v[136:139]\n"
      "s_waitcnt lgkmcnt(3)\n"
      "v_mfma_f32_16x16x32_bf16 v[136:139], a[104:107], v[84:87], v[136:139]\n"
      "global_store_dwordx4 v247, v[216:219], s[54:55] offset:768 sc1\n"
      "v_mfma_f32_16x16x32_bf16 v[136:139], a[108:111], v[88:91], v[136:139]\n"
      "s_add_u32 m0, s17, 0xd000\n"
      "v_mfma_f32_16x16x32_bf16 v[136:139], a[112:115], v[92:95], v[136:139]\n"
      "global_load_lds_dwordx4 v237, s[52:53] offset:0 nt\n"
      "v_mfma_f32_16x16x32_bf16 v[136:139], a[116:119], v[108:111], v[136:139]\n"
      "ds_write_b64 v244, v[208:209] offset:24576\n"
      "v_mfma_f32_16x16x32_bf16 v[136:139], a[120:123], v[112:115], v[136:139]\n"
      "v_lshlrev_b32 v4, 16, v50\n"
      "v_mfma_f32_16x16x32_bf16 v[136:139], a[124:127], v[116:119], v[136:139]\n"
      "v_and_b32 v5, 0xffff0000, v50\n"
      "v_mfma_f32_16x16x32_bf16 v[132:135], a[128:131], v[36:39], v[8:11]\n"
      "v_lshlrev_b32 v6, 16, v51\n"
      "v_mfma_f32_16x16x32_bf16 v[132:135], a[132:135], v[40:43], v[132:135]\n"
      "v_and_b32 v7, 0xffff0000, v51\n"
      "v_mfma_f32_16x16x32_bf16 v[132:135], a[136:139], v[44:47], v[132:135]\n"
      "v_mfma_f32_16x16x32_bf16 v[132:135], a[140:143], v[60:63], v[132:135]\n"
      "v_cvt_pk_bf16_f32 v210, v136, v137\n"
      "v_mfma_f32_16x16x32_bf16 v[132:135], a[144:147], v[64:67], v[132:135]\n"
      "v_cvt_pk_bf16_f32 v211, v138, v139\n"
      "v_mfma_f32_16x16x32_bf16 v[132:135], a[148:151], v[68:71], v[132:135]\n"
      "v_lshlrev_b32 v8, 16, v52\n"
      "v_mfma_f32_16x16x32_bf16 v[132:135], a[152:155], v[84:87], v[132:135]\n"
      "s_waitcnt lgkmcnt(3)\n"
      "v_mfma_f32_16x16x32_bf16 v[132:135], a[156:159], v[88:91], v[132:135]\n"
      "global_store_dwordx4 v252, v[220:223], s[54:55] offset:768 sc1\n"
      "v_mfma_f32_16x16x32_bf16 v[132:135], a[160:163], v[92:95], v[132:135]\n"
      "s_add_u32 m0, s17, 0xef00\n"
      "v_mfma_f32_16x16x32_bf16 v[132:135], a[164:167], v[108:111], v[132:135]\n"
      "ds_write_b64 v244, v[210:211] offset:24584\n"
      "v_mfma_f32_16x16x32_bf16 v[132:135], a[168:171], v[112:115], v[132:135]\n"
      "global_load_lds_dwordx4 v237, s[52:53] offset:256 nt\n"
      "v_mfma_f32_16x16x32_bf16 v[132:135], a[172:175], v[116:119], v[132:135]\n"
      "v_and_b32 v9, 0xffff0000, v52\n"
      "v_mfma_f32_16x16x32_bf16 v[136:139], a[176:179], v[36:39], v[12:15]\n"
      "v_lshlrev_b32 v10, 16, v53\n"
      "v_mfma_f32_16x16x32_bf16 v[136:139], a[180:183], v[40:43], v[136:139]\n"
      "v_and_b32 v11, 0xffff0000, v53\n"
      "v_mfma_f32_16x16x32_bf16 v[136:139], a[184:187], v[44:47], v[136:139]\n"
      "v_mfma_f32_16x16x32_bf16 v[136:139], a[188:191], v[60:63], v[136:139]\n"
      "v_cvt_pk_bf16_f32 v212, v132, v133\n"
      "v_mfma_f32_16x16x32_bf16 v[136:139], a[192:195], v[64:67], v[136:139]\n"
      "v_cvt_pk_bf16_f32 v213, v134, v135\n"
      "v_mfma_f32_16x16x32_bf16 v[136:139], a[196:199], v[68:71], v[136:139]\n"
      "v_lshlrev_b32 v12, 16, v54\n"
      "v_mfma_f32_16x16x32_bf16 v[136:139], a[200:203], v[84:87], v[136:139]\n"
      "v_and_b32 v13, 0xffff0000, v54\n"
      "v_mfma_f32_16x16x32_bf16 v[136:139], a[204:207], v[88:91], v[136:139]\n"
      "s_waitcnt lgkmcnt(3)\n"
      "v_mfma_f32_16x16x32_bf16 v[136:139], a[208:211], v[92:95], v[136:139]\n"
      "global_store_dwordx4 v253, v[224:227], s[54:55] offset:768 sc1\n"
      "v_mfma_f32_16x16x32_bf16 v[136:139], a[212:215], v[108:111], v[136:139]\n"
      "ds_write_b64 v245, v[212:213] offset:24576\n"
      "v_mfma_f32_16x16x32_bf16 v[136:139], a[216:219], v[112:115], v[136:139]\n"
      "s_add_u32 m0, s17, 0x10e00\n"
      "v_mfma_f32_16x16x32_bf16 v[136:139], a[220:223], v[116:119], v[136:139]\n"
      "global_load_lds_dwordx4 v237, s[52:53] offset:512 nt\n"
      "v_mfma_f32_16x16x32_bf16 v[132:135], a[224:227], v[36:39], v[16:19]\n"
      "v_lshlrev_b32 v14, 16, v55\n"
      "v_mfma_f32_16x16x32_bf16 v[132:135], a[228:231], v[40:43], v[132:135]\n"
      "v_and_b32 v15, 0xffff0000, v55\n"
      "v_mfma_f32_16x16x32_bf16 v[132:135], a[232:235], v[44:47], v[132:135]\n"
      "v_mfma_f32_16x16x32_bf16 v[132:135], a[236:239], v[60:63], v[132:135]\n"
      "v_cvt_pk_bf16_f32 v214, v136, v137\n"
      "v_mfma_f32_16x16x32_bf16 v[132:135], a[0:3], v[64:67], v[132:135]\n"
      "v_cvt_pk_bf16_f32 v215, v138, v139\n"
      "v_mfma_f32_16x16x32_bf16 v[132:135], a[4:7], v[68:71], v[132:135]\n"
      "v_lshlrev_b32 v16, 16, v56\n"
      "v_mfma_f32_16x16x32_bf16 v[132:135], a[8:11], v[84:87], v[132:135]\n"
      "v_and_b32 v17, 0xffff0000, v56\n"
      "v_mfma_f32_16x16x32_bf16 v[132:135], a[12:15], v[88:91], v[132:135]\n"
      "v_lshlrev_b32 v18, 16, v57\n"
      "v_mfma_f32_16x16x32_bf16 v[132:135], a[16:19], v[92:95], v[132:135]\n"
      "v_and_b32 v19, 0xffff0000, v57\n"
      "v_mfma_f32_16x16x32_bf16 v[132:135], a[20:23], v[108:111], v[132:135]\n"
      "ds_write_b64 v245, v[214:215] offset:24584\n"
      "v_mfma_f32_16x16x32_bf16 v[132:135], v[140:143], v[112:115], v[132:135]\n"
      "v_mfma_f32_16x16x32_bf16 v[132:135], v[156:159], v[116:119], v[132:135]\n"
      "s_waitcnt vmcnt(25) lgkmcnt(4)\n"
      "s_barrier\n"
      "v_mfma_f32_16x16x32_bf16 v[136:139], v[160:163], v[36:39], v[20:23]\n"
      "ds_read_b128 v[36:39], v232 offset:0\n"
      "v_mfma_f32_16x16x32_bf16 v[136:139], v[164:167], v[40:43], v[136:139]\n"
      "ds_read_b128 v[40:43], v233 offset:0\n"
      "v_mfma_f32_16x16x32_bf16 v[136:139], v[180:183], v[44:47], v[136:139]\n"
      "ds_read_b128 v[44:47], v234 offset:0\n"
      "v_mfma_f32_16x16x32_bf16 v[136:139], v[184:187], v[60:63], v[136:139]\n"
      "ds_read_b128 v[60:63], v235 offset:0\n"
      "v_cvt_pk_bf16_f32 v208, v132, v133\n"
      "v_mfma_f32_16x16x32_bf16 v[136:139], v[188:191], v[64:67], v[136:139]\n"
      "ds_read_b128 v[64:67], v232 offset:8192\n"
      "v_cvt_pk_bf16_f32 v209, v134, v135\n"
      "v_mfma_f32_16x16x32_bf16 v[136:139], v[204:207], v[68:71], v[136:139]\n"
      "ds_read_b128 v[68:71], v233 offset:8192\n"
      "v_mfma_f32_16x16x32_bf16 v[136:139], a[24:27], v[84:87], v[136:139]\n"
      "ds_read_b128 v[84:87], v234 offset:8192\n"
      "v_mfma_f32_16x16x32_bf16 v[136:139], a[28:31], v[88:91], v[136:139]\n"
      "ds_read_b128 v[88:91], v235 offset:8192\n"
      "v_mfma_f32_16x16x32_bf16 v[136:139], a[32:35], v[92:95], v[136:139]\n"
      "ds_read_b128 v[92:95], v232 offset:16384\n"
      "v_mfma_f32_16x16x32_bf16 v[136:139], a[36:39], v[108:111], v[136:139]\n"
      "ds_read_b128 v[108:111], v233 offset:16384\n"
      "v_mfma_f32_16x16x32_bf16 v[136:139], a[40:43], v[112:115], v[136:139]\n"
      "ds_read_b128 v[112:115], v234 offset:16384\n"
      "v_mfma_f32_16x16x32_bf16 v[136:139], a[44:47], v[116:119], v[136:139]\n"
      "ds_read_b128 v[116:119], v235 offset:16384\n"
      "s_waitcnt lgkmcnt(8)\n"
      "v_mfma_f32_16x16x32_bf16 v[132:135], a[240:243], v[36:39], v[0:3]\n"
      "ds_write_b64 v244, v[208:209] offset:24704\n"
      "v_mfma_f32_16x16x32_bf16 v[132:135], a[244:247], v[40:43], v[132:135]\n"
      "v_lshlrev_b32 v20, 16, v58\n"
      "v_mfma_f32_16x16x32_bf16 v[132:135], a[248:251], v[44:47], v[132:135]\n"
      "v_and_b32 v21, 0xffff0000, v58\n"
      "v_mfma_f32_16x16x32_bf16 v[132:135], a[252:255], v[60:63], v[132:135]\n"
      "v_lshlrev_b32 v22, 16, v59\n"
      "s_waitcnt lgkmcnt(5)\n"
      "v_mfma_f32_16x16x32_bf16 v[132:135], a[48:51], v[64:67], v[132:135]\n"
      "v_and_b32 v23, 0xffff0000, v59\n"
      "v_mfma_f32_16x16x32_bf16 v[132:135], a[52:55], v[68:71], v[132:135]\n"
      "v_cvt_pk_bf16_f32 v210, v136, v137\n"
      "v_mfma_f32_16x16x32_bf16 v[132:135], a[56:59], v[84:87], v[132:135]\n"
      "v_cvt_pk_bf16_f32 v211, v138, v139\n"
      "v_mfma_f32_16x16x32_bf16 v[132:135], a[60:63], v[88:91], v[132:135]\n"
      "s_add_u32 s54, s44, s33\n"
      "s_addc_u32 s55, s45, 0\n"
      "s_waitcnt lgkmcnt(1)\n"
      "v_mfma_f32_16x16x32_bf16 v[132:135], a[64:67], v[92:95], v[132:135]\n"
      "ds_read_b128 v[216:219], v246 offset:12288\n"
      "v_mfma_f32_16x16x32_bf16 v[132:135], a[68:71], v[108:111], v[132:135]\n"
      "ds_read_b128 v[220:223], v246 offset:13312\n"
      "v_mfma_f32_16x16x32_bf16 v[132:135], a[72:75], v[112:115], v[132:135]\n"
      "ds_read_b128 v[224:227], v246 offset:14336\n"
      "v_mfma_f32_16x16x32_bf16 v[132:135], a[76:79], v[116:119], v[132:135]\n"
      "ds_write_b64 v244, v[210:211] offset:24712\n"
      "v_mfma_f32_16x16x32_bf16 v[136:139], a[80:83], v[36:39], v[4:7]\n"
      "v_mfma_f32_16x16x32_bf16 v[136:139], a[84:87], v[40:43], v[136:139]\n"
      "v_mfma_f32_16x16x32_bf16 v[136:139], a[88:91], v[44:47], v[136:139]\n"
      "v_mfma_f32_16x16x32_bf16 v[136:139], a[92:95], v[60:63], v[136:139]\n"
      "v_cvt_pk_bf16_f32 v208, v132, v133\n"
      "v_mfma_f32_16x16x32_bf16 v[136:139], a[96:99], v[64:67], v[136:139]\n"
      "v_cvt_pk_bf16_f32 v209, v134, v135\n"
      "v_mfma_f32_16x16x32_bf16 v[136:139], a[100:103], v[68:71], v[136:139]\n"
      "s_waitcnt lgkmcnt(3)\n"
      "v_mfma_f32_16x16x32_bf16 v[136:139], a[104:107], v[84:87], v[136:139]\n"
      "global_store_dwordx4 v247, v[216:219], s[54:55] offset:0 sc1\n"
      "v_mfma_f32_16x16x32_bf16 v[136:139], a[108:111], v[88:91], v[136:139]\n"
      "s_add_u32 s52, s42, s35\n"
      "s_addc_u32 s53, s43, 0\n"
      "v_mfma_f32_16x16x32_bf16 v[136:139], a[112:115], v[92:95], v[136:139]\n"
      "s_add_u32 m0, s17, 0x12000\n"
      "v_mfma_f32_16x16x32_bf16 v[136:139], a[116:119], v[108:111], v[136:139]\n"
      "ds_write_b64 v244, v[208:209] offset:0\n"
      "v_mfma_f32_16x16x32_bf16 v[136:139], a[120:123], v[112:115], v[136:139]\n"
      "global_load_lds_dwordx4 v236, s[52:53] offset:0 nt\n"
      "v_mfma_f32_16x16x32_bf16 v[136:139], a[124:127], v[116:119], v[136:139]\n"
      "v_mfma_f32_16x16x32_bf16 v[132:135], a[128:131], v[36:39], v[8:11]\n"
      "v_mfma_f32_16x16x32_bf16 v[132:135], a[132:135], v[40:43], v[132:135]\n"
      "v_mfma_f32_16x16x32_bf16 v[132:135], a[136:139], v[44:47], v[132:135]\n"
      "v_mfma_f32_16x16x32_bf16 v[132:135], a[140:143], v[60:63], v[132:135]\n"
      "v_cvt_pk_bf16_f32 v210, v136, v137\n"
      "v_mfma_f32_16x16x32_bf16 v[132:135], a[144:147], v[64:67], v[132:135]\n"
      "v_cvt_pk_bf16_f32 v211, v138, v139\n"
      "v_mfma_f32_16x16x32_bf16 v[132:135], a[148:151], v[68:71], v[132:135]\n"
      "v_mfma_f32_16x16x32_bf16 v[132:135], a[152:155], v[84:87], v[132:135]\n"
      "s_waitcnt lgkmcnt(3)\n"
      "v_mfma_f32_16x16x32_bf16 v[132:135], a[156:159], v[88:91], v[132:135]\n"
      "global_store_dwordx4 v252, v[220:223], s[54:55] offset:0 sc1\n"
      "v_mfma_f32_16x16x32_bf16 v[132:135], a[160:163], v[92:95], v[132:135]\n"
      "s_add_u32 m0, s17, 0x13f00\n"
      "v_mfma_f32_16x16x32_bf16 v[132:135], a[164:167], v[108:111], v[132:135]\n"
      "ds_write_b64 v244, v[210:211] offset:8\n"
      "v_mfma_f32_16x16x32_bf16 v[132:135], a[168:171], v[112:115], v[132:135]\n"
      "global_load_lds_dwordx4 v236, s[52:53] offset:256 nt\n"
      "v_mfma_f32_16x16x32_bf16 v[132:135], a[172:175], v[116:119], v[132:135]\n"
      "v_mfma_f32_16x16x32_bf16 v[136:139], a[176:179], v[36:39], v[12:15]\n"
      "v_mfma_f32_16x16x32_bf16 v[136:139], a[180:183], v[40:43], v[136:139]\n"
      "v_mfma_f32_16x16x32_bf16 v[136:139], a[184:187], v[44:47], v[136:139]\n"
      "v_mfma_f32_16x16x32_bf16 v[136:139], a[188:191], v[60:63], v[136:139]\n"
      "v_cvt_pk_bf16_f32 v212, v132, v133\n"
      "v_mfma_f32_16x16x32_bf16 v[136:139], a[192:195], v[64:67], v[136:139]\n"
      "v_cvt_pk_bf16_f32 v213, v134, v135\n"
      "v_mfma_f32_16x16x32_bf16 v[136:139], a[196:199], v[68:71], v[136:139]\n"
      "v_mfma_f32_16x16x32_bf16 v[136:139], a[200:203], v[84:87], v[136:139]\n"
      "v_mfma_f32_16x16x32_bf16 v[136:139], a[204:207], v[88:91], v[136:139]\n"
      "s_waitcnt lgkmcnt(3)\n"
      "v_mfma_f32_16x16x32_bf16 v[136:139], a[208:211], v[92:95], v[136:139]\n"
      "global_store_dwordx4 v253, v[224:227], s[54:55] offset:0 sc1\n"
      "v_mfma_f32_16x16x32_bf16 v[136:139], a[212:215], v[108:111], v[136:139]\n"
      "ds_write_b64 v245, v[212:213] offset:0\n"
      "v_mfma_f32_16x16x32_bf16 v[136:139], a[216:219], v[112:115], v[136:139]\n"
      "s_add_u32 m0, s17, 0x15e00\n"
      "v_mfma_f32_16x16x32_bf16 v[136:139], a[220:223], v[116:119], v[136:139]\n"
      "global_load_lds_dwordx4 v236, s[52:53] offset:512 nt\n"
      "v_mfma_f32_16x16x32_bf16 v[132:135], a[224:227], v[36:39], v[16:19]\n"
      "v_mfma_f32_16x16x32_bf16 v[132:135], a[228:231], v[40:43], v[132:135]\n"
      "v_mfma_f32_16x16x32_bf16 v[132:135], a[232:235], v[44:47], v[132:135]\n"
      "v_mfma_f32_16x16x32_bf16 v[132:135], a[236:239], v[60:63], v[132:135]\n"
      "v_cvt_pk_bf16_f32 v214, v136, v137\n"
      "v_mfma_f32_16x16x32_bf16 v[132:135], a[0:3], v[64:67], v[132:135]\n"
      "v_cvt_pk_bf16_f32 v215, v138, v139\n"
      "v_mfma_f32_16x16x32_bf16 v[132:135], a[4:7], v[68:71], v[132:135]\n"
      "v_mfma_f32_16x16x32_bf16 v[132:135], a[8:11], v[84:87], v[132:135]\n"
      "v_mfma_f32_16x16x32_bf16 v[132:135], a[12:15], v[88:91], v[132:135]\n"
      "v_mfma_f32_16x16x32_bf16 v[132:135], a[16:19], v[92:95], v[132:135]\n"
      "v_mfma_f32_16x16x32_bf16 v[132:135], a[20:23], v[108:111], v[132:135]\n"
      "ds_write_b64 v245, v[214:215] offset:8\n"
      "v_mfma_f32_16x16x32_bf16 v[132:135], v[140:143], v[112:115], v[132:135]\n"
      "v_mfma_f32_16x16x32_bf16 v[132:135], v[156:159], v[116:119], v[132:135]\n"
      "s_waitcnt lgkmcnt(4)\n"
      "s_barrier\n"
      "v_mfma_f32_16x16x32_bf16 v[136:139], v[160:163], v[36:39], v[20:23]\n"
      "ds_read_b128 v[36:39], v232 offset:256\n"
      "v_mfma_f32_16x16x32_bf16 v[136:139], v[164:167], v[40:43], v[136:139]\n"
      "ds_read_b128 v[40:43], v233 offset:256\n"
      "v_mfma_f32_16x16x32_bf16 v[136:139], v[180:183], v[44:47], v[136:139]\n"
      "ds_read_b128 v[44:47], v234 offset:256\n"
      "v_mfma_f32_16x16x32_bf16 v[136:139], v[184:187], v[60:63], v[136:139]\n"
      "ds_read_b128 v[60:63], v235 offset:256\n"
      "v_cvt_pk_bf16_f32 v208, v132, v133\n"
      "v_mfma_f32_16x16x32_bf16 v[136:139], v[188:191], v[64:67], v[136:139]\n"
      "ds_read_b128 v[64:67], v232 offset:8448\n"
      "v_cvt_pk_bf16_f32 v209, v134, v135\n"
      "v_mfma_f32_16x16x32_bf16 v[136:139], v[204:207], v[68:71], v[136:139]\n"
      "ds_read_b128 v[68:71], v233 offset:8448\n"
      "v_mfma_f32_16x16x32_bf16 v[136:139], a[24:27], v[84:87], v[136:139]\n"
      "ds_read_b128 v[84:87], v234 offset:8448\n"
      "v_mfma_f32_16x16x32_bf16 v[136:139], a[28:31], v[88:91], v[136:139]\n"
      "ds_read_b128 v[88:91], v235 offset:8448\n"
      "v_mfma_f32_16x16x32_bf16 v[136:139], a[32:35], v[92:95], v[136:139]\n"
      "ds_read_b128 v[92:95], v232 offset:16640\n"
      "v_mfma_f32_16x16x32_bf16 v[136:139], a[36:39], v[108:111], v[136:139]\n"
      "ds_read_b128 v[108:111], v233 offset:16640\n"
      "v_mfma_f32_16x16x32_bf16 v[136:139], a[40:43], v[112:115], v[136:139]\n"
      "ds_read_b128 v[112:115], v234 offset:16640\n"
      "v_mfma_f32_16x16x32_bf16 v[136:139], a[44:47], v[116:119], v[136:139]\n"
      "ds_read_b128 v[116:119], v235 offset:16640\n"
      "s_waitcnt lgkmcnt(8)\n"
      "v_mfma_f32_16x16x32_bf16 v[132:135], a[240:243], v[36:39], v[0:3]\n"
      "ds_write_b64 v244, v[208:209] offset:128\n"
      "v_mfma_f32_16x16x32_bf16 v[132:135], a[244:247], v[40:43], v[132:135]\n"
      "v_mfma_f32_16x16x32_bf16 v[132:135], a[248:251], v[44:47], v[132:135]\n"
      "v_mfma_f32_16x16x32_bf16 v[132:135], a[252:255], v[60:63], v[132:135]\n"
      "v_cvt_pk_bf16_f32 v210, v136, v137\n"
      "s_waitcnt lgkmcnt(5)\n"
      "v_mfma_f32_16x16x32_bf16 v[132:135], a[48:51], v[64:67], v[132:135]\n"
      "v_cvt_pk_bf16_f32 v211, v138, v139\n"
      "v_mfma_f32_16x16x32_bf16 v[132:135], a[52:55], v[68:71], v[132:135]\n"
      "v_mfma_f32_16x16x32_bf16 v[132:135], a[56:59], v[84:87], v[132:135]\n"
      "ds_read_b128 v[216:219], v246 offset:24576\n"
      "v_mfma_f32_16x16x32_bf16 v[132:135], a[60:63], v[88:91], v[132:135]\n"
      "ds_read_b128 v[220:223], v246 offset:25600\n"
      "s_waitcnt lgkmcnt(3)\n"
      "v_mfma_f32_16x16x32_bf16 v[132:135], a[64:67], v[92:95], v[132:135]\n"
      "ds_read_b128 v[224:227], v246 offset:26624\n"
      "v_mfma_f32_16x16x32_bf16 v[132:135], a[68:71], v[108:111], v[132:135]\n"
      "ds_write_b64 v244, v[210:211] offset:136\n"
      "v_mfma_f32_16x16x32_bf16 v[132:135], a[72:75], v[112:115], v[132:135]\n"
      "v_mfma_f32_16x16x32_bf16 v[132:135], a[76:79], v[116:119], v[132:135]\n"
      "v_mfma_f32_16x16x32_bf16 v[136:139], a[80:83], v[36:39], v[4:7]\n"
      "v_mfma_f32_16x16x32_bf16 v[136:139], a[84:87], v[40:43], v[136:139]\n"
      "v_mfma_f32_16x16x32_bf16 v[136:139], a[88:91], v[44:47], v[136:139]\n"
      "v_mfma_f32_16x16x32_bf16 v[136:139], a[92:95], v[60:63], v[136:139]\n"
      "v_cvt_pk_bf16_f32 v208, v132, v133\n"
      "v_mfma_f32_16x16x32_bf16 v[136:139], a[96:99], v[64:67], v[136:139]\n"
      "v_cvt_pk_bf16_f32 v209, v134, v135\n"
      "v_mfma_f32_16x16x32_bf16 v[136:139], a[100:103], v[68:71], v[136:139]\n"
      "s_waitcnt lgkmcnt(3)\n"
      "v_mfma_f32_16x16x32_bf16 v[136:139], a[104:107], v[84:87], v[136:139]\n"
      "global_store_dwordx4 v247, v[216:219], s[54:55] offset:768 sc1\n"
      "v_mfma_f32_16x16x32_bf16 v[136:139], a[108:111], v[88:91], v[136:139]\n"
      "s_add_u32 m0, s17, 0x13000\n"
      "v_mfma_f32_16x16x32_bf16 v[136:139], a[112:115], v[92:95], v[136:139]\n"
      "global_load_lds_dwordx4 v237, s[52:53] offset:0 nt\n"
      "v_mfma_f32_16x16x32_bf16 v[136:139], a[116:119], v[108:111], v[136:139]\n"
      "ds_write_b64 v244, v[208:209] offset:12288\n"
      "v_mfma_f32_16x16x32_bf16 v[136:139], a[120:123], v[112:115], v[136:139]\n"
      "v_mfma_f32_16x16x32_bf16 v[136:139], a[124:127], v[116:119], v[136:139]\n"
      "v_mfma_f32_16x16x32_bf16 v[132:135], a[128:131], v[36:39], v[8:11]\n"
      "v_mfma_f32_16x16x32_bf16 v[132:135], a[132:135], v[40:43], v[132:135]\n"
      "v_mfma_f32_16x16x32_bf16 v[132:135], a[136:139], v[44:47], v[132:135]\n"
      "v_mfma_f32_16x16x32_bf16 v[132:135], a[140:143], v[60:63], v[132:135]\n"
      "v_cvt_pk_bf16_f32 v210, v136, v137\n"
      "v_mfma_f32_16x16x32_bf16 v[132:135], a[144:147], v[64:67], v[132:135]\n"
      "v_cvt_pk_bf16_f32 v211, v138, v139\n"
      "v_mfma_f32_16x16x32_bf16 v[132:135], a[148:151], v[68:71], v[132:135]\n"
      "v_mfma_f32_16x16x32_bf16 v[132:135], a[152:155], v[84:87], v[132:135]\n"
      "s_waitcnt lgkmcnt(3)\n"
      "s_waitcnt vmcnt(18)\n"
      "s_mov_b32 s87, 1\n"
      "s_or_b32 s56, s51, s76\n"
      "s_cmp_lg_u32 s56, 0\n"
      "s_cbranch_scc1 LG1\n"
      "v_cmp_ne_u64 s[58:59], s[80:81], v[24:25]\n"
      "v_cmp_ne_u64 s[56:57], s[80:81], v[26:27]\n"
      "s_mov_b32 s87, 0\n"
      "s_nop 3\n"
      "s_or_b64 s[56:57], s[56:57], s[58:59]\n"
      "s_cbranch_scc1 LG1\n"
      "v_mov_b32 v28, 0\n"
      "v_mov_b32 v30, s80\n"
      "v_mov_b32 v31, s81\n"
      "s_mov_b32 s87, 1\n"
      "s_mov_b64 exec, 1\n"
      "global_store_dwordx2 v28, v[30:31], s[28:29] sc1\n"
      "s_mov_b64 exec, -1\n"
      "s_nop 4\n"
      "LG1:\n"
      "v_mfma_f32_16x16x32_bf16 v[132:135], a[156:159], v[88:91], v[132:135]\n"
      "global_store_dwordx4 v252, v[220:223], s[54:55] offset:768 sc1\n"
      "v_mfma_f32_16x16x32_bf16 v[132:135], a[160:163], v[92:95], v[132:135]\n"
      "s_add_u32 m0, s17, 0x14f00\n"
      "v_mfma_f32_16x16x32_bf16 v[132:135], a[164:167], v[108:111], v[132:135]\n"
      "ds_write_b64 v244, v[210:211] offset:12296\n"
      "v_mfma_f32_16x16x32_bf16 v[132:135], a[168:171], v[112:115], v[132:135]\n"
      "global_load_lds_dwordx4 v237, s[52:53] offset:256 nt\n"
      "v_mfma_f32_16x16x32_bf16 v[132:135], a[172:175], v[116:119], v[132:135]\n"
      "v_mfma_f32_16x16x32_bf16 v[136:139], a[176:179], v[36:39], v[12:15]\n"
      "v_mfma_f32_16x16x32_bf16 v[136:139], a[180:183], v[40:43], v[136:139]\n"
      "v_mfma_f32_16x16x32_bf16 v[136:139], a[184:187], v[44:47], v[136:139]\n"
      "v_mfma_f32_16x16x32_bf16 v[136:139], a[188:191], v[60:63], v[136:139]\n"
      "v_cvt_pk_bf16_f32 v212, v132, v133\n"
      "v_mfma_f32_16x16x32_bf16 v[136:139], a[192:195], v[64:67], v[136:139]\n"
      "v_cvt_pk_bf16_f32 v213, v134, v135\n"
      "v_mfma_f32_16x16x32_bf16 v[136:139], a[196:199], v[68:71], v[136:139]\n"
      "v_mfma_f32_16x16x32_bf16 v[136:139], a[200:203], v[84:87], v[136:139]\n"
      "v_mfma_f32_16x16x32_bf16 v[136:139], a[204:207], v[88:91], v[136:139]\n"
      "s_waitcnt lgkmcnt(3)\n"
      "v_mfma_f32_16x16x32_bf16 v[136:139], a[208:211], v[92:95], v[136:139]\n"
      "global_store_dwordx4 v253, v[224:227], s[54:55] offset:768 sc1\n"
      "v_mfma_f32_16x16x32_bf16 v[136:139], a[212:215], v[108:111], v[136:139]\n"
      "ds_write_b64 v245, v[212:213] offset:12288\n"
      "v_mfma_f32_16x16x32_bf16 v[136:139], a[216:219], v[112:115], v[136:139]\n"
      "s_add_u32 m0, s17, 0x16e00\n"
      "v_mfma_f32_16x16x32_bf16 v[136:139], a[220:223], v[116:119], v[136:139]\n"
      "global_load_lds_dwordx4 v237, s[52:53] offset:512 nt\n"
      "v_mfma_f32_16x16x32_bf16 v[132:135], a[224:227], v[36:39], v[16:19]\n"
      "v_mfma_f32_16x16x32_bf16 v[132:135], a[228:231], v[40:43], v[132:135]\n"
      "v_mfma_f32_16x16x32_bf16 v[132:135], a[232:235], v[44:47], v[132:135]\n"
      "v_mfma_f32_16x16x32_bf16 v[132:135], a[236:239], v[60:63], v[132:135]\n"
      "s_memrealtime s[84:85]\n"
      "v_mfma_f32_16x16x32_bf16 v[132:135], a[0:3], v[64:67], v[132:135]\n"
      "v_cvt_pk_bf16_f32 v214, v136, v137\n"
      "v_mfma_f32_16x16x32_bf16 v[132:135], a[4:7], v[68:71], v[132:135]\n"
      "v_cvt_pk_bf16_f32 v215, v138, v139\n"
      "v_mfma_f32_16x16x32_bf16 v[132:135], a[8:11], v[84:87], v[132:135]\n"
      "v_mfma_f32_16x16x32_bf16 v[132:135], a[12:15], v[88:91], v[132:135]\n"
      "v_mfma_f32_16x16x32_bf16 v[132:135], a[16:19], v[92:95], v[132:135]\n"
      "v_mfma_f32_16x16x32_bf16 v[132:135], a[20:23], v[108:111], v[132:135]\n"
      "v_mfma_f32_16x16x32_bf16 v[132:135], v[140:143], v[112:115], v[132:135]\n"
      "s_waitcnt lgkmcnt(0)\n"
      "s_sub_u32 s48, s84, s48\n"
      "v_mfma_f32_16x16x32_bf16 v[132:135], v[156:159], v[116:119], v[132:135]\n"
      "s_barrier\n"
      "ds_write_b64 v245, v[214:215] offset:12296\n"
      "v_mfma_f32_16x16x32_bf16 v[136:139], v[160:163], v[36:39], v[20:23]\n"
      "ds_read_b128 v[36:39], v232 offset:24576\n"
      "s_lshl_b32 s79, s91, 1\n"
      "v_mfma_f32_16x16x32_bf16 v[136:139], v[164:167], v[40:43], v[136:139]\n"
      "ds_read_b128 v[40:43], v233 offset:24576\n"
      "v_mad_i32_i24 v252, v251, s79, v252\n"
      "v_mfma_f32_16x16x32_bf16 v[136:139], v[180:183], v[44:47], v[136:139]\n"
      "ds_read_b128 v[44:47], v234 offset:24576\n"
      "s_mul_i32 s79, s79, s100\n"
      "v_mfma_f32_16x16x32_bf16 v[136:139], v[184:187], v[60:63], v[136:139]\n"
      "ds_read_b128 v[60:63], v235 offset:24576\n"
      "v_cvt_pk_bf16_f32 v208, v132, v133\n"
      "v_mfma_f32_16x16x32_bf16 v[136:139], v[188:191], v[64:67], v[136:139]\n"
      "ds_read_b128 v[64:67], v232 offset:32768\n"
      "v_cvt_pk_bf16_f32 v209, v134, v135\n"
      "v_mfma_f32_16x16x32_bf16 v[136:139], v[204:207], v[68:71], v[136:139]\n"
      "ds_read_b128 v[68:71], v233 offset:32768\n"
      "v_add_u32 v247, s79, v247\n"
      "v_mfma_f32_16x16x32_bf16 v[136:139], a[24:27], v[84:87], v[136:139]\n"
      "ds_read_b128 v[84:87], v234 offset:32768\n"
      "s_mul_i32 s79, s79, s101\n"
      "v_mfma_f32_16x16x32_bf16 v[136:139], a[28:31], v[88:91], v[136:139]\n"
      "ds_read_b128 v[88:91], v235 offset:32768\n"
      "v_add_u32 v253, s79, v253\n"
      "v_mfma_f32_16x16x32_bf16 v[136:139], a[32:35], v[92:95], v[136:139]\n"
      "ds_read_b128 v[92:95], v232 offset:40960\n"
      "v_mfma_f32_16x16x32_bf16 v[136:139], a[36:39], v[108:111], v[136:139]\n"
      "ds_read_b128 v[108:111], v233 offset:40960\n"
      "v_mfma_f32_16x16x32_bf16 v[136:139], a[40:43], v[112:115], v[136:139]\n"
      "ds_read_b128 v[112:115], v234 offset:40960\n"
      "v_mfma_f32_16x16x32_bf16 v[136:139], a[44:47], v[116:119], v[136:139]\n"
      "ds_read_b128 v[116:119], v235 offset:40960\n"
      "s_waitcnt lgkmcnt(8)\n"
      "v_mfma_f32_16x16x32_bf16 v[132:135], a[240:243], v[36:39], v[0:3]\n"
      "ds_write_b64 v244, v[208:209] offset:12416\n"
      "v_mfma_f32_16x16x32_bf16 v[132:135], a[244:247], v[40:43], v[132:135]\n"
      "s_add_u32 s54, s30, s34\n"
      "s_addc_u32 s55, s31, 0\n"
      "v_mfma_f32_16x16x32_bf16 v[132:135], a[248:251], v[44:47], v[132:135]\n"
      "s_mul_i32 s48, s48, 939\n"
      "v_mfma_f32_16x16x32_bf16 v[132:135], a[252:255], v[60:63], v[132:135]\n"
      "s_lshr_b32 s48, s48, 8\n"
      "v_cvt_pk_bf16_f32 v210, v136, v137\n"
      "s_waitcnt lgkmcnt(5)\n"
      "v_mfma_f32_16x16x32_bf16 v[132:135], a[48:51], v[64:67], v[132:135]\n"
      "s_add_u32 s48, s48, s84\n"
      "v_cvt_pk_bf16_f32 v211, v138, v139\n"
      "v_mfma_f32_16x16x32_bf16 v[132:135], a[52:55], v[68:71], v[132:135]\n"
      "s_xor_b32 s49, s48, s80\n"
      "s_mul_i32 s78, s93, s19\n"
      "v_mfma_f32_16x16x32_bf16 v[132:135], a[56:59], v[84:87], v[132:135]\n"
      "v_mov_b32 v230, s49\n"
      "v_add_u32 v236, s78, v236\n"
      "v_mfma_f32_16x16x32_bf16 v[132:135], a[60:63], v[88:91], v[132:135]\n"
      "s_mul_i32 s49, s51, 0x1100\n"
      "s_mul_i32 s78, s78, s99\n"
      "s_waitcnt lgkmcnt(1)\n"
      "v_mfma_f32_16x16x32_bf16 v[132:135], a[64:67], v[92:95], v[132:135]\n"
      "s_and_b32 s49, s49, 0xf000\n"
      "v_add_u32 v237, s78, v237\n"
      "v_mfma_f32_16x16x32_bf16 v[132:135], a[68:71], v[108:111], v[132:135]\n"
      "s_lshr_b32 s49, s49, 0\n"
      "ds_write_b64 v244, v[210:211] offset:12424\n"
      "v_mfma_f32_16x16x32_bf16 v[132:135], a[72:75], v[112:115], v[132:135]\n"
      "s_add_u32 s49, s49, 1114112\n"
      "v_mfma_f32_16x16x32_bf16 v[132:135], a[76:79], v[116:119], v[132:135]\n"
      "v_mov_b32 v229, s49\n"
      "v_mfma_f32_16x16x32_bf16 v[136:139], a[80:83], v[36:39], v[4:7]\n"
      "s_and_b32 s49, s51, 0x78\n"
      "v_mfma_f32_16x16x32_bf16 v[136:139], a[84:87], v[40:43], v[136:139]\n"
      "s_xor_b32 s49, s49, 0x40\n"
      "v_mfma_f32_16x16x32_bf16 v[136:139], a[88:91], v[44:47], v[136:139]\n"
      "s_or_b32 s49, s49, s76\n"
      "v_mfma_f32_16x16x32_bf16 v[136:139], a[92:95], v[60:63], v[136:139]\n"
      "v_cvt_pk_bf16_f32 v208, v132, v133\n"
      "v_mfma_f32_16x16x32_bf16 v[136:139], a[96:99], v[64:67], v[136:139]\n"
      "s_cmp_lg_u32 s49, 0\n"
      "s_cbranch_scc0 LPS\n"
      "LPR:\n"
      "v_mfma_f32_16x16x32_bf16 v[136:139], a[100:103], v[68:71], v[136:139]\n"
      "v_cvt_pk_bf16_f32 v209, v134, v135\n"
      "v_mfma_f32_16x16x32_bf16 v[136:139], a[104:107], v[84:87], v[136:139]\n"
      "ds_read_b128 v[216:219], v246 offset:0\n"
      "v_mfma_f32_16x16x32_bf16 v[136:139], a[108:111], v[88:91], v[136:139]\n"
      "ds_read_b128 v[220:223], v246 offset:1024\n"
      "v_mfma_f32_16x16x32_bf16 v[136:139], a[112:115], v[92:95], v[136:139]\n"
      "ds_read_b128 v[224:227], v246 offset:2048\n"
      "v_mfma_f32_16x16x32_bf16 v[136:139], a[116:119], v[108:111], v[136:139]\n"
      "s_waitcnt lgkmcnt(2)\n"
      "v_mfma_f32_16x16x32_bf16 v[136:139], a[120:123], v[112:115], v[136:139]\n"
      "ds_write_b64 v244, v[208:209] offset:24576\n"
      "v_mfma_f32_16x16x32_bf16 v[136:139], a[124:127], v[116:119], v[136:139]\n"
      "global_store_dwordx4 v247, v[216:219], s[54:55] offset:0 sc1\n"
      "v_mfma_f32_16x16x32_bf16 v[132:135], a[128:131], v[36:39], v[8:11]\n"
      "s_add_u32 s52, s20, s36\n"
      "s_addc_u32 s53, s21, 0\n"
      "v_mfma_f32_16x16x32_bf16 v[132:135], a[132:135], v[40:43], v[132:135]\n"
      "s_add_u32 m0, s17, 0x0\n"
      "v_mfma_f32_16x16x32_bf16 v[132:135], a[136:139], v[44:47], v[132:135]\n"
      "global_load_lds_dwordx4 v236, s[52:53] offset:0 nt\n"
      "v_mfma_f32_16x16x32_bf16 v[132:135], a[140:143], v[60:63], v[132:135]\n"
      "v_cvt_pk_bf16_f32 v210, v136, v137\n"
      "v_mfma_f32_16x16x32_bf16 v[132:135], a[144:147], v[64:67], v[132:135]\n"
      "v_cvt_pk_bf16_f32 v211, v138, v139\n"
      "v_mfma_f32_16x16x32_bf16 v[132:135], a[148:151], v[68:71], v[132:135]\n"
      "v_mfma_f32_16x16x32_bf16 v[132:135], a[152:155], v[84:87], v[132:135]\n"
      "s_waitcnt lgkmcnt(2)\n"
      "v_mfma_f32_16x16x32_bf16 v[132:135], a[156:159], v[88:91], v[132:135]\n"
      "global_store_dwordx4 v252, v[220:223], s[54:55] offset:0 sc1\n"
      "v_mfma_f32_16x16x32_bf16 v[132:135], a[160:163], v[92:95], v[132:135]\n"
      "v_mfma_f32_16x16x32_bf16 v[132:135], a[164:167], v[108:111], v[132:135]\n"
      "ds_write_b64 v244, v[210:211] offset:24584\n"
      "v_mfma_f32_16x16x32_bf16 v[132:135], a[168:171], v[112:115], v[132:135]\n"
      "v_mfma_f32_16x16x32_bf16 v[132:135], a[172:175], v[116:119], v[132:135]\n"
      "v_mfma_f32_16x16x32_bf16 v[136:139], a[176:179], v[36:39], v[12:15]\n"
      "s_add_u32 m0, s17, 0x1f00\n"
      "v_mfma_f32_16x16x32_bf16 v[136:139], a[180:183], v[40:43], v[136:139]\n"
      "global_load_lds_dwordx4 v236, s[52:53] offset:256 nt\n"
      "v_mfma_f32_16x16x32_bf16 v[136:139], a[184:187], v[44:47], v[136:139]\n"
      "v_mfma_f32_16x16x32_bf16 v[136:139], a[188:191], v[60:63], v[136:139]\n"
      "v_cvt_pk_bf16_f32 v212, v132, v133\n"
      "v_mfma_f32_16x16x32_bf16 v[136:139], a[192:195], v[64:67], v[136:139]\n"
      "v_cvt_pk_bf16_f32 v213, v134, v135\n"
      "v_mfma_f32_16x16x32_bf16 v[136:139], a[196:199], v[68:71], v[136:139]\n"
      "v_mfma_f32_16x16x32_bf16 v[136:139], a[200:203], v[84:87], v[136:139]\n"
      "v_mfma_f32_16x16x32_bf16 v[136:139], a[204:207], v[88:91], v[136:139]\n"
      "s_waitcnt lgkmcnt(2)\n"
      "v_mfma_f32_16x16x32_bf16 v[136:139], a[208:211], v[92:95], v[136:139]\n"
      "global_store_dwordx4 v253, v[224:227], s[54:55] offset:0 sc1\n"
      "v_mfma_f32_16x16x32_bf16 v[136:139], a[212:215], v[108:111], v[136:139]\n"
      "ds_write_b64 v245, v[212:213] offset:24576\n"
      "v_mfma_f32_16x16x32_bf16 v[136:139], a[216:219], v[112:115], v[136:139]\n"
      "v_mfma_f32_16x16x32_bf16 v[136:139], a[220:223], v[116:119], v[136:139]\n"
      "s_add_u32 m0, s17, 0x3e00\n"
      "v_mfma_f32_16x16x32_bf16 v[132:135], a[224:227], v[36:39], v[16:19]\n"
      "global_load_lds_dwordx4 v236, s[52:53] offset:512 nt\n"
      "v_mfma_f32_16x16x32_bf16 v[132:135], a[228:231], v[40:43], v[132:135]\n"
      "v_mfma_f32_16x16x32_bf16 v[132:135], a[232:235], v[44:47], v[132:135]\n"
      "v_mfma_f32_16x16x32_bf16 v[132:135], a[236:239], v[60:63], v[132:135]\n"
      "v_cvt_pk_bf16_f32 v214, v136, v137\n"
      "v_mfma_f32_16x16x32_bf16 v[132:135], a[0:3], v[64:67], v[132:135]\n"
      "v_cvt_pk_bf16_f32 v215, v138, v139\n"
      "v_mfma_f32_16x16x32_bf16 v[132:135], a[4:7], v[68:71], v[132:135]\n"
      "v_mfma_f32_16x16x32_bf16 v[132:135], a[8:11], v[84:87], v[132:135]\n"
      "v_mfma_f32_16x16x32_bf16 v[132:135], a[12:15], v[88:91], v[132:135]\n"
      "v_mfma_f32_16x16x32_bf16 v[132:135], a[16:19], v[92:95], v[132:135]\n"
      "v_mfma_f32_16x16x32_bf16 v[132:135], a[20:23], v[108:111], v[132:135]\n"
      "ds_write_b64 v245, v[214:215] offset:24584\n"
      "v_mfma_f32_16x16x32_bf16 v[132:135], v[140:143], v[112:115], v[132:135]\n"
      "v_mfma_f32_16x16x32_bf16 v[132:135], v[156:159], v[116:119], v[132:135]\n"
      "s_barrier\n"
      "v_mfma_f32_16x16x32_bf16 v[136:139], v[160:163], v[36:39], v[20:23]\n"
      "ds_read_b128 v[36:39], v232 offset:24832\n"
      "v_mfma_f32_16x16x32_bf16 v[136:139], v[164:167], v[40:43], v[136:139]\n"
      "ds_read_b128 v[40:43], v233 offset:24832\n"
      "v_mfma_f32_16x16x32_bf16 v[136:139], v[180:183], v[44:47], v[136:139]\n"
      "ds_read_b128 v[44:47], v234 offset:24832\n"
      "v_mfma_f32_16x16x32_bf16 v[136:139], v[184:187], v[60:63], v[136:139]\n"
      "ds_read_b128 v[60:63], v235 offset:24832\n"
      "v_cvt_pk_bf16_f32 v208, v132, v133\n"
      "v_mfma_f32_16x16x32_bf16 v[136:139], v[188:191], v[64:67], v[136:139]\n"
      "ds_read_b128 v[64:67], v232 offset:33024\n"
      "v_cvt_pk_bf16_f32 v209, v134, v135\n"
      "v_mfma_f32_16x16x32_bf16 v[136:139], v[204:207], v[68:71], v[136:139]\n"
      "ds_read_b128 v[68:71], v233 offset:33024\n"
      "v_mfma_f32_16x16x32_bf16 v[136:139], a[24:27], v[84:87], v[136:139]\n"
      "ds_read_b128 v[84:87], v234 offset:33024\n"
      "v_mfma_f32_16x16x32_bf16 v[136:139], a[28:31], v[88:91], v[136:139]\n"
      "ds_read_b128 v[88:91], v235 offset:33024\n"
      "v_mfma_f32_16x16x32_bf16 v[136:139], a[32:35], v[92:95], v[136:139]\n"
      "ds_read_b128 v[92:95], v232 offset:41216\n"
      "v_mfma_f32_16x16x32_bf16 v[136:139], a[36:39], v[108:111], v[136:139]\n"
      "LPD10:\n"
      "s_cbranch_execz LPD13\n"
      "ds_read_b128 v[108:111], v233 offset:41216\n"
      "ds_write_b64 v244, v[208:209] offset:24704\n"
      "v_mfma_f32_16x16x32_bf16 v[136:139], a[40:43], v[112:115], v[136:139]\n"
      "ds_read_b128 v[112:115], v234 offset:41216\n"
      "v_mfma_f32_16x16x32_bf16 v[136:139], a[44:47], v[116:119], v[136:139]\n"
      "ds_read_b128 v[116:119], v235 offset:41216\n"
      "s_waitcnt lgkmcnt(9)\n"
      "v_mfma_f32_16x16x32_bf16 v[132:135], a[240:243], v[36:39], v[0:3]\n"
      "v_add_u32 v232, 0xc000, v232\n"
      "v_mfma_f32_16x16x32_bf16 v[132:135], a[244:247], v[40:43], v[132:135]\n"
      "v_add_u32 v233, 0xc000, v233\n"
      "v_mfma_f32_16x16x32_bf16 v[132:135], a[248:251], v[44:47], v[132:135]\n"
      "v_add_u32 v234, 0xc000, v234\n"
      "v_mfma_f32_16x16x32_bf16 v[132:135], a[252:255], v[60:63], v[132:135]\n"
      "v_add_u32 v235, 0xc000, v235\n"
      "s_waitcnt lgkmcnt(5)\n"
      "v_mfma_f32_16x16x32_bf16 v[132:135], a[48:51], v[64:67], v[132:135]\n"
      "v_cvt_pk_bf16_f32 v210, v136, v137\n"
      "v_mfma_f32_16x16x32_bf16 v[132:135], a[52:55], v[68:71], v[132:135]\n"
      "v_cvt_pk_bf16_f32 v211, v138, v139\n"
      "v_mfma_f32_16x16x32_bf16 v[132:135], a[56:59], v[84:87], v[132:135]\n"
      "ds_read_b128 v[216:219], v246 offset:12288\n"
      "v_mfma_f32_16x16x32_bf16 v[132:135], a[60:63], v[88:91], v[132:135]\n"
      "ds_read_b128 v[220:223], v246 offset:13312\n"
      "s_waitcnt lgkmcnt(2)\n"
      "v_mfma_f32_16x16x32_bf16 v[132:135], a[64:67], v[92:95], v[132:135]\n"
      "ds_read_b128 v[224:227], v246 offset:14336\n"
      "v_mfma_f32_16x16x32_bf16 v[132:135], a[68:71], v[108:111], v[132:135]\n"
      "v_lshlrev_b32 v0, 16, v72\n"
      "v_mfma_f32_16x16x32_bf16 v[132:135], a[72:75], v[112:115], v[132:135]\n"
      "ds_write_b64 v244, v[210:211] offset:24712\n"
      "v_mfma_f32_16x16x32_bf16 v[132:135], a[76:79], v[116:119], v[132:135]\n"
      "v_and_b32 v1, 0xffff0000, v72\n"
      "v_mfma_f32_16x16x32_bf16 v[136:139], a[80:83], v[36:39], v[4:7]\n"
      "v_lshlrev_b32 v2, 16, v73\n"
      "v_mfma_f32_16x16x32_bf16 v[136:139], a[84:87], v[40:43], v[136:139]\n"
      "v_and_b32 v3, 0xffff0000, v73\n"
      "v_mfma_f32_16x16x32_bf16 v[136:139], a[88:91], v[44:47], v[136:139]\n"
      "v_mfma_f32_16x16x32_bf16 v[136:139], a[92:95], v[60:63], v[136:139]\n"
      "v_cvt_pk_bf16_f32 v208, v132, v133\n"
      "v_mfma_f32_16x16x32_bf16 v[136:139], a[96:99], v[64:67], v[136:139]\n"
      "v_cvt_pk_bf16_f32 v209, v134, v135\n"
      "v_mfma_f32_16x16x32_bf16 v[136:139], a[100:103], v[68:71], v[136:139]\n"
      "s_waitcnt lgkmcnt(3)\n"
      "v_mfma_f32_16x16x32_bf16 v[136:139], a[104:107], v[84:87], v[136:139]\n"
      "global_store_dwordx4 v247, v[216:219], s[54:55] offset:768 sc1\n"
      "v_mfma_f32_16x16x32_bf16 v[136:139], a[108:111], v[88:91], v[136:139]\n"
      "s_add_u32 m0, s17, 0x1000\n"
      "v_mfma_f32_16x16x32_bf16 v[136:139], a[112:115], v[92:95], v[136:139]\n"
      "global_load_lds_dwordx4 v237, s[52:53] offset:0 nt\n"
      "v_mfma_f32_16x16x32_bf16 v[136:139], a[116:119], v[108:111], v[136:139]\n"
      "ds_write_b64 v244, v[208:209] offset:0\n"
      "v_mfma_f32_16x16x32_bf16 v[136:139], a[120:123], v[112:115], v[136:139]\n"
      "v_lshlrev_b32 v4, 16, v74\n"
      "v_mfma_f32_16x16x32_bf16 v[136:139], a[124:127], v[116:119], v[136:139]\n"
      "v_and_b32 v5, 0xffff0000, v74\n"
      "v_mfma_f32_16x16x32_bf16 v[132:135], a[128:131], v[36:39], v[8:11]\n"
      "v_lshlrev_b32 v6, 16, v75\n"
      "v_mfma_f32_16x16x32_bf16 v[132:135], a[132:135], v[40:43], v[132:135]\n"
      "v_and_b32 v7, 0xffff0000, v75\n"
      "v_mfma_f32_16x16x32_bf16 v[132:135], a[136:139], v[44:47], v[132:135]\n"
      "v_mfma_f32_16x16x32_bf16 v[132:135], a[140:143], v[60:63], v[132:135]\n"
      "v_cvt_pk_bf16_f32 v210, v136, v137\n"
      "v_mfma_f32_16x16x32_bf16 v[132:135], a[144:147], v[64:67], v[132:135]\n"
      "v_cvt_pk_bf16_f32 v211, v138, v139\n"
      "v_mfma_f32_16x16x32_bf16 v[132:135], a[148:151], v[68:71], v[132:135]\n"
      "v_lshlrev_b32 v8, 16, v76\n"
      "v_mfma_f32_16x16x32_bf16 v[132:135], a[152:155], v[84:87], v[132:135]\n"
      "s_waitcnt lgkmcnt(3)\n"
      "v_mfma_f32_16x16x32_bf16 v[132:135], a[156:159], v[88:91], v[132:135]\n"
      "global_store_dwordx4 v252, v[220:223], s[54:55] offset:768 sc1\n"
      "v_mfma_f32_16x16x32_bf16 v[132:135], a[160:163], v[92:95], v[132:135]\n"
      "s_add_u32 m0, s17, 0x2f00\n"
      "v_mfma_f32_16x16x32_bf16 v[132:135], a[164:167], v[108:111], v[132:135]\n"
      "ds_write_b64 v244, v[210:211] offset:8\n"
      "v_mfma_f32_16x16x32_bf16 v[132:135], a[168:171], v[112:115], v[132:135]\n"
      "global_load_lds_dwordx4 v237, s[52:53] offset:256 nt\n"
      "v_mfma_f32_16x16x32_bf16 v[132:135], a[172:175], v[116:119], v[132:135]\n"
      "v_and_b32 v9, 0xffff0000, v76\n"
      "v_mfma_f32_16x16x32_bf16 v[136:139], a[176:179], v[36:39], v[12:15]\n"
      "v_lshlrev_b32 v10, 16, v77\n"
      "v_mfma_f32_16x16x32_bf16 v[136:139], a[180:183], v[40:43], v[136:139]\n"
      "v_and_b32 v11, 0xffff0000, v77\n"
      "v_mfma_f32_16x16x32_bf16 v[136:139], a[184:187], v[44:47], v[136:139]\n"
      "v_mfma_f32_16x16x32_bf16 v[136:139], a[188:191], v[60:63], v[136:139]\n"
      "v_cvt_pk_bf16_f32 v212, v132, v133\n"
      "v_mfma_f32_16x16x32_bf16 v[136:139], a[192:195], v[64:67], v[136:139]\n"
      "v_cvt_pk_bf16_f32 v213, v134, v135\n"
      "v_mfma_f32_16x16x32_bf16 v[136:139], a[196:199], v[68:71], v[136:139]\n"
      "v_lshlrev_b32 v12, 16, v78\n"
      "v_mfma_f32_16x16x32_bf16 v[136:139], a[200:203], v[84:87], v[136:139]\n"
      "v_and_b32 v13, 0xffff0000, v78\n"
      "v_mfma_f32_16x16x32_bf16 v[136:139], a[204:207], v[88:91], v[136:139]\n"
      "s_waitcnt lgkmcnt(3)\n"
      "v_mfma_f32_16x16x32_bf16 v[136:139], a[208:211], v[92:95], v[136:139]\n"
      "global_store_dwordx4 v253, v[224:227], s[54:55] offset:768 sc1\n"
      "v_mfma_f32_16x16x32_bf16 v[136:139], a[212:215], v[108:111], v[136:139]\n"
      "ds_write_b64 v245, v[212:213] offset:0\n"
      "v_mfma_f32_16x16x32_bf16 v[136:139], a[216:219], v[112:115], v[136:139]\n"
      "s_add_u32 m0, s17, 0x4e00\n"
      "v_mfma_f32_16x16x32_bf16 v[136:139], a[220:223], v[116:119], v[136:139]\n"
      "global_load_lds_dwordx4 v237, s[52:53] offset:512 nt\n"
      "v_mfma_f32_16x16x32_bf16 v[132:135], a[224:227], v[36:39], v[16:19]\n"
      "v_lshlrev_b32 v14, 16, v79\n"
      "v_mfma_f32_16x16x32_bf16 v[132:135], a[228:231], v[40:43], v[132:135]\n"
      "v_and_b32 v15, 0xffff0000, v79\n"
      "v_mfma_f32_16x16x32_bf16 v[132:135], a[232:235], v[44:47], v[132:135]\n"
      "v_mfma_f32_16x16x32_bf16 v[132:135], a[236:239], v[60:63], v[132:135]\n"
      "v_cvt_pk_bf16_f32 v214, v136, v137\n"
      "v_mfma_f32_16x16x32_bf16 v[132:135], a[0:3], v[64:67], v[132:135]\n"
      "v_cvt_pk_bf16_f32 v215, v138, v139\n"
      "v_mfma_f32_16x16x32_bf16 v[132:135], a[4:7], v[68:71], v[132:135]\n"
      "v_lshlrev_b32 v16, 16, v80\n"
      "v_mfma_f32_16x16x32_bf16 v[132:135], a[8:11], v[84:87], v[132:135]\n"
      "v_and_b32 v17, 0xffff0000, v80\n"
      "v_mfma_f32_16x16x32_bf16 v[132:135], a[12:15], v[88:91], v[132:135]\n"
      "v_lshlrev_b32 v18, 16, v81\n"
      "v_mfma_f32_16x16x32_bf16 v[132:135], a[16:19], v[92:95], v[132:135]\n"
      "v_and_b32 v19, 0xffff0000, v81\n"
      "v_mfma_f32_16x16x32_bf16 v[132:135], a[20:23], v[108:111], v[132:135]\n"
      "ds_write_b64 v245, v[214:215] offset:8\n"
      "v_mfma_f32_16x16x32_bf16 v[132:135], v[140:143], v[112:115], v[132:135]\n"
      "v_lshlrev_b32 v229, 8, v239\n"
      "v_mfma_f32_16x16x32_bf16 v[132:135], v[156:159], v[116:119], v[132:135]\n"
      "s_waitcnt vmcnt(24) lgkmcnt(4)\n"
      "s_barrier\n"
      "v_add_u32 v229, 1114112, v229\n"
      "v_mfma_f32_16x16x32_bf16 v[136:139], v[160:163], v[36:39], v[20:23]\n"
      "ds_read_b128 v[36:39], v232 offset:0\n"
      "v_mfma_f32_16x16x32_bf16 v[136:139], v[164:167], v[40:43], v[136:139]\n"
      "ds_read_b128 v[40:43], v233 offset:0\n"
      "s_mov_b64 exec, 0xffff\n"
      "global_load_dword v228, v229, s[28:29] sc1\n"
      "s_mov_b64 exec, -1\n"
      "s_nop 1\n"
      "v_mfma_f32_16x16x32_bf16 v[136:139], v[180:183], v[44:47], v[136:139]\n"
      "ds_read_b128 v[44:47], v234 offset:0\n"
      "v_mfma_f32_16x16x32_bf16 v[136:139], v[184:187], v[60:63], v[136:139]\n"
      "ds_read_b128 v[60:63], v235 offset:0\n"
      "v_cvt_pk_bf16_f32 v208, v132, v133\n"
      "v_mfma_f32_16x16x32_bf16 v[136:139], v[188:191], v[64:67], v[136:139]\n"
      "ds_read_b128 v[64:67], v232 offset:8192\n"
      "v_cvt_pk_bf16_f32 v209, v134, v135\n"
      "v_mfma_f32_16x16x32_bf16 v[136:139], v[204:207], v[68:71], v[136:139]\n"
      "ds_read_b128 v[68:71], v233 offset:8192\n"
      "v_mfma_f32_16x16x32_bf16 v[136:139], a[24:27], v[84:87], v[136:139]\n"
      "ds_read_b128 v[84:87], v234 offset:8192\n"
      "v_mfma_f32_16x16x32_bf16 v[136:139], a[28:31], v[88:91], v[136:139]\n"
      "ds_read_b128 v[88:91], v235 offset:8192\n"
      "v_mfma_f32_16x16x32_bf16 v[136:139], a[32:35], v[92:95], v[136:139]\n"
      "ds_read_b128 v[92:95], v232 offset:16384\n"
      "v_mfma_f32_16x16x32_bf16 v[136:139], a[36:39], v[108:111], v[136:139]\n"
      "ds_read_b128 v[108:111], v233 offset:16384\n"
      "v_mfma_f32_16x16x32_bf16 v[136:139], a[40:43], v[112:115], v[136:139]\n"
      "ds_read_b128 v[112:115], v234 offset:16384\n"
      "v_mfma_f32_16x16x32_bf16 v[136:139], a[44:47], v[116:119], v[136:139]\n"
      "ds_read_b128 v[116:119], v235 offset:16384\n"
      "s_waitcnt lgkmcnt(8)\n"
      "v_mfma_f32_16x16x32_bf16 v[132:135], a[240:243], v[36:39], v[0:3]\n"
      "ds_write_b64 v244, v[208:209] offset:128\n"
      "v_mfma_f32_16x16x32_bf16 v[132:135], a[244:247], v[40:43], v[132:135]\n"
      "v_lshlrev_b32 v20, 16, v82\n"
      "v_mfma_f32_16x16x32_bf16 v[132:135], a[248:251], v[44:47], v[132:135]\n"
      "v_and_b32 v21, 0xffff0000, v82\n"
      "v_mfma_f32_16x16x32_bf16 v[132:135], a[252:255], v[60:63], v[132:135]\n"
      "v_lshlrev_b32 v22, 16, v83\n"
      "s_waitcnt lgkmcnt(5)\n"
      "v_mfma_f32_16x16x32_bf16 v[132:135], a[48:51], v[64:67], v[132:135]\n"
      "v_and_b32 v23, 0xffff0000, v83\n"
      "v_mfma_f32_16x16x32_bf16 v[132:135], a[52:55], v[68:71], v[132:135]\n"
      "v_cvt_pk_bf16_f32 v210, v136, v137\n"
      "v_mfma_f32_16x16x32_bf16 v[132:135], a[56:59], v[84:87], v[132:135]\n"
      "v_cvt_pk_bf16_f32 v211, v138, v139\n"
      "v_mfma_f32_16x16x32_bf16 v[132:135], a[60:63], v[88:91], v[132:135]\n"
      "s_add_u32 s54, s44, s34\n"
      "s_addc_u32 s55, s45, 0\n"
      "s_waitcnt lgkmcnt(1)\n"
      "v_mfma_f32_16x16x32_bf16 v[132:135], a[64:67], v[92:95], v[132:135]\n"
      "ds_read_b128 v[216:219], v246 offset:24576\n"
      "v_mfma_f32_16x16x32_bf16 v[132:135], a[68:71], v[108:111], v[132:135]\n"
      "ds_read_b128 v[220:223], v246 offset:25600\n"
      "v_mfma_f32_16x16x32_bf16 v[132:135], a[72:75], v[112:115], v[132:135]\n"
      "ds_read_b128 v[224:227], v246 offset:26624\n"
      "v_mfma_f32_16x16x32_bf16 v[132:135], a[76:79], v[116:119], v[132:135]\n"
      "ds_write_b64 v244, v[210:211] offset:136\n"
      "v_mfma_f32_16x16x32_bf16 v[136:139], a[80:83], v[36:39], v[4:7]\n"
      "v_mfma_f32_16x16x32_bf16 v[136:139], a[84:87], v[40:43], v[136:139]\n"
      "v_mfma_f32_16x16x32_bf16 v[136:139], a[88:91], v[44:47], v[136:139]\n"
      "v_mfma_f32_16x16x32_bf16 v[136:139], a[92:95], v[60:63], v[136:139]\n"
      "v_cvt_pk_bf16_f32 v208, v132, v133\n"
      "v_mfma_f32_16x16x32_bf16 v[136:139], a[96:99], v[64:67], v[136:139]\n"
      "v_cvt_pk_bf16_f32 v209, v134, v135\n"
      "v_mfma_f32_16x16x32_bf16 v[136:139], a[100:103], v[68:71], v[136:139]\n"
      "s_waitcnt lgkmcnt(3)\n"
      "v_mfma_f32_16x16x32_bf16 v[136:139], a[104:107], v[84:87], v[136:139]\n"
      "global_store_dwordx4 v247, v[216:219], s[54:55] offset:0 sc1\n"
      "v_mfma_f32_16x16x32_bf16 v[136:139], a[108:111], v[88:91], v[136:139]\n"
      "s_add_u32 s52, s42, s36\n"
      "s_addc_u32 s53, s43, 0\n"
      "v_mfma_f32_16x16x32_bf16 v[136:139], a[112:115], v[92:95], v[136:139]\n"
      "s_add_u32 m0, s17, 0x6000\n"
      "v_mfma_f32_16x16x32_bf16 v[136:139], a[116:119], v[108:111], v[136:139]\n"
      "ds_write_b64 v244, v[208:209] offset:12288\n"
      "v_mfma_f32_16x16x32_bf16 v[136:139], a[120:123], v[112:115], v[136:139]\n"
      "global_load_lds_dwordx4 v236, s[52:53] offset:0 nt\n"
      "v_mfma_f32_16x16x32_bf16 v[136:139], a[124:127], v[116:119], v[136:139]\n"
      "v_mfma_f32_16x16x32_bf16 v[132:135], a[128:131], v[36:39], v[8:11]\n"
      "v_mfma_f32_16x16x32_bf16 v[132:135], a[132:135], v[40:43], v[132:135]\n"
      "v_mfma_f32_16x16x32_bf16 v[132:135], a[136:139], v[44:47], v[132:135]\n"
      "v_mfma_f32_16x16x32_bf16 v[132:135], a[140:143], v[60:63], v[132:135]\n"
      "v_cvt_pk_bf16_f32 v210, v136, v137\n"
      "v_mfma_f32_16x16x32_bf16 v[132:135], a[144:147], v[64:67], v[132:135]\n"
      "v_cvt_pk_bf16_f32 v211, v138, v139\n"
      "v_mfma_f32_16x16x32_bf16 v[132:135], a[148:151], v[68:71], v[132:135]\n"
      "v_mfma_f32_16x16x32_bf16 v[132:135], a[152:155], v[84:87], v[132:135]\n"
      "s_waitcnt lgkmcnt(3)\n"
      "v_mfma_f32_16x16x32_bf16 v[132:135], a[156:159], v[88:91], v[132:135]\n"
      "global_store_dwordx4 v252, v[220:223], s[54:55] offset:0 sc1\n"
      "v_mfma_f32_16x16x32_bf16 v[132:135], a[160:163], v[92:95], v[132:135]\n"
      "s_add_u32 m0, s17, 0x7f00\n"
      "v_mfma_f32_16x16x32_bf16 v[132:135], a[164:167], v[108:111], v[132:135]\n"
      "ds_write_b64 v244, v[210:211] offset:12296\n"
      "v_mfma_f32_16x16x32_bf16 v[132:135], a[168:171], v[112:115], v[132:135]\n"
      "global_load_lds_dwordx4 v236, s[52:53] offset:256 nt\n"
      "v_mfma_f32_16x16x32_bf16 v[132:135], a[172:175], v[116:119], v[132:135]\n"
      "v_mfma_f32_16x16x32_bf16 v[136:139], a[176:179], v[36:39], v[12:15]\n"
      "v_mfma_f32_16x16x32_bf16 v[136:139], a[180:183], v[40:43], v[136:139]\n"
      "v_mfma_f32_16x16x32_bf16 v[136:139], a[184:187], v[44:47], v[136:139]\n"
      "v_mfma_f32_16x16x32_bf16 v[136:139], a[188:191], v[60:63], v[136:139]\n"
      "v_cvt_pk_bf16_f32 v212, v132, v133\n"
      "v_mfma_f32_16x16x32_bf16 v[136:139], a[192:195], v[64:67], v[136:139]\n"
      "v_cvt_pk_bf16_f32 v213, v134, v135\n"
      "v_mfma_f32_16x16x32_bf16 v[136:139], a[196:199], v[68:71], v[136:139]\n"
      "v_mfma_f32_16x16x32_bf16 v[136:139], a[200:203], v[84:87], v[136:139]\n"
      "v_mfma_f32_16x16x32_bf16 v[136:139], a[204:207], v[88:91], v[136:139]\n"
      "s_waitcnt lgkmcnt(3)\n"
      "v_mfma_f32_16x16x32_bf16 v[136:139], a[208:211], v[92:95], v[136:139]\n"
      "global_store_dwordx4 v253, v[224:227], s[54:55] offset:0 sc1\n"
      "v_mfma_f32_16x16x32_bf16 v[136:139], a[212:215], v[108:111], v[136:139]\n"
      "ds_write_b64 v245, v[212:213] offset:12288\n"
      "v_mfma_f32_16x16x32_bf16 v[136:139], a[216:219], v[112:115], v[136:139]\n"
      "s_add_u32 m0, s17, 0x9e00\n"
      "v_mfma_f32_16x16x32_bf16 v[136:139], a[220:223], v[116:119], v[136:139]\n"
      "global_load_lds_dwordx4 v236, s[52:53] offset:512 nt\n"
      "v_mfma_f32_16x16x32_bf16 v[132:135], a[224:227], v[36:39], v[16:19]\n"
      "v_mfma_f32_16x16x32_bf16 v[132:135], a[228:231], v[40:43], v[132:135]\n"
      "v_mfma_f32_16x16x32_bf16 v[132:135], a[232:235], v[44:47], v[132:135]\n"
      "v_mfma_f32_16x16x32_bf16 v[132:135], a[236:239], v[60:63], v[132:135]\n"
      "v_cvt_pk_bf16_f32 v214, v136, v137\n"
      "v_mfma_f32_16x16x32_bf16 v[132:135], a[0:3], v[64:67], v[132:135]\n"
      "v_cvt_pk_bf16_f32 v215, v138, v139\n"
      "v_mfma_f32_16x16x32_bf16 v[132:135], a[4:7], v[68:71], v[132:135]\n"
      "v_mfma_f32_16x16x32_bf16 v[132:135], a[8:11], v[84:87], v[132:135]\n"
      "v_mfma_f32_16x16x32_bf16 v[132:135], a[12:15], v[88:91], v[132:135]\n"
      "v_mfma_f32_16x16x32_bf16 v[132:135], a[16:19], v[92:95], v[132:135]\n"
      "v_mfma_f32_16x16x32_bf16 v[132:135], a[20:23], v[108:111], v[132:135]\n"
      "ds_write_b64 v245, v[214:215] offset:12296\n"
      "v_mfma_f32_16x16x32_bf16 v[132:135], v[140:143], v[112:115], v[132:135]\n"
      "v_mfma_f32_16x16x32_bf16 v[132:135], v[156:159], v[116:119], v[132:135]\n"
      "s_waitcnt lgkmcnt(4)\n"
      "s_barrier\n"
      "v_mfma_f32_16x16x32_bf16 v[136:139], v[160:163], v[36:39], v[20:23]\n"
      "ds_read_b128 v[36:39], v232 offset:256\n"
      "v_mfma_f32_16x16x32_bf16 v[136:139], v[164:167], v[40:43], v[136:139]\n"
      "ds_read_b128 v[40:43], v233 offset:256\n"
      "v_mfma_f32_16x16x32_bf16 v[136:139], v[180:183], v[44:47], v[136:139]\n"
      "ds_read_b128 v[44:47], v234 offset:256\n"
      "v_mfma_f32_16x16x32_bf16 v[136:139], v[184:187], v[60:63], v[136:139]\n"
      "ds_read_b128 v[60:63], v235 offset:256\n"
      "v_cvt_pk_bf16_f32 v208, v132, v133\n"
      "v_mfma_f32_16x16x32_bf16 v[136:139], v[188:191], v[64:67], v[136:139]\n"
      "ds_read_b128 v[64:67], v232 offset:8448\n"
      "v_cvt_pk_bf16_f32 v209, v134, v135\n"
      "v_mfma_f32_16x16x32_bf16 v[136:139], v[204:207], v[68:71], v[136:139]\n"
      "ds_read_b128 v[68:71], v233 offset:8448\n"
      "v_mfma_f32_16x16x32_bf16 v[136:139], a[24:27], v[84:87], v[136:139]\n"
      "ds_read_b128 v[84:87], v234 offset:8448\n"
      "v_mfma_f32_16x16x32_bf16 v[136:139], a[28:31], v[88:91], v[136:139]\n"
      "ds_read_b128 v[88:91], v235 offset:8448\n"
      "v_mfma_f32_16x16x32_bf16 v[136:139], a[32:35], v[92:95], v[136:139]\n"
      "ds_read_b128 v[92:95], v232 offset:16640\n"
      "v_mfma_f32_16x16x32_bf16 v[136:139], a[36:39], v[108:111], v[136:139]\n"
      "ds_read_b128 v[108:111], v233 offset:16640\n"
      "v_mfma_f32_16x16x32_bf16 v[136:139], a[40:43], v[112:115], v[136:139]\n"
      "ds_read_b128 v[112:115], v234 offset:16640\n"
      "v_mfma_f32_16x16x32_bf16 v[136:139], a[44:47], v[116:119], v[136:139]\n"
      "ds_read_b128 v[116:119], v235 offset:16640\n"
      "s_waitcnt lgkmcnt(8)\n"
      "v_mfma_f32_16x16x32_bf16 v[132:135], a[240:243], v[36:39], v[0:3]\n"
      "ds_write_b64 v244, v[208:209] offset:12416\n"
      "v_mfma_f32_16x16x32_bf16 v[132:135], a[244:247], v[40:43], v[132:135]\n"
      "v_mfma_f32_16x16x32_bf16 v[132:135], a[248:251], v[44:47], v[132:135]\n"
      "v_mfma_f32_16x16x32_bf16 v[132:135], a[252:255], v[60:63], v[132:135]\n"
      "v_cvt_pk_bf16_f32 v210, v136, v137\n"
      "s_waitcnt lgkmcnt(5)\n"
      "v_mfma_f32_16x16x32_bf16 v[132:135], a[48:51], v[64:67], v[132:135]\n"
      "v_cvt_pk_bf16_f32 v211, v138, v139\n"
      "v_mfma_f32_16x16x32_bf16 v[132:135], a[52:55], v[68:71], v[132:135]\n"
      "v_mfma_f32_16x16x32_bf16 v[132:135], a[56:59], v[84:87], v[132:135]\n"
      "ds_read_b128 v[216:219], v246 offset:0\n"
      "v_mfma_f32_16x16x32_bf16 v[132:135], a[60:63], v[88:91], v[132:135]\n"
      "ds_read_b128 v[220:223], v246 offset:1024\n"
      "s_waitcnt lgkmcnt(3)\n"
      "v_mfma_f32_16x16x32_bf16 v[132:135], a[64:67], v[92:95], v[132:135]\n"
      "ds_read_b128 v[224:227], v246 offset:2048\n"
      "v_mfma_f32_16x16x32_bf16 v[132:135], a[68:71], v[108:111], v[132:135]\n"
      "ds_write_b64 v244, v[210:211] offset:12424\n"
      "v_mfma_f32_16x16x32_bf16 v[132:135], a[72:75], v[112:115], v[132:135]\n"
      "v_mfma_f32_16x16x32_bf16 v[132:135], a[76:79], v[116:119], v[132:135]\n"
      "v_mfma_f32_16x16x32_bf16 v[136:139], a[80:83], v[36:39], v[4:7]\n"
      "v_mfma_f32_16x16x32_bf16 v[136:139], a[84:87], v[40:43], v[136:139]\n"
      "v_mfma_f32_16x16x32_bf16 v[136:139], a[88:91], v[44:47], v[136:139]\n"
      "v_mfma_f32_16x16x32_bf16 v[136:139], a[92:95], v[60:63], v[136:139]\n"
      "v_cvt_pk_bf16_f32 v208, v132, v133\n"
      "v_mfma_f32_16x16x32_bf16 v[136:139], a[96:99], v[64:67], v[136:139]\n"
      "v_cvt_pk_bf16_f32 v209, v134, v135\n"
      "v_mfma_f32_16x16x32_bf16 v[136:139], a[100:103], v[68:71], v[136:139]\n"
      "s_waitcnt lgkmcnt(3)\n"
      "v_mfma_f32_16x16x32_bf16 v[136:139], a[104:107], v[84:87], v[136:139]\n"
      "global_store_dwordx4 v247, v[216:219], s[54:55] offset:768 sc1\n"
      "v_mfma_f32_16x16x32_bf16 v[136:139], a[108:111], v[88:91], v[136:139]\n"
      "s_add_u32 m0, s17, 0x7000\n"
      "v_mfma_f32_16x16x32_bf16 v[136:139], a[112:115], v[92:95], v[136:139]\n"
      "global_load_lds_dwordx4 v237, s[52:53] offset:0 nt\n"
      "v_mfma_f32_16x16x32_bf16 v[136:139], a[116:119], v[108:111], v[136:139]\n"
      "ds_write_b64 v244, v[208:209] offset:24576\n"
      "v_mfma_f32_16x16x32_bf16 v[136:139], a[120:123], v[112:115], v[136:139]\n"
      "v_mfma_f32_16x16x32_bf16 v[136:139], a[124:127], v[116:119], v[136:139]\n"
      "v_mfma_f32_16x16x32_bf16 v[132:135], a[128:131], v[36:39], v[8:11]\n"
      "v_mfma_f32_16x16x32_bf16 v[132:135], a[132:135], v[40:43], v[132:135]\n"
      "v_mfma_f32_16x16x32_bf16 v[132:135], a[136:139], v[44:47], v[132:135]\n"
      "v_mfma_f32_16x16x32_bf16 v[132:135], a[140:143], v[60:63], v[132:135]\n"
      "v_cvt_pk_bf16_f32 v210, v136, v137\n"
      "v_mfma_f32_16x16x32_bf16 v[132:135], a[144:147], v[64:67], v[132:135]\n"
      "v_cvt_pk_bf16_f32 v211, v138, v139\n"
      "v_mfma_f32_16x16x32_bf16 v[132:135], a[148:151], v[68:71], v[132:135]\n"
      "v_mfma_f32_16x16x32_bf16 v[132:135], a[152:155], v[84:87], v[132:135]\n"
      "s_waitcnt lgkmcnt(3)\n"
      "v_mfma_f32_16x16x32_bf16 v[132:135], a[156:159], v[88:91], v[132:135]\n"
      "global_store_dwordx4 v252, v[220:223], s[54:55] offset:768 sc1\n"
      "v_mfma_f32_16x16x32_bf16 v[132:135], a[160:163], v[92:95], v[132:135]\n"
      "s_add_u32 m0, s17, 0x8f00\n"
      "v_mfma_f32_16x16x32_bf16 v[132:135], a[164:167], v[108:111], v[132:135]\n"
      "ds_write_b64 v244, v[210:211] offset:24584\n"
      "v_mfma_f32_16x16x32_bf16 v[132:135], a[168:171], v[112:115], v[132:135]\n"
      "global_load_lds_dwordx4 v237, s[52:53] offset:256 nt\n"
      "v_mfma_f32_16x16x32_bf16 v[132:135], a[172:175], v[116:119], v[132:135]\n"
      "v_mfma_f32_16x16x32_bf16 v[136:139], a[176:179], v[36:39], v[12:15]\n"
      "v_mfma_f32_16x16x32_bf16 v[136:139], a[180:183], v[40:43], v[136:139]\n"
      "v_mfma_f32_16x16x32_bf16 v[136:139], a[184:187], v[44:47], v[136:139]\n"
      "v_mfma_f32_16x16x32_bf16 v[136:139], a[188:191], v[60:63], v[136:139]\n"
      "v_cvt_pk_bf16_f32 v212, v132, v133\n"
      "v_mfma_f32_16x16x32_bf16 v[136:139], a[192:195], v[64:67], v[136:139]\n"
      "v_cvt_pk_bf16_f32 v213, v134, v135\n"
      "v_mfma_f32_16x16x32_bf16 v[136:139], a[196:199], v[68:71], v[136:139]\n"
      "v_mfma_f32_16x16x32_bf16 v[136:139], a[200:203], v[84:87], v[136:139]\n"
      "v_mfma_f32_16x16x32_bf16 v[136:139], a[204:207], v[88:91], v[136:139]\n"
      "s_waitcnt lgkmcnt(3)\n"
      "v_mfma_f32_16x16x32_bf16 v[136:139], a[208:211], v[92:95], v[136:139]\n"
      "global_store_dwordx4 v253, v[224:227], s[54:55] offset:768 sc1\n"
      "v_mfma_f32_16x16x32_bf16 v[136:139], a[212:215], v[108:111], v[136:139]\n"
      "ds_write_b64 v245, v[212:213] offset:24576\n"
      "v_mfma_f32_16x16x32_bf16 v[136:139], a[216:219], v[112:115], v[136:139]\n"
      "s_add_u32 m0, s17, 0xae00\n"
      "v_mfma_f32_16x16x32_bf16 v[136:139], a[220:223], v[116:119], v[136:139]\n"
      "global_load_lds_dwordx4 v237, s[52:53] offset:512 nt\n"
      "v_mfma_f32_16x16x32_bf16 v[132:135], a[224:227], v[36:39], v[16:19]\n"
      "v_mfma_f32_16x16x32_bf16 v[132:135], a[228:231], v[40:43], v[132:135]\n"
      "v_mfma_f32_16x16x32_bf16 v[132:135], a[232:235], v[44:47], v[132:135]\n"
      "v_mfma_f32_16x16x32_bf16 v[132:135], a[236:239], v[60:63], v[132:135]\n"
      "v_cvt_pk_bf16_f32 v214, v136, v137\n"
      "v_mfma_f32_16x16x32_bf16 v[132:135], a[0:3], v[64:67], v[132:135]\n"
      "v_cvt_pk_bf16_f32 v215, v138, v139\n"
      "v_mfma_f32_16x16x32_bf16 v[132:135], a[4:7], v[68:71], v[132:135]\n"
      "v_mfma_f32_16x16x32_bf16 v[132:135], a[8:11], v[84:87], v[132:135]\n"
      "v_mfma_f32_16x16x32_bf16 v[132:135], a[12:15], v[88:91], v[132:135]\n"
      "v_mfma_f32_16x16x32_bf16 v[132:135], a[16:19], v[92:95], v[132:135]\n"
      "v_mfma_f32_16x16x32_bf16 v[132:135], a[20:23], v[108:111], v[132:135]\n"
      "ds_write_b64 v245, v[214:215] offset:24584\n"
      "v_mfma_f32_16x16x32_bf16 v[132:135], v[140:143], v[112:115], v[132:135]\n"
      "s_lshl_b32 s79, s92, 1\n"
      "v_mfma_f32_16x16x32_bf16 v[132:135], v[156:159], v[116:119], v[132:135]\n"
      "s_waitcnt vmcnt(25) lgkmcnt(4)\n"
      "s_barrier\n"
      "v_mad_i32_i24 v252, v251, s79, v252\n"
      "v_mfma_f32_16x16x32_bf16 v[136:139], v[160:163], v[36:39], v[20:23]\n"
      "ds_read_b128 v[36:39], v232 offset:24576\n"
      "s_mul_i32 s79, s79, s100\n"
      "v_mfma_f32_16x16x32_bf16 v[136:139], v[164:167], v[40:43], v[136:139]\n"
      "ds_read_b128 v[40:43], v233 offset:24576\n"
      "v_add_u32 v247, s79, v247\n"
      "v_mfma_f32_16x16x32_bf16 v[136:139], v[180:183], v[44:47], v[136:139]\n"
      "ds_read_b128 v[44:47], v234 offset:24576\n"
      "s_mul_i32 s79, s79, s101\n"
      "v_mfma_f32_16x16x32_bf16 v[136:139], v[184:187], v[60:63], v[136:139]\n"
      "ds_read_b128 v[60:63], v235 offset:24576\n"
      "v_cvt_pk_bf16_f32 v208, v132, v133\n"
      "v_mfma_f32_16x16x32_bf16 v[136:139], v[188:191], v[64:67], v[136:139]\n"
      "ds_read_b128 v[64:67], v232 offset:32768\n"
      "v_cvt_pk_bf16_f32 v209, v134, v135\n"
      "v_mfma_f32_16x16x32_bf16 v[136:139], v[204:207], v[68:71], v[136:139]\n"
      "ds_read_b128 v[68:71], v233 offset:32768\n"
      "v_add_u32 v253, s79, v253\n"
      "v_mfma_f32_16x16x32_bf16 v[136:139], a[24:27], v[84:87], v[136:139]\n"
      "ds_read_b128 v[84:87], v234 offset:32768\n"
      "v_mfma_f32_16x16x32_bf16 v[136:139], a[28:31], v[88:91], v[136:139]\n"
      "ds_read_b128 v[88:91], v235 offset:32768\n"
      "v_mfma_f32_16x16x32_bf16 v[136:139], a[32:35], v[92:95], v[136:139]\n"
      "ds_read_b128 v[92:95], v232 offset:40960\n"
      "v_mfma_f32_16x16x32_bf16 v[136:139], a[36:39], v[108:111], v[136:139]\n"
      "ds_read_b128 v[108:111], v233 offset:40960\n"
      "v_mfma_f32_16x16x32_bf16 v[136:139], a[40:43], v[112:115], v[136:139]\n"
      "ds_read_b128 v[112:115], v234 offset:40960\n"
      "v_mfma_f32_16x16x32_bf16 v[136:139], a[44:47], v[116:119], v[136:139]\n"
      "ds_read_b128 v[116:119], v235 offset:40960\n"
      "s_waitcnt lgkmcnt(8)\n"
      "v_mfma_f32_16x16x32_bf16 v[132:135], a[240:243], v[36:39], v[0:3]\n"
      "ds_write_b64 v244, v[208:209] offset:24704\n"
      "v_mfma_f32_16x16x32_bf16 v[132:135], a[244:247], v[40:43], v[132:135]\n"
      "s_add_u32 s54, s30, s35\n"
      "s_addc_u32 s55, s31, 0\n"
      "v_mfma_f32_16x16x32_bf16 v[132:135], a[248:251], v[44:47], v[132:135]\n"
      "v_mfma_f32_16x16x32_bf16 v[132:135], a[252:255], v[60:63], v[132:135]\n"
      "v_cvt_pk_bf16_f32 v210, v136, v137\n"
      "s_waitcnt lgkmcnt(5)\n"
      "v_mfma_f32_16x16x32_bf16 v[132:135], a[48:51], v[64:67], v[132:135]\n"
      "v_cvt_pk_bf16_f32 v211, v138, v139\n"
      "v_mfma_f32_16x16x32_bf16 v[132:135], a[52:55], v[68:71], v[132:135]\n"
      "s_mul_i32 s78, s94, s19\n"
      "v_mfma_f32_16x16x32_bf16 v[132:135], a[56:59], v[84:87], v[132:135]\n"
      "ds_read_b128 v[216:219], v246 offset:12288\n"
      "v_add_u32 v236, s78, v236\n"
      "v_mfma_f32_16x16x32_bf16 v[132:135], a[60:63], v[88:91], v[132:135]\n"
      "ds_read_b128 v[220:223], v246 offset:13312\n"
      "s_waitcnt lgkmcnt(3)\n"
      "v_mfma_f32_16x16x32_bf16 v[132:135], a[64:67], v[92:95], v[132:135]\n"
      "ds_read_b128 v[224:227], v246 offset:14336\n"
      "v_mfma_f32_16x16x32_bf16 v[132:135], a[68:71], v[108:111], v[132:135]\n"
      "ds_write_b64 v244, v[210:211] offset:24712\n"
      "v_mfma_f32_16x16x32_bf16 v[132:135], a[72:75], v[112:115], v[132:135]\n"
      "s_mul_i32 s78, s78, s99\n"
      "v_mfma_f32_16x16x32_bf16 v[132:135], a[76:79], v[116:119], v[132:135]\n"
      "v_add_u32 v237, s78, v237\n"
      "v_mfma_f32_16x16x32_bf16 v[136:139], a[80:83], v[36:39], v[4:7]\n"
      "v_mfma_f32_16x16x32_bf16 v[136:139], a[84:87], v[40:43], v[136:139]\n"
      "v_mfma_f32_16x16x32_bf16 v[136:139], a[88:91], v[44:47], v[136:139]\n"
      "v_mfma_f32_16x16x32_bf16 v[136:139], a[92:95], v[60:63], v[136:139]\n"
      "v_cvt_pk_bf16_f32 v208, v132, v133\n"
      "v_mfma_f32_16x16x32_bf16 v[136:139], a[96:99], v[64:67], v[136:139]\n"
      "v_cvt_pk_bf16_f32 v209, v134, v135\n"
      "v_mfma_f32_16x16x32_bf16 v[136:139], a[100:103], v[68:71], v[136:139]\n"
      "s_waitcnt lgkmcnt(3)\n"
      "v_mfma_f32_16x16x32_bf16 v[136:139], a[104:107], v[84:87], v[136:139]\n"
      "global_store_dwordx4 v247, v[216:219], s[54:55] offset:0 sc1\n"
      "v_mfma_f32_16x16x32_bf16 v[136:139], a[108:111], v[88:91], v[136:139]\n"
      "s_add_u32 s52, s20, s37\n"
      "s_addc_u32 s53, s21, 0\n"
      "v_mfma_f32_16x16x32_bf16 v[136:139], a[112:115], v[92:95], v[136:139]\n"
      "s_add_u32 m0, s17, 0xc000\n"
      "v_mfma_f32_16x16x32_bf16 v[136:139], a[116:119], v[108:111], v[136:139]\n"
      "ds_write_b64 v244, v[208:209] offset:0\n"
      "v_mfma_f32_16x16x32_bf16 v[136:139], a[120:123], v[112:115], v[136:139]\n"
      "global_load_lds_dwordx4 v236, s[52:53] offset:0 nt\n"
      "v_mfma_f32_16x16x32_bf16 v[136:139], a[124:127], v[116:119], v[136:139]\n"
      "v_mfma_f32_16x16x32_bf16 v[132:135], a[128:131], v[36:39], v[8:11]\n"
      "v_mfma_f32_16x16x32_bf16 v[132:135], a[132:135], v[40:43], v[132:135]\n"
      "v_mfma_f32_16x16x32_bf16 v[132:135], a[136:139], v[44:47], v[132:135]\n"
      "v_mfma_f32_16x16x32_bf16 v[132:135], a[140:143], v[60:63], v[132:135]\n"
      "v_cvt_pk_bf16_f32 v210, v136, v137\n"
      "v_mfma_f32_16x16x32_bf16 v[132:135], a[144:147], v[64:67], v[132:135]\n"
      "v_cvt_pk_bf16_f32 v211, v138, v139\n"
      "v_mfma_f32_16x16x32_bf16 v[132:135], a[148:151], v[68:71], v[132:135]\n"
      "v_mfma_f32_16x16x32_bf16 v[132:135], a[152:155], v[84:87], v[132:135]\n"
      "s_waitcnt lgkmcnt(3)\n"
      "v_mfma_f32_16x16x32_bf16 v[132:135], a[156:159], v[88:91], v[132:135]\n"
      "global_store_dwordx4 v252, v[220:223], s[54:55] offset:0 sc1\n"
      "v_mfma_f32_16x16x32_bf16 v[132:135], a[160:163], v[92:95], v[132:135]\n"
      "s_add_u32 m0, s17, 0xdf00\n"
      "v_mfma_f32_16x16x32_bf16 v[132:135], a[164:167], v[108:111], v[132:135]\n"
      "ds_write_b64 v244, v[210:211] offset:8\n"
      "v_mfma_f32_16x16x32_bf16 v[132:135], a[168:171], v[112:115], v[132:135]\n"
      "global_load_lds_dwordx4 v236, s[52:53] offset:256 nt\n"
      "v_mfma_f32_16x16x32_bf16 v[132:135], a[172:175], v[116:119], v[132:135]\n"
      "v_mfma_f32_16x16x32_bf16 v[136:139], a[176:179], v[36:39], v[12:15]\n"
      "v_mfma_f32_16x16x32_bf16 v[136:139], a[180:183], v[40:43], v[136:139]\n"
      "v_mfma_f32_16x16x32_bf16 v[136:139], a[184:187], v[44:47], v[136:139]\n"
      "v_mfma_f32_16x16x32_bf16 v[136:139], a[188:191], v[60:63], v[136:139]\n"
      "v_cvt_pk_bf16_f32 v212, v132, v133\n"
      "v_mfma_f32_16x16x32_bf16 v[136:139], a[192:195], v[64:67], v[136:139]\n"
      "v_cvt_pk_bf16_f32 v213, v134, v135\n"
      "v_mfma_f32_16x16x32_bf16 v[136:139], a[196:199], v[68:71], v[136:139]\n"
      "v_mfma_f32_16x16x32_bf16 v[136:139], a[200:203], v[84:87], v[136:139]\n"
      "v_mfma_f32_16x16x32_bf16 v[136:139], a[204:207], v[88:91], v[136:139]\n"
      "s_waitcnt lgkmcnt(3)\n"
      "v_mfma_f32_16x16x32_bf16 v[136:139], a[208:211], v[92:95], v[136:139]\n"
      "global_store_dwordx4 v253, v[224:227], s[54:55] offset:0 sc1\n"
      "v_mfma_f32_16x16x32_bf16 v[136:139], a[212:215], v[108:111], v[136:139]\n"
      "ds_write_b64 v245, v[212:213] offset:0\n"
      "v_mfma_f32_16x16x32_bf16 v[136:139], a[216:219], v[112:115], v[136:139]\n"
      "s_add_u32 m0, s17, 0xfe00\n"
      "v_mfma_f32_16x16x32_bf16 v[136:139], a[220:223], v[116:119], v[136:139]\n"
      "global_load_lds_dwordx4 v236, s[52:53] offset:512 nt\n"
      "v_mfma_f32_16x16x32_bf16 v[132:135], a[224:227], v[36:39], v[16:19]\n"
      "v_mfma_f32_16x16x32_bf16 v[132:135], a[228:231], v[40:43], v[132:135]\n"
      "v_mfma_f32_16x16x32_bf16 v[132:135], a[232:235], v[44:47], v[132:135]\n"
      "v_mfma_f32_16x16x32_bf16 v[132:135], a[236:239], v[60:63], v[132:135]\n"
      "v_cvt_pk_bf16_f32 v214, v136, v137\n"
      "v_mfma_f32_16x16x32_bf16 v[132:135], a[0:3], v[64:67], v[132:135]\n"
      "v_cvt_pk_bf16_f32 v215, v138, v139\n"
      "v_mfma_f32_16x16x32_bf16 v[132:135], a[4:7], v[68:71], v[132:135]\n"
      "v_mfma_f32_16x16x32_bf16 v[132:135], a[8:11], v[84:87], v[132:135]\n"
      "v_mfma_f32_16x16x32_bf16 v[132:135], a[12:15], v[88:91], v[132:135]\n"
      "v_mfma_f32_16x16x32_bf16 v[132:135], a[16:19], v[92:95], v[132:135]\n"
      "v_mfma_f32_16x16x32_bf16 v[132:135], a[20:23], v[108:111], v[132:135]\n"
      "ds_write_b64 v245, v[214:215] offset:8\n"
      "v_mfma_f32_16x16x32_bf16 v[132:135], v[140:143], v[112:115], v[132:135]\n"
      "v_mfma_f32_16x16x32_bf16 v[132:135], v[156:159], v[116:119], v[132:135]\n"
      "s_waitcnt lgkmcnt(4)\n"
      "s_barrier\n"
      "v_mfma_f32_16x16x32_bf16 v[136:139], v[160:163], v[36:39], v[20:23]\n"
      "ds_read_b128 v[36:39], v232 offset:24832\n"
      "v_mfma_f32_16x16x32_bf16 v[136:139], v[164:167], v[40:43], v[136:139]\n"
      "ds_read_b128 v[40:43], v233 offset:24832\n"
      "v_mfma_f32_16x16x32_bf16 v[136:139], v[180:183], v[44:47], v[136:139]\n"
      "ds_read_b128 v[44:47], v234 offset:24832\n"
      "v_mfma_f32_16x16x32_bf16 v[136:139], v[184:187], v[60:63], v[136:139]\n"
      "ds_read_b128 v[60:63], v235 offset:24832\n"
      "v_cvt_pk_bf16_f32 v208, v132, v133\n"
      "v_mfma_f32_16x16x32_bf16 v[136:139], v[188:191], v[64:67], v[136:139]\n"
      "ds_read_b128 v[64:67], v232 offset:33024\n"
      "v_cvt_pk_bf16_f32 v209, v134, v135\n"
      "v_mfma_f32_16x16x32_bf16 v[136:139], v[204:207], v[68:71], v[136:139]\n"
      "ds_read_b128 v[68:71], v233 offset:33024\n"
      "v_mfma_f32_16x16x32_bf16 v[136:139], a[24:27], v[84:87], v[136:139]\n"
      "ds_read_b128 v[84:87], v234 offset:33024\n"
      "v_mfma_f32_16x16x32_bf16 v[136:139], a[28:31], v[88:91], v[136:139]\n"
      "ds_read_b128 v[88:91], v235 offset:33024\n"
      "v_mfma_f32_16x16x32_bf16 v[136:139], a[32:35], v[92:95], v[136:139]\n"
      "ds_read_b128 v[92:95], v232 offset:41216\n"
      "v_mfma_f32_16x16x32_bf16 v[136:139], a[36:39], v[108:111], v[136:139]\n"
      "ds_read_b128 v[108:111], v233 offset:41216\n"
      "ds_write_b64 v244, v[208:209] offset:128\n"
      "v_mfma_f32_16x16x32_bf16 v[136:139], a[40:43], v[112:115], v[136:139]\n"
      "LPD11:\n"
      "s_cbranch_execz LPD14\n"
      "ds_read_b128 v[112:115], v234 offset:41216\n"
      "v_mfma_f32_16x16x32_bf16 v[136:139], a[44:47], v[116:119], v[136:139]\n"
      "ds_read_b128 v[116:119], v235 offset:41216\n"
      "s_waitcnt lgkmcnt(9)\n"
      "v_mfma_f32_16x16x32_bf16 v[132:135], a[240:243], v[36:39], v[0:3]\n"
      "v_add_u32 v232, 0xffff4000, v232\n"
      "v_mfma_f32_16x16x32_bf16 v[132:135], a[244:247], v[40:43], v[132:135]\n"
      "v_add_u32 v233, 0xffff4000, v233\n"
      "v_mfma_f32_16x16x32_bf16 v[132:135], a[248:251], v[44:47], v[132:135]\n"
      "v_add_u32 v234, 0xffff4000, v234\n"
      "v_mfma_f32_16x16x32_bf16 v[132:135], a[252:255], v[60:63], v[132:135]\n"
      "v_add_u32 v235, 0xffff4000, v235\n"
      "s_waitcnt lgkmcnt(5)\n"
      "v_mfma_f32_16x16x32_bf16 v[132:135], a[48:51], v[64:67], v[132:135]\n"
      "v_cvt_pk_bf16_f32 v210, v136, v137\n"
      "v_mfma_f32_16x16x32_bf16 v[132:135], a[52:55], v[68:71], v[132:135]\n"
      "v_cvt_pk_bf16_f32 v211, v138, v139\n"
      "v_mfma_f32_16x16x32_bf16 v[132:135], a[56:59], v[84:87], v[132:135]\n"
      "ds_read_b128 v[216:219], v246 offset:24576\n"
      "v_mfma_f32_16x16x32_bf16 v[132:135], a[60:63], v[88:91], v[132:135]\n"
      "ds_read_b128 v[220:223], v246 offset:25600\n"
      "s_waitcnt lgkmcnt(2)\n"
      "v_mfma_f32_16x16x32_bf16 v[132:135], a[64:67], v[92:95], v[132:135]\n"
      "ds_read_b128 v[224:227], v246 offset:26624\n"
      "v_mfma_f32_16x16x32_bf16 v[132:135], a[68:71], v[108:111], v[132:135]\n"
      "v_lshlrev_b32 v0, 16, v96\n"
      "v_mfma_f32_16x16x32_bf16 v[132:135], a[72:75], v[112:115], v[132:135]\n"
      "ds_write_b64 v244, v[210:211] offset:136\n"
      "v_mfma_f32_16x16x32_bf16 v[132:135], a[76:79], v[116:119], v[132:135]\n"
      "v_and_b32 v1, 0xffff0000, v96\n"
      "v_mfma_f32_16x16x32_bf16 v[136:139], a[80:83], v[36:39], v[4:7]\n"
      "v_lshlrev_b32 v2, 16, v97\n"
      "v_mfma_f32_16x16x32_bf16 v[136:139], a[84:87], v[40:43], v[136:139]\n"
      "v_and_b32 v3, 0xffff0000, v97\n"
      "v_mfma_f32_16x16x32_bf16 v[136:139], a[88:91], v[44:47], v[136:139]\n"
      "v_mfma_f32_16x16x32_bf16 v[136:139], a[92:95], v[60:63], v[136:139]\n"
      "v_cvt_pk_bf16_f32 v208, v132, v133\n"
      "v_mfma_f32_16x16x32_bf16 v[136:139], a[96:99], v[64:67], v[136:139]\n"
      "v_cvt_pk_bf16_f32 v209, v134, v135\n"
      "v_mfma_f32_16x16x32_bf16 v[136:139], a[100:103], v[68:71], v[136:139]\n"
      "s_waitcnt lgkmcnt(3)\n"
      "v_mfma_f32_16x16x32_bf16 v[136:139], a[104:107], v[84:87], v[136:139]\n"
      "global_store_dwordx4 v247, v[216:219], s[54:55] offset:768 sc1\n"
      "v_mfma_f32_16x16x32_bf16 v[136:139], a[108:111], v[88:91], v[136:139]\n"
      "s_add_u32 m0, s17, 0xd000\n"
      "v_mfma_f32_16x16x32_bf16 v[136:139], a[112:115], v[92:95], v[136:139]\n"
      "s_waitcnt vmcnt(19)\n"
      "s_sub_u32 s49, s48, 1000\n"
      "v_mfma_f32_16x16x32_bf16 v[136:139], a[116:119], v[108:111], v[136:139]\n"
      "v_mov_b32 v230, 1100\n"
      "ds_write_b64 v244, v[208:209] offset:12288\n"
      "v_mfma_f32_16x16x32_bf16 v[136:139], a[120:123], v[112:115], v[136:139]\n"
      "v_xor_b32 v228, s80, v228\n"
      "v_mfma_f32_16x16x32_bf16 v[136:139], a[124:127], v[116:119], v[136:139]\n"
      "v_subrev_u32 v228, s49, v228\n"
      "v_mfma_f32_16x16x32_bf16 v[132:135], a[128:131], v[36:39], v[8:11]\n"
      "v_cmp_gt_u32 vcc, 2000, v228\n"
      "v_cndmask_b32 v228, v230, v228, vcc\n"
      "v_mfma_f32_16x16x32_bf16 v[132:135], a[132:135], v[40:43], v[132:135]\n"
      "global_load_lds_dwordx4 v237, s[52:53] offset:0 nt\n"
      "v_mfma_f32_16x16x32_bf16 v[132:135], a[136:139], v[44:47], v[132:135]\n"
      "v_add_u32_dpp v228, v228, v228 row_shr:1 row_mask:0xf bank_mask:0xf bound_ctrl:0\n"
      "v_mfma_f32_16x16x32_bf16 v[132:135], a[140:143], v[60:63], v[132:135]\n"
      "v_cvt_pk_bf16_f32 v210, v136, v137\n"
      "v_mfma_f32_16x16x32_bf16 v[132:135], a[144:147], v[64:67], v[132:135]\n"
      "v_add_u32_dpp v228, v228, v228 row_shr:2 row_mask:0xf bank_mask:0xf bound_ctrl:0\n"
      "v_mfma_f32_16x16x32_bf16 v[132:135], a[148:151], v[68:71], v[132:135]\n"
      "v_cvt_pk_bf16_f32 v211, v138, v139\n"
      "v_mfma_f32_16x16x32_bf16 v[132:135], a[152:155], v[84:87], v[132:135]\n"
      "v_add_u32_dpp v228, v228, v228 row_shr:4 row_mask:0xf bank_mask:0xf bound_ctrl:0\n"
      "v_mfma_f32_16x16x32_bf16 v[132:135], a[156:159], v[88:91], v[132:135]\n"
      "s_waitcnt lgkmcnt(3)\n"
      "v_mfma_f32_16x16x32_bf16 v[132:135], a[160:163], v[92:95], v[132:135]\n"
      "v_add_u32_dpp v228, v228, v228 row_shr:8 row_mask:0xf bank_mask:0xf bound_ctrl:0\n"
      "v_mfma_f32_16x16x32_bf16 v[132:135], a[164:167], v[108:111], v[132:135]\n"
      "global_store_dwordx4 v252, v[220:223], s[54:55] offset:768 sc1\n"
      "v_mfma_f32_16x16x32_bf16 v[132:135], a[168:171], v[112:115], v[132:135]\n"
      "v_readlane_b32 s62, v228, 15\n"
      "v_mfma_f32_16x16x32_bf16 v[132:135], a[172:175], v[116:119], v[132:135]\n"
      "ds_write_b64 v244, v[210:211] offset:12296\n"
      "v_mfma_f32_16x16x32_bf16 v[136:139], a[176:179], v[36:39], v[12:15]\n"
      "s_add_u32 m0, s17, 0xef00\n"
      "v_mfma_f32_16x16x32_bf16 v[136:139], a[180:183], v[40:43], v[136:139]\n"
      "s_lshr_b32 s62, s62, 4\n"
      "v_mfma_f32_16x16x32_bf16 v[136:139], a[184:187], v[44:47], v[136:139]\n"
      "s_add_u32 s62, s62, 20\n"
      "v_mfma_f32_16x16x32_bf16 v[136:139], a[188:191], v[60:63], v[136:139]\n"
      "s_add_u32 s49, s49, s62\n"
      "v_mfma_f32_16x16x32_bf16 v[136:139], a[192:195], v[64:67], v[136:139]\n"
      "v_cvt_pk_bf16_f32 v212, v132, v133\n"
      "v_mfma_f32_16x16x32_bf16 v[136:139], a[196:199], v[68:71], v[136:139]\n"
      "v_cvt_pk_bf16_f32 v213, v134, v135\n"
      "v_mfma_f32_16x16x32_bf16 v[136:139], a[200:203], v[84:87], v[136:139]\n"
      "global_load_lds_dwordx4 v237, s[52:53] offset:256 nt\n"
      "v_mfma_f32_16x16x32_bf16 v[136:139], a[204:207], v[88:91], v[136:139]\n"
      "s_waitcnt lgkmcnt(3)\n"
      "v_mfma_f32_16x16x32_bf16 v[136:139], a[208:211], v[92:95], v[136:139]\n"
      "global_store_dwordx4 v253, v[224:227], s[54:55] offset:768 sc1\n"
      "v_mfma_f32_16x16x32_bf16 v[136:139], a[212:215], v[108:111], v[136:139]\n"
      "v_lshlrev_b32 v4, 16, v98\n"
      "v_mfma_f32_16x16x32_bf16 v[136:139], a[216:219], v[112:115], v[136:139]\n"
      "ds_write_b64 v245, v[212:213] offset:12288\n"
      "v_mfma_f32_16x16x32_bf16 v[136:139], a[220:223], v[116:119], v[136:139]\n"
      "v_and_b32 v5, 0xffff0000, v98\n"
      "v_mfma_f32_16x16x32_bf16 v[132:135], a[224:227], v[36:39], v[16:19]\n"
      "v_lshlrev_b32 v6, 16, v99\n"
      "v_mfma_f32_16x16x32_bf16 v[132:135], a[228:231], v[40:43], v[132:135]\n"
      "v_and_b32 v7, 0xffff0000, v99\n"
      "v_mfma_f32_16x16x32_bf16 v[132:135], a[232:235], v[44:47], v[132:135]\n"
      "v_lshlrev_b32 v8, 16, v100\n"
      "v_mfma_f32_16x16x32_bf16 v[132:135], a[236:239], v[60:63], v[132:135]\n"
      "s_memrealtime s[84:85]\n"
      "v_mfma_f32_16x16x32_bf16 v[132:135], a[0:3], v[64:67], v[132:135]\n"
      "v_cvt_pk_bf16_f32 v214, v136, v137\n"
      "v_mfma_f32_16x16x32_bf16 v[132:135], a[4:7], v[68:71], v[132:135]\n"
      "v_cvt_pk_bf16_f32 v215, v138, v139\n"
      "v_mfma_f32_16x16x32_bf16 v[132:135], a[8:11], v[84:87], v[132:135]\n"
      "s_add_u32 m0, s17, 0x10e00\n"
      "v_mfma_f32_16x16x32_bf16 v[132:135], a[12:15], v[88:91], v[132:135]\n"
      "global_load_lds_dwordx4 v237, s[52:53] offset:512 nt\n"
      "v_mfma_f32_16x16x32_bf16 v[132:135], a[16:19], v[92:95], v[132:135]\n"
      "v_and_b32 v9, 0xffff0000, v100\n"
      "v_mfma_f32_16x16x32_bf16 v[132:135], a[20:23], v[108:111], v[132:135]\n"
      "v_lshlrev_b32 v10, 16, v101\n"
      "v_mfma_f32_16x16x32_bf16 v[132:135], v[140:143], v[112:115], v[132:135]\n"
      "s_waitcnt lgkmcnt(0)\n"
      "s_sub_u32 s86, s49, s84\n"
      "s_mov_b32 s83, s84\n"
      "v_mfma_f32_16x16x32_bf16 v[132:135], v[156:159], v[116:119], v[132:135]\n"
      "s_cmp_gt_u32 s62, 1080\n"
      "s_cselect_b64 vcc, -1, 0\n"
      "s_barrier\n"
      "v_mfma_f32_16x16x32_bf16 v[136:139], v[160:163], v[36:39], v[20:23]\n"
      "ds_read_b128 v[36:39], v232 offset:0\n"
      "ds_write_b64 v245, v[214:215] offset:12296\n"
      "v_mfma_f32_16x16x32_bf16 v[136:139], v[164:167], v[40:43], v[136:139]\n"
      "ds_read_b128 v[40:43], v233 offset:0\n"
      "v_mfma_f32_16x16x32_bf16 v[136:139], v[180:183], v[44:47], v[136:139]\n"
      "ds_read_b128 v[44:47], v234 offset:0\n"
      "v_mfma_f32_16x16x32_bf16 v[136:139], v[184:187], v[60:63], v[136:139]\n"
      "ds_read_b128 v[60:63], v235 offset:0\n"
      "v_cvt_pk_bf16_f32 v208, v132, v133\n"
      "v_mfma_f32_16x16x32_bf16 v[136:139], v[188:191], v[64:67], v[136:139]\n"
      "ds_read_b128 v[64:67], v232 offset:8192\n"
      "v_cvt_pk_bf16_f32 v209, v134, v135\n"
      "v_mfma_f32_16x16x32_bf16 v[136:139], v[204:207], v[68:71], v[136:139]\n"
      "ds_read_b128 v[68:71], v233 offset:8192\n"
      "v_mfma_f32_16x16x32_bf16 v[136:139], a[24:27], v[84:87], v[136:139]\n"
      "ds_read_b128 v[84:87], v234 offset:8192\n"
      "v_mfma_f32_16x16x32_bf16 v[136:139], a[28:31], v[88:91], v[136:139]\n"
      "ds_read_b128 v[88:91], v235 offset:8192\n"
      "v_mfma_f32_16x16x32_bf16 v[136:139], a[32:35], v[92:95], v[136:139]\n"
      "ds_read_b128 v[92:95], v232 offset:16384\n"
      "v_mfma_f32_16x16x32_bf16 v[136:139], a[36:39], v[108:111], v[136:139]\n"
      "ds_read_b128 v[108:111], v233 offset:16384\n"
      "v_mfma_f32_16x16x32_bf16 v[136:139], a[40:43], v[112:115], v[136:139]\n"
      "ds_read_b128 v[112:115], v234 offset:16384\n"
      "v_mfma_f32_16x16x32_bf16 v[136:139], a[44:47], v[116:119], v[136:139]\n"
      "ds_read_b128 v[116:119], v235 offset:16384\n"
      "s_waitcnt lgkmcnt(8)\n"
      "v_mfma_f32_16x16x32_bf16 v[132:135], a[240:243], v[36:39], v[0:3]\n"
      "ds_write_b64 v244, v[208:209] offset:12416\n"
      "s_cbranch_vccnz LT0\n"
      "LS0:\n"
      "v_mfma_f32_16x16x32_bf16 v[132:135], a[244:247], v[40:43], v[132:135]\n"
      "v_and_b32 v11, 0xffff0000, v101\n"
      "v_mfma_f32_16x16x32_bf16 v[132:135], a[248:251], v[44:47], v[132:135]\n"
      "s_mul_i32 s86, s86, 128\n"
      "v_mfma_f32_16x16x32_bf16 v[132:135], a[252:255], v[60:63], v[132:135]\n"
      "v_lshlrev_b32 v12, 16, v102\n"
      "v_cvt_pk_bf16_f32 v210, v136, v137\n"
      "s_waitcnt lgkmcnt(5)\n"
      "v_mfma_f32_16x16x32_bf16 v[132:135], a[48:51], v[64:67], v[132:135]\n"
      "s_ashr_i32 s86, s86, 10\n"
      "v_cvt_pk_bf16_f32 v211, v138, v139\n"
      "v_mfma_f32_16x16x32_bf16 v[132:135], a[52:55], v[68:71], v[132:135]\n"
      "v_and_b32 v13, 0xffff0000, v102\n"
      "v_mfma_f32_16x16x32_bf16 v[132:135], a[56:59], v[84:87], v[132:135]\n"
      "v_lshlrev_b32 v14, 16, v103\n"
      "v_mfma_f32_16x16x32_bf16 v[132:135], a[60:63], v[88:91], v[132:135]\n"
      "v_and_b32 v15, 0xffff0000, v103\n"
      "s_waitcnt lgkmcnt(1)\n"
      "v_mfma_f32_16x16x32_bf16 v[132:135], a[64:67], v[92:95], v[132:135]\n"
      "v_lshlrev_b32 v16, 16, v104\n"
      "v_mfma_f32_16x16x32_bf16 v[132:135], a[68:71], v[108:111], v[132:135]\n"
      "v_and_b32 v17, 0xffff0000, v104\n"
      "ds_write_b64 v244, v[210:211] offset:12424\n"
      "v_mfma_f32_16x16x32_bf16 v[132:135], a[72:75], v[112:115], v[132:135]\n"
      "v_lshlrev_b32 v18, 16, v105\n"
      "v_mfma_f32_16x16x32_bf16 v[132:135], a[76:79], v[116:119], v[132:135]\n"
      "v_and_b32 v19, 0xffff0000, v105\n"
      "v_mfma_f32_16x16x32_bf16 v[136:139], a[80:83], v[36:39], v[4:7]\n"
      "v_lshlrev_b32 v20, 16, v106\n"
      "v_mfma_f32_16x16x32_bf16 v[136:139], a[84:87], v[40:43], v[136:139]\n"
      "v_and_b32 v21, 0xffff0000, v106\n"
      "v_mfma_f32_16x16x32_bf16 v[136:139], a[88:91], v[44:47], v[136:139]\n"
      "v_lshlrev_b32 v22, 16, v107\n"
      "v_mfma_f32_16x16x32_bf16 v[136:139], a[92:95], v[60:63], v[136:139]\n"
      "v_and_b32 v23, 0xffff0000, v107\n"
      "v_mfma_f32_16x16x32_bf16 v[136:139], a[96:99], v[64:67], v[136:139]\n"
      "v_cvt_pk_bf16_f32 v208, v132, v133\n"
      "v_mfma_f32_16x16x32_bf16 v[136:139], a[100:103], v[68:71], v[136:139]\n"
      "v_cvt_pk_bf16_f32 v209, v134, v135\n"
      "v_mfma_f32_16x16x32_bf16 v[136:139], a[104:107], v[84:87], v[136:139]\n"
      "s_add_u32 s54, s44, s35\n"
      "s_addc_u32 s55, s45, 0\n"
      "v_mfma_f32_16x16x32_bf16 v[136:139], a[108:111], v[88:91], v[136:139]\n"
      "ds_read_b128 v[216:219], v246 offset:0\n"
      "v_mfma_f32_16x16x32_bf16 v[136:139], a[112:115], v[92:95], v[136:139]\n"
      "ds_read_b128 v[220:223], v246 offset:1024\n"
      "v_mfma_f32_16x16x32_bf16 v[136:139], a[116:119], v[108:111], v[136:139]\n"
      "ds_read_b128 v[224:227], v246 offset:2048\n"
      "v_mfma_f32_16x16x32_bf16 v[136:139], a[120:123], v[112:115], v[136:139]\n"
      "ds_write_b64 v244, v[208:209] offset:24576\n"
      "v_mfma_f32_16x16x32_bf16 v[136:139], a[124:127], v[116:119], v[136:139]\n"
      "s_waitcnt lgkmcnt(3)\n"
      "v_mfma_f32_16x16x32_bf16 v[132:135], a[128:131], v[36:39], v[8:11]\n"
      "global_store_dwordx4 v247, v[216:219], s[54:55] offset:0 sc1\n"
      "v_mfma_f32_16x16x32_bf16 v[132:135], a[132:135], v[40:43], v[132:135]\n"
      "s_add_u32 s52, s42, s37\n"
      "s_addc_u32 s53, s43, 0\n"
      "v_mfma_f32_16x16x32_bf16 v[132:135], a[136:139], v[44:47], v[132:135]\n"
      "s_add_u32 m0, s17, 0x12000\n"
      "v_mfma_f32_16x16x32_bf16 v[132:135], a[140:143], v[60:63], v[132:135]\n"
      "v_cvt_pk_bf16_f32 v210, v136, v137\n"
      "v_mfma_f32_16x16x32_bf16 v[132:135], a[144:147], v[64:67], v[132:135]\n"
      "v_cvt_pk_bf16_f32 v211, v138, v139\n"
      "v_mfma_f32_16x16x32_bf16 v[132:135], a[148:151], v[68:71], v[132:135]\n"
      "global_load_lds_dwordx4 v236, s[52:53] offset:0 nt\n"
      "v_mfma_f32_16x16x32_bf16 v[132:135], a[152:155], v[84:87], v[132:135]\n"
      "s_waitcnt lgkmcnt(2)\n"
      "v_mfma_f32_16x16x32_bf16 v[132:135], a[156:159], v[88:91], v[132:135]\n"
      "global_store_dwordx4 v252, v[220:223], s[54:55] offset:0 sc1\n"
      "v_mfma_f32_16x16x32_bf16 v[132:135], a[160:163], v[92:95], v[132:135]\n"
      "v_mfma_f32_16x16x32_bf16 v[132:135], a[164:167], v[108:111], v[132:135]\n"
      "ds_write_b64 v244, v[210:211] offset:24584\n"
      "v_mfma_f32_16x16x32_bf16 v[132:135], a[168:171], v[112:115], v[132:135]\n"
      "v_mfma_f32_16x16x32_bf16 v[132:135], a[172:175], v[116:119], v[132:135]\n"
      "v_mfma_f32_16x16x32_bf16 v[136:139], a[176:179], v[36:39], v[12:15]\n"
      "s_cbranch_vccnz LT1\n"
      "LS1:\n"
      "v_mfma_f32_16x16x32_bf16 v[136:139], a[180:183], v[40:43], v[136:139]\n"
      "v_mfma_f32_16x16x32_bf16 v[136:139], a[184:187], v[44:47], v[136:139]\n"
      "v_mfma_f32_16x16x32_bf16 v[136:139], a[188:191], v[60:63], v[136:139]\n"
      "v_cvt_pk_bf16_f32 v212, v132, v133\n"
      "v_mfma_f32_16x16x32_bf16 v[136:139], a[192:195], v[64:67], v[136:139]\n"
      "v_cvt_pk_bf16_f32 v213, v134, v135\n"
      "v_mfma_f32_16x16x32_bf16 v[136:139], a[196:199], v[68:71], v[136:139]\n"
      "s_add_u32 m0, s17, 0x13f00\n"
      "v_mfma_f32_16x16x32_bf16 v[136:139], a[200:203], v[84:87], v[136:139]\n"
      "global_load_lds_dwordx4 v236, s[52:53] offset:256 nt\n"
      "v_mfma_f32_16x16x32_bf16 v[136:139], a[204:207], v[88:91], v[136:139]\n"
      "s_waitcnt lgkmcnt(2)\n"
      "v_mfma_f32_16x16x32_bf16 v[136:139], a[208:211], v[92:95], v[136:139]\n"
      "global_store_dwordx4 v253, v[224:227], s[54:55] offset:0 sc1\n"
      "v_mfma_f32_16x16x32_bf16 v[136:139], a[212:215], v[108:111], v[136:139]\n"
      "ds_write_b64 v245, v[212:213] offset:24576\n"
      "v_mfma_f32_16x16x32_bf16 v[136:139], a[216:219], v[112:115], v[136:139]\n"
      "v_mfma_f32_16x16x32_bf16 v[136:139], a[220:223], v[116:119], v[136:139]\n"
      "v_mfma_f32_16x16x32_bf16 v[132:135], a[224:227], v[36:39], v[16:19]\n"
      "v_mfma_f32_16x16x32_bf16 v[132:135], a[228:231], v[40:43], v[132:135]\n"
      "v_mfma_f32_16x16x32_bf16 v[132:135], a[232:235], v[44:47], v[132:135]\n"
      "v_mfma_f32_16x16x32_bf16 v[132:135], a[236:239], v[60:63], v[132:135]\n"
      "v_cvt_pk_bf16_f32 v214, v136, v137\n"
      "v_mfma_f32_16x16x32_bf16 v[132:135], a[0:3], v[64:67], v[132:135]\n"
      "v_cvt_pk_bf16_f32 v215, v138, v139\n"
      "v_mfma_f32_16x16x32_bf16 v[132:135], a[4:7], v[68:71], v[132:135]\n"
      "s_add_u32 m0, s17, 0x15e00\n"
      "v_mfma_f32_16x16x32_bf16 v[132:135], a[8:11], v[84:87], v[132:135]\n"
      "global_load_lds_dwordx4 v236, s[52:53] offset:512 nt\n"
      "v_mfma_f32_16x16x32_bf16 v[132:135], a[12:15], v[88:91], v[132:135]\n"
      "v_mfma_f32_16x16x32_bf16 v[132:135], a[16:19], v[92:95], v[132:135]\n"
      "v_mfma_f32_16x16x32_bf16 v[132:135], a[20:23], v[108:111], v[132:135]\n"
      "ds_write_b64 v245, v[214:215] offset:24584\n"
      "v_mfma_f32_16x16x32_bf16 v[132:135], v[140:143], v[112:115], v[132:135]\n"
      "v_mfma_f32_16x16x32_bf16 v[132:135], v[156:159], v[116:119], v[132:135]\n"
      "s_barrier\n"
      "v_mfma_f32_16x16x32_bf16 v[136:139], v[160:163], v[36:39], v[20:23]\n"
      "ds_read_b128 v[36:39], v232 offset:256\n"
      "v_mfma_f32_16x16x32_bf16 v[136:139], v[164:167], v[40:43], v[136:139]\n"
      "ds_read_b128 v[40:43], v233 offset:256\n"
      "v_mfma_f32_16x16x32_bf16 v[136:139], v[180:183], v[44:47], v[136:139]\n"
      "ds_read_b128 v[44:47], v234 offset:256\n"
      "v_mfma_f32_16x16x32_bf16 v[136:139], v[184:187], v[60:63], v[136:139]\n"
      "ds_read_b128 v[60:63], v235 offset:256\n"
      "v_cvt_pk_bf16_f32 v208, v132, v133\n"
      "v_mfma_f32_16x16x32_bf16 v[136:139], v[188:191], v[64:67], v[136:139]\n"
      "ds_read_b128 v[64:67], v232 offset:8448\n"
      "v_cvt_pk_bf16_f32 v209, v134, v135\n"
      "v_mfma_f32_16x16x32_bf16 v[136:139], v[204:207], v[68:71], v[136:139]\n"
      "ds_read_b128 v[68:71], v233 offset:8448\n"
      "v_mfma_f32_16x16x32_bf16 v[136:139], a[24:27], v[84:87], v[136:139]\n"
      "ds_read_b128 v[84:87], v234 offset:8448\n"
      "v_mfma_f32_16x16x32_bf16 v[136:139], a[28:31], v[88:91], v[136:139]\n"
      "ds_read_b128 v[88:91], v235 offset:8448\n"
      "v_mfma_f32_16x16x32_bf16 v[136:139], a[32:35], v[92:95], v[136:139]\n"
      "ds_read_b128 v[92:95], v232 offset:16640\n"
      "v_mfma_f32_16x16x32_bf16 v[136:139], a[36:39], v[108:111], v[136:139]\n"
      "ds_read_b128 v[108:111], v233 offset:16640\n"
      "v_mfma_f32_16x16x32_bf16 v[136:139], a[40:43], v[112:115], v[136:139]\n"
      "ds_read_b128 v[112:115], v234 offset:16640\n"
      "v_mfma_f32_16x16x32_bf16 v[136:139], a[44:47], v[116:119], v[136:139]\n"
      "ds_read_b128 v[116:119], v235 offset:16640\n"
      "s_waitcnt lgkmcnt(8)\n"
      "v_mfma_f32_16x16x32_bf16 v[132:135], a[240:243], v[36:39], v[0:3]\n"
      "ds_write_b64 v244, v[208:209] offset:24704\n"
      "v_mfma_f32_16x16x32_bf16 v[132:135], a[244:247], v[40:43], v[132:135]\n"
      "s_cbranch_vccnz LT2\n"
      "LS2:\n"
      "v_mfma_f32_16x16x32_bf16 v[132:135], a[248:251], v[44:47], v[132:135]\n"
      "v_mfma_f32_16x16x32_bf16 v[132:135], a[252:255], v[60:63], v[132:135]\n"
      "v_cvt_pk_bf16_f32 v210, v136, v137\n"
      "s_waitcnt lgkmcnt(5)\n"
      "v_mfma_f32_16x16x32_bf16 v[132:135], a[48:51], v[64:67], v[132:135]\n"
      "v_cvt_pk_bf16_f32 v211, v138, v139\n"
      "v_mfma_f32_16x16x32_bf16 v[132:135], a[52:55], v[68:71], v[132:135]\n"
      "v_mfma_f32_16x16x32_bf16 v[132:135], a[56:59], v[84:87], v[132:135]\n"
      "ds_read_b128 v[216:219], v246 offset:12288\n"
      "v_mfma_f32_16x16x32_bf16 v[132:135], a[60:63], v[88:91], v[132:135]\n"
      "ds_read_b128 v[220:223], v246 offset:13312\n"
      "s_waitcnt lgkmcnt(3)\n"
      "v_mfma_f32_16x16x32_bf16 v[132:135], a[64:67], v[92:95], v[132:135]\n"
      "ds_read_b128 v[224:227], v246 offset:14336\n"
      "v_mfma_f32_16x16x32_bf16 v[132:135], a[68:71], v[108:111], v[132:135]\n"
      "ds_write_b64 v244, v[210:211] offset:24712\n"
      "v_mfma_f32_16x16x32_bf16 v[132:135], a[72:75], v[112:115], v[132:135]\n"
      "v_mfma_f32_16x16x32_bf16 v[132:135], a[76:79], v[116:119], v[132:135]\n"
      "v_mfma_f32_16x16x32_bf16 v[136:139], a[80:83], v[36:39], v[4:7]\n"
      "v_mfma_f32_16x16x32_bf16 v[136:139], a[84:87], v[40:43], v[136:139]\n"
      "v_mfma_f32_16x16x32_bf16 v[136:139], a[88:91], v[44:47], v[136:139]\n"
      "v_mfma_f32_16x16x32_bf16 v[136:139], a[92:95], v[60:63], v[136:139]\n"
      "v_cvt_pk_bf16_f32 v208, v132, v133\n"
      "v_mfma_f32_16x16x32_bf16 v[136:139], a[96:99], v[64:67], v[136:139]\n"
      "v_cvt_pk_bf16_f32 v209, v134, v135\n"
      "v_mfma_f32_16x16x32_bf16 v[136:139], a[100:103], v[68:71], v[136:139]\n"
      "s_waitcnt lgkmcnt(3)\n"
      "v_mfma_f32_16x16x32_bf16 v[136:139], a[104:107], v[84:87], v[136:139]\n"
      "global_store_dwordx4 v247, v[216:219], s[54:55] offset:768 sc1\n"
      "v_mfma_f32_16x16x32_bf16 v[136:139], a[108:111], v[88:91], v[136:139]\n"
      "s_add_u32 m0, s17, 0x13000\n"
      "v_mfma_f32_16x16x32_bf16 v[136:139], a[112:115], v[92:95], v[136:139]\n"
      "global_load_lds_dwordx4 v237, s[52:53] offset:0 nt\n"
      "v_mfma_f32_16x16x32_bf16 v[136:139], a[116:119], v[108:111], v[136:139]\n"
      "ds_write_b64 v244, v[208:209] offset:0\n"
      "v_mfma_f32_16x16x32_bf16 v[136:139], a[120:123], v[112:115], v[136:139]\n"
      "v_mfma_f32_16x16x32_bf16 v[136:139], a[124:127], v[116:119], v[136:139]\n"
      "v_mfma_f32_16x16x32_bf16 v[132:135], a[128:131], v[36:39], v[8:11]\n"
      "v_mfma_f32_16x16x32_bf16 v[132:135], a[132:135], v[40:43], v[132:135]\n"
      "v_mfma_f32_16x16x32_bf16 v[132:135], a[136:139], v[44:47], v[132:135]\n"
      "v_mfma_f32_16x16x32_bf16 v[132:135], a[140:143], v[60:63], v[132:135]\n"
      "v_cvt_pk_bf16_f32 v210, v136, v137\n"
      "v_mfma_f32_16x16x32_bf16 v[132:135], a[144:147], v[64:67], v[132:135]\n"
      "v_cvt_pk_bf16_f32 v211, v138, v139\n"
      "v_mfma_f32_16x16x32_bf16 v[132:135], a[148:151], v[68:71], v[132:135]\n"
      "v_mfma_f32_16x16x32_bf16 v[132:135], a[152:155], v[84:87], v[132:135]\n"
      "s_waitcnt lgkmcnt(3)\n"
      "v_mfma_f32_16x16x32_bf16 v[132:135], a[156:159], v[88:91], v[132:135]\n"
      "global_store_dwordx4 v252, v[220:223], s[54:55] offset:768 sc1\n"
      "v_mfma_f32_16x16x32_bf16 v[132:135], a[160:163], v[92:95], v[132:135]\n"
      "s_add_u32 m0, s17, 0x14f00\n"
      "v_mfma_f32_16x16x32_bf16 v[132:135], a[164:167], v[108:111], v[132:135]\n"
      "ds_write_b64 v244, v[210:211] offset:8\n"
      "v_mfma_f32_16x16x32_bf16 v[132:135], a[168:171], v[112:115], v[132:135]\n"
      "global_load_lds_dwordx4 v237, s[52:53] offset:256 nt\n"
      "v_mfma_f32_16x16x32_bf16 v[132:135], a[172:175], v[116:119], v[132:135]\n"
      "v_mfma_f32_16x16x32_bf16 v[136:139], a[176:179], v[36:39], v[12:15]\n"
      "s_cbranch_vccnz LT3\n"
      "LS3:\n"
      "v_mfma_f32_16x16x32_bf16 v[136:139], a[180:183], v[40:43], v[136:139]\n"
      "v_mfma_f32_16x16x32_bf16 v[136:139], a[184:187], v[44:47], v[136:139]\n"
      "v_mfma_f32_16x16x32_bf16 v[136:139], a[188:191], v[60:63], v[136:139]\n"
      "v_cvt_pk_bf16_f32 v212, v132, v133\n"
      "v_mfma_f32_16x16x32_bf16 v[136:139], a[192:195], v[64:67], v[136:139]\n"
      "v_cvt_pk_bf16_f32 v213, v134, v135\n"
      "v_mfma_f32_16x16x32_bf16 v[136:139], a[196:199], v[68:71], v[136:139]\n"
      "v_mfma_f32_16x16x32_bf16 v[136:139], a[200:203], v[84:87], v[136:139]\n"
      "v_mfma_f32_16x16x32_bf16 v[136:139], a[204:207], v[88:91], v[136:139]\n"
      "s_waitcnt lgkmcnt(3)\n"
      "v_mfma_f32_16x16x32_bf16 v[136:139], a[208:211], v[92:95], v[136:139]\n"
      "global_store_dwordx4 v253, v[224:227], s[54:55] offset:768 sc1\n"
      "v_mfma_f32_16x16x32_bf16 v[136:139], a[212:215], v[108:111], v[136:139]\n"
      "ds_write_b64 v245, v[212:213] offset:0\n"
      "v_mfma_f32_16x16x32_bf16 v[136:139], a[216:219], v[112:115], v[136:139]\n"
      "s_add_u32 m0, s17, 0x16e00\n"
      "v_mfma_f32_16x16x32_bf16 v[136:139], a[220:223], v[116:119], v[136:139]\n"
      "global_load_lds_dwordx4 v237, s[52:53] offset:512 nt\n"
      "v_mfma_f32_16x16x32_bf16 v[132:135], a[224:227], v[36:39], v[16:19]\n"
      "v_mfma_f32_16x16x32_bf16 v[132:135], a[228:231], v[40:43], v[132:135]\n"
      "v_mfma_f32_16x16x32_bf16 v[132:135], a[232:235], v[44:47], v[132:135]\n"
      "v_mfma_f32_16x16x32_bf16 v[132:135], a[236:239], v[60:63], v[132:135]\n"
      "s_memrealtime s[84:85]\n"
      "v_mfma_f32_16x16x32_bf16 v[132:135], a[0:3], v[64:67], v[132:135]\n"
      "v_cvt_pk_bf16_f32 v214, v136, v137\n"
      "v_mfma_f32_16x16x32_bf16 v[132:135], a[4:7], v[68:71], v[132:135]\n"
      "v_cvt_pk_bf16_f32 v215, v138, v139\n"
      "v_mfma_f32_16x16x32_bf16 v[132:135], a[8:11], v[84:87], v[132:135]\n"
      "v_mfma_f32_16x16x32_bf16 v[132:135], a[12:15], v[88:91], v[132:135]\n"
      "s_add_u32 s83, s83, s86\n"
      "v_mfma_f32_16x16x32_bf16 v[132:135], a[16:19], v[92:95], v[132:135]\n"
      "v_mfma_f32_16x16x32_bf16 v[132:135], a[20:23], v[108:111], v[132:135]\n"
      "v_mfma_f32_16x16x32_bf16 v[132:135], v[140:143], v[112:115], v[132:135]\n"
      "s_waitcnt lgkmcnt(0)\n"
      "s_sub_u32 s84, s84, s83\n"
      "v_mfma_f32_16x16x32_bf16 v[132:135], v[156:159], v[116:119], v[132:135]\n"
      "s_cmp_lt_i32 s84, 0\n"
      "s_cselect_b64 vcc, -1, 0\n"
      "s_waitcnt vmcnt(24)\n"
      "s_barrier\n"
      "v_mfma_f32_16x16x32_bf16 v[136:139], v[160:163], v[36:39], v[20:23]\n"
      "ds_read_b128 v[36:39], v232 offset:24576\n"
      "ds_write_b64 v245, v[214:215] offset:8\n"
      "v_mfma_f32_16x16x32_bf16 v[136:139], v[164:167], v[40:43], v[136:139]\n"
      "ds_read_b128 v[40:43], v233 offset:24576\n"
      "s_lshl_b32 s79, s93, 1\n"
      "v_mfma_f32_16x16x32_bf16 v[136:139], v[180:183], v[44:47], v[136:139]\n"
      "ds_read_b128 v[44:47], v234 offset:24576\n"
      "v_mad_i32_i24 v252, v251, s79, v252\n"
      "v_mfma_f32_16x16x32_bf16 v[136:139], v[184:187], v[60:63], v[136:139]\n"
      "ds_read_b128 v[60:63], v235 offset:24576\n"
      "v_cvt_pk_bf16_f32 v208, v132, v133\n"
      "v_mfma_f32_16x16x32_bf16 v[136:139], v[188:191], v[64:67], v[136:139]\n"
      "ds_read_b128 v[64:67], v232 offset:32768\n"
      "v_cvt_pk_bf16_f32 v209, v134, v135\n"
      "v_mfma_f32_16x16x32_bf16 v[136:139], v[204:207], v[68:71], v[136:139]\n"
      "ds_read_b128 v[68:71], v233 offset:32768\n"
      "s_mul_i32 s79, s79, s100\n"
      "v_mfma_f32_16x16x32_bf16 v[136:139], a[24:27], v[84:87], v[136:139]\n"
      "ds_read_b128 v[84:87], v234 offset:32768\n"
      "v_add_u32 v247, s79, v247\n"
      "v_mfma_f32_16x16x32_bf16 v[136:139], a[28:31], v[88:91], v[136:139]\n"
      "ds_read_b128 v[88:91], v235 offset:32768\n"
      "s_mul_i32 s79, s79, s101\n"
      "v_mfma_f32_16x16x32_bf16 v[136:139], a[32:35], v[92:95], v[136:139]\n"
      "ds_read_b128 v[92:95], v232 offset:40960\n"
      "v_add_u32 v253, s79, v253\n"
      "v_mfma_f32_16x16x32_bf16 v[136:139], a[36:39], v[108:111], v[136:139]\n"
      "ds_read_b128 v[108:111], v233 offset:40960\n"
      "v_mfma_f32_16x16x32_bf16 v[136:139], a[40:43], v[112:115], v[136:139]\n"
      "ds_read_b128 v[112:115], v234 offset:40960\n"
      "v_mfma_f32_16x16x32_bf16 v[136:139], a[44:47], v[116:119], v[136:139]\n"
      "ds_read_b128 v[116:119], v235 offset:40960\n"
      "s_waitcnt lgkmcnt(8)\n"
      "v_mfma_f32_16x16x32_bf16 v[132:135], a[240:243], v[36:39], v[0:3]\n"
      "ds_write_b64 v244, v[208:209] offset:128\n"
      "v_mfma_f32_16x16x32_bf16 v[132:135], a[244:247], v[40:43], v[132:135]\n"
      "s_add_u32 s54, s30, s36\n"
      "s_addc_u32 s55, s31, 0\n"
      "v_mfma_f32_16x16x32_bf16 v[132:135], a[248:251], v[44:47], v[132:135]\n"
      "s_cbranch_vccnz LT4\n"
      "LS4:\n"
      "v_mfma_f32_16x16x32_bf16 v[132:135], a[252:255], v[60:63], v[132:135]\n"
      "v_cvt_pk_bf16_f32 v210, v136, v137\n"
      "s_waitcnt lgkmcnt(5)\n"
      "v_mfma_f32_16x16x32_bf16 v[132:135], a[48:51], v[64:67], v[132:135]\n"
      "v_cvt_pk_bf16_f32 v211, v138, v139\n"
      "v_mfma_f32_16x16x32_bf16 v[132:135], a[52:55], v[68:71], v[132:135]\n"
      "s_mul_i32 s78, s95, s19\n"
      "v_mfma_f32_16x16x32_bf16 v[132:135], a[56:59], v[84:87], v[132:135]\n"
      "ds_read_b128 v[216:219], v246 offset:24576\n"
      "v_add_u32 v236, s78, v236\n"
      "v_mfma_f32_16x16x32_bf16 v[132:135], a[60:63], v[88:91], v[132:135]\n"
      "ds_read_b128 v[220:223], v246 offset:25600\n"
      "s_waitcnt lgkmcnt(3)\n"
      "v_mfma_f32_16x16x32_bf16 v[132:135], a[64:67], v[92:95], v[132:135]\n"
      "ds_read_b128 v[224:227], v246 offset:26624\n"
      "v_mfma_f32_16x16x32_bf16 v[132:135], a[68:71], v[108:111], v[132:135]\n"
      "ds_write_b64 v244, v[210:211] offset:136\n"
      "v_mfma_f32_16x16x32_bf16 v[132:135], a[72:75], v[112:115], v[132:135]\n"
      "s_mul_i32 s78, s78, s99\n"
      "v_mfma_f32_16x16x32_bf16 v[132:135], a[76:79], v[116:119], v[132:135]\n"
      "v_add_u32 v237, s78, v237\n"
      "v_mfma_f32_16x16x32_bf16 v[136:139], a[80:83], v[36:39], v[4:7]\n"
      "v_mfma_f32_16x16x32_bf16 v[136:139], a[84:87], v[40:43], v[136:139]\n"
      "v_mfma_f32_16x16x32_bf16 v[136:139], a[88:91], v[44:47], v[136:139]\n"
      "v_mfma_f32_16x16x32_bf16 v[136:139], a[92:95], v[60:63], v[136:139]\n"
      "v_cvt_pk_bf16_f32 v208, v132, v133\n"
      "v_mfma_f32_16x16x32_bf16 v[136:139], a[96:99], v[64:67], v[136:139]\n"
      "v_cvt_pk_bf16_f32 v209, v134, v135\n"
      "v_mfma_f32_16x16x32_bf16 v[136:139], a[100:103], v[68:71], v[136:139]\n"
      "s_waitcnt lgkmcnt(3)\n"
      "v_mfma_f32_16x16x32_bf16 v[136:139], a[104:107], v[84:87], v[136:139]\n"
      "global_store_dwordx4 v247, v[216:219], s[54:55] offset:0 sc1\n"
      "v_mfma_f32_16x16x32_bf16 v[136:139], a[108:111], v[88:91], v[136:139]\n"
      "s_add_u32 s52, s20, s38\n"
      "s_addc_u32 s53, s21, 0\n"
      "v_mfma_f32_16x16x32_bf16 v[136:139], a[112:115], v[92:95], v[136:139]\n"
      "s_add_u32 m0, s17, 0x0\n"
      "v_mfma_f32_16x16x32_bf16 v[136:139], a[116:119], v[108:111], v[136:139]\n"
      "ds_write_b64 v244, v[208:209] offset:12288\n"
      "v_mfma_f32_16x16x32_bf16 v[136:139], a[120:123], v[112:115], v[136:139]\n"
      "global_load_lds_dwordx4 v236, s[52:53] offset:0 nt\n"
      "v_mfma_f32_16x16x32_bf16 v[136:139], a[124:127], v[116:119], v[136:139]\n"
      "v_mfma_f32_16x16x32_bf16 v[132:135], a[128:131], v[36:39], v[8:11]\n"
      "v_mfma_f32_16x16x32_bf16 v[132:135], a[132:135], v[40:43], v[132:135]\n"
      "v_mfma_f32_16x16x32_bf16 v[132:135], a[136:139], v[44:47], v[132:135]\n"
      "v_mfma_f32_16x16x32_bf16 v[132:135], a[140:143], v[60:63], v[132:135]\n"
      "v_cvt_pk_bf16_f32 v210, v136, v137\n"
      "v_mfma_f32_16x16x32_bf16 v[132:135], a[144:147], v[64:67], v[132:135]\n"
      "v_cvt_pk_bf16_f32 v211, v138, v139\n"
      "v_mfma_f32_16x16x32_bf16 v[132:135], a[148:151], v[68:71], v[132:135]\n"
      "v_mfma_f32_16x16x32_bf16 v[132:135], a[152:155], v[84:87], v[132:135]\n"
      "s_waitcnt lgkmcnt(3)\n"
      "v_mfma_f32_16x16x32_bf16 v[132:135], a[156:159], v[88:91], v[132:135]\n"
      "global_store_dwordx4 v252, v[220:223], s[54:55] offset:0 sc1\n"
      "v_mfma_f32_16x16x32_bf16 v[132:135], a[160:163], v[92:95], v[132:135]\n"
      "s_add_u32 m0, s17, 0x1f00\n"
      "v_mfma_f32_16x16x32_bf16 v[132:135], a[164:167], v[108:111], v[132:135]\n"
      "ds_write_b64 v244, v[210:211] offset:12296\n"
      "v_mfma_f32_16x16x32_bf16 v[132:135], a[168:171], v[112:115], v[132:135]\n"
      "global_load_lds_dwordx4 v236, s[52:53] offset:256 nt\n"
      "v_mfma_f32_16x16x32_bf16 v[132:135], a[172:175], v[116:119], v[132:135]\n"
      "v_mfma_f32_16x16x32_bf16 v[136:139], a[176:179], v[36:39], v[12:15]\n"
      "s_cbranch_vccnz LT5\n"
      "LS5:\n"
      "v_mfma_f32_16x16x32_bf16 v[136:139], a[180:183], v[40:43], v[136:139]\n"
      "v_mfma_f32_16x16x32_bf16 v[136:139], a[184:187], v[44:47], v[136:139]\n"
      "v_mfma_f32_16x16x32_bf16 v[136:139], a[188:191], v[60:63], v[136:139]\n"
      "v_cvt_pk_bf16_f32 v212, v132, v133\n"
      "v_mfma_f32_16x16x32_bf16 v[136:139], a[192:195], v[64:67], v[136:139]\n"
      "v_cvt_pk_bf16_f32 v213, v134, v135\n"
      "v_mfma_f32_16x16x32_bf16 v[136:139], a[196:199], v[68:71], v[136:139]\n"
      "v_mfma_f32_16x16x32_bf16 v[136:139], a[200:203], v[84:87], v[136:139]\n"
      "v_mfma_f32_16x16x32_bf16 v[136:139], a[204:207], v[88:91], v[136:139]\n"
      "s_waitcnt lgkmcnt(3)\n"
      "v_mfma_f32_16x16x32_bf16 v[136:139], a[208:211], v[92:95], v[136:139]\n"
      "global_store_dwordx4 v253, v[224:227], s[54:55] offset:0 sc1\n"
      "v_mfma_f32_16x16x32_bf16 v[136:139], a[212:215], v[108:111], v[136:139]\n"
      "ds_write_b64 v245, v[212:213] offset:12288\n"
      "v_mfma_f32_16x16x32_bf16 v[136:139], a[216:219], v[112:115], v[136:139]\n"
      "s_add_u32 m0, s17, 0x3e00\n"
      "v_mfma_f32_16x16x32_bf16 v[136:139], a[220:223], v[116:119], v[136:139]\n"
      "global_load_lds_dwordx4 v236, s[52:53] offset:512 nt\n"
      "v_mfma_f32_16x16x32_bf16 v[132:135], a[224:227], v[36:39], v[16:19]\n"
      "v_mfma_f32_16x16x32_bf16 v[132:135], a[228:231], v[40:43], v[132:135]\n"
      "v_mfma_f32_16x16x32_bf16 v[132:135], a[232:235], v[44:47], v[132:135]\n"
      "v_mfma_f32_16x16x32_bf16 v[132:135], a[236:239], v[60:63], v[132:135]\n"
      "v_cvt_pk_bf16_f32 v214, v136, v137\n"
      "v_mfma_f32_16x16x32_bf16 v[132:135], a[0:3], v[64:67], v[132:135]\n"
      "v_cvt_pk_bf16_f32 v215, v138, v139\n"
      "v_mfma_f32_16x16x32_bf16 v[132:135], a[4:7], v[68:71], v[132:135]\n"
      "v_mfma_f32_16x16x32_bf16 v[132:135], a[8:11], v[84:87], v[132:135]\n"
      "v_mfma_f32_16x16x32_bf16 v[132:135], a[12:15], v[88:91], v[132:135]\n"
      "v_mfma_f32_16x16x32_bf16 v[132:135], a[16:19], v[92:95], v[132:135]\n"
      "v_mfma_f32_16x16x32_bf16 v[132:135], a[20:23], v[108:111], v[132:135]\n"
      "ds_write_b64 v245, v[214:215] offset:12296\n"
      "v_mfma_f32_16x16x32_bf16 v[132:135], v[140:143], v[112:115], v[132:135]\n"
      "v_mfma_f32_16x16x32_bf16 v[132:135], v[156:159], v[116:119], v[132:135]\n"
      "s_waitcnt lgkmcnt(4)\n"
      "s_barrier\n"
      "v_mfma_f32_16x16x32_bf16 v[136:139], v[160:163], v[36:39], v[20:23]\n"
      "ds_read_b128 v[36:39], v232 offset:24832\n"
      "v_mfma_f32_16x16x32_bf16 v[136:139], v[164:167], v[40:43], v[136:139]\n"
      "ds_read_b128 v[40:43], v233 offset:24832\n"
      "v_mfma_f32_16x16x32_bf16 v[136:139], v[180:183], v[44:47], v[136:139]\n"
      "ds_read_b128 v[44:47], v234 offset:24832\n"
      "v_mfma_f32_16x16x32_bf16 v[136:139], v[184:187], v[60:63], v[136:139]\n"
      "ds_read_b128 v[60:63], v235 offset:24832\n"
      "v_cvt_pk_bf16_f32 v208, v132, v133\n"
      "v_mfma_f32_16x16x32_bf16 v[136:139], v[188:191], v[64:67], v[136:139]\n"
      "LPD12:\n"
      "s_cbranch_execz LPD15\n"
      "ds_read_b128 v[64:67], v232 offset:33024\n"
      "v_cvt_pk_bf16_f32 v209, v134, v135\n"
      "v_mfma_f32_16x16x32_bf16 v[136:139], v[204:207], v[68:71], v[136:139]\n"
      "ds_read_b128 v[68:71], v233 offset:33024\n"
      "v_mfma_f32_16x16x32_bf16 v[136:139], a[24:27], v[84:87], v[136:139]\n"
      "ds_read_b128 v[84:87], v234 offset:33024\n"
      "v_mfma_f32_16x16x32_bf16 v[136:139], a[28:31], v[88:91], v[136:139]\n"
      "ds_read_b128 v[88:91], v235 offset:33024\n"
      "v_mfma_f32_16x16x32_bf16 v[136:139], a[32:35], v[92:95], v[136:139]\n"
      "ds_read_b128 v[92:95], v232 offset:41216\n"
      "v_mfma_f32_16x16x32_bf16 v[136:139], a[36:39], v[108:111], v[136:139]\n"
      "ds_read_b128 v[108:111], v233 offset:41216\n"
      "ds_write_b64 v244, v[208:209] offset:12416\n"
      "v_mfma_f32_16x16x32_bf16 v[136:139], a[40:43], v[112:115], v[136:139]\n"
      "ds_read_b128 v[112:115], v234 offset:41216\n"
      "v_mfma_f32_16x16x32_bf16 v[136:139], a[44:47], v[116:119], v[136:139]\n"
      "ds_read_b128 v[116:119], v235 offset:41216\n"
      "s_waitcnt lgkmcnt(9)\n"
      "v_mfma_f32_16x16x32_bf16 v[132:135], a[240:243], v[36:39], v[0:3]\n"
      "v_add_u32 v232, 0xc000, v232\n"
      "s_cbranch_vccnz LT6\n"
      "LS6:\n"
      "v_mfma_f32_16x16x32_bf16 v[132:135], a[244:247], v[40:43], v[132:135]\n"
      "v_add_u32 v233, 0xc000, v233\n"
      "v_mfma_f32_16x16x32_bf16 v[132:135], a[248:251], v[44:47], v[132:135]\n"
      "v_add_u32 v234, 0xc000, v234\n"
      "v_mfma_f32_16x16x32_bf16 v[132:135], a[252:255], v[60:63], v[132:135]\n"
      "v_add_u32 v235, 0xc000, v235\n"
      "s_waitcnt lgkmcnt(5)\n"
      "v_mfma_f32_16x16x32_bf16 v[132:135], a[48:51], v[64:67], v[132:135]\n"
      "v_cvt_pk_bf16_f32 v210, v136, v137\n"
      "v_mfma_f32_16x16x32_bf16 v[132:135], a[52:55], v[68:71], v[132:135]\n"
      "v_cvt_pk_bf16_f32 v211, v138, v139\n"
      "v_mfma_f32_16x16x32_bf16 v[132:135], a[56:59], v[84:87], v[132:135]\n"
      "ds_read_b128 v[216:219], v246 offset:0\n"
      "v_mfma_f32_16x16x32_bf16 v[132:135], a[60:63], v[88:91], v[132:135]\n"
      "ds_read_b128 v[220:223], v246 offset:1024\n"
      "s_waitcnt lgkmcnt(2)\n"
      "v_mfma_f32_16x16x32_bf16 v[132:135], a[64:67], v[92:95], v[132:135]\n"
      "ds_read_b128 v[224:227], v246 offset:2048\n"
      "v_mfma_f32_16x16x32_bf16 v[132:135], a[68:71], v[108:111], v[132:135]\n"
      "v_lshlrev_b32 v0, 16, v120\n"
      "v_mfma_f32_16x16x32_bf16 v[132:135], a[72:75], v[112:115], v[132:135]\n"
      "ds_write_b64 v244, v[210:211] offset:12424\n"
      "v_mfma_f32_16x16x32_bf16 v[132:135], a[76:79], v[116:119], v[132:135]\n"
      "v_and_b32 v1, 0xffff0000, v120\n"
      "v_mfma_f32_16x16x32_bf16 v[136:139], a[80:83], v[36:39], v[4:7]\n"
      "v_lshlrev_b32 v2, 16, v121\n"
      "v_mfma_f32_16x16x32_bf16 v[136:139], a[84:87], v[40:43], v[136:139]\n"
      "v_and_b32 v3, 0xffff0000, v121\n"
      "v_mfma_f32_16x16x32_bf16 v[136:139], a[88:91], v[44:47], v[136:139]\n"
      "v_mfma_f32_16x16x32_bf16 v[136:139], a[92:95], v[60:63], v[136:139]\n"
      "v_cvt_pk_bf16_f32 v208, v132, v133\n"
      "v_mfma_f32_16x16x32_bf16 v[136:139], a[96:99], v[64:67], v[136:139]\n"
      "v_cvt_pk_bf16_f32 v209, v134, v135\n"
      "v_mfma_f32_16x16x32_bf16 v[136:139], a[100:103], v[68:71], v[136:139]\n"
      "s_waitcnt lgkmcnt(3)\n"
      "v_mfma_f32_16x16x32_bf16 v[136:139], a[104:107], v[84:87], v[136:139]\n"
      "global_store_dwordx4 v247, v[216:219], s[54:55] offset:768 sc1\n"
      "v_mfma_f32_16x16x32_bf16 v[136:139], a[108:111], v[88:91], v[136:139]\n"
      "s_add_u32 m0, s17, 0x1000\n"
      "v_mfma_f32_16x16x32_bf16 v[136:139], a[112:115], v[92:95], v[136:139]\n"
      "global_load_lds_dwordx4 v237, s[52:53] offset:0 nt\n"
      "v_mfma_f32_16x16x32_bf16 v[136:139], a[116:119], v[108:111], v[136:139]\n"
      "ds_write_b64 v244, v[208:209] offset:24576\n"
      "v_mfma_f32_16x16x32_bf16 v[136:139], a[120:123], v[112:115], v[136:139]\n"
      "v_lshlrev_b32 v4, 16, v122\n"
      "v_mfma_f32_16x16x32_bf16 v[136:139], a[124:127], v[116:119], v[136:139]\n"
      "v_and_b32 v5, 0xffff0000, v122\n"
      "v_mfma_f32_16x16x32_bf16 v[132:135], a[128:131], v[36:39], v[8:11]\n"
      "v_lshlrev_b32 v6, 16, v123\n"
      "v_mfma_f32_16x16x32_bf16 v[132:135], a[132:135], v[40:43], v[132:135]\n"
      "v_and_b32 v7, 0xffff0000, v123\n"
      "v_mfma_f32_16x16x32_bf16 v[132:135], a[136:139], v[44:47], v[132:135]\n"
      "v_mfma_f32_16x16x32_bf16 v[132:135], a[140:143], v[60:63], v[132:135]\n"
      "v_cvt_pk_bf16_f32 v210, v136, v137\n"
      "v_mfma_f32_16x16x32_bf16 v[132:135], a[144:147], v[64:67], v[132:135]\n"
      "v_cvt_pk_bf16_f32 v211, v138, v139\n"
      "v_mfma_f32_16x16x32_bf16 v[132:135], a[148:151], v[68:71], v[132:135]\n"
      "v_lshlrev_b32 v8, 16, v124\n"
      "v_mfma_f32_16x16x32_bf16 v[132:135], a[152:155], v[84:87], v[132:135]\n"
      "s_waitcnt lgkmcnt(3)\n"
      "v_mfma_f32_16x16x32_bf16 v[132:135], a[156:159], v[88:91], v[132:135]\n"
      "global_store_dwordx4 v252, v[220:223], s[54:55] offset:768 sc1\n"
      "v_mfma_f32_16x16x32_bf16 v[132:135], a[160:163], v[92:95], v[132:135]\n"
      "s_add_u32 m0, s17, 0x2f00\n"
      "v_mfma_f32_16x16x32_bf16 v[132:135], a[164:167], v[108:111], v[132:135]\n"
      "ds_write_b64 v244, v[210:211] offset:24584\n"
      "v_mfma_f32_16x16x32_bf16 v[132:135], a[168:171], v[112:115], v[132:135]\n"
      "global_load_lds_dwordx4 v237, s[52:53] offset:256 nt\n"
      "v_mfma_f32_16x16x32_bf16 v[132:135], a[172:175], v[116:119], v[132:135]\n"
      "v_and_b32 v9, 0xffff0000, v124\n"
      "v_mfma_f32_16x16x32_bf16 v[136:139], a[176:179], v[36:39], v[12:15]\n"
      "v_lshlrev_b32 v10, 16, v125\n"
      "v_mfma_f32_16x16x32_bf16 v[136:139], a[180:183], v[40:43], v[136:139]\n"
      "v_and_b32 v11, 0xffff0000, v125\n"
      "v_mfma_f32_16x16x32_bf16 v[136:139], a[184:187], v[44:47], v[136:139]\n"
      "s_cbranch_vccnz LT7\n"
      "LS7:\n"
      "v_mfma_f32_16x16x32_bf16 v[136:139], a[188:191], v[60:63], v[136:139]\n"
      "v_cvt_pk_bf16_f32 v212, v132, v133\n"
      "v_mfma_f32_16x16x32_bf16 v[136:139], a[192:195], v[64:67], v[136:139]\n"
      "v_cvt_pk_bf16_f32 v213, v134, v135\n"
      "v_mfma_f32_16x16x32_bf16 v[136:139], a[196:199], v[68:71], v[136:139]\n"
      "v_lshlrev_b32 v12, 16, v126\n"
      "v_mfma_f32_16x16x32_bf16 v[136:139], a[200:203], v[84:87], v[136:139]\n"
      "v_and_b32 v13, 0xffff0000, v126\n"
      "v_mfma_f32_16x16x32_bf16 v[136:139], a[204:207], v[88:91], v[136:139]\n"
      "s_waitcnt lgkmcnt(3)\n"
      "v_mfma_f32_16x16x32_bf16 v[136:139], a[208:211], v[92:95], v[136:139]\n"
      "global_store_dwordx4 v253, v[224:227], s[54:55] offset:768 sc1\n"
      "v_mfma_f32_16x16x32_bf16 v[136:139], a[212:215], v[108:111], v[136:139]\n"
      "ds_write_b64 v245, v[212:213] offset:24576\n"
      "v_mfma_f32_16x16x32_bf16 v[136:139], a[216:219], v[112:115], v[136:139]\n"
      "s_add_u32 m0, s17, 0x4e00\n"
      "v_mfma_f32_16x16x32_bf16 v[136:139], a[220:223], v[116:119], v[136:139]\n"
      "global_load_lds_dwordx4 v237, s[52:53] offset:512 nt\n"
      "v_mfma_f32_16x16x32_bf16 v[132:135], a[224:227], v[36:39], v[16:19]\n"
      "v_lshlrev_b32 v14, 16, v127\n"
      "v_mfma_f32_16x16x32_bf16 v[132:135], a[228:231], v[40:43], v[132:135]\n"
      "v_and_b32 v15, 0xffff0000, v127\n"
      "v_mfma_f32_16x16x32_bf16 v[132:135], a[232:235], v[44:47], v[132:135]\n"
      "v_mfma_f32_16x16x32_bf16 v[132:135], a[236:239], v[60:63], v[132:135]\n"
      "s_memrealtime s[84:85]\n"
      "v_mfma_f32_16x16x32_bf16 v[132:135], a[0:3], v[64:67], v[132:135]\n"
      "v_cvt_pk_bf16_f32 v214, v136, v137\n"
      "v_mfma_f32_16x16x32_bf16 v[132:135], a[4:7], v[68:71], v[132:135]\n"
      "v_cvt_pk_bf16_f32 v215, v138, v139\n"
      "v_mfma_f32_16x16x32_bf16 v[132:135], a[8:11], v[84:87], v[132:135]\n"
      "v_lshlrev_b32 v16, 16, v128\n"
      "v_mfma_f32_16x16x32_bf16 v[132:135], a[12:15], v[88:91], v[132:135]\n"
      "s_add_u32 s83, s83, s86\n"
      "v_mfma_f32_16x16x32_bf16 v[132:135], a[16:19], v[92:95], v[132:135]\n"
      "v_and_b32 v17, 0xffff0000, v128\n"
      "v_mfma_f32_16x16x32_bf16 v[132:135], a[20:23], v[108:111], v[132:135]\n"
      "v_lshlrev_b32 v18, 16, v129\n"
      "v_mfma_f32_16x16x32_bf16 v[132:135], v[140:143], v[112:115], v[132:135]\n"
      "s_waitcnt lgkmcnt(0)\n"
      "s_sub_u32 s84, s84, s83\n"
      "v_mfma_f32_16x16x32_bf16 v[132:135], v[156:159], v[116:119], v[132:135]\n"
      "s_cmp_lt_i32 s84, 0\n"
      "s_cselect_b64 vcc, -1, 0\n"
      "s_waitcnt vmcnt(24)\n"
      "s_barrier\n"
      "v_mfma_f32_16x16x32_bf16 v[136:139], v[160:163], v[36:39], v[20:23]\n"
      "ds_read_b128 v[36:39], v232 offset:0\n"
      "ds_write_b64 v245, v[214:215] offset:24584\n"
      "v_mfma_f32_16x16x32_bf16 v[136:139], v[164:167], v[40:43], v[136:139]\n"
      "ds_read_b128 v[40:43], v233 offset:0\n"
      "v_mfma_f32_16x16x32_bf16 v[136:139], v[180:183], v[44:47], v[136:139]\n"
      "ds_read_b128 v[44:47], v234 offset:0\n"
      "v_mfma_f32_16x16x32_bf16 v[136:139], v[184:187], v[60:63], v[136:139]\n"
      "ds_read_b128 v[60:63], v235 offset:0\n"
      "v_cvt_pk_bf16_f32 v208, v132, v133\n"
      "v_mfma_f32_16x16x32_bf16 v[136:139], v[188:191], v[64:67], v[136:139]\n"
      "ds_read_b128 v[64:67], v232 offset:8192\n"
      "v_cvt_pk_bf16_f32 v209, v134, v135\n"
      "v_mfma_f32_16x16x32_bf16 v[136:139], v[204:207], v[68:71], v[136:139]\n"
      "ds_read_b128 v[68:71], v233 offset:8192\n"
      "v_mfma_f32_16x16x32_bf16 v[136:139], a[24:27], v[84:87], v[136:139]\n"
      "ds_read_b128 v[84:87], v234 offset:8192\n"
      "v_mfma_f32_16x16x32_bf16 v[136:139], a[28:31], v[88:91], v[136:139]\n"
      "ds_read_b128 v[88:91], v235 offset:8192\n"
      "v_mfma_f32_16x16x32_bf16 v[136:139], a[32:35], v[92:95], v[136:139]\n"
      "ds_read_b128 v[92:95], v232 offset:16384\n"
      "v_mfma_f32_16x16x32_bf16 v[136:139], a[36:39], v[108:111], v[136:139]\n"
      "ds_read_b128 v[108:111], v233 offset:16384\n"
      "v_mfma_f32_16x16x32_bf16 v[136:139], a[40:43], v[112:115], v[136:139]\n"
      "ds_read_b128 v[112:115], v234 offset:16384\n"
      "v_mfma_f32_16x16x32_bf16 v[136:139], a[44:47], v[116:119], v[136:139]\n"
      "ds_read_b128 v[116:119], v235 offset:16384\n"
      "s_waitcnt lgkmcnt(8)\n"
      "v_mfma_f32_16x16x32_bf16 v[132:135], a[240:243], v[36:39], v[0:3]\n"
      "ds_write_b64 v244, v[208:209] offset:24704\n"
      "s_cbranch_vccnz LT8\n"
      "LS8:\n"
      "v_mfma_f32_16x16x32_bf16 v[132:135], a[244:247], v[40:43], v[132:135]\n"
      "v_and_b32 v19, 0xffff0000, v129\n"
      "v_mfma_f32_16x16x32_bf16 v[132:135], a[248:251], v[44:47], v[132:135]\n"
      "v_lshlrev_b32 v20, 16, v130\n"
      "v_mfma_f32_16x16x32_bf16 v[132:135], a[252:255], v[60:63], v[132:135]\n"
      "v_and_b32 v21, 0xffff0000, v130\n"
      "s_waitcnt lgkmcnt(5)\n"
      "v_mfma_f32_16x16x32_bf16 v[132:135], a[48:51], v[64:67], v[132:135]\n"
      "v_lshlrev_b32 v22, 16, v131\n"
      "v_mfma_f32_16x16x32_bf16 v[132:135], a[52:55], v[68:71], v[132:135]\n"
      "v_and_b32 v23, 0xffff0000, v131\n"
      "v_mfma_f32_16x16x32_bf16 v[132:135], a[56:59], v[84:87], v[132:135]\n"
      "v_cvt_pk_bf16_f32 v210, v136, v137\n"
      "v_mfma_f32_16x16x32_bf16 v[132:135], a[60:63], v[88:91], v[132:135]\n"
      "v_cvt_pk_bf16_f32 v211, v138, v139\n"
      "s_waitcnt lgkmcnt(1)\n"
      "v_mfma_f32_16x16x32_bf16 v[132:135], a[64:67], v[92:95], v[132:135]\n"
      "s_add_u32 s54, s44, s36\n"
      "s_addc_u32 s55, s45, 0\n"
      "v_mfma_f32_16x16x32_bf16 v[132:135], a[68:71], v[108:111], v[132:135]\n"
      "ds_read_b128 v[216:219], v246 offset:12288\n"
      "v_mfma_f32_16x16x32_bf16 v[132:135], a[72:75], v[112:115], v[132:135]\n"
      "ds_read_b128 v[220:223], v246 offset:13312\n"
      "v_mfma_f32_16x16x32_bf16 v[132:135], a[76:79], v[116:119], v[132:135]\n"
      "ds_read_b128 v[224:227], v246 offset:14336\n"
      "v_mfma_f32_16x16x32_bf16 v[136:139], a[80:83], v[36:39], v[4:7]\n"
      "ds_write_b64 v244, v[210:211] offset:24712\n"
      "v_mfma_f32_16x16x32_bf16 v[136:139], a[84:87], v[40:43], v[136:139]\n"
      "v_mfma_f32_16x16x32_bf16 v[136:139], a[88:91], v[44:47], v[136:139]\n"
      "v_mfma_f32_16x16x32_bf16 v[136:139], a[92:95], v[60:63], v[136:139]\n"
      "v_cvt_pk_bf16_f32 v208, v132, v133\n"
      "v_mfma_f32_16x16x32_bf16 v[136:139], a[96:99], v[64:67], v[136:139]\n"
      "v_cvt_pk_bf16_f32 v209, v134, v135\n"
      "v_mfma_f32_16x16x32_bf16 v[136:139], a[100:103], v[68:71], v[136:139]\n"
      "s_waitcnt lgkmcnt(3)\n"
      "v_mfma_f32_16x16x32_bf16 v[136:139], a[104:107], v[84:87], v[136:139]\n"
      "global_store_dwordx4 v247, v[216:219], s[54:55] offset:0 sc1\n"
      "v_mfma_f32_16x16x32_bf16 v[136:139], a[108:111], v[88:91], v[136:139]\n"
      "s_add_u32 s52, s42, s38\n"
      "s_addc_u32 s53, s43, 0\n"
      "v_mfma_f32_16x16x32_bf16 v[136:139], a[112:115], v[92:95], v[136:139]\n"
      "s_add_u32 m0, s17, 0x6000\n"
      "v_mfma_f32_16x16x32_bf16 v[136:139], a[116:119], v[108:111], v[136:139]\n"
      "ds_write_b64 v244, v[208:209] offset:0\n"
      "v_mfma_f32_16x16x32_bf16 v[136:139], a[120:123], v[112:115], v[136:139]\n"
      "global_load_lds_dwordx4 v236, s[52:53] offset:0 nt\n"
      "v_mfma_f32_16x16x32_bf16 v[136:139], a[124:127], v[116:119], v[136:139]\n"
      "v_mfma_f32_16x16x32_bf16 v[132:135], a[128:131], v[36:39], v[8:11]\n"
      "v_mfma_f32_16x16x32_bf16 v[132:135], a[132:135], v[40:43], v[132:135]\n"
      "v_mfma_f32_16x16x32_bf16 v[132:135], a[136:139], v[44:47], v[132:135]\n"
      "v_mfma_f32_16x16x32_bf16 v[132:135], a[140:143], v[60:63], v[132:135]\n"
      "v_cvt_pk_bf16_f32 v210, v136, v137\n"
      "v_mfma_f32_16x16x32_bf16 v[132:135], a[144:147], v[64:67], v[132:135]\n"
      "v_cvt_pk_bf16_f32 v211, v138, v139\n"
      "v_mfma_f32_16x16x32_bf16 v[132:135], a[148:151], v[68:71], v[132:135]\n"
      "v_mfma_f32_16x16x32_bf16 v[132:135], a[152:155], v[84:87], v[132:135]\n"
      "s_waitcnt lgkmcnt(3)\n"
      "v_mfma_f32_16x16x32_bf16 v[132:135], a[156:159], v[88:91], v[132:135]\n"
      "global_store_dwordx4 v252, v[220:223], s[54:55] offset:0 sc1\n"
      "v_mfma_f32_16x16x32_bf16 v[132:135], a[160:163], v[92:95], v[132:135]\n"
      "s_add_u32 m0, s17, 0x7f00\n"
      "v_mfma_f32_16x16x32_bf16 v[132:135], a[164:167], v[108:111], v[132:135]\n"
      "ds_write_b64 v244, v[210:211] offset:8\n"
      "v_mfma_f32_16x16x32_bf16 v[132:135], a[168:171], v[112:115], v[132:135]\n"
      "global_load_lds_dwordx4 v236, s[52:53] offset:256 nt\n"
      "v_mfma_f32_16x16x32_bf16 v[132:135], a[172:175], v[116:119], v[132:135]\n"
      "v_mfma_f32_16x16x32_bf16 v[136:139], a[176:179], v[36:39], v[12:15]\n"
      "s_cbranch_vccnz LT9\n"
      "LS9:\n"
      "v_mfma_f32_16x16x32_bf16 v[136:139], a[180:183], v[40:43], v[136:139]\n"
      "v_mfma_f32_16x16x32_bf16 v[136:139], a[184:187], v[44:47], v[136:139]\n"
      "v_mfma_f32_16x16x32_bf16 v[136:139], a[188:191], v[60:63], v[136:139]\n"
      "v_cvt_pk_bf16_f32 v212, v132, v133\n"
      "v_mfma_f32_16x16x32_bf16 v[136:139], a[192:195], v[64:67], v[136:139]\n"
      "v_cvt_pk_bf16_f32 v213, v134, v135\n"
      "v_mfma_f32_16x16x32_bf16 v[136:139], a[196:199], v[68:71], v[136:139]\n"
      "v_mfma_f32_16x16x32_bf16 v[136:139], a[200:203], v[84:87], v[136:139]\n"
      "v_mfma_f32_16x16x32_bf16 v[136:139], a[204:207], v[88:91], v[136:139]\n"
      "s_waitcnt lgkmcnt(3)\n"
      "v_mfma_f32_16x16x32_bf16 v[136:139], a[208:211], v[92:95], v[136:139]\n"
      "global_store_dwordx4 v253, v[224:227], s[54:55] offset:0 sc1\n"
      "v_mfma_f32_16x16x32_bf16 v[136:139], a[212:215], v[108:111], v[136:139]\n"
      "ds_write_b64 v245, v[212:213] offset:0\n"
      "v_mfma_f32_16x16x32_bf16 v[136:139], a[216:219], v[112:115], v[136:139]\n"
      "s_add_u32 m0, s17, 0x9e00\n"
      "v_mfma_f32_16x16x32_bf16 v[136:139], a[220:223], v[116:119], v[136:139]\n"
      "global_load_lds_dwordx4 v236, s[52:53] offset:512 nt\n"
      "v_mfma_f32_16x16x32_bf16 v[132:135], a[224:227], v[36:39], v[16:19]\n"
      "v_mfma_f32_16x16x32_bf16 v[132:135], a[228:231], v[40:43], v[132:135]\n"
      "v_mfma_f32_16x16x32_bf16 v[132:135], a[232:235], v[44:47], v[132:135]\n"
      "v_mfma_f32_16x16x32_bf16 v[132:135], a[236:239], v[60:63], v[132:135]\n"
      "v_cvt_pk_bf16_f32 v214, v136, v137\n"
      "v_mfma_f32_16x16x32_bf16 v[132:135], a[0:3], v[64:67], v[132:135]\n"
      "v_cvt_pk_bf16_f32 v215, v138, v139\n"
      "v_mfma_f32_16x16x32_bf16 v[132:135], a[4:7], v[68:71], v[132:135]\n"
      "v_mfma_f32_16x16x32_bf16 v[132:135], a[8:11], v[84:87], v[132:135]\n"
      "v_mfma_f32_16x16x32_bf16 v[132:135], a[12:15], v[88:91], v[132:135]\n"
      "v_mfma_f32_16x16x32_bf16 v[132:135], a[16:19], v[92:95], v[132:135]\n"
      "v_mfma_f32_16x16x32_bf16 v[132:135], a[20:23], v[108:111], v[132:135]\n"
      "ds_write_b64 v245, v[214:215] offset:8\n"
      "v_mfma_f32_16x16x32_bf16 v[132:135], v[140:143], v[112:115], v[132:135]\n"
      "v_mfma_f32_16x16x32_bf16 v[132:135], v[156:159], v[116:119], v[132:135]\n"
      "s_waitcnt lgkmcnt(4)\n"
      "s_barrier\n"
      "v_mfma_f32_16x16x32_bf16 v[136:139], v[160:163], v[36:39], v[20:23]\n"
      "ds_read_b128 v[36:39], v232 offset:256\n"
      "v_mfma_f32_16x16x32_bf16 v[136:139], v[164:167], v[40:43], v[136:139]\n"
      "ds_read_b128 v[40:43], v233 offset:256\n"
      "v_mfma_f32_16x16x32_bf16 v[136:139], v[180:183], v[44:47], v[136:139]\n"
      "ds_read_b128 v[44:47], v234 offset:256\n"
      "v_mfma_f32_16x16x32_bf16 v[136:139], v[184:187], v[60:63], v[136:139]\n"
      "ds_read_b128 v[60:63], v235 offset:256\n"
      "v_cvt_pk_bf16_f32 v208, v132, v133\n"
      "v_mfma_f32_16x16x32_bf16 v[136:139], v[188:191], v[64:67], v[136:139]\n"
      "ds_read_b128 v[64:67], v232 offset:8448\n"
      "v_cvt_pk_bf16_f32 v209, v134, v135\n"
      "v_mfma_f32_16x16x32_bf16 v[136:139], v[204:207], v[68:71], v[136:139]\n"
      "ds_read_b128 v[68:71], v233 offset:8448\n"
      "v_mfma_f32_16x16x32_bf16 v[136:139], a[24:27], v[84:87], v[136:139]\n"
      "ds_read_b128 v[84:87], v234 offset:8448\n"
      "v_mfma_f32_16x16x32_bf16 v[136:139], a[28:31], v[88:91], v[136:139]\n"
      "ds_read_b128 v[88:91], v235 offset:8448\n"
      "v_mfma_f32_16x16x32_bf16 v[136:139], a[32:35], v[92:95], v[136:139]\n"
      "ds_read_b128 v[92:95], v232 offset:16640\n"
      "v_mfma_f32_16x16x32_bf16 v[136:139], a[36:39], v[108:111], v[136:139]\n"
      "ds_read_b128 v[108:111], v233 offset:16640\n"
      "v_mfma_f32_16x16x32_bf16 v[136:139], a[40:43], v[112:115], v[136:139]\n"
      "ds_read_b128 v[112:115], v234 offset:16640\n"
      "v_mfma_f32_16x16x32_bf16 v[136:139], a[44:47], v[116:119], v[136:139]\n"
      "ds_read_b128 v[116:119], v235 offset:16640\n"
      "s_waitcnt lgkmcnt(8)\n"
      "v_mfma_f32_16x16x32_bf16 v[132:135], a[240:243], v[36:39], v[0:3]\n"
      "ds_write_b64 v244, v[208:209] offset:128\n"
      "v_mfma_f32_16x16x32_bf16 v[132:135], a[244:247], v[40:43], v[132:135]\n"
      "s_cbranch_vccnz LT10\n"
      "LS10:\n"
      "v_mfma_f32_16x16x32_bf16 v[132:135], a[248:251], v[44:47], v[132:135]\n"
      "v_mfma_f32_16x16x32_bf16 v[132:135], a[252:255], v[60:63], v[132:135]\n"
      "v_cvt_pk_bf16_f32 v210, v136, v137\n"
      "s_waitcnt lgkmcnt(5)\n"
      "v_mfma_f32_16x16x32_bf16 v[132:135], a[48:51], v[64:67], v[132:135]\n"
      "v_cvt_pk_bf16_f32 v211, v138, v139\n"
      "v_mfma_f32_16x16x32_bf16 v[132:135], a[52:55], v[68:71], v[132:135]\n"
      "v_mfma_f32_16x16x32_bf16 v[132:135], a[56:59], v[84:87], v[132:135]\n"
      "ds_read_b128 v[216:219], v246 offset:24576\n"
      "v_mfma_f32_16x16x32_bf16 v[132:135], a[60:63], v[88:91], v[132:135]\n"
      "ds_read_b128 v[220:223], v246 offset:25600\n"
      "s_waitcnt lgkmcnt(3)\n"
      "v_mfma_f32_16x16x32_bf16 v[132:135], a[64:67], v[92:95], v[132:135]\n"
      "ds_read_b128 v[224:227], v246 offset:26624\n"
      "v_mfma_f32_16x16x32_bf16 v[132:135], a[68:71], v[108:111], v[132:135]\n"
      "ds_write_b64 v244, v[210:211] offset:136\n"
      "v_mfma_f32_16x16x32_bf16 v[132:135], a[72:75], v[112:115], v[132:135]\n"
      "v_mfma_f32_16x16x32_bf16 v[132:135], a[76:79], v[116:119], v[132:135]\n"
      "v_mfma_f32_16x16x32_bf16 v[136:139], a[80:83], v[36:39], v[4:7]\n"
      "v_mfma_f32_16x16x32_bf16 v[136:139], a[84:87], v[40:43], v[136:139]\n"
      "v_mfma_f32_16x16x32_bf16 v[136:139], a[88:91], v[44:47], v[136:139]\n"
      "v_mfma_f32_16x16x32_bf16 v[136:139], a[92:95], v[60:63], v[136:139]\n"
      "v_cvt_pk_bf16_f32 v208, v132, v133\n"
      "v_mfma_f32_16x16x32_bf16 v[136:139], a[96:99], v[64:67], v[136:139]\n"
      "v_cvt_pk_bf16_f32 v209, v134, v135\n"
      "v_mfma_f32_16x16x32_bf16 v[136:139], a[100:103], v[68:71], v[136:139]\n"
      "s_waitcnt lgkmcnt(3)\n"
      "v_mfma_f32_16x16x32_bf16 v[136:139], a[104:107], v[84:87], v[136:139]\n"
      "global_store_dwordx4 v247, v[216:219], s[54:55] offset:768 sc1\n"
      "v_mfma_f32_16x16x32_bf16 v[136:139], a[108:111], v[88:91], v[136:139]\n"
      "s_add_u32 m0, s17, 0x7000\n"
      "v_mfma_f32_16x16x32_bf16 v[136:139], a[112:115], v[92:95], v[136:139]\n"
      "global_load_lds_dwordx4 v237, s[52:53] offset:0 nt\n"
      "v_mfma_f32_16x16x32_bf16 v[136:139], a[116:119], v[108:111], v[136:139]\n"
      "ds_write_b64 v244, v[208:209] offset:12288\n"
      "v_mfma_f32_16x16x32_bf16 v[136:139], a[120:123], v[112:115], v[136:139]\n"
      "v_mfma_f32_16x16x32_bf16 v[136:139], a[124:127], v[116:119], v[136:139]\n"
      "v_mfma_f32_16x16x32_bf16 v[132:135], a[128:131], v[36:39], v[8:11]\n"
      "v_mfma_f32_16x16x32_bf16 v[132:135], a[132:135], v[40:43], v[132:135]\n"
      "v_mfma_f32_16x16x32_bf16 v[132:135], a[136:139], v[44:47], v[132:135]\n"
      "v_mfma_f32_16x16x32_bf16 v[132:135], a[140:143], v[60:63], v[132:135]\n"
      "v_cvt_pk_bf16_f32 v210, v136, v137\n"
      "v_mfma_f32_16x16x32_bf16 v[132:135], a[144:147], v[64:67], v[132:135]\n"
      "v_cvt_pk_bf16_f32 v211, v138, v139\n"
      "v_mfma_f32_16x16x32_bf16 v[132:135], a[148:151], v[68:71], v[132:135]\n"
      "v_mfma_f32_16x16x32_bf16 v[132:135], a[152:155], v[84:87], v[132:135]\n"
      "s_waitcnt lgkmcnt(3)\n"
      "v_mfma_f32_16x16x32_bf16 v[132:135], a[156:159], v[88:91], v[132:135]\n"
      "global_store_dwordx4 v252, v[220:223], s[54:55] offset:768 sc1\n"
      "v_mfma_f32_16x16x32_bf16 v[132:135], a[160:163], v[92:95], v[132:135]\n"
      "s_add_u32 m0, s17, 0x8f00\n"
      "v_mfma_f32_16x16x32_bf16 v[132:135], a[164:167], v[108:111], v[132:135]\n"
      "ds_write_b64 v244, v[210:211] offset:12296\n"
      "v_mfma_f32_16x16x32_bf16 v[132:135], a[168:171], v[112:115], v[132:135]\n"
      "global_load_lds_dwordx4 v237, s[52:53] offset:256 nt\n"
      "v_mfma_f32_16x16x32_bf16 v[132:135], a[172:175], v[116:119], v[132:135]\n"
      "v_mfma_f32_16x16x32_bf16 v[136:139], a[176:179], v[36:39], v[12:15]\n"
      "s_cbranch_vccnz LT11\n"
      "LS11:\n"
      "v_mfma_f32_16x16x32_bf16 v[136:139], a[180:183], v[40:43], v[136:139]\n"
      "v_mfma_f32_16x16x32_bf16 v[136:139], a[184:187], v[44:47], v[136:139]\n"
      "v_mfma_f32_16x16x32_bf16 v[136:139], a[188:191], v[60:63], v[136:139]\n"
      "v_cvt_pk_bf16_f32 v212, v132, v133\n"
      "v_mfma_f32_16x16x32_bf16 v[136:139], a[192:195], v[64:67], v[136:139]\n"
      "v_cvt_pk_bf16_f32 v213, v134, v135\n"
      "v_mfma_f32_16x16x32_bf16 v[136:139], a[196:199], v[68:71], v[136:139]\n"
      "v_mfma_f32_16x16x32_bf16 v[136:139], a[200:203], v[84:87], v[136:139]\n"
      "v_mfma_f32_16x16x32_bf16 v[136:139], a[204:207], v[88:91], v[136:139]\n"
      "s_waitcnt lgkmcnt(3)\n"
      "v_mfma_f32_16x16x32_bf16 v[136:139], a[208:211], v[92:95], v[136:139]\n"
      "global_store_dwordx4 v253, v[224:227], s[54:55] offset:768 sc1\n"
      "v_mfma_f32_16x16x32_bf16 v[136:139], a[212:215], v[108:111], v[136:139]\n"
      "ds_write_b64 v245, v[212:213] offset:12288\n"
      "v_mfma_f32_16x16x32_bf16 v[136:139], a[216:219], v[112:115], v[136:139]\n"
      "s_add_u32 m0, s17, 0xae00\n"
      "v_mfma_f32_16x16x32_bf16 v[136:139], a[220:223], v[116:119], v[136:139]\n"
      "global_load_lds_dwordx4 v237, s[52:53] offset:512 nt\n"
      "v_mfma_f32_16x16x32_bf16 v[132:135], a[224:227], v[36:39], v[16:19]\n"
      "v_mfma_f32_16x16x32_bf16 v[132:135], a[228:231], v[40:43], v[132:135]\n"
      "v_mfma_f32_16x16x32_bf16 v[132:135], a[232:235], v[44:47], v[132:135]\n"
      "v_mfma_f32_16x16x32_bf16 v[132:135], a[236:239], v[60:63], v[132:135]\n"
      "s_memrealtime s[84:85]\n"
      "v_mfma_f32_16x16x32_bf16 v[132:135], a[0:3], v[64:67], v[132:135]\n"
      "v_cvt_pk_bf16_f32 v214, v136, v137\n"
      "v_mfma_f32_16x16x32_bf16 v[132:135], a[4:7], v[68:71], v[132:135]\n"
      "v_cvt_pk_bf16_f32 v215, v138, v139\n"
      "v_mfma_f32_16x16x32_bf16 v[132:135], a[8:11], v[84:87], v[132:135]\n"
      "v_mfma_f32_16x16x32_bf16 v[132:135], a[12:15], v[88:91], v[132:135]\n"
      "s_add_u32 s83, s83, s86\n"
      "v_mfma_f32_16x16x32_bf16 v[132:135], a[16:19], v[92:95], v[132:135]\n"
      "v_mfma_f32_16x16x32_bf16 v[132:135], a[20:23], v[108:111], v[132:135]\n"
      "v_mfma_f32_16x16x32_bf16 v[132:135], v[140:143], v[112:115], v[132:135]\n"
      "s_waitcnt lgkmcnt(0)\n"
      "s_sub_u32 s84, s84, s83\n"
      "v_mfma_f32_16x16x32_bf16 v[132:135], v[156:159], v[116:119], v[132:135]\n"
      "s_cmp_lt_i32 s84, 0\n"
      "s_cselect_b64 vcc, -1, 0\n"
      "s_waitcnt vmcnt(24)\n"
      "s_barrier\n"
      "v_mfma_f32_16x16x32_bf16 v[136:139], v[160:163], v[36:39], v[20:23]\n"
      "ds_read_b128 v[36:39], v232 offset:24576\n"
      "ds_write_b64 v245, v[214:215] offset:12296\n"
      "v_mfma_f32_16x16x32_bf16 v[136:139], v[164:167], v[40:43], v[136:139]\n"
      "ds_read_b128 v[40:43], v233 offset:24576\n"
      "s_lshl_b32 s79, s94, 1\n"
      "v_mfma_f32_16x16x32_bf16 v[136:139], v[180:183], v[44:47], v[136:139]\n"
      "ds_read_b128 v[44:47], v234 offset:24576\n"
      "v_mad_i32_i24 v252, v251, s79, v252\n"
      "v_mfma_f32_16x16x32_bf16 v[136:139], v[184:187], v[60:63], v[136:139]\n"
      "ds_read_b128 v[60:63], v235 offset:24576\n"
      "v_cvt_pk_bf16_f32 v208, v132, v133\n"
      "v_mfma_f32_16x16x32_bf16 v[136:139], v[188:191], v[64:67], v[136:139]\n"
      "ds_read_b128 v[64:67], v232 offset:32768\n"
      "v_cvt_pk_bf16_f32 v209, v134, v135\n"
      "v_mfma_f32_16x16x32_bf16 v[136:139], v[204:207], v[68:71], v[136:139]\n"
      "ds_read_b128 v[68:71], v233 offset:32768\n"
      "s_mul_i32 s79, s79, s100\n"
      "v_mfma_f32_16x16x32_bf16 v[136:139], a[24:27], v[84:87], v[136:139]\n"
      "ds_read_b128 v[84:87], v234 offset:32768\n"
      "v_add_u32 v247, s79, v247\n"
      "v_mfma_f32_16x16x32_bf16 v[136:139], a[28:31], v[88:91], v[136:139]\n"
      "ds_read_b128 v[88:91], v235 offset:32768\n"
      "s_mul_i32 s79, s79, s101\n"
      "v_mfma_f32_16x16x32_bf16 v[136:139], a[32:35], v[92:95], v[136:139]\n"
      "ds_read_b128 v[92:95], v232 offset:40960\n"
      "v_add_u32 v253, s79, v253\n"
      "v_mfma_f32_16x16x32_bf16 v[136:139], a[36:39], v[108:111], v[136:139]\n"
      "ds_read_b128 v[108:111], v233 offset:40960\n"
      "v_mfma_f32_16x16x32_bf16 v[136:139], a[40:43], v[112:115], v[136:139]\n"
      "ds_read_b128 v[112:115], v234 offset:40960\n"
      "v_mfma_f32_16x16x32_bf16 v[136:139], a[44:47], v[116:119], v[136:139]\n"
      "ds_read_b128 v[116:119], v235 offset:40960\n"
      "s_waitcnt lgkmcnt(8)\n"
      "v_mfma_f32_16x16x32_bf16 v[132:135], a[240:243], v[36:39], v[0:3]\n"
      "ds_write_b64 v244, v[208:209] offset:12416\n"
      "v_mfma_f32_16x16x32_bf16 v[132:135], a[244:247], v[40:43], v[132:135]\n"
      "s_add_u32 s54, s30, s37\n"
      "s_addc_u32 s55, s31, 0\n"
      "v_mfma_f32_16x16x32_bf16 v[132:135], a[248:251], v[44:47], v[132:135]\n"
      "s_cbranch_vccnz LT12\n"
      "LS12:\n"
      "v_mfma_f32_16x16x32_bf16 v[132:135], a[252:255], v[60:63], v[132:135]\n"
      "v_cvt_pk_bf16_f32 v210, v136, v137\n"
      "s_waitcnt lgkmcnt(5)\n"
      "v_mfma_f32_16x16x32_bf16 v[132:135], a[48:51], v[64:67], v[132:135]\n"
      "v_cvt_pk_bf16_f32 v211, v138, v139\n"
      "v_mfma_f32_16x16x32_bf16 v[132:135], a[52:55], v[68:71], v[132:135]\n"
      "s_mul_i32 s78, s96, s19\n"
      "v_mfma_f32_16x16x32_bf16 v[132:135], a[56:59], v[84:87], v[132:135]\n"
      "ds_read_b128 v[216:219], v246 offset:0\n"
      "v_add_u32 v236, s78, v236\n"
      "v_mfma_f32_16x16x32_bf16 v[132:135], a[60:63], v[88:91], v[132:135]\n"
      "ds_read_b128 v[220:223], v246 offset:1024\n"
      "s_waitcnt lgkmcnt(3)\n"
      "v_mfma_f32_16x16x32_bf16 v[132:135], a[64:67], v[92:95], v[132:135]\n"
      "ds_read_b128 v[224:227], v246 offset:2048\n"
      "v_mfma_f32_16x16x32_bf16 v[132:135], a[68:71], v[108:111], v[132:135]\n"
      "ds_write_b64 v244, v[210:211] offset:12424\n"
      "v_mfma_f32_16x16x32_bf16 v[132:135], a[72:75], v[112:115], v[132:135]\n"
      "s_mul_i32 s78, s78, s99\n"
      "v_mfma_f32_16x16x32_bf16 v[132:135], a[76:79], v[116:119], v[132:135]\n"
      "v_add_u32 v237, s78, v237\n"
      "v_mfma_f32_16x16x32_bf16 v[136:139], a[80:83], v[36:39], v[4:7]\n"
      "v_mfma_f32_16x16x32_bf16 v[136:139], a[84:87], v[40:43], v[136:139]\n"
      "v_mfma_f32_16x16x32_bf16 v[136:139], a[88:91], v[44:47], v[136:139]\n"
      "v_mfma_f32_16x16x32_bf16 v[136:139], a[92:95], v[60:63], v[136:139]\n"
      "v_cvt_pk_bf16_f32 v208, v132, v133\n"
      "v_mfma_f32_16x16x32_bf16 v[136:139], a[96:99], v[64:67], v[136:139]\n"
      "v_cvt_pk_bf16_f32 v209, v134, v135\n"
      "v_mfma_f32_16x16x32_bf16 v[136:139], a[100:103], v[68:71], v[136:139]\n"
      "s_waitcnt lgkmcnt(3)\n"
      "v_mfma_f32_16x16x32_bf16 v[136:139], a[104:107], v[84:87], v[136:139]\n"
      "global_store_dwordx4 v247, v[216:219], s[54:55] offset:0 sc1\n"
      "v_mfma_f32_16x16x32_bf16 v[136:139], a[108:111], v[88:91], v[136:139]\n"
      "s_add_u32 s52, s20, s39\n"
      "s_addc_u32 s53, s21, 0\n"
      "v_mfma_f32_16x16x32_bf16 v[136:139], a[112:115], v[92:95], v[136:139]\n"
      "s_add_u32 m0, s17, 0xc000\n"
      "v_mfma_f32_16x16x32_bf16 v[136:139], a[116:119], v[108:111], v[136:139]\n"
      "ds_write_b64 v244, v[208:209] offset:24576\n"
      "v_mfma_f32_16x16x32_bf16 v[136:139], a[120:123], v[112:115], v[136:139]\n"
      "global_load_lds_dwordx4 v236, s[52:53] offset:0 nt\n"
      "v_mfma_f32_16x16x32_bf16 v[136:139], a[124:127], v[116:119], v[136:139]\n"
      "v_mfma_f32_16x16x32_bf16 v[132:135], a[128:131], v[36:39], v[8:11]\n"
      "v_mfma_f32_16x16x32_bf16 v[132:135], a[132:135], v[40:43], v[132:135]\n"
      "v_mfma_f32_16x16x32_bf16 v[132:135], a[136:139], v[44:47], v[132:135]\n"
      "v_mfma_f32_16x16x32_bf16 v[132:135], a[140:143], v[60:63], v[132:135]\n"
      "v_cvt_pk_bf16_f32 v210, v136, v137\n"
      "v_mfma_f32_16x16x32_bf16 v[132:135], a[144:147], v[64:67], v[132:135]\n"
      "v_cvt_pk_bf16_f32 v211, v138, v139\n"
      "v_mfma_f32_16x16x32_bf16 v[132:135], a[148:151], v[68:71], v[132:135]\n"
      "v_mfma_f32_16x16x32_bf16 v[132:135], a[152:155], v[84:87], v[132:135]\n"
      "s_waitcnt lgkmcnt(3)\n"
      "v_mfma_f32_16x16x32_bf16 v[132:135], a[156:159], v[88:91], v[132:135]\n"
      "global_store_dwordx4 v252, v[220:223], s[54:55] offset:0 sc1\n"
      "v_mfma_f32_16x16x32_bf16 v[132:135], a[160:163], v[92:95], v[132:135]\n"
      "s_add_u32 m0, s17, 0xdf00\n"
      "v_mfma_f32_16x16x32_bf16 v[132:135], a[164:167], v[108:111], v[132:135]\n"
      "ds_write_b64 v244, v[210:211] offset:24584\n"
      "v_mfma_f32_16x16x32_bf16 v[132:135], a[168:171], v[112:115], v[132:135]\n"
      "global_load_lds_dwordx4 v236, s[52:53] offset:256 nt\n"
      "v_mfma_f32_16x16x32_bf16 v[132:135], a[172:175], v[116:119], v[132:135]\n"
      "v_mfma_f32_16x16x32_bf16 v[136:139], a[176:179], v[36:39], v[12:15]\n"
      "s_cbranch_vccnz LT13\n"
      "LS13:\n"
      "v_mfma_f32_16x16x32_bf16 v[136:139], a[180:183], v[40:43], v[136:139]\n"
      "v_mfma_f32_16x16x32_bf16 v[136:139], a[184:187], v[44:47], v[136:139]\n"
      "v_mfma_f32_16x16x32_bf16 v[136:139], a[188:191], v[60:63], v[136:139]\n"
      "v_cvt_pk_bf16_f32 v212, v132, v133\n"
      "v_mfma_f32_16x16x32_bf16 v[136:139], a[192:195], v[64:67], v[136:139]\n"
      "v_cvt_pk_bf16_f32 v213, v134, v135\n"
      "v_mfma_f32_16x16x32_bf16 v[136:139], a[196:199], v[68:71], v[136:139]\n"
      "v_mfma_f32_16x16x32_bf16 v[136:139], a[200:203], v[84:87], v[136:139]\n"
      "v_mfma_f32_16x16x32_bf16 v[136:139], a[204:207], v[88:91], v[136:139]\n"
      "s_waitcnt lgkmcnt(3)\n"
      "v_mfma_f32_16x16x32_bf16 v[136:139], a[208:211], v[92:95], v[136:139]\n"
      "global_store_dwordx4 v253, v[224:227], s[54:55] offset:0 sc1\n"
      "v_mfma_f32_16x16x32_bf16 v[136:139], a[212:215], v[108:111], v[136:139]\n"
      "ds_write_b64 v245, v[212:213] offset:24576\n"
      "v_mfma_f32_16x16x32_bf16 v[136:139], a[216:219], v[112:115], v[136:139]\n"
      "s_add_u32 m0, s17, 0xfe00\n"
      "v_mfma_f32_16x16x32_bf16 v[136:139], a[220:223], v[116:119], v[136:139]\n"
      "global_load_lds_dwordx4 v236, s[52:53] offset:512 nt\n"
      "v_mfma_f32_16x16x32_bf16 v[132:135], a[224:227], v[36:39], v[16:19]\n"
      "v_mfma_f32_16x16x32_bf16 v[132:135], a[228:231], v[40:43], v[132:135]\n"
      "v_mfma_f32_16x16x32_bf16 v[132:135], a[232:235], v[44:47], v[132:135]\n"
      "v_mfma_f32_16x16x32_bf16 v[132:135], a[236:239], v[60:63], v[132:135]\n"
      "v_cvt_pk_bf16_f32 v214, v136, v137\n"
      "v_mfma_f32_16x16x32_bf16 v[132:135], a[0:3], v[64:67], v[132:135]\n"
      "v_cvt_pk_bf16_f32 v215, v138, v139\n"
      "v_mfma_f32_16x16x32_bf16 v[132:135], a[4:7], v[68:71], v[132:135]\n"
      "v_mfma_f32_16x16x32_bf16 v[132:135], a[8:11], v[84:87], v[132:135]\n"
      "v_mfma_f32_16x16x32_bf16 v[132:135], a[12:15], v[88:91], v[132:135]\n"
      "v_mfma_f32_16x16x32_bf16 v[132:135], a[16:19], v[92:95], v[132:135]\n"
      "v_mfma_f32_16x16x32_bf16 v[132:135], a[20:23], v[108:111], v[132:135]\n"
      "ds_write_b64 v245, v[214:215] offset:24584\n"
      "v_mfma_f32_16x16x32_bf16 v[132:135], v[140:143], v[112:115], v[132:135]\n"
      "v_mfma_f32_16x16x32_bf16 v[132:135], v[156:159], v[116:119], v[132:135]\n"
      "s_waitcnt lgkmcnt(4)\n"
      "s_barrier\n"
      "v_mfma_f32_16x16x32_bf16 v[136:139], v[160:163], v[36:39], v[20:23]\n"
      "ds_read_b128 v[36:39], v232 offset:24832\n"
      "v_mfma_f32_16x16x32_bf16 v[136:139], v[164:167], v[40:43], v[136:139]\n"
      "ds_read_b128 v[40:43], v233 offset:24832\n"
      "v_mfma_f32_16x16x32_bf16 v[136:139], v[180:183], v[44:47], v[136:139]\n"
      "ds_read_b128 v[44:47], v234 offset:24832\n"
      "v_mfma_f32_16x16x32_bf16 v[136:139], v[184:187], v[60:63], v[136:139]\n"
      "LPD13:\n"
      "s_cbranch_execz LPD16\n"
      "ds_read_b128 v[60:63], v235 offset:24832\n"
      "v_cvt_pk_bf16_f32 v208, v132, v133\n"
      "v_mfma_f32_16x16x32_bf16 v[136:139], v[188:191], v[64:67], v[136:139]\n"
      "ds_read_b128 v[64:67], v232 offset:33024\n"
      "v_cvt_pk_bf16_f32 v209, v134, v135\n"
      "v_mfma_f32_16x16x32_bf16 v[136:139], v[204:207], v[68:71], v[136:139]\n"
      "ds_read_b128 v[68:71], v233 offset:33024\n"
      "v_mfma_f32_16x16x32_bf16 v[136:139], a[24:27], v[84:87], v[136:139]\n"
      "ds_read_b128 v[84:87], v234 offset:33024\n"
      "v_mfma_f32_16x16x32_bf16 v[136:139], a[28:31], v[88:91], v[136:139]\n"
      "ds_read_b128 v[88:91], v235 offset:33024\n"
      "v_mfma_f32_16x16x32_bf16 v[136:139], a[32:35], v[92:95], v[136:139]\n"
      "ds_read_b128 v[92:95], v232 offset:41216\n"
      "v_mfma_f32_16x16x32_bf16 v[136:139], a[36:39], v[108:111], v[136:139]\n"
      "ds_read_b128 v[108:111], v233 offset:41216\n"
      "ds_write_b64 v244, v[208:209] offset:24704\n"
      "v_mfma_f32_16x16x32_bf16 v[136:139], a[40:43], v[112:115], v[136:139]\n"
      "ds_read_b128 v[112:115], v234 offset:41216\n"
      "v_mfma_f32_16x16x32_bf16 v[136:139], a[44:47], v[116:119], v[136:139]\n"
      "ds_read_b128 v[116:119], v235 offset:41216\n"
      "s_waitcnt lgkmcnt(9)\n"
      "v_mfma_f32_16x16x32_bf16 v[132:135], a[240:243], v[36:39], v[0:3]\n"
      "v_add_u32 v232, 0xffff4000, v232\n"
      "s_cbranch_vccnz LT14\n"
      "LS14:\n"
      "v_mfma_f32_16x16x32_bf16 v[132:135], a[244:247], v[40:43], v[132:135]\n"
      "v_add_u32 v233, 0xffff4000, v233\n"
      "v_mfma_f32_16x16x32_bf16 v[132:135], a[248:251], v[44:47], v[132:135]\n"
      "v_add_u32 v234, 0xffff4000, v234\n"
      "v_mfma_f32_16x16x32_bf16 v[132:135], a[252:255], v[60:63], v[132:135]\n"
      "v_add_u32 v235, 0xffff4000, v235\n"
      "s_waitcnt lgkmcnt(5)\n"
      "v_mfma_f32_16x16x32_bf16 v[132:135], a[48:51], v[64:67], v[132:135]\n"
      "v_cvt_pk_bf16_f32 v210, v136, v137\n"
      "v_mfma_f32_16x16x32_bf16 v[132:135], a[52:55], v[68:71], v[132:135]\n"
      "v_cvt_pk_bf16_f32 v211, v138, v139\n"
      "v_mfma_f32_16x16x32_bf16 v[132:135], a[56:59], v[84:87], v[132:135]\n"
      "ds_read_b128 v[216:219], v246 offset:12288\n"
      "v_mfma_f32_16x16x32_bf16 v[132:135], a[60:63], v[88:91], v[132:135]\n"
      "ds_read_b128 v[220:223], v246 offset:13312\n"
      "s_waitcnt lgkmcnt(2)\n"
      "v_mfma_f32_16x16x32_bf16 v[132:135], a[64:67], v[92:95], v[132:135]\n"
      "ds_read_b128 v[224:227], v246 offset:14336\n"
      "v_mfma_f32_16x16x32_bf16 v[132:135], a[68:71], v[108:111], v[132:135]\n"
      "v_lshlrev_b32 v0, 16, v144\n"
      "v_mfma_f32_16x16x32_bf16 v[132:135], a[72:75], v[112:115], v[132:135]\n"
      "ds_write_b64 v244, v[210:211] offset:24712\n"
      "v_mfma_f32_16x16x32_bf16 v[132:135], a[76:79], v[116:119], v[132:135]\n"
      "v_and_b32 v1, 0xffff0000, v144\n"
      "v_mfma_f32_16x16x32_bf16 v[136:139], a[80:83], v[36:39], v[4:7]\n"
      "v_lshlrev_b32 v2, 16, v145\n"
      "v_mfma_f32_16x16x32_bf16 v[136:139], a[84:87], v[40:43], v[136:139]\n"
      "v_and_b32 v3, 0xffff0000, v145\n"
      "v_mfma_f32_16x16x32_bf16 v[136:139], a[88:91], v[44:47], v[136:139]\n"
      "v_mfma_f32_16x16x32_bf16 v[136:139], a[92:95], v[60:63], v[136:139]\n"
      "v_cvt_pk_bf16_f32 v208, v132, v133\n"
      "v_mfma_f32_16x16x32_bf16 v[136:139], a[96:99], v[64:67], v[136:139]\n"
      "v_cvt_pk_bf16_f32 v209, v134, v135\n"
      "v_mfma_f32_16x16x32_bf16 v[136:139], a[100:103], v[68:71], v[136:139]\n"
      "s_waitcnt lgkmcnt(3)\n"
      "v_mfma_f32_16x16x32_bf16 v[136:139], a[104:107], v[84:87], v[136:139]\n"
      "global_store_dwordx4 v247, v[216:219], s[54:55] offset:768 sc1\n"
      "v_mfma_f32_16x16x32_bf16 v[136:139], a[108:111], v[88:91], v[136:139]\n"
      "s_add_u32 m0, s17, 0xd000\n"
      "v_mfma_f32_16x16x32_bf16 v[136:139], a[112:115], v[92:95], v[136:139]\n"
      "global_load_lds_dwordx4 v237, s[52:53] offset:0 nt\n"
      "v_mfma_f32_16x16x32_bf16 v[136:139], a[116:119], v[108:111], v[136:139]\n"
      "ds_write_b64 v244, v[208:209] offset:0\n"
      "v_mfma_f32_16x16x32_bf16 v[136:139], a[120:123], v[112:115], v[136:139]\n"
      "v_lshlrev_b32 v4, 16, v146\n"
      "v_mfma_f32_16x16x32_bf16 v[136:139], a[124:127], v[116:119], v[136:139]\n"
      "v_and_b32 v5, 0xffff0000, v146\n"
      "v_mfma_f32_16x16x32_bf16 v[132:135], a[128:131], v[36:39], v[8:11]\n"
      "v_lshlrev_b32 v6, 16, v147\n"
      "v_mfma_f32_16x16x32_bf16 v[132:135], a[132:135], v[40:43], v[132:135]\n"
      "v_and_b32 v7, 0xffff0000, v147\n"
      "v_mfma_f32_16x16x32_bf16 v[132:135], a[136:139], v[44:47], v[132:135]\n"
      "v_mfma_f32_16x16x32_bf16 v[132:135], a[140:143], v[60:63], v[132:135]\n"
      "v_cvt_pk_bf16_f32 v210, v136, v137\n"
      "v_mfma_f32_16x16x32_bf16 v[132:135], a[144:147], v[64:67], v[132:135]\n"
      "v_cvt_pk_bf16_f32 v211, v138, v139\n"
      "v_mfma_f32_16x16x32_bf16 v[132:135], a[148:151], v[68:71], v[132:135]\n"
      "v_lshlrev_b32 v8, 16, v148\n"
      "v_mfma_f32_16x16x32_bf16 v[132:135], a[152:155], v[84:87], v[132:135]\n"
      "s_waitcnt lgkmcnt(3)\n"
      "v_mfma_f32_16x16x32_bf16 v[132:135], a[156:159], v[88:91], v[132:135]\n"
      "global_store_dwordx4 v252, v[220:223], s[54:55] offset:768 sc1\n"
      "v_mfma_f32_16x16x32_bf16 v[132:135], a[160:163], v[92:95], v[132:135]\n"
      "s_add_u32 m0, s17, 0xef00\n"
      "v_mfma_f32_16x16x32_bf16 v[132:135], a[164:167], v[108:111], v[132:135]\n"
      "ds_write_b64 v244, v[210:211] offset:8\n"
      "v_mfma_f32_16x16x32_bf16 v[132:135], a[168:171], v[112:115], v[132:135]\n"
      "global_load_lds_dwordx4 v237, s[52:53] offset:256 nt\n"
      "v_mfma_f32_16x16x32_bf16 v[132:135], a[172:175], v[116:119], v[132:135]\n"
      "v_and_b32 v9, 0xffff0000, v148\n"
      "v_mfma_f32_16x16x32_bf16 v[136:139], a[176:179], v[36:39], v[12:15]\n"
      "v_lshlrev_b32 v10, 16, v149\n"
      "v_mfma_f32_16x16x32_bf16 v[136:139], a[180:183], v[40:43], v[136:139]\n"
      "v_and_b32 v11, 0xffff0000, v149\n"
      "v_mfma_f32_16x16x32_bf16 v[136:139], a[184:187], v[44:47], v[136:139]\n"
      "s_cbranch_vccnz LT15\n"
      "LS15:\n"
      "v_mfma_f32_16x16x32_bf16 v[136:139], a[188:191], v[60:63], v[136:139]\n"
      "v_cvt_pk_bf16_f32 v212, v132, v133\n"
      "v_mfma_f32_16x16x32_bf16 v[136:139], a[192:195], v[64:67], v[136:139]\n"
      "v_cvt_pk_bf16_f32 v213, v134, v135\n"
      "v_mfma_f32_16x16x32_bf16 v[136:139], a[196:199], v[68:71], v[136:139]\n"
      "v_lshlrev_b32 v12, 16, v150\n"
      "v_mfma_f32_16x16x32_bf16 v[136:139], a[200:203], v[84:87], v[136:139]\n"
      "v_and_b32 v13, 0xffff0000, v150\n"
      "v_mfma_f32_16x16x32_bf16 v[136:139], a[204:207], v[88:91], v[136:139]\n"
      "s_waitcnt lgkmcnt(3)\n"
      "v_mfma_f32_16x16x32_bf16 v[136:139], a[208:211], v[92:95], v[136:139]\n"
      "global_store_dwordx4 v253, v[224:227], s[54:55] offset:768 sc1\n"
      "v_mfma_f32_16x16x32_bf16 v[136:139], a[212:215], v[108:111], v[136:139]\n"
      "ds_write_b64 v245, v[212:213] offset:0\n"
      "v_mfma_f32_16x16x32_bf16 v[136:139], a[216:219], v[112:115], v[136:139]\n"
      "s_add_u32 m0, s17, 0x10e00\n"
      "v_mfma_f32_16x16x32_bf16 v[136:139], a[220:223], v[116:119], v[136:139]\n"
      "global_load_lds_dwordx4 v237, s[52:53] offset:512 nt\n"
      "v_mfma_f32_16x16x32_bf16 v[132:135], a[224:227], v[36:39], v[16:19]\n"
      "v_lshlrev_b32 v14, 16, v151\n"
      "v_mfma_f32_16x16x32_bf16 v[132:135], a[228:231], v[40:43], v[132:135]\n"
      "v_and_b32 v15, 0xffff0000, v151\n"
      "v_mfma_f32_16x16x32_bf16 v[132:135], a[232:235], v[44:47], v[132:135]\n"
      "v_mfma_f32_16x16x32_bf16 v[132:135], a[236:239], v[60:63], v[132:135]\n"
      "s_memrealtime s[84:85]\n"
      "v_mfma_f32_16x16x32_bf16 v[132:135], a[0:3], v[64:67], v[132:135]\n"
      "v_cvt_pk_bf16_f32 v214, v136, v137\n"
      "v_mfma_f32_16x16x32_bf16 v[132:135], a[4:7], v[68:71], v[132:135]\n"
      "v_cvt_pk_bf16_f32 v215, v138, v139\n"
      "v_mfma_f32_16x16x32_bf16 v[132:135], a[8:11], v[84:87], v[132:135]\n"
      "v_lshlrev_b32 v16, 16, v152\n"
      "v_mfma_f32_16x16x32_bf16 v[132:135], a[12:15], v[88:91], v[132:135]\n"
      "s_add_u32 s83, s83, s86\n"
      "v_mfma_f32_16x16x32_bf16 v[132:135], a[16:19], v[92:95], v[132:135]\n"
      "v_and_b32 v17, 0xffff0000, v152\n"
      "v_mfma_f32_16x16x32_bf16 v[132:135], a[20:23], v[108:111], v[132:135]\n"
      "v_lshlrev_b32 v18, 16, v153\n"
      "v_mfma_f32_16x16x32_bf16 v[132:135], v[140:143], v[112:115], v[132:135]\n"
      "s_waitcnt lgkmcnt(0)\n"
      "s_sub_u32 s84, s84, s83\n"
      "v_mfma_f32_16x16x32_bf16 v[132:135], v[156:159], v[116:119], v[132:135]\n"
      "s_cmp_lt_i32 s84, 0\n"
      "s_cselect_b64 vcc, -1, 0\n"
      "s_waitcnt vmcnt(24)\n"
      "s_barrier\n"
      "v_mfma_f32_16x16x32_bf16 v[136:139], v[160:163], v[36:39], v[20:23]\n"
      "ds_read_b128 v[36:39], v232 offset:0\n"
      "ds_write_b64 v245, v[214:215] offset:8\n"
      "v_mfma_f32_16x16x32_bf16 v[136:139], v[164:167], v[40:43], v[136:139]\n"
      "ds_read_b128 v[40:43], v233 offset:0\n"
      "v_mfma_f32_16x16x32_bf16 v[136:139], v[180:183], v[44:47], v[136:139]\n"
      "ds_read_b128 v[44:47], v234 offset:0\n"
      "v_mfma_f32_16x16x32_bf16 v[136:139], v[184:187], v[60:63], v[136:139]\n"
      "ds_read_b128 v[60:63], v235 offset:0\n"
      "v_cvt_pk_bf16_f32 v208, v132, v133\n"
      "v_mfma_f32_16x16x32_bf16 v[136:139], v[188:191], v[64:67], v[136:139]\n"
      "ds_read_b128 v[64:67], v232 offset:8192\n"
      "v_cvt_pk_bf16_f32 v209, v134, v135\n"
      "v_mfma_f32_16x16x32_bf16 v[136:139], v[204:207], v[68:71], v[136:139]\n"
      "ds_read_b128 v[68:71], v233 offset:8192\n"
      "v_mfma_f32_16x16x32_bf16 v[136:139], a[24:27], v[84:87], v[136:139]\n"
      "ds_read_b128 v[84:87], v234 offset:8192\n"
      "v_mfma_f32_16x16x32_bf16 v[136:139], a[28:31], v[88:91], v[136:139]\n"
      "ds_read_b128 v[88:91], v235 offset:8192\n"
      "v_mfma_f32_16x16x32_bf16 v[136:139], a[32:35], v[92:95], v[136:139]\n"
      "ds_read_b128 v[92:95], v232 offset:16384\n"
      "v_mfma_f32_16x16x32_bf16 v[136:139], a[36:39], v[108:111], v[136:139]\n"
      "ds_read_b128 v[108:111], v233 offset:16384\n"
      "v_mfma_f32_16x16x32_bf16 v[136:139], a[40:43], v[112:115], v[136:139]\n"
      "ds_read_b128 v[112:115], v234 offset:16384\n"
      "v_mfma_f32_16x16x32_bf16 v[136:139], a[44:47], v[116:119], v[136:139]\n"
      "ds_read_b128 v[116:119], v235 offset:16384\n"
      "s_waitcnt lgkmcnt(8)\n"
      "v_mfma_f32_16x16x32_bf16 v[132:135], a[240:243], v[36:39], v[0:3]\n"
      "ds_write_b64 v244, v[208:209] offset:128\n"
      "s_cbranch_vccnz LT16\n"
      "LS16:\n"
      "v_mfma_f32_16x16x32_bf16 v[132:135], a[244:247], v[40:43], v[132:135]\n"
      "v_and_b32 v19, 0xffff0000, v153\n"
      "v_mfma_f32_16x16x32_bf16 v[132:135], a[248:251], v[44:47], v[132:135]\n"
      "v_lshlrev_b32 v20, 16, v154\n"
      "v_mfma_f32_16x16x32_bf16 v[132:135], a[252:255], v[60:63], v[132:135]\n"
      "v_and_b32 v21, 0xffff0000, v154\n"
      "s_waitcnt lgkmcnt(5)\n"
      "v_mfma_f32_16x16x32_bf16 v[132:135], a[48:51], v[64:67], v[132:135]\n"
      "v_lshlrev_b32 v22, 16, v155\n"
      "v_mfma_f32_16x16x32_bf16 v[132:135], a[52:55], v[68:71], v[132:135]\n"
      "v_and_b32 v23, 0xffff0000, v155\n"
      "v_mfma_f32_16x16x32_bf16 v[132:135], a[56:59], v[84:87], v[132:135]\n"
      "v_cvt_pk_bf16_f32 v210, v136, v137\n"
      "v_mfma_f32_16x16x32_bf16 v[132:135], a[60:63], v[88:91], v[132:135]\n"
      "v_cvt_pk_bf16_f32 v211, v138, v139\n"
      "s_waitcnt lgkmcnt(1)\n"
      "v_mfma_f32_16x16x32_bf16 v[132:135], a[64:67], v[92:95], v[132:135]\n"
      "s_add_u32 s54, s44, s37\n"
      "s_addc_u32 s55, s45, 0\n"
      "v_mfma_f32_16x16x32_bf16 v[132:135], a[68:71], v[108:111], v[132:135]\n"
      "ds_read_b128 v[216:219], v246 offset:24576\n"
      "v_mfma_f32_16x16x32_bf16 v[132:135], a[72:75], v[112:115], v[132:135]\n"
      "ds_read_b128 v[220:223], v246 offset:25600\n"
      "v_mfma_f32_16x16x32_bf16 v[132:135], a[76:79], v[116:119], v[132:135]\n"
      "ds_read_b128 v[224:227], v246 offset:26624\n"
      "v_mfma_f32_16x16x32_bf16 v[136:139], a[80:83], v[36:39], v[4:7]\n"
      "ds_write_b64 v244, v[210:211] offset:136\n"
      "v_mfma_f32_16x16x32_bf16 v[136:139], a[84:87], v[40:43], v[136:139]\n"
      "v_mfma_f32_16x16x32_bf16 v[136:139], a[88:91], v[44:47], v[136:139]\n"
      "v_mfma_f32_16x16x32_bf16 v[136:139], a[92:95], v[60:63], v[136:139]\n"
      "v_cvt_pk_bf16_f32 v208, v132, v133\n"
      "v_mfma_f32_16x16x32_bf16 v[136:139], a[96:99], v[64:67], v[136:139]\n"
      "v_cvt_pk_bf16_f32 v209, v134, v135\n"
      "v_mfma_f32_16x16x32_bf16 v[136:139], a[100:103], v[68:71], v[136:139]\n"
      "s_waitcnt lgkmcnt(3)\n"
      "v_mfma_f32_16x16x32_bf16 v[136:139], a[104:107], v[84:87], v[136:139]\n"
      "global_store_dwordx4 v247, v[216:219], s[54:55] offset:0 sc1\n"
      "v_mfma_f32_16x16x32_bf16 v[136:139], a[108:111], v[88:91], v[136:139]\n"
      "s_add_u32 s52, s42, s39\n"
      "s_addc_u32 s53, s43, 0\n"
      "v_mfma_f32_16x16x32_bf16 v[136:139], a[112:115], v[92:95], v[136:139]\n"
      "s_add_u32 m0, s17, 0x12000\n"
      "v_mfma_f32_16x16x32_bf16 v[136:139], a[116:119], v[108:111], v[136:139]\n"
      "ds_write_b64 v244, v[208:209] offset:12288\n"
      "v_mfma_f32_16x16x32_bf16 v[136:139], a[120:123], v[112:115], v[136:139]\n"
      "global_load_lds_dwordx4 v236, s[52:53] offset:0 nt\n"
      "v_mfma_f32_16x16x32_bf16 v[136:139], a[124:127], v[116:119], v[136:139]\n"
      "v_mfma_f32_16x16x32_bf16 v[132:135], a[128:131], v[36:39], v[8:11]\n"
      "v_mfma_f32_16x16x32_bf16 v[132:135], a[132:135], v[40:43], v[132:135]\n"
      "v_mfma_f32_16x16x32_bf16 v[132:135], a[136:139], v[44:47], v[132:135]\n"
      "v_mfma_f32_16x16x32_bf16 v[132:135], a[140:143], v[60:63], v[132:135]\n"
      "v_cvt_pk_bf16_f32 v210, v136, v137\n"
      "v_mfma_f32_16x16x32_bf16 v[132:135], a[144:147], v[64:67], v[132:135]\n"
      "v_cvt_pk_bf16_f32 v211, v138, v139\n"
      "v_mfma_f32_16x16x32_bf16 v[132:135], a[148:151], v[68:71], v[132:135]\n"
      "v_mfma_f32_16x16x32_bf16 v[132:135], a[152:155], v[84:87], v[132:135]\n"
      "s_waitcnt lgkmcnt(3)\n"
      "v_mfma_f32_16x16x32_bf16 v[132:135], a[156:159], v[88:91], v[132:135]\n"
      "global_store_dwordx4 v252, v[220:223], s[54:55] offset:0 sc1\n"
      "v_mfma_f32_16x16x32_bf16 v[132:135], a[160:163], v[92:95], v[132:135]\n"
      "s_add_u32 m0, s17, 0x13f00\n"
      "v_mfma_f32_16x16x32_bf16 v[132:135], a[164:167], v[108:111], v[132:135]\n"
      "ds_write_b64 v244, v[210:211] offset:12296\n"
      "v_mfma_f32_16x16x32_bf16 v[132:135], a[168:171], v[112:115], v[132:135]\n"
      "global_load_lds_dwordx4 v236, s[52:53] offset:256 nt\n"
      "v_mfma_f32_16x16x32_bf16 v[132:135], a[172:175], v[116:119], v[132:135]\n"
      "v_mfma_f32_16x16x32_bf16 v[136:139], a[176:179], v[36:39], v[12:15]\n"
      "s_cbranch_vccnz LT17\n"
      "LS17:\n"
      "v_mfma_f32_16x16x32_bf16 v[136:139], a[180:183], v[40:43], v[136:139]\n"
      "v_mfma_f32_16x16x32_bf16 v[136:139], a[184:187], v[44:47], v[136:139]\n"
      "v_mfma_f32_16x16x32_bf16 v[136:139], a[188:191], v[60:63], v[136:139]\n"
      "v_cvt_pk_bf16_f32 v212, v132, v133\n"
      "v_mfma_f32_16x16x32_bf16 v[136:139], a[192:195], v[64:67], v[136:139]\n"
      "v_cvt_pk_bf16_f32 v213, v134, v135\n"
      "v_mfma_f32_16x16x32_bf16 v[136:139], a[196:199], v[68:71], v[136:139]\n"
      "v_mfma_f32_16x16x32_bf16 v[136:139], a[200:203], v[84:87], v[136:139]\n"
      "v_mfma_f32_16x16x32_bf16 v[136:139], a[204:207], v[88:91], v[136:139]\n"
      "s_waitcnt lgkmcnt(3)\n"
      "v_mfma_f32_16x16x32_bf16 v[136:139], a[208:211], v[92:95], v[136:139]\n"
      "global_store_dwordx4 v253, v[224:227], s[54:55] offset:0 sc1\n"
      "v_mfma_f32_16x16x32_bf16 v[136:139], a[212:215], v[108:111], v[136:139]\n"
      "ds_write_b64 v245, v[212:213] offset:12288\n"
      "v_mfma_f32_16x16x32_bf16 v[136:139], a[216:219], v[112:115], v[136:139]\n"
      "s_add_u32 m0, s17, 0x15e00\n"
      "v_mfma_f32_16x16x32_bf16 v[136:139], a[220:223], v[116:119], v[136:139]\n"
      "global_load_lds_dwordx4 v236, s[52:53] offset:512 nt\n"
      "v_mfma_f32_16x16x32_bf16 v[132:135], a[224:227], v[36:39], v[16:19]\n"
      "v_mfma_f32_16x16x32_bf16 v[132:135], a[228:231], v[40:43], v[132:135]\n"
      "v_mfma_f32_16x16x32_bf16 v[132:135], a[232:235], v[44:47], v[132:135]\n"
      "v_mfma_f32_16x16x32_bf16 v[132:135], a[236:239], v[60:63], v[132:135]\n"
      "v_cvt_pk_bf16_f32 v214, v136, v137\n"
      "v_mfma_f32_16x16x32_bf16 v[132:135], a[0:3], v[64:67], v[132:135]\n"
      "v_cvt_pk_bf16_f32 v215, v138, v139\n"
      "v_mfma_f32_16x16x32_bf16 v[132:135], a[4:7], v[68:71], v[132:135]\n"
      "v_mfma_f32_16x16x32_bf16 v[132:135], a[8:11], v[84:87], v[132:135]\n"
      "v_mfma_f32_16x16x32_bf16 v[132:135], a[12:15], v[88:91], v[132:135]\n"
      "v_mfma_f32_16x16x32_bf16 v[132:135], a[16:19], v[92:95], v[132:135]\n"
      "v_mfma_f32_16x16x32_bf16 v[132:135], a[20:23], v[108:111], v[132:135]\n"
      "ds_write_b64 v245, v[214:215] offset:12296\n"
      "v_mfma_f32_16x16x32_bf16 v[132:135], v[140:143], v[112:115], v[132:135]\n"
      "v_mfma_f32_16x16x32_bf16 v[132:135], v[156:159], v[116:119], v[132:135]\n"
      "s_waitcnt lgkmcnt(4)\n"
      "s_barrier\n"
      "v_mfma_f32_16x16x32_bf16 v[136:139], v[160:163], v[36:39], v[20:23]\n"
      "ds_read_b128 v[36:39], v232 offset:256\n"
      "v_mfma_f32_16x16x32_bf16 v[136:139], v[164:167], v[40:43], v[136:139]\n"
      "ds_read_b128 v[40:43], v233 offset:256\n"
      "v_mfma_f32_16x16x32_bf16 v[136:139], v[180:183], v[44:47], v[136:139]\n"
      "ds_read_b128 v[44:47], v234 offset:256\n"
      "v_mfma_f32_16x16x32_bf16 v[136:139], v[184:187], v[60:63], v[136:139]\n"
      "ds_read_b128 v[60:63], v235 offset:256\n"
      "v_cvt_pk_bf16_f32 v208, v132, v133\n"
      "v_mfma_f32_16x16x32_bf16 v[136:139], v[188:191], v[64:67], v[136:139]\n"
      "ds_read_b128 v[64:67], v232 offset:8448\n"
      "v_cvt_pk_bf16_f32 v209, v134, v135\n"
      "v_mfma_f32_16x16x32_bf16 v[136:139], v[204:207], v[68:71], v[136:139]\n"
      "ds_read_b128 v[68:71], v233 offset:8448\n"
      "v_mfma_f32_16x16x32_bf16 v[136:139], a[24:27], v[84:87], v[136:139]\n"
      "ds_read_b128 v[84:87], v234 offset:8448\n"
      "v_mfma_f32_16x16x32_bf16 v[136:139], a[28:31], v[88:91], v[136:139]\n"
      "ds_read_b128 v[88:91], v235 offset:8448\n"
      "v_mfma_f32_16x16x32_bf16 v[136:139], a[32:35], v[92:95], v[136:139]\n"
      "ds_read_b128 v[92:95], v232 offset:16640\n"
      "v_mfma_f32_16x16x32_bf16 v[136:139], a[36:39], v[108:111], v[136:139]\n"
      "ds_read_b128 v[108:111], v233 offset:16640\n"
      "v_mfma_f32_16x16x32_bf16 v[136:139], a[40:43], v[112:115], v[136:139]\n"
      "ds_read_b128 v[112:115], v234 offset:16640\n"
      "v_mfma_f32_16x16x32_bf16 v[136:139], a[44:47], v[116:119], v[136:139]\n"
      "ds_read_b128 v[116:119], v235 offset:16640\n"
      "s_waitcnt lgkmcnt(8)\n"
      "v_mfma_f32_16x16x32_bf16 v[132:135], a[240:243], v[36:39], v[0:3]\n"
      "ds_write_b64 v244, v[208:209] offset:12416\n"
      "v_mfma_f32_16x16x32_bf16 v[132:135], a[244:247], v[40:43], v[132:135]\n"
      "s_cbranch_vccnz LT18\n"
      "LS18:\n"
      "v_mfma_f32_16x16x32_bf16 v[132:135], a[248:251], v[44:47], v[132:135]\n"
      "v_mfma_f32_16x16x32_bf16 v[132:135], a[252:255], v[60:63], v[132:135]\n"
      "v_cvt_pk_bf16_f32 v210, v136, v137\n"
      "s_waitcnt lgkmcnt(5)\n"
      "v_mfma_f32_16x16x32_bf16 v[132:135], a[48:51], v[64:67], v[132:135]\n"
      "v_cvt_pk_bf16_f32 v211, v138, v139\n"
      "v_mfma_f32_16x16x32_bf16 v[132:135], a[52:55], v[68:71], v[132:135]\n"
      "v_mfma_f32_16x16x32_bf16 v[132:135], a[56:59], v[84:87], v[132:135]\n"
      "ds_read_b128 v[216:219], v246 offset:0\n"
      "v_mfma_f32_16x16x32_bf16 v[132:135], a[60:63], v[88:91], v[132:135]\n"
      "ds_read_b128 v[220:223], v246 offset:1024\n"
      "s_waitcnt lgkmcnt(3)\n"
      "v_mfma_f32_16x16x32_bf16 v[132:135], a[64:67], v[92:95], v[132:135]\n"
      "ds_read_b128 v[224:227], v246 offset:2048\n"
      "v_mfma_f32_16x16x32_bf16 v[132:135], a[68:71], v[108:111], v[132:135]\n"
      "ds_write_b64 v244, v[210:211] offset:12424\n"
      "v_mfma_f32_16x16x32_bf16 v[132:135], a[72:75], v[112:115], v[132:135]\n"
      "v_mfma_f32_16x16x32_bf16 v[132:135], a[76:79], v[116:119], v[132:135]\n"
      "v_mfma_f32_16x16x32_bf16 v[136:139], a[80:83], v[36:39], v[4:7]\n"
      "v_mfma_f32_16x16x32_bf16 v[136:139], a[84:87], v[40:43], v[136:139]\n"
      "v_mfma_f32_16x16x32_bf16 v[136:139], a[88:91], v[44:47], v[136:139]\n"
      "v_mfma_f32_16x16x32_bf16 v[136:139], a[92:95], v[60:63], v[136:139]\n"
      "v_cvt_pk_bf16_f32 v208, v132, v133\n"
      "v_mfma_f32_16x16x32_bf16 v[136:139], a[96:99], v[64:67], v[136:139]\n"
      "v_cvt_pk_bf16_f32 v209, v134, v135\n"
      "v_mfma_f32_16x16x32_bf16 v[136:139], a[100:103], v[68:71], v[136:139]\n"
      "s_waitcnt lgkmcnt(3)\n"
      "v_mfma_f32_16x16x32_bf16 v[136:139], a[104:107], v[84:87], v[136:139]\n"
      "global_store_dwordx4 v247, v[216:219], s[54:55] offset:768 sc1\n"
      "v_mfma_f32_16x16x32_bf16 v[136:139], a[108:111], v[88:91], v[136:139]\n"
      "s_add_u32 m0, s17, 0x13000\n"
      "v_mfma_f32_16x16x32_bf16 v[136:139], a[112:115], v[92:95], v[136:139]\n"
      "global_load_lds_dwordx4 v237, s[52:53] offset:0 nt\n"
      "v_mfma_f32_16x16x32_bf16 v[136:139], a[116:119], v[108:111], v[136:139]\n"
      "ds_write_b64 v244, v[208:209] offset:24576\n"
      "v_mfma_f32_16x16x32_bf16 v[136:139], a[120:123], v[112:115], v[136:139]\n"
      "v_mfma_f32_16x16x32_bf16 v[136:139], a[124:127], v[116:119], v[136:139]\n"
      "v_mfma_f32_16x16x32_bf16 v[132:135], a[128:131], v[36:39], v[8:11]\n"
      "v_mfma_f32_16x16x32_bf16 v[132:135], a[132:135], v[40:43], v[132:135]\n"
      "v_mfma_f32_16x16x32_bf16 v[132:135], a[136:139], v[44:47], v[132:135]\n"
      "v_mfma_f32_16x16x32_bf16 v[132:135], a[140:143], v[60:63], v[132:135]\n"
      "v_cvt_pk_bf16_f32 v210, v136, v137\n"
      "v_mfma_f32_16x16x32_bf16 v[132:135], a[144:147], v[64:67], v[132:135]\n"
      "v_cvt_pk_bf16_f32 v211, v138, v139\n"
      "v_mfma_f32_16x16x32_bf16 v[132:135], a[148:151], v[68:71], v[132:135]\n"
      "v_mfma_f32_16x16x32_bf16 v[132:135], a[152:155], v[84:87], v[132:135]\n"
      "s_waitcnt lgkmcnt(3)\n"
      "v_mfma_f32_16x16x32_bf16 v[132:135], a[156:159], v[88:91], v[132:135]\n"
      "global_store_dwordx4 v252, v[220:223], s[54:55] offset:768 sc1\n"
      "v_mfma_f32_16x16x32_bf16 v[132:135], a[160:163], v[92:95], v[132:135]\n"
      "s_add_u32 m0, s17, 0x14f00\n"
      "v_mfma_f32_16x16x32_bf16 v[132:135], a[164:167], v[108:111], v[132:135]\n"
      "ds_write_b64 v244, v[210:211] offset:24584\n"
      "v_mfma_f32_16x16x32_bf16 v[132:135], a[168:171], v[112:115], v[132:135]\n"
      "global_load_lds_dwordx4 v237, s[52:53] offset:256 nt\n"
      "v_mfma_f32_16x16x32_bf16 v[132:135], a[172:175], v[116:119], v[132:135]\n"
      "v_mfma_f32_16x16x32_bf16 v[136:139], a[176:179], v[36:39], v[12:15]\n"
      "s_cbranch_vccnz LT19\n"
      "LS19:\n"
      "v_mfma_f32_16x16x32_bf16 v[136:139], a[180:183], v[40:43], v[136:139]\n"
      "v_mfma_f32_16x16x32_bf16 v[136:139], a[184:187], v[44:47], v[136:139]\n"
      "v_mfma_f32_16x16x32_bf16 v[136:139], a[188:191], v[60:63], v[136:139]\n"
      "v_cvt_pk_bf16_f32 v212, v132, v133\n"
      "v_mfma_f32_16x16x32_bf16 v[136:139], a[192:195], v[64:67], v[136:139]\n"
      "v_cvt_pk_bf16_f32 v213, v134, v135\n"
      "v_mfma_f32_16x16x32_bf16 v[136:139], a[196:199], v[68:71], v[136:139]\n"
      "v_mfma_f32_16x16x32_bf16 v[136:139], a[200:203], v[84:87], v[136:139]\n"
      "v_mfma_f32_16x16x32_bf16 v[136:139], a[204:207], v[88:91], v[136:139]\n"
      "s_waitcnt lgkmcnt(3)\n"
      "v_mfma_f32_16x16x32_bf16 v[136:139], a[208:211], v[92:95], v[136:139]\n"
      "global_store_dwordx4 v253, v[224:227], s[54:55] offset:768 sc1\n"
      "v_mfma_f32_16x16x32_bf16 v[136:139], a[212:215], v[108:111], v[136:139]\n"
      "ds_write_b64 v245, v[212:213] offset:24576\n"
      "v_mfma_f32_16x16x32_bf16 v[136:139], a[216:219], v[112:115], v[136:139]\n"
      "s_add_u32 m0, s17, 0x16e00\n"
      "v_mfma_f32_16x16x32_bf16 v[136:139], a[220:223], v[116:119], v[136:139]\n"
      "global_load_lds_dwordx4 v237, s[52:53] offset:512 nt\n"
      "v_mfma_f32_16x16x32_bf16 v[132:135], a[224:227], v[36:39], v[16:19]\n"
      "v_mfma_f32_16x16x32_bf16 v[132:135], a[228:231], v[40:43], v[132:135]\n"
      "v_mfma_f32_16x16x32_bf16 v[132:135], a[232:235], v[44:47], v[132:135]\n"
      "v_mfma_f32_16x16x32_bf16 v[132:135], a[236:239], v[60:63], v[132:135]\n"
      "s_memrealtime s[84:85]\n"
      "v_mfma_f32_16x16x32_bf16 v[132:135], a[0:3], v[64:67], v[132:135]\n"
      "v_cvt_pk_bf16_f32 v214, v136, v137\n"
      "v_mfma_f32_16x16x32_bf16 v[132:135], a[4:7], v[68:71], v[132:135]\n"
      "v_cvt_pk_bf16_f32 v215, v138, v139\n"
      "v_mfma_f32_16x16x32_bf16 v[132:135], a[8:11], v[84:87], v[132:135]\n"
      "v_mfma_f32_16x16x32_bf16 v[132:135], a[12:15], v[88:91], v[132:135]\n"
      "s_add_u32 s83, s83, s86\n"
      "v_mfma_f32_16x16x32_bf16 v[132:135], a[16:19], v[92:95], v[132:135]\n"
      "v_mfma_f32_16x16x32_bf16 v[132:135], a[20:23], v[108:111], v[132:135]\n"
      "v_mfma_f32_16x16x32_bf16 v[132:135], v[140:143], v[112:115], v[132:135]\n"
      "s_waitcnt lgkmcnt(0)\n"
      "s_sub_u32 s84, s84, s83\n"
      "v_mfma_f32_16x16x32_bf16 v[132:135], v[156:159], v[116:119], v[132:135]\n"
      "s_cmp_lt_i32 s84, 0\n"
      "s_cselect_b64 vcc, -1, 0\n"
      "s_waitcnt vmcnt(24)\n"
      "s_barrier\n"
      "v_mfma_f32_16x16x32_bf16 v[136:139], v[160:163], v[36:39], v[20:23]\n"
      "ds_read_b128 v[36:39], v232 offset:24576\n"
      "ds_write_b64 v245, v[214:215] offset:24584\n"
      "v_mfma_f32_16x16x32_bf16 v[136:139], v[164:167], v[40:43], v[136:139]\n"
      "ds_read_b128 v[40:43], v233 offset:24576\n"
      "v_mfma_f32_16x16x32_bf16 v[136:139], v[180:183], v[44:47], v[136:139]\n"
      "ds_read_b128 v[44:47], v234 offset:24576\n"
      "v_mfma_f32_16x16x32_bf16 v[136:139], v[184:187], v[60:63], v[136:139]\n"
      "ds_read_b128 v[60:63], v235 offset:24576\n"
      "v_cvt_pk_bf16_f32 v208, v132, v133\n"
      "v_mfma_f32_16x16x32_bf16 v[136:139], v[188:191], v[64:67], v[136:139]\n"
      "ds_read_b128 v[64:67], v232 offset:32768\n"
      "v_cvt_pk_bf16_f32 v209, v134, v135\n"
      "v_mfma_f32_16x16x32_bf16 v[136:139], v[204:207], v[68:71], v[136:139]\n"
      "ds_read_b128 v[68:71], v233 offset:32768\n"
      "v_mfma_f32_16x16x32_bf16 v[136:139], a[24:27], v[84:87], v[136:139]\n"
      "ds_read_b128 v[84:87], v234 offset:32768\n"
      "v_mfma_f32_16x16x32_bf16 v[136:139], a[28:31], v[88:91], v[136:139]\n"
      "ds_read_b128 v[88:91], v235 offset:32768\n"
      "v_mfma_f32_16x16x32_bf16 v[136:139], a[32:35], v[92:95], v[136:139]\n"
      "ds_read_b128 v[92:95], v232 offset:40960\n"
      "v_mfma_f32_16x16x32_bf16 v[136:139], a[36:39], v[108:111], v[136:139]\n"
      "ds_read_b128 v[108:111], v233 offset:40960\n"
      "v_mfma_f32_16x16x32_bf16 v[136:139], a[40:43], v[112:115], v[136:139]\n"
      "ds_read_b128 v[112:115], v234 offset:40960\n"
      "v_mfma_f32_16x16x32_bf16 v[136:139], a[44:47], v[116:119], v[136:139]\n"
      "ds_read_b128 v[116:119], v235 offset:40960\n"
      "s_waitcnt lgkmcnt(8)\n"
      "v_mfma_f32_16x16x32_bf16 v[132:135], a[240:243], v[36:39], v[0:3]\n"
      "ds_write_b64 v244, v[208:209] offset:24704\n"
      "v_mfma_f32_16x16x32_bf16 v[132:135], a[244:247], v[40:43], v[132:135]\n"
      "s_cbranch_vccnz LT20\n"
      "LS20:\n"
      "v_mfma_f32_16x16x32_bf16 v[132:135], a[248:251], v[44:47], v[132:135]\n"
      "v_mfma_f32_16x16x32_bf16 v[132:135], a[252:255], v[60:63], v[132:135]\n"
      "v_cvt_pk_bf16_f32 v210, v136, v137\n"
      "s_lshl_b32 s79, s95, 1\n"
      "s_waitcnt lgkmcnt(5)\n"
      "v_mfma_f32_16x16x32_bf16 v[132:135], a[48:51], v[64:67], v[132:135]\n"
      "v_cvt_pk_bf16_f32 v211, v138, v139\n"
      "s_mul_i32 s78, s97, s19\n"
      "v_mfma_f32_16x16x32_bf16 v[132:135], a[52:55], v[68:71], v[132:135]\n"
      "v_mad_i32_i24 v252, v251, s79, v252\n"
      "v_add_u32 v236, s78, v236\n"
      "v_mfma_f32_16x16x32_bf16 v[132:135], a[56:59], v[84:87], v[132:135]\n"
      "ds_read_b128 v[24:27], v246 offset:12288\n"
      "s_mul_i32 s79, s79, s100\n"
      "v_mfma_f32_16x16x32_bf16 v[132:135], a[60:63], v[88:91], v[132:135]\n"
      "ds_read_b128 v[28:31], v246 offset:13312\n"
      "v_add_u32 v247, s79, v247\n"
      "s_waitcnt lgkmcnt(3)\n"
      "v_mfma_f32_16x16x32_bf16 v[132:135], a[64:67], v[92:95], v[132:135]\n"
      "ds_read_b128 v[32:35], v246 offset:14336\n"
      "s_mul_i32 s79, s79, s101\n"
      "v_mfma_f32_16x16x32_bf16 v[132:135], a[68:71], v[108:111], v[132:135]\n"
      "ds_write_b64 v244, v[210:211] offset:24712\n"
      "s_mul_i32 s78, s78, s99\n"
      "v_mfma_f32_16x16x32_bf16 v[132:135], a[72:75], v[112:115], v[132:135]\n"
      "v_add_u32 v253, s79, v253\n"
      "v_add_u32 v237, s78, v237\n"
      "v_mfma_f32_16x16x32_bf16 v[132:135], a[76:79], v[116:119], v[132:135]\n"
      "s_add_u32 s54, s30, s38\n"
      "s_addc_u32 s55, s31, 0\n"
      "v_mfma_f32_16x16x32_bf16 v[136:139], a[80:83], v[36:39], v[4:7]\n"
      "s_waitcnt lgkmcnt(3)\n"
      "v_mfma_f32_16x16x32_bf16 v[136:139], a[84:87], v[40:43], v[136:139]\n"
      "global_store_dwordx4 v247, v[24:27], s[54:55] offset:0 sc1\n"
      "v_mfma_f32_16x16x32_bf16 v[136:139], a[88:91], v[44:47], v[136:139]\n"
      "v_mfma_f32_16x16x32_bf16 v[136:139], a[92:95], v[60:63], v[136:139]\n"
      "v_cvt_pk_bf16_f32 v208, v132, v133\n"
      "v_mfma_f32_16x16x32_bf16 v[136:139], a[96:99], v[64:67], v[136:139]\n"
      "v_cvt_pk_bf16_f32 v209, v134, v135\n"
      "v_mfma_f32_16x16x32_bf16 v[136:139], a[100:103], v[68:71], v[136:139]\n"
      "v_mfma_f32_16x16x32_bf16 v[136:139], a[104:107], v[84:87], v[136:139]\n"
      "v_mfma_f32_16x16x32_bf16 v[136:139], a[108:111], v[88:91], v[136:139]\n"
      "s_add_u32 s52, s20, s40\n"
      "s_addc_u32 s53, s21, 0\n"
      "v_mfma_f32_16x16x32_bf16 v[136:139], a[112:115], v[92:95], v[136:139]\n"
      "s_add_u32 m0, s17, 0x0\n"
      "v_mfma_f32_16x16x32_bf16 v[136:139], a[116:119], v[108:111], v[136:139]\n"
      "ds_write_b64 v244, v[208:209] offset:0\n"
      "v_mfma_f32_16x16x32_bf16 v[136:139], a[120:123], v[112:115], v[136:139]\n"
      "global_load_lds_dwordx4 v236, s[52:53] offset:0 nt\n"
      "v_mfma_f32_16x16x32_bf16 v[136:139], a[124:127], v[116:119], v[136:139]\n"
      "v_mfma_f32_16x16x32_bf16 v[132:135], a[128:131], v[36:39], v[8:11]\n"
      "v_mfma_f32_16x16x32_bf16 v[132:135], a[132:135], v[40:43], v[132:135]\n"
      "v_mfma_f32_16x16x32_bf16 v[132:135], a[136:139], v[44:47], v[132:135]\n"
      "v_mfma_f32_16x16x32_bf16 v[132:135], a[140:143], v[60:63], v[132:135]\n"
      "v_cvt_pk_bf16_f32 v210, v136, v137\n"
      "v_mfma_f32_16x16x32_bf16 v[132:135], a[144:147], v[64:67], v[132:135]\n"
      "v_cvt_pk_bf16_f32 v211, v138, v139\n"
      "v_mfma_f32_16x16x32_bf16 v[132:135], a[148:151], v[68:71], v[132:135]\n"
      "v_mfma_f32_16x16x32_bf16 v[132:135], a[152:155], v[84:87], v[132:135]\n"
      "s_waitcnt lgkmcnt(3)\n"
      "v_mfma_f32_16x16x32_bf16 v[132:135], a[156:159], v[88:91], v[132:135]\n"
      "global_store_dwordx4 v252, v[28:31], s[54:55] offset:0 sc1\n"
      "v_mfma_f32_16x16x32_bf16 v[132:135], a[160:163], v[92:95], v[132:135]\n"
      "s_add_u32 m0, s17, 0x1f00\n"
      "v_mfma_f32_16x16x32_bf16 v[132:135], a[164:167], v[108:111], v[132:135]\n"
      "ds_write_b64 v244, v[210:211] offset:8\n"
      "v_mfma_f32_16x16x32_bf16 v[132:135], a[168:171], v[112:115], v[132:135]\n"
      "global_load_lds_dwordx4 v236, s[52:53] offset:256 nt\n"
      "v_mfma_f32_16x16x32_bf16 v[132:135], a[172:175], v[116:119], v[132:135]\n"
      "v_mfma_f32_16x16x32_bf16 v[136:139], a[176:179], v[36:39], v[12:15]\n"
      "s_cbranch_vccnz LT21\n"
      "LS21:\n"
      "v_mfma_f32_16x16x32_bf16 v[136:139], a[180:183], v[40:43], v[136:139]\n"
      "v_mfma_f32_16x16x32_bf16 v[136:139], a[184:187], v[44:47], v[136:139]\n"
      "v_mfma_f32_16x16x32_bf16 v[136:139], a[188:191], v[60:63], v[136:139]\n"
      "v_cvt_pk_bf16_f32 v212, v132, v133\n"
      "v_mfma_f32_16x16x32_bf16 v[136:139], a[192:195], v[64:67], v[136:139]\n"
      "v_cvt_pk_bf16_f32 v213, v134, v135\n"
      "v_mfma_f32_16x16x32_bf16 v[136:139], a[196:199], v[68:71], v[136:139]\n"
      "v_mfma_f32_16x16x32_bf16 v[136:139], a[200:203], v[84:87], v[136:139]\n"
      "v_mfma_f32_16x16x32_bf16 v[136:139], a[204:207], v[88:91], v[136:139]\n"
      "s_waitcnt lgkmcnt(3)\n"
      "v_mfma_f32_16x16x32_bf16 v[136:139], a[208:211], v[92:95], v[136:139]\n"
      "global_store_dwordx4 v253, v[32:35], s[54:55] offset:0 sc1\n"
      "v_mfma_f32_16x16x32_bf16 v[136:139], a[212:215], v[108:111], v[136:139]\n"
      "ds_write_b64 v245, v[212:213] offset:0\n"
      "v_mfma_f32_16x16x32_bf16 v[136:139], a[216:219], v[112:115], v[136:139]\n"
      "s_add_u32 m0, s17, 0x3e00\n"
      "v_mfma_f32_16x16x32_bf16 v[136:139], a[220:223], v[116:119], v[136:139]\n"
      "global_load_lds_dwordx4 v236, s[52:53] offset:512 nt\n"
      "v_mfma_f32_16x16x32_bf16 v[132:135], a[224:227], v[36:39], v[16:19]\n"
      "v_mfma_f32_16x16x32_bf16 v[132:135], a[228:231], v[40:43], v[132:135]\n"
      "v_mfma_f32_16x16x32_bf16 v[132:135], a[232:235], v[44:47], v[132:135]\n"
      "v_mfma_f32_16x16x32_bf16 v[132:135], a[236:239], v[60:63], v[132:135]\n"
      "v_cvt_pk_bf16_f32 v214, v136, v137\n"
      "v_mfma_f32_16x16x32_bf16 v[132:135], a[0:3], v[64:67], v[132:135]\n"
      "v_cvt_pk_bf16_f32 v215, v138, v139\n"
      "v_mfma_f32_16x16x32_bf16 v[132:135], a[4:7], v[68:71], v[132:135]\n"
      "v_mfma_f32_16x16x32_bf16 v[132:135], a[8:11], v[84:87], v[132:135]\n"
      "v_mfma_f32_16x16x32_bf16 v[132:135], a[12:15], v[88:91], v[132:135]\n"
      "v_mfma_f32_16x16x32_bf16 v[132:135], a[16:19], v[92:95], v[132:135]\n"
      "v_mfma_f32_16x16x32_bf16 v[132:135], a[20:23], v[108:111], v[132:135]\n"
      "ds_write_b64 v245, v[214:215] offset:8\n"
      "v_mfma_f32_16x16x32_bf16 v[132:135], v[140:143], v[112:115], v[132:135]\n"
      "v_mfma_f32_16x16x32_bf16 v[132:135], v[156:159], v[116:119], v[132:135]\n"
      "s_waitcnt lgkmcnt(4)\n"
      "s_barrier\n"
      "v_mfma_f32_16x16x32_bf16 v[136:139], v[160:163], v[36:39], v[20:23]\n"
      "ds_read_b128 v[36:39], v232 offset:24832\n"
      "v_mfma_f32_16x16x32_bf16 v[136:139], v[164:167], v[40:43], v[136:139]\n"
      "ds_read_b128 v[40:43], v233 offset:24832\n"
      "v_mfma_f32_16x16x32_bf16 v[136:139], v[180:183], v[44:47], v[136:139]\n"
      "LPD14:\n"
      "s_cbranch_execz LRET\n"
      "ds_read_b128 v[44:47], v234 offset:24832\n"
      "v_mfma_f32_16x16x32_bf16 v[136:139], v[184:187], v[60:63], v[136:139]\n"
      "ds_read_b128 v[60:63], v235 offset:24832\n"
      "v_cvt_pk_bf16_f32 v208, v132, v133\n"
      "v_mfma_f32_16x16x32_bf16 v[136:139], v[188:191], v[64:67], v[136:139]\n"
      "ds_read_b128 v[64:67], v232 offset:33024\n"
      "v_cvt_pk_bf16_f32 v209, v134, v135\n"
      "v_mfma_f32_16x16x32_bf16 v[136:139], v[204:207], v[68:71], v[136:139]\n"
      "ds_read_b128 v[68:71], v233 offset:33024\n"
      "v_mfma_f32_16x16x32_bf16 v[136:139], a[24:27], v[84:87], v[136:139]\n"
      "ds_read_b128 v[84:87], v234 offset:33024\n"
      "v_mfma_f32_16x16x32_bf16 v[136:139], a[28:31], v[88:91], v[136:139]\n"
      "ds_read_b128 v[88:91], v235 offset:33024\n"
      "v_mfma_f32_16x16x32_bf16 v[136:139], a[32:35], v[92:95], v[136:139]\n"
      "ds_read_b128 v[92:95], v232 offset:41216\n"
      "v_mfma_f32_16x16x32_bf16 v[136:139], a[36:39], v[108:111], v[136:139]\n"
      "ds_read_b128 v[108:111], v233 offset:41216\n"
      "ds_write_b64 v244, v[208:209] offset:128\n"
      "v_mfma_f32_16x16x32_bf16 v[136:139], a[40:43], v[112:115], v[136:139]\n"
      "ds_read_b128 v[112:115], v234 offset:41216\n"
      "v_mfma_f32_16x16x32_bf16 v[136:139], a[44:47], v[116:119], v[136:139]\n"
      "ds_read_b128 v[116:119], v235 offset:41216\n"
      "s_waitcnt lgkmcnt(9)\n"
      "v_mfma_f32_16x16x32_bf16 v[132:135], a[240:243], v[36:39], v[0:3]\n"
      "v_add_u32 v232, 0xc000, v232\n"
      "s_cbranch_vccnz LT22\n"
      "LS22:\n"
      "v_mfma_f32_16x16x32_bf16 v[132:135], a[244:247], v[40:43], v[132:135]\n"
      "v_add_u32 v233, 0xc000, v233\n"
      "v_mfma_f32_16x16x32_bf16 v[132:135], a[248:251], v[44:47], v[132:135]\n"
      "v_add_u32 v234, 0xc000, v234\n"
      "v_mfma_f32_16x16x32_bf16 v[132:135], a[252:255], v[60:63], v[132:135]\n"
      "v_add_u32 v235, 0xc000, v235\n"
      "s_waitcnt lgkmcnt(5)\n"
      "v_mfma_f32_16x16x32_bf16 v[132:135], a[48:51], v[64:67], v[132:135]\n"
      "v_cvt_pk_bf16_f32 v210, v136, v137\n"
      "v_mfma_f32_16x16x32_bf16 v[132:135], a[52:55], v[68:71], v[132:135]\n"
      "v_cvt_pk_bf16_f32 v211, v138, v139\n"
      "v_mfma_f32_16x16x32_bf16 v[132:135], a[56:59], v[84:87], v[132:135]\n"
      "ds_read_b128 v[48:51], v246 offset:24576\n"
      "v_mfma_f32_16x16x32_bf16 v[132:135], a[60:63], v[88:91], v[132:135]\n"
      "ds_read_b128 v[52:55], v246 offset:25600\n"
      "s_waitcnt lgkmcnt(2)\n"
      "v_mfma_f32_16x16x32_bf16 v[132:135], a[64:67], v[92:95], v[132:135]\n"
      "ds_read_b128 v[56:59], v246 offset:26624\n"
      "v_mfma_f32_16x16x32_bf16 v[132:135], a[68:71], v[108:111], v[132:135]\n"
      "v_lshlrev_b32 v0, 16, v168\n"
      "v_mfma_f32_16x16x32_bf16 v[132:135], a[72:75], v[112:115], v[132:135]\n"
      "ds_write_b64 v244, v[210:211] offset:136\n"
      "v_mfma_f32_16x16x32_bf16 v[132:135], a[76:79], v[116:119], v[132:135]\n"
      "v_and_b32 v1, 0xffff0000, v168\n"
      "v_mfma_f32_16x16x32_bf16 v[136:139], a[80:83], v[36:39], v[4:7]\n"
      "v_lshlrev_b32 v2, 16, v169\n"
      "v_mfma_f32_16x16x32_bf16 v[136:139], a[84:87], v[40:43], v[136:139]\n"
      "v_and_b32 v3, 0xffff0000, v169\n"
      "v_mfma_f32_16x16x32_bf16 v[136:139], a[88:91], v[44:47], v[136:139]\n"
      "v_mfma_f32_16x16x32_bf16 v[136:139], a[92:95], v[60:63], v[136:139]\n"
      "v_cvt_pk_bf16_f32 v208, v132, v133\n"
      "v_mfma_f32_16x16x32_bf16 v[136:139], a[96:99], v[64:67], v[136:139]\n"
      "v_cvt_pk_bf16_f32 v209, v134, v135\n"
      "v_mfma_f32_16x16x32_bf16 v[136:139], a[100:103], v[68:71], v[136:139]\n"
      "s_waitcnt lgkmcnt(3)\n"
      "v_mfma_f32_16x16x32_bf16 v[136:139], a[104:107], v[84:87], v[136:139]\n"
      "global_store_dwordx4 v247, v[48:51], s[54:55] offset:768 sc1\n"
      "v_mfma_f32_16x16x32_bf16 v[136:139], a[108:111], v[88:91], v[136:139]\n"
      "s_add_u32 m0, s17, 0x1000\n"
      "v_mfma_f32_16x16x32_bf16 v[136:139], a[112:115], v[92:95], v[136:139]\n"
      "global_load_lds_dwordx4 v237, s[52:53] offset:0 nt\n"
      "v_mfma_f32_16x16x32_bf16 v[136:139], a[116:119], v[108:111], v[136:139]\n"
      "ds_write_b64 v244, v[208:209] offset:12288\n"
      "v_mfma_f32_16x16x32_bf16 v[136:139], a[120:123], v[112:115], v[136:139]\n"
      "v_lshlrev_b32 v4, 16, v170\n"
      "v_mfma_f32_16x16x32_bf16 v[136:139], a[124:127], v[116:119], v[136:139]\n"
      "v_and_b32 v5, 0xffff0000, v170\n"
      "v_mfma_f32_16x16x32_bf16 v[132:135], a[128:131], v[36:39], v[8:11]\n"
      "v_lshlrev_b32 v6, 16, v171\n"
      "v_mfma_f32_16x16x32_bf16 v[132:135], a[132:135], v[40:43], v[132:135]\n"
      "v_and_b32 v7, 0xffff0000, v171\n"
      "v_mfma_f32_16x16x32_bf16 v[132:135], a[136:139], v[44:47], v[132:135]\n"
      "v_mfma_f32_16x16x32_bf16 v[132:135], a[140:143], v[60:63], v[132:135]\n"
      "v_cvt_pk_bf16_f32 v210, v136, v137\n"
      "v_mfma_f32_16x16x32_bf16 v[132:135], a[144:147], v[64:67], v[132:135]\n"
      "v_cvt_pk_bf16_f32 v211, v138, v139\n"
      "v_mfma_f32_16x16x32_bf16 v[132:135], a[148:151], v[68:71], v[132:135]\n"
      "v_lshlrev_b32 v8, 16, v172\n"
      "v_mfma_f32_16x16x32_bf16 v[132:135], a[152:155], v[84:87], v[132:135]\n"
      "s_waitcnt lgkmcnt(3)\n"
      "v_mfma_f32_16x16x32_bf16 v[132:135], a[156:159], v[88:91], v[132:135]\n"
      "global_store_dwordx4 v252, v[52:55], s[54:55] offset:768 sc1\n"
      "v_mfma_f32_16x16x32_bf16 v[132:135], a[160:163], v[92:95], v[132:135]\n"
      "s_add_u32 m0, s17, 0x2f00\n"
      "v_mfma_f32_16x16x32_bf16 v[132:135], a[164:167], v[108:111], v[132:135]\n"
      "ds_write_b64 v244, v[210:211] offset:12296\n"
      "v_mfma_f32_16x16x32_bf16 v[132:135], a[168:171], v[112:115], v[132:135]\n"
      "global_load_lds_dwordx4 v237, s[52:53] offset:256 nt\n"
      "v_mfma_f32_16x16x32_bf16 v[132:135], a[172:175], v[116:119], v[132:135]\n"
      "v_and_b32 v9, 0xffff0000, v172\n"
      "v_mfma_f32_16x16x32_bf16 v[136:139], a[176:179], v[36:39], v[12:15]\n"
      "v_lshlrev_b32 v10, 16, v173\n"
      "v_mfma_f32_16x16x32_bf16 v[136:139], a[180:183], v[40:43], v[136:139]\n"
      "v_and_b32 v11, 0xffff0000, v173\n"
      "v_mfma_f32_16x16x32_bf16 v[136:139], a[184:187], v[44:47], v[136:139]\n"
      "s_cbranch_vccnz LT23\n"
      "LS23:\n"
      "v_mfma_f32_16x16x32_bf16 v[136:139], a[188:191], v[60:63], v[136:139]\n"
      "v_cvt_pk_bf16_f32 v212, v132, v133\n"
      "v_mfma_f32_16x16x32_bf16 v[136:139], a[192:195], v[64:67], v[136:139]\n"
      "v_cvt_pk_bf16_f32 v213, v134, v135\n"
      "v_mfma_f32_16x16x32_bf16 v[136:139], a[196:199], v[68:71], v[136:139]\n"
      "v_lshlrev_b32 v12, 16, v174\n"
      "v_mfma_f32_16x16x32_bf16 v[136:139], a[200:203], v[84:87], v[136:139]\n"
      "v_and_b32 v13, 0xffff0000, v174\n"
      "v_mfma_f32_16x16x32_bf16 v[136:139], a[204:207], v[88:91], v[136:139]\n"
      "s_waitcnt lgkmcnt(3)\n"
      "v_mfma_f32_16x16x32_bf16 v[136:139], a[208:211], v[92:95], v[136:139]\n"
      "global_store_dwordx4 v253, v[56:59], s[54:55] offset:768 sc1\n"
      "v_mfma_f32_16x16x32_bf16 v[136:139], a[212:215], v[108:111], v[136:139]\n"
      "ds_write_b64 v245, v[212:213] offset:12288\n"
      "v_mfma_f32_16x16x32_bf16 v[136:139], a[216:219], v[112:115], v[136:139]\n"
      "s_add_u32 m0, s17, 0x4e00\n"
      "v_mfma_f32_16x16x32_bf16 v[136:139], a[220:223], v[116:119], v[136:139]\n"
      "global_load_lds_dwordx4 v237, s[52:53] offset:512 nt\n"
      "v_mfma_f32_16x16x32_bf16 v[132:135], a[224:227], v[36:39], v[16:19]\n"
      "v_lshlrev_b32 v14, 16, v175\n"
      "v_mfma_f32_16x16x32_bf16 v[132:135], a[228:231], v[40:43], v[132:135]\n"
      "v_and_b32 v15, 0xffff0000, v175\n"
      "v_mfma_f32_16x16x32_bf16 v[132:135], a[232:235], v[44:47], v[132:135]\n"
      "v_mfma_f32_16x16x32_bf16 v[132:135], a[236:239], v[60:63], v[132:135]\n"
      "s_memrealtime s[84:85]\n"
      "v_mfma_f32_16x16x32_bf16 v[132:135], a[0:3], v[64:67], v[132:135]\n"
      "v_cvt_pk_bf16_f32 v214, v136, v137\n"
      "v_mfma_f32_16x16x32_bf16 v[132:135], a[4:7], v[68:71], v[132:135]\n"
      "v_cvt_pk_bf16_f32 v215, v138, v139\n"
      "v_mfma_f32_16x16x32_bf16 v[132:135], a[8:11], v[84:87], v[132:135]\n"
      "v_lshlrev_b32 v16, 16, v176\n"
      "v_mfma_f32_16x16x32_bf16 v[132:135], a[12:15], v[88:91], v[132:135]\n"
      "s_add_u32 s83, s83, s86\n"
      "v_mfma_f32_16x16x32_bf16 v[132:135], a[16:19], v[92:95], v[132:135]\n"
      "v_and_b32 v17, 0xffff0000, v176\n"
      "v_mfma_f32_16x16x32_bf16 v[132:135], a[20:23], v[108:111], v[132:135]\n"
      "v_lshlrev_b32 v18, 16, v177\n"
      "v_mfma_f32_16x16x32_bf16 v[132:135], v[140:143], v[112:115], v[132:135]\n"
      "s_waitcnt lgkmcnt(0)\n"
      "s_sub_u32 s84, s84, s83\n"
      "v_mfma_f32_16x16x32_bf16 v[132:135], v[156:159], v[116:119], v[132:135]\n"
      "s_cmp_lt_i32 s84, 0\n"
      "s_cselect_b64 vcc, -1, 0\n"
      "s_waitcnt vmcnt(24)\n"
      "s_barrier\n"
      "v_mfma_f32_16x16x32_bf16 v[136:139], v[160:163], v[36:39], v[20:23]\n"
      "ds_read_b128 v[36:39], v232 offset:0\n"
      "ds_write_b64 v245, v[214:215] offset:12296\n"
      "v_mfma_f32_16x16x32_bf16 v[136:139], v[164:167], v[40:43], v[136:139]\n"
      "ds_read_b128 v[40:43], v233 offset:0\n"
      "v_mfma_f32_16x16x32_bf16 v[136:139], v[180:183], v[44:47], v[136:139]\n"
      "ds_read_b128 v[44:47], v234 offset:0\n"
      "v_mfma_f32_16x16x32_bf16 v[136:139], v[184:187], v[60:63], v[136:139]\n"
      "ds_read_b128 v[60:63], v235 offset:0\n"
      "v_cvt_pk_bf16_f32 v208, v132, v133\n"
      "v_mfma_f32_16x16x32_bf16 v[136:139], v[188:191], v[64:67], v[136:139]\n"
      "ds_read_b128 v[64:67], v232 offset:8192\n"
      "v_cvt_pk_bf16_f32 v209, v134, v135\n"
      "v_mfma_f32_16x16x32_bf16 v[136:139], v[204:207], v[68:71], v[136:139]\n"
      "ds_read_b128 v[68:71], v233 offset:8192\n"
      "v_mfma_f32_16x16x32_bf16 v[136:139], a[24:27], v[84:87], v[136:139]\n"
      "ds_read_b128 v[84:87], v234 offset:8192\n"
      "v_mfma_f32_16x16x32_bf16 v[136:139], a[28:31], v[88:91], v[136:139]\n"
      "ds_read_b128 v[88:91], v235 offset:8192\n"
      "v_mfma_f32_16x16x32_bf16 v[136:139], a[32:35], v[92:95], v[136:139]\n"
      "ds_read_b128 v[92:95], v232 offset:16384\n"
      "v_mfma_f32_16x16x32_bf16 v[136:139], a[36:39], v[108:111], v[136:139]\n"
      "ds_read_b128 v[108:111], v233 offset:16384\n"
      "v_mfma_f32_16x16x32_bf16 v[136:139], a[40:43], v[112:115], v[136:139]\n"
      "ds_read_b128 v[112:115], v234 offset:16384\n"
      "v_mfma_f32_16x16x32_bf16 v[136:139], a[44:47], v[116:119], v[136:139]\n"
      "ds_read_b128 v[116:119], v235 offset:16384\n"
      "s_waitcnt lgkmcnt(8)\n"
      "v_mfma_f32_16x16x32_bf16 v[132:135], a[240:243], v[36:39], v[0:3]\n"
      "ds_write_b64 v244, v[208:209] offset:12416\n"
      "s_cbranch_vccnz LT24\n"
      "LS24:\n"
      "v_mfma_f32_16x16x32_bf16 v[132:135], a[244:247], v[40:43], v[132:135]\n"
      "v_and_b32 v19, 0xffff0000, v177\n"
      "v_mfma_f32_16x16x32_bf16 v[132:135], a[248:251], v[44:47], v[132:135]\n"
      "v_lshlrev_b32 v20, 16, v178\n"
      "v_mfma_f32_16x16x32_bf16 v[132:135], a[252:255], v[60:63], v[132:135]\n"
      "v_and_b32 v21, 0xffff0000, v178\n"
      "s_waitcnt lgkmcnt(5)\n"
      "v_mfma_f32_16x16x32_bf16 v[132:135], a[48:51], v[64:67], v[132:135]\n"
      "v_lshlrev_b32 v22, 16, v179\n"
      "v_mfma_f32_16x16x32_bf16 v[132:135], a[52:55], v[68:71], v[132:135]\n"
      "v_and_b32 v23, 0xffff0000, v179\n"
      "v_mfma_f32_16x16x32_bf16 v[132:135], a[56:59], v[84:87], v[132:135]\n"
      "v_cvt_pk_bf16_f32 v210, v136, v137\n"
      "v_mfma_f32_16x16x32_bf16 v[132:135], a[60:63], v[88:91], v[132:135]\n"
      "v_cvt_pk_bf16_f32 v211, v138, v139\n"
      "s_waitcnt lgkmcnt(1)\n"
      "v_mfma_f32_16x16x32_bf16 v[132:135], a[64:67], v[92:95], v[132:135]\n"
      "ds_read_b128 v[72:75], v246 offset:0\n"
      "v_mfma_f32_16x16x32_bf16 v[132:135], a[68:71], v[108:111], v[132:135]\n"
      "ds_read_b128 v[76:79], v246 offset:1024\n"
      "v_mfma_f32_16x16x32_bf16 v[132:135], a[72:75], v[112:115], v[132:135]\n"
      "ds_read_b128 v[80:83], v246 offset:2048\n"
      "v_mfma_f32_16x16x32_bf16 v[132:135], a[76:79], v[116:119], v[132:135]\n"
      "v_mfma_f32_16x16x32_bf16 v[136:139], a[80:83], v[36:39], v[4:7]\n"
      "ds_write_b64 v244, v[210:211] offset:12424\n"
      "v_mfma_f32_16x16x32_bf16 v[136:139], a[84:87], v[40:43], v[136:139]\n"
      "v_mfma_f32_16x16x32_bf16 v[136:139], a[88:91], v[44:47], v[136:139]\n"
      "v_mfma_f32_16x16x32_bf16 v[136:139], a[92:95], v[60:63], v[136:139]\n"
      "v_cvt_pk_bf16_f32 v208, v132, v133\n"
      "s_add_u32 s54, s44, s38\n"
      "s_addc_u32 s55, s45, 0\n"
      "v_mfma_f32_16x16x32_bf16 v[136:139], a[96:99], v[64:67], v[136:139]\n"
      "v_cvt_pk_bf16_f32 v209, v134, v135\n"
      "v_mfma_f32_16x16x32_bf16 v[136:139], a[100:103], v[68:71], v[136:139]\n"
      "s_waitcnt lgkmcnt(3)\n"
      "v_mfma_f32_16x16x32_bf16 v[136:139], a[104:107], v[84:87], v[136:139]\n"
      "global_store_dwordx4 v247, v[72:75], s[54:55] offset:0 sc1\n"
      "v_mfma_f32_16x16x32_bf16 v[136:139], a[108:111], v[88:91], v[136:139]\n"
      "s_add_u32 s52, s42, s40\n"
      "s_addc_u32 s53, s43, 0\n"
      "v_mfma_f32_16x16x32_bf16 v[136:139], a[112:115], v[92:95], v[136:139]\n"
      "s_add_u32 m0, s17, 0x6000\n"
      "v_mfma_f32_16x16x32_bf16 v[136:139], a[116:119], v[108:111], v[136:139]\n"
      "ds_write_b64 v244, v[208:209] offset:24576\n"
      "v_mfma_f32_16x16x32_bf16 v[136:139], a[120:123], v[112:115], v[136:139]\n"
      "global_load_lds_dwordx4 v236, s[52:53] offset:0 nt\n"
      "v_mfma_f32_16x16x32_bf16 v[136:139], a[124:127], v[116:119], v[136:139]\n"
      "v_mfma_f32_16x16x32_bf16 v[132:135], a[128:131], v[36:39], v[8:11]\n"
      "v_mfma_f32_16x16x32_bf16 v[132:135], a[132:135], v[40:43], v[132:135]\n"
      "v_mfma_f32_16x16x32_bf16 v[132:135], a[136:139], v[44:47], v[132:135]\n"
      "v_mfma_f32_16x16x32_bf16 v[132:135], a[140:143], v[60:63], v[132:135]\n"
      "v_cvt_pk_bf16_f32 v210, v136, v137\n"
      "v_mfma_f32_16x16x32_bf16 v[132:135], a[144:147], v[64:67], v[132:135]\n"
      "v_cvt_pk_bf16_f32 v211, v138, v139\n"
      "v_mfma_f32_16x16x32_bf16 v[132:135], a[148:151], v[68:71], v[132:135]\n"
      "v_mfma_f32_16x16x32_bf16 v[132:135], a[152:155], v[84:87], v[132:135]\n"
      "s_waitcnt lgkmcnt(3)\n"
      "v_mfma_f32_16x16x32_bf16 v[132:135], a[156:159], v[88:91], v[132:135]\n"
      "global_store_dwordx4 v252, v[76:79], s[54:55] offset:0 sc1\n"
      "v_mfma_f32_16x16x32_bf16 v[132:135], a[160:163], v[92:95], v[132:135]\n"
      "s_add_u32 m0, s17, 0x7f00\n"
      "v_mfma_f32_16x16x32_bf16 v[132:135], a[164:167], v[108:111], v[132:135]\n"
      "ds_write_b64 v244, v[210:211] offset:24584\n"
      "v_mfma_f32_16x16x32_bf16 v[132:135], a[168:171], v[112:115], v[132:135]\n"
      "global_load_lds_dwordx4 v236, s[52:53] offset:256 nt\n"
      "v_mfma_f32_16x16x32_bf16 v[132:135], a[172:175], v[116:119], v[132:135]\n"
      "v_mfma_f32_16x16x32_bf16 v[136:139], a[176:179], v[36:39], v[12:15]\n"
      "s_cbranch_vccnz LT25\n"
      "LS25:\n"
      "v_mfma_f32_16x16x32_bf16 v[136:139], a[180:183], v[40:43], v[136:139]\n"
      "v_mfma_f32_16x16x32_bf16 v[136:139], a[184:187], v[44:47], v[136:139]\n"
      "v_mfma_f32_16x16x32_bf16 v[136:139], a[188:191], v[60:63], v[136:139]\n"
      "v_cvt_pk_bf16_f32 v212, v132, v133\n"
      "v_mfma_f32_16x16x32_bf16 v[136:139], a[192:195], v[64:67], v[136:139]\n"
      "v_cvt_pk_bf16_f32 v213, v134, v135\n"
      "v_mfma_f32_16x16x32_bf16 v[136:139], a[196:199], v[68:71], v[136:139]\n"
      "v_mfma_f32_16x16x32_bf16 v[136:139], a[200:203], v[84:87], v[136:139]\n"
      "v_mfma_f32_16x16x32_bf16 v[136:139], a[204:207], v[88:91], v[136:139]\n"
      "s_waitcnt lgkmcnt(3)\n"
      "v_mfma_f32_16x16x32_bf16 v[136:139], a[208:211], v[92:95], v[136:139]\n"
      "global_store_dwordx4 v253, v[80:83], s[54:55] offset:0 sc1\n"
      "v_mfma_f32_16x16x32_bf16 v[136:139], a[212:215], v[108:111], v[136:139]\n"
      "ds_write_b64 v245, v[212:213] offset:24576\n"
      "v_mfma_f32_16x16x32_bf16 v[136:139], a[216:219], v[112:115], v[136:139]\n"
      "s_add_u32 m0, s17, 0x9e00\n"
      "v_mfma_f32_16x16x32_bf16 v[136:139], a[220:223], v[116:119], v[136:139]\n"
      "global_load_lds_dwordx4 v236, s[52:53] offset:512 nt\n"
      "v_mfma_f32_16x16x32_bf16 v[132:135], a[224:227], v[36:39], v[16:19]\n"
      "v_mfma_f32_16x16x32_bf16 v[132:135], a[228:231], v[40:43], v[132:135]\n"
      "v_mfma_f32_16x16x32_bf16 v[132:135], a[232:235], v[44:47], v[132:135]\n"
      "v_mfma_f32_16x16x32_bf16 v[132:135], a[236:239], v[60:63], v[132:135]\n"
      "v_cvt_pk_bf16_f32 v214, v136, v137\n"
      "v_mfma_f32_16x16x32_bf16 v[132:135], a[0:3], v[64:67], v[132:135]\n"
      "v_cvt_pk_bf16_f32 v215, v138, v139\n"
      "v_mfma_f32_16x16x32_bf16 v[132:135], a[4:7], v[68:71], v[132:135]\n"
      "v_mfma_f32_16x16x32_bf16 v[132:135], a[8:11], v[84:87], v[132:135]\n"
      "v_mfma_f32_16x16x32_bf16 v[132:135], a[12:15], v[88:91], v[132:135]\n"
      "v_mfma_f32_16x16x32_bf16 v[132:135], a[16:19], v[92:95], v[132:135]\n"
      "v_mfma_f32_16x16x32_bf16 v[132:135], a[20:23], v[108:111], v[132:135]\n"
      "ds_write_b64 v245, v[214:215] offset:24584\n"
      "v_mfma_f32_16x16x32_bf16 v[132:135], v[140:143], v[112:115], v[132:135]\n"
      "v_mfma_f32_16x16x32_bf16 v[132:135], v[156:159], v[116:119], v[132:135]\n"
      "s_waitcnt lgkmcnt(4)\n"
      "s_barrier\n"
      "v_mfma_f32_16x16x32_bf16 v[136:139], v[160:163], v[36:39], v[20:23]\n"
      "ds_read_b128 v[36:39], v232 offset:256\n"
      "v_mfma_f32_16x16x32_bf16 v[136:139], v[164:167], v[40:43], v[136:139]\n"
      "ds_read_b128 v[40:43], v233 offset:256\n"
      "v_mfma_f32_16x16x32_bf16 v[136:139], v[180:183], v[44:47], v[136:139]\n"
      "ds_read_b128 v[44:47], v234 offset:256\n"
      "v_mfma_f32_16x16x32_bf16 v[136:139], v[184:187], v[60:63], v[136:139]\n"
      "ds_read_b128 v[60:63], v235 offset:256\n"
      "v_cvt_pk_bf16_f32 v208, v132, v133\n"
      "v_mfma_f32_16x16x32_bf16 v[136:139], v[188:191], v[64:67], v[136:139]\n"
      "ds_read_b128 v[64:67], v232 offset:8448\n"
      "v_cvt_pk_bf16_f32 v209, v134, v135\n"
      "v_mfma_f32_16x16x32_bf16 v[136:139], v[204:207], v[68:71], v[136:139]\n"
      "ds_read_b128 v[68:71], v233 offset:8448\n"
      "v_mfma_f32_16x16x32_bf16 v[136:139], a[24:27], v[84:87], v[136:139]\n"
      "ds_read_b128 v[84:87], v234 offset:8448\n"
      "v_mfma_f32_16x16x32_bf16 v[136:139], a[28:31], v[88:91], v[136:139]\n"
      "ds_read_b128 v[88:91], v235 offset:8448\n"
      "v_mfma_f32_16x16x32_bf16 v[136:139], a[32:35], v[92:95], v[136:139]\n"
      "ds_read_b128 v[92:95], v232 offset:16640\n"
      "v_mfma_f32_16x16x32_bf16 v[136:139], a[36:39], v[108:111], v[136:139]\n"
      "ds_read_b128 v[108:111], v233 offset:16640\n"
      "v_mfma_f32_16x16x32_bf16 v[136:139], a[40:43], v[112:115], v[136:139]\n"
      "ds_read_b128 v[112:115], v234 offset:16640\n"
      "v_mfma_f32_16x16x32_bf16 v[136:139], a[44:47], v[116:119], v[136:139]\n"
      "ds_read_b128 v[116:119], v235 offset:16640\n"
      "s_waitcnt lgkmcnt(8)\n"
      "v_mfma_f32_16x16x32_bf16 v[132:135], a[240:243], v[36:39], v[0:3]\n"
      "ds_write_b64 v244, v[208:209] offset:24704\n"
      "v_mfma_f32_16x16x32_bf16 v[132:135], a[244:247], v[40:43], v[132:135]\n"
      "s_cbranch_vccnz LT26\n"
      "LS26:\n"
      "v_mfma_f32_16x16x32_bf16 v[132:135], a[248:251], v[44:47], v[132:135]\n"
      "v_mfma_f32_16x16x32_bf16 v[132:135], a[252:255], v[60:63], v[132:135]\n"
      "v_cvt_pk_bf16_f32 v210, v136, v137\n"
      "s_waitcnt lgkmcnt(5)\n"
      "v_mfma_f32_16x16x32_bf16 v[132:135], a[48:51], v[64:67], v[132:135]\n"
      "v_cvt_pk_bf16_f32 v211, v138, v139\n"
      "v_mfma_f32_16x16x32_bf16 v[132:135], a[52:55], v[68:71], v[132:135]\n"
      "v_mfma_f32_16x16x32_bf16 v[132:135], a[56:59], v[84:87], v[132:135]\n"
      "ds_read_b128 v[96:99], v246 offset:12288\n"
      "v_mfma_f32_16x16x32_bf16 v[132:135], a[60:63], v[88:91], v[132:135]\n"
      "ds_read_b128 v[100:103], v246 offset:13312\n"
      "s_waitcnt lgkmcnt(3)\n"
      "v_mfma_f32_16x16x32_bf16 v[132:135], a[64:67], v[92:95], v[132:135]\n"
      "ds_read_b128 v[104:107], v246 offset:14336\n"
      "v_mfma_f32_16x16x32_bf16 v[132:135], a[68:71], v[108:111], v[132:135]\n"
      "ds_write_b64 v244, v[210:211] offset:24712\n"
      "v_mfma_f32_16x16x32_bf16 v[132:135], a[72:75], v[112:115], v[132:135]\n"
      "v_mfma_f32_16x16x32_bf16 v[132:135], a[76:79], v[116:119], v[132:135]\n"
      "v_mfma_f32_16x16x32_bf16 v[136:139], a[80:83], v[36:39], v[4:7]\n"
      "v_mfma_f32_16x16x32_bf16 v[136:139], a[84:87], v[40:43], v[136:139]\n"
      "v_mfma_f32_16x16x32_bf16 v[136:139], a[88:91], v[44:47], v[136:139]\n"
      "v_mfma_f32_16x16x32_bf16 v[136:139], a[92:95], v[60:63], v[136:139]\n"
      "v_cvt_pk_bf16_f32 v208, v132, v133\n"
      "v_mfma_f32_16x16x32_bf16 v[136:139], a[96:99], v[64:67], v[136:139]\n"
      "v_cvt_pk_bf16_f32 v209, v134, v135\n"
      "v_mfma_f32_16x16x32_bf16 v[136:139], a[100:103], v[68:71], v[136:139]\n"
      "v_mfma_f32_16x16x32_bf16 v[136:139], a[104:107], v[84:87], v[136:139]\n"
      "v_mfma_f32_16x16x32_bf16 v[136:139], a[108:111], v[88:91], v[136:139]\n"
      "s_add_u32 m0, s17, 0x7000\n"
      "v_mfma_f32_16x16x32_bf16 v[136:139], a[112:115], v[92:95], v[136:139]\n"
      "global_load_lds_dwordx4 v237, s[52:53] offset:0 nt\n"
      "v_mfma_f32_16x16x32_bf16 v[136:139], a[116:119], v[108:111], v[136:139]\n"
      "ds_write_b64 v244, v[208:209] offset:0\n"
      "v_mfma_f32_16x16x32_bf16 v[136:139], a[120:123], v[112:115], v[136:139]\n"
      "v_mfma_f32_16x16x32_bf16 v[136:139], a[124:127], v[116:119], v[136:139]\n"
      "v_mfma_f32_16x16x32_bf16 v[132:135], a[128:131], v[36:39], v[8:11]\n"
      "v_mfma_f32_16x16x32_bf16 v[132:135], a[132:135], v[40:43], v[132:135]\n"
      "v_mfma_f32_16x16x32_bf16 v[132:135], a[136:139], v[44:47], v[132:135]\n"
      "v_mfma_f32_16x16x32_bf16 v[132:135], a[140:143], v[60:63], v[132:135]\n"
      "v_cvt_pk_bf16_f32 v210, v136, v137\n"
      "v_mfma_f32_16x16x32_bf16 v[132:135], a[144:147], v[64:67], v[132:135]\n"
      "v_cvt_pk_bf16_f32 v211, v138, v139\n"
      "v_mfma_f32_16x16x32_bf16 v[132:135], a[148:151], v[68:71], v[132:135]\n"
      "v_mfma_f32_16x16x32_bf16 v[132:135], a[152:155], v[84:87], v[132:135]\n"
      "s_add_u32 m0, s17, 0x8f00\n"
      "v_mfma_f32_16x16x32_bf16 v[132:135], a[156:159], v[88:91], v[132:135]\n"
      "global_load_lds_dwordx4 v237, s[52:53] offset:256 nt\n"
      "v_mfma_f32_16x16x32_bf16 v[132:135], a[160:163], v[92:95], v[132:135]\n"
      "v_mfma_f32_16x16x32_bf16 v[132:135], a[164:167], v[108:111], v[132:135]\n"
      "ds_write_b64 v244, v[210:211] offset:8\n"
      "v_mfma_f32_16x16x32_bf16 v[132:135], a[168:171], v[112:115], v[132:135]\n"
      "v_mfma_f32_16x16x32_bf16 v[132:135], a[172:175], v[116:119], v[132:135]\n"
      "v_mfma_f32_16x16x32_bf16 v[136:139], a[176:179], v[36:39], v[12:15]\n"
      "s_cbranch_vccnz LT27\n"
      "LS27:\n"
      "v_mfma_f32_16x16x32_bf16 v[136:139], a[180:183], v[40:43], v[136:139]\n"
      "v_mfma_f32_16x16x32_bf16 v[136:139], a[184:187], v[44:47], v[136:139]\n"
      "v_mfma_f32_16x16x32_bf16 v[136:139], a[188:191], v[60:63], v[136:139]\n"
      "v_cvt_pk_bf16_f32 v212, v132, v133\n"
      "v_mfma_f32_16x16x32_bf16 v[136:139], a[192:195], v[64:67], v[136:139]\n"
      "v_cvt_pk_bf16_f32 v213, v134, v135\n"
      "v_mfma_f32_16x16x32_bf16 v[136:139], a[196:199], v[68:71], v[136:139]\n"
      "s_add_u32 m0, s17, 0xae00\n"
      "v_mfma_f32_16x16x32_bf16 v[136:139], a[200:203], v[84:87], v[136:139]\n"
      "global_load_lds_dwordx4 v237, s[52:53] offset:512 nt\n"
      "v_mfma_f32_16x16x32_bf16 v[136:139], a[204:207], v[88:91], v[136:139]\n"
      "v_mfma_f32_16x16x32_bf16 v[136:139], a[208:211], v[92:95], v[136:139]\n"
      "v_mfma_f32_16x16x32_bf16 v[136:139], a[212:215], v[108:111], v[136:139]\n"
      "ds_write_b64 v245, v[212:213] offset:0\n"
      "v_mfma_f32_16x16x32_bf16 v[136:139], a[216:219], v[112:115], v[136:139]\n"
      "v_mfma_f32_16x16x32_bf16 v[136:139], a[220:223], v[116:119], v[136:139]\n"
      "v_mfma_f32_16x16x32_bf16 v[132:135], a[224:227], v[36:39], v[16:19]\n"
      "v_mfma_f32_16x16x32_bf16 v[132:135], a[228:231], v[40:43], v[132:135]\n"
      "v_mfma_f32_16x16x32_bf16 v[132:135], a[232:235], v[44:47], v[132:135]\n"
      "v_mfma_f32_16x16x32_bf16 v[132:135], a[236:239], v[60:63], v[132:135]\n"
      "s_memrealtime s[84:85]\n"
      "v_mfma_f32_16x16x32_bf16 v[132:135], a[0:3], v[64:67], v[132:135]\n"
      "v_cvt_pk_bf16_f32 v214, v136, v137\n"
      "v_mfma_f32_16x16x32_bf16 v[132:135], a[4:7], v[68:71], v[132:135]\n"
      "v_cvt_pk_bf16_f32 v215, v138, v139\n"
      "v_mfma_f32_16x16x32_bf16 v[132:135], a[8:11], v[84:87], v[132:135]\n"
      "v_mfma_f32_16x16x32_bf16 v[132:135], a[12:15], v[88:91], v[132:135]\n"
      "s_add_u32 s83, s83, s86\n"
      "v_mfma_f32_16x16x32_bf16 v[132:135], a[16:19], v[92:95], v[132:135]\n"
      "v_mfma_f32_16x16x32_bf16 v[132:135], a[20:23], v[108:111], v[132:135]\n"
      "v_mfma_f32_16x16x32_bf16 v[132:135], v[140:143], v[112:115], v[132:135]\n"
      "s_waitcnt lgkmcnt(0)\n"
      "s_sub_u32 s84, s84, s83\n"
      "v_mfma_f32_16x16x32_bf16 v[132:135], v[156:159], v[116:119], v[132:135]\n"
      "s_cmp_lt_i32 s84, 0\n"
      "s_cselect_b64 vcc, -1, 0\n"
      "s_waitcnt vmcnt(21)\n"
      "s_barrier\n"
      "v_mfma_f32_16x16x32_bf16 v[136:139], v[160:163], v[36:39], v[20:23]\n"
      "ds_read_b128 v[36:39], v232 offset:24576\n"
      "ds_write_b64 v245, v[214:215] offset:8\n"
      "v_mfma_f32_16x16x32_bf16 v[136:139], v[164:167], v[40:43], v[136:139]\n"
      "ds_read_b128 v[40:43], v233 offset:24576\n"
      "v_mfma_f32_16x16x32_bf16 v[136:139], v[180:183], v[44:47], v[136:139]\n"
      "ds_read_b128 v[44:47], v234 offset:24576\n"
      "v_mfma_f32_16x16x32_bf16 v[136:139], v[184:187], v[60:63], v[136:139]\n"
      "ds_read_b128 v[60:63], v235 offset:24576\n"
      "v_cvt_pk_bf16_f32 v208, v132, v133\n"
      "v_mfma_f32_16x16x32_bf16 v[136:139], v[188:191], v[64:67], v[136:139]\n"
      "ds_read_b128 v[64:67], v232 offset:32768\n"
      "v_cvt_pk_bf16_f32 v209, v134, v135\n"
      "v_mfma_f32_16x16x32_bf16 v[136:139], v[204:207], v[68:71], v[136:139]\n"
      "ds_read_b128 v[68:71], v233 offset:32768\n"
      "v_mfma_f32_16x16x32_bf16 v[136:139], a[24:27], v[84:87], v[136:139]\n"
      "ds_read_b128 v[84:87], v234 offset:32768\n"
      "v_mfma_f32_16x16x32_bf16 v[136:139], a[28:31], v[88:91], v[136:139]\n"
      "ds_read_b128 v[88:91], v235 offset:32768\n"
      "v_mfma_f32_16x16x32_bf16 v[136:139], a[32:35], v[92:95], v[136:139]\n"
      "ds_read_b128 v[92:95], v232 offset:40960\n"
      "v_mfma_f32_16x16x32_bf16 v[136:139], a[36:39], v[108:111], v[136:139]\n"
      "ds_read_b128 v[108:111], v233 offset:40960\n"
      "v_mfma_f32_16x16x32_bf16 v[136:139], a[40:43], v[112:115], v[136:139]\n"
      "ds_read_b128 v[112:115], v234 offset:40960\n"
      "v_mfma_f32_16x16x32_bf16 v[136:139], a[44:47], v[116:119], v[136:139]\n"
      "ds_read_b128 v[116:119], v235 offset:40960\n"
      "s_waitcnt lgkmcnt(8)\n"
      "v_mfma_f32_16x16x32_bf16 v[132:135], a[240:243], v[36:39], v[0:3]\n"
      "ds_write_b64 v244, v[208:209] offset:128\n"
      "v_mfma_f32_16x16x32_bf16 v[132:135], a[244:247], v[40:43], v[132:135]\n"
      "s_cbranch_vccnz LT28\n"
      "LS28:\n"
      "v_mfma_f32_16x16x32_bf16 v[132:135], a[248:251], v[44:47], v[132:135]\n"
      "v_mfma_f32_16x16x32_bf16 v[132:135], a[252:255], v[60:63], v[132:135]\n"
      "v_cvt_pk_bf16_f32 v210, v136, v137\n"
      "s_waitcnt lgkmcnt(5)\n"
      "v_mfma_f32_16x16x32_bf16 v[132:135], a[48:51], v[64:67], v[132:135]\n"
      "v_cvt_pk_bf16_f32 v211, v138, v139\n"
      "v_mfma_f32_16x16x32_bf16 v[132:135], a[52:55], v[68:71], v[132:135]\n"
      "v_mfma_f32_16x16x32_bf16 v[132:135], a[56:59], v[84:87], v[132:135]\n"
      "ds_read_b128 v[120:123], v246 offset:24576\n"
      "v_mfma_f32_16x16x32_bf16 v[132:135], a[60:63], v[88:91], v[132:135]\n"
      "ds_read_b128 v[124:127], v246 offset:25600\n"
      "s_waitcnt lgkmcnt(3)\n"
      "v_mfma_f32_16x16x32_bf16 v[132:135], a[64:67], v[92:95], v[132:135]\n"
      "ds_read_b128 v[128:131], v246 offset:26624\n"
      "v_mfma_f32_16x16x32_bf16 v[132:135], a[68:71], v[108:111], v[132:135]\n"
      "ds_write_b64 v244, v[210:211] offset:136\n"
      "v_mfma_f32_16x16x32_bf16 v[132:135], a[72:75], v[112:115], v[132:135]\n"
      "v_mfma_f32_16x16x32_bf16 v[132:135], a[76:79], v[116:119], v[132:135]\n"
      "v_mfma_f32_16x16x32_bf16 v[136:139], a[80:83], v[36:39], v[4:7]\n"
      "v_mfma_f32_16x16x32_bf16 v[136:139], a[84:87], v[40:43], v[136:139]\n"
      "v_mfma_f32_16x16x32_bf16 v[136:139], a[88:91], v[44:47], v[136:139]\n"
      "v_mfma_f32_16x16x32_bf16 v[136:139], a[92:95], v[60:63], v[136:139]\n"
      "v_cvt_pk_bf16_f32 v208, v132, v133\n"
      "v_mfma_f32_16x16x32_bf16 v[136:139], a[96:99], v[64:67], v[136:139]\n"
      "v_cvt_pk_bf16_f32 v209, v134, v135\n"
      "v_mfma_f32_16x16x32_bf16 v[136:139], a[100:103], v[68:71], v[136:139]\n"
      "v_mfma_f32_16x16x32_bf16 v[136:139], a[104:107], v[84:87], v[136:139]\n"
      "v_mfma_f32_16x16x32_bf16 v[136:139], a[108:111], v[88:91], v[136:139]\n"
      "v_mfma_f32_16x16x32_bf16 v[136:139], a[112:115], v[92:95], v[136:139]\n"
      "v_mfma_f32_16x16x32_bf16 v[136:139], a[116:119], v[108:111], v[136:139]\n"
      "ds_write_b64 v244, v[208:209] offset:12288\n"
      "v_mfma_f32_16x16x32_bf16 v[136:139], a[120:123], v[112:115], v[136:139]\n"
      "v_mfma_f32_16x16x32_bf16 v[136:139], a[124:127], v[116:119], v[136:139]\n"
      "v_mfma_f32_16x16x32_bf16 v[132:135], a[128:131], v[36:39], v[8:11]\n"
      "v_mfma_f32_16x16x32_bf16 v[132:135], a[132:135], v[40:43], v[132:135]\n"
      "v_mfma_f32_16x16x32_bf16 v[132:135], a[136:139], v[44:47], v[132:135]\n"
      "v_mfma_f32_16x16x32_bf16 v[132:135], a[140:143], v[60:63], v[132:135]\n"
      "v_cvt_pk_bf16_f32 v210, v136, v137\n"
      "v_mfma_f32_16x16x32_bf16 v[132:135], a[144:147], v[64:67], v[132:135]\n"
      "v_cvt_pk_bf16_f32 v211, v138, v139\n"
      "v_mfma_f32_16x16x32_bf16 v[132:135], a[148:151], v[68:71], v[132:135]\n"
      "v_mfma_f32_16x16x32_bf16 v[132:135], a[152:155], v[84:87], v[132:135]\n"
      "v_mfma_f32_16x16x32_bf16 v[132:135], a[156:159], v[88:91], v[132:135]\n"
      "v_mfma_f32_16x16x32_bf16 v[132:135], a[160:163], v[92:95], v[132:135]\n"
      "v_mfma_f32_16x16x32_bf16 v[132:135], a[164:167], v[108:111], v[132:135]\n"
      "ds_write_b64 v244, v[210:211] offset:12296\n"
      "v_mfma_f32_16x16x32_bf16 v[132:135], a[168:171], v[112:115], v[132:135]\n"
      "v_mfma_f32_16x16x32_bf16 v[132:135], a[172:175], v[116:119], v[132:135]\n"
      "v_mfma_f32_16x16x32_bf16 v[136:139], a[176:179], v[36:39], v[12:15]\n"
      "s_cbranch_vccnz LT29\n"
      "LS29:\n"
      "v_mfma_f32_16x16x32_bf16 v[136:139], a[180:183], v[40:43], v[136:139]\n"
      "v_mfma_f32_16x16x32_bf16 v[136:139], a[184:187], v[44:47], v[136:139]\n"
      "v_mfma_f32_16x16x32_bf16 v[136:139], a[188:191], v[60:63], v[136:139]\n"
      "v_cvt_pk_bf16_f32 v212, v132, v133\n"
      "v_mfma_f32_16x16x32_bf16 v[136:139], a[192:195], v[64:67], v[136:139]\n"
      "v_cvt_pk_bf16_f32 v213, v134, v135\n"
      "v_mfma_f32_16x16x32_bf16 v[136:139], a[196:199], v[68:71], v[136:139]\n"
      "v_mfma_f32_16x16x32_bf16 v[136:139], a[200:203], v[84:87], v[136:139]\n"
      "v_mfma_f32_16x16x32_bf16 v[136:139], a[204:207], v[88:91], v[136:139]\n"
      "v_mfma_f32_16x16x32_bf16 v[136:139], a[208:211], v[92:95], v[136:139]\n"
      "v_mfma_f32_16x16x32_bf16 v[136:139], a[212:215], v[108:111], v[136:139]\n"
      "ds_write_b64 v245, v[212:213] offset:12288\n"
      "v_mfma_f32_16x16x32_bf16 v[136:139], a[216:219], v[112:115], v[136:139]\n"
      "v_mfma_f32_16x16x32_bf16 v[136:139], a[220:223], v[116:119], v[136:139]\n"
      "v_mfma_f32_16x16x32_bf16 v[132:135], a[224:227], v[36:39], v[16:19]\n"
      "v_mfma_f32_16x16x32_bf16 v[132:135], a[228:231], v[40:43], v[132:135]\n"
      "v_mfma_f32_16x16x32_bf16 v[132:135], a[232:235], v[44:47], v[132:135]\n"
      "v_mfma_f32_16x16x32_bf16 v[132:135], a[236:239], v[60:63], v[132:135]\n"
      "v_cvt_pk_bf16_f32 v214, v136, v137\n"
      "v_mfma_f32_16x16x32_bf16 v[132:135], a[0:3], v[64:67], v[132:135]\n"
      "v_cvt_pk_bf16_f32 v215, v138, v139\n"
      "v_mfma_f32_16x16x32_bf16 v[132:135], a[4:7], v[68:71], v[132:135]\n"
      "v_mfma_f32_16x16x32_bf16 v[132:135], a[8:11], v[84:87], v[132:135]\n"
      "v_mfma_f32_16x16x32_bf16 v[132:135], a[12:15], v[88:91], v[132:135]\n"
      "v_mfma_f32_16x16x32_bf16 v[132:135], a[16:19], v[92:95], v[132:135]\n"
      "v_mfma_f32_16x16x32_bf16 v[132:135], a[20:23], v[108:111], v[132:135]\n"
      "ds_write_b64 v245, v[214:215] offset:12296\n"
      "v_mfma_f32_16x16x32_bf16 v[132:135], v[140:143], v[112:115], v[132:135]\n"
      "v_mfma_f32_16x16x32_bf16 v[132:135], v[156:159], v[116:119], v[132:135]\n"
      "s_waitcnt lgkmcnt(4)\n"
      "s_barrier\n"
      "v_mfma_f32_16x16x32_bf16 v[136:139], v[160:163], v[36:39], v[20:23]\n"
      "ds_read_b128 v[36:39], v232 offset:24832\n"
      "v_mfma_f32_16x16x32_bf16 v[136:139], v[164:167], v[40:43], v[136:139]\n"
      "ds_read_b128 v[40:43], v233 offset:24832\n"
      "v_mfma_f32_16x16x32_bf16 v[136:139], v[180:183], v[44:47], v[136:139]\n"
      "ds_read_b128 v[44:47], v234 offset:24832\n"
      "v_mfma_f32_16x16x32_bf16 v[136:139], v[184:187], v[60:63], v[136:139]\n"
      "ds_read_b128 v[60:63], v235 offset:24832\n"
      "v_cvt_pk_bf16_f32 v208, v132, v133\n"
      "v_mfma_f32_16x16x32_bf16 v[136:139], v[188:191], v[64:67], v[136:139]\n"
      "ds_read_b128 v[64:67], v232 offset:33024\n"
      "v_cvt_pk_bf16_f32 v209, v134, v135\n"
      "v_mfma_f32_16x16x32_bf16 v[136:139], v[204:207], v[68:71], v[136:139]\n"
      "ds_read_b128 v[68:71], v233 offset:33024\n"
      "v_mfma_f32_16x16x32_bf16 v[136:139], a[24:27], v[84:87], v[136:139]\n"
      "ds_read_b128 v[84:87], v234 offset:33024\n"
      "v_mfma_f32_16x16x32_bf16 v[136:139], a[28:31], v[88:91], v[136:139]\n"
      "ds_read_b128 v[88:91], v235 offset:33024\n"
      "v_mfma_f32_16x16x32_bf16 v[136:139], a[32:35], v[92:95], v[136:139]\n"
      "ds_read_b128 v[92:95], v232 offset:41216\n"
      "v_mfma_f32_16x16x32_bf16 v[136:139], a[36:39], v[108:111], v[136:139]\n"
      "ds_read_b128 v[108:111], v233 offset:41216\n"
      "ds_write_b64 v244, v[208:209] offset:12416\n"
      "v_mfma_f32_16x16x32_bf16 v[136:139], a[40:43], v[112:115], v[136:139]\n"
      "LPD15:\n"
      "s_cbranch_execz LRET\n"
      "ds_read_b128 v[112:115], v234 offset:41216\n"
      "v_mfma_f32_16x16x32_bf16 v[136:139], a[44:47], v[116:119], v[136:139]\n"
      "ds_read_b128 v[116:119], v235 offset:41216\n"
      "s_waitcnt lgkmcnt(9)\n"
      "v_mfma_f32_16x16x32_bf16 v[132:135], a[240:243], v[36:39], v[0:3]\n"
      "v_add_u32 v232, 0xffff4000, v232\n"
      "s_cbranch_vccnz LT30\n"
      "LS30:\n"
      "v_mfma_f32_16x16x32_bf16 v[132:135], a[244:247], v[40:43], v[132:135]\n"
      "v_add_u32 v233, 0xffff4000, v233\n"
      "v_mfma_f32_16x16x32_bf16 v[132:135], a[248:251], v[44:47], v[132:135]\n"
      "v_add_u32 v234, 0xffff4000, v234\n"
      "v_mfma_f32_16x16x32_bf16 v[132:135], a[252:255], v[60:63], v[132:135]\n"
      "v_add_u32 v235, 0xffff4000, v235\n"
      "s_waitcnt lgkmcnt(5)\n"
      "v_mfma_f32_16x16x32_bf16 v[132:135], a[48:51], v[64:67], v[132:135]\n"
      "v_cvt_pk_bf16_f32 v210, v136, v137\n"
      "v_mfma_f32_16x16x32_bf16 v[132:135], a[52:55], v[68:71], v[132:135]\n"
      "v_cvt_pk_bf16_f32 v211, v138, v139\n"
      "v_mfma_f32_16x16x32_bf16 v[132:135], a[56:59], v[84:87], v[132:135]\n"
      "ds_read_b128 v[144:147], v246 offset:0\n"
      "v_mfma_f32_16x16x32_bf16 v[132:135], a[60:63], v[88:91], v[132:135]\n"
      "ds_read_b128 v[148:151], v246 offset:1024\n"
      "s_waitcnt lgkmcnt(2)\n"
      "v_mfma_f32_16x16x32_bf16 v[132:135], a[64:67], v[92:95], v[132:135]\n"
      "ds_read_b128 v[152:155], v246 offset:2048\n"
      "v_mfma_f32_16x16x32_bf16 v[132:135], a[68:71], v[108:111], v[132:135]\n"
      "v_lshlrev_b32 v0, 16, v192\n"
      "v_mfma_f32_16x16x32_bf16 v[132:135], a[72:75], v[112:115], v[132:135]\n"
      "ds_write_b64 v244, v[210:211] offset:12424\n"
      "v_mfma_f32_16x16x32_bf16 v[132:135], a[76:79], v[116:119], v[132:135]\n"
      "v_and_b32 v1, 0xffff0000, v192\n"
      "v_mfma_f32_16x16x32_bf16 v[136:139], a[80:83], v[36:39], v[4:7]\n"
      "v_lshlrev_b32 v2, 16, v193\n"
      "v_mfma_f32_16x16x32_bf16 v[136:139], a[84:87], v[40:43], v[136:139]\n"
      "v_and_b32 v3, 0xffff0000, v193\n"
      "v_mfma_f32_16x16x32_bf16 v[136:139], a[88:91], v[44:47], v[136:139]\n"
      "v_mfma_f32_16x16x32_bf16 v[136:139], a[92:95], v[60:63], v[136:139]\n"
      "v_cvt_pk_bf16_f32 v208, v132, v133\n"
      "v_mfma_f32_16x16x32_bf16 v[136:139], a[96:99], v[64:67], v[136:139]\n"
      "v_cvt_pk_bf16_f32 v209, v134, v135\n"
      "v_mfma_f32_16x16x32_bf16 v[136:139], a[100:103], v[68:71], v[136:139]\n"
      "v_lshlrev_b32 v4, 16, v194\n"
      "v_mfma_f32_16x16x32_bf16 v[136:139], a[104:107], v[84:87], v[136:139]\n"
      "v_and_b32 v5, 0xffff0000, v194\n"
      "v_mfma_f32_16x16x32_bf16 v[136:139], a[108:111], v[88:91], v[136:139]\n"
      "v_lshlrev_b32 v6, 16, v195\n"
      "v_mfma_f32_16x16x32_bf16 v[136:139], a[112:115], v[92:95], v[136:139]\n"
      "v_and_b32 v7, 0xffff0000, v195\n"
      "v_mfma_f32_16x16x32_bf16 v[136:139], a[116:119], v[108:111], v[136:139]\n"
      "ds_write_b64 v244, v[208:209] offset:24576\n"
      "v_mfma_f32_16x16x32_bf16 v[136:139], a[120:123], v[112:115], v[136:139]\n"
      "v_mfma_f32_16x16x32_bf16 v[136:139], a[124:127], v[116:119], v[136:139]\n"
      "v_mfma_f32_16x16x32_bf16 v[132:135], a[128:131], v[36:39], v[8:11]\n"
      "v_mfma_f32_16x16x32_bf16 v[132:135], a[132:135], v[40:43], v[132:135]\n"
      "v_mfma_f32_16x16x32_bf16 v[132:135], a[136:139], v[44:47], v[132:135]\n"
      "v_mfma_f32_16x16x32_bf16 v[132:135], a[140:143], v[60:63], v[132:135]\n"
      "v_cvt_pk_bf16_f32 v210, v136, v137\n"
      "v_mfma_f32_16x16x32_bf16 v[132:135], a[144:147], v[64:67], v[132:135]\n"
      "v_cvt_pk_bf16_f32 v211, v138, v139\n"
      "v_mfma_f32_16x16x32_bf16 v[132:135], a[148:151], v[68:71], v[132:135]\n"
      "v_lshlrev_b32 v8, 16, v196\n"
      "v_mfma_f32_16x16x32_bf16 v[132:135], a[152:155], v[84:87], v[132:135]\n"
      "v_and_b32 v9, 0xffff0000, v196\n"
      "v_mfma_f32_16x16x32_bf16 v[132:135], a[156:159], v[88:91], v[132:135]\n"
      "v_lshlrev_b32 v10, 16, v197\n"
      "v_mfma_f32_16x16x32_bf16 v[132:135], a[160:163], v[92:95], v[132:135]\n"
      "v_and_b32 v11, 0xffff0000, v197\n"
      "v_mfma_f32_16x16x32_bf16 v[132:135], a[164:167], v[108:111], v[132:135]\n"
      "ds_write_b64 v244, v[210:211] offset:24584\n"
      "v_mfma_f32_16x16x32_bf16 v[132:135], a[168:171], v[112:115], v[132:135]\n"
      "v_mfma_f32_16x16x32_bf16 v[132:135], a[172:175], v[116:119], v[132:135]\n"
      "v_mfma_f32_16x16x32_bf16 v[136:139], a[176:179], v[36:39], v[12:15]\n"
      "s_cbranch_vccnz LT31\n"
      "LS31:\n"
      "v_mfma_f32_16x16x32_bf16 v[136:139], a[180:183], v[40:43], v[136:139]\n"
      "v_mfma_f32_16x16x32_bf16 v[136:139], a[184:187], v[44:47], v[136:139]\n"
      "v_mfma_f32_16x16x32_bf16 v[136:139], a[188:191], v[60:63], v[136:139]\n"
      "v_cvt_pk_bf16_f32 v212, v132, v133\n"
      "v_mfma_f32_16x16x32_bf16 v[136:139], a[192:195], v[64:67], v[136:139]\n"
      "v_cvt_pk_bf16_f32 v213, v134, v135\n"
      "v_mfma_f32_16x16x32_bf16 v[136:139], a[196:199], v[68:71], v[136:139]\n"
      "v_lshlrev_b32 v12, 16, v198\n"
      "v_mfma_f32_16x16x32_bf16 v[136:139], a[200:203], v[84:87], v[136:139]\n"
      "v_and_b32 v13, 0xffff0000, v198\n"
      "v_mfma_f32_16x16x32_bf16 v[136:139], a[204:207], v[88:91], v[136:139]\n"
      "v_lshlrev_b32 v14, 16, v199\n"
      "v_mfma_f32_16x16x32_bf16 v[136:139], a[208:211], v[92:95], v[136:139]\n"
      "v_and_b32 v15, 0xffff0000, v199\n"
      "v_mfma_f32_16x16x32_bf16 v[136:139], a[212:215], v[108:111], v[136:139]\n"
      "ds_write_b64 v245, v[212:213] offset:24576\n"
      "v_mfma_f32_16x16x32_bf16 v[136:139], a[216:219], v[112:115], v[136:139]\n"
      "v_mfma_f32_16x16x32_bf16 v[136:139], a[220:223], v[116:119], v[136:139]\n"
      "v_mfma_f32_16x16x32_bf16 v[132:135], a[224:227], v[36:39], v[16:19]\n"
      "v_mfma_f32_16x16x32_bf16 v[132:135], a[228:231], v[40:43], v[132:135]\n"
      "v_mfma_f32_16x16x32_bf16 v[132:135], a[232:235], v[44:47], v[132:135]\n"
      "v_mfma_f32_16x16x32_bf16 v[132:135], a[236:239], v[60:63], v[132:135]\n"
      "v_cvt_pk_bf16_f32 v214, v136, v137\n"
      "v_mfma_f32_16x16x32_bf16 v[132:135], a[0:3], v[64:67], v[132:135]\n"
      "v_cvt_pk_bf16_f32 v215, v138, v139\n"
      "v_mfma_f32_16x16x32_bf16 v[132:135], a[4:7], v[68:71], v[132:135]\n"
      "v_lshlrev_b32 v16, 16, v200\n"
      "v_mfma_f32_16x16x32_bf16 v[132:135], a[8:11], v[84:87], v[132:135]\n"
      "v_and_b32 v17, 0xffff0000, v200\n"
      "v_mfma_f32_16x16x32_bf16 v[132:135], a[12:15], v[88:91], v[132:135]\n"
      "v_lshlrev_b32 v18, 16, v201\n"
      "v_mfma_f32_16x16x32_bf16 v[132:135], a[16:19], v[92:95], v[132:135]\n"
      "v_and_b32 v19, 0xffff0000, v201\n"
      "v_mfma_f32_16x16x32_bf16 v[132:135], a[20:23], v[108:111], v[132:135]\n"
      "ds_write_b64 v245, v[214:215] offset:24584\n"
      "v_mfma_f32_16x16x32_bf16 v[132:135], v[140:143], v[112:115], v[132:135]\n"
      "v_mfma_f32_16x16x32_bf16 v[132:135], v[156:159], v[116:119], v[132:135]\n"
      "s_waitcnt vmcnt(9) lgkmcnt(4)\n"
      "s_barrier\n"
      "v_mfma_f32_16x16x32_bf16 v[136:139], v[160:163], v[36:39], v[20:23]\n"
      "ds_read_b128 v[36:39], v232 offset:0\n"
      "v_mfma_f32_16x16x32_bf16 v[136:139], v[164:167], v[40:43], v[136:139]\n"
      "ds_read_b128 v[40:43], v233 offset:0\n"
      "v_mfma_f32_16x16x32_bf16 v[136:139], v[180:183], v[44:47], v[136:139]\n"
      "ds_read_b128 v[44:47], v234 offset:0\n"
      "v_mfma_f32_16x16x32_bf16 v[136:139], v[184:187], v[60:63], v[136:139]\n"
      "ds_read_b128 v[60:63], v235 offset:0\n"
      "v_cvt_pk_bf16_f32 v208, v132, v133\n"
      "v_mfma_f32_16x16x32_bf16 v[136:139], v[188:191], v[64:67], v[136:139]\n"
      "ds_read_b128 v[64:67], v232 offset:8192\n"
      "v_cvt_pk_bf16_f32 v209, v134, v135\n"
      "v_mfma_f32_16x16x32_bf16 v[136:139], v[204:207], v[68:71], v[136:139]\n"
      "ds_read_b128 v[68:71], v233 offset:8192\n"
      "v_mfma_f32_16x16x32_bf16 v[136:139], a[24:27], v[84:87], v[136:139]\n"
      "ds_read_b128 v[84:87], v234 offset:8192\n"
      "v_mfma_f32_16x16x32_bf16 v[136:139], a[28:31], v[88:91], v[136:139]\n"
      "ds_read_b128 v[88:91], v235 offset:8192\n"
      "v_mfma_f32_16x16x32_bf16 v[136:139], a[32:35], v[92:95], v[136:139]\n"
      "ds_read_b128 v[92:95], v232 offset:16384\n"
      "v_mfma_f32_16x16x32_bf16 v[136:139], a[36:39], v[108:111], v[136:139]\n"
      "ds_read_b128 v[108:111], v233 offset:16384\n"
      "v_mfma_f32_16x16x32_bf16 v[136:139], a[40:43], v[112:115], v[136:139]\n"
      "ds_read_b128 v[112:115], v234 offset:16384\n"
      "v_mfma_f32_16x16x32_bf16 v[136:139], a[44:47], v[116:119], v[136:139]\n"
      "ds_read_b128 v[116:119], v235 offset:16384\n"
      "s_waitcnt lgkmcnt(8)\n"
      "v_mfma_f32_16x16x32_bf16 v[132:135], a[240:243], v[36:39], v[0:3]\n"
      "ds_write_b64 v244, v[208:209] offset:24704\n"
      "v_mfma_f32_16x16x32_bf16 v[132:135], a[244:247], v[40:43], v[132:135]\n"
      "v_lshlrev_b32 v20, 16, v202\n"
      "v_mfma_f32_16x16x32_bf16 v[132:135], a[248:251], v[44:47], v[132:135]\n"
      "v_and_b32 v21, 0xffff0000, v202\n"
      "v_mfma_f32_16x16x32_bf16 v[132:135], a[252:255], v[60:63], v[132:135]\n"
      "v_lshlrev_b32 v22, 16, v203\n"
      "global_store_dwordx4 v247, v[96:99], s[54:55] offset:768 sc1\n"
      "s_waitcnt lgkmcnt(5)\n"
      "v_mfma_f32_16x16x32_bf16 v[132:135], a[48:51], v[64:67], v[132:135]\n"
      "v_and_b32 v23, 0xffff0000, v203\n"
      "v_mfma_f32_16x16x32_bf16 v[132:135], a[52:55], v[68:71], v[132:135]\n"
      "v_cvt_pk_bf16_f32 v210, v136, v137\n"
      "v_mfma_f32_16x16x32_bf16 v[132:135], a[56:59], v[84:87], v[132:135]\n"
      "v_cvt_pk_bf16_f32 v211, v138, v139\n"
      "v_mfma_f32_16x16x32_bf16 v[132:135], a[60:63], v[88:91], v[132:135]\n"
      "ds_read_b128 v[24:27], v246 offset:12288\n"
      "s_waitcnt lgkmcnt(2)\n"
      "v_mfma_f32_16x16x32_bf16 v[132:135], a[64:67], v[92:95], v[132:135]\n"
      "ds_read_b128 v[28:31], v246 offset:13312\n"
      "v_mfma_f32_16x16x32_bf16 v[132:135], a[68:71], v[108:111], v[132:135]\n"
      "ds_read_b128 v[32:35], v246 offset:14336\n"
      "v_mfma_f32_16x16x32_bf16 v[132:135], a[72:75], v[112:115], v[132:135]\n"
      "v_mfma_f32_16x16x32_bf16 v[132:135], a[76:79], v[116:119], v[132:135]\n"
      "ds_write_b64 v244, v[210:211] offset:24712\n"
      "v_mfma_f32_16x16x32_bf16 v[136:139], a[80:83], v[36:39], v[4:7]\n"
      "v_mfma_f32_16x16x32_bf16 v[136:139], a[84:87], v[40:43], v[136:139]\n"
      "v_mfma_f32_16x16x32_bf16 v[136:139], a[88:91], v[44:47], v[136:139]\n"
      "v_mfma_f32_16x16x32_bf16 v[136:139], a[92:95], v[60:63], v[136:139]\n"
      "v_cvt_pk_bf16_f32 v208, v132, v133\n"
      "global_store_dwordx4 v252, v[100:103], s[54:55] offset:768 sc1\n"
      "v_mfma_f32_16x16x32_bf16 v[136:139], a[96:99], v[64:67], v[136:139]\n"
      "v_cvt_pk_bf16_f32 v209, v134, v135\n"
      "v_mfma_f32_16x16x32_bf16 v[136:139], a[100:103], v[68:71], v[136:139]\n"
      "v_mfma_f32_16x16x32_bf16 v[136:139], a[104:107], v[84:87], v[136:139]\n"
      "v_mfma_f32_16x16x32_bf16 v[136:139], a[108:111], v[88:91], v[136:139]\n"
      "v_mfma_f32_16x16x32_bf16 v[136:139], a[112:115], v[92:95], v[136:139]\n"
      "v_mfma_f32_16x16x32_bf16 v[136:139], a[116:119], v[108:111], v[136:139]\n"
      "ds_write_b64 v244, v[208:209] offset:0\n"
      "v_mfma_f32_16x16x32_bf16 v[136:139], a[120:123], v[112:115], v[136:139]\n"
      "v_mfma_f32_16x16x32_bf16 v[136:139], a[124:127], v[116:119], v[136:139]\n"
      "v_mfma_f32_16x16x32_bf16 v[132:135], a[128:131], v[36:39], v[8:11]\n"
      "v_mfma_f32_16x16x32_bf16 v[132:135], a[132:135], v[40:43], v[132:135]\n"
      "v_mfma_f32_16x16x32_bf16 v[132:135], a[136:139], v[44:47], v[132:135]\n"
      "v_mfma_f32_16x16x32_bf16 v[132:135], a[140:143], v[60:63], v[132:135]\n"
      "v_cvt_pk_bf16_f32 v210, v136, v137\n"
      "v_mfma_f32_16x16x32_bf16 v[132:135], a[144:147], v[64:67], v[132:135]\n"
      "v_cvt_pk_bf16_f32 v211, v138, v139\n"
      "v_mfma_f32_16x16x32_bf16 v[132:135], a[148:151], v[68:71], v[132:135]\n"
      "global_store_dwordx4 v253, v[104:107], s[54:55] offset:768 sc1\n"
      "v_mfma_f32_16x16x32_bf16 v[132:135], a[152:155], v[84:87], v[132:135]\n"
      "v_mfma_f32_16x16x32_bf16 v[132:135], a[156:159], v[88:91], v[132:135]\n"
      "s_lshl_b32 s79, s96, 1\n"
      "v_mfma_f32_16x16x32_bf16 v[132:135], a[160:163], v[92:95], v[132:135]\n"
      "v_mad_i32_i24 v252, v251, s79, v252\n"
      "v_mfma_f32_16x16x32_bf16 v[132:135], a[164:167], v[108:111], v[132:135]\n"
      "ds_write_b64 v244, v[210:211] offset:8\n"
      "v_mfma_f32_16x16x32_bf16 v[132:135], a[168:171], v[112:115], v[132:135]\n"
      "s_mul_i32 s79, s79, s100\n"
      "v_mfma_f32_16x16x32_bf16 v[132:135], a[172:175], v[116:119], v[132:135]\n"
      "v_add_u32 v247, s79, v247\n"
      "v_mfma_f32_16x16x32_bf16 v[136:139], a[176:179], v[36:39], v[12:15]\n"
      "s_mul_i32 s79, s79, s101\n"
      "v_mfma_f32_16x16x32_bf16 v[136:139], a[180:183], v[40:43], v[136:139]\n"
      "v_add_u32 v253, s79, v253\n"
      "v_mfma_f32_16x16x32_bf16 v[136:139], a[184:187], v[44:47], v[136:139]\n"
      "s_add_u32 s54, s30, s39\n"
      "s_addc_u32 s55, s31, 0\n"
      "v_mfma_f32_16x16x32_bf16 v[136:139], a[188:191], v[60:63], v[136:139]\n"
      "v_cvt_pk_bf16_f32 v212, v132, v133\n"
      "v_mfma_f32_16x16x32_bf16 v[136:139], a[192:195], v[64:67], v[136:139]\n"
      "v_cvt_pk_bf16_f32 v213, v134, v135\n"
      "v_mfma_f32_16x16x32_bf16 v[136:139], a[196:199], v[68:71], v[136:139]\n"
      "global_store_dwordx4 v247, v[120:123], s[54:55] offset:0 sc1\n"
      "v_mfma_f32_16x16x32_bf16 v[136:139], a[200:203], v[84:87], v[136:139]\n"
      "v_mfma_f32_16x16x32_bf16 v[136:139], a[204:207], v[88:91], v[136:139]\n"
      "v_mfma_f32_16x16x32_bf16 v[136:139], a[208:211], v[92:95], v[136:139]\n"
      "v_mfma_f32_16x16x32_bf16 v[136:139], a[212:215], v[108:111], v[136:139]\n"
      "ds_write_b64 v245, v[212:213] offset:0\n"
      "v_mfma_f32_16x16x32_bf16 v[136:139], a[216:219], v[112:115], v[136:139]\n"
      "v_mfma_f32_16x16x32_bf16 v[136:139], a[220:223], v[116:119], v[136:139]\n"
      "v_mfma_f32_16x16x32_bf16 v[132:135], a[224:227], v[36:39], v[16:19]\n"
      "v_mfma_f32_16x16x32_bf16 v[132:135], a[228:231], v[40:43], v[132:135]\n"
      "v_mfma_f32_16x16x32_bf16 v[132:135], a[232:235], v[44:47], v[132:135]\n"
      "v_mfma_f32_16x16x32_bf16 v[132:135], a[236:239], v[60:63], v[132:135]\n"
      "v_cvt_pk_bf16_f32 v214, v136, v137\n"
      "v_mfma_f32_16x16x32_bf16 v[132:135], a[0:3], v[64:67], v[132:135]\n"
      "v_cvt_pk_bf16_f32 v215, v138, v139\n"
      "v_mfma_f32_16x16x32_bf16 v[132:135], a[4:7], v[68:71], v[132:135]\n"
      "v_mfma_f32_16x16x32_bf16 v[132:135], a[8:11], v[84:87], v[132:135]\n"
      "global_store_dwordx4 v252, v[124:127], s[54:55] offset:0 sc1\n"
      "v_mfma_f32_16x16x32_bf16 v[132:135], a[12:15], v[88:91], v[132:135]\n"
      "v_mfma_f32_16x16x32_bf16 v[132:135], a[16:19], v[92:95], v[132:135]\n"
      "v_mfma_f32_16x16x32_bf16 v[132:135], a[20:23], v[108:111], v[132:135]\n"
      "ds_write_b64 v245, v[214:215] offset:8\n"
      "v_mfma_f32_16x16x32_bf16 v[132:135], v[140:143], v[112:115], v[132:135]\n"
      "v_mfma_f32_16x16x32_bf16 v[132:135], v[156:159], v[116:119], v[132:135]\n"
      "s_waitcnt lgkmcnt(4)\n"
      "s_barrier\n"
      "v_mfma_f32_16x16x32_bf16 v[136:139], v[160:163], v[36:39], v[20:23]\n"
      "ds_read_b128 v[36:39], v232 offset:256\n"
      "v_mfma_f32_16x16x32_bf16 v[136:139], v[164:167], v[40:43], v[136:139]\n"
      "ds_read_b128 v[40:43], v233 offset:256\n"
      "v_mfma_f32_16x16x32_bf16 v[136:139], v[180:183], v[44:47], v[136:139]\n"
      "ds_read_b128 v[44:47], v234 offset:256\n"
      "v_mfma_f32_16x16x32_bf16 v[136:139], v[184:187], v[60:63], v[136:139]\n"
      "ds_read_b128 v[60:63], v235 offset:256\n"
      "v_cvt_pk_bf16_f32 v208, v132, v133\n"
      "v_mfma_f32_16x16x32_bf16 v[136:139], v[188:191], v[64:67], v[136:139]\n"
      "ds_read_b128 v[64:67], v232 offset:8448\n"
      "v_cvt_pk_bf16_f32 v209, v134, v135\n"
      "v_mfma_f32_16x16x32_bf16 v[136:139], v[204:207], v[68:71], v[136:139]\n"
      "ds_read_b128 v[68:71], v233 offset:8448\n"
      "v_mfma_f32_16x16x32_bf16 v[136:139], a[24:27], v[84:87], v[136:139]\n"
      "ds_read_b128 v[84:87], v234 offset:8448\n"
      "v_mfma_f32_16x16x32_bf16 v[136:139], a[28:31], v[88:91], v[136:139]\n"
      "ds_read_b128 v[88:91], v235 offset:8448\n"
      "v_mfma_f32_16x16x32_bf16 v[136:139], a[32:35], v[92:95], v[136:139]\n"
      "ds_read_b128 v[92:95], v232 offset:16640\n"
      "v_mfma_f32_16x16x32_bf16 v[136:139], a[36:39], v[108:111], v[136:139]\n"
      "ds_read_b128 v[108:111], v233 offset:16640\n"
      "v_mfma_f32_16x16x32_bf16 v[136:139], a[40:43], v[112:115], v[136:139]\n"
      "ds_read_b128 v[112:115], v234 offset:16640\n"
      "v_mfma_f32_16x16x32_bf16 v[136:139], a[44:47], v[116:119], v[136:139]\n"
      "ds_read_b128 v[116:119], v235 offset:16640\n"
      "s_waitcnt lgkmcnt(8)\n"
      "v_mfma_f32_16x16x32_bf16 v[132:135], a[240:243], v[36:39], v[0:3]\n"
      "ds_write_b64 v244, v[208:209] offset:128\n"
      "v_mfma_f32_16x16x32_bf16 v[132:135], a[244:247], v[40:43], v[132:135]\n"
      "v_mfma_f32_16x16x32_bf16 v[132:135], a[248:251], v[44:47], v[132:135]\n"
      "v_mfma_f32_16x16x32_bf16 v[132:135], a[252:255], v[60:63], v[132:135]\n"
      "v_cvt_pk_bf16_f32 v210, v136, v137\n"
      "global_store_dwordx4 v253, v[128:131], s[54:55] offset:0 sc1\n"
      "s_waitcnt lgkmcnt(5)\n"
      "v_mfma_f32_16x16x32_bf16 v[132:135], a[48:51], v[64:67], v[132:135]\n"
      "v_cvt_pk_bf16_f32 v211, v138, v139\n"
      "v_mfma_f32_16x16x32_bf16 v[132:135], a[52:55], v[68:71], v[132:135]\n"
      "v_mfma_f32_16x16x32_bf16 v[132:135], a[56:59], v[84:87], v[132:135]\n"
      "ds_read_b128 v[48:51], v246 offset:24576\n"
      "v_mfma_f32_16x16x32_bf16 v[132:135], a[60:63], v[88:91], v[132:135]\n"
      "ds_read_b128 v[52:55], v246 offset:25600\n"
      "s_waitcnt lgkmcnt(3)\n"
      "v_mfma_f32_16x16x32_bf16 v[132:135], a[64:67], v[92:95], v[132:135]\n"
      "ds_read_b128 v[56:59], v246 offset:26624\n"
      "v_mfma_f32_16x16x32_bf16 v[132:135], a[68:71], v[108:111], v[132:135]\n"
      "ds_write_b64 v244, v[210:211] offset:136\n"
      "v_mfma_f32_16x16x32_bf16 v[132:135], a[72:75], v[112:115], v[132:135]\n"
      "v_mfma_f32_16x16x32_bf16 v[132:135], a[76:79], v[116:119], v[132:135]\n"
      "v_mfma_f32_16x16x32_bf16 v[136:139], a[80:83], v[36:39], v[4:7]\n"
      "v_mfma_f32_16x16x32_bf16 v[136:139], a[84:87], v[40:43], v[136:139]\n"
      "v_mfma_f32_16x16x32_bf16 v[136:139], a[88:91], v[44:47], v[136:139]\n"
      "v_mfma_f32_16x16x32_bf16 v[136:139], a[92:95], v[60:63], v[136:139]\n"
      "v_cvt_pk_bf16_f32 v208, v132, v133\n"
      "global_store_dwordx4 v247, v[144:147], s[54:55] offset:768 sc1\n"
      "v_mfma_f32_16x16x32_bf16 v[136:139], a[96:99], v[64:67], v[136:139]\n"
      "v_cvt_pk_bf16_f32 v209, v134, v135\n"
      "v_mfma_f32_16x16x32_bf16 v[136:139], a[100:103], v[68:71], v[136:139]\n"
      "v_mfma_f32_16x16x32_bf16 v[136:139], a[104:107], v[84:87], v[136:139]\n"
      "v_mfma_f32_16x16x32_bf16 v[136:139], a[108:111], v[88:91], v[136:139]\n"
      "v_mfma_f32_16x16x32_bf16 v[136:139], a[112:115], v[92:95], v[136:139]\n"
      "v_mfma_f32_16x16x32_bf16 v[136:139], a[116:119], v[108:111], v[136:139]\n"
      "ds_write_b64 v244, v[208:209] offset:12288\n"
      "v_mfma_f32_16x16x32_bf16 v[136:139], a[120:123], v[112:115], v[136:139]\n"
      "v_mfma_f32_16x16x32_bf16 v[136:139], a[124:127], v[116:119], v[136:139]\n"
      "v_mfma_f32_16x16x32_bf16 v[132:135], a[128:131], v[36:39], v[8:11]\n"
      "v_mfma_f32_16x16x32_bf16 v[132:135], a[132:135], v[40:43], v[132:135]\n"
      "v_mfma_f32_16x16x32_bf16 v[132:135], a[136:139], v[44:47], v[132:135]\n"
      "v_mfma_f32_16x16x32_bf16 v[132:135], a[140:143], v[60:63], v[132:135]\n"
      "v_cvt_pk_bf16_f32 v210, v136, v137\n"
      "v_mfma_f32_16x16x32_bf16 v[132:135], a[144:147], v[64:67], v[132:135]\n"
      "v_cvt_pk_bf16_f32 v211, v138, v139\n"
      "v_mfma_f32_16x16x32_bf16 v[132:135], a[148:151], v[68:71], v[132:135]\n"
      "global_store_dwordx4 v252, v[148:151], s[54:55] offset:768 sc1\n"
      "v_mfma_f32_16x16x32_bf16 v[132:135], a[152:155], v[84:87], v[132:135]\n"
      "v_mfma_f32_16x16x32_bf16 v[132:135], a[156:159], v[88:91], v[132:135]\n"
      "v_mfma_f32_16x16x32_bf16 v[132:135], a[160:163], v[92:95], v[132:135]\n"
      "v_mfma_f32_16x16x32_bf16 v[132:135], a[164:167], v[108:111], v[132:135]\n"
      "ds_write_b64 v244, v[210:211] offset:12296\n"
      "v_mfma_f32_16x16x32_bf16 v[132:135], a[168:171], v[112:115], v[132:135]\n"
      "v_mfma_f32_16x16x32_bf16 v[132:135], a[172:175], v[116:119], v[132:135]\n"
      "v_mfma_f32_16x16x32_bf16 v[136:139], a[176:179], v[36:39], v[12:15]\n"
      "v_mfma_f32_16x16x32_bf16 v[136:139], a[180:183], v[40:43], v[136:139]\n"
      "v_mfma_f32_16x16x32_bf16 v[136:139], a[184:187], v[44:47], v[136:139]\n"
      "v_mfma_f32_16x16x32_bf16 v[136:139], a[188:191], v[60:63], v[136:139]\n"
      "v_cvt_pk_bf16_f32 v212, v132, v133\n"
      "v_mfma_f32_16x16x32_bf16 v[136:139], a[192:195], v[64:67], v[136:139]\n"
      "v_cvt_pk_bf16_f32 v213, v134, v135\n"
      "v_mfma_f32_16x16x32_bf16 v[136:139], a[196:199], v[68:71], v[136:139]\n"
      "v_mfma_f32_16x16x32_bf16 v[136:139], a[200:203], v[84:87], v[136:139]\n"
      "global_store_dwordx4 v253, v[152:155], s[54:55] offset:768 sc1\n"
      "v_mfma_f32_16x16x32_bf16 v[136:139], a[204:207], v[88:91], v[136:139]\n"
      "v_mfma_f32_16x16x32_bf16 v[136:139], a[208:211], v[92:95], v[136:139]\n"
      "v_mfma_f32_16x16x32_bf16 v[136:139], a[212:215], v[108:111], v[136:139]\n"
      "ds_write_b64 v245, v[212:213] offset:12288\n"
      "v_mfma_f32_16x16x32_bf16 v[136:139], a[216:219], v[112:115], v[136:139]\n"
      "v_mfma_f32_16x16x32_bf16 v[136:139], a[220:223], v[116:119], v[136:139]\n"
      "v_mfma_f32_16x16x32_bf16 v[132:135], a[224:227], v[36:39], v[16:19]\n"
      "v_mfma_f32_16x16x32_bf16 v[132:135], a[228:231], v[40:43], v[132:135]\n"
      "v_mfma_f32_16x16x32_bf16 v[132:135], a[232:235], v[44:47], v[132:135]\n"
      "v_mfma_f32_16x16x32_bf16 v[132:135], a[236:239], v[60:63], v[132:135]\n"
      "v_cvt_pk_bf16_f32 v214, v136, v137\n"
      "s_add_u32 s54, s44, s39\n"
      "s_addc_u32 s55, s45, 0\n"
      "v_mfma_f32_16x16x32_bf16 v[132:135], a[0:3], v[64:67], v[132:135]\n"
      "v_cvt_pk_bf16_f32 v215, v138, v139\n"
      "v_mfma_f32_16x16x32_bf16 v[132:135], a[4:7], v[68:71], v[132:135]\n"
      "v_mfma_f32_16x16x32_bf16 v[132:135], a[8:11], v[84:87], v[132:135]\n"
      "global_store_dwordx4 v247, v[24:27], s[54:55] offset:0 sc1\n"
      "v_mfma_f32_16x16x32_bf16 v[132:135], a[12:15], v[88:91], v[132:135]\n"
      "v_mfma_f32_16x16x32_bf16 v[132:135], a[16:19], v[92:95], v[132:135]\n"
      "v_mfma_f32_16x16x32_bf16 v[132:135], a[20:23], v[108:111], v[132:135]\n"
      "ds_write_b64 v245, v[214:215] offset:12296\n"
      "v_mfma_f32_16x16x32_bf16 v[132:135], v[140:143], v[112:115], v[132:135]\n"
      "v_mfma_f32_16x16x32_bf16 v[132:135], v[156:159], v[116:119], v[132:135]\n"
      "s_waitcnt vmcnt(10) lgkmcnt(4)\n"
      "s_barrier\n"
      "v_mfma_f32_16x16x32_bf16 v[136:139], v[160:163], v[36:39], v[20:23]\n"
      "ds_read_b128 v[36:39], v232 offset:24576\n"
      "v_mfma_f32_16x16x32_bf16 v[136:139], v[164:167], v[40:43], v[136:139]\n"
      "ds_read_b128 v[40:43], v233 offset:24576\n"
      "v_mfma_f32_16x16x32_bf16 v[136:139], v[180:183], v[44:47], v[136:139]\n"
      "ds_read_b128 v[44:47], v234 offset:24576\n"
      "v_mfma_f32_16x16x32_bf16 v[136:139], v[184:187], v[60:63], v[136:139]\n"
      "ds_read_b128 v[60:63], v235 offset:24576\n"
      "v_cvt_pk_bf16_f32 v208, v132, v133\n"
      "v_mfma_f32_16x16x32_bf16 v[136:139], v[188:191], v[64:67], v[136:139]\n"
      "ds_read_b128 v[64:67], v232 offset:32768\n"
      "v_cvt_pk_bf16_f32 v209, v134, v135\n"
      "v_mfma_f32_16x16x32_bf16 v[136:139], v[204:207], v[68:71], v[136:139]\n"
      "ds_read_b128 v[68:71], v233 offset:32768\n"
      "v_mfma_f32_16x16x32_bf16 v[136:139], a[24:27], v[84:87], v[136:139]\n"
      "ds_read_b128 v[84:87], v234 offset:32768\n"
      "v_mfma_f32_16x16x32_bf16 v[136:139], a[28:31], v[88:91], v[136:139]\n"
      "ds_read_b128 v[88:91], v235 offset:32768\n"
      "v_mfma_f32_16x16x32_bf16 v[136:139], a[32:35], v[92:95], v[136:139]\n"
      "ds_read_b128 v[92:95], v232 offset:40960\n"
      "v_mfma_f32_16x16x32_bf16 v[136:139], a[36:39], v[108:111], v[136:139]\n"
      "ds_read_b128 v[108:111], v233 offset:40960\n"
      "v_mfma_f32_16x16x32_bf16 v[136:139], a[40:43], v[112:115], v[136:139]\n"
      "ds_read_b128 v[112:115], v234 offset:40960\n"
      "v_mfma_f32_16x16x32_bf16 v[136:139], a[44:47], v[116:119], v[136:139]\n"
      "ds_read_b128 v[116:119], v235 offset:40960\n"
      "s_waitcnt lgkmcnt(8)\n"
      "v_mfma_f32_16x16x32_bf16 v[132:135], a[240:243], v[36:39], v[0:3]\n"
      "ds_write_b64 v244, v[208:209] offset:12416\n"
      "v_mfma_f32_16x16x32_bf16 v[132:135], a[244:247], v[40:43], v[132:135]\n"
      "v_mfma_f32_16x16x32_bf16 v[132:135], a[248:251], v[44:47], v[132:135]\n"
      "v_mfma_f32_16x16x32_bf16 v[132:135], a[252:255], v[60:63], v[132:135]\n"
      "v_cvt_pk_bf16_f32 v210, v136, v137\n"
      "global_store_dwordx4 v252, v[28:31], s[54:55] offset:0 sc1\n"
      "s_waitcnt lgkmcnt(5)\n"
      "v_mfma_f32_16x16x32_bf16 v[132:135], a[48:51], v[64:67], v[132:135]\n"
      "v_cvt_pk_bf16_f32 v211, v138, v139\n"
      "v_mfma_f32_16x16x32_bf16 v[132:135], a[52:55], v[68:71], v[132:135]\n"
      "v_mfma_f32_16x16x32_bf16 v[132:135], a[56:59], v[84:87], v[132:135]\n"
      "ds_read_b128 v[72:75], v246 offset:0\n"
      "v_mfma_f32_16x16x32_bf16 v[132:135], a[60:63], v[88:91], v[132:135]\n"
      "ds_read_b128 v[76:79], v246 offset:1024\n"
      "s_waitcnt lgkmcnt(3)\n"
      "v_mfma_f32_16x16x32_bf16 v[132:135], a[64:67], v[92:95], v[132:135]\n"
      "ds_read_b128 v[80:83], v246 offset:2048\n"
      "v_mfma_f32_16x16x32_bf16 v[132:135], a[68:71], v[108:111], v[132:135]\n"
      "ds_write_b64 v244, v[210:211] offset:12424\n"
      "v_mfma_f32_16x16x32_bf16 v[132:135], a[72:75], v[112:115], v[132:135]\n"
      "v_mfma_f32_16x16x32_bf16 v[132:135], a[76:79], v[116:119], v[132:135]\n"
      "v_mfma_f32_16x16x32_bf16 v[136:139], a[80:83], v[36:39], v[4:7]\n"
      "v_mfma_f32_16x16x32_bf16 v[136:139], a[84:87], v[40:43], v[136:139]\n"
      "v_mfma_f32_16x16x32_bf16 v[136:139], a[88:91], v[44:47], v[136:139]\n"
      "v_mfma_f32_16x16x32_bf16 v[136:139], a[92:95], v[60:63], v[136:139]\n"
      "v_cvt_pk_bf16_f32 v208, v132, v133\n"
      "global_store_dwordx4 v253, v[32:35], s[54:55] offset:0 sc1\n"
      "v_mfma_f32_16x16x32_bf16 v[136:139], a[96:99], v[64:67], v[136:139]\n"
      "v_cvt_pk_bf16_f32 v209, v134, v135\n"
      "v_mfma_f32_16x16x32_bf16 v[136:139], a[100:103], v[68:71], v[136:139]\n"
      "v_mfma_f32_16x16x32_bf16 v[136:139], a[104:107], v[84:87], v[136:139]\n"
      "v_mfma_f32_16x16x32_bf16 v[136:139], a[108:111], v[88:91], v[136:139]\n"
      "v_mfma_f32_16x16x32_bf16 v[136:139], a[112:115], v[92:95], v[136:139]\n"
      "v_mfma_f32_16x16x32_bf16 v[136:139], a[116:119], v[108:111], v[136:139]\n"
      "ds_write_b64 v244, v[208:209] offset:24576\n"
      "v_mfma_f32_16x16x32_bf16 v[136:139], a[120:123], v[112:115], v[136:139]\n"
      "v_mfma_f32_16x16x32_bf16 v[136:139], a[124:127], v[116:119], v[136:139]\n"
      "v_mfma_f32_16x16x32_bf16 v[132:135], a[128:131], v[36:39], v[8:11]\n"
      "v_mfma_f32_16x16x32_bf16 v[132:135], a[132:135], v[40:43], v[132:135]\n"
      "v_mfma_f32_16x16x32_bf16 v[132:135], a[136:139], v[44:47], v[132:135]\n"
      "v_mfma_f32_16x16x32_bf16 v[132:135], a[140:143], v[60:63], v[132:135]\n"
      "v_cvt_pk_bf16_f32 v210, v136, v137\n"
      "v_mfma_f32_16x16x32_bf16 v[132:135], a[144:147], v[64:67], v[132:135]\n"
      "v_cvt_pk_bf16_f32 v211, v138, v139\n"
      "v_mfma_f32_16x16x32_bf16 v[132:135], a[148:151], v[68:71], v[132:135]\n"
      "global_store_dwordx4 v247, v[48:51], s[54:55] offset:768 sc1\n"
      "v_mfma_f32_16x16x32_bf16 v[132:135], a[152:155], v[84:87], v[132:135]\n"
      "v_mfma_f32_16x16x32_bf16 v[132:135], a[156:159], v[88:91], v[132:135]\n"
      "v_mfma_f32_16x16x32_bf16 v[132:135], a[160:163], v[92:95], v[132:135]\n"
      "v_mfma_f32_16x16x32_bf16 v[132:135], a[164:167], v[108:111], v[132:135]\n"
      "ds_write_b64 v244, v[210:211] offset:24584\n"
      "v_mfma_f32_16x16x32_bf16 v[132:135], a[168:171], v[112:115], v[132:135]\n"
      "v_mfma_f32_16x16x32_bf16 v[132:135], a[172:175], v[116:119], v[132:135]\n"
      "v_mfma_f32_16x16x32_bf16 v[136:139], a[176:179], v[36:39], v[12:15]\n"
      "v_mfma_f32_16x16x32_bf16 v[136:139], a[180:183], v[40:43], v[136:139]\n"
      "v_mfma_f32_16x16x32_bf16 v[136:139], a[184:187], v[44:47], v[136:139]\n"
      "v_mfma_f32_16x16x32_bf16 v[136:139], a[188:191], v[60:63], v[136:139]\n"
      "v_cvt_pk_bf16_f32 v212, v132, v133\n"
      "v_mfma_f32_16x16x32_bf16 v[136:139], a[192:195], v[64:67], v[136:139]\n"
      "v_cvt_pk_bf16_f32 v213, v134, v135\n"
      "v_mfma_f32_16x16x32_bf16 v[136:139], a[196:199], v[68:71], v[136:139]\n"
      "v_mfma_f32_16x16x32_bf16 v[136:139], a[200:203], v[84:87], v[136:139]\n"
      "global_store_dwordx4 v252, v[52:55], s[54:55] offset:768 sc1\n"
      "v_mfma_f32_16x16x32_bf16 v[136:139], a[204:207], v[88:91], v[136:139]\n"
      "v_mfma_f32_16x16x32_bf16 v[136:139], a[208:211], v[92:95], v[136:139]\n"
      "v_mfma_f32_16x16x32_bf16 v[136:139], a[212:215], v[108:111], v[136:139]\n"
      "ds_write_b64 v245, v[212:213] offset:24576\n"
      "v_mfma_f32_16x16x32_bf16 v[136:139], a[216:219], v[112:115], v[136:139]\n"
      "v_mfma_f32_16x16x32_bf16 v[136:139], a[220:223], v[116:119], v[136:139]\n"
      "v_mfma_f32_16x16x32_bf16 v[132:135], a[224:227], v[36:39], v[16:19]\n"
      "v_mfma_f32_16x16x32_bf16 v[132:135], a[228:231], v[40:43], v[132:135]\n"
      "v_mfma_f32_16x16x32_bf16 v[132:135], a[232:235], v[44:47], v[132:135]\n"
      "v_mfma_f32_16x16x32_bf16 v[132:135], a[236:239], v[60:63], v[132:135]\n"
      "v_cvt_pk_bf16_f32 v214, v136, v137\n"
      "v_mfma_f32_16x16x32_bf16 v[132:135], a[0:3], v[64:67], v[132:135]\n"
      "v_cvt_pk_bf16_f32 v215, v138, v139\n"
      "v_mfma_f32_16x16x32_bf16 v[132:135], a[4:7], v[68:71], v[132:135]\n"
      "v_mfma_f32_16x16x32_bf16 v[132:135], a[8:11], v[84:87], v[132:135]\n"
      "global_store_dwordx4 v253, v[56:59], s[54:55] offset:768 sc1\n"
      "v_mfma_f32_16x16x32_bf16 v[132:135], a[12:15], v[88:91], v[132:135]\n"
      "v_mfma_f32_16x16x32_bf16 v[132:135], a[16:19], v[92:95], v[132:135]\n"
      "v_mfma_f32_16x16x32_bf16 v[132:135], a[20:23], v[108:111], v[132:135]\n"
      "ds_write_b64 v245, v[214:215] offset:24584\n"
      "v_mfma_f32_16x16x32_bf16 v[132:135], v[140:143], v[112:115], v[132:135]\n"
      "v_mfma_f32_16x16x32_bf16 v[132:135], v[156:159], v[116:119], v[132:135]\n"
      "s_waitcnt lgkmcnt(4)\n"
      "s_barrier\n"
      "v_mfma_f32_16x16x32_bf16 v[136:139], v[160:163], v[36:39], v[20:23]\n"
      "ds_read_b128 v[36:39], v232 offset:24832\n"
      "s_lshl_b32 s79, s97, 1\n"
      "v_mfma_f32_16x16x32_bf16 v[136:139], v[164:167], v[40:43], v[136:139]\n"
      "ds_read_b128 v[40:43], v233 offset:24832\n"
      "v_mad_i32_i24 v252, v251, s79, v252\n"
      "v_mfma_f32_16x16x32_bf16 v[136:139], v[180:183], v[44:47], v[136:139]\n"
      "ds_read_b128 v[44:47], v234 offset:24832\n"
      "s_mul_i32 s79, s79, s100\n"
      "v_mfma_f32_16x16x32_bf16 v[136:139], v[184:187], v[60:63], v[136:139]\n"
      "ds_read_b128 v[60:63], v235 offset:24832\n"
      "v_cvt_pk_bf16_f32 v208, v132, v133\n"
      "v_add_u32 v247, s79, v247\n"
      "v_mfma_f32_16x16x32_bf16 v[136:139], v[188:191], v[64:67], v[136:139]\n"
      "ds_read_b128 v[64:67], v232 offset:33024\n"
      "v_cvt_pk_bf16_f32 v209, v134, v135\n"
      "v_mfma_f32_16x16x32_bf16 v[136:139], v[204:207], v[68:71], v[136:139]\n"
      "ds_read_b128 v[68:71], v233 offset:33024\n"
      "s_mul_i32 s79, s79, s101\n"
      "v_mfma_f32_16x16x32_bf16 v[136:139], a[24:27], v[84:87], v[136:139]\n"
      "ds_read_b128 v[84:87], v234 offset:33024\n"
      "v_add_u32 v253, s79, v253\n"
      "v_mfma_f32_16x16x32_bf16 v[136:139], a[28:31], v[88:91], v[136:139]\n"
      "ds_read_b128 v[88:91], v235 offset:33024\n"
      "s_add_u32 s54, s30, s40\n"
      "s_addc_u32 s55, s31, 0\n"
      "v_mfma_f32_16x16x32_bf16 v[136:139], a[32:35], v[92:95], v[136:139]\n"
      "ds_read_b128 v[92:95], v232 offset:41216\n"
      "v_mfma_f32_16x16x32_bf16 v[136:139], a[36:39], v[108:111], v[136:139]\n"
      "ds_read_b128 v[108:111], v233 offset:41216\n"
      "global_store_dwordx4 v247, v[72:75], s[54:55] offset:0 sc1\n"
      "v_mfma_f32_16x16x32_bf16 v[136:139], a[40:43], v[112:115], v[136:139]\n"
      "ds_read_b128 v[112:115], v234 offset:41216\n"
      "v_mfma_f32_16x16x32_bf16 v[136:139], a[44:47], v[116:119], v[136:139]\n"
      "ds_read_b128 v[116:119], v235 offset:41216\n"
      "s_waitcnt lgkmcnt(8)\n"
      "v_mfma_f32_16x16x32_bf16 v[132:135], a[240:243], v[36:39], v[0:3]\n"
      "ds_write_b64 v244, v[208:209] offset:24704\n"
      "v_mfma_f32_16x16x32_bf16 v[132:135], a[244:247], v[40:43], v[132:135]\n"
      "v_mfma_f32_16x16x32_bf16 v[132:135], a[248:251], v[44:47], v[132:135]\n"
      "v_mfma_f32_16x16x32_bf16 v[132:135], a[252:255], v[60:63], v[132:135]\n"
      "v_cvt_pk_bf16_f32 v210, v136, v137\n"
      "s_waitcnt lgkmcnt(5)\n"
      "v_mfma_f32_16x16x32_bf16 v[132:135], a[48:51], v[64:67], v[132:135]\n"
      "v_cvt_pk_bf16_f32 v211, v138, v139\n"
      "v_mfma_f32_16x16x32_bf16 v[132:135], a[52:55], v[68:71], v[132:135]\n"
      "v_mfma_f32_16x16x32_bf16 v[132:135], a[56:59], v[84:87], v[132:135]\n"
      "ds_read_b128 v[96:99], v246 offset:12288\n"
      "v_mfma_f32_16x16x32_bf16 v[132:135], a[60:63], v[88:91], v[132:135]\n"
      "ds_read_b128 v[100:103], v246 offset:13312\n"
      "s_waitcnt lgkmcnt(3)\n"
      "v_mfma_f32_16x16x32_bf16 v[132:135], a[64:67], v[92:95], v[132:135]\n"
      "ds_read_b128 v[104:107], v246 offset:14336\n"
      "v_mfma_f32_16x16x32_bf16 v[132:135], a[68:71], v[108:111], v[132:135]\n"
      "ds_write_b64 v244, v[210:211] offset:24712\n"
      "v_mfma_f32_16x16x32_bf16 v[132:135], a[72:75], v[112:115], v[132:135]\n"
      "v_mfma_f32_16x16x32_bf16 v[132:135], a[76:79], v[116:119], v[132:135]\n"
      "v_mfma_f32_16x16x32_bf16 v[136:139], a[80:83], v[36:39], v[4:7]\n"
      "v_mfma_f32_16x16x32_bf16 v[136:139], a[84:87], v[40:43], v[136:139]\n"
      "global_store_dwordx4 v252, v[76:79], s[54:55] offset:0 sc1\n"
      "v_mfma_f32_16x16x32_bf16 v[136:139], a[88:91], v[44:47], v[136:139]\n"
      "v_mfma_f32_16x16x32_bf16 v[136:139], a[92:95], v[60:63], v[136:139]\n"
      "v_cvt_pk_bf16_f32 v208, v132, v133\n"
      "v_mfma_f32_16x16x32_bf16 v[136:139], a[96:99], v[64:67], v[136:139]\n"
      "v_cvt_pk_bf16_f32 v209, v134, v135\n"
      "v_mfma_f32_16x16x32_bf16 v[136:139], a[100:103], v[68:71], v[136:139]\n"
      "v_mfma_f32_16x16x32_bf16 v[136:139], a[104:107], v[84:87], v[136:139]\n"
      "LPD16:\n"
      "s_cbranch_execz LRET\n"
      "v_mfma_f32_16x16x32_bf16 v[136:139], a[108:111], v[88:91], v[136:139]\n"
      "v_mfma_f32_16x16x32_bf16 v[136:139], a[112:115], v[92:95], v[136:139]\n"
      "v_mfma_f32_16x16x32_bf16 v[136:139], a[116:119], v[108:111], v[136:139]\n"
      "ds_write_b64 v244, v[208:209] offset:0\n"
      "v_mfma_f32_16x16x32_bf16 v[136:139], a[120:123], v[112:115], v[136:139]\n"
      "v_mfma_f32_16x16x32_bf16 v[136:139], a[124:127], v[116:119], v[136:139]\n"
      "v_mfma_f32_16x16x32_bf16 v[132:135], a[128:131], v[36:39], v[8:11]\n"
      "global_store_dwordx4 v253, v[80:83], s[54:55] offset:0 sc1\n"
      "v_mfma_f32_16x16x32_bf16 v[132:135], a[132:135], v[40:43], v[132:135]\n"
      "v_mfma_f32_16x16x32_bf16 v[132:135], a[136:139], v[44:47], v[132:135]\n"
      "v_mfma_f32_16x16x32_bf16 v[132:135], a[140:143], v[60:63], v[132:135]\n"
      "v_cvt_pk_bf16_f32 v210, v136, v137\n"
      "v_mfma_f32_16x16x32_bf16 v[132:135], a[144:147], v[64:67], v[132:135]\n"
      "v_cvt_pk_bf16_f32 v211, v138, v139\n"
      "v_mfma_f32_16x16x32_bf16 v[132:135], a[148:151], v[68:71], v[132:135]\n"
      "v_mfma_f32_16x16x32_bf16 v[132:135], a[152:155], v[84:87], v[132:135]\n"
      "v_mfma_f32_16x16x32_bf16 v[132:135], a[156:159], v[88:91], v[132:135]\n"
      "v_mfma_f32_16x16x32_bf16 v[132:135], a[160:163], v[92:95], v[132:135]\n"
      "v_mfma_f32_16x16x32_bf16 v[132:135], a[164:167], v[108:111], v[132:135]\n"
      "ds_write_b64 v244, v[210:211] offset:8\n"
      "v_mfma_f32_16x16x32_bf16 v[132:135], a[168:171], v[112:115], v[132:135]\n"
      "s_waitcnt lgkmcnt(5)\n"
      "v_mfma_f32_16x16x32_bf16 v[132:135], a[172:175], v[116:119], v[132:135]\n"
      "global_store_dwordx4 v247, v[96:99], s[54:55] offset:768 sc1\n"
      "v_mfma_f32_16x16x32_bf16 v[136:139], a[176:179], v[36:39], v[12:15]\n"
      "v_mfma_f32_16x16x32_bf16 v[136:139], a[180:183], v[40:43], v[136:139]\n"
      "v_mfma_f32_16x16x32_bf16 v[136:139], a[184:187], v[44:47], v[136:139]\n"
      "v_mfma_f32_16x16x32_bf16 v[136:139], a[188:191], v[60:63], v[136:139]\n"
      "v_cvt_pk_bf16_f32 v212, v132, v133\n"
      "v_mfma_f32_16x16x32_bf16 v[136:139], a[192:195], v[64:67], v[136:139]\n"
      "v_cvt_pk_bf16_f32 v213, v134, v135\n"
      "v_mfma_f32_16x16x32_bf16 v[136:139], a[196:199], v[68:71], v[136:139]\n"
      "v_mfma_f32_16x16x32_bf16 v[136:139], a[200:203], v[84:87], v[136:139]\n"
      "v_mfma_f32_16x16x32_bf16 v[136:139], a[204:207], v[88:91], v[136:139]\n"
      "v_mfma_f32_16x16x32_bf16 v[136:139], a[208:211], v[92:95], v[136:139]\n"
      "s_waitcnt lgkmcnt(4)\n"
      "v_mfma_f32_16x16x32_bf16 v[136:139], a[212:215], v[108:111], v[136:139]\n"
      "ds_write_b64 v245, v[212:213] offset:0\n"
      "v_mfma_f32_16x16x32_bf16 v[136:139], a[216:219], v[112:115], v[136:139]\n"
      "global_store_dwordx4 v252, v[100:103], s[54:55] offset:768 sc1\n"
      "v_mfma_f32_16x16x32_bf16 v[136:139], a[220:223], v[116:119], v[136:139]\n"
      "v_mfma_f32_16x16x32_bf16 v[132:135], a[224:227], v[36:39], v[16:19]\n"
      "v_mfma_f32_16x16x32_bf16 v[132:135], a[228:231], v[40:43], v[132:135]\n"
      "v_mfma_f32_16x16x32_bf16 v[132:135], a[232:235], v[44:47], v[132:135]\n"
      "v_mfma_f32_16x16x32_bf16 v[132:135], a[236:239], v[60:63], v[132:135]\n"
      "v_cvt_pk_bf16_f32 v214, v136, v137\n"
      "v_mfma_f32_16x16x32_bf16 v[132:135], a[0:3], v[64:67], v[132:135]\n"
      "v_cvt_pk_bf16_f32 v215, v138, v139\n"
      "v_mfma_f32_16x16x32_bf16 v[132:135], a[4:7], v[68:71], v[132:135]\n"
      "v_mfma_f32_16x16x32_bf16 v[132:135], a[8:11], v[84:87], v[132:135]\n"
      "v_mfma_f32_16x16x32_bf16 v[132:135], a[12:15], v[88:91], v[132:135]\n"
      "s_waitcnt lgkmcnt(4)\n"
      "v_mfma_f32_16x16x32_bf16 v[132:135], a[16:19], v[92:95], v[132:135]\n"
      "global_store_dwordx4 v253, v[104:107], s[54:55] offset:768 sc1\n"
      "v_mfma_f32_16x16x32_bf16 v[132:135], a[20:23], v[108:111], v[132:135]\n"
      "ds_write_b64 v245, v[214:215] offset:8\n"
      "v_mfma_f32_16x16x32_bf16 v[132:135], v[140:143], v[112:115], v[132:135]\n"
      "v_mfma_f32_16x16x32_bf16 v[132:135], v[156:159], v[116:119], v[132:135]\n"
      "s_waitcnt lgkmcnt(4)\n"
      "s_barrier\n"
      "v_mfma_f32_16x16x32_bf16 v[136:139], v[160:163], v[36:39], v[20:23]\n"
      "v_mfma_f32_16x16x32_bf16 v[136:139], v[164:167], v[40:43], v[136:139]\n"
      "v_mfma_f32_16x16x32_bf16 v[136:139], v[180:183], v[44:47], v[136:139]\n"
      "v_mfma_f32_16x16x32_bf16 v[136:139], v[184:187], v[60:63], v[136:139]\n"
      "v_cvt_pk_bf16_f32 v208, v132, v133\n"
      "v_mfma_f32_16x16x32_bf16 v[136:139], v[188:191], v[64:67], v[136:139]\n"
      "v_cvt_pk_bf16_f32 v209, v134, v135\n"
      "v_mfma_f32_16x16x32_bf16 v[136:139], v[204:207], v[68:71], v[136:139]\n"
      "v_mfma_f32_16x16x32_bf16 v[136:139], a[24:27], v[84:87], v[136:139]\n"
      "v_mfma_f32_16x16x32_bf16 v[136:139], a[28:31], v[88:91], v[136:139]\n"
      "v_mfma_f32_16x16x32_bf16 v[136:139], a[32:35], v[92:95], v[136:139]\n"
      "v_mfma_f32_16x16x32_bf16 v[136:139], a[36:39], v[108:111], v[136:139]\n"
      "ds_write_b64 v244, v[208:209] offset:128\n"
      "v_mfma_f32_16x16x32_bf16 v[136:139], a[40:43], v[112:115], v[136:139]\n"
      "v_mfma_f32_16x16x32_bf16 v[136:139], a[44:47], v[116:119], v[136:139]\n"
      "ds_read_b128 v[120:123], v246 offset:24576\n"
      "ds_read_b128 v[124:127], v246 offset:25600\n"
      "ds_read_b128 v[128:131], v246 offset:26624\n"
      "s_add_u32 s54, s44, s40\n"
      "s_addc_u32 s55, s45, 0\n"
      "s_nop 3\n"
      "v_cvt_pk_bf16_f32 v210, v136, v137\n"
      "v_cvt_pk_bf16_f32 v211, v138, v139\n"
      "ds_write_b128 v244, v[208:211] offset:128\n"
      "s_waitcnt lgkmcnt(3)\n"
      "global_store_dwordx4 v247, v[120:123], s[54:55] offset:0 sc1\n"
      "s_waitcnt lgkmcnt(2)\n"
      "global_store_dwordx4 v252, v[124:127], s[54:55] offset:0 sc1\n"
      "s_waitcnt lgkmcnt(1)\n"
      "global_store_dwordx4 v253, v[128:131], s[54:55] offset:0 sc1\n"
      "s_waitcnt lgkmcnt(0)\n"
      "s_barrier\n"
      "ds_read_b128 v[144:147], v246 offset:0\n"
      "ds_read_b128 v[148:151], v246 offset:1024\n"
      "ds_read_b128 v[152:155], v246 offset:2048\n"
      "s_waitcnt lgkmcnt(2)\n"
      "global_store_dwordx4 v247, v[144:147], s[54:55] offset:768 sc1\n"
      "s_waitcnt lgkmcnt(1)\n"
      "global_store_dwordx4 v252, v[148:151], s[54:55] offset:768 sc1\n"
      "s_waitcnt lgkmcnt(0)\n"
      "global_store_dwordx4 v253, v[152:155], s[54:55] offset:768 sc1\n"
      "s_cmp_lg_u32 s87, 0\n"
      "s_cbranch_scc1 LE1\n"
      "v_min_u32 v0, 0x370, v239\n"
      "v_add_u32 v0, 0x2480, v0\n"
      "s_mov_b32 s82, 0x40000\n"
      "LE0:\n"
      "global_load_dwordx4 v[4:7], v0, s[28:29] sc1\n"
      "s_sub_u32 s82, s82, 1\n"
      "s_cbranch_scc1 LE1\n"
      "s_waitcnt vmcnt(0)\n"
      "v_cmp_ne_u64 vcc, s[80:81], v[4:5]\n"
      "v_cmp_ne_u64 s[56:57], s[80:81], v[6:7]\n"
      "s_nop 3\n"
      "s_or_b64 vcc, vcc, s[56:57]\n"
      "s_cbranch_scc1 LE0\n"
      "v_mov_b32 v0, 0\n"
      "v_mov_b32 v2, s80\n"
      "v_mov_b32 v3, s81\n"
      "s_mov_b64 exec, 1\n"
      "global_store_dwordx2 v0, v[2:3], s[28:29] sc1\n"
      "s_mov_b64 exec, -1\n"
      "LE1:\n"
      "s_waitcnt vmcnt(0)\n"
      "s_branch LCEND\n"
      "LPS:\n"
      "s_mov_b64 exec, 1\n"
      "global_store_dword v229, v230, s[28:29] sc1\n"
      "s_mov_b64 exec, -1\n"
      "s_branch LPR\n"
      "LT0:\n"
      "s_nop 15\n"
      "s_nop 3\n"
      "s_branch LS0\n"
      "LT1:\n"
      "s_nop 15\n"
      "s_nop 3\n"
      "s_branch LS1\n"
      "LT2:\n"
      "s_nop 15\n"
      "s_nop 3\n"
      "s_branch LS2\n"
      "LT3:\n"
      "s_nop 15\n"
      "s_nop 3\n"
      "s_branch LS3\n"
      "LT4:\n"
      "s_nop 15\n"
      "s_nop 3\n"
      "s_branch LS4\n"
      "LT5:\n"
      "s_nop 15\n"
      "s_nop 3\n"
      "s_branch LS5\n"
      "LT6:\n"
      "s_nop 15\n"
      "s_nop 3\n"
      "s_branch LS6\n"
      "LT7:\n"
      "s_nop 15\n"
      "s_nop 3\n"
      "s_branch LS7\n"
      "LT8:\n"
      "s_nop 15\n"
      "s_nop 3\n"
      "s_branch LS8\n"
      "LT9:\n"
      "s_nop 15\n"
      "s_nop 3\n"
      "s_branch LS9\n"
      "LT10:\n"
      "s_nop 15\n"
      "s_nop 3\n"
      "s_branch LS10\n"
      "LT11:\n"
      "s_nop 15\n"
      "s_nop 3\n"
      "s_branch LS11\n"
      "LT12:\n"
      "s_nop 15\n"
      "s_nop 3\n"
      "s_branch LS12\n"
      "LT13:\n"
      "s_nop 15\n"
      "s_nop 3\n"
      "s_branch LS13\n"
      "LT14:\n"
      "s_nop 15\n"
      "s_nop 3\n"
      "s_branch LS14\n"
      "LT15:\n"
      "s_nop 15\n"
      "s_nop 3\n"
      "s_branch LS15\n"
      "LT16:\n"
      "s_nop 15\n"
      "s_nop 3\n"
      "s_branch LS16\n"
      "LT17:\n"
      "s_nop 15\n"
      "s_nop 3\n"
      "s_branch LS17\n"
      "LT18:\n"
      "s_nop 15\n"
      "s_nop 3\n"
      "s_branch LS18\n"
      "LT19:\n"
      "s_nop 15\n"
      "s_nop 3\n"
      "s_branch LS19\n"
      "LT20:\n"
      "s_nop 15\n"
      "s_nop 3\n"
      "s_branch LS20\n"
      "LT21:\n"
      "s_nop 15\n"
      "s_nop 3\n"
      "s_branch LS21\n"
      "LT22:\n"
      "s_nop 15\n"
      "s_nop 3\n"
      "s_branch LS22\n"
      "LT23:\n"
      "s_nop 15\n"
      "s_nop 3\n"
      "s_branch LS23\n"
      "LT24:\n"
      "s_nop 15\n"
      "s_nop 3\n"
      "s_branch LS24\n"
      "LT25:\n"
      "s_nop 15\n"
      "s_nop 3\n"
      "s_branch LS25\n"
      "LT26:\n"
      "s_nop 15\n"
      "s_nop 3\n"
      "s_branch LS26\n"
      "LT27:\n"
      "s_nop 15\n"
      "s_nop 3\n"
      "s_branch LS27\n"
      "LT28:\n"
      "s_nop 15\n"
      "s_nop 3\n"
      "s_branch LS28\n"
      "LT29:\n"
      "s_nop 15\n"
      "s_nop 3\n"
      "s_branch LS29\n"
      "LT30:\n"
      "s_nop 15\n"
      "s_nop 3\n"
      "s_branch LS30\n"
      "LT31:\n"
      "s_nop 15\n"
      "s_nop 3\n"
      "s_branch LS31\n"
      "LCEND:\n"

      :
      : "{v232}"(cB0), "{v233}"(cB0 ^ 64), "{v234}"(cB0 ^ 128), "{v235}"(cB0 ^ 192), "{v238}"(pvo),
        "{v239}"(lo16), "{v240}"(wro[0]), "{v241}"(wro[1]), "{v242}"(wro[2]), "{v243}"(tra), "{v248}"(fla), "{v249}"(c256),
        "{v250}"(pk), "{v105}"(two), "{v106}"(tcode), "{v[252:253]}"(ep),
        "{s[20:21]}"(cf), "{s[22:23]}"(pc), "{s[24:25]}"(wb), "{s[30:31]}"(out), "{s[28:29]}"(ws), "{s18}"(mP), "{s17}"(mX),
        "{s32}"(xo[0]), "{s33}"(xo[1]), "{s34}"(xo[2]), "{s35}"(xo[3]), "{s36}"(xo[4]), "{s37}"(xo[5]), "{s38}"(xo[6]),
        "{s39}"(xo[7]), "{s40}"(xo[8]), "{s[42:43]}"(cf + 294912), "{s[44:45]}"(out + 294912), "{s[46:47]}"(tab), "{s50}"(var), "{s51}"(b),
        "{s[64:65]}"(w2w), "{s74}"(mW), "{s76}"(wid), "{s77}"(jfl), "{v251}"(sgn),
        "{s89}"(dj[0]), "{s90}"(dj[1]), "{s91}"(dj[2]), "{s92}"(dj[3]), "{s93}"(dj[4]), "{s94}"(dj[5]), "{s95}"(dj[6]),
        "{s96}"(dj[7]), "{s97}"(dj[8]), "{s98}"(d0), "{s99}"(wid ? -1 : 1), "{s100}"(wid < 3 ? 1 : -1), "{s101}"(wid == 2 ? -1 : 1),
        "{v102}"(jt), "{v103}"(jrw), "{v104}"(jrr), "{v[110:111]}"(cd), "{v[112:113]}"(cr), "{v107}"(sg1), "{v108}"(scr), "{s[72:73]}"(jo),
        "{v[4:7]}"(ja0), "{v[8:11]}"(ja1), "{v[12:15]}"(ja2), "{v[16:19]}"(ja3), "{v[20:23]}"(ja4), "{v[24:27]}"(ja5),
        "{s83}"(t_start + (0) + var * ((0) - (0))), "{s86}"(158 + var * ((158) - (158)))
      : "memory", "scc", "vcc", "v0", "v1", "v2", "v3", "v28", "v29", "v30", "v31", "v32", "v33", "v34", "v35", "v36", "v37", "v38", "v39", "v40", "v41", "v42", "v43", "v44", "v45", "v46", "v47", "v48", "v49", "v50", "v51", "v52", "v53", "v54", "v55", "v56", "v57", "v58", "v59", "v60", "v61", "v62", "v63", "v64", "v65", "v66", "v67", "v68", "v69", "v70", "v71", "v72", "v73", "v74", "v75", "v76", "v77", "v78", "v79", "v80", "v81", "v82", "v83", "v84", "v85", "v86", "v87", "v88", "v89", "v90", "v91", "v92", "v93", "v94", "v95", "v96", "v97", "v98", "v99", "v100", "v101", "v109", "v114", "v115", "v116", "v117", "v118", "v119", "v120", "v121", "v122", "v123", "v124", "v125", "v126", "v127", "v128", "v129", "v130", "v131", "v132", "v133", "v134", "v135", "v136", "v137", "v138", "v139", "v140", "v141", "v142", "v143", "v144", "v145", "v146", "v147", "v148", "v149", "v150", "v151", "v152", "v153", "v154", "v155", "v156", "v157", "v158", "v159", "v160", "v161", "v162", "v163", "v164", "v165", "v166", "v167", "v168", "v169", "v170", "v171", "v172", "v173", "v174", "v175", "v176", "v177", "v178", "v179", "v180", "v181", "v182", "v183", "v184", "v185", "v186", "v187", "v188", "v189", "v190", "v191", "v192", "v193", "v194", "v195", "v196", "v197", "v198", "v199", "v200", "v201", "v202", "v203", "v204", "v205", "v206", "v207", "v208", "v209", "v210", "v211", "v212", "v213", "v214", "v215", "v216", "v217", "v218", "v219", "v220", "v221", "v222", "v223", "v224", "v225", "v226", "v227", "v228", "v229", "v230", "v231", "v236", "v237", "v244", "v245", "v246", "v247", "v254", "v255", "a0", "a1", "a2", "a3", "a4", "a5", "a6", "a7", "a8", "a9", "a10", "a11", "a12", "a13", "a14", "a15", "a16", "a17", "a18", "a19", "a20", "a21", "a22", "a23", "a24", "a25", "a26", "a27", "a28", "a29", "a30", "a31", "a32", "a33", "a34", "a35", "a36", "a37", "a38", "a39", "a40", "a41", "a42", "a43", "a44", "a45", "a46", "a47", "a48", "a49", "a50", "a51", "a52", "a53", "a54", "a55", "a56", "a57", "a58", "a59", "a60", "a61", "a62", "a63", "a64", "a65", "a66", "a67", "a68", "a69", "a70", "a71", "a72", "a73", "a74", "a75", "a76", "a77", "a78", "a79", "a80", "a81", "a82", "a83", "a84", "a85", "a86", "a87", "a88", "a89", "a90", "a91", "a92", "a93", "a94", "a95", "a96", "a97", "a98", "a99", "a100", "a101", "a102", "a103", "a104", "a105", "a106", "a107", "a108", "a109", "a110", "a111", "a112", "a113", "a114", "a115", "a116", "a117", "a118", "a119", "a120", "a121", "a122", "a123", "a124", "a125", "a126", "a127", "a128", "a129", "a130", "a131", "a132", "a133", "a134", "a135", "a136", "a137", "a138", "a139", "a140", "a141", "a142", "a143", "a144", "a145", "a146", "a147", "a148", "a149", "a150", "a151", "a152", "a153", "a154", "a155", "a156", "a157", "a158", "a159", "a160", "a161", "a162", "a163", "a164", "a165", "a166", "a167", "a168", "a169", "a170", "a171", "a172", "a173", "a174", "a175", "a176", "a177", "a178", "a179", "a180", "a181", "a182", "a183", "a184", "a185", "a186", "a187", "a188", "a189", "a190", "a191", "a192", "a193", "a194", "a195", "a196", "a197", "a198", "a199", "a200", "a201", "a202", "a203", "a204", "a205", "a206", "a207", "a208", "a209", "a210", "a211", "a212", "a213", "a214", "a215", "a216", "a217", "a218", "a219", "a220", "a221", "a222", "a223", "a224", "a225", "a226", "a227", "a228", "a229", "a230", "a231", "a232", "a233", "a234", "a235", "a236", "a237", "a238", "a239", "a240", "a241", "a242", "a243", "a244", "a245", "a246", "a247", "a248", "a249", "a250", "a251", "a252", "a253", "a254", "a255", "s48", "s49", "s52", "s53", "s54", "s55", "s56", "s57", "s58", "s59", "s60", "s61", "s62", "s63", "s78", "s79", "s80", "s81", "s82", "s84", "s85", "s87", "s19");
}

extern "C" void kernel_launch(void* const* d_in, const int* in_sizes, int n_in,
                              void* d_out, int out_size, void* d_ws, size_t ws_size,
                              hipStream_t stream) {
  (void)hipFuncSetAttribute((const void*)fpn_kernel, hipFuncAttributeMaxDynamicSharedMemorySize, 159808);
  fpn_kernel<<<256, 256, 159808, stream>>>((const char*)d_in[0], (const char*)d_in[1], (const char*)d_in[2],
                                                (const char*)d_in[3], (char*)d_ws, (char*)d_out);
}
```

```cpp
#include <hip/hip_runtime.h>
#include <hip/hip_bf16.h>

typedef __attribute__((ext_vector_type(4))) unsigned u32x4;
typedef __attribute__((ext_vector_type(2))) unsigned u32x2;
#define NSTAMPS 0

__device__ __forceinline__ unsigned col0(unsigned i) { return i + (i >= 5 ? 6 : 0) + (i >= 10 ? 5 : 0); }

static __device__ const unsigned __attribute__((aligned(128))) lane_tab[2048] = {
  98304, 98368, 98304, 0, 99088, 99152, 98320, 16, 99872, 99936, 98336, 32, 100656, 100720, 98352, 48,
  101440, 101376, 98368, 64, 102224, 102160, 98384, 80, 103008, 102944, 98400, 96, 103792, 103728, 98416, 112,
  104448, 104512, 98432, 128, 105232, 105296, 98448, 144, 106016, 106080, 98464, 160, 106800, 106864, 98480, 176,
  107584, 107520, 98496, 192, 108368, 108304, 98512, 208, 109152, 109088, 98528, 224, 109936, 109872, 98544, 240,
  98320, 98384, 98560, 256, 99072, 99136, 98576, 272, 99888, 99952, 98592, 288, 100640, 100704, 98608, 304,
  101456, 101392, 98624, 320, 102208, 102144, 98640, 336, 103024, 102960, 98656, 352, 103776, 103712, 98672, 368,
  104464, 104528, 98688, 384, 105216, 105280, 98704, 400, 106032, 106096, 98720, 416, 106784, 106848, 98736, 432,
  107600, 107536, 98752, 448, 108352, 108288, 98768, 464, 109168, 109104, 98784, 480, 109920, 109856, 98800, 496,
  98336, 98400, 98816, 512, 99120, 99184, 98832, 528, 99840, 99904, 98848, 544, 100624, 100688, 98864, 560,
  101472, 101408, 98880, 576, 102256, 102192, 98896, 592, 102976, 102912, 98912, 608, 103760, 103696, 98928, 624,
  104480, 104544, 98944, 640, 105264, 105328, 98960, 656, 105984, 106048, 98976, 672, 106768, 106832, 98992, 688,
  107616, 107552, 99008, 704, 108400, 108336, 99024, 720, 109120, 109056, 99040, 736, 109904, 109840, 99056, 752,
  98352, 98416, 99072, 1552, 99104, 99168, 99088, 1536, 99856, 99920, 99104, 1584, 100608, 100672, 99120, 1568,
  101488, 101424, 99136, 1616, 102240, 102176, 99152, 1600, 102992, 102928, 99168, 1648, 103744, 103680, 99184, 1632,
  104496, 104560, 99200, 1680, 105248, 105312, 99216, 1664, 106000, 106064, 99232, 1712, 106752, 106816, 99248, 1696,
  107632, 107568, 99264, 1744, 108384, 108320, 99280, 1728, 109136, 109072, 99296, 1776, 109888, 109824, 99312, 1760,
  98496, 98560, 101376, 6208, 99280, 99344, 101392, 6224, 100064, 100128, 101408, 6240, 100848, 100912, 101424, 6256,
  101504, 101696, 101440, 6144, 102288, 102480, 101456, 6160, 103072, 103264, 101472, 6176, 103856, 104048, 101488, 6192,
  104640, 104704, 101504, 6336, 105424, 105488, 101520, 6352, 106208, 106272, 101536, 6368, 106992, 107056, 101552, 6384,
  107648, 107840, 101568, 6272, 108432, 108624, 101584, 6288, 109216, 109408, 101600, 6304, 110000, 110192, 101616, 6320,
  98512, 98576, 101632, 6464, 99264, 99328, 101648, 6480, 100080, 100144, 101664, 6496, 100832, 100896, 101680, 6512,
  101520, 101712, 101696, 6400, 102272, 102464, 101712, 6416, 103088, 103280, 101728, 6432, 103840, 104032, 101744, 6448,
  104656, 104720, 101760, 6592, 105408, 105472, 101776, 6608, 106224, 106288, 101792, 6624, 106976, 107040, 101808, 6640,
  107664, 107856, 101824, 6528, 108416, 108608, 101840, 6544, 109232, 109424, 101856, 6560, 109984, 110176, 101872, 6576,
  98528, 98592, 101888, 6720, 99312, 99376, 101904, 6736, 100032, 100096, 101920, 6752, 100816, 100880, 101936, 6768,
  101536, 101728, 101952, 6656, 102320, 102512, 101968, 6672, 103040, 103232, 101984, 6688, 103824, 104016, 102000, 6704,
  104672, 104736, 102016, 6848, 105456, 105520, 102032, 6864, 106176, 106240, 102048, 6880, 106960, 107024, 102064, 6896,
  107680, 107872, 102080, 6784, 108464, 108656, 102096, 6800, 109184, 109376, 102112, 6816, 109968, 110160, 102128, 6832,
  98544, 98608, 102144, 16976, 99296, 99360, 102160, 16960, 100048, 100112, 102176, 17008, 100800, 100864, 102192, 16992,
  101552, 101744, 102208, 16912, 102304, 102496, 102224, 16896, 103056, 103248, 102240, 16944, 103808, 104000, 102256, 16928,
  104688, 104752, 102272, 17104, 105440, 105504, 102288, 17088, 106192, 106256, 102304, 17136, 106944, 107008, 102320, 17120,
  107696, 107888, 102336, 17040, 108448, 108640, 102352, 17024, 109200, 109392, 102368, 17072, 109952, 110144, 102384, 17056,
  1808, 3616, 0, 21632, 1792, 3632, 16, 21648, 1840, 3584, 32, 21664, 1824, 3600, 48, 21680,
  1872, 3680, 64, 21696, 1856, 3696, 80, 21712, 1904, 3648, 96, 21728, 1888, 3664, 112, 21744,
  1936, 3744, 128, 21504, 1920, 3760, 144, 21520, 1968, 3712, 160, 21536, 1952, 3728, 176, 21552,
  2000, 3808, 192, 21568, 1984, 3824, 208, 21584, 2032, 3776, 224, 21600, 2016, 3792, 240, 21616,
  2064, 4656, 768, 22400, 2048, 4640, 784, 22416, 2096, 4624, 800, 22432, 2080, 4608, 816, 22448,
  2128, 4720, 832, 22464, 2112, 4704, 848, 22480, 2160, 4688, 864, 22496, 2144, 4672, 880, 22512,
  2192, 4784, 896, 22272, 2176, 4768, 912, 22288, 2224, 4752, 928, 22304, 2208, 4736, 944, 22320,
  2256, 4848, 960, 22336, 2240, 4832, 976, 22352, 2288, 4816, 992, 22368, 2272, 4800, 1008, 22384,
  3104, 4912, 1552, 23184, 3120, 4896, 1536, 23168, 3072, 4880, 1584, 23216, 3088, 4864, 1568, 23200,
  3168, 4976, 1616, 23248, 3184, 4960, 1600, 23232, 3136, 4944, 1648, 23280, 3152, 4928, 1632, 23264,
  3232, 5040, 1680, 23056, 3248, 5024, 1664, 23040, 3200, 5008, 1712, 23088, 3216, 4992, 1696, 23072,
  3296, 5104, 1744, 23120, 3312, 5088, 1728, 23104, 3264, 5072, 1776, 23152, 3280, 5056, 1760, 23136,
  3360, 5168, 2320, 23952, 3376, 5152, 2304, 23936, 3328, 5136, 2352, 23984, 3344, 5120, 2336, 23968,
  3424, 5232, 2384, 24016, 3440, 5216, 2368, 24000, 3392, 5200, 2416, 24048, 3408, 5184, 2400, 24032,
  3488, 5296, 2448, 23824, 3504, 5280, 2432, 23808, 3456, 5264, 2480, 23856, 3472, 5248, 2464, 23840,
  3552, 5360, 2512, 23888, 3568, 5344, 2496, 23872, 3520, 5328, 2544, 23920, 3536, 5312, 2528, 23904,
  17232, 19040, 3104, 32416, 17216, 19056, 3120, 32432, 17264, 19008, 3072, 32384, 17248, 19024, 3088, 32400,
  17168, 18976, 3168, 32480, 17152, 18992, 3184, 32496, 17200, 18944, 3136, 32448, 17184, 18960, 3152, 32464,
  17360, 19168, 3232, 32288, 17344, 19184, 3248, 32304, 17392, 19136, 3200, 32256, 17376, 19152, 3216, 32272,
  17296, 19104, 3296, 32352, 17280, 19120, 3312, 32368, 17328, 19072, 3264, 32320, 17312, 19088, 3280, 32336,
  17488, 20080, 3872, 33184, 17472, 20064, 3888, 33200, 17520, 20048, 3840, 33152, 17504, 20032, 3856, 33168,
  17424, 20016, 3936, 33248, 17408, 20000, 3952, 33264, 17456, 19984, 3904, 33216, 17440, 19968, 3920, 33232,
  17616, 20208, 4000, 33056, 17600, 20192, 4016, 33072, 17648, 20176, 3968, 33024, 17632, 20160, 3984, 33040,
  17552, 20144, 4064, 33120, 17536, 20128, 4080, 33136, 17584, 20112, 4032, 33088, 17568, 20096, 4048, 33104,
  18528, 20336, 4656, 33968, 18544, 20320, 4640, 33952, 18496, 20304, 4624, 33936, 18512, 20288, 4608, 33920,
  18464, 20272, 4720, 34032, 18480, 20256, 4704, 34016, 18432, 20240, 4688, 34000, 18448, 20224, 4672, 33984,
  18656, 20464, 4784, 33840, 18672, 20448, 4768, 33824, 18624, 20432, 4752, 33808, 18640, 20416, 4736, 33792,
  18592, 20400, 4848, 33904, 18608, 20384, 4832, 33888, 18560, 20368, 4816, 33872, 18576, 20352, 4800, 33856,
  18784, 20592, 5424, 34736, 18800, 20576, 5408, 34720, 18752, 20560, 5392, 34704, 18768, 20544, 5376, 34688,
  18720, 20528, 5488, 34800, 18736, 20512, 5472, 34784, 18688, 20496, 5456, 34768, 18704, 20480, 5440, 34752,
  18912, 20720, 5552, 34608, 18928, 20704, 5536, 34592, 18880, 20688, 5520, 34576, 18896, 20672, 5504, 34560,
  18848, 20656, 5616, 34672, 18864, 20640, 5600, 34656, 18816, 20624, 5584, 34640, 18832, 20608, 5568, 34624,
  98688, 98752, 104448, 21504, 99472, 99536, 104464, 21520, 100256, 100320, 104480, 21536, 101040, 101104, 104496, 21552,
  101824, 101760, 104512, 21568, 102608, 102544, 104528, 21584, 103392, 103328, 104544, 21600, 104176, 104112, 104560, 21616,
  104832, 104896, 104576, 21632, 105616, 105680, 104592, 21648, 106400, 106464, 104608, 21664, 107184, 107248, 104624, 21680,
  107968, 107904, 104640, 21696, 108752, 108688, 104656, 21712, 109536, 109472, 104672, 21728, 110320, 110256, 104688, 21744,
  98704, 98768, 104704, 21760, 99456, 99520, 104720, 21776, 100272, 100336, 104736, 21792, 101024, 101088, 104752, 21808,
  101840, 101776, 104768, 21824, 102592, 102528, 104784, 21840, 103408, 103344, 104800, 21856, 104160, 104096, 104816, 21872,
  104848, 104912, 104832, 21888, 105600, 105664, 104848, 21904, 106416, 106480, 104864, 21920, 107168, 107232, 104880, 21936,
  107984, 107920, 104896, 21952, 108736, 108672, 104912, 21968, 109552, 109488, 104928, 21984, 110304, 110240, 104944, 22000,
  98720, 98784, 104960, 22016, 99504, 99568, 104976, 22032, 100224, 100288, 104992, 22048, 101008, 101072, 105008, 22064,
  101856, 101792, 105024, 22080, 102640, 102576, 105040, 22096, 103360, 103296, 105056, 22112, 104144, 104080, 105072, 22128,
  104864, 104928, 105088, 22144, 105648, 105712, 105104, 22160, 106368, 106432, 105120, 22176, 107152, 107216, 105136, 22192,
  108000, 107936, 105152, 22208, 108784, 108720, 105168, 22224, 109504, 109440, 105184, 22240, 110288, 110224, 105200, 22256,
  98736, 98800, 105216, 23056, 99488, 99552, 105232, 23040, 100240, 100304, 105248, 23088, 100992, 101056, 105264, 23072,
  101872, 101808, 105280, 23120, 102624, 102560, 105296, 23104, 103376, 103312, 105312, 23152, 104128, 104064, 105328, 23136,
  104880, 104944, 105344, 23184, 105632, 105696, 105360, 23168, 106384, 106448, 105376, 23216, 107136, 107200, 105392, 23200,
  108016, 107952, 105408, 23248, 108768, 108704, 105424, 23232, 109520, 109456, 105440, 23280, 110272, 110208, 105456, 23264,
  98880, 98944, 107520, 35392, 99664, 99728, 107536, 35408, 100448, 100512, 107552, 35424, 101232, 101296, 107568, 35440,
  101888, 102080, 107584, 35328, 102672, 102864, 107600, 35344, 103456, 103648, 107616, 35360, 104240, 104432, 107632, 35376,
  105024, 105088, 107648, 35520, 105808, 105872, 107664, 35536, 106592, 106656, 107680, 35552, 107376, 107440, 107696, 35568,
  108032, 108224, 107712, 35456, 108816, 109008, 107728, 35472, 109600, 109792, 107744, 35488, 110384, 110576, 107760, 35504,
  98896, 98960, 107776, 35648, 99648, 99712, 107792, 35664, 100464, 100528, 107808, 35680, 101216, 101280, 107824, 35696,
  101904, 102096, 107840, 35584, 102656, 102848, 107856, 35600, 103472, 103664, 107872, 35616, 104224, 104416, 107888, 35632,
  105040, 105104, 107904, 35776, 105792, 105856, 107920, 35792, 106608, 106672, 107936, 35808, 107360, 107424, 107952, 35824,
  108048, 108240, 107968, 35712, 108800, 108992, 107984, 35728, 109616, 109808, 108000, 35744, 110368, 110560, 108016, 35760,
  98912, 98976, 108032, 35904, 99696, 99760, 108048, 35920, 100416, 100480, 108064, 35936, 101200, 101264, 108080, 35952,
  101920, 102112, 108096, 35840, 102704, 102896, 108112, 35856, 103424, 103616, 108128, 35872, 104208, 104400, 108144, 35888,
  105056, 105120, 108160, 36032, 105840, 105904, 108176, 36048, 106560, 106624, 108192, 36064, 107344, 107408, 108208, 36080,
  108064, 108256, 108224, 35968, 108848, 109040, 108240, 35984, 109568, 109760, 108256, 36000, 110352, 110544, 108272, 36016,
  98928, 98992, 108288, 36944, 99680, 99744, 108304, 36928, 100432, 100496, 108320, 36976, 101184, 101248, 108336, 36960,
  101936, 102128, 108352, 36880, 102688, 102880, 108368, 36864, 103440, 103632, 108384, 36912, 104192, 104384, 108400, 36896,
  105072, 105136, 108416, 37072, 105824, 105888, 108432, 37056, 106576, 106640, 108448, 37104, 107328, 107392, 108464, 37088,
  108080, 108272, 108480, 37008, 108832, 109024, 108496, 36992, 109584, 109776, 108512, 37040, 110336, 110528, 108528, 37024,
  23312, 32800, 6208, 35520, 23296, 32816, 6224, 35536, 23344, 32768, 6240, 35552, 23328, 32784, 6256, 35568,
  23376, 32864, 6144, 35456, 23360, 32880, 6160, 35472, 23408, 32832, 6176, 35488, 23392, 32848, 6192, 35504,
  23440, 32928, 6336, 35392, 23424, 32944, 6352, 35408, 23472, 32896, 6368, 35424, 23456, 32912, 6384, 35440,
  23504, 32992, 6272, 35328, 23488, 33008, 6288, 35344, 23536, 32960, 6304, 35360, 23520, 32976, 6320, 35376,
  23568, 33840, 6976, 36288, 23552, 33824, 6992, 36304, 23600, 33808, 7008, 36320, 23584, 33792, 7024, 36336,
  23632, 33904, 6912, 36224, 23616, 33888, 6928, 36240, 23664, 33872, 6944, 36256, 23648, 33856, 6960, 36272,
  23696, 33968, 7104, 36160, 23680, 33952, 7120, 36176, 23728, 33936, 7136, 36192, 23712, 33920, 7152, 36208,
  23760, 34032, 7040, 36096, 23744, 34016, 7056, 36112, 23792, 34000, 7072, 36128, 23776, 33984, 7088, 36144,
  32288, 34096, 16976, 37072, 32304, 34080, 16960, 37056, 32256, 34064, 17008, 37104, 32272, 34048, 16992, 37088,
  32352, 34160, 16912, 37008, 32368, 34144, 16896, 36992, 32320, 34128, 16944, 37040, 32336, 34112, 16928, 37024,
  32416, 34224, 17104, 36944, 32432, 34208, 17088, 36928, 32384, 34192, 17136, 36976, 32400, 34176, 17120, 36960,
  32480, 34288, 17040, 36880, 32496, 34272, 17024, 36864, 32448, 34256, 17072, 36912, 32464, 34240, 17056, 36896,
  32544, 34352, 17744, 37840, 32560, 34336, 17728, 37824, 32512, 34320, 17776, 37872, 32528, 34304, 17760, 37856,
  32608, 34416, 17680, 37776, 32624, 34400, 17664, 37760, 32576, 34384, 17712, 37808, 32592, 34368, 17696, 37792,
  32672, 34480, 17872, 37712, 32688, 34464, 17856, 37696, 32640, 34448, 17904, 37744, 32656, 34432, 17888, 37728,
  32736, 34544, 17808, 37648, 32752, 34528, 17792, 37632, 32704, 34512, 17840, 37680, 32720, 34496, 17824, 37664,
  37200, 39008, 18528, 38624, 37184, 39024, 18544, 38640, 37232, 38976, 18496, 38592, 37216, 38992, 18512, 38608,
  37136, 38944, 18464, 38560, 37120, 38960, 18480, 38576, 37168, 38912, 18432, 38528, 37152, 38928, 18448, 38544,
  37328, 39136, 18656, 38496, 37312, 39152, 18672, 38512, 37360, 39104, 18624, 38464, 37344, 39120, 18640, 38480,
  37264, 39072, 18592, 38432, 37248, 39088, 18608, 38448, 37296, 39040, 18560, 38400, 37280, 39056, 18576, 38416,
  37456, 40048, 19296, 39392, 37440, 40032, 19312, 39408, 37488, 40016, 19264, 39360, 37472, 40000, 19280, 39376,
  37392, 39984, 19232, 39328, 37376, 39968, 19248, 39344, 37424, 39952, 19200, 39296, 37408, 39936, 19216, 39312,
  37584, 40176, 19424, 39264, 37568, 40160, 19440, 39280, 37616, 40144, 19392, 39232, 37600, 40128, 19408, 39248,
  37520, 40112, 19360, 39200, 37504, 40096, 19376, 39216, 37552, 40080, 19328, 39168, 37536, 40064, 19344, 39184,
  38496, 40304, 20080, 40176, 38512, 40288, 20064, 40160, 38464, 40272, 20048, 40144, 38480, 40256, 20032, 40128,
  38432, 40240, 20016, 40112, 38448, 40224, 20000, 40096, 38400, 40208, 19984, 40080, 38416, 40192, 19968, 40064,
  38624, 40432, 20208, 40048, 38640, 40416, 20192, 40032, 38592, 40400, 20176, 40016, 38608, 40384, 20160, 40000,
  38560, 40368, 20144, 39984, 38576, 40352, 20128, 39968, 38528, 40336, 20112, 39952, 38544, 40320, 20096, 39936,
  38752, 40560, 20848, 40944, 38768, 40544, 20832, 40928, 38720, 40528, 20816, 40912, 38736, 40512, 20800, 40896,
  38688, 40496, 20784, 40880, 38704, 40480, 20768, 40864, 38656, 40464, 20752, 40848, 38672, 40448, 20736, 40832,
  38880, 40688, 20976, 40816, 38896, 40672, 20960, 40800, 38848, 40656, 20944, 40784, 38864, 40640, 20928, 40768,
  38816, 40624, 20912, 40752, 38832, 40608, 20896, 40736, 38784, 40592, 20880, 40720, 38800, 40576, 20864, 40704};

__global__ __attribute__((aligned(4096))) __launch_bounds__(256, 1) void fpn_kernel(const char* __restrict__ cf, const char* __restrict__ pc,
                                                     const char* __restrict__ w1, const char* __restrict__ w2,
                                                     char* __restrict__ ws, char* __restrict__ out) {
  unsigned lane = threadIdx.x & 63;
  unsigned wid = __builtin_amdgcn_readfirstlane(threadIdx.x >> 6);
  unsigned b = blockIdx.x;
  unsigned long long t64;
  asm volatile("s_memrealtime %0" : "={s[88:89]}"(t64), "+s"(b), "+s"(wid));
  const unsigned t_start = (unsigned)t64;
  const unsigned pr = (b >> 3) * 4 + (b & 3);
  unsigned xo[9], pg[9];
#pragma unroll
  for (int j = 0; j < 9; ++j) {
    xo[j] = ((3 * pr) >> 1) * 589824u + (pr & 1) * (147456u + (j >= 3 && j < 6 ? 294912u : 0)) + j * 49152u + (j >= 6 ? 294912u : 0);
    pg[j] = xo[j] >> 21;
  }
  int dj[9];
#pragma unroll
  for (int j = 1; j < 9; ++j) dj[j] = (int)(pg[j] - pg[j - 1]) * (j <= 4 ? -12288 : 12288);
  const int d0 = (int)(pg[4] - pg[0]) * 12288;
  dj[0] = d0 - (int)(pg[8] - pg[4]) * 12288;
  const unsigned scr = 159744 + wid * 4;
  const int sgn = (4 * wid + (lane >> 4)) < 10 ? 1 : -1;
  const unsigned mP = 73728 + wid * 1024;
  unsigned pvo = __umul24(col0(4 * wid + (lane >> 4)), 768) + ((((lane & 15) ^ (lane >> 4) ^ (4 * wid)) & 15) << 4)
                 + pr * (9 * 24576);
  int sg1;
  u32x2 cd, cr, po;
  po[0] = xo[4] + lane * 1024 + (b & 4) * 128;
  po[1] = po[0] + 8192;
  const unsigned mJ = 110592 + wid * 6144;
  const unsigned var = (b + 112) >> 8;
  u32x4 ja0, ja1, ja2, ja3, ja4, ja5;
  u32x2 ep;
  unsigned two = min((((b >> 3) - 18) * 4 + wid) * 42 + min(lane, 41u), 2303u) * 128;
  unsigned tcode = ((((b >> 3) - 18) * 4 + wid) * 11 + min(lane, 10u)) * 128;
  const unsigned jb = b < 144 ? b : 0;
  const unsigned G32 = (jb * 171) >> 11, jcb = jb - 12 * G32;
  {
    const unsigned jw = __umul24(lane >> 2, 768) + (lane & 3) * 16 + 64 * G32 + wid * (96 * 768);
    const unsigned ja = __umul24(8 * ((lane & 15) >> 2) + (lane & 3), 768) + 16 * (lane >> 4) + jcb * (32 * 768) + wid * 192;
    const unsigned cla = 1048576 + pr * 128;
    asm volatile(
      "s_nop 4\n"
      "s_cmp_lg_u32 s76, 0\n"
      "s_cbranch_scc1 LQ0\n"
      "s_mul_i32 s62, s88, 0x1080\n"
      "s_and_b32 s62, s62, 0x4180\n"
      "s_add_u32 s56, s30, s62\n"
      "s_addc_u32 s57, s31, 0\n"
      "s_cmp_lg_u32 s76, 0\n"
      "s_mov_b64 exec, 0x1\n"
      "global_load_dword v116, v118, s[56:57] nt\n"
      "global_load_dword v117, v119, s[56:57] nt\n"
      "s_mov_b64 exec, -1\n"
      "LQ0:\n"
      "v_mov_b32 v0, 0\n"
      "s_barrier\n"
      "global_load_dwordx2 v[252:253], v0, s[28:29] sc1\n"
      "s_cbranch_scc1 LQ1\n"
      ".p2align 5\n"
      "s_waitcnt vmcnt(2)\n"
      "s_memtime s[60:61]\n"
      "s_waitcnt vmcnt(1)\n"
      "s_memtime s[62:63]\n"
      "s_waitcnt vmcnt(0) lgkmcnt(0)\n"
      "s_sub_u32 s62, s62, s60\n"
      "s_bfe_u32 s63, s51, 0x10002\n"
      "s_cmp_lt_u32 s62, 30\n"
      "s_cselect_b32 s62, 1, 0\n"
      "v_mov_b32 v116, s62\n"
      "ds_write_b32 v108, v116 offset:16\n"
      "s_xor_b32 s62, s62, s63\n"
      "v_add_co_u32 v110, vcc, 0x7f4a7c15, v252\n"
      "v_mov_b32 v111, 0x9e3779b9\n"
      "v_addc_co_u32 v111, vcc, v111, v253, vcc\n"
      "v_and_or_b32 v110, v110, -2, s62\n"
      "s_mov_b64 exec, 1\n"
      "global_atomic_swap_x2 v[112:113], v114, v[110:111], s[28:29] sc0 sc1\n"
      "s_mov_b64 exec, -1\n"
      "LQ1:\n"
      "s_cmp_lg_u32 s50, 0\n"
      "s_cbranch_scc0 LU1\n"
      "s_getpc_b64 s[56:57]\n"
      "LC0_1:\n"
      "v_min_u32 v106, LCEND-LC0_1-4, v106\n"
      "global_load_dword v106, v106, s[56:57]\n"
      "global_load_dword v105, v105, s[26:27]\n"
      "v_and_b32 v117, -16, v108\n"
      "s_waitcnt lgkmcnt(0)\n"
      "s_barrier\n"
      "ds_read_b32 v116, v117 offset:16\n"
      "s_waitcnt lgkmcnt(0)\n"
      "v_readfirstlane_b32 s62, v116\n"
      "s_nop 3\n"
      "s_mul_i32 s63, s62, 0x3000\n"
      "s_sub_u32 s82, s98, s63\n"
      "s_lshl_b32 s62, s62, 1\n"
      "s_sub_u32 s62, 1, s62\n"
      "v_mul_i32_i24 v107, s62, v251\n"
      "v_mad_i32_i24 v238, v107, s82, v238\n"
      "s_add_u32 s56, s22, 0x0\n"
      "s_addc_u32 s57, s23, 0\n"
      "s_add_u32 m0, s18, 0x0\n"
      "s_nop 0\n"
      "global_load_lds_dwordx4 v238, s[56:57] offset:0 \n"
      "v_mad_i32_i24 v238, v107, s90, v238\n"
      "s_add_u32 s56, s22, 0x6000\n"
      "s_addc_u32 s57, s23, 0\n"
      "s_add_u32 m0, s18, 0x1000\n"
      "s_nop 0\n"
      "global_load_lds_dwordx4 v238, s[56:57] offset:0 \n"
      "v_mad_i32_i24 v238, v107, s91, v238\n"
      "s_add_u32 s56, s22, 0xc000\n"
      "s_addc_u32 s57, s23, 0\n"
      "s_add_u32 m0, s18, 0x2000\n"
      "s_nop 0\n"
      "global_load_lds_dwordx4 v238, s[56:57] offset:0 \n"
      "v_mad_i32_i24 v238, v107, s92, v238\n"
      "s_add_u32 s56, s22, 0x12000\n"
      "s_addc_u32 s57, s23, 0\n"
      "s_add_u32 m0, s18, 0x3000\n"
      "s_nop 0\n"
      "global_load_lds_dwordx4 v238, s[56:57] offset:0 \n"
      "v_mad_i32_i24 v238, v107, s93, v238\n"
      "s_add_u32 s56, s22, 0x18000\n"
      "s_addc_u32 s57, s23, 0\n"
      "s_add_u32 m0, s18, 0x4000\n"
      "s_nop 0\n"
      "global_load_lds_dwordx4 v238, s[56:57] offset:0 \n"
      "v_mad_i32_i24 v238, v107, s94, v238\n"
      "s_add_u32 s56, s22, 0x1e000\n"
      "s_addc_u32 s57, s23, 0\n"
      "s_add_u32 m0, s18, 0x5000\n"
      "s_nop 0\n"
      "global_load_lds_dwordx4 v238, s[56:57] offset:0 \n"
      "v_mad_i32_i24 v238, v107, s95, v238\n"
      "s_add_u32 s56, s22, 0x24000\n"
      "s_addc_u32 s57, s23, 0\n"
      "s_add_u32 m0, s18, 0x6000\n"
      "s_nop 0\n"
      "global_load_lds_dwordx4 v238, s[56:57] offset:0 \n"
      "v_mad_i32_i24 v238, v107, s96, v238\n"
      "s_add_u32 s56, s22, 0x2a000\n"
      "s_addc_u32 s57, s23, 0\n"
      "s_add_u32 m0, s18, 0x7000\n"
      "s_nop 0\n"
      "global_load_lds_dwordx4 v238, s[56:57] offset:0 \n"
      "v_mad_i32_i24 v238, v107, s97, v238\n"
      "s_add_u32 s56, s22, 0x30000\n"
      "s_addc_u32 s57, s23, 0\n"
      "s_add_u32 m0, s18, 0x8000\n"
      "s_nop 0\n"
      "global_load_lds_dwordx4 v238, s[56:57] offset:0 \n"
      "s_branch LUE\n"
      "LU1:\n"
      "s_add_u32 s56, s26, 0x0\n"
      "s_addc_u32 s57, s27, 0\n"
      "s_add_u32 m0, s75, 0x0\n"
      "s_nop 0\n"
      "global_load_lds_dwordx4 v100, s[56:57]\n"
      "s_add_u32 s56, s26, 0x3000\n"
      "s_addc_u32 s57, s27, 0\n"
      "s_add_u32 m0, s75, 0x400\n"
      "s_nop 0\n"
      "global_load_lds_dwordx4 v100, s[56:57]\n"
      "s_add_u32 s56, s26, 0x6000\n"
      "s_addc_u32 s57, s27, 0\n"
      "s_add_u32 m0, s75, 0x800\n"
      "s_nop 0\n"
      "global_load_lds_dwordx4 v100, s[56:57]\n"
      "s_add_u32 s56, s26, 0x9000\n"
      "s_addc_u32 s57, s27, 0\n"
      "s_add_u32 m0, s75, 0xc00\n"
      "s_nop 0\n"
      "global_load_lds_dwordx4 v100, s[56:57]\n"
      "s_add_u32 s56, s26, 0xc000\n"
      "s_addc_u32 s57, s27, 0\n"
      "s_add_u32 m0, s75, 0x1000\n"
      "s_nop 0\n"
      "global_load_lds_dwordx4 v100, s[56:57]\n"
      "s_add_u32 s56, s26, 0xf000\n"
      "s_addc_u32 s57, s27, 0\n"
      "s_add_u32 m0, s75, 0x1400\n"
      "s_nop 0\n"
      "global_load_lds_dwordx4 v100, s[56:57]\n"
      "global_load_dwordx4 v[4:7], v101, s[24:25] offset:0\n"
      "global_load_dwordx4 v[8:11], v101, s[24:25] offset:3072\n"
      "global_load_dwordx4 v[12:15], v101, s[24:25] offset:64\n"
      "global_load_dwordx4 v[16:19], v101, s[24:25] offset:3136\n"
      "global_load_dwordx4 v[20:23], v101, s[24:25] offset:128\n"
      "global_load_dwordx4 v[24:27], v101, s[24:25] offset:3200\n"
      "LUE:\n"

      : "={v[4:7]}"(ja0), "={v[8:11]}"(ja1), "={v[12:15]}"(ja2), "={v[16:19]}"(ja3), "={v[20:23]}"(ja4), "={v[24:27]}"(ja5),
        "={v[252:253]}"(ep), "+{v105}"(two), "+{v106}"(tcode), "={v[110:111]}"(cd), "={v[112:113]}"(cr), "+{v[118:119]}"(po),
        "+{v238}"(pvo), "={v107}"(sg1)
      : "{v114}"(cla), "{s[88:89]}"(t64), "{v100}"(jw), "{v101}"(ja), "{s[20:21]}"(cf), "{s[24:25]}"(w1), "{s[26:27]}"(w2), "{s[28:29]}"(ws), "{s[30:31]}"(out),
        "{s75}"(mJ), "{s51}"(b), "{s50}"(var), "{s76}"(wid), "{s[22:23]}"(pc), "{s18}"(mP), "{v108}"(scr), "{v251}"(sgn),
        "{s90}"(dj[1]), "{s91}"(dj[2]), "{s92}"(dj[3]), "{s93}"(dj[4]), "{s94}"(dj[5]), "{s95}"(dj[6]), "{s96}"(dj[7]), "{s97}"(dj[8]), "{s98}"(d0)
      : "memory", "scc", "vcc", "v0", "v116", "v117", "s56", "s57", "s60", "s61", "s62", "s63", "s82");
  }
  asm volatile("" : "+v"(lane));
  asm volatile("" : "+s"(wid));
  unsigned wro[3];
#pragma unroll
  for (int i = 0; i < 3; ++i) { const unsigned x = lane + 64 * i, q = __umul24(x, 171) >> 11; wro[i] = __umul24(q, 576) + x * 16; }
  const unsigned mW = wid * 6144;
  const char* w2w = w2 + wid * 192;
  const unsigned lo16 = lane * 16;
  const char* wb = ws + (65536 + wid * 73728);
  const unsigned c = lane & 15, g = lane >> 4;
  const unsigned cB0 = 73728 + c * 256 + ((g ^ c) << 4);
  const unsigned r8 = 8 * g + (c >> 2), c3 = (c & 3) * 16;
  const unsigned tra = mW + __umul24(r8, 192) + c3;
  const unsigned mX = wid * 1024;
  const unsigned fla = 4096 + wid * 288 + min(lo16, 272u);
  const unsigned c256 = c * 256 - 73728;
  const unsigned jfl = (b < 144 ? 4096 : 8192) + 8 * b;
  char* jo = ws + (65536 + (24 * G32 + jcb) * 1024);
  const unsigned jt = mJ + r8 * 64 + c3;
  const unsigned jrr = 135168 + lo16;
  const unsigned jrw = jrr + wid * 4096;
  const unsigned pk = 151552 + mX + lo16;
  const char* tab = (const char*)lane_tab + ((wid >> 1) * 4096 + (wid & 1) * 1024);
  asm volatile(
      "s_nop 4\n"
      "LBODY:\n"
      "s_branch LB0\n"
      "LRET:\n"
      "s_setpc_b64 s[56:57]\n"
      "LB0:\n"
      "s_cmp_lg_u32 s50, 0\n"
      "s_cbranch_scc1 LV1\n"
      "s_getpc_b64 s[56:57]\n"
      "LTR_0:\n"
      "s_add_u32 s56, s56, LTE_0-LTR_0\n"
      "s_addc_u32 s57, s57, 0\n"
      "s_mov_b64 exec, 0\n"
      "s_cmp_eq_u32 s76, 3\n"
      "s_cbranch_scc1 LPD2\n"
      "s_cmp_eq_u32 s76, 2\n"
      "s_cbranch_scc1 LPD1\n"
      "s_cmp_eq_u32 s76, 1\n"
      "s_cbranch_scc1 LPD0\n"
      "LTE_0:\n"
      "s_mov_b64 exec, -1\n"
      "s_waitcnt vmcnt(12)\n"
      "v_readfirstlane_b32 s80, v252\n"
      "v_readfirstlane_b32 s81, v253\n"
      "s_nop 3\n"
      "s_add_u32 s80, s80, 0x7f4a7c15\n"
      "s_addc_u32 s81, s81, 0x9e3779b9\n"
      "v_mov_b32 v252, s80\n"
      "v_mov_b32 v253, s81\n"
      "v_xor_b32 v116, v112, v110\n"
      "v_xor_b32 v117, v113, v111\n"
      "v_and_or_b32 v116, v116, -2, v117\n"
      "v_cmp_eq_u32 vcc, 0, v116\n"
      "v_cndmask_b32 v116, v110, v112, vcc\n"
      "s_mov_b64 exec, 1\n"
      "ds_write_b32 v108, v116\n"
      "s_mov_b64 exec, -1\n"
      "v_and_b32 v108, -16, v108\n"
      "s_waitcnt lgkmcnt(0)\n"
      "s_barrier\n"
      "ds_read_b32 v116, v108\n"
      "s_waitcnt lgkmcnt(0)\n"
      "v_readfirstlane_b32 s62, v116\n"
      "s_lshr_b32 s63, s51, 2\n"
      "s_nop 2\n"
      "s_xor_b32 s62, s62, s63\n"
      "s_and_b32 s62, s62, 1\n"
      "s_mul_i32 s63, s62, 0x3000\n"
      "s_sub_u32 s98, s98, s63\n"
      "s_lshl_b32 s62, s62, 1\n"
      "s_sub_u32 s62, 1, s62\n"
      "v_mul_i32_i24 v251, s62, v251\n"
      "s_lshl_b32 s19, s62, 1\n"
      "s_mul_i32 s100, s100, s62\n"
      "v_mad_i32_i24 v238, v251, s98, v238\n"
      "s_add_u32 s56, s22, 0x0\n"
      "s_addc_u32 s57, s23, 0\n"
      "s_add_u32 m0, s18, 0x0\n"
      "s_nop 0\n"
      "global_load_lds_dwordx4 v238, s[56:57] offset:0 \n"
      "v_mad_i32_i24 v238, v251, s90, v238\n"
      "s_add_u32 s56, s22, 0x6000\n"
      "s_addc_u32 s57, s23, 0\n"
      "s_add_u32 m0, s18, 0x1000\n"
      "s_nop 0\n"
      "global_load_lds_dwordx4 v238, s[56:57] offset:0 \n"
      "v_mad_i32_i24 v238, v251, s91, v238\n"
      "s_add_u32 s56, s22, 0xc000\n"
      "s_addc_u32 s57, s23, 0\n"
      "s_add_u32 m0, s18, 0x2000\n"
      "s_nop 0\n"
      "global_load_lds_dwordx4 v238, s[56:57] offset:0 \n"
      "v_mad_i32_i24 v238, v251, s92, v238\n"
      "s_add_u32 s56, s22, 0x12000\n"
      "s_addc_u32 s57, s23, 0\n"
      "s_add_u32 m0, s18, 0x3000\n"
      "s_nop 0\n"
      "global_load_lds_dwordx4 v238, s[56:57] offset:0 \n"
      "v_mad_i32_i24 v238, v251, s93, v238\n"
      "s_add_u32 s56, s22, 0x18000\n"
      "s_addc_u32 s57, s23, 0\n"
      "s_add_u32 m0, s18, 0x4000\n"
      "s_nop 0\n"
      "global_load_lds_dwordx4 v238, s[56:57] offset:0 \n"
      "v_mad_i32_i24 v238, v251, s94, v238\n"
      "s_add_u32 s56, s22, 0x1e000\n"
      "s_addc_u32 s57, s23, 0\n"
      "s_add_u32 m0, s18, 0x5000\n"
      "s_nop 0\n"
      "global_load_lds_dwordx4 v238, s[56:57] offset:0 \n"
      "v_mad_i32_i24 v238, v251, s95, v238\n"
      "s_add_u32 s56, s22, 0x24000\n"
      "s_addc_u32 s57, s23, 0\n"
      "s_add_u32 m0, s18, 0x6000\n"
      "s_nop 0\n"
      "global_load_lds_dwordx4 v238, s[56:57] offset:0 \n"
      "v_mad_i32_i24 v238, v251, s96, v238\n"
      "s_add_u32 s56, s22, 0x2a000\n"
      "s_addc_u32 s57, s23, 0\n"
      "s_add_u32 m0, s18, 0x7000\n"
      "s_nop 0\n"
      "global_load_lds_dwordx4 v238, s[56:57] offset:0 \n"
      "v_mad_i32_i24 v238, v251, s97, v238\n"
      "s_add_u32 s56, s22, 0x30000\n"
      "s_addc_u32 s57, s23, 0\n"
      "s_add_u32 m0, s18, 0x8000\n"
      "s_nop 0\n"
      "global_load_lds_dwordx4 v238, s[56:57] offset:0 \n"
      "s_waitcnt vmcnt(9)\n"
      "ds_read_b64_tr_b16 v[28:29], v102 offset:0\n"
      "ds_read_b64_tr_b16 v[30:31], v102 offset:256\n"
      "ds_read_b64_tr_b16 v[32:33], v102 offset:8\n"
      "ds_read_b64_tr_b16 v[34:35], v102 offset:264\n"
      "ds_read_b64_tr_b16 v[36:37], v102 offset:2048\n"
      "ds_read_b64_tr_b16 v[38:39], v102 offset:2304\n"
      "ds_read_b64_tr_b16 v[40:41], v102 offset:2056\n"
      "ds_read_b64_tr_b16 v[42:43], v102 offset:2312\n"
      "ds_read_b64_tr_b16 v[44:45], v102 offset:4096\n"
      "ds_read_b64_tr_b16 v[46:47], v102 offset:4352\n"
      "ds_read_b64_tr_b16 v[48:49], v102 offset:4104\n"
      "ds_read_b64_tr_b16 v[50:51], v102 offset:4360\n"
      "s_waitcnt lgkmcnt(0)\n"
      "v_mfma_f32_16x16x32_bf16 v[52:55], v[4:7], v[28:31], 0\n"
      "v_mfma_f32_16x16x32_bf16 v[56:59], v[8:11], v[28:31], 0\n"
      "v_mfma_f32_16x16x32_bf16 v[60:63], v[4:7], v[32:35], 0\n"
      "v_mfma_f32_16x16x32_bf16 v[64:67], v[8:11], v[32:35], 0\n"
      "v_mfma_f32_16x16x32_bf16 v[52:55], v[12:15], v[36:39], v[52:55]\n"
      "v_mfma_f32_16x16x32_bf16 v[56:59], v[16:19], v[36:39], v[56:59]\n"
      "v_mfma_f32_16x16x32_bf16 v[60:63], v[12:15], v[40:43], v[60:63]\n"
      "v_mfma_f32_16x16x32_bf16 v[64:67], v[16:19], v[40:43], v[64:67]\n"
      "v_mfma_f32_16x16x32_bf16 v[52:55], v[20:23], v[44:47], v[52:55]\n"
      "v_mfma_f32_16x16x32_bf16 v[56:59], v[24:27], v[44:47], v[56:59]\n"
      "v_mfma_f32_16x16x32_bf16 v[60:63], v[20:23], v[48:51], v[60:63]\n"
      "v_mfma_f32_16x16x32_bf16 v[64:67], v[24:27], v[48:51], v[64:67]\n"
      "s_nop 7\n"
      "ds_write_b128 v103, v[52:55] offset:0\n"
      "ds_write_b128 v103, v[56:59] offset:1024\n"
      "ds_write_b128 v103, v[60:63] offset:2048\n"
      "ds_write_b128 v103, v[64:67] offset:3072\n"
      "s_waitcnt lgkmcnt(0)\n"
      "s_barrier\n"
      "s_cmp_lg_u32 s76, 1\n"
      "s_cbranch_scc1 LJ1\n"
      "ds_read_b128 v[4:7], v104 offset:0\n"
      "ds_read_b128 v[8:11], v104 offset:1024\n"
      "ds_read_b128 v[12:15], v104 offset:2048\n"
      "ds_read_b128 v[16:19], v104 offset:3072\n"
      "ds_read_b128 v[20:23], v104 offset:4096\n"
      "ds_read_b128 v[24:27], v104 offset:5120\n"
      "ds_read_b128 v[28:31], v104 offset:6144\n"
      "ds_read_b128 v[32:35], v104 offset:7168\n"
      "ds_read_b128 v[36:39], v104 offset:8192\n"
      "ds_read_b128 v[40:43], v104 offset:9216\n"
      "ds_read_b128 v[44:47], v104 offset:10240\n"
      "ds_read_b128 v[48:51], v104 offset:11264\n"
      "ds_read_b128 v[52:55], v104 offset:12288\n"
      "ds_read_b128 v[56:59], v104 offset:13312\n"
      "ds_read_b128 v[60:63], v104 offset:14336\n"
      "ds_read_b128 v[64:67], v104 offset:15360\n"
      "s_waitcnt lgkmcnt(8)\n"
      "v_add_f32 v4, v4, v20\n"
      "v_add_f32 v5, v5, v21\n"
      "v_add_f32 v6, v6, v22\n"
      "v_add_f32 v7, v7, v23\n"
      "v_add_f32 v8, v8, v24\n"
      "v_add_f32 v9, v9, v25\n"
      "v_add_f32 v10, v10, v26\n"
      "v_add_f32 v11, v11, v27\n"
      "v_add_f32 v12, v12, v28\n"
      "v_add_f32 v13, v13, v29\n"
      "v_add_f32 v14, v14, v30\n"
      "v_add_f32 v15, v15, v31\n"
      "v_add_f32 v16, v16, v32\n"
      "v_add_f32 v17, v17, v33\n"
      "v_add_f32 v18, v18, v34\n"
      "v_add_f32 v19, v19, v35\n"
      "s_waitcnt lgkmcnt(4)\n"
      "v_add_f32 v4, v4, v36\n"
      "v_add_f32 v5, v5, v37\n"
      "v_add_f32 v6, v6, v38\n"
      "v_add_f32 v7, v7, v39\n"
      "v_add_f32 v8, v8, v40\n"
      "v_add_f32 v9, v9, v41\n"
      "v_add_f32 v10, v10, v42\n"
      "v_add_f32 v11, v11, v43\n"
      "v_add_f32 v12, v12, v44\n"
      "v_add_f32 v13, v13, v45\n"
      "v_add_f32 v14, v14, v46\n"
      "v_add_f32 v15, v15, v47\n"
      "v_add_f32 v16, v16, v48\n"
      "v_add_f32 v17, v17, v49\n"
      "v_add_f32 v18, v18, v50\n"
      "v_add_f32 v19, v19, v51\n"
      "s_waitcnt lgkmcnt(0)\n"
      "v_add_f32 v4, v4, v52\n"
      "v_add_f32 v5, v5, v53\n"
      "v_add_f32 v6, v6, v54\n"
      "v_add_f32 v7, v7, v55\n"
      "v_add_f32 v8, v8, v56\n"
      "v_add_f32 v9, v9, v57\n"
      "v_add_f32 v10, v10, v58\n"
      "v_add_f32 v11, v11, v59\n"
      "v_add_f32 v12, v12, v60\n"
      "v_add_f32 v13, v13, v61\n"
      "v_add_f32 v14, v14, v62\n"
      "v_add_f32 v15, v15, v63\n"
      "v_add_f32 v16, v16, v64\n"
      "v_add_f32 v17, v17, v65\n"
      "v_add_f32 v18, v18, v66\n"
      "v_add_f32 v19, v19, v67\n"
      "v_cvt_pk_bf16_f32 v68, v4, v5\n"
      "v_cvt_pk_bf16_f32 v69, v6, v7\n"
      "v_cvt_pk_bf16_f32 v70, v8, v9\n"
      "v_cvt_pk_bf16_f32 v71, v10, v11\n"
      "v_cvt_pk_bf16_f32 v72, v12, v13\n"
      "v_cvt_pk_bf16_f32 v73, v14, v15\n"
      "v_cvt_pk_bf16_f32 v74, v16, v17\n"
      "v_cvt_pk_bf16_f32 v75, v18, v19\n"
      "global_store_dwordx4 v239, v[68:71], s[72:73] sc1\n"
      "s_add_u32 s72, s72, 0x3000\n"
      "s_addc_u32 s73, s73, 0\n"
      "global_store_dwordx4 v239, v[72:75], s[72:73] sc1\n"
      "LJ1:\n"
      "s_add_u32 s78, s64, 0x0\n"
      "s_addc_u32 s79, s65, 0\n"
      "s_add_u32 m0, s74, 0x0\n"
      "s_nop 0\n"
      "global_load_lds_dwordx4 v240, s[78:79]\n"
      "s_add_u32 m0, s74, 0x400\n"
      "s_nop 0\n"
      "global_load_lds_dwordx4 v241, s[78:79]\n"
      "s_add_u32 m0, s74, 0x800\n"
      "s_nop 0\n"
      "global_load_lds_dwordx4 v242, s[78:79]\n"
      "s_add_u32 s78, s64, 0x3000\n"
      "s_addc_u32 s79, s65, 0\n"
      "s_add_u32 m0, s74, 0xc00\n"
      "s_nop 0\n"
      "global_load_lds_dwordx4 v240, s[78:79]\n"
      "s_add_u32 m0, s74, 0x1000\n"
      "s_nop 0\n"
      "global_load_lds_dwordx4 v241, s[78:79]\n"
      "s_add_u32 m0, s74, 0x1400\n"
      "s_nop 0\n"
      "global_load_lds_dwordx4 v242, s[78:79]\n"
      "s_add_u32 s78, s64, 0x6000\n"
      "s_addc_u32 s79, s65, 0\n"
      "s_add_u32 m0, s74, 0x6000\n"
      "s_nop 0\n"
      "global_load_lds_dwordx4 v240, s[78:79]\n"
      "s_add_u32 m0, s74, 0x6400\n"
      "s_nop 0\n"
      "global_load_lds_dwordx4 v241, s[78:79]\n"
      "s_add_u32 m0, s74, 0x6800\n"
      "s_nop 0\n"
      "global_load_lds_dwordx4 v242, s[78:79]\n"
      "s_add_u32 s78, s64, 0x9000\n"
      "s_addc_u32 s79, s65, 0\n"
      "s_add_u32 m0, s74, 0x6c00\n"
      "s_nop 0\n"
      "global_load_lds_dwordx4 v240, s[78:79]\n"
      "s_add_u32 m0, s74, 0x7000\n"
      "s_nop 0\n"
      "global_load_lds_dwordx4 v241, s[78:79]\n"
      "s_add_u32 m0, s74, 0x7400\n"
      "s_nop 0\n"
      "global_load_lds_dwordx4 v242, s[78:79]\n"
      "s_add_u32 s78, s64, 0xc000\n"
      "s_addc_u32 s79, s65, 0\n"
      "s_add_u32 m0, s74, 0xc000\n"
      "s_nop 0\n"
      "global_load_lds_dwordx4 v240, s[78:79]\n"
      "s_add_u32 m0, s74, 0xc400\n"
      "s_nop 0\n"
      "global_load_lds_dwordx4 v241, s[78:79]\n"
      "s_add_u32 m0, s74, 0xc800\n"
      "s_nop 0\n"
      "global_load_lds_dwordx4 v242, s[78:79]\n"
      "s_add_u32 s78, s64, 0xf000\n"
      "s_addc_u32 s79, s65, 0\n"
      "s_add_u32 m0, s74, 0xcc00\n"
      "s_nop 0\n"
      "global_load_lds_dwordx4 v240, s[78:79]\n"
      "s_add_u32 m0, s74, 0xd000\n"
      "s_nop 0\n"
      "global_load_lds_dwordx4 v241, s[78:79]\n"
      "s_add_u32 m0, s74, 0xd400\n"
      "s_nop 0\n"
      "global_load_lds_dwordx4 v242, s[78:79]\n"
      "s_add_u32 m0, s17, 0x25000\n"
      "s_nop 0\n"
      "global_load_lds_dwordx4 v239, s[46:47] offset:0\n"
      "s_add_u32 m0, s17, 0x25800\n"
      "s_nop 0\n"
      "global_load_lds_dwordx4 v239, s[46:47] offset:2048\n"
      "s_waitcnt vmcnt(14)\n"
      "ds_read_b64_tr_b16 a[0:1], v243 offset:0\n"
      "ds_read_b64_tr_b16 a[2:3], v243 offset:768\n"
      "ds_read_b64_tr_b16 a[4:5], v243 offset:8\n"
      "ds_read_b64_tr_b16 a[6:7], v243 offset:776\n"
      "ds_read_b64_tr_b16 a[8:9], v243 offset:64\n"
      "ds_read_b64_tr_b16 a[10:11], v243 offset:832\n"
      "ds_read_b64_tr_b16 a[12:13], v243 offset:72\n"
      "ds_read_b64_tr_b16 a[14:15], v243 offset:840\n"
      "ds_read_b64_tr_b16 a[16:17], v243 offset:128\n"
      "ds_read_b64_tr_b16 a[18:19], v243 offset:896\n"
      "ds_read_b64_tr_b16 a[20:21], v243 offset:136\n"
      "ds_read_b64_tr_b16 a[22:23], v243 offset:904\n"
      "s_barrier\n"
      "s_cmp_lg_u32 s76, 1\n"
      "s_cbranch_scc1 LA_0\n"
      "v_mov_b32 v244, s77\n"
      "s_mov_b64 exec, 1\n"
      "global_store_dwordx2 v244, v[252:253], s[28:29] sc1\n"
      "s_mov_b64 exec, -1\n"
      "s_nop 4\n"
      "LA_0:\n"
      "ds_read_b128 v[216:219], v232 offset:0\n"
      "ds_read_b128 v[220:223], v232 offset:4096\n"
      "ds_read_b128 v[224:227], v232 offset:8192\n"
      "s_waitcnt lgkmcnt(2)\n"
      "v_mfma_f32_16x16x32_bf16 v[0:3], a[0:3], v[216:219], 0\n"
      "s_add_u32 s78, s64, 0x12000\n"
      "s_addc_u32 s79, s65, 0\n"
      "s_add_u32 m0, s74, 0x0\n"
      "v_mfma_f32_16x16x32_bf16 v[4:7], a[4:7], v[216:219], 0\n"
      "global_load_lds_dwordx4 v240, s[78:79]\n"
      "v_mfma_f32_16x16x32_bf16 v[8:11], a[8:11], v[216:219], 0\n"
      "ds_read_b128 v[228:231], v232 offset:12288\n"
      "v_mfma_f32_16x16x32_bf16 v[12:15], a[12:15], v[216:219], 0\n"
      "v_mfma_f32_16x16x32_bf16 v[16:19], a[16:19], v[216:219], 0\n"
      "s_add_u32 m0, s74, 0x400\n"
      "v_mfma_f32_16x16x32_bf16 v[20:23], a[20:23], v[216:219], 0\n"
      "global_load_lds_dwordx4 v241, s[78:79]\n"
      "s_waitcnt lgkmcnt(2)\n"
      "v_mfma_f32_16x16x32_bf16 v[24:27], a[0:3], v[220:223], 0\n"
      "s_waitcnt vmcnt(10)\n"
      "v_mfma_f32_16x16x32_bf16 v[28:31], a[4:7], v[220:223], 0\n"
      "ds_read_b64_tr_b16 a[24:25], v243 offset:24576\n"
      "v_mfma_f32_16x16x32_bf16 v[32:35], a[8:11], v[220:223], 0\n"
      "ds_read_b128 v[216:219], v232 offset:16384\n"
      "s_add_u32 m0, s74, 0x800\n"
      "v_mfma_f32_16x16x32_bf16 v[36:39], a[12:15], v[220:223], 0\n"
      "ds_read_b64_tr_b16 a[26:27], v243 offset:25344\n"
      "global_load_lds_dwordx4 v242, s[78:79]\n"
      "v_mfma_f32_16x16x32_bf16 v[40:43], a[16:19], v[220:223], 0\n"
      "LPD0:\n"
      "s_cbranch_execz LPD3\n"
      "ds_read_b64_tr_b16 a[28:29], v243 offset:24584\n"
      "v_mfma_f32_16x16x32_bf16 v[44:47], a[20:23], v[220:223], 0\n"
      "s_waitcnt lgkmcnt(5)\n"
      "v_mfma_f32_16x16x32_bf16 v[48:51], a[0:3], v[224:227], 0\n"
      "ds_read_b64_tr_b16 a[30:31], v243 offset:25352\n"
      "s_add_u32 s78, s64, 0x15000\n"
      "s_addc_u32 s79, s65, 0\n"
      "s_add_u32 m0, s74, 0xc00\n"
      "v_mfma_f32_16x16x32_bf16 v[52:55], a[4:7], v[224:227], 0\n"
      "ds_read_b64_tr_b16 a[32:33], v243 offset:24640\n"
      "global_load_lds_dwordx4 v240, s[78:79]\n"
      "v_mfma_f32_16x16x32_bf16 v[56:59], a[8:11], v[224:227], 0\n"
      "ds_read_b128 v[220:223], v232 offset:20480\n"
      "v_mfma_f32_16x16x32_bf16 v[60:63], a[12:15], v[224:227], 0\n"
      "ds_read_b64_tr_b16 a[34:35], v243 offset:25408\n"
      "v_mfma_f32_16x16x32_bf16 v[64:67], a[16:19], v[224:227], 0\n"
      "ds_read_b64_tr_b16 a[36:37], v243 offset:24648\n"
      "s_add_u32 m0, s74, 0x1000\n"
      "v_mfma_f32_16x16x32_bf16 v[68:71], a[20:23], v[224:227], 0\n"
      "global_load_lds_dwordx4 v241, s[78:79]\n"
      "s_waitcnt lgkmcnt(9)\n"
      "v_mfma_f32_16x16x32_bf16 v[72:75], a[0:3], v[228:231], 0\n"
      "ds_read_b64_tr_b16 a[38:39], v243 offset:25416\n"
      "v_mfma_f32_16x16x32_bf16 v[76:79], a[4:7], v[228:231], 0\n"
      "ds_read_b64_tr_b16 a[40:41], v243 offset:24704\n"
      "v_mfma_f32_16x16x32_bf16 v[80:83], a[8:11], v[228:231], 0\n"
      "ds_read_b128 v[224:227], v232 offset:24576\n"
      "s_add_u32 m0, s74, 0x1400\n"
      "v_mfma_f32_16x16x32_bf16 v[84:87], a[12:15], v[228:231], 0\n"
      "ds_read_b64_tr_b16 a[42:43], v243 offset:25472\n"
      "global_load_lds_dwordx4 v242, s[78:79]\n"
      "v_mfma_f32_16x16x32_bf16 v[88:91], a[16:19], v[228:231], 0\n"
      "ds_read_b64_tr_b16 a[44:45], v243 offset:24712\n"
      "v_mfma_f32_16x16x32_bf16 v[92:95], a[20:23], v[228:231], 0\n"
      "s_waitcnt lgkmcnt(12)\n"
      "v_mfma_f32_16x16x32_bf16 v[96:99], a[0:3], v[216:219], 0\n"
      "ds_read_b64_tr_b16 a[46:47], v243 offset:25480\n"
      "v_mfma_f32_16x16x32_bf16 v[100:103], a[4:7], v[216:219], 0\n"
      "s_waitcnt vmcnt(8)\n"
      "v_mfma_f32_16x16x32_bf16 v[104:107], a[8:11], v[216:219], 0\n"
      "ds_read_b128 v[228:231], v232 offset:28672\n"
      "v_mfma_f32_16x16x32_bf16 v[108:111], a[12:15], v[216:219], 0\n"
      "ds_read_b64_tr_b16 a[48:49], v243 offset:49152\n"
      "v_mfma_f32_16x16x32_bf16 v[112:115], a[16:19], v[216:219], 0\n"
      "ds_read_b64_tr_b16 a[50:51], v243 offset:49920\n"
      "v_mfma_f32_16x16x32_bf16 v[116:119], a[20:23], v[216:219], 0\n"
      "s_waitcnt lgkmcnt(11)\n"
      "v_mfma_f32_16x16x32_bf16 v[120:123], a[0:3], v[220:223], 0\n"
      "ds_read_b64_tr_b16 a[52:53], v243 offset:49160\n"
      "v_mfma_f32_16x16x32_bf16 v[124:127], a[4:7], v[220:223], 0\n"
      "ds_read_b64_tr_b16 a[54:55], v243 offset:49928\n"
      "v_mfma_f32_16x16x32_bf16 v[128:131], a[8:11], v[220:223], 0\n"
      "ds_read_b128 v[216:219], v232 offset:32768\n"
      "v_add_u32 v232, 0x9000, v232\n"
      "v_mfma_f32_16x16x32_bf16 v[132:135], a[12:15], v[220:223], 0\n"
      "ds_read_b64_tr_b16 a[56:57], v243 offset:49216\n"
      "v_mfma_f32_16x16x32_bf16 v[136:139], a[16:19], v[220:223], 0\n"
      "ds_read_b64_tr_b16 a[58:59], v243 offset:49984\n"
      "s_waitcnt lgkmcnt(8)\n"
      "s_add_u32 s78, s64, 0x18000\n"
      "s_addc_u32 s79, s65, 0\n"
      "s_add_u32 m0, s74, 0x6000\n"
      "v_mfma_f32_16x16x32_bf16 v[140:143], a[20:23], v[220:223], 0\n"
      "global_load_lds_dwordx4 v240, s[78:79]\n"
      "v_mfma_f32_16x16x32_bf16 v[144:147], a[0:3], v[224:227], 0\n"
      "ds_read_b64_tr_b16 a[60:61], v243 offset:49224\n"
      "v_mfma_f32_16x16x32_bf16 v[148:151], a[4:7], v[224:227], 0\n"
      "ds_read_b64_tr_b16 a[62:63], v243 offset:49992\n"
      "v_mfma_f32_16x16x32_bf16 v[152:155], a[8:11], v[224:227], 0\n"
      "ds_read_b128 v[220:223], v233 offset:0\n"
      "s_add_u32 m0, s74, 0x6400\n"
      "v_mfma_f32_16x16x32_bf16 v[156:159], a[12:15], v[224:227], 0\n"
      "ds_read_b64_tr_b16 a[64:65], v243 offset:49280\n"
      "global_load_lds_dwordx4 v241, s[78:79]\n"
      "v_mfma_f32_16x16x32_bf16 v[160:163], a[16:19], v[224:227], 0\n"
      "ds_read_b64_tr_b16 a[66:67], v243 offset:50048\n"
      "v_mfma_f32_16x16x32_bf16 v[164:167], a[20:23], v[224:227], 0\n"
      "s_waitcnt lgkmcnt(12)\n"
      "v_mfma_f32_16x16x32_bf16 v[168:171], a[0:3], v[228:231], 0\n"
      "ds_read_b64_tr_b16 a[68:69], v243 offset:49288\n"
      "s_add_u32 m0, s74, 0x6800\n"
      "v_mfma_f32_16x16x32_bf16 v[172:175], a[4:7], v[228:231], 0\n"
      "ds_read_b64_tr_b16 a[70:71], v243 offset:50056\n"
      "global_load_lds_dwordx4 v242, s[78:79]\n"
      "v_mfma_f32_16x16x32_bf16 v[176:179], a[8:11], v[228:231], 0\n"
      "ds_read_b128 v[224:227], v233 offset:4096\n"
      "v_mfma_f32_16x16x32_bf16 v[180:183], a[12:15], v[228:231], 0\n"
      "v_mfma_f32_16x16x32_bf16 v[184:187], a[16:19], v[228:231], 0\n"
      "s_add_u32 s78, s64, 0x1b000\n"
      "s_addc_u32 s79, s65, 0\n"
      "s_add_u32 m0, s74, 0x6c00\n"
      "v_mfma_f32_16x16x32_bf16 v[188:191], a[20:23], v[228:231], 0\n"
      "global_load_lds_dwordx4 v240, s[78:79]\n"
      "s_waitcnt lgkmcnt(10)\n"
      "v_mfma_f32_16x16x32_bf16 v[192:195], a[0:3], v[216:219], 0\n"
      "v_mfma_f32_16x16x32_bf16 v[196:199], a[4:7], v[216:219], 0\n"
      "v_mfma_f32_16x16x32_bf16 v[200:203], a[8:11], v[216:219], 0\n"
      "ds_read_b128 v[228:231], v233 offset:8192\n"
      "s_add_u32 m0, s74, 0x7000\n"
      "v_mfma_f32_16x16x32_bf16 v[204:207], a[12:15], v[216:219], 0\n"
      "global_load_lds_dwordx4 v241, s[78:79]\n"
      "v_mfma_f32_16x16x32_bf16 v[208:211], a[16:19], v[216:219], 0\n"
      "v_mfma_f32_16x16x32_bf16 v[212:215], a[20:23], v[216:219], 0\n"
      "s_waitcnt lgkmcnt(6)\n"
      "v_mfma_f32_16x16x32_bf16 v[0:3], a[24:27], v[220:223], v[0:3]\n"
      "s_add_u32 m0, s74, 0x7400\n"
      "v_mfma_f32_16x16x32_bf16 v[4:7], a[28:31], v[220:223], v[4:7]\n"
      "global_load_lds_dwordx4 v242, s[78:79]\n"
      "v_mfma_f32_16x16x32_bf16 v[8:11], a[32:35], v[220:223], v[8:11]\n"
      "ds_read_b128 v[216:219], v233 offset:12288\n"
      "s_waitcnt vmcnt(6)\n"
      "v_mfma_f32_16x16x32_bf16 v[12:15], a[36:39], v[220:223], v[12:15]\n"
      "ds_read_b64_tr_b16 a[72:73], v243 offset:0\n"
      "v_mfma_f32_16x16x32_bf16 v[16:19], a[40:43], v[220:223], v[16:19]\n"
      "ds_read_b64_tr_b16 a[74:75], v243 offset:768\n"
      "v_mad_i32_i24 v238, v251, s89, v238\n"
      "s_add_u32 s56, s22, 0x0\n"
      "s_addc_u32 s57, s23, 0\n"
      "s_add_u32 m0, s18, 0x8f00\n"
      "v_mfma_f32_16x16x32_bf16 v[20:23], a[44:47], v[220:223], v[20:23]\n"
      "global_load_lds_dwordx4 v238, s[56:57] offset:256 \n"
      "s_waitcnt lgkmcnt(4)\n"
      "v_mfma_f32_16x16x32_bf16 v[24:27], a[24:27], v[224:227], v[24:27]\n"
      "ds_read_b64_tr_b16 a[76:77], v243 offset:8\n"
      "v_mfma_f32_16x16x32_bf16 v[28:31], a[28:31], v[224:227], v[28:31]\n"
      "ds_read_b64_tr_b16 a[78:79], v243 offset:776\n"
      "v_mad_i32_i24 v238, v251, s90, v238\n"
      "s_add_u32 s56, s22, 0x6000\n"
      "s_addc_u32 s57, s23, 0\n"
      "s_add_u32 m0, s18, 0x9f00\n"
      "v_mfma_f32_16x16x32_bf16 v[32:35], a[32:35], v[224:227], v[32:35]\n"
      "ds_read_b128 v[220:223], v233 offset:16384\n"
      "global_load_lds_dwordx4 v238, s[56:57] offset:256 \n"
      "v_mfma_f32_16x16x32_bf16 v[36:39], a[36:39], v[224:227], v[36:39]\n"
      "ds_read_b64_tr_b16 a[80:81], v243 offset:64\n"
      "v_mfma_f32_16x16x32_bf16 v[40:43], a[40:43], v[224:227], v[40:43]\n"
      "ds_read_b64_tr_b16 a[82:83], v243 offset:832\n"
      "v_mad_i32_i24 v238, v251, s91, v238\n"
      "s_add_u32 s56, s22, 0xc000\n"
      "s_addc_u32 s57, s23, 0\n"
      "s_add_u32 m0, s18, 0xaf00\n"
      "v_mfma_f32_16x16x32_bf16 v[44:47], a[44:47], v[224:227], v[44:47]\n"
      "global_load_lds_dwordx4 v238, s[56:57] offset:256 \n"
      "s_waitcnt lgkmcnt(8)\n"
      "v_mfma_f32_16x16x32_bf16 v[48:51], a[24:27], v[228:231], v[48:51]\n"
      "ds_read_b64_tr_b16 a[84:85], v243 offset:72\n"
      "v_mfma_f32_16x16x32_bf16 v[52:55], a[28:31], v[228:231], v[52:55]\n"
      "ds_read_b64_tr_b16 a[86:87], v243 offset:840\n"
      "v_mad_i32_i24 v238, v251, s92, v238\n"
      "s_add_u32 s56, s22, 0x12000\n"
      "s_addc_u32 s57, s23, 0\n"
      "s_add_u32 m0, s18, 0xbf00\n"
      "v_mfma_f32_16x16x32_bf16 v[56:59], a[32:35], v[228:231], v[56:59]\n"
      "ds_read_b128 v[224:227], v233 offset:20480\n"
      "global_load_lds_dwordx4 v238, s[56:57] offset:256 \n"
      "v_mfma_f32_16x16x32_bf16 v[60:63], a[36:39], v[228:231], v[60:63]\n"
      "ds_read_b64_tr_b16 a[88:89], v243 offset:128\n"
      "v_mfma_f32_16x16x32_bf16 v[64:67], a[40:43], v[228:231], v[64:67]\n"
      "ds_read_b64_tr_b16 a[90:91], v243 offset:896\n"
      "v_mad_i32_i24 v238, v251, s93, v238\n"
      "s_add_u32 s56, s22, 0x18000\n"
      "s_addc_u32 s57, s23, 0\n"
      "s_add_u32 m0, s18, 0xcf00\n"
      "v_mfma_f32_16x16x32_bf16 v[68:71], a[44:47], v[228:231], v[68:71]\n"
      "global_load_lds_dwordx4 v238, s[56:57] offset:256 \n"
      "s_waitcnt lgkmcnt(12)\n"
      "v_mfma_f32_16x16x32_bf16 v[72:75], a[24:27], v[216:219], v[72:75]\n"
      "ds_read_b64_tr_b16 a[92:93], v243 offset:136\n"
      "v_mfma_f32_16x16x32_bf16 v[76:79], a[28:31], v[216:219], v[76:79]\n"
      "ds_read_b64_tr_b16 a[94:95], v243 offset:904\n"
      "v_mad_i32_i24 v238, v251, s94, v238\n"
      "s_add_u32 s56, s22, 0x1e000\n"
      "s_addc_u32 s57, s23, 0\n"
      "s_add_u32 m0, s18, 0xdf00\n"
      "v_mfma_f32_16x16x32_bf16 v[80:83], a[32:35], v[216:219], v[80:83]\n"
      "ds_read_b128 v[228:231], v233 offset:24576\n"
      "global_load_lds_dwordx4 v238, s[56:57] offset:256 \n"
      "v_mfma_f32_16x16x32_bf16 v[84:87], a[36:39], v[216:219], v[84:87]\n"
      "v_mfma_f32_16x16x32_bf16 v[88:91], a[40:43], v[216:219], v[88:91]\n"
      "v_mad_i32_i24 v238, v251, s95, v238\n"
      "s_add_u32 s56, s22, 0x24000\n"
      "s_addc_u32 s57, s23, 0\n"
      "s_add_u32 m0, s18, 0xef00\n"
      "v_mfma_f32_16x16x32_bf16 v[92:95], a[44:47], v[216:219], v[92:95]\n"
      "global_load_lds_dwordx4 v238, s[56:57] offset:256 \n"
      "s_waitcnt lgkmcnt(10)\n"
      "v_mfma_f32_16x16x32_bf16 v[96:99], a[24:27], v[220:223], v[96:99]\n"
      "v_mfma_f32_16x16x32_bf16 v[100:103], a[28:31], v[220:223], v[100:103]\n"
      "v_mad_i32_i24 v238, v251, s96, v238\n"
      "s_add_u32 s56, s22, 0x2a000\n"
      "s_addc_u32 s57, s23, 0\n"
      "s_add_u32 m0, s18, 0xff00\n"
      "v_mfma_f32_16x16x32_bf16 v[104:107], a[32:35], v[220:223], v[104:107]\n"
      "ds_read_b128 v[216:219], v233 offset:28672\n"
      "global_load_lds_dwordx4 v238, s[56:57] offset:256 \n"
      "v_mfma_f32_16x16x32_bf16 v[108:111], a[36:39], v[220:223], v[108:111]\n"
      "v_mfma_f32_16x16x32_bf16 v[112:115], a[40:43], v[220:223], v[112:115]\n"
      "v_mad_i32_i24 v238, v251, s97, v238\n"
      "s_add_u32 s56, s22, 0x30000\n"
      "s_addc_u32 s57, s23, 0\n"
      "s_add_u32 m0, s18, 0x10f00\n"
      "v_mfma_f32_16x16x32_bf16 v[116:119], a[44:47], v[220:223], v[116:119]\n"
      "global_load_lds_dwordx4 v238, s[56:57] offset:256 \n"
      "s_waitcnt lgkmcnt(6)\n"
      "v_mfma_f32_16x16x32_bf16 v[120:123], a[24:27], v[224:227], v[120:123]\n"
      "v_mfma_f32_16x16x32_bf16 v[124:127], a[28:31], v[224:227], v[124:127]\n"
      "v_mfma_f32_16x16x32_bf16 v[128:131], a[32:35], v[224:227], v[128:131]\n"
      "ds_read_b128 v[220:223], v233 offset:32768\n"
      "v_add_u32 v233, 0x9000, v233\n"
      "s_add_u32 s78, s64, 0x1e000\n"
      "s_addc_u32 s79, s65, 0\n"
      "s_add_u32 m0, s74, 0xc000\n"
      "v_mfma_f32_16x16x32_bf16 v[132:135], a[36:39], v[224:227], v[132:135]\n"
      "global_load_lds_dwordx4 v240, s[78:79]\n"
      "v_mfma_f32_16x16x32_bf16 v[136:139], a[40:43], v[224:227], v[136:139]\n"
      "v_mfma_f32_16x16x32_bf16 v[140:143], a[44:47], v[224:227], v[140:143]\n"
      "s_waitcnt lgkmcnt(2)\n"
      "v_mfma_f32_16x16x32_bf16 v[144:147], a[24:27], v[228:231], v[144:147]\n"
      "s_waitcnt vmcnt(10)\n"
      "s_add_u32 m0, s74, 0xc400\n"
      "v_mfma_f32_16x16x32_bf16 v[148:151], a[28:31], v[228:231], v[148:151]\n"
      "ds_read_b64_tr_b16 a[96:97], v243 offset:24576\n"
      "global_load_lds_dwordx4 v241, s[78:79]\n"
      "v_mfma_f32_16x16x32_bf16 v[152:155], a[32:35], v[228:231], v[152:155]\n"
      "ds_read_b128 v[224:227], v234 offset:0\n"
      "v_mfma_f32_16x16x32_bf16 v[156:159], a[36:39], v[228:231], v[156:159]\n"
      "ds_read_b64_tr_b16 a[98:99], v243 offset:25344\n"
      "v_mfma_f32_16x16x32_bf16 v[160:163], a[40:43], v[228:231], v[160:163]\n"
      "ds_read_b64_tr_b16 a[100:101], v243 offset:24584\n"
      "s_add_u32 m0, s74, 0xc800\n"
      "v_mfma_f32_16x16x32_bf16 v[164:167], a[44:47], v[228:231], v[164:167]\n"
      "global_load_lds_dwordx4 v242, s[78:79]\n"
      "s_waitcnt lgkmcnt(5)\n"
      "v_mfma_f32_16x16x32_bf16 v[168:171], a[24:27], v[216:219], v[168:171]\n"
      "ds_read_b64_tr_b16 a[102:103], v243 offset:25352\n"
      "v_mfma_f32_16x16x32_bf16 v[172:175], a[28:31], v[216:219], v[172:175]\n"
      "ds_read_b64_tr_b16 a[104:105], v243 offset:24640\n"
      "v_mfma_f32_16x16x32_bf16 v[176:179], a[32:35], v[216:219], v[176:179]\n"
      "ds_read_b128 v[228:231], v234 offset:4096\n"
      "s_add_u32 s78, s64, 0x21000\n"
      "s_addc_u32 s79, s65, 0\n"
      "s_add_u32 m0, s74, 0xcc00\n"
      "v_mfma_f32_16x16x32_bf16 v[180:183], a[36:39], v[216:219], v[180:183]\n"
      "ds_read_b64_tr_b16 a[106:107], v243 offset:25408\n"
      "global_load_lds_dwordx4 v240, s[78:79]\n"
      "v_mfma_f32_16x16x32_bf16 v[184:187], a[40:43], v[216:219], v[184:187]\n"
      "ds_read_b64_tr_b16 a[108:109], v243 offset:24648\n"
      "v_mfma_f32_16x16x32_bf16 v[188:191], a[44:47], v[216:219], v[188:191]\n"
      "s_waitcnt lgkmcnt(9)\n"
      "v_mfma_f32_16x16x32_bf16 v[192:195], a[24:27], v[220:223], v[192:195]\n"
      "ds_read_b64_tr_b16 a[110:111], v243 offset:25416\n"
      "s_add_u32 m0, s74, 0xd000\n"
      "v_mfma_f32_16x16x32_bf16 v[196:199], a[28:31], v[220:223], v[196:199]\n"
      "ds_read_b64_tr_b16 a[112:113], v243 offset:24704\n"
      "global_load_lds_dwordx4 v241, s[78:79]\n"
      "v_mfma_f32_16x16x32_bf16 v[200:203], a[32:35], v[220:223], v[200:203]\n"
      "ds_read_b128 v[216:219], v234 offset:8192\n"
      "v_mfma_f32_16x16x32_bf16 v[204:207], a[36:39], v[220:223], v[204:207]\n"
      "ds_read_b64_tr_b16 a[114:115], v243 offset:25472\n"
      "v_mfma_f32_16x16x32_bf16 v[208:211], a[40:43], v[220:223], v[208:211]\n"
      "ds_read_b64_tr_b16 a[116:117], v243 offset:24712\n"
      "s_add_u32 m0, s74, 0xd400\n"
      "v_mfma_f32_16x16x32_bf16 v[212:215], a[44:47], v[220:223], v[212:215]\n"
      "global_load_lds_dwordx4 v242, s[78:79]\n"
      "s_waitcnt lgkmcnt(12)\n"
      "v_mfma_f32_16x16x32_bf16 v[0:3], a[48:51], v[224:227], v[0:3]\n"
      "ds_read_b64_tr_b16 a[118:119], v243 offset:25480\n"
      "v_mfma_f32_16x16x32_bf16 v[4:7], a[52:55], v[224:227], v[4:7]\n"
      "v_mfma_f32_16x16x32_bf16 v[8:11], a[56:59], v[224:227], v[8:11]\n"
      "ds_read_b128 v[220:223], v234 offset:12288\n"
      "s_add_u32 s78, s64, 0x24000\n"
      "s_addc_u32 s79, s65, 0\n"
      "s_add_u32 m0, s74, 0x0\n"
      "v_mfma_f32_16x16x32_bf16 v[12:15], a[60:63], v[224:227], v[12:15]\n"
      "global_load_lds_dwordx4 v240, s[78:79]\n"
      "v_mfma_f32_16x16x32_bf16 v[16:19], a[64:67], v[224:227], v[16:19]\n"
      "v_mfma_f32_16x16x32_bf16 v[20:23], a[68:71], v[224:227], v[20:23]\n"
      "s_waitcnt lgkmcnt(9)\n"
      "v_mfma_f32_16x16x32_bf16 v[24:27], a[48:51], v[228:231], v[24:27]\n"
      "s_add_u32 m0, s74, 0x400\n"
      "v_mfma_f32_16x16x32_bf16 v[28:31], a[52:55], v[228:231], v[28:31]\n"
      "global_load_lds_dwordx4 v241, s[78:79]\n"
      "v_mfma_f32_16x16x32_bf16 v[32:35], a[56:59], v[228:231], v[32:35]\n"
      "ds_read_b128 v[224:227], v234 offset:16384\n"
      "v_mfma_f32_16x16x32_bf16 v[36:39], a[60:63], v[228:231], v[36:39]\n"
      "v_mfma_f32_16x16x32_bf16 v[40:43], a[64:67], v[228:231], v[40:43]\n"
      "s_add_u32 m0, s74, 0x800\n"
      "v_mfma_f32_16x16x32_bf16 v[44:47], a[68:71], v[228:231], v[44:47]\n"
      "global_load_lds_dwordx4 v242, s[78:79]\n"
      "s_waitcnt lgkmcnt(5)\n"
      "v_mfma_f32_16x16x32_bf16 v[48:51], a[48:51], v[216:219], v[48:51]\n"
      "v_mfma_f32_16x16x32_bf16 v[52:55], a[52:55], v[216:219], v[52:55]\n"
      "v_mfma_f32_16x16x32_bf16 v[56:59], a[56:59], v[216:219], v[56:59]\n"
      "ds_read_b128 v[228:231], v234 offset:20480\n"
      "s_add_u32 s78, s64, 0x27000\n"
      "s_addc_u32 s79, s65, 0\n"
      "s_add_u32 m0, s74, 0xc00\n"
      "v_mfma_f32_16x16x32_bf16 v[60:63], a[60:63], v[216:219], v[60:63]\n"
      "global_load_lds_dwordx4 v240, s[78:79]\n"
      "v_mfma_f32_16x16x32_bf16 v[64:67], a[64:67], v[216:219], v[64:67]\n"
      "v_mfma_f32_16x16x32_bf16 v[68:71], a[68:71], v[216:219], v[68:71]\n"
      "s_waitcnt lgkmcnt(2)\n"
      "v_mfma_f32_16x16x32_bf16 v[72:75], a[48:51], v[220:223], v[72:75]\n"
      "s_add_u32 m0, s74, 0x1000\n"
      "v_mfma_f32_16x16x32_bf16 v[76:79], a[52:55], v[220:223], v[76:79]\n"
      "global_load_lds_dwordx4 v241, s[78:79]\n"
      "v_mfma_f32_16x16x32_bf16 v[80:83], a[56:59], v[220:223], v[80:83]\n"
      "ds_read_b128 v[216:219], v234 offset:24576\n"
      "v_mfma_f32_16x16x32_bf16 v[84:87], a[60:63], v[220:223], v[84:87]\n"
      "v_mfma_f32_16x16x32_bf16 v[88:91], a[64:67], v[220:223], v[88:91]\n"
      "s_add_u32 m0, s74, 0x1400\n"
      "v_mfma_f32_16x16x32_bf16 v[92:95], a[68:71], v[220:223], v[92:95]\n"
      "global_load_lds_dwordx4 v242, s[78:79]\n"
      "s_waitcnt lgkmcnt(2)\n"
      "v_mfma_f32_16x16x32_bf16 v[96:99], a[48:51], v[224:227], v[96:99]\n"
      "v_mfma_f32_16x16x32_bf16 v[100:103], a[52:55], v[224:227], v[100:103]\n"
      "v_mfma_f32_16x16x32_bf16 v[104:107], a[56:59], v[224:227], v[104:107]\n"
      "ds_read_b128 v[220:223], v234 offset:28672\n"
      "s_add_u32 s78, s64, 0x2a000\n"
      "s_addc_u32 s79, s65, 0\n"
      "s_add_u32 m0, s74, 0x6000\n"
      "v_mfma_f32_16x16x32_bf16 v[108:111], a[60:63], v[224:227], v[108:111]\n"
      "global_load_lds_dwordx4 v240, s[78:79]\n"
      "v_mfma_f32_16x16x32_bf16 v[112:115], a[64:67], v[224:227], v[112:115]\n"
      "v_mfma_f32_16x16x32_bf16 v[116:119], a[68:71], v[224:227], v[116:119]\n"
      "s_waitcnt lgkmcnt(2)\n"
      "v_mfma_f32_16x16x32_bf16 v[120:123], a[48:51], v[228:231], v[120:123]\n"
      "s_add_u32 m0, s74, 0x6400\n"
      "v_mfma_f32_16x16x32_bf16 v[124:127], a[52:55], v[228:231], v[124:127]\n"
      "global_load_lds_dwordx4 v241, s[78:79]\n"
      "v_mfma_f32_16x16x32_bf16 v[128:131], a[56:59], v[228:231], v[128:131]\n"
      "ds_read_b128 v[224:227], v234 offset:32768\n"
      "v_add_u32 v234, 0x9000, v234\n"
      "v_mfma_f32_16x16x32_bf16 v[132:135], a[60:63], v[228:231], v[132:135]\n"
      "v_mfma_f32_16x16x32_bf16 v[136:139], a[64:67], v[228:231], v[136:139]\n"
      "s_waitcnt vmcnt(8)\n"
      "s_add_u32 m0, s74, 0x6800\n"
      "v_mfma_f32_16x16x32_bf16 v[140:143], a[68:71], v[228:231], v[140:143]\n"
      "global_load_lds_dwordx4 v242, s[78:79]\n"
      "s_waitcnt lgkmcnt(2)\n"
      "v_mfma_f32_16x16x32_bf16 v[144:147], a[48:51], v[216:219], v[144:147]\n"
      "ds_read_b64_tr_b16 a[120:121], v243 offset:49152\n"
      "v_mfma_f32_16x16x32_bf16 v[148:151], a[52:55], v[216:219], v[148:151]\n"
      "ds_read_b64_tr_b16 a[122:123], v243 offset:49920\n"
      "v_mfma_f32_16x16x32_bf16 v[152:155], a[56:59], v[216:219], v[152:155]\n"
      "ds_read_b128 v[228:231], v235 offset:0\n"
      "s_add_u32 s78, s64, 0x2d000\n"
      "s_addc_u32 s79, s65, 0\n"
      "s_add_u32 m0, s74, 0x6c00\n"
      "v_mfma_f32_16x16x32_bf16 v[156:159], a[60:63], v[216:219], v[156:159]\n"
      "ds_read_b64_tr_b16 a[124:125], v243 offset:49160\n"
      "global_load_lds_dwordx4 v240, s[78:79]\n"
      "v_mfma_f32_16x16x32_bf16 v[160:163], a[64:67], v[216:219], v[160:163]\n"
      "ds_read_b64_tr_b16 a[126:127], v243 offset:49928\n"
      "v_mfma_f32_16x16x32_bf16 v[164:167], a[68:71], v[216:219], v[164:167]\n"
      "s_waitcnt lgkmcnt(6)\n"
      "v_mfma_f32_16x16x32_bf16 v[168:171], a[48:51], v[220:223], v[168:171]\n"
      "ds_read_b64_tr_b16 a[128:129], v243 offset:49216\n"
      "s_add_u32 m0, s74, 0x7000\n"
      "v_mfma_f32_16x16x32_bf16 v[172:175], a[52:55], v[220:223], v[172:175]\n"
      "ds_read_b64_tr_b16 a[130:131], v243 offset:49984\n"
      "global_load_lds_dwordx4 v241, s[78:79]\n"
      "v_mfma_f32_16x16x32_bf16 v[176:179], a[56:59], v[220:223], v[176:179]\n"
      "ds_read_b128 v[216:219], v235 offset:4096\n"
      "v_mfma_f32_16x16x32_bf16 v[180:183], a[60:63], v[220:223], v[180:183]\n"
      "ds_read_b64_tr_b16 a[132:133], v243 offset:49224\n"
      "v_mfma_f32_16x16x32_bf16 v[184:187], a[64:67], v[220:223], v[184:187]\n"
      "ds_read_b64_tr_b16 a[134:135], v243 offset:49992\n"
      "s_add_u32 m0, s74, 0x7400\n"
      "v_mfma_f32_16x16x32_bf16 v[188:191], a[68:71], v[220:223], v[188:191]\n"
      "global_load_lds_dwordx4 v242, s[78:79]\n"
      "s_waitcnt lgkmcnt(10)\n"
      "v_mfma_f32_16x16x32_bf16 v[192:195], a[48:51], v[224:227], v[192:195]\n"
      "ds_read_b64_tr_b16 a[136:137], v243 offset:49280\n"
      "v_mfma_f32_16x16x32_bf16 v[196:199], a[52:55], v[224:227], v[196:199]\n"
      "ds_read_b64_tr_b16 a[138:139], v243 offset:50048\n"
      "v_mfma_f32_16x16x32_bf16 v[200:203], a[56:59], v[224:227], v[200:203]\n"
      "ds_read_b128 v[220:223], v235 offset:8192\n"
      "global_load_dwordx4 v[244:247], v248, s[28:29] sc1\n"
      "v_mfma_f32_16x16x32_bf16 v[204:207], a[60:63], v[224:227], v[204:207]\n"
      "ds_read_b64_tr_b16 a[140:141], v243 offset:49288\n"
      "v_mfma_f32_16x16x32_bf16 v[208:211], a[64:67], v[224:227], v[208:211]\n"
      "ds_read_b64_tr_b16 a[142:143], v243 offset:50056\n"
      "v_mfma_f32_16x16x32_bf16 v[212:215], a[68:71], v[224:227], v[212:215]\n"
      "s_waitcnt lgkmcnt(12)\n"
      "v_mfma_f32_16x16x32_bf16 v[0:3], a[72:75], v[228:231], v[0:3]\n"
      "v_mfma_f32_16x16x32_bf16 v[4:7], a[76:79], v[228:231], v[4:7]\n"
      "v_mfma_f32_16x16x32_bf16 v[8:11], a[80:83], v[228:231], v[8:11]\n"
      "ds_read_b128 v[224:227], v235 offset:12288\n"
      "v_mfma_f32_16x16x32_bf16 v[12:15], a[84:87], v[228:231], v[12:15]\n"
      "v_mfma_f32_16x16x32_bf16 v[16:19], a[88:91], v[228:231], v[16:19]\n"
      "s_waitcnt vmcnt(7)\n"
      "v_mfma_f32_16x16x32_bf16 v[20:23], a[92:95], v[228:231], v[20:23]\n"
      "s_waitcnt lgkmcnt(8)\n"
      "v_mfma_f32_16x16x32_bf16 v[24:27], a[72:75], v[216:219], v[24:27]\n"
      "ds_read_b64_tr_b16 a[144:145], v243 offset:0\n"
      "v_mfma_f32_16x16x32_bf16 v[28:31], a[76:79], v[216:219], v[28:31]\n"
      "ds_read_b64_tr_b16 a[146:147], v243 offset:768\n"
      "v_mfma_f32_16x16x32_bf16 v[32:35], a[80:83], v[216:219], v[32:35]\n"
      "ds_read_b128 v[228:231], v235 offset:16384\n"
      "s_waitcnt lgkmcnt(4)\n"
      "s_add_u32 s78, s64, 0x30000\n"
      "s_addc_u32 s79, s65, 0\n"
      "s_add_u32 m0, s74, 0xc000\n"
      "v_mfma_f32_16x16x32_bf16 v[36:39], a[84:87], v[216:219], v[36:39]\n"
      "ds_read_b64_tr_b16 a[148:149], v243 offset:8\n"
      "global_load_lds_dwordx4 v240, s[78:79]\n"
      "v_mfma_f32_16x16x32_bf16 v[40:43], a[88:91], v[216:219], v[40:43]\n"
      "ds_read_b64_tr_b16 a[150:151], v243 offset:776\n"
      "v_mfma_f32_16x16x32_bf16 v[44:47], a[92:95], v[216:219], v[44:47]\n"
      "v_mfma_f32_16x16x32_bf16 v[48:51], a[72:75], v[220:223], v[48:51]\n"
      "ds_read_b64_tr_b16 a[152:153], v243 offset:64\n"
      "s_add_u32 m0, s74, 0xc400\n"
      "v_mfma_f32_16x16x32_bf16 v[52:55], a[76:79], v[220:223], v[52:55]\n"
      "ds_read_b64_tr_b16 a[154:155], v243 offset:832\n"
      "global_load_lds_dwordx4 v241, s[78:79]\n"
      "v_mfma_f32_16x16x32_bf16 v[56:59], a[80:83], v[220:223], v[56:59]\n"
      "ds_read_b128 v[216:219], v235 offset:20480\n"
      "v_mfma_f32_16x16x32_bf16 v[60:63], a[84:87], v[220:223], v[60:63]\n"
      "ds_read_b64_tr_b16 a[156:157], v243 offset:72\n"
      "v_mfma_f32_16x16x32_bf16 v[64:67], a[88:91], v[220:223], v[64:67]\n"
      "ds_read_b64_tr_b16 a[158:159], v243 offset:840\n"
      "s_add_u32 m0, s74, 0xc800\n"
      "v_mfma_f32_16x16x32_bf16 v[68:71], a[92:95], v[220:223], v[68:71]\n"
      "global_load_lds_dwordx4 v242, s[78:79]\n"
      "s_waitcnt lgkmcnt(10)\n"
      "v_mfma_f32_16x16x32_bf16 v[72:75], a[72:75], v[224:227], v[72:75]\n"
      "ds_read_b64_tr_b16 a[160:161], v243 offset:128\n"
      "v_mfma_f32_16x16x32_bf16 v[76:79], a[76:79], v[224:227], v[76:79]\n"
      "ds_read_b64_tr_b16 a[162:163], v243 offset:896\n"
      "v_mfma_f32_16x16x32_bf16 v[80:83], a[80:83], v[224:227], v[80:83]\n"
      "ds_read_b128 v[220:223], v235 offset:24576\n"
      "s_add_u32 s78, s64, 0x33000\n"
      "s_addc_u32 s79, s65, 0\n"
      "s_add_u32 m0, s74, 0xcc00\n"
      "v_mfma_f32_16x16x32_bf16 v[84:87], a[84:87], v[224:227], v[84:87]\n"
      "ds_read_b64_tr_b16 a[164:165], v243 offset:136\n"
      "global_load_lds_dwordx4 v240, s[78:79]\n"
      "v_mfma_f32_16x16x32_bf16 v[88:91], a[88:91], v[224:227], v[88:91]\n"
      "ds_read_b64_tr_b16 a[166:167], v243 offset:904\n"
      "v_mfma_f32_16x16x32_bf16 v[92:95], a[92:95], v[224:227], v[92:95]\n"
      "s_waitcnt lgkmcnt(12)\n"
      "v_mfma_f32_16x16x32_bf16 v[96:99], a[72:75], v[228:231], v[96:99]\n"
      "s_add_u32 m0, s74, 0xd000\n"
      "v_mfma_f32_16x16x32_bf16 v[100:103], a[76:79], v[228:231], v[100:103]\n"
      "global_load_lds_dwordx4 v241, s[78:79]\n"
      "v_mfma_f32_16x16x32_bf16 v[104:107], a[80:83], v[228:231], v[104:107]\n"
      "ds_read_b128 v[224:227], v235 offset:28672\n"
      "v_mfma_f32_16x16x32_bf16 v[108:111], a[84:87], v[228:231], v[108:111]\n"
      "v_mfma_f32_16x16x32_bf16 v[112:115], a[88:91], v[228:231], v[112:115]\n"
      "s_waitcnt vmcnt(6)\n"
      "s_add_u32 m0, s74, 0xd400\n"
      "v_mfma_f32_16x16x32_bf16 v[116:119], a[92:95], v[228:231], v[116:119]\n"
      "global_load_lds_dwordx4 v242, s[78:79]\n"
      "s_waitcnt lgkmcnt(8)\n"
      "v_mfma_f32_16x16x32_bf16 v[120:123], a[72:75], v[216:219], v[120:123]\n"
      "ds_read_b64_tr_b16 a[168:169], v243 offset:24576\n"
      "v_mfma_f32_16x16x32_bf16 v[124:127], a[76:79], v[216:219], v[124:127]\n"
      "ds_read_b64_tr_b16 a[170:171], v243 offset:25344\n"
      "v_mfma_f32_16x16x32_bf16 v[128:131], a[80:83], v[216:219], v[128:131]\n"
      "ds_read_b128 v[228:231], v235 offset:32768\n"
      "v_add_u32 v235, 0x9000, v235\n"
      "s_waitcnt lgkmcnt(4)\n"
      "s_add_u32 s78, s64, 0x36000\n"
      "s_addc_u32 s79, s65, 0\n"
      "s_add_u32 m0, s74, 0x0\n"
      "v_mfma_f32_16x16x32_bf16 v[132:135], a[84:87], v[216:219], v[132:135]\n"
      "ds_read_b64_tr_b16 a[172:173], v243 offset:24584\n"
      "global_load_lds_dwordx4 v240, s[78:79]\n"
      "v_mfma_f32_16x16x32_bf16 v[136:139], a[88:91], v[216:219], v[136:139]\n"
      "ds_read_b64_tr_b16 a[174:175], v243 offset:25352\n"
      "v_mfma_f32_16x16x32_bf16 v[140:143], a[92:95], v[216:219], v[140:143]\n"
      "v_mfma_f32_16x16x32_bf16 v[144:147], a[72:75], v[220:223], v[144:147]\n"
      "ds_read_b64_tr_b16 a[176:177], v243 offset:24640\n"
      "s_waitcnt vmcnt(7)\n"
      "v_cmp_ne_u64 vcc, s[80:81], v[244:245]\n"
      "v_cmp_ne_u64 s[56:57], s[80:81], v[246:247]\n"
      "s_nop 3\n"
      "s_or_b64 vcc, vcc, s[56:57]\n"
      "s_cbranch_scc0 LP1_0\n"
      "s_mov_b32 s82, 0x40000\n"
      "LP0_0:\n"
      "s_sleep 2\n"
      "global_load_dwordx4 v[244:247], v248, s[28:29] sc1\n"
      "s_sub_u32 s82, s82, 1\n"
      "s_cbranch_scc1 LP2_0\n"
      "s_waitcnt vmcnt(0)\n"
      "v_cmp_ne_u64 vcc, s[80:81], v[244:245]\n"
      "v_cmp_ne_u64 s[56:57], s[80:81], v[246:247]\n"
      "s_nop 3\n"
      "s_or_b64 vcc, vcc, s[56:57]\n"
      "s_cbranch_scc1 LP0_0\n"
      "LP2_0:\n"
      "s_waitcnt vmcnt(0)\n"
      "LP1_0:\n"
      "s_nop 0\n"
      "v_mfma_f32_16x16x32_bf16 v[148:151], a[76:79], v[220:223], v[148:151]\n"
      "s_barrier\n"
      "s_add_u32 m0, s74, 0x400\n"
      "v_mfma_f32_16x16x32_bf16 v[152:155], a[80:83], v[220:223], v[152:155]\n"
      "ds_read_b128 v[216:219], v232 offset:0\n"
      "global_load_lds_dwordx4 v241, s[78:79]\n"
      "v_mfma_f32_16x16x32_bf16 v[156:159], a[84:87], v[220:223], v[156:159]\n"
      "ds_read_b64_tr_b16 a[178:179], v243 offset:25408\n"
      "v_mfma_f32_16x16x32_bf16 v[160:163], a[88:91], v[220:223], v[160:163]\n"
      "ds_read_b64_tr_b16 a[180:181], v243 offset:24648\n"
      "v_mfma_f32_16x16x32_bf16 v[164:167], a[92:95], v[220:223], v[164:167]\n"
      "s_add_u32 m0, s74, 0x800\n"
      "s_waitcnt lgkmcnt(9)\n"
      "v_mfma_f32_16x16x32_bf16 v[168:171], a[72:75], v[224:227], v[168:171]\n"
      "ds_read_b64_tr_b16 a[182:183], v243 offset:25416\n"
      "global_load_lds_dwordx4 v242, s[78:79]\n"
      "v_mfma_f32_16x16x32_bf16 v[172:175], a[76:79], v[224:227], v[172:175]\n"
      "ds_read_b64_tr_b16 a[184:185], v243 offset:24704\n"
      "v_mfma_f32_16x16x32_bf16 v[176:179], a[80:83], v[224:227], v[176:179]\n"
      "ds_read_b128 v[220:223], v232 offset:4096\n"
      "v_mfma_f32_16x16x32_bf16 v[180:183], a[84:87], v[224:227], v[180:183]\n"
      "ds_read_b64_tr_b16 a[186:187], v243 offset:25472\n"
      "s_add_u32 s78, s64, 0x39000\n"
      "s_addc_u32 s79, s65, 0\n"
      "s_add_u32 m0, s74, 0xc00\n"
      "v_mfma_f32_16x16x32_bf16 v[184:187], a[88:91], v[224:227], v[184:187]\n"
      "ds_read_b64_tr_b16 a[188:189], v243 offset:24712\n"
      "global_load_lds_dwordx4 v240, s[78:79]\n"
      "v_mfma_f32_16x16x32_bf16 v[188:191], a[92:95], v[224:227], v[188:191]\n"
      "s_waitcnt lgkmcnt(11)\n"
      "v_mfma_f32_16x16x32_bf16 v[192:195], a[72:75], v[228:231], v[192:195]\n"
      "ds_read_b64_tr_b16 a[190:191], v243 offset:25480\n"
      "v_mfma_f32_16x16x32_bf16 v[196:199], a[76:79], v[228:231], v[196:199]\n"
      "s_add_u32 m0, s74, 0x1000\n"
      "v_mfma_f32_16x16x32_bf16 v[200:203], a[80:83], v[228:231], v[200:203]\n"
      "ds_read_b128 v[224:227], v232 offset:8192\n"
      "global_load_lds_dwordx4 v241, s[78:79]\n"
      "v_mfma_f32_16x16x32_bf16 v[204:207], a[84:87], v[228:231], v[204:207]\n"
      "v_mfma_f32_16x16x32_bf16 v[208:211], a[88:91], v[228:231], v[208:211]\n"
      "v_mfma_f32_16x16x32_bf16 v[212:215], a[92:95], v[228:231], v[212:215]\n"
      "s_add_u32 m0, s74, 0x1400\n"
      "s_waitcnt lgkmcnt(9)\n"
      "v_mfma_f32_16x16x32_bf16 v[0:3], a[96:99], v[216:219], v[0:3]\n"
      "global_load_lds_dwordx4 v242, s[78:79]\n"
      "v_mfma_f32_16x16x32_bf16 v[4:7], a[100:103], v[216:219], v[4:7]\n"
      "s_barrier\n"
      "v_mfma_f32_16x16x32_bf16 v[8:11], a[104:107], v[216:219], v[8:11]\n"
      "ds_read_b128 v[228:231], v232 offset:12288\n"
      "v_mfma_f32_16x16x32_bf16 v[12:15], a[108:111], v[216:219], v[12:15]\n"
      "s_add_u32 s58, s24, 0x0\n"
      "s_addc_u32 s59, s25, 0\n"
      "global_load_dwordx4 a[240:243], v239, s[58:59] offset:0 sc1\n"
      "v_mfma_f32_16x16x32_bf16 v[16:19], a[112:115], v[216:219], v[16:19]\n"
      "v_mfma_f32_16x16x32_bf16 v[20:23], a[116:119], v[216:219], v[20:23]\n"
      "s_waitcnt lgkmcnt(5)\n"
      "v_mfma_f32_16x16x32_bf16 v[24:27], a[96:99], v[220:223], v[24:27]\n"
      "v_mfma_f32_16x16x32_bf16 v[28:31], a[100:103], v[220:223], v[28:31]\n"
      "s_waitcnt lgkmcnt(2)\n"
      "s_add_u32 s78, s64, 0x3c000\n"
      "s_addc_u32 s79, s65, 0\n"
      "s_add_u32 m0, s74, 0x6000\n"
      "v_mfma_f32_16x16x32_bf16 v[32:35], a[104:107], v[220:223], v[32:35]\n"
      "ds_read_b128 v[216:219], v232 offset:16384\n"
      "LPD1:\n"
      "s_cbranch_execz LPD8\n"
      "global_load_lds_dwordx4 v240, s[78:79]\n"
      "v_mfma_f32_16x16x32_bf16 v[36:39], a[108:111], v[220:223], v[36:39]\n"
      "v_mfma_f32_16x16x32_bf16 v[40:43], a[112:115], v[220:223], v[40:43]\n"
      "s_waitcnt vmcnt(8)\n"
      "v_mfma_f32_16x16x32_bf16 v[44:47], a[116:119], v[220:223], v[44:47]\n"
      "s_add_u32 m0, s74, 0x6400\n"
      "s_waitcnt lgkmcnt(2)\n"
      "v_mfma_f32_16x16x32_bf16 v[48:51], a[96:99], v[224:227], v[48:51]\n"
      "ds_read_b64_tr_b16 a[192:193], v243 offset:49152\n"
      "global_load_lds_dwordx4 v241, s[78:79]\n"
      "v_mfma_f32_16x16x32_bf16 v[52:55], a[100:103], v[224:227], v[52:55]\n"
      "ds_read_b64_tr_b16 a[194:195], v243 offset:49920\n"
      "v_mfma_f32_16x16x32_bf16 v[56:59], a[104:107], v[224:227], v[56:59]\n"
      "ds_read_b128 v[220:223], v232 offset:20480\n"
      "v_mfma_f32_16x16x32_bf16 v[60:63], a[108:111], v[224:227], v[60:63]\n"
      "ds_read_b64_tr_b16 a[196:197], v243 offset:49160\n"
      "s_add_u32 m0, s74, 0x6800\n"
      "v_mfma_f32_16x16x32_bf16 v[64:67], a[112:115], v[224:227], v[64:67]\n"
      "ds_read_b64_tr_b16 a[198:199], v243 offset:49928\n"
      "global_load_lds_dwordx4 v242, s[78:79]\n"
      "v_mfma_f32_16x16x32_bf16 v[68:71], a[116:119], v[224:227], v[68:71]\n"
      "s_waitcnt lgkmcnt(6)\n"
      "v_mfma_f32_16x16x32_bf16 v[72:75], a[96:99], v[228:231], v[72:75]\n"
      "ds_read_b64_tr_b16 a[200:201], v243 offset:49216\n"
      "v_mfma_f32_16x16x32_bf16 v[76:79], a[100:103], v[228:231], v[76:79]\n"
      "ds_read_b64_tr_b16 a[202:203], v243 offset:49984\n"
      "s_add_u32 s78, s64, 0x3f000\n"
      "s_addc_u32 s79, s65, 0\n"
      "s_add_u32 m0, s74, 0x6c00\n"
      "v_mfma_f32_16x16x32_bf16 v[80:83], a[104:107], v[228:231], v[80:83]\n"
      "ds_read_b128 v[224:227], v232 offset:24576\n"
      "global_load_lds_dwordx4 v240, s[78:79]\n"
      "v_mfma_f32_16x16x32_bf16 v[84:87], a[108:111], v[228:231], v[84:87]\n"
      "ds_read_b64_tr_b16 a[204:205], v243 offset:49224\n"
      "v_mfma_f32_16x16x32_bf16 v[88:91], a[112:115], v[228:231], v[88:91]\n"
      "ds_read_b64_tr_b16 a[206:207], v243 offset:49992\n"
      "v_mfma_f32_16x16x32_bf16 v[92:95], a[116:119], v[228:231], v[92:95]\n"
      "s_add_u32 m0, s74, 0x7000\n"
      "s_waitcnt lgkmcnt(10)\n"
      "v_mfma_f32_16x16x32_bf16 v[96:99], a[96:99], v[216:219], v[96:99]\n"
      "ds_read_b64_tr_b16 a[208:209], v243 offset:49280\n"
      "global_load_lds_dwordx4 v241, s[78:79]\n"
      "v_mfma_f32_16x16x32_bf16 v[100:103], a[100:103], v[216:219], v[100:103]\n"
      "ds_read_b64_tr_b16 a[210:211], v243 offset:50048\n"
      "v_mfma_f32_16x16x32_bf16 v[104:107], a[104:107], v[216:219], v[104:107]\n"
      "ds_read_b128 v[228:231], v232 offset:28672\n"
      "v_mfma_f32_16x16x32_bf16 v[108:111], a[108:111], v[216:219], v[108:111]\n"
      "ds_read_b64_tr_b16 a[212:213], v243 offset:49288\n"
      "s_add_u32 m0, s74, 0x7400\n"
      "v_mfma_f32_16x16x32_bf16 v[112:115], a[112:115], v[216:219], v[112:115]\n"
      "ds_read_b64_tr_b16 a[214:215], v243 offset:50056\n"
      "global_load_lds_dwordx4 v242, s[78:79]\n"
      "v_mfma_f32_16x16x32_bf16 v[116:119], a[116:119], v[216:219], v[116:119]\n"
      "s_waitcnt lgkmcnt(12)\n"
      "v_mfma_f32_16x16x32_bf16 v[120:123], a[96:99], v[220:223], v[120:123]\n"
      "v_mfma_f32_16x16x32_bf16 v[124:127], a[100:103], v[220:223], v[124:127]\n"
      "global_load_dwordx4 a[244:247], v239, s[58:59] offset:1024 sc1\n"
      "v_mfma_f32_16x16x32_bf16 v[128:131], a[104:107], v[220:223], v[128:131]\n"
      "ds_read_b128 v[216:219], v232 offset:32768\n"
      "v_add_u32 v232, 0xffff7000, v232\n"
      "v_mfma_f32_16x16x32_bf16 v[132:135], a[108:111], v[220:223], v[132:135]\n"
      "v_mfma_f32_16x16x32_bf16 v[136:139], a[112:115], v[220:223], v[136:139]\n"
      "v_mfma_f32_16x16x32_bf16 v[140:143], a[116:119], v[220:223], v[140:143]\n"
      "global_load_dwordx4 a[248:251], v239, s[58:59] offset:2048 sc1\n"
      "s_waitcnt lgkmcnt(8)\n"
      "v_mfma_f32_16x16x32_bf16 v[144:147], a[96:99], v[224:227], v[144:147]\n"
      "s_waitcnt vmcnt(9)\n"
      "v_mfma_f32_16x16x32_bf16 v[148:151], a[100:103], v[224:227], v[148:151]\n"
      "ds_read_b64_tr_b16 a[216:217], v243 offset:0\n"
      "v_mfma_f32_16x16x32_bf16 v[152:155], a[104:107], v[224:227], v[152:155]\n"
      "ds_read_b128 v[220:223], v233 offset:0\n"
      "v_mfma_f32_16x16x32_bf16 v[156:159], a[108:111], v[224:227], v[156:159]\n"
      "ds_read_b64_tr_b16 a[218:219], v243 offset:768\n"
      "s_waitcnt lgkmcnt(4)\n"
      "s_add_u32 s78, s64, 0x42000\n"
      "s_addc_u32 s79, s65, 0\n"
      "s_add_u32 m0, s74, 0xc000\n"
      "v_mfma_f32_16x16x32_bf16 v[160:163], a[112:115], v[224:227], v[160:163]\n"
      "ds_read_b64_tr_b16 a[220:221], v243 offset:8\n"
      "global_load_lds_dwordx4 v240, s[78:79]\n"
      "v_mfma_f32_16x16x32_bf16 v[164:167], a[116:119], v[224:227], v[164:167]\n"
      "v_mfma_f32_16x16x32_bf16 v[168:171], a[96:99], v[228:231], v[168:171]\n"
      "ds_read_b64_tr_b16 a[222:223], v243 offset:776\n"
      "v_mfma_f32_16x16x32_bf16 v[172:175], a[100:103], v[228:231], v[172:175]\n"
      "ds_read_b64_tr_b16 a[224:225], v243 offset:64\n"
      "s_add_u32 m0, s74, 0xc400\n"
      "v_mfma_f32_16x16x32_bf16 v[176:179], a[104:107], v[228:231], v[176:179]\n"
      "ds_read_b128 v[224:227], v233 offset:4096\n"
      "global_load_lds_dwordx4 v241, s[78:79]\n"
      "v_mfma_f32_16x16x32_bf16 v[180:183], a[108:111], v[228:231], v[180:183]\n"
      "ds_read_b64_tr_b16 a[226:227], v243 offset:832\n"
      "v_mfma_f32_16x16x32_bf16 v[184:187], a[112:115], v[228:231], v[184:187]\n"
      "ds_read_b64_tr_b16 a[228:229], v243 offset:72\n"
      "v_mfma_f32_16x16x32_bf16 v[188:191], a[116:119], v[228:231], v[188:191]\n"
      "s_add_u32 m0, s74, 0xc800\n"
      "s_waitcnt lgkmcnt(9)\n"
      "v_mfma_f32_16x16x32_bf16 v[192:195], a[96:99], v[216:219], v[192:195]\n"
      "ds_read_b64_tr_b16 a[230:231], v243 offset:840\n"
      "global_load_lds_dwordx4 v242, s[78:79]\n"
      "v_mfma_f32_16x16x32_bf16 v[196:199], a[100:103], v[216:219], v[196:199]\n"
      "ds_read_b64_tr_b16 a[232:233], v243 offset:128\n"
      "v_mfma_f32_16x16x32_bf16 v[200:203], a[104:107], v[216:219], v[200:203]\n"
      "ds_read_b128 v[228:231], v233 offset:8192\n"
      "v_mfma_f32_16x16x32_bf16 v[204:207], a[108:111], v[216:219], v[204:207]\n"
      "ds_read_b64_tr_b16 a[234:235], v243 offset:896\n"
      "s_add_u32 s78, s64, 0x45000\n"
      "s_addc_u32 s79, s65, 0\n"
      "s_add_u32 m0, s74, 0xcc00\n"
      "v_mfma_f32_16x16x32_bf16 v[208:211], a[112:115], v[216:219], v[208:211]\n"
      "ds_read_b64_tr_b16 a[236:237], v243 offset:136\n"
      "global_load_lds_dwordx4 v240, s[78:79]\n"
      "v_mfma_f32_16x16x32_bf16 v[212:215], a[116:119], v[216:219], v[212:215]\n"
      "s_waitcnt lgkmcnt(12)\n"
      "v_mfma_f32_16x16x32_bf16 v[0:3], a[120:123], v[220:223], v[0:3]\n"
      "ds_read_b64_tr_b16 a[238:239], v243 offset:904\n"
      "v_mfma_f32_16x16x32_bf16 v[4:7], a[124:127], v[220:223], v[4:7]\n"
      "s_add_u32 m0, s74, 0xd000\n"
      "v_mfma_f32_16x16x32_bf16 v[8:11], a[128:131], v[220:223], v[8:11]\n"
      "ds_read_b128 v[216:219], v233 offset:12288\n"
      "global_load_lds_dwordx4 v241, s[78:79]\n"
      "v_mfma_f32_16x16x32_bf16 v[12:15], a[132:135], v[220:223], v[12:15]\n"
      "v_mfma_f32_16x16x32_bf16 v[16:19], a[136:139], v[220:223], v[16:19]\n"
      "v_mfma_f32_16x16x32_bf16 v[20:23], a[140:143], v[220:223], v[20:23]\n"
      "s_add_u32 m0, s74, 0xd400\n"
      "s_waitcnt lgkmcnt(9)\n"
      "v_mfma_f32_16x16x32_bf16 v[24:27], a[120:123], v[224:227], v[24:27]\n"
      "global_load_lds_dwordx4 v242, s[78:79]\n"
      "v_mfma_f32_16x16x32_bf16 v[28:31], a[124:127], v[224:227], v[28:31]\n"
      "v_mfma_f32_16x16x32_bf16 v[32:35], a[128:131], v[224:227], v[32:35]\n"
      "ds_read_b128 v[220:223], v233 offset:16384\n"
      "v_mfma_f32_16x16x32_bf16 v[36:39], a[132:135], v[224:227], v[36:39]\n"
      "s_waitcnt vmcnt(8)\n"
      "v_mad_i32_i24 v238, v251, s89, v238\n"
      "s_add_u32 s56, s22, 0x0\n"
      "s_addc_u32 s57, s23, 0\n"
      "s_add_u32 m0, s18, 0xfffffe00\n"
      "v_mfma_f32_16x16x32_bf16 v[40:43], a[136:139], v[224:227], v[40:43]\n"
      "ds_read_b64_tr_b16 a[0:1], v243 offset:24576\n"
      "global_load_lds_dwordx4 v238, s[56:57] offset:512 \n"
      "v_mfma_f32_16x16x32_bf16 v[44:47], a[140:143], v[224:227], v[44:47]\n"
      "s_waitcnt lgkmcnt(6)\n"
      "v_mfma_f32_16x16x32_bf16 v[48:51], a[120:123], v[228:231], v[48:51]\n"
      "ds_read_b64_tr_b16 a[2:3], v243 offset:25344\n"
      "v_mad_i32_i24 v238, v251, s90, v238\n"
      "s_add_u32 s56, s22, 0x6000\n"
      "s_addc_u32 s57, s23, 0\n"
      "s_add_u32 m0, s18, 0xe00\n"
      "v_mfma_f32_16x16x32_bf16 v[52:55], a[124:127], v[228:231], v[52:55]\n"
      "ds_read_b64_tr_b16 a[4:5], v243 offset:24584\n"
      "global_load_lds_dwordx4 v238, s[56:57] offset:512 \n"
      "v_mfma_f32_16x16x32_bf16 v[56:59], a[128:131], v[228:231], v[56:59]\n"
      "ds_read_b128 v[224:227], v233 offset:20480\n"
      "v_mfma_f32_16x16x32_bf16 v[60:63], a[132:135], v[228:231], v[60:63]\n"
      "ds_read_b64_tr_b16 a[6:7], v243 offset:25352\n"
      "v_mad_i32_i24 v238, v251, s91, v238\n"
      "s_add_u32 s56, s22, 0xc000\n"
      "s_addc_u32 s57, s23, 0\n"
      "s_add_u32 m0, s18, 0x1e00\n"
      "v_mfma_f32_16x16x32_bf16 v[64:67], a[136:139], v[228:231], v[64:67]\n"
      "ds_read_b64_tr_b16 a[8:9], v243 offset:24640\n"
      "global_load_lds_dwordx4 v238, s[56:57] offset:512 \n"
      "v_mfma_f32_16x16x32_bf16 v[68:71], a[140:143], v[228:231], v[68:71]\n"
      "s_waitcnt lgkmcnt(7)\n"
      "v_mfma_f32_16x16x32_bf16 v[72:75], a[120:123], v[216:219], v[72:75]\n"
      "ds_read_b64_tr_b16 a[10:11], v243 offset:25408\n"
      "v_mad_i32_i24 v238, v251, s92, v238\n"
      "s_add_u32 s56, s22, 0x12000\n"
      "s_addc_u32 s57, s23, 0\n"
      "s_add_u32 m0, s18, 0x2e00\n"
      "v_mfma_f32_16x16x32_bf16 v[76:79], a[124:127], v[216:219], v[76:79]\n"
      "ds_read_b64_tr_b16 a[12:13], v243 offset:24648\n"
      "global_load_lds_dwordx4 v238, s[56:57] offset:512 \n"
      "v_mfma_f32_16x16x32_bf16 v[80:83], a[128:131], v[216:219], v[80:83]\n"
      "ds_read_b128 v[228:231], v233 offset:24576\n"
      "v_mfma_f32_16x16x32_bf16 v[84:87], a[132:135], v[216:219], v[84:87]\n"
      "ds_read_b64_tr_b16 a[14:15], v243 offset:25416\n"
      "v_mad_i32_i24 v238, v251, s93, v238\n"
      "s_add_u32 s56, s22, 0x18000\n"
      "s_addc_u32 s57, s23, 0\n"
      "s_add_u32 m0, s18, 0x3e00\n"
      "v_mfma_f32_16x16x32_bf16 v[88:91], a[136:139], v[216:219], v[88:91]\n"
      "ds_read_b64_tr_b16 a[16:17], v243 offset:24704\n"
      "global_load_lds_dwordx4 v238, s[56:57] offset:512 \n"
      "v_mfma_f32_16x16x32_bf16 v[92:95], a[140:143], v[216:219], v[92:95]\n"
      "s_waitcnt lgkmcnt(11)\n"
      "v_mfma_f32_16x16x32_bf16 v[96:99], a[120:123], v[220:223], v[96:99]\n"
      "ds_read_b64_tr_b16 a[18:19], v243 offset:25472\n"
      "v_mad_i32_i24 v238, v251, s94, v238\n"
      "s_add_u32 s56, s22, 0x1e000\n"
      "s_addc_u32 s57, s23, 0\n"
      "s_add_u32 m0, s18, 0x4e00\n"
      "v_mfma_f32_16x16x32_bf16 v[100:103], a[124:127], v[220:223], v[100:103]\n"
      "ds_read_b64_tr_b16 a[20:21], v243 offset:24712\n"
      "global_load_lds_dwordx4 v238, s[56:57] offset:512 \n"
      "v_mfma_f32_16x16x32_bf16 v[104:107], a[128:131], v[220:223], v[104:107]\n"
      "ds_read_b128 v[216:219], v233 offset:28672\n"
      "v_mfma_f32_16x16x32_bf16 v[108:111], a[132:135], v[220:223], v[108:111]\n"
      "ds_read_b64_tr_b16 a[22:23], v243 offset:25480\n"
      "v_mad_i32_i24 v238, v251, s95, v238\n"
      "s_add_u32 s56, s22, 0x24000\n"
      "s_addc_u32 s57, s23, 0\n"
      "s_add_u32 m0, s18, 0x5e00\n"
      "v_mfma_f32_16x16x32_bf16 v[112:115], a[136:139], v[220:223], v[112:115]\n"
      "global_load_lds_dwordx4 v238, s[56:57] offset:512 \n"
      "v_mfma_f32_16x16x32_bf16 v[116:119], a[140:143], v[220:223], v[116:119]\n"
      "s_waitcnt lgkmcnt(11)\n"
      "v_mfma_f32_16x16x32_bf16 v[120:123], a[120:123], v[224:227], v[120:123]\n"
      "v_mad_i32_i24 v238, v251, s96, v238\n"
      "s_add_u32 s56, s22, 0x2a000\n"
      "s_addc_u32 s57, s23, 0\n"
      "s_add_u32 m0, s18, 0x6e00\n"
      "v_mfma_f32_16x16x32_bf16 v[124:127], a[124:127], v[224:227], v[124:127]\n"
      "global_load_lds_dwordx4 v238, s[56:57] offset:512 \n"
      "v_mfma_f32_16x16x32_bf16 v[128:131], a[128:131], v[224:227], v[128:131]\n"
      "ds_read_b128 v[220:223], v233 offset:32768\n"
      "v_add_u32 v233, 0xffff7000, v233\n"
      "v_mfma_f32_16x16x32_bf16 v[132:135], a[132:135], v[224:227], v[132:135]\n"
      "v_mad_i32_i24 v238, v251, s97, v238\n"
      "s_add_u32 s56, s22, 0x30000\n"
      "s_addc_u32 s57, s23, 0\n"
      "s_add_u32 m0, s18, 0x7e00\n"
      "v_mfma_f32_16x16x32_bf16 v[136:139], a[136:139], v[224:227], v[136:139]\n"
      "global_load_lds_dwordx4 v238, s[56:57] offset:512 \n"
      "v_mfma_f32_16x16x32_bf16 v[140:143], a[140:143], v[224:227], v[140:143]\n"
      "s_waitcnt lgkmcnt(7)\n"
      "v_mfma_f32_16x16x32_bf16 v[144:147], a[120:123], v[228:231], v[144:147]\n"
      "v_mfma_f32_16x16x32_bf16 v[148:151], a[124:127], v[228:231], v[148:151]\n"
      "global_load_dwordx4 a[252:255], v239, s[58:59] offset:3072 sc1\n"
      "v_mfma_f32_16x16x32_bf16 v[152:155], a[128:131], v[228:231], v[152:155]\n"
      "ds_read_b128 v[224:227], v234 offset:0\n"
      "v_mfma_f32_16x16x32_bf16 v[156:159], a[132:135], v[228:231], v[156:159]\n"
      "v_mfma_f32_16x16x32_bf16 v[160:163], a[136:139], v[228:231], v[160:163]\n"
      "v_mfma_f32_16x16x32_bf16 v[164:167], a[140:143], v[228:231], v[164:167]\n"
      "s_add_u32 s58, s24, 0x1000\n"
      "s_addc_u32 s59, s25, 0\n"
      "global_load_dwordx4 a[48:51], v239, s[58:59] offset:0 sc1\n"
      "s_waitcnt lgkmcnt(3)\n"
      "v_mfma_f32_16x16x32_bf16 v[168:171], a[120:123], v[216:219], v[168:171]\n"
      "s_waitcnt vmcnt(11)\n"
      "v_mfma_f32_16x16x32_bf16 v[172:175], a[124:127], v[216:219], v[172:175]\n"
      "ds_read_b64_tr_b16 a[24:25], v243 offset:49152\n"
      "v_mfma_f32_16x16x32_bf16 v[176:179], a[128:131], v[216:219], v[176:179]\n"
      "ds_read_b128 v[228:231], v234 offset:4096\n"
      "v_mfma_f32_16x16x32_bf16 v[180:183], a[132:135], v[216:219], v[180:183]\n"
      "ds_read_b64_tr_b16 a[26:27], v243 offset:49920\n"
      "global_load_dwordx4 a[52:55], v239, s[58:59] offset:1024 sc1\n"
      "v_mfma_f32_16x16x32_bf16 v[184:187], a[136:139], v[216:219], v[184:187]\n"
      "ds_read_b64_tr_b16 a[28:29], v243 offset:49160\n"
      "v_mfma_f32_16x16x32_bf16 v[188:191], a[140:143], v[216:219], v[188:191]\n"
      "s_waitcnt lgkmcnt(5)\n"
      "v_mfma_f32_16x16x32_bf16 v[192:195], a[120:123], v[220:223], v[192:195]\n"
      "ds_read_b64_tr_b16 a[30:31], v243 offset:49928\n"
      "v_mfma_f32_16x16x32_bf16 v[196:199], a[124:127], v[220:223], v[196:199]\n"
      "ds_read_b64_tr_b16 a[32:33], v243 offset:49216\n"
      "global_load_dwordx4 a[56:59], v239, s[58:59] offset:2048 sc1\n"
      "v_mfma_f32_16x16x32_bf16 v[200:203], a[128:131], v[220:223], v[200:203]\n"
      "ds_read_b128 v[216:219], v234 offset:8192\n"
      "v_mfma_f32_16x16x32_bf16 v[204:207], a[132:135], v[220:223], v[204:207]\n"
      "ds_read_b64_tr_b16 a[34:35], v243 offset:49984\n"
      "v_mfma_f32_16x16x32_bf16 v[208:211], a[136:139], v[220:223], v[208:211]\n"
      "ds_read_b64_tr_b16 a[36:37], v243 offset:49224\n"
      "v_mfma_f32_16x16x32_bf16 v[212:215], a[140:143], v[220:223], v[212:215]\n"
      "global_load_dwordx4 a[60:63], v239, s[58:59] offset:3072 sc1\n"
      "s_waitcnt lgkmcnt(9)\n"
      "v_mfma_f32_16x16x32_bf16 v[0:3], a[144:147], v[224:227], v[0:3]\n"
      "ds_read_b64_tr_b16 a[38:39], v243 offset:49992\n"
      "v_mfma_f32_16x16x32_bf16 v[4:7], a[148:151], v[224:227], v[4:7]\n"
      "ds_read_b64_tr_b16 a[40:41], v243 offset:49280\n"
      "v_mfma_f32_16x16x32_bf16 v[8:11], a[152:155], v[224:227], v[8:11]\n"
      "ds_read_b128 v[220:223], v234 offset:12288\n"
      "v_mfma_f32_16x16x32_bf16 v[12:15], a[156:159], v[224:227], v[12:15]\n"
      "ds_read_b64_tr_b16 a[42:43], v243 offset:50048\n"
      "s_add_u32 s58, s24, 0x2000\n"
      "s_addc_u32 s59, s25, 0\n"
      "global_load_dwordx4 a[64:67], v239, s[58:59] offset:0 sc1\n"
      "v_mfma_f32_16x16x32_bf16 v[16:19], a[160:163], v[224:227], v[16:19]\n"
      "ds_read_b64_tr_b16 a[44:45], v243 offset:49288\n"
      "v_mfma_f32_16x16x32_bf16 v[20:23], a[164:167], v[224:227], v[20:23]\n"
      "s_waitcnt lgkmcnt(12)\n"
      "v_mfma_f32_16x16x32_bf16 v[24:27], a[144:147], v[228:231], v[24:27]\n"
      "ds_read_b64_tr_b16 a[46:47], v243 offset:50056\n"
      "v_mfma_f32_16x16x32_bf16 v[28:31], a[148:151], v[228:231], v[28:31]\n"
      "global_load_dwordx4 a[68:71], v239, s[58:59] offset:1024 sc1\n"
      "v_mfma_f32_16x16x32_bf16 v[32:35], a[152:155], v[228:231], v[32:35]\n"
      "ds_read_b128 v[224:227], v234 offset:16384\n"
      "v_mfma_f32_16x16x32_bf16 v[36:39], a[156:159], v[228:231], v[36:39]\n"
      "v_mfma_f32_16x16x32_bf16 v[40:43], a[160:163], v[228:231], v[40:43]\n"
      "v_mfma_f32_16x16x32_bf16 v[44:47], a[164:167], v[228:231], v[44:47]\n"
      "global_load_dwordx4 a[72:75], v239, s[58:59] offset:2048 sc1\n"
      "s_waitcnt lgkmcnt(9)\n"
      "v_mfma_f32_16x16x32_bf16 v[48:51], a[144:147], v[216:219], v[48:51]\n"
      "v_mfma_f32_16x16x32_bf16 v[52:55], a[148:151], v[216:219], v[52:55]\n"
      "v_mfma_f32_16x16x32_bf16 v[56:59], a[152:155], v[216:219], v[56:59]\n"
      "ds_read_b128 v[228:231], v234 offset:20480\n"
      "v_mfma_f32_16x16x32_bf16 v[60:63], a[156:159], v[216:219], v[60:63]\n"
      "global_load_dwordx4 a[76:79], v239, s[58:59] offset:3072 sc1\n"
      "v_mfma_f32_16x16x32_bf16 v[64:67], a[160:163], v[216:219], v[64:67]\n"
      "v_mfma_f32_16x16x32_bf16 v[68:71], a[164:167], v[216:219], v[68:71]\n"
      "s_waitcnt lgkmcnt(5)\n"
      "v_mfma_f32_16x16x32_bf16 v[72:75], a[144:147], v[220:223], v[72:75]\n"
      "v_mfma_f32_16x16x32_bf16 v[76:79], a[148:151], v[220:223], v[76:79]\n"
      "s_add_u32 s58, s24, 0x3000\n"
      "s_addc_u32 s59, s25, 0\n"
      "global_load_dwordx4 a[80:83], v239, s[58:59] offset:0 sc1\n"
      "v_mfma_f32_16x16x32_bf16 v[80:83], a[152:155], v[220:223], v[80:83]\n"
      "ds_read_b128 v[216:219], v234 offset:24576\n"
      "v_mfma_f32_16x16x32_bf16 v[84:87], a[156:159], v[220:223], v[84:87]\n"
      "v_mfma_f32_16x16x32_bf16 v[88:91], a[160:163], v[220:223], v[88:91]\n"
      "v_mfma_f32_16x16x32_bf16 v[92:95], a[164:167], v[220:223], v[92:95]\n"
      "global_load_dwordx4 a[84:87], v239, s[58:59] offset:1024 sc1\n"
      "s_waitcnt lgkmcnt(2)\n"
      "v_mfma_f32_16x16x32_bf16 v[96:99], a[144:147], v[224:227], v[96:99]\n"
      "v_mfma_f32_16x16x32_bf16 v[100:103], a[148:151], v[224:227], v[100:103]\n"
      "v_mfma_f32_16x16x32_bf16 v[104:107], a[152:155], v[224:227], v[104:107]\n"
      "ds_read_b128 v[220:223], v234 offset:28672\n"
      "v_mfma_f32_16x16x32_bf16 v[108:111], a[156:159], v[224:227], v[108:111]\n"
      "global_load_dwordx4 a[88:91], v239, s[58:59] offset:2048 sc1\n"
      "v_mfma_f32_16x16x32_bf16 v[112:115], a[160:163], v[224:227], v[112:115]\n"
      "v_mfma_f32_16x16x32_bf16 v[116:119], a[164:167], v[224:227], v[116:119]\n"
      "s_waitcnt lgkmcnt(2)\n"
      "v_mfma_f32_16x16x32_bf16 v[120:123], a[144:147], v[228:231], v[120:123]\n"
      "v_mfma_f32_16x16x32_bf16 v[124:127], a[148:151], v[228:231], v[124:127]\n"
      "global_load_dwordx4 a[92:95], v239, s[58:59] offset:3072 sc1\n"
      "v_mfma_f32_16x16x32_bf16 v[128:131], a[152:155], v[228:231], v[128:131]\n"
      "ds_read_b128 v[224:227], v234 offset:32768\n"
      "v_add_u32 v234, 0xffff7000, v234\n"
      "v_mfma_f32_16x16x32_bf16 v[132:135], a[156:159], v[228:231], v[132:135]\n"
      "v_mfma_f32_16x16x32_bf16 v[136:139], a[160:163], v[228:231], v[136:139]\n"
      "v_mfma_f32_16x16x32_bf16 v[140:143], a[164:167], v[228:231], v[140:143]\n"
      "s_add_u32 s58, s24, 0x4000\n"
      "s_addc_u32 s59, s25, 0\n"
      "global_load_dwordx4 a[96:99], v239, s[58:59] offset:0 sc1\n"
      "s_waitcnt lgkmcnt(2)\n"
      "v_mfma_f32_16x16x32_bf16 v[144:147], a[144:147], v[216:219], v[144:147]\n"
      "ds_read_b64 v[236:237], v250 offset:4104\n"
      "v_mfma_f32_16x16x32_bf16 v[148:151], a[148:151], v[216:219], v[148:151]\n"
      "v_mfma_f32_16x16x32_bf16 v[152:155], a[152:155], v[216:219], v[152:155]\n"
      "ds_read_b128 v[228:231], v235 offset:0\n"
      "v_mfma_f32_16x16x32_bf16 v[156:159], a[156:159], v[216:219], v[156:159]\n"
      "global_load_dwordx4 a[100:103], v239, s[58:59] offset:1024 sc1\n"
      "v_mfma_f32_16x16x32_bf16 v[160:163], a[160:163], v[216:219], v[160:163]\n"
      "v_mfma_f32_16x16x32_bf16 v[164:167], a[164:167], v[216:219], v[164:167]\n"
      "s_waitcnt lgkmcnt(3)\n"
      "v_mfma_f32_16x16x32_bf16 v[168:171], a[144:147], v[220:223], v[168:171]\n"
      "v_mfma_f32_16x16x32_bf16 v[172:175], a[148:151], v[220:223], v[172:175]\n"
      "global_load_dwordx4 a[104:107], v239, s[58:59] offset:2048 sc1\n"
      "v_mfma_f32_16x16x32_bf16 v[176:179], a[152:155], v[220:223], v[176:179]\n"
      "ds_read_b128 v[216:219], v235 offset:4096\n"
      "v_mfma_f32_16x16x32_bf16 v[180:183], a[156:159], v[220:223], v[180:183]\n"
      "v_mfma_f32_16x16x32_bf16 v[184:187], a[160:163], v[220:223], v[184:187]\n"
      "v_mfma_f32_16x16x32_bf16 v[188:191], a[164:167], v[220:223], v[188:191]\n"
      "global_load_dwordx4 a[108:111], v239, s[58:59] offset:3072 sc1\n"
      "s_waitcnt lgkmcnt(3)\n"
      "v_mfma_f32_16x16x32_bf16 v[192:195], a[144:147], v[224:227], v[192:195]\n"
      "v_mfma_f32_16x16x32_bf16 v[196:199], a[148:151], v[224:227], v[196:199]\n"
      "v_mfma_f32_16x16x32_bf16 v[200:203], a[152:155], v[224:227], v[200:203]\n"
      "ds_read_b128 v[220:223], v235 offset:8192\n"
      "v_mfma_f32_16x16x32_bf16 v[204:207], a[156:159], v[224:227], v[204:207]\n"
      "s_add_u32 s58, s24, 0x5000\n"
      "s_addc_u32 s59, s25, 0\n"
      "global_load_dwordx4 a[112:115], v239, s[58:59] offset:0 sc1\n"
      "v_mfma_f32_16x16x32_bf16 v[208:211], a[160:163], v[224:227], v[208:211]\n"
      "v_mfma_f32_16x16x32_bf16 v[212:215], a[164:167], v[224:227], v[212:215]\n"
      "s_waitcnt lgkmcnt(2)\n"
      "v_mfma_f32_16x16x32_bf16 v[0:3], a[168:171], v[228:231], v[0:3]\n"
      "v_mfma_f32_16x16x32_bf16 v[4:7], a[172:175], v[228:231], v[4:7]\n"
      "global_load_dwordx4 a[116:119], v239, s[58:59] offset:1024 sc1\n"
      "v_mfma_f32_16x16x32_bf16 v[8:11], a[176:179], v[228:231], v[8:11]\n"
      "ds_read_b128 v[224:227], v235 offset:12288\n"
      "v_mfma_f32_16x16x32_bf16 v[12:15], a[180:183], v[228:231], v[12:15]\n"
      "v_mfma_f32_16x16x32_bf16 v[16:19], a[184:187], v[228:231], v[16:19]\n"
      "v_mfma_f32_16x16x32_bf16 v[20:23], a[188:191], v[228:231], v[20:23]\n"
      "global_load_dwordx4 a[120:123], v239, s[58:59] offset:2048 sc1\n"
      "s_waitcnt lgkmcnt(2)\n"
      "v_mfma_f32_16x16x32_bf16 v[24:27], a[168:171], v[216:219], v[24:27]\n"
      "v_mfma_f32_16x16x32_bf16 v[28:31], a[172:175], v[216:219], v[28:31]\n"
      "v_mfma_f32_16x16x32_bf16 v[32:35], a[176:179], v[216:219], v[32:35]\n"
      "ds_read_b128 v[228:231], v235 offset:16384\n"
      "v_mfma_f32_16x16x32_bf16 v[36:39], a[180:183], v[216:219], v[36:39]\n"
      "global_load_dwordx4 a[124:127], v239, s[58:59] offset:3072 sc1\n"
      "v_mfma_f32_16x16x32_bf16 v[40:43], a[184:187], v[216:219], v[40:43]\n"
      "v_mfma_f32_16x16x32_bf16 v[44:47], a[188:191], v[216:219], v[44:47]\n"
      "s_waitcnt lgkmcnt(2)\n"
      "v_mfma_f32_16x16x32_bf16 v[48:51], a[168:171], v[220:223], v[48:51]\n"
      "v_mfma_f32_16x16x32_bf16 v[52:55], a[172:175], v[220:223], v[52:55]\n"
      "s_add_u32 s58, s24, 0x6000\n"
      "s_addc_u32 s59, s25, 0\n"
      "global_load_dwordx4 a[128:131], v239, s[58:59] offset:0 sc1\n"
      "v_mfma_f32_16x16x32_bf16 v[56:59], a[176:179], v[220:223], v[56:59]\n"
      "ds_read_b128 v[216:219], v235 offset:20480\n"
      "v_mfma_f32_16x16x32_bf16 v[60:63], a[180:183], v[220:223], v[60:63]\n"
      "v_mfma_f32_16x16x32_bf16 v[64:67], a[184:187], v[220:223], v[64:67]\n"
      "v_mfma_f32_16x16x32_bf16 v[68:71], a[188:191], v[220:223], v[68:71]\n"
      "global_load_dwordx4 a[132:135], v239, s[58:59] offset:1024 sc1\n"
      "s_waitcnt lgkmcnt(2)\n"
      "v_mfma_f32_16x16x32_bf16 v[72:75], a[168:171], v[224:227], v[72:75]\n"
      "v_mfma_f32_16x16x32_bf16 v[76:79], a[172:175], v[224:227], v[76:79]\n"
      "v_mfma_f32_16x16x32_bf16 v[80:83], a[176:179], v[224:227], v[80:83]\n"
      "ds_read_b128 v[220:223], v235 offset:24576\n"
      "v_mfma_f32_16x16x32_bf16 v[84:87], a[180:183], v[224:227], v[84:87]\n"
      "global_load_dwordx4 a[136:139], v239, s[58:59] offset:2048 sc1\n"
      "v_mfma_f32_16x16x32_bf16 v[88:91], a[184:187], v[224:227], v[88:91]\n"
      "v_mfma_f32_16x16x32_bf16 v[92:95], a[188:191], v[224:227], v[92:95]\n"
      "s_waitcnt lgkmcnt(2)\n"
      "v_mfma_f32_16x16x32_bf16 v[96:99], a[168:171], v[228:231], v[96:99]\n"
      "v_mfma_f32_16x16x32_bf16 v[100:103], a[172:175], v[228:231], v[100:103]\n"
      "global_load_dwordx4 a[140:143], v239, s[58:59] offset:3072 sc1\n"
      "v_mfma_f32_16x16x32_bf16 v[104:107], a[176:179], v[228:231], v[104:107]\n"
      "ds_read_b128 v[224:227], v235 offset:28672\n"
      "v_mfma_f32_16x16x32_bf16 v[108:111], a[180:183], v[228:231], v[108:111]\n"
      "v_mfma_f32_16x16x32_bf16 v[112:115], a[184:187], v[228:231], v[112:115]\n"
      "v_mfma_f32_16x16x32_bf16 v[116:119], a[188:191], v[228:231], v[116:119]\n"
      "s_add_u32 s58, s24, 0x7000\n"
      "s_addc_u32 s59, s25, 0\n"
      "global_load_dwordx4 a[144:147], v239, s[58:59] offset:0 sc1\n"
      "s_waitcnt lgkmcnt(2)\n"
      "v_mfma_f32_16x16x32_bf16 v[120:123], a[168:171], v[216:219], v[120:123]\n"
      "v_mfma_f32_16x16x32_bf16 v[124:127], a[172:175], v[216:219], v[124:127]\n"
      "v_mfma_f32_16x16x32_bf16 v[128:131], a[176:179], v[216:219], v[128:131]\n"
      "ds_read_b128 v[228:231], v235 offset:32768\n"
      "v_add_u32 v235, 0xffff7000, v235\n"
      "v_mfma_f32_16x16x32_bf16 v[132:135], a[180:183], v[216:219], v[132:135]\n"
      "global_load_dwordx4 a[148:151], v239, s[58:59] offset:1024 sc1\n"
      "v_mfma_f32_16x16x32_bf16 v[136:139], a[184:187], v[216:219], v[136:139]\n"
      "v_mfma_f32_16x16x32_bf16 v[140:143], a[188:191], v[216:219], v[140:143]\n"
      "s_waitcnt lgkmcnt(2)\n"
      "v_mfma_f32_16x16x32_bf16 v[144:147], a[168:171], v[220:223], v[144:147]\n"
      "v_mfma_f32_16x16x32_bf16 v[148:151], a[172:175], v[220:223], v[148:151]\n"
      "s_waitcnt vmcnt(27)\n"
      "s_barrier\n"
      "global_load_dwordx4 a[152:155], v239, s[58:59] offset:2048 sc1\n"
      "v_mfma_f32_16x16x32_bf16 v[152:155], a[176:179], v[220:223], v[152:155]\n"
      "ds_read_b128 v[216:219], v232 offset:0\n"
      "v_mfma_f32_16x16x32_bf16 v[156:159], a[180:183], v[220:223], v[156:159]\n"
      "v_mfma_f32_16x16x32_bf16 v[160:163], a[184:187], v[220:223], v[160:163]\n"
      "v_mfma_f32_16x16x32_bf16 v[164:167], a[188:191], v[220:223], v[164:167]\n"
      "global_load_dwordx4 a[156:159], v239, s[58:59] offset:3072 sc1\n"
      "s_waitcnt lgkmcnt(2)\n"
      "v_mfma_f32_16x16x32_bf16 v[168:171], a[168:171], v[224:227], v[168:171]\n"
      "v_mfma_f32_16x16x32_bf16 v[172:175], a[172:175], v[224:227], v[172:175]\n"
      "v_mfma_f32_16x16x32_bf16 v[176:179], a[176:179], v[224:227], v[176:179]\n"
      "ds_read_b128 v[220:223], v232 offset:4096\n"
      "v_mfma_f32_16x16x32_bf16 v[180:183], a[180:183], v[224:227], v[180:183]\n"
      "s_add_u32 s58, s24, 0x8000\n"
      "s_addc_u32 s59, s25, 0\n"
      "global_load_dwordx4 a[160:163], v239, s[58:59] offset:0 sc1\n"
      "v_mfma_f32_16x16x32_bf16 v[184:187], a[184:187], v[224:227], v[184:187]\n"
      "v_mfma_f32_16x16x32_bf16 v[188:191], a[188:191], v[224:227], v[188:191]\n"
      "s_waitcnt lgkmcnt(2)\n"
      "v_mfma_f32_16x16x32_bf16 v[192:195], a[168:171], v[228:231], v[192:195]\n"
      "v_mfma_f32_16x16x32_bf16 v[196:199], a[172:175], v[228:231], v[196:199]\n"
      "global_load_dwordx4 a[164:167], v239, s[58:59] offset:1024 sc1\n"
      "v_mfma_f32_16x16x32_bf16 v[200:203], a[176:179], v[228:231], v[200:203]\n"
      "ds_read_b128 v[224:227], v232 offset:8192\n"
      "v_mfma_f32_16x16x32_bf16 v[204:207], a[180:183], v[228:231], v[204:207]\n"
      "v_mfma_f32_16x16x32_bf16 v[208:211], a[184:187], v[228:231], v[208:211]\n"
      "v_mfma_f32_16x16x32_bf16 v[212:215], a[188:191], v[228:231], v[212:215]\n"
      "s_waitcnt lgkmcnt(2)\n"
      "v_mfma_f32_16x16x32_bf16 v[0:3], a[192:195], v[216:219], v[0:3]\n"
      "global_load_dwordx4 a[168:171], v239, s[58:59] offset:2048 sc1\n"
      "v_mfma_f32_16x16x32_bf16 v[4:7], a[196:199], v[216:219], v[4:7]\n"
      "v_mfma_f32_16x16x32_bf16 v[8:11], a[200:203], v[216:219], v[8:11]\n"
      "ds_read_b128 v[228:231], v232 offset:12288\n"
      "v_mfma_f32_16x16x32_bf16 v[12:15], a[204:207], v[216:219], v[12:15]\n"
      "v_mfma_f32_16x16x32_bf16 v[16:19], a[208:211], v[216:219], v[16:19]\n"
      "global_load_dwordx4 a[172:175], v239, s[58:59] offset:3072 sc1\n"
      "v_mfma_f32_16x16x32_bf16 v[20:23], a[212:215], v[216:219], v[20:23]\n"
      "s_waitcnt lgkmcnt(2)\n"
      "v_mfma_f32_16x16x32_bf16 v[24:27], a[192:195], v[220:223], v[24:27]\n"
      "v_mfma_f32_16x16x32_bf16 v[28:31], a[196:199], v[220:223], v[28:31]\n"
      "v_mfma_f32_16x16x32_bf16 v[32:35], a[200:203], v[220:223], v[32:35]\n"
      "ds_read_b128 v[216:219], v232 offset:16384\n"
      "s_add_u32 s58, s24, 0x9000\n"
      "s_addc_u32 s59, s25, 0\n"
      "global_load_dwordx4 a[176:179], v239, s[58:59] offset:0 sc1\n"
      "v_mfma_f32_16x16x32_bf16 v[36:39], a[204:207], v[220:223], v[36:39]\n"
      "v_mfma_f32_16x16x32_bf16 v[40:43], a[208:211], v[220:223], v[40:43]\n"
      "v_mfma_f32_16x16x32_bf16 v[44:47], a[212:215], v[220:223], v[44:47]\n"
      "s_waitcnt lgkmcnt(2)\n"
      "v_mfma_f32_16x16x32_bf16 v[48:51], a[192:195], v[224:227], v[48:51]\n"
      "global_load_dwordx4 a[180:183], v239, s[58:59] offset:1024 sc1\n"
      "v_mfma_f32_16x16x32_bf16 v[52:55], a[196:199], v[224:227], v[52:55]\n"
      "v_mfma_f32_16x16x32_bf16 v[56:59], a[200:203], v[224:227], v[56:59]\n"
      "ds_read_b128 v[220:223], v232 offset:20480\n"
      "v_mfma_f32_16x16x32_bf16 v[60:63], a[204:207], v[224:227], v[60:63]\n"
      "v_mfma_f32_16x16x32_bf16 v[64:67], a[208:211], v[224:227], v[64:67]\n"
      "global_load_dwordx4 a[184:187], v239, s[58:59] offset:2048 sc1\n"
      "v_mfma_f32_16x16x32_bf16 v[68:71], a[212:215], v[224:227], v[68:71]\n"
      "s_waitcnt lgkmcnt(2)\n"
      "v_mfma_f32_16x16x32_bf16 v[72:75], a[192:195], v[228:231], v[72:75]\n"
      "v_mfma_f32_16x16x32_bf16 v[76:79], a[196:199], v[228:231], v[76:79]\n"
      "v_mfma_f32_16x16x32_bf16 v[80:83], a[200:203], v[228:231], v[80:83]\n"
      "ds_read_b128 v[224:227], v232 offset:24576\n"
      "global_load_dwordx4 a[188:191], v239, s[58:59] offset:3072 sc1\n"
      "v_mfma_f32_16x16x32_bf16 v[84:87], a[204:207], v[228:231], v[84:87]\n"
      "v_mfma_f32_16x16x32_bf16 v[88:91], a[208:211], v[228:231], v[88:91]\n"
      "v_mfma_f32_16x16x32_bf16 v[92:95], a[212:215], v[228:231], v[92:95]\n"
      "s_waitcnt lgkmcnt(2)\n"
      "v_mfma_f32_16x16x32_bf16 v[96:99], a[192:195], v[216:219], v[96:99]\n"
      "s_mul_i32 s78, s98, s19\n"
      "v_mfma_f32_16x16x32_bf16 v[100:103], a[196:199], v[216:219], v[100:103]\n"
      "v_add_u32 v236, s78, v236\n"
      "v_mfma_f32_16x16x32_bf16 v[104:107], a[200:203], v[216:219], v[104:107]\n"
      "ds_read_b128 v[228:231], v232 offset:28672\n"
      "s_mul_i32 s78, s78, s99\n"
      "v_mfma_f32_16x16x32_bf16 v[108:111], a[204:207], v[216:219], v[108:111]\n"
      "v_add_u32 v237, s78, v237\n"
      "v_mfma_f32_16x16x32_bf16 v[112:115], a[208:211], v[216:219], v[112:115]\n"
      "s_add_u32 s52, s20, s32\n"
      "s_addc_u32 s53, s21, 0\n"
      "s_add_u32 m0, s17, 0x0\n"
      "v_mfma_f32_16x16x32_bf16 v[116:119], a[212:215], v[216:219], v[116:119]\n"
      "global_load_lds_dwordx4 v236, s[52:53] offset:0 \n"
      "s_waitcnt lgkmcnt(2)\n"
      "v_mfma_f32_16x16x32_bf16 v[120:123], a[192:195], v[220:223], v[120:123]\n"
      "v_mfma_f32_16x16x32_bf16 v[124:127], a[196:199], v[220:223], v[124:127]\n"
      "s_add_u32 m0, s17, 0x1f00\n"
      "v_mfma_f32_16x16x32_bf16 v[128:131], a[200:203], v[220:223], v[128:131]\n"
      "ds_read_b128 v[216:219], v232 offset:32768\n"
      "v_add_u32 v232, v249, v232\n"
      "global_load_lds_dwordx4 v236, s[52:53] offset:256 \n"
      "v_mfma_f32_16x16x32_bf16 v[132:135], a[204:207], v[220:223], v[132:135]\n"
      "v_mfma_f32_16x16x32_bf16 v[136:139], a[208:211], v[220:223], v[136:139]\n"
      "s_add_u32 m0, s17, 0x3e00\n"
      "v_mfma_f32_16x16x32_bf16 v[140:143], a[212:215], v[220:223], v[140:143]\n"
      "global_load_lds_dwordx4 v236, s[52:53] offset:512 \n"
      "s_waitcnt lgkmcnt(2)\n"
      "v_mfma_f32_16x16x32_bf16 v[144:147], a[192:195], v[224:227], v[144:147]\n"
      "v_mfma_f32_16x16x32_bf16 v[148:151], a[196:199], v[224:227], v[148:151]\n"
      "s_add_u32 m0, s17, 0x1000\n"
      "v_mfma_f32_16x16x32_bf16 v[152:155], a[200:203], v[224:227], v[152:155]\n"
      "ds_read_b128 v[220:223], v233 offset:0\n"
      "global_load_lds_dwordx4 v237, s[52:53] offset:0 \n"
      "v_mfma_f32_16x16x32_bf16 v[156:159], a[204:207], v[224:227], v[156:159]\n"
      "v_mfma_f32_16x16x32_bf16 v[160:163], a[208:211], v[224:227], v[160:163]\n"
      "s_add_u32 m0, s17, 0x2f00\n"
      "v_mfma_f32_16x16x32_bf16 v[164:167], a[212:215], v[224:227], v[164:167]\n"
      "global_load_lds_dwordx4 v237, s[52:53] offset:256 \n"
      "s_waitcnt lgkmcnt(2)\n"
      "v_mfma_f32_16x16x32_bf16 v[168:171], a[192:195], v[228:231], v[168:171]\n"
      "v_mfma_f32_16x16x32_bf16 v[172:175], a[196:199], v[228:231], v[172:175]\n"
      "s_add_u32 m0, s17, 0x4e00\n"
      "v_mfma_f32_16x16x32_bf16 v[176:179], a[200:203], v[228:231], v[176:179]\n"
      "ds_read_b128 v[224:227], v233 offset:4096\n"
      "global_load_lds_dwordx4 v237, s[52:53] offset:512 \n"
      "v_mfma_f32_16x16x32_bf16 v[180:183], a[204:207], v[228:231], v[180:183]\n"
      "v_mfma_f32_16x16x32_bf16 v[184:187], a[208:211], v[228:231], v[184:187]\n"
      "v_mfma_f32_16x16x32_bf16 v[188:191], a[212:215], v[228:231], v[188:191]\n"
      "s_waitcnt lgkmcnt(2)\n"
      "v_mfma_f32_16x16x32_bf16 v[192:195], a[192:195], v[216:219], v[192:195]\n"
      "v_mfma_f32_16x16x32_bf16 v[196:199], a[196:199], v[216:219], v[196:199]\n"
      "LPD2:\n"
      "s_cbranch_execz LPD7\n"
      "v_mfma_f32_16x16x32_bf16 v[200:203], a[200:203], v[216:219], v[200:203]\n"
      "ds_read_b128 v[228:231], v233 offset:8192\n"
      "v_mfma_f32_16x16x32_bf16 v[204:207], a[204:207], v[216:219], v[204:207]\n"
      "v_mfma_f32_16x16x32_bf16 v[208:211], a[208:211], v[216:219], v[208:211]\n"
      "v_mfma_f32_16x16x32_bf16 v[212:215], a[212:215], v[216:219], v[212:215]\n"
      "s_waitcnt lgkmcnt(2)\n"
      "v_mfma_f32_16x16x32_bf16 v[0:3], a[216:219], v[220:223], v[0:3]\n"
      "s_add_u32 s58, s24, 0xa000\n"
      "s_addc_u32 s59, s25, 0\n"
      "global_load_dwordx4 a[192:195], v239, s[58:59] offset:0 sc1\n"
      "v_mfma_f32_16x16x32_bf16 v[4:7], a[220:223], v[220:223], v[4:7]\n"
      "v_mfma_f32_16x16x32_bf16 v[8:11], a[224:227], v[220:223], v[8:11]\n"
      "ds_read_b128 v[216:219], v233 offset:12288\n"
      "v_mfma_f32_16x16x32_bf16 v[12:15], a[228:231], v[220:223], v[12:15]\n"
      "v_mfma_f32_16x16x32_bf16 v[16:19], a[232:235], v[220:223], v[16:19]\n"
      "global_load_dwordx4 a[196:199], v239, s[58:59] offset:1024 sc1\n"
      "v_mfma_f32_16x16x32_bf16 v[20:23], a[236:239], v[220:223], v[20:23]\n"
      "s_waitcnt lgkmcnt(2)\n"
      "v_mfma_f32_16x16x32_bf16 v[24:27], a[216:219], v[224:227], v[24:27]\n"
      "v_mfma_f32_16x16x32_bf16 v[28:31], a[220:223], v[224:227], v[28:31]\n"
      "v_mfma_f32_16x16x32_bf16 v[32:35], a[224:227], v[224:227], v[32:35]\n"
      "ds_read_b128 v[220:223], v233 offset:16384\n"
      "global_load_dwordx4 a[200:203], v239, s[58:59] offset:2048 sc1\n"
      "v_mfma_f32_16x16x32_bf16 v[36:39], a[228:231], v[224:227], v[36:39]\n"
      "v_mfma_f32_16x16x32_bf16 v[40:43], a[232:235], v[224:227], v[40:43]\n"
      "v_mfma_f32_16x16x32_bf16 v[44:47], a[236:239], v[224:227], v[44:47]\n"
      "s_waitcnt lgkmcnt(2)\n"
      "v_mfma_f32_16x16x32_bf16 v[48:51], a[216:219], v[228:231], v[48:51]\n"
      "global_load_dwordx4 a[204:207], v239, s[58:59] offset:3072 sc1\n"
      "v_mfma_f32_16x16x32_bf16 v[52:55], a[220:223], v[228:231], v[52:55]\n"
      "v_mfma_f32_16x16x32_bf16 v[56:59], a[224:227], v[228:231], v[56:59]\n"
      "ds_read_b128 v[224:227], v233 offset:20480\n"
      "v_mfma_f32_16x16x32_bf16 v[60:63], a[228:231], v[228:231], v[60:63]\n"
      "v_mfma_f32_16x16x32_bf16 v[64:67], a[232:235], v[228:231], v[64:67]\n"
      "s_add_u32 s58, s24, 0xb000\n"
      "s_addc_u32 s59, s25, 0\n"
      "global_load_dwordx4 a[208:211], v239, s[58:59] offset:0 sc1\n"
      "v_mfma_f32_16x16x32_bf16 v[68:71], a[236:239], v[228:231], v[68:71]\n"
      "s_waitcnt lgkmcnt(2)\n"
      "v_mfma_f32_16x16x32_bf16 v[72:75], a[216:219], v[216:219], v[72:75]\n"
      "v_mfma_f32_16x16x32_bf16 v[76:79], a[220:223], v[216:219], v[76:79]\n"
      "v_mfma_f32_16x16x32_bf16 v[80:83], a[224:227], v[216:219], v[80:83]\n"
      "ds_read_b128 v[228:231], v233 offset:24576\n"
      "global_load_dwordx4 a[212:215], v239, s[58:59] offset:1024 sc1\n"
      "v_mfma_f32_16x16x32_bf16 v[84:87], a[228:231], v[216:219], v[84:87]\n"
      "v_mfma_f32_16x16x32_bf16 v[88:91], a[232:235], v[216:219], v[88:91]\n"
      "v_mfma_f32_16x16x32_bf16 v[92:95], a[236:239], v[216:219], v[92:95]\n"
      "s_waitcnt lgkmcnt(2)\n"
      "v_mfma_f32_16x16x32_bf16 v[96:99], a[216:219], v[220:223], v[96:99]\n"
      "v_mfma_f32_16x16x32_bf16 v[100:103], a[220:223], v[220:223], v[100:103]\n"
      "v_mfma_f32_16x16x32_bf16 v[104:107], a[224:227], v[220:223], v[104:107]\n"
      "ds_read_b128 v[216:219], v233 offset:28672\n"
      "v_mfma_f32_16x16x32_bf16 v[108:111], a[228:231], v[220:223], v[108:111]\n"
      "v_mfma_f32_16x16x32_bf16 v[112:115], a[232:235], v[220:223], v[112:115]\n"
      "v_mfma_f32_16x16x32_bf16 v[116:119], a[236:239], v[220:223], v[116:119]\n"
      "s_waitcnt lgkmcnt(2)\n"
      "v_mfma_f32_16x16x32_bf16 v[120:123], a[216:219], v[224:227], v[120:123]\n"
      "v_mfma_f32_16x16x32_bf16 v[124:127], a[220:223], v[224:227], v[124:127]\n"
      "v_mfma_f32_16x16x32_bf16 v[128:131], a[224:227], v[224:227], v[128:131]\n"
      "ds_read_b128 v[220:223], v233 offset:32768\n"
      "v_add_u32 v233, v249, v233\n"
      "v_mfma_f32_16x16x32_bf16 v[132:135], a[228:231], v[224:227], v[132:135]\n"
      "v_mfma_f32_16x16x32_bf16 v[136:139], a[232:235], v[224:227], v[136:139]\n"
      "v_mfma_f32_16x16x32_bf16 v[140:143], a[236:239], v[224:227], v[140:143]\n"
      "s_waitcnt lgkmcnt(2)\n"
      "v_mfma_f32_16x16x32_bf16 v[144:147], a[216:219], v[228:231], v[144:147]\n"
      "v_mfma_f32_16x16x32_bf16 v[148:151], a[220:223], v[228:231], v[148:151]\n"
      "v_mfma_f32_16x16x32_bf16 v[152:155], a[224:227], v[228:231], v[152:155]\n"
      "ds_read_b128 v[224:227], v234 offset:0\n"
      "v_mfma_f32_16x16x32_bf16 v[156:159], a[228:231], v[228:231], v[156:159]\n"
      "v_mfma_f32_16x16x32_bf16 v[160:163], a[232:235], v[228:231], v[160:163]\n"
      "v_mfma_f32_16x16x32_bf16 v[164:167], a[236:239], v[228:231], v[164:167]\n"
      "s_waitcnt lgkmcnt(2)\n"
      "v_mfma_f32_16x16x32_bf16 v[168:171], a[216:219], v[216:219], v[168:171]\n"
      "v_mfma_f32_16x16x32_bf16 v[172:175], a[220:223], v[216:219], v[172:175]\n"
      "v_mfma_f32_16x16x32_bf16 v[176:179], a[224:227], v[216:219], v[176:179]\n"
      "ds_read_b128 v[228:231], v234 offset:4096\n"
      "v_mfma_f32_16x16x32_bf16 v[180:183], a[228:231], v[216:219], v[180:183]\n"
      "v_mfma_f32_16x16x32_bf16 v[184:187], a[232:235], v[216:219], v[184:187]\n"
      "s_add_u32 s52, s42, s32\n"
      "s_addc_u32 s53, s43, 0\n"
      "s_add_u32 m0, s17, 0x6000\n"
      "v_mfma_f32_16x16x32_bf16 v[188:191], a[236:239], v[216:219], v[188:191]\n"
      "global_load_lds_dwordx4 v236, s[52:53] offset:0 \n"
      "s_waitcnt lgkmcnt(2)\n"
      "v_mfma_f32_16x16x32_bf16 v[192:195], a[216:219], v[220:223], v[192:195]\n"
      "v_mfma_f32_16x16x32_bf16 v[196:199], a[220:223], v[220:223], v[196:199]\n"
      "s_add_u32 m0, s17, 0x7f00\n"
      "v_mfma_f32_16x16x32_bf16 v[200:203], a[224:227], v[220:223], v[200:203]\n"
      "ds_read_b128 v[216:219], v234 offset:8192\n"
      "global_load_lds_dwordx4 v236, s[52:53] offset:256 \n"
      "v_mfma_f32_16x16x32_bf16 v[204:207], a[228:231], v[220:223], v[204:207]\n"
      "v_mfma_f32_16x16x32_bf16 v[208:211], a[232:235], v[220:223], v[208:211]\n"
      "s_add_u32 m0, s17, 0x9e00\n"
      "v_mfma_f32_16x16x32_bf16 v[212:215], a[236:239], v[220:223], v[212:215]\n"
      "global_load_lds_dwordx4 v236, s[52:53] offset:512 \n"
      "s_waitcnt lgkmcnt(2)\n"
      "v_mfma_f32_16x16x32_bf16 v[0:3], a[0:3], v[224:227], v[0:3]\n"
      "ds_read_b128 v[244:247], v250\n"
      "v_mfma_f32_16x16x32_bf16 v[4:7], a[4:7], v[224:227], v[4:7]\n"
      "s_add_u32 m0, s17, 0x7000\n"
      "ds_read_b64 v[252:253], v250 offset:4096\n"
      "v_mfma_f32_16x16x32_bf16 v[8:11], a[8:11], v[224:227], v[8:11]\n"
      "ds_read_b128 v[220:223], v234 offset:12288\n"
      "global_load_lds_dwordx4 v237, s[52:53] offset:0 \n"
      "v_mfma_f32_16x16x32_bf16 v[12:15], a[12:15], v[224:227], v[12:15]\n"
      "v_mfma_f32_16x16x32_bf16 v[16:19], a[16:19], v[224:227], v[16:19]\n"
      "s_add_u32 m0, s17, 0x8f00\n"
      "v_mfma_f32_16x16x32_bf16 v[20:23], a[20:23], v[224:227], v[20:23]\n"
      "global_load_lds_dwordx4 v237, s[52:53] offset:256 \n"
      "s_waitcnt lgkmcnt(4)\n"
      "v_mfma_f32_16x16x32_bf16 v[24:27], a[0:3], v[228:231], v[24:27]\n"
      "v_mfma_f32_16x16x32_bf16 v[28:31], a[4:7], v[228:231], v[28:31]\n"
      "s_add_u32 m0, s17, 0xae00\n"
      "v_mfma_f32_16x16x32_bf16 v[32:35], a[8:11], v[228:231], v[32:35]\n"
      "ds_read_b128 v[224:227], v234 offset:16384\n"
      "global_load_lds_dwordx4 v237, s[52:53] offset:512 \n"
      "v_mfma_f32_16x16x32_bf16 v[36:39], a[12:15], v[228:231], v[36:39]\n"
      "v_mfma_f32_16x16x32_bf16 v[40:43], a[16:19], v[228:231], v[40:43]\n"
      "v_mfma_f32_16x16x32_bf16 v[44:47], a[20:23], v[228:231], v[44:47]\n"
      "global_load_dwordx4 a[216:219], v239, s[58:59] offset:2048 sc1\n"
      "s_waitcnt lgkmcnt(4)\n"
      "v_mfma_f32_16x16x32_bf16 v[48:51], a[0:3], v[216:219], v[48:51]\n"
      "v_mfma_f32_16x16x32_bf16 v[52:55], a[4:7], v[216:219], v[52:55]\n"
      "v_mfma_f32_16x16x32_bf16 v[56:59], a[8:11], v[216:219], v[56:59]\n"
      "ds_read_b128 v[228:231], v234 offset:20480\n"
      "v_mfma_f32_16x16x32_bf16 v[60:63], a[12:15], v[216:219], v[60:63]\n"
      "global_load_dwordx4 a[220:223], v239, s[58:59] offset:3072 sc1\n"
      "v_mfma_f32_16x16x32_bf16 v[64:67], a[16:19], v[216:219], v[64:67]\n"
      "v_mfma_f32_16x16x32_bf16 v[68:71], a[20:23], v[216:219], v[68:71]\n"
      "s_waitcnt lgkmcnt(2)\n"
      "v_mfma_f32_16x16x32_bf16 v[72:75], a[0:3], v[220:223], v[72:75]\n"
      "v_mfma_f32_16x16x32_bf16 v[76:79], a[4:7], v[220:223], v[76:79]\n"
      "s_add_u32 s58, s24, 0xc000\n"
      "s_addc_u32 s59, s25, 0\n"
      "global_load_dwordx4 a[224:227], v239, s[58:59] offset:0 sc1\n"
      "v_mfma_f32_16x16x32_bf16 v[80:83], a[8:11], v[220:223], v[80:83]\n"
      "ds_read_b128 v[216:219], v234 offset:24576\n"
      "v_mfma_f32_16x16x32_bf16 v[84:87], a[12:15], v[220:223], v[84:87]\n"
      "v_mfma_f32_16x16x32_bf16 v[88:91], a[16:19], v[220:223], v[88:91]\n"
      "v_mfma_f32_16x16x32_bf16 v[92:95], a[20:23], v[220:223], v[92:95]\n"
      "global_load_dwordx4 a[228:231], v239, s[58:59] offset:1024 sc1\n"
      "s_waitcnt lgkmcnt(2)\n"
      "v_mfma_f32_16x16x32_bf16 v[96:99], a[0:3], v[224:227], v[96:99]\n"
      "v_mfma_f32_16x16x32_bf16 v[100:103], a[4:7], v[224:227], v[100:103]\n"
      "v_mfma_f32_16x16x32_bf16 v[104:107], a[8:11], v[224:227], v[104:107]\n"
      "ds_read_b128 v[220:223], v234 offset:28672\n"
      "v_mfma_f32_16x16x32_bf16 v[108:111], a[12:15], v[224:227], v[108:111]\n"
      "global_load_dwordx4 a[232:235], v239, s[58:59] offset:2048 sc1\n"
      "v_mfma_f32_16x16x32_bf16 v[112:115], a[16:19], v[224:227], v[112:115]\n"
      "v_mfma_f32_16x16x32_bf16 v[116:119], a[20:23], v[224:227], v[116:119]\n"
      "s_waitcnt lgkmcnt(2)\n"
      "v_mfma_f32_16x16x32_bf16 v[120:123], a[0:3], v[228:231], v[120:123]\n"
      "v_mfma_f32_16x16x32_bf16 v[124:127], a[4:7], v[228:231], v[124:127]\n"
      "global_load_dwordx4 a[236:239], v239, s[58:59] offset:3072 sc1\n"
      "v_mfma_f32_16x16x32_bf16 v[128:131], a[8:11], v[228:231], v[128:131]\n"
      "ds_read_b128 v[224:227], v234 offset:32768\n"
      "v_add_u32 v234, v249, v234\n"
      "v_mfma_f32_16x16x32_bf16 v[132:135], a[12:15], v[228:231], v[132:135]\n"
      "v_mfma_f32_16x16x32_bf16 v[136:139], a[16:19], v[228:231], v[136:139]\n"
      "v_mfma_f32_16x16x32_bf16 v[140:143], a[20:23], v[228:231], v[140:143]\n"
      "s_waitcnt lgkmcnt(2)\n"
      "v_mfma_f32_16x16x32_bf16 v[144:147], a[0:3], v[216:219], v[144:147]\n"
      "v_mfma_f32_16x16x32_bf16 v[148:151], a[4:7], v[216:219], v[148:151]\n"
      "v_mfma_f32_16x16x32_bf16 v[152:155], a[8:11], v[216:219], v[152:155]\n"
      "ds_read_b128 v[228:231], v235 offset:4096\n"
      "v_mfma_f32_16x16x32_bf16 v[156:159], a[12:15], v[216:219], v[156:159]\n"
      "v_mfma_f32_16x16x32_bf16 v[160:163], a[16:19], v[216:219], v[160:163]\n"
      "v_mfma_f32_16x16x32_bf16 v[164:167], a[20:23], v[216:219], v[164:167]\n"
      "s_waitcnt lgkmcnt(2)\n"
      "v_mfma_f32_16x16x32_bf16 v[168:171], a[0:3], v[220:223], v[168:171]\n"
      "v_mfma_f32_16x16x32_bf16 v[172:175], a[4:7], v[220:223], v[172:175]\n"
      "v_mfma_f32_16x16x32_bf16 v[176:179], a[8:11], v[220:223], v[176:179]\n"
      "ds_read_b128 v[216:219], v235 offset:8192\n"
      "v_mfma_f32_16x16x32_bf16 v[180:183], a[12:15], v[220:223], v[180:183]\n"
      "v_mfma_f32_16x16x32_bf16 v[184:187], a[16:19], v[220:223], v[184:187]\n"
      "v_mfma_f32_16x16x32_bf16 v[188:191], a[20:23], v[220:223], v[188:191]\n"
      "s_waitcnt lgkmcnt(2)\n"
      "v_mfma_f32_16x16x32_bf16 v[192:195], a[0:3], v[224:227], v[192:195]\n"
      "v_mfma_f32_16x16x32_bf16 v[196:199], a[4:7], v[224:227], v[196:199]\n"
      "v_mfma_f32_16x16x32_bf16 v[200:203], a[8:11], v[224:227], v[200:203]\n"
      "ds_read_b128 v[220:223], v235 offset:12288\n"
      "v_mfma_f32_16x16x32_bf16 v[204:207], a[12:15], v[224:227], v[204:207]\n"
      "v_mfma_f32_16x16x32_bf16 v[208:211], a[16:19], v[224:227], v[208:211]\n"
      "v_mfma_f32_16x16x32_bf16 v[212:215], a[20:23], v[224:227], v[212:215]\n"
      "s_waitcnt lgkmcnt(2)\n"
      "v_mfma_f32_16x16x32_bf16 v[24:27], a[24:27], v[228:231], v[24:27]\n"
      "s_add_u32 s58, s24, 0xd000\n"
      "s_addc_u32 s59, s25, 0\n"
      "global_load_dwordx4 a[0:3], v239, s[58:59] offset:0 sc1\n"
      "v_mfma_f32_16x16x32_bf16 v[28:31], a[28:31], v[228:231], v[28:31]\n"
      "v_mfma_f32_16x16x32_bf16 v[32:35], a[32:35], v[228:231], v[32:35]\n"
      "ds_read_b128 v[224:227], v235 offset:16384\n"
      "v_mfma_f32_16x16x32_bf16 v[36:39], a[36:39], v[228:231], v[36:39]\n"
      "v_mfma_f32_16x16x32_bf16 v[40:43], a[40:43], v[228:231], v[40:43]\n"
      "global_load_dwordx4 a[4:7], v239, s[58:59] offset:1024 sc1\n"
      "v_mfma_f32_16x16x32_bf16 v[44:47], a[44:47], v[228:231], v[44:47]\n"
      "s_waitcnt lgkmcnt(2)\n"
      "v_mfma_f32_16x16x32_bf16 v[48:51], a[24:27], v[216:219], v[48:51]\n"
      "v_cvt_pk_bf16_f32 v24, v24, v25\n"
      "v_cvt_pk_bf16_f32 v25, v26, v27\n"
      "v_mfma_f32_16x16x32_bf16 v[52:55], a[28:31], v[216:219], v[52:55]\n"
      "v_cvt_pk_bf16_f32 v26, v28, v29\n"
      "v_cvt_pk_bf16_f32 v27, v30, v31\n"
      "v_mfma_f32_16x16x32_bf16 v[56:59], a[32:35], v[216:219], v[56:59]\n"
      "ds_read_b128 v[228:231], v235 offset:20480\n"
      "v_cvt_pk_bf16_f32 v28, v32, v33\n"
      "v_cvt_pk_bf16_f32 v29, v34, v35\n"
      "global_load_dwordx4 a[8:11], v239, s[58:59] offset:2048 sc1\n"
      "v_mfma_f32_16x16x32_bf16 v[60:63], a[36:39], v[216:219], v[60:63]\n"
      "v_cvt_pk_bf16_f32 v30, v36, v37\n"
      "v_cvt_pk_bf16_f32 v31, v38, v39\n"
      "v_mfma_f32_16x16x32_bf16 v[64:67], a[40:43], v[216:219], v[64:67]\n"
      "v_cvt_pk_bf16_f32 v32, v40, v41\n"
      "v_cvt_pk_bf16_f32 v33, v42, v43\n"
      "v_mfma_f32_16x16x32_bf16 v[68:71], a[44:47], v[216:219], v[68:71]\n"
      "v_cvt_pk_bf16_f32 v34, v44, v45\n"
      "v_cvt_pk_bf16_f32 v35, v46, v47\n"
      "s_waitcnt lgkmcnt(2)\n"
      "v_mfma_f32_16x16x32_bf16 v[72:75], a[24:27], v[220:223], v[72:75]\n"
      "v_cvt_pk_bf16_f32 v48, v48, v49\n"
      "v_cvt_pk_bf16_f32 v49, v50, v51\n"
      "global_load_dwordx4 a[12:15], v239, s[58:59] offset:3072 sc1\n"
      "v_mfma_f32_16x16x32_bf16 v[76:79], a[28:31], v[220:223], v[76:79]\n"
      "v_cvt_pk_bf16_f32 v50, v52, v53\n"
      "v_cvt_pk_bf16_f32 v51, v54, v55\n"
      "v_mfma_f32_16x16x32_bf16 v[80:83], a[32:35], v[220:223], v[80:83]\n"
      "ds_read_b128 v[216:219], v235 offset:24576\n"
      "v_cvt_pk_bf16_f32 v52, v56, v57\n"
      "v_cvt_pk_bf16_f32 v53, v58, v59\n"
      "v_mfma_f32_16x16x32_bf16 v[84:87], a[36:39], v[220:223], v[84:87]\n"
      "v_cvt_pk_bf16_f32 v54, v60, v61\n"
      "v_cvt_pk_bf16_f32 v55, v62, v63\n"
      "v_mfma_f32_16x16x32_bf16 v[88:91], a[40:43], v[220:223], v[88:91]\n"
      "v_cvt_pk_bf16_f32 v56, v64, v65\n"
      "v_cvt_pk_bf16_f32 v57, v66, v67\n"
      "s_add_u32 s58, s24, 0xe000\n"
      "s_addc_u32 s59, s25, 0\n"
      "global_load_dwordx4 a[16:19], v239, s[58:59] offset:0 sc1\n"
      "v_mfma_f32_16x16x32_bf16 v[92:95], a[44:47], v[220:223], v[92:95]\n"
      "v_cvt_pk_bf16_f32 v58, v68, v69\n"
      "v_cvt_pk_bf16_f32 v59, v70, v71\n"
      "s_waitcnt lgkmcnt(2)\n"
      "v_mfma_f32_16x16x32_bf16 v[96:99], a[24:27], v[224:227], v[96:99]\n"
      "v_cvt_pk_bf16_f32 v72, v72, v73\n"
      "v_cvt_pk_bf16_f32 v73, v74, v75\n"
      "v_mfma_f32_16x16x32_bf16 v[100:103], a[28:31], v[224:227], v[100:103]\n"
      "v_cvt_pk_bf16_f32 v74, v76, v77\n"
      "v_cvt_pk_bf16_f32 v75, v78, v79\n"
      "v_mfma_f32_16x16x32_bf16 v[104:107], a[32:35], v[224:227], v[104:107]\n"
      "ds_read_b128 v[220:223], v235 offset:28672\n"
      "v_cvt_pk_bf16_f32 v76, v80, v81\n"
      "v_cvt_pk_bf16_f32 v77, v82, v83\n"
      "global_load_dwordx4 a[20:23], v239, s[58:59] offset:1024 sc1\n"
      "v_mfma_f32_16x16x32_bf16 v[108:111], a[36:39], v[224:227], v[108:111]\n"
      "v_cvt_pk_bf16_f32 v78, v84, v85\n"
      "v_cvt_pk_bf16_f32 v79, v86, v87\n"
      "v_mfma_f32_16x16x32_bf16 v[112:115], a[40:43], v[224:227], v[112:115]\n"
      "v_cvt_pk_bf16_f32 v80, v88, v89\n"
      "v_cvt_pk_bf16_f32 v81, v90, v91\n"
      "v_mfma_f32_16x16x32_bf16 v[116:119], a[44:47], v[224:227], v[116:119]\n"
      "v_cvt_pk_bf16_f32 v82, v92, v93\n"
      "v_cvt_pk_bf16_f32 v83, v94, v95\n"
      "s_waitcnt lgkmcnt(2)\n"
      "v_mfma_f32_16x16x32_bf16 v[120:123], a[24:27], v[228:231], v[120:123]\n"
      "v_cvt_pk_bf16_f32 v96, v96, v97\n"
      "v_cvt_pk_bf16_f32 v97, v98, v99\n"
      "v_mfma_f32_16x16x32_bf16 v[124:127], a[28:31], v[228:231], v[124:127]\n"
      "v_cvt_pk_bf16_f32 v98, v100, v101\n"
      "v_cvt_pk_bf16_f32 v99, v102, v103\n"
      "v_mfma_f32_16x16x32_bf16 v[128:131], a[32:35], v[228:231], v[128:131]\n"
      "ds_read_b128 v[224:227], v235 offset:32768\n"
      "v_cvt_pk_bf16_f32 v100, v104, v105\n"
      "v_cvt_pk_bf16_f32 v101, v106, v107\n"
      "v_mfma_f32_16x16x32_bf16 v[132:135], a[36:39], v[228:231], v[132:135]\n"
      "v_cvt_pk_bf16_f32 v102, v108, v109\n"
      "v_cvt_pk_bf16_f32 v103, v110, v111\n"
      "v_mfma_f32_16x16x32_bf16 v[136:139], a[40:43], v[228:231], v[136:139]\n"
      "v_cvt_pk_bf16_f32 v104, v112, v113\n"
      "v_cvt_pk_bf16_f32 v105, v114, v115\n"
      "v_mfma_f32_16x16x32_bf16 v[140:143], a[44:47], v[228:231], v[140:143]\n"
      "v_cvt_pk_bf16_f32 v106, v116, v117\n"
      "v_cvt_pk_bf16_f32 v107, v118, v119\n"
      "s_waitcnt lgkmcnt(2)\n"
      "v_mfma_f32_16x16x32_bf16 v[144:147], a[24:27], v[216:219], v[144:147]\n"
      "v_cvt_pk_bf16_f32 v120, v120, v121\n"
      "v_cvt_pk_bf16_f32 v121, v122, v123\n"
      "v_mfma_f32_16x16x32_bf16 v[148:151], a[28:31], v[216:219], v[148:151]\n"
      "v_cvt_pk_bf16_f32 v122, v124, v125\n"
      "v_cvt_pk_bf16_f32 v123, v126, v127\n"
      "v_mfma_f32_16x16x32_bf16 v[152:155], a[32:35], v[216:219], v[152:155]\n"
      "ds_read_b128 v[228:231], v235 offset:0\n"
      "v_add_u32 v235, v249, v235\n"
      "s_waitcnt vmcnt(24)\n"
      "s_barrier\n"
      "v_cvt_pk_bf16_f32 v124, v128, v129\n"
      "v_cvt_pk_bf16_f32 v125, v130, v131\n"
      "v_mfma_f32_16x16x32_bf16 v[156:159], a[36:39], v[216:219], v[156:159]\n"
      "ds_read_b128 v[36:39], v232 offset:0\n"
      "v_cvt_pk_bf16_f32 v126, v132, v133\n"
      "v_cvt_pk_bf16_f32 v127, v134, v135\n"
      "v_mfma_f32_16x16x32_bf16 v[160:163], a[40:43], v[216:219], v[160:163]\n"
      "v_cvt_pk_bf16_f32 v128, v136, v137\n"
      "v_cvt_pk_bf16_f32 v129, v138, v139\n"
      "v_mfma_f32_16x16x32_bf16 v[164:167], a[44:47], v[216:219], v[164:167]\n"
      "ds_read_b128 v[40:43], v233 offset:0\n"
      "v_cvt_pk_bf16_f32 v130, v140, v141\n"
      "v_cvt_pk_bf16_f32 v131, v142, v143\n"
      "s_waitcnt lgkmcnt(4)\n"
      "v_mfma_f32_16x16x32_bf16 v[168:171], a[24:27], v[220:223], v[168:171]\n"
      "v_cvt_pk_bf16_f32 v144, v144, v145\n"
      "v_cvt_pk_bf16_f32 v145, v146, v147\n"
      "global_load_dwordx4 v[140:143], v239, s[58:59] offset:2048 sc1\n"
      "v_mfma_f32_16x16x32_bf16 v[172:175], a[28:31], v[220:223], v[172:175]\n"
      "ds_read_b128 v[44:47], v234 offset:0\n"
      "v_cvt_pk_bf16_f32 v146, v148, v149\n"
      "v_cvt_pk_bf16_f32 v147, v150, v151\n"
      "v_mfma_f32_16x16x32_bf16 v[176:179], a[32:35], v[220:223], v[176:179]\n"
      "v_cvt_pk_bf16_f32 v148, v152, v153\n"
      "v_cvt_pk_bf16_f32 v149, v154, v155\n"
      "v_mfma_f32_16x16x32_bf16 v[180:183], a[36:39], v[220:223], v[180:183]\n"
      "ds_read_b128 v[60:63], v235 offset:0\n"
      "v_cvt_pk_bf16_f32 v150, v156, v157\n"
      "v_cvt_pk_bf16_f32 v151, v158, v159\n"
      "v_mfma_f32_16x16x32_bf16 v[184:187], a[40:43], v[220:223], v[184:187]\n"
      "v_cvt_pk_bf16_f32 v152, v160, v161\n"
      "v_cvt_pk_bf16_f32 v153, v162, v163\n"
      "v_mfma_f32_16x16x32_bf16 v[188:191], a[44:47], v[220:223], v[188:191]\n"
      "ds_read_b128 v[64:67], v232 offset:8192\n"
      "v_cvt_pk_bf16_f32 v154, v164, v165\n"
      "v_cvt_pk_bf16_f32 v155, v166, v167\n"
      "s_waitcnt lgkmcnt(6)\n"
      "v_mfma_f32_16x16x32_bf16 v[192:195], a[24:27], v[224:227], v[192:195]\n"
      "v_cvt_pk_bf16_f32 v168, v168, v169\n"
      "v_cvt_pk_bf16_f32 v169, v170, v171\n"
      "global_load_dwordx4 v[156:159], v239, s[58:59] offset:3072 sc1\n"
      "v_mfma_f32_16x16x32_bf16 v[196:199], a[28:31], v[224:227], v[196:199]\n"
      "ds_read_b128 v[68:71], v233 offset:8192\n"
      "v_cvt_pk_bf16_f32 v170, v172, v173\n"
      "v_cvt_pk_bf16_f32 v171, v174, v175\n"
      "v_mfma_f32_16x16x32_bf16 v[200:203], a[32:35], v[224:227], v[200:203]\n"
      "v_cvt_pk_bf16_f32 v172, v176, v177\n"
      "v_cvt_pk_bf16_f32 v173, v178, v179\n"
      "v_mfma_f32_16x16x32_bf16 v[204:207], a[36:39], v[224:227], v[204:207]\n"
      "ds_read_b128 v[84:87], v234 offset:8192\n"
      "v_cvt_pk_bf16_f32 v174, v180, v181\n"
      "v_cvt_pk_bf16_f32 v175, v182, v183\n"
      "v_mfma_f32_16x16x32_bf16 v[208:211], a[40:43], v[224:227], v[208:211]\n"
      "v_cvt_pk_bf16_f32 v176, v184, v185\n"
      "v_cvt_pk_bf16_f32 v177, v186, v187\n"
      "s_add_u32 s58, s24, 0xf000\n"
      "s_addc_u32 s59, s25, 0\n"
      "global_load_dwordx4 v[160:163], v239, s[58:59] offset:0 sc1\n"
      "v_mfma_f32_16x16x32_bf16 v[212:215], a[44:47], v[224:227], v[212:215]\n"
      "ds_read_b128 v[88:91], v235 offset:8192\n"
      "v_cvt_pk_bf16_f32 v178, v188, v189\n"
      "v_cvt_pk_bf16_f32 v179, v190, v191\n"
      "s_waitcnt lgkmcnt(8)\n"
      "v_mfma_f32_16x16x32_bf16 v[0:3], a[24:27], v[228:231], v[0:3]\n"
      "v_cvt_pk_bf16_f32 v192, v192, v193\n"
      "v_cvt_pk_bf16_f32 v193, v194, v195\n"
      "v_mfma_f32_16x16x32_bf16 v[4:7], a[28:31], v[228:231], v[4:7]\n"
      "ds_read_b128 v[92:95], v232 offset:16384\n"
      "v_cvt_pk_bf16_f32 v194, v196, v197\n"
      "v_cvt_pk_bf16_f32 v195, v198, v199\n"
      "v_mfma_f32_16x16x32_bf16 v[8:11], a[32:35], v[228:231], v[8:11]\n"
      "v_cvt_pk_bf16_f32 v196, v200, v201\n"
      "v_cvt_pk_bf16_f32 v197, v202, v203\n"
      "global_load_dwordx4 v[164:167], v239, s[58:59] offset:1024 sc1\n"
      "v_mfma_f32_16x16x32_bf16 v[12:15], a[36:39], v[228:231], v[12:15]\n"
      "ds_read_b128 v[108:111], v233 offset:16384\n"
      "v_cvt_pk_bf16_f32 v198, v204, v205\n"
      "v_cvt_pk_bf16_f32 v199, v206, v207\n"
      "v_mfma_f32_16x16x32_bf16 v[16:19], a[40:43], v[228:231], v[16:19]\n"
      "ds_read_b128 v[112:115], v234 offset:16384\n"
      "v_cvt_pk_bf16_f32 v200, v208, v209\n"
      "v_cvt_pk_bf16_f32 v201, v210, v211\n"
      "v_mfma_f32_16x16x32_bf16 v[20:23], a[44:47], v[228:231], v[20:23]\n"
      "ds_read_b128 v[116:119], v235 offset:16384\n"
      "v_cvt_pk_bf16_f32 v202, v212, v213\n"
      "v_cvt_pk_bf16_f32 v203, v214, v215\n"
      "s_waitcnt lgkmcnt(8)\n"
      "v_mfma_f32_16x16x32_bf16 v[132:135], a[240:243], v[36:39], v[0:3]\n"
      "global_load_dwordx4 v[180:183], v239, s[58:59] offset:2048 sc1\n"
      "s_mov_b64 vcc, 0\n"
      "v_mfma_f32_16x16x32_bf16 v[132:135], a[244:247], v[40:43], v[132:135]\n"
      "v_mfma_f32_16x16x32_bf16 v[132:135], a[248:251], v[44:47], v[132:135]\n"
      "v_mfma_f32_16x16x32_bf16 v[132:135], a[252:255], v[60:63], v[132:135]\n"
      "global_load_dwordx4 v[184:187], v239, s[58:59] offset:3072 sc1\n"
      "s_waitcnt lgkmcnt(4)\n"
      "v_mfma_f32_16x16x32_bf16 v[132:135], a[48:51], v[64:67], v[132:135]\n"
      "v_mfma_f32_16x16x32_bf16 v[132:135], a[52:55], v[68:71], v[132:135]\n"
      "v_mfma_f32_16x16x32_bf16 v[132:135], a[56:59], v[84:87], v[132:135]\n"
      "s_add_u32 s58, s24, 0x10000\n"
      "s_addc_u32 s59, s25, 0\n"
      "global_load_dwordx4 v[188:191], v239, s[58:59] offset:0 sc1\n"
      "v_mfma_f32_16x16x32_bf16 v[132:135], a[60:63], v[88:91], v[132:135]\n"
      "s_waitcnt lgkmcnt(0)\n"
      "v_mfma_f32_16x16x32_bf16 v[132:135], a[64:67], v[92:95], v[132:135]\n"
      "v_mfma_f32_16x16x32_bf16 v[132:135], a[68:71], v[108:111], v[132:135]\n"
      "global_load_dwordx4 v[204:207], v239, s[58:59] offset:1024 sc1\n"
      "v_mfma_f32_16x16x32_bf16 v[132:135], a[72:75], v[112:115], v[132:135]\n"
      "v_mfma_f32_16x16x32_bf16 v[132:135], a[76:79], v[116:119], v[132:135]\n"
      "v_mfma_f32_16x16x32_bf16 v[136:139], a[80:83], v[36:39], v[4:7]\n"
      "global_load_dwordx4 a[24:27], v239, s[58:59] offset:2048 sc1\n"
      "v_mfma_f32_16x16x32_bf16 v[136:139], a[84:87], v[40:43], v[136:139]\n"
      "v_mfma_f32_16x16x32_bf16 v[136:139], a[88:91], v[44:47], v[136:139]\n"
      "v_mfma_f32_16x16x32_bf16 v[136:139], a[92:95], v[60:63], v[136:139]\n"
      "global_load_dwordx4 a[28:31], v239, s[58:59] offset:3072 sc1\n"
      "v_mfma_f32_16x16x32_bf16 v[136:139], a[96:99], v[64:67], v[136:139]\n"
      "v_cvt_pk_bf16_f32 v208, v132, v133\n"
      "v_mfma_f32_16x16x32_bf16 v[136:139], a[100:103], v[68:71], v[136:139]\n"
      "v_cvt_pk_bf16_f32 v209, v134, v135\n"
      "v_mfma_f32_16x16x32_bf16 v[136:139], a[104:107], v[84:87], v[136:139]\n"
      "s_add_u32 s58, s24, 0x11000\n"
      "s_addc_u32 s59, s25, 0\n"
      "global_load_dwordx4 a[32:35], v239, s[58:59] offset:0 sc1\n"
      "v_mfma_f32_16x16x32_bf16 v[136:139], a[108:111], v[88:91], v[136:139]\n"
      "v_mfma_f32_16x16x32_bf16 v[136:139], a[112:115], v[92:95], v[136:139]\n"
      "s_add_u32 s68, s83, 1160\n"
      "v_mfma_f32_16x16x32_bf16 v[136:139], a[116:119], v[108:111], v[136:139]\n"
      "global_load_dwordx4 a[36:39], v239, s[58:59] offset:1024 sc1\n"
      "v_mfma_f32_16x16x32_bf16 v[136:139], a[120:123], v[112:115], v[136:139]\n"
      "ds_write_b64 v244, v[208:209] offset:12288\n"
      "s_add_u32 s67, s68, 150\n"
      "v_mfma_f32_16x16x32_bf16 v[136:139], a[124:127], v[116:119], v[136:139]\n"
      "v_mfma_f32_16x16x32_bf16 v[132:135], a[128:131], v[36:39], v[8:11]\n"
      "global_load_dwordx4 a[40:43], v239, s[58:59] offset:2048 sc1\n"
      "s_mov_b64 s[70:71], 0\n"
      "v_mfma_f32_16x16x32_bf16 v[132:135], a[132:135], v[40:43], v[132:135]\n"
      "v_mfma_f32_16x16x32_bf16 v[132:135], a[136:139], v[44:47], v[132:135]\n"
      "v_mfma_f32_16x16x32_bf16 v[132:135], a[140:143], v[60:63], v[132:135]\n"
      "global_load_dwordx4 a[44:47], v239, s[58:59] offset:3072 sc1\n"
      "v_mfma_f32_16x16x32_bf16 v[132:135], a[144:147], v[64:67], v[132:135]\n"
      "v_cvt_pk_bf16_f32 v210, v136, v137\n"
      "v_mfma_f32_16x16x32_bf16 v[132:135], a[148:151], v[68:71], v[132:135]\n"
      "v_cvt_pk_bf16_f32 v211, v138, v139\n"
      "v_mfma_f32_16x16x32_bf16 v[132:135], a[152:155], v[84:87], v[132:135]\n"
      "s_mul_i32 s78, s90, s19\n"
      "v_mfma_f32_16x16x32_bf16 v[132:135], a[156:159], v[88:91], v[132:135]\n"
      "v_add_u32 v236, s78, v236\n"
      "v_mfma_f32_16x16x32_bf16 v[132:135], a[160:163], v[92:95], v[132:135]\n"
      "s_mul_i32 s78, s78, s99\n"
      "v_mfma_f32_16x16x32_bf16 v[132:135], a[164:167], v[108:111], v[132:135]\n"
      "v_add_u32 v237, s78, v237\n"
      "v_mfma_f32_16x16x32_bf16 v[132:135], a[168:171], v[112:115], v[132:135]\n"
      "s_add_u32 s52, s20, s33\n"
      "s_addc_u32 s53, s21, 0\n"
      "s_add_u32 m0, s17, 0xc000\n"
      "v_mfma_f32_16x16x32_bf16 v[132:135], a[172:175], v[116:119], v[132:135]\n"
      "global_load_lds_dwordx4 v236, s[52:53] offset:0 \n"
      "v_mfma_f32_16x16x32_bf16 v[136:139], a[176:179], v[36:39], v[12:15]\n"
      "ds_write_b64 v244, v[210:211] offset:12296\n"
      "v_mfma_f32_16x16x32_bf16 v[136:139], a[180:183], v[40:43], v[136:139]\n"
      "v_mfma_f32_16x16x32_bf16 v[136:139], a[184:187], v[44:47], v[136:139]\n"
      "v_mfma_f32_16x16x32_bf16 v[136:139], a[188:191], v[60:63], v[136:139]\n"
      "v_cvt_pk_bf16_f32 v212, v132, v133\n"
      "s_waitcnt vmcnt(35)\n"
      "v_mfma_f32_16x16x32_bf16 v[136:139], a[192:195], v[64:67], v[136:139]\n"
      "s_add_u32 m0, s17, 0xdf00\n"
      "v_mfma_f32_16x16x32_bf16 v[136:139], a[196:199], v[68:71], v[136:139]\n"
      "global_load_lds_dwordx4 v236, s[52:53] offset:256 \n"
      "v_mfma_f32_16x16x32_bf16 v[136:139], a[200:203], v[84:87], v[136:139]\n"
      "v_cvt_pk_bf16_f32 v213, v134, v135\n"
      "v_mfma_f32_16x16x32_bf16 v[136:139], a[204:207], v[88:91], v[136:139]\n"
      "s_waitcnt vmcnt(26)\n"
      "v_mfma_f32_16x16x32_bf16 v[136:139], a[208:211], v[92:95], v[136:139]\n"
      "v_mfma_f32_16x16x32_bf16 v[136:139], a[212:215], v[108:111], v[136:139]\n"
      "v_mfma_f32_16x16x32_bf16 v[136:139], a[216:219], v[112:115], v[136:139]\n"
      "s_add_u32 m0, s17, 0xfe00\n"
      "v_mfma_f32_16x16x32_bf16 v[136:139], a[220:223], v[116:119], v[136:139]\n"
      "global_load_lds_dwordx4 v236, s[52:53] offset:512 \n"
      "s_waitcnt vmcnt(23)\n"
      "v_mfma_f32_16x16x32_bf16 v[132:135], a[224:227], v[36:39], v[16:19]\n"
      "ds_write_b64 v245, v[212:213] offset:12288\n"
      "v_mfma_f32_16x16x32_bf16 v[132:135], a[228:231], v[40:43], v[132:135]\n"
      "v_mfma_f32_16x16x32_bf16 v[132:135], a[232:235], v[44:47], v[132:135]\n"
      "v_mfma_f32_16x16x32_bf16 v[132:135], a[236:239], v[60:63], v[132:135]\n"
      "v_cvt_pk_bf16_f32 v214, v136, v137\n"
      "s_waitcnt vmcnt(19)\n"
      "v_mfma_f32_16x16x32_bf16 v[132:135], a[0:3], v[64:67], v[132:135]\n"
      "s_add_u32 m0, s17, 0xd000\n"
      "v_mfma_f32_16x16x32_bf16 v[132:135], a[4:7], v[68:71], v[132:135]\n"
      "global_load_lds_dwordx4 v237, s[52:53] offset:0 \n"
      "v_mfma_f32_16x16x32_bf16 v[132:135], a[8:11], v[84:87], v[132:135]\n"
      "v_cvt_pk_bf16_f32 v215, v138, v139\n"
      "v_mfma_f32_16x16x32_bf16 v[132:135], a[12:15], v[88:91], v[132:135]\n"
      "s_waitcnt vmcnt(16)\n"
      "v_mfma_f32_16x16x32_bf16 v[132:135], a[16:19], v[92:95], v[132:135]\n"
      "v_mfma_f32_16x16x32_bf16 v[132:135], a[20:23], v[108:111], v[132:135]\n"
      "v_mfma_f32_16x16x32_bf16 v[132:135], v[140:143], v[112:115], v[132:135]\n"
      "s_add_u32 m0, s17, 0xef00\n"
      "v_mfma_f32_16x16x32_bf16 v[132:135], v[156:159], v[116:119], v[132:135]\n"
      "s_barrier\n"
      "global_load_lds_dwordx4 v237, s[52:53] offset:256 \n"
      "s_waitcnt vmcnt(13)\n"
      "v_mfma_f32_16x16x32_bf16 v[136:139], v[160:163], v[36:39], v[20:23]\n"
      "ds_read_b128 v[36:39], v232 offset:256\n"
      "ds_write_b64 v245, v[214:215] offset:12296\n"
      "v_mfma_f32_16x16x32_bf16 v[136:139], v[164:167], v[40:43], v[136:139]\n"
      "ds_read_b128 v[40:43], v233 offset:256\n"
      "v_mfma_f32_16x16x32_bf16 v[136:139], v[180:183], v[44:47], v[136:139]\n"
      "ds_read_b128 v[44:47], v234 offset:256\n"
      "v_mfma_f32_16x16x32_bf16 v[136:139], v[184:187], v[60:63], v[136:139]\n"
      "ds_read_b128 v[60:63], v235 offset:256\n"
      "v_cvt_pk_bf16_f32 v208, v132, v133\n"
      "s_waitcnt vmcnt(9)\n"
      "v_mfma_f32_16x16x32_bf16 v[136:139], v[188:191], v[64:67], v[136:139]\n"
      "ds_read_b128 v[64:67], v232 offset:8448\n"
      "s_add_u32 m0, s17, 0x10e00\n"
      "v_mfma_f32_16x16x32_bf16 v[136:139], v[204:207], v[68:71], v[136:139]\n"
      "ds_read_b128 v[68:71], v233 offset:8448\n"
      "global_load_lds_dwordx4 v237, s[52:53] offset:512 \n"
      "v_mfma_f32_16x16x32_bf16 v[136:139], a[24:27], v[84:87], v[136:139]\n"
      "ds_read_b128 v[84:87], v234 offset:8448\n"
      "v_cvt_pk_bf16_f32 v209, v134, v135\n"
      "v_mfma_f32_16x16x32_bf16 v[136:139], a[28:31], v[88:91], v[136:139]\n"
      "ds_read_b128 v[88:91], v235 offset:8448\n"
      "s_waitcnt vmcnt(6)\n"
      "v_mfma_f32_16x16x32_bf16 v[136:139], a[32:35], v[92:95], v[136:139]\n"
      "ds_read_b128 v[92:95], v232 offset:16640\n"
      "v_mfma_f32_16x16x32_bf16 v[136:139], a[36:39], v[108:111], v[136:139]\n"
      "ds_read_b128 v[108:111], v233 offset:16640\n"
      "v_mfma_f32_16x16x32_bf16 v[136:139], a[40:43], v[112:115], v[136:139]\n"
      "ds_read_b128 v[112:115], v234 offset:16640\n"
      "v_mfma_f32_16x16x32_bf16 v[136:139], a[44:47], v[116:119], v[136:139]\n"
      "ds_read_b128 v[116:119], v235 offset:16640\n"
      "s_waitcnt lgkmcnt(8)\n"
      "v_mfma_f32_16x16x32_bf16 v[132:135], a[240:243], v[36:39], v[0:3]\n"
      "s_add_u32 s52, s42, s33\n"
      "s_addc_u32 s53, s43, 0\n"
      "s_add_u32 m0, s17, 0x12000\n"
      "v_mfma_f32_16x16x32_bf16 v[132:135], a[244:247], v[40:43], v[132:135]\n"
      "global_load_lds_dwordx4 v236, s[52:53] offset:0 \n"
      "v_mfma_f32_16x16x32_bf16 v[132:135], a[248:251], v[44:47], v[132:135]\n"
      "ds_write_b64 v244, v[208:209] offset:12416\n"
      "v_mfma_f32_16x16x32_bf16 v[132:135], a[252:255], v[60:63], v[132:135]\n"
      "v_cvt_pk_bf16_f32 v210, v136, v137\n"
      "s_waitcnt lgkmcnt(5)\n"
      "v_mfma_f32_16x16x32_bf16 v[132:135], a[48:51], v[64:67], v[132:135]\n"
      "v_cvt_pk_bf16_f32 v211, v138, v139\n"
      "v_mfma_f32_16x16x32_bf16 v[132:135], a[52:55], v[68:71], v[132:135]\n"
      "v_mfma_f32_16x16x32_bf16 v[132:135], a[56:59], v[84:87], v[132:135]\n"
      "s_add_u32 m0, s17, 0x13f00\n"
      "LPD3:\n"
      "s_cbranch_execz LPD9\n"
      "v_mfma_f32_16x16x32_bf16 v[132:135], a[60:63], v[88:91], v[132:135]\n"
      "global_load_lds_dwordx4 v236, s[52:53] offset:256 \n"
      "s_waitcnt lgkmcnt(1)\n"
      "v_mfma_f32_16x16x32_bf16 v[132:135], a[64:67], v[92:95], v[132:135]\n"
      "v_mfma_f32_16x16x32_bf16 v[132:135], a[68:71], v[108:111], v[132:135]\n"
      "ds_write_b64 v244, v[210:211] offset:12424\n"
      "v_mfma_f32_16x16x32_bf16 v[132:135], a[72:75], v[112:115], v[132:135]\n"
      "v_mfma_f32_16x16x32_bf16 v[132:135], a[76:79], v[116:119], v[132:135]\n"
      "v_mfma_f32_16x16x32_bf16 v[136:139], a[80:83], v[36:39], v[4:7]\n"
      "s_add_u32 m0, s17, 0x15e00\n"
      "v_mfma_f32_16x16x32_bf16 v[136:139], a[84:87], v[40:43], v[136:139]\n"
      "global_load_lds_dwordx4 v236, s[52:53] offset:512 \n"
      "v_mfma_f32_16x16x32_bf16 v[136:139], a[88:91], v[44:47], v[136:139]\n"
      "v_mfma_f32_16x16x32_bf16 v[136:139], a[92:95], v[60:63], v[136:139]\n"
      "v_cvt_pk_bf16_f32 v208, v132, v133\n"
      "v_mfma_f32_16x16x32_bf16 v[136:139], a[96:99], v[64:67], v[136:139]\n"
      "v_cvt_pk_bf16_f32 v209, v134, v135\n"
      "v_mfma_f32_16x16x32_bf16 v[136:139], a[100:103], v[68:71], v[136:139]\n"
      "v_mfma_f32_16x16x32_bf16 v[136:139], a[104:107], v[84:87], v[136:139]\n"
      "s_add_u32 m0, s17, 0x13000\n"
      "v_mfma_f32_16x16x32_bf16 v[136:139], a[108:111], v[88:91], v[136:139]\n"
      "global_load_lds_dwordx4 v237, s[52:53] offset:0 \n"
      "v_mfma_f32_16x16x32_bf16 v[136:139], a[112:115], v[92:95], v[136:139]\n"
      "v_mfma_f32_16x16x32_bf16 v[136:139], a[116:119], v[108:111], v[136:139]\n"
      "ds_write_b64 v244, v[208:209] offset:24576\n"
      "v_mfma_f32_16x16x32_bf16 v[136:139], a[120:123], v[112:115], v[136:139]\n"
      "v_mfma_f32_16x16x32_bf16 v[136:139], a[124:127], v[116:119], v[136:139]\n"
      "v_mfma_f32_16x16x32_bf16 v[132:135], a[128:131], v[36:39], v[8:11]\n"
      "s_add_u32 m0, s17, 0x14f00\n"
      "v_mfma_f32_16x16x32_bf16 v[132:135], a[132:135], v[40:43], v[132:135]\n"
      "global_load_lds_dwordx4 v237, s[52:53] offset:256 \n"
      "v_mfma_f32_16x16x32_bf16 v[132:135], a[136:139], v[44:47], v[132:135]\n"
      "v_mfma_f32_16x16x32_bf16 v[132:135], a[140:143], v[60:63], v[132:135]\n"
      "v_cvt_pk_bf16_f32 v210, v136, v137\n"
      "v_mfma_f32_16x16x32_bf16 v[132:135], a[144:147], v[64:67], v[132:135]\n"
      "v_cvt_pk_bf16_f32 v211, v138, v139\n"
      "v_mfma_f32_16x16x32_bf16 v[132:135], a[148:151], v[68:71], v[132:135]\n"
      "v_mfma_f32_16x16x32_bf16 v[132:135], a[152:155], v[84:87], v[132:135]\n"
      "s_add_u32 m0, s17, 0x16e00\n"
      "v_mfma_f32_16x16x32_bf16 v[132:135], a[156:159], v[88:91], v[132:135]\n"
      "global_load_lds_dwordx4 v237, s[52:53] offset:512 \n"
      "v_mfma_f32_16x16x32_bf16 v[132:135], a[160:163], v[92:95], v[132:135]\n"
      "v_mfma_f32_16x16x32_bf16 v[132:135], a[164:167], v[108:111], v[132:135]\n"
      "ds_write_b64 v244, v[210:211] offset:24584\n"
      "v_mfma_f32_16x16x32_bf16 v[132:135], a[168:171], v[112:115], v[132:135]\n"
      "v_mfma_f32_16x16x32_bf16 v[132:135], a[172:175], v[116:119], v[132:135]\n"
      "v_mfma_f32_16x16x32_bf16 v[136:139], a[176:179], v[36:39], v[12:15]\n"
      "v_mfma_f32_16x16x32_bf16 v[136:139], a[180:183], v[40:43], v[136:139]\n"
      "v_mfma_f32_16x16x32_bf16 v[136:139], a[184:187], v[44:47], v[136:139]\n"
      "v_mfma_f32_16x16x32_bf16 v[136:139], a[188:191], v[60:63], v[136:139]\n"
      "v_cvt_pk_bf16_f32 v212, v132, v133\n"
      "v_mfma_f32_16x16x32_bf16 v[136:139], a[192:195], v[64:67], v[136:139]\n"
      "v_cvt_pk_bf16_f32 v213, v134, v135\n"
      "v_mfma_f32_16x16x32_bf16 v[136:139], a[196:199], v[68:71], v[136:139]\n"
      "v_mfma_f32_16x16x32_bf16 v[136:139], a[200:203], v[84:87], v[136:139]\n"
      "v_mfma_f32_16x16x32_bf16 v[136:139], a[204:207], v[88:91], v[136:139]\n"
      "v_mfma_f32_16x16x32_bf16 v[136:139], a[208:211], v[92:95], v[136:139]\n"
      "v_mfma_f32_16x16x32_bf16 v[136:139], a[212:215], v[108:111], v[136:139]\n"
      "ds_write_b64 v245, v[212:213] offset:24576\n"
      "v_mfma_f32_16x16x32_bf16 v[136:139], a[216:219], v[112:115], v[136:139]\n"
      "v_mfma_f32_16x16x32_bf16 v[136:139], a[220:223], v[116:119], v[136:139]\n"
      "v_mfma_f32_16x16x32_bf16 v[132:135], a[224:227], v[36:39], v[16:19]\n"
      "v_mfma_f32_16x16x32_bf16 v[132:135], a[228:231], v[40:43], v[132:135]\n"
      "v_mfma_f32_16x16x32_bf16 v[132:135], a[232:235], v[44:47], v[132:135]\n"
      "v_mfma_f32_16x16x32_bf16 v[132:135], a[236:239], v[60:63], v[132:135]\n"
      "s_memrealtime s[84:85]\n"
      "v_mfma_f32_16x16x32_bf16 v[132:135], a[0:3], v[64:67], v[132:135]\n"
      "v_cvt_pk_bf16_f32 v214, v136, v137\n"
      "v_mfma_f32_16x16x32_bf16 v[132:135], a[4:7], v[68:71], v[132:135]\n"
      "v_cvt_pk_bf16_f32 v215, v138, v139\n"
      "v_mfma_f32_16x16x32_bf16 v[132:135], a[8:11], v[84:87], v[132:135]\n"
      "v_mfma_f32_16x16x32_bf16 v[132:135], a[12:15], v[88:91], v[132:135]\n"
      "v_mfma_f32_16x16x32_bf16 v[132:135], a[16:19], v[92:95], v[132:135]\n"
      "v_mfma_f32_16x16x32_bf16 v[132:135], a[20:23], v[108:111], v[132:135]\n"
      "v_mfma_f32_16x16x32_bf16 v[132:135], v[140:143], v[112:115], v[132:135]\n"
      "s_waitcnt lgkmcnt(0)\n"
      "s_mov_b32 s66, s84\n"
      "v_mfma_f32_16x16x32_bf16 v[132:135], v[156:159], v[116:119], v[132:135]\n"
      "s_barrier\n"
      "ds_write_b64 v245, v[214:215] offset:24584\n"
      "v_mfma_f32_16x16x32_bf16 v[136:139], v[160:163], v[36:39], v[20:23]\n"
      "ds_read_b128 v[36:39], v232 offset:24576\n"
      "s_lshl_b32 s79, s98, 1\n"
      "v_mfma_f32_16x16x32_bf16 v[136:139], v[164:167], v[40:43], v[136:139]\n"
      "ds_read_b128 v[40:43], v233 offset:24576\n"
      "v_mad_i32_i24 v252, v251, s79, v252\n"
      "v_mfma_f32_16x16x32_bf16 v[136:139], v[180:183], v[44:47], v[136:139]\n"
      "ds_read_b128 v[44:47], v234 offset:24576\n"
      "s_mul_i32 s79, s79, s100\n"
      "v_mfma_f32_16x16x32_bf16 v[136:139], v[184:187], v[60:63], v[136:139]\n"
      "ds_read_b128 v[60:63], v235 offset:24576\n"
      "v_cvt_pk_bf16_f32 v208, v132, v133\n"
      "v_mfma_f32_16x16x32_bf16 v[136:139], v[188:191], v[64:67], v[136:139]\n"
      "ds_read_b128 v[64:67], v232 offset:32768\n"
      "v_cvt_pk_bf16_f32 v209, v134, v135\n"
      "v_mfma_f32_16x16x32_bf16 v[136:139], v[204:207], v[68:71], v[136:139]\n"
      "ds_read_b128 v[68:71], v233 offset:32768\n"
      "v_add_u32 v247, s79, v247\n"
      "v_mfma_f32_16x16x32_bf16 v[136:139], a[24:27], v[84:87], v[136:139]\n"
      "ds_read_b128 v[84:87], v234 offset:32768\n"
      "s_mul_i32 s79, s79, s101\n"
      "v_mfma_f32_16x16x32_bf16 v[136:139], a[28:31], v[88:91], v[136:139]\n"
      "ds_read_b128 v[88:91], v235 offset:32768\n"
      "v_add_u32 v253, s79, v253\n"
      "v_mfma_f32_16x16x32_bf16 v[136:139], a[32:35], v[92:95], v[136:139]\n"
      "ds_read_b128 v[92:95], v232 offset:40960\n"
      "v_mfma_f32_16x16x32_bf16 v[136:139], a[36:39], v[108:111], v[136:139]\n"
      "ds_read_b128 v[108:111], v233 offset:40960\n"
      "ds_write_b64 v244, v[208:209] offset:24704\n"
      "v_mfma_f32_16x16x32_bf16 v[136:139], a[40:43], v[112:115], v[136:139]\n"
      "ds_read_b128 v[112:115], v234 offset:40960\n"
      "v_mfma_f32_16x16x32_bf16 v[136:139], a[44:47], v[116:119], v[136:139]\n"
      "ds_read_b128 v[116:119], v235 offset:40960\n"
      "s_branch LJOIN\n"
      "LF_1:\n"
      "s_mul_i32 s62, s62, 0x3000\n"
      "s_sub_u32 s62, s98, s62\n"
      "s_sub_u32 s62, s62, s89\n"
      "v_mad_i32_i24 v238, v251, s62, v238\n"
      "v_mad_i32_i24 v238, v251, s98, v238\n"
      "s_add_u32 s56, s22, 0x0\n"
      "s_addc_u32 s57, s23, 0\n"
      "s_add_u32 m0, s18, 0x0\n"
      "s_nop 0\n"
      "global_load_lds_dwordx4 v238, s[56:57] offset:0 \n"
      "v_mad_i32_i24 v238, v251, s90, v238\n"
      "s_add_u32 s56, s22, 0x6000\n"
      "s_addc_u32 s57, s23, 0\n"
      "s_add_u32 m0, s18, 0x1000\n"
      "s_nop 0\n"
      "global_load_lds_dwordx4 v238, s[56:57] offset:0 \n"
      "v_mad_i32_i24 v238, v251, s91, v238\n"
      "s_add_u32 s56, s22, 0xc000\n"
      "s_addc_u32 s57, s23, 0\n"
      "s_add_u32 m0, s18, 0x2000\n"
      "s_nop 0\n"
      "global_load_lds_dwordx4 v238, s[56:57] offset:0 \n"
      "v_mad_i32_i24 v238, v251, s92, v238\n"
      "s_add_u32 s56, s22, 0x12000\n"
      "s_addc_u32 s57, s23, 0\n"
      "s_add_u32 m0, s18, 0x3000\n"
      "s_nop 0\n"
      "global_load_lds_dwordx4 v238, s[56:57] offset:0 \n"
      "v_mad_i32_i24 v238, v251, s93, v238\n"
      "s_add_u32 s56, s22, 0x18000\n"
      "s_addc_u32 s57, s23, 0\n"
      "s_add_u32 m0, s18, 0x4000\n"
      "s_nop 0\n"
      "global_load_lds_dwordx4 v238, s[56:57] offset:0 \n"
      "v_mad_i32_i24 v238, v251, s94, v238\n"
      "s_add_u32 s56, s22, 0x1e000\n"
      "s_addc_u32 s57, s23, 0\n"
      "s_add_u32 m0, s18, 0x5000\n"
      "s_nop 0\n"
      "global_load_lds_dwordx4 v238, s[56:57] offset:0 \n"
      "v_mad_i32_i24 v238, v251, s95, v238\n"
      "s_add_u32 s56, s22, 0x24000\n"
      "s_addc_u32 s57, s23, 0\n"
      "s_add_u32 m0, s18, 0x6000\n"
      "s_nop 0\n"
      "global_load_lds_dwordx4 v238, s[56:57] offset:0 \n"
      "v_mad_i32_i24 v238, v251, s96, v238\n"
      "s_add_u32 s56, s22, 0x2a000\n"
      "s_addc_u32 s57, s23, 0\n"
      "s_add_u32 m0, s18, 0x7000\n"
      "s_nop 0\n"
      "global_load_lds_dwordx4 v238, s[56:57] offset:0 \n"
      "v_mad_i32_i24 v238, v251, s97, v238\n"
      "s_add_u32 s56, s22, 0x30000\n"
      "s_addc_u32 s57, s23, 0\n"
      "s_add_u32 m0, s18, 0x8000\n"
      "s_nop 0\n"
      "global_load_lds_dwordx4 v238, s[56:57] offset:0 \n"
      "s_branch LR_1\n"
      "LV1:\n"
      "s_add_u32 s78, s64, 0x0\n"
      "s_addc_u32 s79, s65, 0\n"
      "s_add_u32 m0, s74, 0x0\n"
      "s_nop 0\n"
      "global_load_lds_dwordx4 v240, s[78:79]\n"
      "s_add_u32 m0, s74, 0x400\n"
      "s_nop 0\n"
      "global_load_lds_dwordx4 v241, s[78:79]\n"
      "s_add_u32 m0, s74, 0x800\n"
      "s_nop 0\n"
      "global_load_lds_dwordx4 v242, s[78:79]\n"
      "s_add_u32 s78, s64, 0x3000\n"
      "s_addc_u32 s79, s65, 0\n"
      "s_add_u32 m0, s74, 0xc00\n"
      "s_nop 0\n"
      "global_load_lds_dwordx4 v240, s[78:79]\n"
      "s_add_u32 m0, s74, 0x1000\n"
      "s_nop 0\n"
      "global_load_lds_dwordx4 v241, s[78:79]\n"
      "s_add_u32 m0, s74, 0x1400\n"
      "s_nop 0\n"
      "global_load_lds_dwordx4 v242, s[78:79]\n"
      "s_getpc_b64 s[56:57]\n"
      "LTR_1:\n"
      "s_add_u32 s56, s56, LTE_1-LTR_1\n"
      "s_addc_u32 s57, s57, 0\n"
      "s_mov_b64 exec, 0\n"
      "s_cmp_eq_u32 s76, 3\n"
      "s_cbranch_scc1 LPD6\n"
      "s_cmp_eq_u32 s76, 2\n"
      "s_cbranch_scc1 LPD5\n"
      "s_cmp_eq_u32 s76, 1\n"
      "s_cbranch_scc1 LPD4\n"
      "LTE_1:\n"
      "s_mov_b64 exec, -1\n"
      "s_waitcnt vmcnt(17)\n"
      "v_readfirstlane_b32 s80, v252\n"
      "v_readfirstlane_b32 s81, v253\n"
      "s_nop 3\n"
      "s_add_u32 s80, s80, 0x7f4a7c15\n"
      "s_addc_u32 s81, s81, 0x9e3779b9\n"
      "v_mov_b32 v252, s80\n"
      "v_mov_b32 v253, s81\n"
      "v_xor_b32 v116, v112, v110\n"
      "v_xor_b32 v117, v113, v111\n"
      "v_and_or_b32 v116, v116, -2, v117\n"
      "v_cmp_eq_u32 vcc, 0, v116\n"
      "v_cndmask_b32 v116, v110, v112, vcc\n"
      "s_mov_b64 exec, 1\n"
      "ds_write_b32 v108, v116\n"
      "s_mov_b64 exec, -1\n"
      "v_and_b32 v108, -16, v108\n"
      "s_waitcnt lgkmcnt(0)\n"
      "s_barrier\n"
      "ds_read_b32 v116, v108\n"
      "s_waitcnt lgkmcnt(0)\n"
      "v_readfirstlane_b32 s62, v116\n"
      "s_lshr_b32 s63, s51, 2\n"
      "s_nop 2\n"
      "s_xor_b32 s62, s62, s63\n"
      "s_and_b32 s62, s62, 1\n"
      "s_mul_i32 s63, s62, 0x3000\n"
      "s_sub_u32 s98, s98, s63\n"
      "s_lshl_b32 s62, s62, 1\n"
      "s_sub_u32 s62, 1, s62\n"
      "v_mul_i32_i24 v251, s62, v251\n"
      "s_lshl_b32 s19, s62, 1\n"
      "s_mul_i32 s100, s100, s62\n"
      "v_cmp_ne_u32 vcc, v107, v251\n"
      "s_nop 3\n"
      "s_cbranch_vccnz LF_1\n"
      "LR_1:\n"
      "s_nop 0\n"
      "s_add_u32 s78, s64, 0x6000\n"
      "s_addc_u32 s79, s65, 0\n"
      "s_add_u32 m0, s74, 0x6000\n"
      "s_nop 0\n"
      "global_load_lds_dwordx4 v240, s[78:79]\n"
      "s_add_u32 m0, s74, 0x6400\n"
      "s_nop 0\n"
      "global_load_lds_dwordx4 v241, s[78:79]\n"
      "s_add_u32 m0, s74, 0x6800\n"
      "s_nop 0\n"
      "global_load_lds_dwordx4 v242, s[78:79]\n"
      "s_add_u32 s78, s64, 0x9000\n"
      "s_addc_u32 s79, s65, 0\n"
      "s_add_u32 m0, s74, 0x6c00\n"
      "s_nop 0\n"
      "global_load_lds_dwordx4 v240, s[78:79]\n"
      "s_add_u32 m0, s74, 0x7000\n"
      "s_nop 0\n"
      "global_load_lds_dwordx4 v241, s[78:79]\n"
      "s_add_u32 m0, s74, 0x7400\n"
      "s_nop 0\n"
      "global_load_lds_dwordx4 v242, s[78:79]\n"
      "s_add_u32 s78, s64, 0xc000\n"
      "s_addc_u32 s79, s65, 0\n"
      "s_add_u32 m0, s74, 0xc000\n"
      "s_nop 0\n"
      "global_load_lds_dwordx4 v240, s[78:79]\n"
      "s_add_u32 m0, s74, 0xc400\n"
      "s_nop 0\n"
      "global_load_lds_dwordx4 v241, s[78:79]\n"
      "s_add_u32 m0, s74, 0xc800\n"
      "s_nop 0\n"
      "global_load_lds_dwordx4 v242, s[78:79]\n"
      "s_add_u32 s78, s64, 0xf000\n"
      "s_addc_u32 s79, s65, 0\n"
      "s_add_u32 m0, s74, 0xcc00\n"
      "s_nop 0\n"
      "global_load_lds_dwordx4 v240, s[78:79]\n"
      "s_add_u32 m0, s74, 0xd000\n"
      "s_nop 0\n"
      "global_load_lds_dwordx4 v241, s[78:79]\n"
      "s_add_u32 m0, s74, 0xd400\n"
      "s_nop 0\n"
      "global_load_lds_dwordx4 v242, s[78:79]\n"
      "s_add_u32 m0, s17, 0x25000\n"
      "s_nop 0\n"
      "global_load_lds_dwordx4 v239, s[46:47] offset:0\n"
      "s_add_u32 m0, s17, 0x25800\n"
      "s_nop 0\n"
      "global_load_lds_dwordx4 v239, s[46:47] offset:2048\n"
      "s_waitcnt vmcnt(14)\n"
      "ds_read_b64_tr_b16 a[0:1], v243 offset:0\n"
      "ds_read_b64_tr_b16 a[2:3], v243 offset:768\n"
      "ds_read_b64_tr_b16 a[4:5], v243 offset:8\n"
      "ds_read_b64_tr_b16 a[6:7], v243 offset:776\n"
      "ds_read_b64_tr_b16 a[8:9], v243 offset:64\n"
      "ds_read_b64_tr_b16 a[10:11], v243 offset:832\n"
      "ds_read_b64_tr_b16 a[12:13], v243 offset:72\n"
      "ds_read_b64_tr_b16 a[14:15], v243 offset:840\n"
      "ds_read_b64_tr_b16 a[16:17], v243 offset:128\n"
      "ds_read_b64_tr_b16 a[18:19], v243 offset:896\n"
      "ds_read_b64_tr_b16 a[20:21], v243 offset:136\n"
      "ds_read_b64_tr_b16 a[22:23], v243 offset:904\n"
      "s_waitcnt vmcnt(13)\n"
      "s_barrier\n"
      "ds_read_b128 v[216:219], v232 offset:0\n"
      "ds_read_b128 v[220:223], v232 offset:4096\n"
      "ds_read_b128 v[224:227], v232 offset:8192\n"
      "s_waitcnt lgkmcnt(2)\n"
      "v_mfma_f32_16x16x32_bf16 v[0:3], a[0:3], v[216:219], 0\n"
      "s_add_u32 s78, s64, 0x12000\n"
      "s_addc_u32 s79, s65, 0\n"
      "s_add_u32 m0, s74, 0x0\n"
      "v_mfma_f32_16x16x32_bf16 v[4:7], a[4:7], v[216:219], 0\n"
      "global_load_lds_dwordx4 v240, s[78:79]\n"
      "v_mfma_f32_16x16x32_bf16 v[8:11], a[8:11], v[216:219], 0\n"
      "ds_read_b128 v[228:231], v232 offset:12288\n"
      "v_mfma_f32_16x16x32_bf16 v[12:15], a[12:15], v[216:219], 0\n"
      "v_mfma_f32_16x16x32_bf16 v[16:19], a[16:19], v[216:219], 0\n"
      "s_add_u32 m0, s74, 0x400\n"
      "v_mfma_f32_16x16x32_bf16 v[20:23], a[20:23], v[216:219], 0\n"
      "global_load_lds_dwordx4 v241, s[78:79]\n"
      "s_waitcnt lgkmcnt(2)\n"
      "v_mfma_f32_16x16x32_bf16 v[24:27], a[0:3], v[220:223], 0\n"
      "s_waitcnt vmcnt(10)\n"
      "s_cmp_lg_u32 s76, 1\n"
      "s_cbranch_scc1 LA_1\n"
      "v_mov_b32 v244, s77\n"
      "s_mov_b64 exec, 1\n"
      "global_store_dwordx2 v244, v[252:253], s[28:29] sc1\n"
      "s_mov_b64 exec, -1\n"
      "s_nop 4\n"
      "LA_1:\n"
      "v_mfma_f32_16x16x32_bf16 v[28:31], a[4:7], v[220:223], 0\n"
      "ds_read_b64_tr_b16 a[24:25], v243 offset:24576\n"
      "v_mfma_f32_16x16x32_bf16 v[32:35], a[8:11], v[220:223], 0\n"
      "ds_read_b128 v[216:219], v232 offset:16384\n"
      "s_add_u32 m0, s74, 0x800\n"
      "v_mfma_f32_16x16x32_bf16 v[36:39], a[12:15], v[220:223], 0\n"
      "ds_read_b64_tr_b16 a[26:27], v243 offset:25344\n"
      "global_load_lds_dwordx4 v242, s[78:79]\n"
      "v_mfma_f32_16x16x32_bf16 v[40:43], a[16:19], v[220:223], 0\n"
      "ds_read_b64_tr_b16 a[28:29], v243 offset:24584\n"
      "v_mfma_f32_16x16x32_bf16 v[44:47], a[20:23], v[220:223], 0\n"
      "s_waitcnt lgkmcnt(5)\n"
      "v_mfma_f32_16x16x32_bf16 v[48:51], a[0:3], v[224:227], 0\n"
      "ds_read_b64_tr_b16 a[30:31], v243 offset:25352\n"
      "s_add_u32 s78, s64, 0x15000\n"
      "s_addc_u32 s79, s65, 0\n"
      "s_add_u32 m0, s74, 0xc00\n"
      "v_mfma_f32_16x16x32_bf16 v[52:55], a[4:7], v[224:227], 0\n"
      "ds_read_b64_tr_b16 a[32:33], v243 offset:24640\n"
      "global_load_lds_dwordx4 v240, s[78:79]\n"
      "v_mfma_f32_16x16x32_bf16 v[56:59], a[8:11], v[224:227], 0\n"
      "ds_read_b128 v[220:223], v232 offset:20480\n"
      "v_mfma_f32_16x16x32_bf16 v[60:63], a[12:15], v[224:227], 0\n"
      "ds_read_b64_tr_b16 a[34:35], v243 offset:25408\n"
      "v_mfma_f32_16x16x32_bf16 v[64:67], a[16:19], v[224:227], 0\n"
      "ds_read_b64_tr_b16 a[36:37], v243 offset:24648\n"
      "s_add_u32 m0, s74, 0x1000\n"
      "v_mfma_f32_16x16x32_bf16 v[68:71], a[20:23], v[224:227], 0\n"
      "global_load_lds_dwordx4 v241, s[78:79]\n"
      "s_waitcnt lgkmcnt(9)\n"
      "v_mfma_f32_16x16x32_bf16 v[72:75], a[0:3], v[228:231], 0\n"
      "ds_read_b64_tr_b16 a[38:39], v243 offset:25416\n"
      "v_mfma_f32_16x16x32_bf16 v[76:79], a[4:7], v[228:231], 0\n"
      "ds_read_b64_tr_b16 a[40:41], v243 offset:24704\n"
      "v_mfma_f32_16x16x32_bf16 v[80:83], a[8:11], v[228:231], 0\n"
      "ds_read_b128 v[224:227], v232 offset:24576\n"
      "s_add_u32 m0, s74, 0x1400\n"
      "v_mfma_f32_16x16x32_bf16 v[84:87], a[12:15], v[228:231], 0\n"
      "ds_read_b64_tr_b16 a[42:43], v243 offset:25472\n"
      "global_load_lds_dwordx4 v242, s[78:79]\n"
      "v_mfma_f32_16x16x32_bf16 v[88:91], a[16:19], v[228:231], 0\n"
      "ds_read_b64_tr_b16 a[44:45], v243 offset:24712\n"
      "v_mfma_f32_16x16x32_bf16 v[92:95], a[20:23], v[228:231], 0\n"
      "s_waitcnt lgkmcnt(12)\n"
      "v_mfma_f32_16x16x32_bf16 v[96:99], a[0:3], v[216:219], 0\n"
      "ds_read_b64_tr_b16 a[46:47], v243 offset:25480\n"
      "v_mfma_f32_16x16x32_bf16 v[100:103], a[4:7], v[216:219], 0\n"
      "s_waitcnt vmcnt(8)\n"
      "v_mfma_f32_16x16x32_bf16 v[104:107], a[8:11], v[216:219], 0\n"
      "ds_read_b128 v[228:231], v232 offset:28672\n"
      "v_mfma_f32_16x16x32_bf16 v[108:111], a[12:15], v[216:219], 0\n"
      "ds_read_b64_tr_b16 a[48:49], v243 offset:49152\n"
      "v_mfma_f32_16x16x32_bf16 v[112:115], a[16:19], v[216:219], 0\n"
      "ds_read_b64_tr_b16 a[50:51], v243 offset:49920\n"
      "v_mfma_f32_16x16x32_bf16 v[116:119], a[20:23], v[216:219], 0\n"
      "s_waitcnt lgkmcnt(11)\n"
      "v_mfma_f32_16x16x32_bf16 v[120:123], a[0:3], v[220:223], 0\n"
      "ds_read_b64_tr_b16 a[52:53], v243 offset:49160\n"
      "v_mfma_f32_16x16x32_bf16 v[124:127], a[4:7], v[220:223], 0\n"
      "ds_read_b64_tr_b16 a[54:55], v243 offset:49928\n"
      "v_mfma_f32_16x16x32_bf16 v[128:131], a[8:11], v[220:223], 0\n"
      "ds_read_b128 v[216:219], v232 offset:32768\n"
      "v_add_u32 v232, 0x9000, v232\n"
      "v_mfma_f32_16x16x32_bf16 v[132:135], a[12:15], v[220:223], 0\n"
      "ds_read_b64_tr_b16 a[56:57], v243 offset:49216\n"
      "v_mfma_f32_16x16x32_bf16 v[136:139], a[16:19], v[220:223], 0\n"
      "ds_read_b64_tr_b16 a[58:59], v243 offset:49984\n"
      "s_waitcnt lgkmcnt(8)\n"
      "s_add_u32 s78, s64, 0x18000\n"
      "s_addc_u32 s79, s65, 0\n"
      "s_add_u32 m0, s74, 0x6000\n"
      "v_mfma_f32_16x16x32_bf16 v[140:143], a[20:23], v[220:223], 0\n"
      "global_load_lds_dwordx4 v240, s[78:79]\n"
      "v_mfma_f32_16x16x32_bf16 v[144:147], a[0:3], v[224:227], 0\n"
      "ds_read_b64_tr_b16 a[60:61], v243 offset:49224\n"
      "v_mfma_f32_16x16x32_bf16 v[148:151], a[4:7], v[224:227], 0\n"
      "ds_read_b64_tr_b16 a[62:63], v243 offset:49992\n"
      "v_mfma_f32_16x16x32_bf16 v[152:155], a[8:11], v[224:227], 0\n"
      "ds_read_b128 v[220:223], v233 offset:0\n"
      "s_add_u32 m0, s74, 0x6400\n"
      "v_mfma_f32_16x16x32_bf16 v[156:159], a[12:15], v[224:227], 0\n"
      "ds_read_b64_tr_b16 a[64:65], v243 offset:49280\n"
      "global_load_lds_dwordx4 v241, s[78:79]\n"
      "v_mfma_f32_16x16x32_bf16 v[160:163], a[16:19], v[224:227], 0\n"
      "ds_read_b64_tr_b16 a[66:67], v243 offset:50048\n"
      "v_mfma_f32_16x16x32_bf16 v[164:167], a[20:23], v[224:227], 0\n"
      "s_waitcnt lgkmcnt(12)\n"
      "v_mfma_f32_16x16x32_bf16 v[168:171], a[0:3], v[228:231], 0\n"
      "ds_read_b64_tr_b16 a[68:69], v243 offset:49288\n"
      "s_add_u32 m0, s74, 0x6800\n"
      "v_mfma_f32_16x16x32_bf16 v[172:175], a[4:7], v[228:231], 0\n"
      "ds_read_b64_tr_b16 a[70:71], v243 offset:50056\n"
      "global_load_lds_dwordx4 v242, s[78:79]\n"
      "v_mfma_f32_16x16x32_bf16 v[176:179], a[8:11], v[228:231], 0\n"
      "ds_read_b128 v[224:227], v233 offset:4096\n"
      "v_mfma_f32_16x16x32_bf16 v[180:183], a[12:15], v[228:231], 0\n"
      "v_mfma_f32_16x16x32_bf16 v[184:187], a[16:19], v[228:231], 0\n"
      "s_add_u32 s78, s64, 0x1b000\n"
      "s_addc_u32 s79, s65, 0\n"
      "s_add_u32 m0, s74, 0x6c00\n"
      "v_mfma_f32_16x16x32_bf16 v[188:191], a[20:23], v[228:231], 0\n"
      "global_load_lds_dwordx4 v240, s[78:79]\n"
      "s_waitcnt lgkmcnt(10)\n"
      "v_mfma_f32_16x16x32_bf16 v[192:195], a[0:3], v[216:219], 0\n"
      "v_mfma_f32_16x16x32_bf16 v[196:199], a[4:7], v[216:219], 0\n"
      "v_mfma_f32_16x16x32_bf16 v[200:203], a[8:11], v[216:219], 0\n"
      "ds_read_b128 v[228:231], v233 offset:8192\n"
      "s_add_u32 m0, s74, 0x7000\n"
      "v_mfma_f32_16x16x32_bf16 v[204:207], a[12:15], v[216:219], 0\n"
      "global_load_lds_dwordx4 v241, s[78:79]\n"
      "v_mfma_f32_16x16x32_bf16 v[208:211], a[16:19], v[216:219], 0\n"
      "v_mfma_f32_16x16x32_bf16 v[212:215], a[20:23], v[216:219], 0\n"
      "s_waitcnt lgkmcnt(6)\n"
      "v_mfma_f32_16x16x32_bf16 v[0:3], a[24:27], v[220:223], v[0:3]\n"
      "s_add_u32 m0, s74, 0x7400\n"
      "v_mfma_f32_16x16x32_bf16 v[4:7], a[28:31], v[220:223], v[4:7]\n"
      "global_load_lds_dwordx4 v242, s[78:79]\n"
      "v_mfma_f32_16x16x32_bf16 v[8:11], a[32:35], v[220:223], v[8:11]\n"
      "ds_read_b128 v[216:219], v233 offset:12288\n"
      "s_waitcnt vmcnt(6)\n"
      "v_mfma_f32_16x16x32_bf16 v[12:15], a[36:39], v[220:223], v[12:15]\n"
      "ds_read_b64_tr_b16 a[72:73], v243 offset:0\n"
      "v_mfma_f32_16x16x32_bf16 v[16:19], a[40:43], v[220:223], v[16:19]\n"
      "ds_read_b64_tr_b16 a[74:75], v243 offset:768\n"
      "v_mad_i32_i24 v238, v251, s89, v238\n"
      "s_add_u32 s56, s22, 0x0\n"
      "s_addc_u32 s57, s23, 0\n"
      "s_add_u32 m0, s18, 0x8f00\n"
      "v_mfma_f32_16x16x32_bf16 v[20:23], a[44:47], v[220:223], v[20:23]\n"
      "global_load_lds_dwordx4 v238, s[56:57] offset:256 \n"
      "s_waitcnt lgkmcnt(4)\n"
      "v_mfma_f32_16x16x32_bf16 v[24:27], a[24:27], v[224:227], v[24:27]\n"
      "ds_read_b64_tr_b16 a[76:77], v243 offset:8\n"
      "v_mfma_f32_16x16x32_bf16 v[28:31], a[28:31], v[224:227], v[28:31]\n"
      "ds_read_b64_tr_b16 a[78:79], v243 offset:776\n"
      "v_mad_i32_i24 v238, v251, s90, v238\n"
      "s_add_u32 s56, s22, 0x6000\n"
      "s_addc_u32 s57, s23, 0\n"
      "s_add_u32 m0, s18, 0x9f00\n"
      "v_mfma_f32_16x16x32_bf16 v[32:35], a[32:35], v[224:227], v[32:35]\n"
      "ds_read_b128 v[220:223], v233 offset:16384\n"
      "global_load_lds_dwordx4 v238, s[56:57] offset:256 \n"
      "v_mfma_f32_16x16x32_bf16 v[36:39], a[36:39], v[224:227], v[36:39]\n"
      "ds_read_b64_tr_b16 a[80:81], v243 offset:64\n"
      "v_mfma_f32_16x16x32_bf16 v[40:43], a[40:43], v[224:227], v[40:43]\n"
      "ds_read_b64_tr_b16 a[82:83], v243 offset:832\n"
      "v_mad_i32_i24 v238, v251, s91, v238\n"
      "s_add_u32 s56, s22, 0xc000\n"
      "s_addc_u32 s57, s23, 0\n"
      "s_add_u32 m0, s18, 0xaf00\n"
      "v_mfma_f32_16x16x32_bf16 v[44:47], a[44:47], v[224:227], v[44:47]\n"
      "global_load_lds_dwordx4 v238, s[56:57] offset:256 \n"
      "s_waitcnt lgkmcnt(8)\n"
      "v_mfma_f32_16x16x32_bf16 v[48:51], a[24:27], v[228:231], v[48:51]\n"
      "ds_read_b64_tr_b16 a[84:85], v243 offset:72\n"
      "v_mfma_f32_16x16x32_bf16 v[52:55], a[28:31], v[228:231], v[52:55]\n"
      "ds_read_b64_tr_b16 a[86:87], v243 offset:840\n"
      "v_mad_i32_i24 v238, v251, s92, v238\n"
      "s_add_u32 s56, s22, 0x12000\n"
      "s_addc_u32 s57, s23, 0\n"
      "s_add_u32 m0, s18, 0xbf00\n"
      "v_mfma_f32_16x16x32_bf16 v[56:59], a[32:35], v[228:231], v[56:59]\n"
      "ds_read_b128 v[224:227], v233 offset:20480\n"
      "global_load_lds_dwordx4 v238, s[56:57] offset:256 \n"
      "v_mfma_f32_16x16x32_bf16 v[60:63], a[36:39], v[228:231], v[60:63]\n"
      "ds_read_b64_tr_b16 a[88:89], v243 offset:128\n"
      "v_mfma_f32_16x16x32_bf16 v[64:67], a[40:43], v[228:231], v[64:67]\n"
      "ds_read_b64_tr_b16 a[90:91], v243 offset:896\n"
      "v_mad_i32_i24 v238, v251, s93, v238\n"
      "s_add_u32 s56, s22, 0x18000\n"
      "s_addc_u32 s57, s23, 0\n"
      "s_add_u32 m0, s18, 0xcf00\n"
      "v_mfma_f32_16x16x32_bf16 v[68:71], a[44:47], v[228:231], v[68:71]\n"
      "global_load_lds_dwordx4 v238, s[56:57] offset:256 \n"
      "s_waitcnt lgkmcnt(12)\n"
      "v_mfma_f32_16x16x32_bf16 v[72:75], a[24:27], v[216:219], v[72:75]\n"
      "ds_read_b64_tr_b16 a[92:93], v243 offset:136\n"
      "v_mfma_f32_16x16x32_bf16 v[76:79], a[28:31], v[216:219], v[76:79]\n"
      "ds_read_b64_tr_b16 a[94:95], v243 offset:904\n"
      "v_mad_i32_i24 v238, v251, s94, v238\n"
      "s_add_u32 s56, s22, 0x1e000\n"
      "s_addc_u32 s57, s23, 0\n"
      "s_add_u32 m0, s18, 0xdf00\n"
      "v_mfma_f32_16x16x32_bf16 v[80:83], a[32:35], v[216:219], v[80:83]\n"
      "ds_read_b128 v[228:231], v233 offset:24576\n"
      "global_load_lds_dwordx4 v238, s[56:57] offset:256 \n"
      "v_mfma_f32_16x16x32_bf16 v[84:87], a[36:39], v[216:219], v[84:87]\n"
      "v_mfma_f32_16x16x32_bf16 v[88:91], a[40:43], v[216:219], v[88:91]\n"
      "v_mad_i32_i24 v238, v251, s95, v238\n"
      "s_add_u32 s56, s22, 0x24000\n"
      "s_addc_u32 s57, s23, 0\n"
      "s_add_u32 m0, s18, 0xef00\n"
      "v_mfma_f32_16x16x32_bf16 v[92:95], a[44:47], v[216:219], v[92:95]\n"
      "global_load_lds_dwordx4 v238, s[56:57] offset:256 \n"
      "s_waitcnt lgkmcnt(10)\n"
      "v_mfma_f32_16x16x32_bf16 v[96:99], a[24:27], v[220:223], v[96:99]\n"
      "v_mfma_f32_16x16x32_bf16 v[100:103], a[28:31], v[220:223], v[100:103]\n"
      "v_mad_i32_i24 v238, v251, s96, v238\n"
      "s_add_u32 s56, s22, 0x2a000\n"
      "s_addc_u32 s57, s23, 0\n"
      "s_add_u32 m0, s18, 0xff00\n"
      "v_mfma_f32_16x16x32_bf16 v[104:107], a[32:35], v[220:223], v[104:107]\n"
      "ds_read_b128 v[216:219], v233 offset:28672\n"
      "global_load_lds_dwordx4 v238, s[56:57] offset:256 \n"
      "v_mfma_f32_16x16x32_bf16 v[108:111], a[36:39], v[220:223], v[108:111]\n"
      "v_mfma_f32_16x16x32_bf16 v[112:115], a[40:43], v[220:223], v[112:115]\n"
      "v_mad_i32_i24 v238, v251, s97, v238\n"
      "s_add_u32 s56, s22, 0x30000\n"
      "s_addc_u32 s57, s23, 0\n"
      "s_add_u32 m0, s18, 0x10f00\n"
      "v_mfma_f32_16x16x32_bf16 v[116:119], a[44:47], v[220:223], v[116:119]\n"
      "global_load_lds_dwordx4 v238, s[56:57] offset:256 \n"
      "s_waitcnt lgkmcnt(6)\n"
      "v_mfma_f32_16x16x32_bf16 v[120:123], a[24:27], v[224:227], v[120:123]\n"
      "v_mfma_f32_16x16x32_bf16 v[124:127], a[28:31], v[224:227], v[124:127]\n"
      "v_mfma_f32_16x16x32_bf16 v[128:131], a[32:35], v[224:227], v[128:131]\n"
      "ds_read_b128 v[220:223], v233 offset:32768\n"
      "v_add_u32 v233, 0x9000, v233\n"
      "s_add_u32 s78, s64, 0x1e000\n"
      "s_addc_u32 s79, s65, 0\n"
      "s_add_u32 m0, s74, 0xc000\n"
      "v_mfma_f32_16x16x32_bf16 v[132:135], a[36:39], v[224:227], v[132:135]\n"
      "global_load_lds_dwordx4 v240, s[78:79]\n"
      "v_mfma_f32_16x16x32_bf16 v[136:139], a[40:43], v[224:227], v[136:139]\n"
      "v_mfma_f32_16x16x32_bf16 v[140:143], a[44:47], v[224:227], v[140:143]\n"
      "s_waitcnt lgkmcnt(2)\n"
      "v_mfma_f32_16x16x32_bf16 v[144:147], a[24:27], v[228:231], v[144:147]\n"
      "s_waitcnt vmcnt(10)\n"
      "s_add_u32 m0, s74, 0xc400\n"
      "v_mfma_f32_16x16x32_bf16 v[148:151], a[28:31], v[228:231], v[148:151]\n"
      "ds_read_b64_tr_b16 a[96:97], v243 offset:24576\n"
      "global_load_lds_dwordx4 v241, s[78:79]\n"
      "v_mfma_f32_16x16x32_bf16 v[152:155], a[32:35], v[228:231], v[152:155]\n"
      "ds_read_b128 v[224:227], v234 offset:0\n"
      "v_mfma_f32_16x16x32_bf16 v[156:159], a[36:39], v[228:231], v[156:159]\n"
      "ds_read_b64_tr_b16 a[98:99], v243 offset:25344\n"
      "v_mfma_f32_16x16x32_bf16 v[160:163], a[40:43], v[228:231], v[160:163]\n"
      "ds_read_b64_tr_b16 a[100:101], v243 offset:24584\n"
      "s_add_u32 m0, s74, 0xc800\n"
      "v_mfma_f32_16x16x32_bf16 v[164:167], a[44:47], v[228:231], v[164:167]\n"
      "global_load_lds_dwordx4 v242, s[78:79]\n"
      "s_waitcnt lgkmcnt(5)\n"
      "v_mfma_f32_16x16x32_bf16 v[168:171], a[24:27], v[216:219], v[168:171]\n"
      "ds_read_b64_tr_b16 a[102:103], v243 offset:25352\n"
      "v_mfma_f32_16x16x32_bf16 v[172:175], a[28:31], v[216:219], v[172:175]\n"
      "ds_read_b64_tr_b16 a[104:105], v243 offset:24640\n"
      "v_mfma_f32_16x16x32_bf16 v[176:179], a[32:35], v[216:219], v[176:179]\n"
      "ds_read_b128 v[228:231], v234 offset:4096\n"
      "s_add_u32 s78, s64, 0x21000\n"
      "s_addc_u32 s79, s65, 0\n"
      "s_add_u32 m0, s74, 0xcc00\n"
      "LPD4:\n"
      "s_cbranch_execz LPD9\n"
      "v_mfma_f32_16x16x32_bf16 v[180:183], a[36:39], v[216:219], v[180:183]\n"
      "ds_read_b64_tr_b16 a[106:107], v243 offset:25408\n"
      "global_load_lds_dwordx4 v240, s[78:79]\n"
      "v_mfma_f32_16x16x32_bf16 v[184:187], a[40:43], v[216:219], v[184:187]\n"
      "ds_read_b64_tr_b16 a[108:109], v243 offset:24648\n"
      "v_mfma_f32_16x16x32_bf16 v[188:191], a[44:47], v[216:219], v[188:191]\n"
      "s_waitcnt lgkmcnt(9)\n"
      "v_mfma_f32_16x16x32_bf16 v[192:195], a[24:27], v[220:223], v[192:195]\n"
      "ds_read_b64_tr_b16 a[110:111], v243 offset:25416\n"
      "s_add_u32 m0, s74, 0xd000\n"
      "v_mfma_f32_16x16x32_bf16 v[196:199], a[28:31], v[220:223], v[196:199]\n"
      "ds_read_b64_tr_b16 a[112:113], v243 offset:24704\n"
      "global_load_lds_dwordx4 v241, s[78:79]\n"
      "v_mfma_f32_16x16x32_bf16 v[200:203], a[32:35], v[220:223], v[200:203]\n"
      "ds_read_b128 v[216:219], v234 offset:8192\n"
      "v_mfma_f32_16x16x32_bf16 v[204:207], a[36:39], v[220:223], v[204:207]\n"
      "ds_read_b64_tr_b16 a[114:115], v243 offset:25472\n"
      "v_mfma_f32_16x16x32_bf16 v[208:211], a[40:43], v[220:223], v[208:211]\n"
      "ds_read_b64_tr_b16 a[116:117], v243 offset:24712\n"
      "s_add_u32 m0, s74, 0xd400\n"
      "v_mfma_f32_16x16x32_bf16 v[212:215], a[44:47], v[220:223], v[212:215]\n"
      "global_load_lds_dwordx4 v242, s[78:79]\n"
      "s_waitcnt lgkmcnt(12)\n"
      "v_mfma_f32_16x16x32_bf16 v[0:3], a[48:51], v[224:227], v[0:3]\n"
      "ds_read_b64_tr_b16 a[118:119], v243 offset:25480\n"
      "v_mfma_f32_16x16x32_bf16 v[4:7], a[52:55], v[224:227], v[4:7]\n"
      "v_mfma_f32_16x16x32_bf16 v[8:11], a[56:59], v[224:227], v[8:11]\n"
      "ds_read_b128 v[220:223], v234 offset:12288\n"
      "s_add_u32 s78, s64, 0x24000\n"
      "s_addc_u32 s79, s65, 0\n"
      "s_add_u32 m0, s74, 0x0\n"
      "v_mfma_f32_16x16x32_bf16 v[12:15], a[60:63], v[224:227], v[12:15]\n"
      "global_load_lds_dwordx4 v240, s[78:79]\n"
      "v_mfma_f32_16x16x32_bf16 v[16:19], a[64:67], v[224:227], v[16:19]\n"
      "v_mfma_f32_16x16x32_bf16 v[20:23], a[68:71], v[224:227], v[20:23]\n"
      "s_waitcnt lgkmcnt(9)\n"
      "v_mfma_f32_16x16x32_bf16 v[24:27], a[48:51], v[228:231], v[24:27]\n"
      "s_add_u32 m0, s74, 0x400\n"
      "v_mfma_f32_16x16x32_bf16 v[28:31], a[52:55], v[228:231], v[28:31]\n"
      "global_load_lds_dwordx4 v241, s[78:79]\n"
      "v_mfma_f32_16x16x32_bf16 v[32:35], a[56:59], v[228:231], v[32:35]\n"
      "ds_read_b128 v[224:227], v234 offset:16384\n"
      "v_mfma_f32_16x16x32_bf16 v[36:39], a[60:63], v[228:231], v[36:39]\n"
      "v_mfma_f32_16x16x32_bf16 v[40:43], a[64:67], v[228:231], v[40:43]\n"
      "s_add_u32 m0, s74, 0x800\n"
      "v_mfma_f32_16x16x32_bf16 v[44:47], a[68:71], v[228:231], v[44:47]\n"
      "global_load_lds_dwordx4 v242, s[78:79]\n"
      "s_waitcnt lgkmcnt(5)\n"
      "v_mfma_f32_16x16x32_bf16 v[48:51], a[48:51], v[216:219], v[48:51]\n"
      "v_mfma_f32_16x16x32_bf16 v[52:55], a[52:55], v[216:219], v[52:55]\n"
      "v_mfma_f32_16x16x32_bf16 v[56:59], a[56:59], v[216:219], v[56:59]\n"
      "ds_read_b128 v[228:231], v234 offset:20480\n"
      "s_add_u32 s78, s64, 0x27000\n"
      "s_addc_u32 s79, s65, 0\n"
      "s_add_u32 m0, s74, 0xc00\n"
      "v_mfma_f32_16x16x32_bf16 v[60:63], a[60:63], v[216:219], v[60:63]\n"
      "global_load_lds_dwordx4 v240, s[78:79]\n"
      "v_mfma_f32_16x16x32_bf16 v[64:67], a[64:67], v[216:219], v[64:67]\n"
      "v_mfma_f32_16x16x32_bf16 v[68:71], a[68:71], v[216:219], v[68:71]\n"
      "s_waitcnt lgkmcnt(2)\n"
      "v_mfma_f32_16x16x32_bf16 v[72:75], a[48:51], v[220:223], v[72:75]\n"
      "s_add_u32 m0, s74, 0x1000\n"
      "v_mfma_f32_16x16x32_bf16 v[76:79], a[52:55], v[220:223], v[76:79]\n"
      "global_load_lds_dwordx4 v241, s[78:79]\n"
      "v_mfma_f32_16x16x32_bf16 v[80:83], a[56:59], v[220:223], v[80:83]\n"
      "ds_read_b128 v[216:219], v234 offset:24576\n"
      "v_mfma_f32_16x16x32_bf16 v[84:87], a[60:63], v[220:223], v[84:87]\n"
      "v_mfma_f32_16x16x32_bf16 v[88:91], a[64:67], v[220:223], v[88:91]\n"
      "s_add_u32 m0, s74, 0x1400\n"
      "v_mfma_f32_16x16x32_bf16 v[92:95], a[68:71], v[220:223], v[92:95]\n"
      "global_load_lds_dwordx4 v242, s[78:79]\n"
      "s_waitcnt lgkmcnt(2)\n"
      "v_mfma_f32_16x16x32_bf16 v[96:99], a[48:51], v[224:227], v[96:99]\n"
      "v_mfma_f32_16x16x32_bf16 v[100:103], a[52:55], v[224:227], v[100:103]\n"
      "v_mfma_f32_16x16x32_bf16 v[104:107], a[56:59], v[224:227], v[104:107]\n"
      "ds_read_b128 v[220:223], v234 offset:28672\n"
      "s_add_u32 s78, s64, 0x2a000\n"
      "s_addc_u32 s79, s65, 0\n"
      "s_add_u32 m0, s74, 0x6000\n"
      "v_mfma_f32_16x16x32_bf16 v[108:111], a[60:63], v[224:227], v[108:111]\n"
      "global_load_lds_dwordx4 v240, s[78:79]\n"
      "v_mfma_f32_16x16x32_bf16 v[112:115], a[64:67], v[224:227], v[112:115]\n"
      "v_mfma_f32_16x16x32_bf16 v[116:119], a[68:71], v[224:227], v[116:119]\n"
      "s_waitcnt lgkmcnt(2)\n"
      "v_mfma_f32_16x16x32_bf16 v[120:123], a[48:51], v[228:231], v[120:123]\n"
      "s_add_u32 m0, s74, 0x6400\n"
      "v_mfma_f32_16x16x32_bf16 v[124:127], a[52:55], v[228:231], v[124:127]\n"
      "global_load_lds_dwordx4 v241, s[78:79]\n"
      "v_mfma_f32_16x16x32_bf16 v[128:131], a[56:59], v[228:231], v[128:131]\n"
      "ds_read_b128 v[224:227], v234 offset:32768\n"
      "v_add_u32 v234, 0x9000, v234\n"
      "v_mfma_f32_16x16x32_bf16 v[132:135], a[60:63], v[228:231], v[132:135]\n"
      "v_mfma_f32_16x16x32_bf16 v[136:139], a[64:67], v[228:231], v[136:139]\n"
      "s_waitcnt vmcnt(8)\n"
      "s_add_u32 m0, s74, 0x6800\n"
      "v_mfma_f32_16x16x32_bf16 v[140:143], a[68:71], v[228:231], v[140:143]\n"
      "global_load_lds_dwordx4 v242, s[78:79]\n"
      "s_waitcnt lgkmcnt(2)\n"
      "v_mfma_f32_16x16x32_bf16 v[144:147], a[48:51], v[216:219], v[144:147]\n"
      "ds_read_b64_tr_b16 a[120:121], v243 offset:49152\n"
      "v_mfma_f32_16x16x32_bf16 v[148:151], a[52:55], v[216:219], v[148:151]\n"
      "ds_read_b64_tr_b16 a[122:123], v243 offset:49920\n"
      "v_mfma_f32_16x16x32_bf16 v[152:155], a[56:59], v[216:219], v[152:155]\n"
      "ds_read_b128 v[228:231], v235 offset:0\n"
      "s_add_u32 s78, s64, 0x2d000\n"
      "s_addc_u32 s79, s65, 0\n"
      "s_add_u32 m0, s74, 0x6c00\n"
      "v_mfma_f32_16x16x32_bf16 v[156:159], a[60:63], v[216:219], v[156:159]\n"
      "ds_read_b64_tr_b16 a[124:125], v243 offset:49160\n"
      "global_load_lds_dwordx4 v240, s[78:79]\n"
      "v_mfma_f32_16x16x32_bf16 v[160:163], a[64:67], v[216:219], v[160:163]\n"
      "ds_read_b64_tr_b16 a[126:127], v243 offset:49928\n"
      "v_mfma_f32_16x16x32_bf16 v[164:167], a[68:71], v[216:219], v[164:167]\n"
      "s_waitcnt lgkmcnt(6)\n"
      "v_mfma_f32_16x16x32_bf16 v[168:171], a[48:51], v[220:223], v[168:171]\n"
      "ds_read_b64_tr_b16 a[128:129], v243 offset:49216\n"
      "s_add_u32 m0, s74, 0x7000\n"
      "v_mfma_f32_16x16x32_bf16 v[172:175], a[52:55], v[220:223], v[172:175]\n"
      "ds_read_b64_tr_b16 a[130:131], v243 offset:49984\n"
      "global_load_lds_dwordx4 v241, s[78:79]\n"
      "v_mfma_f32_16x16x32_bf16 v[176:179], a[56:59], v[220:223], v[176:179]\n"
      "ds_read_b128 v[216:219], v235 offset:4096\n"
      "v_mfma_f32_16x16x32_bf16 v[180:183], a[60:63], v[220:223], v[180:183]\n"
      "ds_read_b64_tr_b16 a[132:133], v243 offset:49224\n"
      "v_mfma_f32_16x16x32_bf16 v[184:187], a[64:67], v[220:223], v[184:187]\n"
      "ds_read_b64_tr_b16 a[134:135], v243 offset:49992\n"
      "s_add_u32 m0, s74, 0x7400\n"
      "v_mfma_f32_16x16x32_bf16 v[188:191], a[68:71], v[220:223], v[188:191]\n"
      "global_load_lds_dwordx4 v242, s[78:79]\n"
      "s_waitcnt lgkmcnt(10)\n"
      "v_mfma_f32_16x16x32_bf16 v[192:195], a[48:51], v[224:227], v[192:195]\n"
      "ds_read_b64_tr_b16 a[136:137], v243 offset:49280\n"
      "v_mfma_f32_16x16x32_bf16 v[196:199], a[52:55], v[224:227], v[196:199]\n"
      "ds_read_b64_tr_b16 a[138:139], v243 offset:50048\n"
      "v_mfma_f32_16x16x32_bf16 v[200:203], a[56:59], v[224:227], v[200:203]\n"
      "ds_read_b128 v[220:223], v235 offset:8192\n"
      "global_load_dwordx4 v[244:247], v248, s[28:29] sc1\n"
      "v_mfma_f32_16x16x32_bf16 v[204:207], a[60:63], v[224:227], v[204:207]\n"
      "ds_read_b64_tr_b16 a[140:141], v243 offset:49288\n"
      "v_mfma_f32_16x16x32_bf16 v[208:211], a[64:67], v[224:227], v[208:211]\n"
      "ds_read_b64_tr_b16 a[142:143], v243 offset:50056\n"
      "v_mfma_f32_16x16x32_bf16 v[212:215], a[68:71], v[224:227], v[212:215]\n"
      "s_waitcnt lgkmcnt(12)\n"
      "v_mfma_f32_16x16x32_bf16 v[0:3], a[72:75], v[228:231], v[0:3]\n"
      "v_mfma_f32_16x16x32_bf16 v[4:7], a[76:79], v[228:231], v[4:7]\n"
      "v_mfma_f32_16x16x32_bf16 v[8:11], a[80:83], v[228:231], v[8:11]\n"
      "ds_read_b128 v[224:227], v235 offset:12288\n"
      "v_mfma_f32_16x16x32_bf16 v[12:15], a[84:87], v[228:231], v[12:15]\n"
      "v_mfma_f32_16x16x32_bf16 v[16:19], a[88:91], v[228:231], v[16:19]\n"
      "s_waitcnt vmcnt(7)\n"
      "v_mfma_f32_16x16x32_bf16 v[20:23], a[92:95], v[228:231], v[20:23]\n"
      "s_waitcnt lgkmcnt(8)\n"
      "v_mfma_f32_16x16x32_bf16 v[24:27], a[72:75], v[216:219], v[24:27]\n"
      "ds_read_b64_tr_b16 a[144:145], v243 offset:0\n"
      "v_mfma_f32_16x16x32_bf16 v[28:31], a[76:79], v[216:219], v[28:31]\n"
      "ds_read_b64_tr_b16 a[146:147], v243 offset:768\n"
      "v_mfma_f32_16x16x32_bf16 v[32:35], a[80:83], v[216:219], v[32:35]\n"
      "ds_read_b128 v[228:231], v235 offset:16384\n"
      "s_waitcnt lgkmcnt(4)\n"
      "s_add_u32 s78, s64, 0x30000\n"
      "s_addc_u32 s79, s65, 0\n"
      "s_add_u32 m0, s74, 0xc000\n"
      "v_mfma_f32_16x16x32_bf16 v[36:39], a[84:87], v[216:219], v[36:39]\n"
      "ds_read_b64_tr_b16 a[148:149], v243 offset:8\n"
      "global_load_lds_dwordx4 v240, s[78:79]\n"
      "v_mfma_f32_16x16x32_bf16 v[40:43], a[88:91], v[216:219], v[40:43]\n"
      "ds_read_b64_tr_b16 a[150:151], v243 offset:776\n"
      "v_mfma_f32_16x16x32_bf16 v[44:47], a[92:95], v[216:219], v[44:47]\n"
      "v_mfma_f32_16x16x32_bf16 v[48:51], a[72:75], v[220:223], v[48:51]\n"
      "ds_read_b64_tr_b16 a[152:153], v243 offset:64\n"
      "s_add_u32 m0, s74, 0xc400\n"
      "v_mfma_f32_16x16x32_bf16 v[52:55], a[76:79], v[220:223], v[52:55]\n"
      "ds_read_b64_tr_b16 a[154:155], v243 offset:832\n"
      "global_load_lds_dwordx4 v241, s[78:79]\n"
      "v_mfma_f32_16x16x32_bf16 v[56:59], a[80:83], v[220:223], v[56:59]\n"
      "ds_read_b128 v[216:219], v235 offset:20480\n"
      "v_mfma_f32_16x16x32_bf16 v[60:63], a[84:87], v[220:223], v[60:63]\n"
      "ds_read_b64_tr_b16 a[156:157], v243 offset:72\n"
      "v_mfma_f32_16x16x32_bf16 v[64:67], a[88:91], v[220:223], v[64:67]\n"
      "ds_read_b64_tr_b16 a[158:159], v243 offset:840\n"
      "s_add_u32 m0, s74, 0xc800\n"
      "v_mfma_f32_16x16x32_bf16 v[68:71], a[92:95], v[220:223], v[68:71]\n"
      "global_load_lds_dwordx4 v242, s[78:79]\n"
      "s_waitcnt lgkmcnt(10)\n"
      "v_mfma_f32_16x16x32_bf16 v[72:75], a[72:75], v[224:227], v[72:75]\n"
      "ds_read_b64_tr_b16 a[160:161], v243 offset:128\n"
      "v_mfma_f32_16x16x32_bf16 v[76:79], a[76:79], v[224:227], v[76:79]\n"
      "ds_read_b64_tr_b16 a[162:163], v243 offset:896\n"
      "v_mfma_f32_16x16x32_bf16 v[80:83], a[80:83], v[224:227], v[80:83]\n"
      "ds_read_b128 v[220:223], v235 offset:24576\n"
      "s_add_u32 s78, s64, 0x33000\n"
      "s_addc_u32 s79, s65, 0\n"
      "s_add_u32 m0, s74, 0xcc00\n"
      "v_mfma_f32_16x16x32_bf16 v[84:87], a[84:87], v[224:227], v[84:87]\n"
      "ds_read_b64_tr_b16 a[164:165], v243 offset:136\n"
      "global_load_lds_dwordx4 v240, s[78:79]\n"
      "v_mfma_f32_16x16x32_bf16 v[88:91], a[88:91], v[224:227], v[88:91]\n"
      "ds_read_b64_tr_b16 a[166:167], v243 offset:904\n"
      "v_mfma_f32_16x16x32_bf16 v[92:95], a[92:95], v[224:227], v[92:95]\n"
      "s_waitcnt lgkmcnt(12)\n"
      "v_mfma_f32_16x16x32_bf16 v[96:99], a[72:75], v[228:231], v[96:99]\n"
      "s_add_u32 m0, s74, 0xd000\n"
      "v_mfma_f32_16x16x32_bf16 v[100:103], a[76:79], v[228:231], v[100:103]\n"
      "global_load_lds_dwordx4 v241, s[78:79]\n"
      "v_mfma_f32_16x16x32_bf16 v[104:107], a[80:83], v[228:231], v[104:107]\n"
      "ds_read_b128 v[224:227], v235 offset:28672\n"
      "v_mfma_f32_16x16x32_bf16 v[108:111], a[84:87], v[228:231], v[108:111]\n"
      "v_mfma_f32_16x16x32_bf16 v[112:115], a[88:91], v[228:231], v[112:115]\n"
      "s_waitcnt vmcnt(6)\n"
      "s_add_u32 m0, s74, 0xd400\n"
      "v_mfma_f32_16x16x32_bf16 v[116:119], a[92:95], v[228:231], v[116:119]\n"
      "global_load_lds_dwordx4 v242, s[78:79]\n"
      "s_waitcnt lgkmcnt(8)\n"
      "v_mfma_f32_16x16x32_bf16 v[120:123], a[72:75], v[216:219], v[120:123]\n"
      "ds_read_b64_tr_b16 a[168:169], v243 offset:24576\n"
      "v_mfma_f32_16x16x32_bf16 v[124:127], a[76:79], v[216:219], v[124:127]\n"
      "ds_read_b64_tr_b16 a[170:171], v243 offset:25344\n"
      "v_mfma_f32_16x16x32_bf16 v[128:131], a[80:83], v[216:219], v[128:131]\n"
      "ds_read_b128 v[228:231], v235 offset:32768\n"
      "v_add_u32 v235, 0x9000, v235\n"
      "s_waitcnt lgkmcnt(4)\n"
      "s_add_u32 s78, s64, 0x36000\n"
      "s_addc_u32 s79, s65, 0\n"
      "s_add_u32 m0, s74, 0x0\n"
      "v_mfma_f32_16x16x32_bf16 v[132:135], a[84:87], v[216:219], v[132:135]\n"
      "ds_read_b64_tr_b16 a[172:173], v243 offset:24584\n"
      "global_load_lds_dwordx4 v240, s[78:79]\n"
      "v_mfma_f32_16x16x32_bf16 v[136:139], a[88:91], v[216:219], v[136:139]\n"
      "ds_read_b64_tr_b16 a[174:175], v243 offset:25352\n"
      "v_mfma_f32_16x16x32_bf16 v[140:143], a[92:95], v[216:219], v[140:143]\n"
      "v_mfma_f32_16x16x32_bf16 v[144:147], a[72:75], v[220:223], v[144:147]\n"
      "ds_read_b64_tr_b16 a[176:177], v243 offset:24640\n"
      "s_waitcnt vmcnt(7)\n"
      "v_cmp_ne_u64 vcc, s[80:81], v[244:245]\n"
      "v_cmp_ne_u64 s[56:57], s[80:81], v[246:247]\n"
      "s_nop 3\n"
      "s_or_b64 vcc, vcc, s[56:57]\n"
      "s_cbranch_scc0 LP1_1\n"
      "s_mov_b32 s82, 0x40000\n"
      "LP0_1:\n"
      "s_sleep 2\n"
      "global_load_dwordx4 v[244:247], v248, s[28:29] sc1\n"
      "s_sub_u32 s82, s82, 1\n"
      "s_cbranch_scc1 LP2_1\n"
      "s_waitcnt vmcnt(0)\n"
      "v_cmp_ne_u64 vcc, s[80:81], v[244:245]\n"
      "v_cmp_ne_u64 s[56:57], s[80:81], v[246:247]\n"
      "s_nop 3\n"
      "s_or_b64 vcc, vcc, s[56:57]\n"
      "s_cbranch_scc1 LP0_1\n"
      "LP2_1:\n"
      "s_waitcnt vmcnt(0)\n"
      "LP1_1:\n"
      "s_nop 0\n"
      "v_mfma_f32_16x16x32_bf16 v[148:151], a[76:79], v[220:223], v[148:151]\n"
      "s_barrier\n"
      "s_add_u32 m0, s74, 0x400\n"
      "v_mfma_f32_16x16x32_bf16 v[152:155], a[80:83], v[220:223], v[152:155]\n"
      "ds_read_b128 v[216:219], v232 offset:0\n"
      "global_load_lds_dwordx4 v241, s[78:79]\n"
      "v_mfma_f32_16x16x32_bf16 v[156:159], a[84:87], v[220:223], v[156:159]\n"
      "ds_read_b64_tr_b16 a[178:179], v243 offset:25408\n"
      "v_mfma_f32_16x16x32_bf16 v[160:163], a[88:91], v[220:223], v[160:163]\n"
      "ds_read_b64_tr_b16 a[180:181], v243 offset:24648\n"
      "v_mfma_f32_16x16x32_bf16 v[164:167], a[92:95], v[220:223], v[164:167]\n"
      "s_add_u32 m0, s74, 0x800\n"
      "s_waitcnt lgkmcnt(9)\n"
      "v_mfma_f32_16x16x32_bf16 v[168:171], a[72:75], v[224:227], v[168:171]\n"
      "ds_read_b64_tr_b16 a[182:183], v243 offset:25416\n"
      "global_load_lds_dwordx4 v242, s[78:79]\n"
      "v_mfma_f32_16x16x32_bf16 v[172:175], a[76:79], v[224:227], v[172:175]\n"
      "ds_read_b64_tr_b16 a[184:185], v243 offset:24704\n"
      "v_mfma_f32_16x16x32_bf16 v[176:179], a[80:83], v[224:227], v[176:179]\n"
      "ds_read_b128 v[220:223], v232 offset:4096\n"
      "v_mfma_f32_16x16x32_bf16 v[180:183], a[84:87], v[224:227], v[180:183]\n"
      "ds_read_b64_tr_b16 a[186:187], v243 offset:25472\n"
      "s_add_u32 s78, s64, 0x39000\n"
      "s_addc_u32 s79, s65, 0\n"
      "s_add_u32 m0, s74, 0xc00\n"
      "v_mfma_f32_16x16x32_bf16 v[184:187], a[88:91], v[224:227], v[184:187]\n"
      "ds_read_b64_tr_b16 a[188:189], v243 offset:24712\n"
      "global_load_lds_dwordx4 v240, s[78:79]\n"
      "v_mfma_f32_16x16x32_bf16 v[188:191], a[92:95], v[224:227], v[188:191]\n"
      "s_waitcnt lgkmcnt(11)\n"
      "v_mfma_f32_16x16x32_bf16 v[192:195], a[72:75], v[228:231], v[192:195]\n"
      "ds_read_b64_tr_b16 a[190:191], v243 offset:25480\n"
      "v_mfma_f32_16x16x32_bf16 v[196:199], a[76:79], v[228:231], v[196:199]\n"
      "s_add_u32 m0, s74, 0x1000\n"
      "v_mfma_f32_16x16x32_bf16 v[200:203], a[80:83], v[228:231], v[200:203]\n"
      "ds_read_b128 v[224:227], v232 offset:8192\n"
      "global_load_lds_dwordx4 v241, s[78:79]\n"
      "v_mfma_f32_16x16x32_bf16 v[204:207], a[84:87], v[228:231], v[204:207]\n"
      "v_mfma_f32_16x16x32_bf16 v[208:211], a[88:91], v[228:231], v[208:211]\n"
      "v_mfma_f32_16x16x32_bf16 v[212:215], a[92:95], v[228:231], v[212:215]\n"
      "s_add_u32 m0, s74, 0x1400\n"
      "s_waitcnt lgkmcnt(9)\n"
      "v_mfma_f32_16x16x32_bf16 v[0:3], a[96:99], v[216:219], v[0:3]\n"
      "global_load_lds_dwordx4 v242, s[78:79]\n"
      "v_mfma_f32_16x16x32_bf16 v[4:7], a[100:103], v[216:219], v[4:7]\n"
      "s_barrier\n"
      "v_mfma_f32_16x16x32_bf16 v[8:11], a[104:107], v[216:219], v[8:11]\n"
      "ds_read_b128 v[228:231], v232 offset:12288\n"
      "v_mfma_f32_16x16x32_bf16 v[12:15], a[108:111], v[216:219], v[12:15]\n"
      "s_add_u32 s58, s24, 0x0\n"
      "s_addc_u32 s59, s25, 0\n"
      "global_load_dwordx4 a[240:243], v239, s[58:59] offset:0 sc1\n"
      "v_mfma_f32_16x16x32_bf16 v[16:19], a[112:115], v[216:219], v[16:19]\n"
      "v_mfma_f32_16x16x32_bf16 v[20:23], a[116:119], v[216:219], v[20:23]\n"
      "s_waitcnt lgkmcnt(5)\n"
      "v_mfma_f32_16x16x32_bf16 v[24:27], a[96:99], v[220:223], v[24:27]\n"
      "v_mfma_f32_16x16x32_bf16 v[28:31], a[100:103], v[220:223], v[28:31]\n"
      "s_waitcnt lgkmcnt(2)\n"
      "s_add_u32 s78, s64, 0x3c000\n"
      "s_addc_u32 s79, s65, 0\n"
      "s_add_u32 m0, s74, 0x6000\n"
      "v_mfma_f32_16x16x32_bf16 v[32:35], a[104:107], v[220:223], v[32:35]\n"
      "ds_read_b128 v[216:219], v232 offset:16384\n"
      "global_load_lds_dwordx4 v240, s[78:79]\n"
      "v_mfma_f32_16x16x32_bf16 v[36:39], a[108:111], v[220:223], v[36:39]\n"
      "v_mfma_f32_16x16x32_bf16 v[40:43], a[112:115], v[220:223], v[40:43]\n"
      "s_waitcnt vmcnt(8)\n"
      "v_mfma_f32_16x16x32_bf16 v[44:47], a[116:119], v[220:223], v[44:47]\n"
      "s_add_u32 m0, s74, 0x6400\n"
      "s_waitcnt lgkmcnt(2)\n"
      "v_mfma_f32_16x16x32_bf16 v[48:51], a[96:99], v[224:227], v[48:51]\n"
      "ds_read_b64_tr_b16 a[192:193], v243 offset:49152\n"
      "global_load_lds_dwordx4 v241, s[78:79]\n"
      "v_mfma_f32_16x16x32_bf16 v[52:55], a[100:103], v[224:227], v[52:55]\n"
      "ds_read_b64_tr_b16 a[194:195], v243 offset:49920\n"
      "v_mfma_f32_16x16x32_bf16 v[56:59], a[104:107], v[224:227], v[56:59]\n"
      "ds_read_b128 v[220:223], v232 offset:20480\n"
      "v_mfma_f32_16x16x32_bf16 v[60:63], a[108:111], v[224:227], v[60:63]\n"
      "ds_read_b64_tr_b16 a[196:197], v243 offset:49160\n"
      "s_add_u32 m0, s74, 0x6800\n"
      "v_mfma_f32_16x16x32_bf16 v[64:67], a[112:115], v[224:227], v[64:67]\n"
      "ds_read_b64_tr_b16 a[198:199], v243 offset:49928\n"
      "global_load_lds_dwordx4 v242, s[78:79]\n"
      "v_mfma_f32_16x16x32_bf16 v[68:71], a[116:119], v[224:227], v[68:71]\n"
      "s_waitcnt lgkmcnt(6)\n"
      "v_mfma_f32_16x16x32_bf16 v[72:75], a[96:99], v[228:231], v[72:75]\n"
      "ds_read_b64_tr_b16 a[200:201], v243 offset:49216\n"
      "v_mfma_f32_16x16x32_bf16 v[76:79], a[100:103], v[228:231], v[76:79]\n"
      "ds_read_b64_tr_b16 a[202:203], v243 offset:49984\n"
      "s_add_u32 s78, s64, 0x3f000\n"
      "s_addc_u32 s79, s65, 0\n"
      "s_add_u32 m0, s74, 0x6c00\n"
      "v_mfma_f32_16x16x32_bf16 v[80:83], a[104:107], v[228:231], v[80:83]\n"
      "ds_read_b128 v[224:227], v232 offset:24576\n"
      "global_load_lds_dwordx4 v240, s[78:79]\n"
      "v_mfma_f32_16x16x32_bf16 v[84:87], a[108:111], v[228:231], v[84:87]\n"
      "ds_read_b64_tr_b16 a[204:205], v243 offset:49224\n"
      "v_mfma_f32_16x16x32_bf16 v[88:91], a[112:115], v[228:231], v[88:91]\n"
      "ds_read_b64_tr_b16 a[206:207], v243 offset:49992\n"
      "v_mfma_f32_16x16x32_bf16 v[92:95], a[116:119], v[228:231], v[92:95]\n"
      "s_add_u32 m0, s74, 0x7000\n"
      "s_waitcnt lgkmcnt(10)\n"
      "v_mfma_f32_16x16x32_bf16 v[96:99], a[96:99], v[216:219], v[96:99]\n"
      "ds_read_b64_tr_b16 a[208:209], v243 offset:49280\n"
      "global_load_lds_dwordx4 v241, s[78:79]\n"
      "v_mfma_f32_16x16x32_bf16 v[100:103], a[100:103], v[216:219], v[100:103]\n"
      "ds_read_b64_tr_b16 a[210:211], v243 offset:50048\n"
      "v_mfma_f32_16x16x32_bf16 v[104:107], a[104:107], v[216:219], v[104:107]\n"
      "ds_read_b128 v[228:231], v232 offset:28672\n"
      "v_mfma_f32_16x16x32_bf16 v[108:111], a[108:111], v[216:219], v[108:111]\n"
      "ds_read_b64_tr_b16 a[212:213], v243 offset:49288\n"
      "s_add_u32 m0, s74, 0x7400\n"
      "v_mfma_f32_16x16x32_bf16 v[112:115], a[112:115], v[216:219], v[112:115]\n"
      "ds_read_b64_tr_b16 a[214:215], v243 offset:50056\n"
      "global_load_lds_dwordx4 v242, s[78:79]\n"
      "v_mfma_f32_16x16x32_bf16 v[116:119], a[116:119], v[216:219], v[116:119]\n"
      "s_waitcnt lgkmcnt(12)\n"
      "v_mfma_f32_16x16x32_bf16 v[120:123], a[96:99], v[220:223], v[120:123]\n"
      "v_mfma_f32_16x16x32_bf16 v[124:127], a[100:103], v[220:223], v[124:127]\n"
      "global_load_dwordx4 a[244:247], v239, s[58:59] offset:1024 sc1\n"
      "v_mfma_f32_16x16x32_bf16 v[128:131], a[104:107], v[220:223], v[128:131]\n"
      "ds_read_b128 v[216:219], v232 offset:32768\n"
      "v_add_u32 v232, 0xffff7000, v232\n"
      "v_mfma_f32_16x16x32_bf16 v[132:135], a[108:111], v[220:223], v[132:135]\n"
      "v_mfma_f32_16x16x32_bf16 v[136:139], a[112:115], v[220:223], v[136:139]\n"
      "v_mfma_f32_16x16x32_bf16 v[140:143], a[116:119], v[220:223], v[140:143]\n"
      "global_load_dwordx4 a[248:251], v239, s[58:59] offset:2048 sc1\n"
      "s_waitcnt lgkmcnt(8)\n"
      "v_mfma_f32_16x16x32_bf16 v[144:147], a[96:99], v[224:227], v[144:147]\n"
      "s_waitcnt vmcnt(9)\n"
      "v_mfma_f32_16x16x32_bf16 v[148:151], a[100:103], v[224:227], v[148:151]\n"
      "ds_read_b64_tr_b16 a[216:217], v243 offset:0\n"
      "v_mfma_f32_16x16x32_bf16 v[152:155], a[104:107], v[224:227], v[152:155]\n"
      "ds_read_b128 v[220:223], v233 offset:0\n"
      "v_mfma_f32_16x16x32_bf16 v[156:159], a[108:111], v[224:227], v[156:159]\n"
      "ds_read_b64_tr_b16 a[218:219], v243 offset:768\n"
      "s_waitcnt lgkmcnt(4)\n"
      "s_add_u32 s78, s64, 0x42000\n"
      "s_addc_u32 s79, s65, 0\n"
      "s_add_u32 m0, s74, 0xc000\n"
      "v_mfma_f32_16x16x32_bf16 v[160:163], a[112:115], v[224:227], v[160:163]\n"
      "ds_read_b64_tr_b16 a[220:221], v243 offset:8\n"
      "global_load_lds_dwordx4 v240, s[78:79]\n"
      "v_mfma_f32_16x16x32_bf16 v[164:167], a[116:119], v[224:227], v[164:167]\n"
      "v_mfma_f32_16x16x32_bf16 v[168:171], a[96:99], v[228:231], v[168:171]\n"
      "ds_read_b64_tr_b16 a[222:223], v243 offset:776\n"
      "v_mfma_f32_16x16x32_bf16 v[172:175], a[100:103], v[228:231], v[172:175]\n"
      "ds_read_b64_tr_b16 a[224:225], v243 offset:64\n"
      "s_add_u32 m0, s74, 0xc400\n"
      "v_mfma_f32_16x16x32_bf16 v[176:179], a[104:107], v[228:231], v[176:179]\n"
      "ds_read_b128 v[224:227], v233 offset:4096\n"
      "global_load_lds_dwordx4 v241, s[78:79]\n"
      "v_mfma_f32_16x16x32_bf16 v[180:183], a[108:111], v[228:231], v[180:183]\n"
      "ds_read_b64_tr_b16 a[226:227], v243 offset:832\n"
      "v_mfma_f32_16x16x32_bf16 v[184:187], a[112:115], v[228:231], v[184:187]\n"
      "ds_read_b64_tr_b16 a[228:229], v243 offset:72\n"
      "v_mfma_f32_16x16x32_bf16 v[188:191], a[116:119], v[228:231], v[188:191]\n"
      "s_add_u32 m0, s74, 0xc800\n"
      "s_waitcnt lgkmcnt(9)\n"
      "v_mfma_f32_16x16x32_bf16 v[192:195], a[96:99], v[216:219], v[192:195]\n"
      "ds_read_b64_tr_b16 a[230:231], v243 offset:840\n"
      "global_load_lds_dwordx4 v242, s[78:79]\n"
      "v_mfma_f32_16x16x32_bf16 v[196:199], a[100:103], v[216:219], v[196:199]\n"
      "ds_read_b64_tr_b16 a[232:233], v243 offset:128\n"
      "v_mfma_f32_16x16x32_bf16 v[200:203], a[104:107], v[216:219], v[200:203]\n"
      "ds_read_b128 v[228:231], v233 offset:8192\n"
      "v_mfma_f32_16x16x32_bf16 v[204:207], a[108:111], v[216:219], v[204:207]\n"
      "ds_read_b64_tr_b16 a[234:235], v243 offset:896\n"
      "s_add_u32 s78, s64, 0x45000\n"
      "s_addc_u32 s79, s65, 0\n"
      "s_add_u32 m0, s74, 0xcc00\n"
      "v_mfma_f32_16x16x32_bf16 v[208:211], a[112:115], v[216:219], v[208:211]\n"
      "ds_read_b64_tr_b16 a[236:237], v243 offset:136\n"
      "global_load_lds_dwordx4 v240, s[78:79]\n"
      "v_mfma_f32_16x16x32_bf16 v[212:215], a[116:119], v[216:219], v[212:215]\n"
      "s_waitcnt lgkmcnt(12)\n"
      "v_mfma_f32_16x16x32_bf16 v[0:3], a[120:123], v[220:223], v[0:3]\n"
      "ds_read_b64_tr_b16 a[238:239], v243 offset:904\n"
      "v_mfma_f32_16x16x32_bf16 v[4:7], a[124:127], v[220:223], v[4:7]\n"
      "s_add_u32 m0, s74, 0xd000\n"
      "v_mfma_f32_16x16x32_bf16 v[8:11], a[128:131], v[220:223], v[8:11]\n"
      "ds_read_b128 v[216:219], v233 offset:12288\n"
      "global_load_lds_dwordx4 v241, s[78:79]\n"
      "v_mfma_f32_16x16x32_bf16 v[12:15], a[132:135], v[220:223], v[12:15]\n"
      "v_mfma_f32_16x16x32_bf16 v[16:19], a[136:139], v[220:223], v[16:19]\n"
      "v_mfma_f32_16x16x32_bf16 v[20:23], a[140:143], v[220:223], v[20:23]\n"
      "s_add_u32 m0, s74, 0xd400\n"
      "s_waitcnt lgkmcnt(9)\n"
      "v_mfma_f32_16x16x32_bf16 v[24:27], a[120:123], v[224:227], v[24:27]\n"
      "global_load_lds_dwordx4 v242, s[78:79]\n"
      "v_mfma_f32_16x16x32_bf16 v[28:31], a[124:127], v[224:227], v[28:31]\n"
      "v_mfma_f32_16x16x32_bf16 v[32:35], a[128:131], v[224:227], v[32:35]\n"
      "ds_read_b128 v[220:223], v233 offset:16384\n"
      "v_mfma_f32_16x16x32_bf16 v[36:39], a[132:135], v[224:227], v[36:39]\n"
      "s_waitcnt vmcnt(8)\n"
      "v_mad_i32_i24 v238, v251, s89, v238\n"
      "s_add_u32 s56, s22, 0x0\n"
      "s_addc_u32 s57, s23, 0\n"
      "s_add_u32 m0, s18, 0xfffffe00\n"
      "v_mfma_f32_16x16x32_bf16 v[40:43], a[136:139], v[224:227], v[40:43]\n"
      "ds_read_b64_tr_b16 a[0:1], v243 offset:24576\n"
      "global_load_lds_dwordx4 v238, s[56:57] offset:512 \n"
      "v_mfma_f32_16x16x32_bf16 v[44:47], a[140:143], v[224:227], v[44:47]\n"
      "s_waitcnt lgkmcnt(6)\n"
      "v_mfma_f32_16x16x32_bf16 v[48:51], a[120:123], v[228:231], v[48:51]\n"
      "ds_read_b64_tr_b16 a[2:3], v243 offset:25344\n"
      "v_mad_i32_i24 v238, v251, s90, v238\n"
      "s_add_u32 s56, s22, 0x6000\n"
      "s_addc_u32 s57, s23, 0\n"
      "s_add_u32 m0, s18, 0xe00\n"
      "v_mfma_f32_16x16x32_bf16 v[52:55], a[124:127], v[228:231], v[52:55]\n"
      "ds_read_b64_tr_b16 a[4:5], v243 offset:24584\n"
      "global_load_lds_dwordx4 v238, s[56:57] offset:512 \n"
      "v_mfma_f32_16x16x32_bf16 v[56:59], a[128:131], v[228:231], v[56:59]\n"
      "ds_read_b128 v[224:227], v233 offset:20480\n"
      "v_mfma_f32_16x16x32_bf16 v[60:63], a[132:135], v[228:231], v[60:63]\n"
      "ds_read_b64_tr_b16 a[6:7], v243 offset:25352\n"
      "v_mad_i32_i24 v238, v251, s91, v238\n"
      "s_add_u32 s56, s22, 0xc000\n"
      "s_addc_u32 s57, s23, 0\n"
      "s_add_u32 m0, s18, 0x1e00\n"
      "v_mfma_f32_16x16x32_bf16 v[64:67], a[136:139], v[228:231], v[64:67]\n"
      "ds_read_b64_tr_b16 a[8:9], v243 offset:24640\n"
      "global_load_lds_dwordx4 v238, s[56:57] offset:512 \n"
      "v_mfma_f32_16x16x32_bf16 v[68:71], a[140:143], v[228:231], v[68:71]\n"
      "s_waitcnt lgkmcnt(7)\n"
      "v_mfma_f32_16x16x32_bf16 v[72:75], a[120:123], v[216:219], v[72:75]\n"
      "ds_read_b64_tr_b16 a[10:11], v243 offset:25408\n"
      "v_mad_i32_i24 v238, v251, s92, v238\n"
      "s_add_u32 s56, s22, 0x12000\n"
      "s_addc_u32 s57, s23, 0\n"
      "s_add_u32 m0, s18, 0x2e00\n"
      "v_mfma_f32_16x16x32_bf16 v[76:79], a[124:127], v[216:219], v[76:79]\n"
      "ds_read_b64_tr_b16 a[12:13], v243 offset:24648\n"
      "global_load_lds_dwordx4 v238, s[56:57] offset:512 \n"
      "v_mfma_f32_16x16x32_bf16 v[80:83], a[128:131], v[216:219], v[80:83]\n"
      "ds_read_b128 v[228:231], v233 offset:24576\n"
      "v_mfma_f32_16x16x32_bf16 v[84:87], a[132:135], v[216:219], v[84:87]\n"
      "ds_read_b64_tr_b16 a[14:15], v243 offset:25416\n"
      "v_mad_i32_i24 v238, v251, s93, v238\n"
      "s_add_u32 s56, s22, 0x18000\n"
      "s_addc_u32 s57, s23, 0\n"
      "s_add_u32 m0, s18, 0x3e00\n"
      "v_mfma_f32_16x16x32_bf16 v[88:91], a[136:139], v[216:219], v[88:91]\n"
      "ds_read_b64_tr_b16 a[16:17], v243 offset:24704\n"
      "global_load_lds_dwordx4 v238, s[56:57] offset:512 \n"
      "v_mfma_f32_16x16x32_bf16 v[92:95], a[140:143], v[216:219], v[92:95]\n"
      "s_waitcnt lgkmcnt(11)\n"
      "v_mfma_f32_16x16x32_bf16 v[96:99], a[120:123], v[220:223], v[96:99]\n"
      "ds_read_b64_tr_b16 a[18:19], v243 offset:25472\n"
      "v_mad_i32_i24 v238, v251, s94, v238\n"
      "s_add_u32 s56, s22, 0x1e000\n"
      "s_addc_u32 s57, s23, 0\n"
      "s_add_u32 m0, s18, 0x4e00\n"
      "v_mfma_f32_16x16x32_bf16 v[100:103], a[124:127], v[220:223], v[100:103]\n"
      "ds_read_b64_tr_b16 a[20:21], v243 offset:24712\n"
      "global_load_lds_dwordx4 v238, s[56:57] offset:512 \n"
      "v_mfma_f32_16x16x32_bf16 v[104:107], a[128:131], v[220:223], v[104:107]\n"
      "ds_read_b128 v[216:219], v233 offset:28672\n"
      "v_mfma_f32_16x16x32_bf16 v[108:111], a[132:135], v[220:223], v[108:111]\n"
      "ds_read_b64_tr_b16 a[22:23], v243 offset:25480\n"
      "v_mad_i32_i24 v238, v251, s95, v238\n"
      "s_add_u32 s56, s22, 0x24000\n"
      "s_addc_u32 s57, s23, 0\n"
      "s_add_u32 m0, s18, 0x5e00\n"
      "v_mfma_f32_16x16x32_bf16 v[112:115], a[136:139], v[220:223], v[112:115]\n"
      "global_load_lds_dwordx4 v238, s[56:57] offset:512 \n"
      "v_mfma_f32_16x16x32_bf16 v[116:119], a[140:143], v[220:223], v[116:119]\n"
      "s_waitcnt lgkmcnt(11)\n"
      "v_mfma_f32_16x16x32_bf16 v[120:123], a[120:123], v[224:227], v[120:123]\n"
      "v_mad_i32_i24 v238, v251, s96, v238\n"
      "s_add_u32 s56, s22, 0x2a000\n"
      "s_addc_u32 s57, s23, 0\n"
      "s_add_u32 m0, s18, 0x6e00\n"
      "v_mfma_f32_16x16x32_bf16 v[124:127], a[124:127], v[224:227], v[124:127]\n"
      "global_load_lds_dwordx4 v238, s[56:57] offset:512 \n"
      "v_mfma_f32_16x16x32_bf16 v[128:131], a[128:131], v[224:227], v[128:131]\n"
      "ds_read_b128 v[220:223], v233 offset:32768\n"
      "v_add_u32 v233, 0xffff7000, v233\n"
      "v_mfma_f32_16x16x32_bf16 v[132:135], a[132:135], v[224:227], v[132:135]\n"
      "v_mad_i32_i24 v238, v251, s97, v238\n"
      "s_add_u32 s56, s22, 0x30000\n"
      "s_addc_u32 s57, s23, 0\n"
      "s_add_u32 m0, s18, 0x7e00\n"
      "v_mfma_f32_16x16x32_bf16 v[136:139], a[136:139], v[224:227], v[136:139]\n"
      "global_load_lds_dwordx4 v238, s[56:57] offset:512 \n"
      "v_mfma_f32_16x16x32_bf16 v[140:143], a[140:143], v[224:227], v[140:143]\n"
      "s_waitcnt lgkmcnt(7)\n"
      "v_mfma_f32_16x16x32_bf16 v[144:147], a[120:123], v[228:231], v[144:147]\n"
      "v_mfma_f32_16x16x32_bf16 v[148:151], a[124:127], v[228:231], v[148:151]\n"
      "global_load_dwordx4 a[252:255], v239, s[58:59] offset:3072 sc1\n"
      "v_mfma_f32_16x16x32_bf16 v[152:155], a[128:131], v[228:231], v[152:155]\n"
      "ds_read_b128 v[224:227], v234 offset:0\n"
      "v_mfma_f32_16x16x32_bf16 v[156:159], a[132:135], v[228:231], v[156:159]\n"
      "v_mfma_f32_16x16x32_bf16 v[160:163], a[136:139], v[228:231], v[160:163]\n"
      "v_mfma_f32_16x16x32_bf16 v[164:167], a[140:143], v[228:231], v[164:167]\n"
      "s_add_u32 s58, s24, 0x1000\n"
      "s_addc_u32 s59, s25, 0\n"
      "global_load_dwordx4 a[48:51], v239, s[58:59] offset:0 sc1\n"
      "s_waitcnt lgkmcnt(3)\n"
      "v_mfma_f32_16x16x32_bf16 v[168:171], a[120:123], v[216:219], v[168:171]\n"
      "s_waitcnt vmcnt(11)\n"
      "v_mfma_f32_16x16x32_bf16 v[172:175], a[124:127], v[216:219], v[172:175]\n"
      "ds_read_b64_tr_b16 a[24:25], v243 offset:49152\n"
      "v_mfma_f32_16x16x32_bf16 v[176:179], a[128:131], v[216:219], v[176:179]\n"
      "LPD5:\n"
      "s_cbranch_execz LPD8\n"
      "ds_read_b128 v[228:231], v234 offset:4096\n"
      "v_mfma_f32_16x16x32_bf16 v[180:183], a[132:135], v[216:219], v[180:183]\n"
      "ds_read_b64_tr_b16 a[26:27], v243 offset:49920\n"
      "global_load_dwordx4 a[52:55], v239, s[58:59] offset:1024 sc1\n"
      "v_mfma_f32_16x16x32_bf16 v[184:187], a[136:139], v[216:219], v[184:187]\n"
      "ds_read_b64_tr_b16 a[28:29], v243 offset:49160\n"
      "v_mfma_f32_16x16x32_bf16 v[188:191], a[140:143], v[216:219], v[188:191]\n"
      "s_waitcnt lgkmcnt(5)\n"
      "v_mfma_f32_16x16x32_bf16 v[192:195], a[120:123], v[220:223], v[192:195]\n"
      "ds_read_b64_tr_b16 a[30:31], v243 offset:49928\n"
      "v_mfma_f32_16x16x32_bf16 v[196:199], a[124:127], v[220:223], v[196:199]\n"
      "ds_read_b64_tr_b16 a[32:33], v243 offset:49216\n"
      "global_load_dwordx4 a[56:59], v239, s[58:59] offset:2048 sc1\n"
      "v_mfma_f32_16x16x32_bf16 v[200:203], a[128:131], v[220:223], v[200:203]\n"
      "ds_read_b128 v[216:219], v234 offset:8192\n"
      "v_mfma_f32_16x16x32_bf16 v[204:207], a[132:135], v[220:223], v[204:207]\n"
      "ds_read_b64_tr_b16 a[34:35], v243 offset:49984\n"
      "v_mfma_f32_16x16x32_bf16 v[208:211], a[136:139], v[220:223], v[208:211]\n"
      "ds_read_b64_tr_b16 a[36:37], v243 offset:49224\n"
      "v_mfma_f32_16x16x32_bf16 v[212:215], a[140:143], v[220:223], v[212:215]\n"
      "global_load_dwordx4 a[60:63], v239, s[58:59] offset:3072 sc1\n"
      "s_waitcnt lgkmcnt(9)\n"
      "v_mfma_f32_16x16x32_bf16 v[0:3], a[144:147], v[224:227], v[0:3]\n"
      "ds_read_b64_tr_b16 a[38:39], v243 offset:49992\n"
      "v_mfma_f32_16x16x32_bf16 v[4:7], a[148:151], v[224:227], v[4:7]\n"
      "ds_read_b64_tr_b16 a[40:41], v243 offset:49280\n"
      "v_mfma_f32_16x16x32_bf16 v[8:11], a[152:155], v[224:227], v[8:11]\n"
      "ds_read_b128 v[220:223], v234 offset:12288\n"
      "v_mfma_f32_16x16x32_bf16 v[12:15], a[156:159], v[224:227], v[12:15]\n"
      "ds_read_b64_tr_b16 a[42:43], v243 offset:50048\n"
      "s_add_u32 s58, s24, 0x2000\n"
      "s_addc_u32 s59, s25, 0\n"
      "global_load_dwordx4 a[64:67], v239, s[58:59] offset:0 sc1\n"
      "v_mfma_f32_16x16x32_bf16 v[16:19], a[160:163], v[224:227], v[16:19]\n"
      "ds_read_b64_tr_b16 a[44:45], v243 offset:49288\n"
      "v_mfma_f32_16x16x32_bf16 v[20:23], a[164:167], v[224:227], v[20:23]\n"
      "s_waitcnt lgkmcnt(12)\n"
      "v_mfma_f32_16x16x32_bf16 v[24:27], a[144:147], v[228:231], v[24:27]\n"
      "ds_read_b64_tr_b16 a[46:47], v243 offset:50056\n"
      "v_mfma_f32_16x16x32_bf16 v[28:31], a[148:151], v[228:231], v[28:31]\n"
      "global_load_dwordx4 a[68:71], v239, s[58:59] offset:1024 sc1\n"
      "v_mfma_f32_16x16x32_bf16 v[32:35], a[152:155], v[228:231], v[32:35]\n"
      "ds_read_b128 v[224:227], v234 offset:16384\n"
      "v_mfma_f32_16x16x32_bf16 v[36:39], a[156:159], v[228:231], v[36:39]\n"
      "v_mfma_f32_16x16x32_bf16 v[40:43], a[160:163], v[228:231], v[40:43]\n"
      "v_mfma_f32_16x16x32_bf16 v[44:47], a[164:167], v[228:231], v[44:47]\n"
      "global_load_dwordx4 a[72:75], v239, s[58:59] offset:2048 sc1\n"
      "s_waitcnt lgkmcnt(9)\n"
      "v_mfma_f32_16x16x32_bf16 v[48:51], a[144:147], v[216:219], v[48:51]\n"
      "v_mfma_f32_16x16x32_bf16 v[52:55], a[148:151], v[216:219], v[52:55]\n"
      "v_mfma_f32_16x16x32_bf16 v[56:59], a[152:155], v[216:219], v[56:59]\n"
      "ds_read_b128 v[228:231], v234 offset:20480\n"
      "v_mfma_f32_16x16x32_bf16 v[60:63], a[156:159], v[216:219], v[60:63]\n"
      "global_load_dwordx4 a[76:79], v239, s[58:59] offset:3072 sc1\n"
      "v_mfma_f32_16x16x32_bf16 v[64:67], a[160:163], v[216:219], v[64:67]\n"
      "v_mfma_f32_16x16x32_bf16 v[68:71], a[164:167], v[216:219], v[68:71]\n"
      "s_waitcnt lgkmcnt(5)\n"
      "v_mfma_f32_16x16x32_bf16 v[72:75], a[144:147], v[220:223], v[72:75]\n"
      "v_mfma_f32_16x16x32_bf16 v[76:79], a[148:151], v[220:223], v[76:79]\n"
      "s_add_u32 s58, s24, 0x3000\n"
      "s_addc_u32 s59, s25, 0\n"
      "global_load_dwordx4 a[80:83], v239, s[58:59] offset:0 sc1\n"
      "v_mfma_f32_16x16x32_bf16 v[80:83], a[152:155], v[220:223], v[80:83]\n"
      "ds_read_b128 v[216:219], v234 offset:24576\n"
      "v_mfma_f32_16x16x32_bf16 v[84:87], a[156:159], v[220:223], v[84:87]\n"
      "v_mfma_f32_16x16x32_bf16 v[88:91], a[160:163], v[220:223], v[88:91]\n"
      "v_mfma_f32_16x16x32_bf16 v[92:95], a[164:167], v[220:223], v[92:95]\n"
      "global_load_dwordx4 a[84:87], v239, s[58:59] offset:1024 sc1\n"
      "s_waitcnt lgkmcnt(2)\n"
      "v_mfma_f32_16x16x32_bf16 v[96:99], a[144:147], v[224:227], v[96:99]\n"
      "v_mfma_f32_16x16x32_bf16 v[100:103], a[148:151], v[224:227], v[100:103]\n"
      "v_mfma_f32_16x16x32_bf16 v[104:107], a[152:155], v[224:227], v[104:107]\n"
      "ds_read_b128 v[220:223], v234 offset:28672\n"
      "v_mfma_f32_16x16x32_bf16 v[108:111], a[156:159], v[224:227], v[108:111]\n"
      "global_load_dwordx4 a[88:91], v239, s[58:59] offset:2048 sc1\n"
      "v_mfma_f32_16x16x32_bf16 v[112:115], a[160:163], v[224:227], v[112:115]\n"
      "v_mfma_f32_16x16x32_bf16 v[116:119], a[164:167], v[224:227], v[116:119]\n"
      "s_waitcnt lgkmcnt(2)\n"
      "v_mfma_f32_16x16x32_bf16 v[120:123], a[144:147], v[228:231], v[120:123]\n"
      "v_mfma_f32_16x16x32_bf16 v[124:127], a[148:151], v[228:231], v[124:127]\n"
      "global_load_dwordx4 a[92:95], v239, s[58:59] offset:3072 sc1\n"
      "v_mfma_f32_16x16x32_bf16 v[128:131], a[152:155], v[228:231], v[128:131]\n"
      "ds_read_b128 v[224:227], v234 offset:32768\n"
      "v_add_u32 v234, 0xffff7000, v234\n"
      "v_mfma_f32_16x16x32_bf16 v[132:135], a[156:159], v[228:231], v[132:135]\n"
      "v_mfma_f32_16x16x32_bf16 v[136:139], a[160:163], v[228:231], v[136:139]\n"
      "v_mfma_f32_16x16x32_bf16 v[140:143], a[164:167], v[228:231], v[140:143]\n"
      "s_add_u32 s58, s24, 0x4000\n"
      "s_addc_u32 s59, s25, 0\n"
      "global_load_dwordx4 a[96:99], v239, s[58:59] offset:0 sc1\n"
      "s_waitcnt lgkmcnt(2)\n"
      "v_mfma_f32_16x16x32_bf16 v[144:147], a[144:147], v[216:219], v[144:147]\n"
      "ds_read_b64 v[236:237], v250 offset:4104\n"
      "v_mfma_f32_16x16x32_bf16 v[148:151], a[148:151], v[216:219], v[148:151]\n"
      "v_mfma_f32_16x16x32_bf16 v[152:155], a[152:155], v[216:219], v[152:155]\n"
      "ds_read_b128 v[228:231], v235 offset:0\n"
      "v_mfma_f32_16x16x32_bf16 v[156:159], a[156:159], v[216:219], v[156:159]\n"
      "global_load_dwordx4 a[100:103], v239, s[58:59] offset:1024 sc1\n"
      "v_mfma_f32_16x16x32_bf16 v[160:163], a[160:163], v[216:219], v[160:163]\n"
      "v_mfma_f32_16x16x32_bf16 v[164:167], a[164:167], v[216:219], v[164:167]\n"
      "s_waitcnt lgkmcnt(3)\n"
      "v_mfma_f32_16x16x32_bf16 v[168:171], a[144:147], v[220:223], v[168:171]\n"
      "v_mfma_f32_16x16x32_bf16 v[172:175], a[148:151], v[220:223], v[172:175]\n"
      "global_load_dwordx4 a[104:107], v239, s[58:59] offset:2048 sc1\n"
      "v_mfma_f32_16x16x32_bf16 v[176:179], a[152:155], v[220:223], v[176:179]\n"
      "ds_read_b128 v[216:219], v235 offset:4096\n"
      "v_mfma_f32_16x16x32_bf16 v[180:183], a[156:159], v[220:223], v[180:183]\n"
      "v_mfma_f32_16x16x32_bf16 v[184:187], a[160:163], v[220:223], v[184:187]\n"
      "v_mfma_f32_16x16x32_bf16 v[188:191], a[164:167], v[220:223], v[188:191]\n"
      "global_load_dwordx4 a[108:111], v239, s[58:59] offset:3072 sc1\n"
      "s_waitcnt lgkmcnt(3)\n"
      "v_mfma_f32_16x16x32_bf16 v[192:195], a[144:147], v[224:227], v[192:195]\n"
      "v_mfma_f32_16x16x32_bf16 v[196:199], a[148:151], v[224:227], v[196:199]\n"
      "v_mfma_f32_16x16x32_bf16 v[200:203], a[152:155], v[224:227], v[200:203]\n"
      "ds_read_b128 v[220:223], v235 offset:8192\n"
      "v_mfma_f32_16x16x32_bf16 v[204:207], a[156:159], v[224:227], v[204:207]\n"
      "s_add_u32 s58, s24, 0x5000\n"
      "s_addc_u32 s59, s25, 0\n"
      "global_load_dwordx4 a[112:115], v239, s[58:59] offset:0 sc1\n"
      "v_mfma_f32_16x16x32_bf16 v[208:211], a[160:163], v[224:227], v[208:211]\n"
      "v_mfma_f32_16x16x32_bf16 v[212:215], a[164:167], v[224:227], v[212:215]\n"
      "s_waitcnt lgkmcnt(2)\n"
      "v_mfma_f32_16x16x32_bf16 v[0:3], a[168:171], v[228:231], v[0:3]\n"
      "v_mfma_f32_16x16x32_bf16 v[4:7], a[172:175], v[228:231], v[4:7]\n"
      "global_load_dwordx4 a[116:119], v239, s[58:59] offset:1024 sc1\n"
      "v_mfma_f32_16x16x32_bf16 v[8:11], a[176:179], v[228:231], v[8:11]\n"
      "ds_read_b128 v[224:227], v235 offset:12288\n"
      "v_mfma_f32_16x16x32_bf16 v[12:15], a[180:183], v[228:231], v[12:15]\n"
      "v_mfma_f32_16x16x32_bf16 v[16:19], a[184:187], v[228:231], v[16:19]\n"
      "v_mfma_f32_16x16x32_bf16 v[20:23], a[188:191], v[228:231], v[20:23]\n"
      "global_load_dwordx4 a[120:123], v239, s[58:59] offset:2048 sc1\n"
      "s_waitcnt lgkmcnt(2)\n"
      "v_mfma_f32_16x16x32_bf16 v[24:27], a[168:171], v[216:219], v[24:27]\n"
      "v_mfma_f32_16x16x32_bf16 v[28:31], a[172:175], v[216:219], v[28:31]\n"
      "v_mfma_f32_16x16x32_bf16 v[32:35], a[176:179], v[216:219], v[32:35]\n"
      "ds_read_b128 v[228:231], v235 offset:16384\n"
      "v_mfma_f32_16x16x32_bf16 v[36:39], a[180:183], v[216:219], v[36:39]\n"
      "global_load_dwordx4 a[124:127], v239, s[58:59] offset:3072 sc1\n"
      "v_mfma_f32_16x16x32_bf16 v[40:43], a[184:187], v[216:219], v[40:43]\n"
      "v_mfma_f32_16x16x32_bf16 v[44:47], a[188:191], v[216:219], v[44:47]\n"
      "s_waitcnt lgkmcnt(2)\n"
      "v_mfma_f32_16x16x32_bf16 v[48:51], a[168:171], v[220:223], v[48:51]\n"
      "v_mfma_f32_16x16x32_bf16 v[52:55], a[172:175], v[220:223], v[52:55]\n"
      "s_add_u32 s58, s24, 0x6000\n"
      "s_addc_u32 s59, s25, 0\n"
      "global_load_dwordx4 a[128:131], v239, s[58:59] offset:0 sc1\n"
      "v_mfma_f32_16x16x32_bf16 v[56:59], a[176:179], v[220:223], v[56:59]\n"
      "ds_read_b128 v[216:219], v235 offset:20480\n"
      "v_mfma_f32_16x16x32_bf16 v[60:63], a[180:183], v[220:223], v[60:63]\n"
      "v_mfma_f32_16x16x32_bf16 v[64:67], a[184:187], v[220:223], v[64:67]\n"
      "v_mfma_f32_16x16x32_bf16 v[68:71], a[188:191], v[220:223], v[68:71]\n"
      "global_load_dwordx4 a[132:135], v239, s[58:59] offset:1024 sc1\n"
      "s_waitcnt lgkmcnt(2)\n"
      "v_mfma_f32_16x16x32_bf16 v[72:75], a[168:171], v[224:227], v[72:75]\n"
      "v_mfma_f32_16x16x32_bf16 v[76:79], a[172:175], v[224:227], v[76:79]\n"
      "v_mfma_f32_16x16x32_bf16 v[80:83], a[176:179], v[224:227], v[80:83]\n"
      "ds_read_b128 v[220:223], v235 offset:24576\n"
      "v_mfma_f32_16x16x32_bf16 v[84:87], a[180:183], v[224:227], v[84:87]\n"
      "global_load_dwordx4 a[136:139], v239, s[58:59] offset:2048 sc1\n"
      "v_mfma_f32_16x16x32_bf16 v[88:91], a[184:187], v[224:227], v[88:91]\n"
      "v_mfma_f32_16x16x32_bf16 v[92:95], a[188:191], v[224:227], v[92:95]\n"
      "s_waitcnt lgkmcnt(2)\n"
      "v_mfma_f32_16x16x32_bf16 v[96:99], a[168:171], v[228:231], v[96:99]\n"
      "v_mfma_f32_16x16x32_bf16 v[100:103], a[172:175], v[228:231], v[100:103]\n"
      "global_load_dwordx4 a[140:143], v239, s[58:59] offset:3072 sc1\n"
      "v_mfma_f32_16x16x32_bf16 v[104:107], a[176:179], v[228:231], v[104:107]\n"
      "ds_read_b128 v[224:227], v235 offset:28672\n"
      "v_mfma_f32_16x16x32_bf16 v[108:111], a[180:183], v[228:231], v[108:111]\n"
      "v_mfma_f32_16x16x32_bf16 v[112:115], a[184:187], v[228:231], v[112:115]\n"
      "v_mfma_f32_16x16x32_bf16 v[116:119], a[188:191], v[228:231], v[116:119]\n"
      "s_add_u32 s58, s24, 0x7000\n"
      "s_addc_u32 s59, s25, 0\n"
      "global_load_dwordx4 a[144:147], v239, s[58:59] offset:0 sc1\n"
      "s_waitcnt lgkmcnt(2)\n"
      "v_mfma_f32_16x16x32_bf16 v[120:123], a[168:171], v[216:219], v[120:123]\n"
      "v_mfma_f32_16x16x32_bf16 v[124:127], a[172:175], v[216:219], v[124:127]\n"
      "v_mfma_f32_16x16x32_bf16 v[128:131], a[176:179], v[216:219], v[128:131]\n"
      "ds_read_b128 v[228:231], v235 offset:32768\n"
      "v_add_u32 v235, 0xffff7000, v235\n"
      "v_mfma_f32_16x16x32_bf16 v[132:135], a[180:183], v[216:219], v[132:135]\n"
      "global_load_dwordx4 a[148:151], v239, s[58:59] offset:1024 sc1\n"
      "v_mfma_f32_16x16x32_bf16 v[136:139], a[184:187], v[216:219], v[136:139]\n"
      "v_mfma_f32_16x16x32_bf16 v[140:143], a[188:191], v[216:219], v[140:143]\n"
      "s_waitcnt lgkmcnt(2)\n"
      "v_mfma_f32_16x16x32_bf16 v[144:147], a[168:171], v[220:223], v[144:147]\n"
      "v_mfma_f32_16x16x32_bf16 v[148:151], a[172:175], v[220:223], v[148:151]\n"
      "s_waitcnt vmcnt(27)\n"
      "s_barrier\n"
      "global_load_dwordx4 a[152:155], v239, s[58:59] offset:2048 sc1\n"
      "v_mfma_f32_16x16x32_bf16 v[152:155], a[176:179], v[220:223], v[152:155]\n"
      "ds_read_b128 v[216:219], v232 offset:0\n"
      "v_mfma_f32_16x16x32_bf16 v[156:159], a[180:183], v[220:223], v[156:159]\n"
      "v_mfma_f32_16x16x32_bf16 v[160:163], a[184:187], v[220:223], v[160:163]\n"
      "v_mfma_f32_16x16x32_bf16 v[164:167], a[188:191], v[220:223], v[164:167]\n"
      "global_load_dwordx4 a[156:159], v239, s[58:59] offset:3072 sc1\n"
      "s_waitcnt lgkmcnt(2)\n"
      "v_mfma_f32_16x16x32_bf16 v[168:171], a[168:171], v[224:227], v[168:171]\n"
      "v_mfma_f32_16x16x32_bf16 v[172:175], a[172:175], v[224:227], v[172:175]\n"
      "v_mfma_f32_16x16x32_bf16 v[176:179], a[176:179], v[224:227], v[176:179]\n"
      "ds_read_b128 v[220:223], v232 offset:4096\n"
      "v_mfma_f32_16x16x32_bf16 v[180:183], a[180:183], v[224:227], v[180:183]\n"
      "s_add_u32 s58, s24, 0x8000\n"
      "s_addc_u32 s59, s25, 0\n"
      "global_load_dwordx4 a[160:163], v239, s[58:59] offset:0 sc1\n"
      "v_mfma_f32_16x16x32_bf16 v[184:187], a[184:187], v[224:227], v[184:187]\n"
      "v_mfma_f32_16x16x32_bf16 v[188:191], a[188:191], v[224:227], v[188:191]\n"
      "s_waitcnt lgkmcnt(2)\n"
      "v_mfma_f32_16x16x32_bf16 v[192:195], a[168:171], v[228:231], v[192:195]\n"
      "v_mfma_f32_16x16x32_bf16 v[196:199], a[172:175], v[228:231], v[196:199]\n"
      "global_load_dwordx4 a[164:167], v239, s[58:59] offset:1024 sc1\n"
      "v_mfma_f32_16x16x32_bf16 v[200:203], a[176:179], v[228:231], v[200:203]\n"
      "ds_read_b128 v[224:227], v232 offset:8192\n"
      "v_mfma_f32_16x16x32_bf16 v[204:207], a[180:183], v[228:231], v[204:207]\n"
      "v_mfma_f32_16x16x32_bf16 v[208:211], a[184:187], v[228:231], v[208:211]\n"
      "v_mfma_f32_16x16x32_bf16 v[212:215], a[188:191], v[228:231], v[212:215]\n"
      "s_waitcnt lgkmcnt(2)\n"
      "v_mfma_f32_16x16x32_bf16 v[0:3], a[192:195], v[216:219], v[0:3]\n"
      "global_load_dwordx4 a[168:171], v239, s[58:59] offset:2048 sc1\n"
      "v_mfma_f32_16x16x32_bf16 v[4:7], a[196:199], v[216:219], v[4:7]\n"
      "v_mfma_f32_16x16x32_bf16 v[8:11], a[200:203], v[216:219], v[8:11]\n"
      "ds_read_b128 v[228:231], v232 offset:12288\n"
      "v_mfma_f32_16x16x32_bf16 v[12:15], a[204:207], v[216:219], v[12:15]\n"
      "v_mfma_f32_16x16x32_bf16 v[16:19], a[208:211], v[216:219], v[16:19]\n"
      "global_load_dwordx4 a[172:175], v239, s[58:59] offset:3072 sc1\n"
      "v_mfma_f32_16x16x32_bf16 v[20:23], a[212:215], v[216:219], v[20:23]\n"
      "s_waitcnt lgkmcnt(2)\n"
      "v_mfma_f32_16x16x32_bf16 v[24:27], a[192:195], v[220:223], v[24:27]\n"
      "v_mfma_f32_16x16x32_bf16 v[28:31], a[196:199], v[220:223], v[28:31]\n"
      "v_mfma_f32_16x16x32_bf16 v[32:35], a[200:203], v[220:223], v[32:35]\n"
      "ds_read_b128 v[216:219], v232 offset:16384\n"
      "s_add_u32 s58, s24, 0x9000\n"
      "s_addc_u32 s59, s25, 0\n"
      "global_load_dwordx4 a[176:179], v239, s[58:59] offset:0 sc1\n"
      "v_mfma_f32_16x16x32_bf16 v[36:39], a[204:207], v[220:223], v[36:39]\n"
      "v_mfma_f32_16x16x32_bf16 v[40:43], a[208:211], v[220:223], v[40:43]\n"
      "v_mfma_f32_16x16x32_bf16 v[44:47], a[212:215], v[220:223], v[44:47]\n"
      "s_waitcnt lgkmcnt(2)\n"
      "v_mfma_f32_16x16x32_bf16 v[48:51], a[192:195], v[224:227], v[48:51]\n"
      "global_load_dwordx4 a[180:183], v239, s[58:59] offset:1024 sc1\n"
      "v_mfma_f32_16x16x32_bf16 v[52:55], a[196:199], v[224:227], v[52:55]\n"
      "v_mfma_f32_16x16x32_bf16 v[56:59], a[200:203], v[224:227], v[56:59]\n"
      "ds_read_b128 v[220:223], v232 offset:20480\n"
      "v_mfma_f32_16x16x32_bf16 v[60:63], a[204:207], v[224:227], v[60:63]\n"
      "v_mfma_f32_16x16x32_bf16 v[64:67], a[208:211], v[224:227], v[64:67]\n"
      "global_load_dwordx4 a[184:187], v239, s[58:59] offset:2048 sc1\n"
      "v_mfma_f32_16x16x32_bf16 v[68:71], a[212:215], v[224:227], v[68:71]\n"
      "s_waitcnt lgkmcnt(2)\n"
      "v_mfma_f32_16x16x32_bf16 v[72:75], a[192:195], v[228:231], v[72:75]\n"
      "v_mfma_f32_16x16x32_bf16 v[76:79], a[196:199], v[228:231], v[76:79]\n"
      "v_mfma_f32_16x16x32_bf16 v[80:83], a[200:203], v[228:231], v[80:83]\n"
      "ds_read_b128 v[224:227], v232 offset:24576\n"
      "global_load_dwordx4 a[188:191], v239, s[58:59] offset:3072 sc1\n"
      "v_mfma_f32_16x16x32_bf16 v[84:87], a[204:207], v[228:231], v[84:87]\n"
      "v_mfma_f32_16x16x32_bf16 v[88:91], a[208:211], v[228:231], v[88:91]\n"
      "v_mfma_f32_16x16x32_bf16 v[92:95], a[212:215], v[228:231], v[92:95]\n"
      "s_waitcnt lgkmcnt(2)\n"
      "v_mfma_f32_16x16x32_bf16 v[96:99], a[192:195], v[216:219], v[96:99]\n"
      "s_mul_i32 s78, s98, s19\n"
      "v_mfma_f32_16x16x32_bf16 v[100:103], a[196:199], v[216:219], v[100:103]\n"
      "v_add_u32 v236, s78, v236\n"
      "v_mfma_f32_16x16x32_bf16 v[104:107], a[200:203], v[216:219], v[104:107]\n"
      "ds_read_b128 v[228:231], v232 offset:28672\n"
      "s_mul_i32 s78, s78, s99\n"
      "v_mfma_f32_16x16x32_bf16 v[108:111], a[204:207], v[216:219], v[108:111]\n"
      "v_add_u32 v237, s78, v237\n"
      "v_mfma_f32_16x16x32_bf16 v[112:115], a[208:211], v[216:219], v[112:115]\n"
      "s_add_u32 s52, s20, s32\n"
      "s_addc_u32 s53, s21, 0\n"
      "s_add_u32 m0, s17, 0x0\n"
      "v_mfma_f32_16x16x32_bf16 v[116:119], a[212:215], v[216:219], v[116:119]\n"
      "global_load_lds_dwordx4 v236, s[52:53] offset:0 \n"
      "s_waitcnt lgkmcnt(2)\n"
      "v_mfma_f32_16x16x32_bf16 v[120:123], a[192:195], v[220:223], v[120:123]\n"
      "v_mfma_f32_16x16x32_bf16 v[124:127], a[196:199], v[220:223], v[124:127]\n"
      "s_add_u32 m0, s17, 0x1f00\n"
      "v_mfma_f32_16x16x32_bf16 v[128:131], a[200:203], v[220:223], v[128:131]\n"
      "ds_read_b128 v[216:219], v232 offset:32768\n"
      "v_add_u32 v232, v249, v232\n"
      "global_load_lds_dwordx4 v236, s[52:53] offset:256 \n"
      "v_mfma_f32_16x16x32_bf16 v[132:135], a[204:207], v[220:223], v[132:135]\n"
      "v_mfma_f32_16x16x32_bf16 v[136:139], a[208:211], v[220:223], v[136:139]\n"
      "s_add_u32 m0, s17, 0x3e00\n"
      "v_mfma_f32_16x16x32_bf16 v[140:143], a[212:215], v[220:223], v[140:143]\n"
      "global_load_lds_dwordx4 v236, s[52:53] offset:512 \n"
      "s_waitcnt lgkmcnt(2)\n"
      "v_mfma_f32_16x16x32_bf16 v[144:147], a[192:195], v[224:227], v[144:147]\n"
      "v_mfma_f32_16x16x32_bf16 v[148:151], a[196:199], v[224:227], v[148:151]\n"
      "s_add_u32 m0, s17, 0x1000\n"
      "v_mfma_f32_16x16x32_bf16 v[152:155], a[200:203], v[224:227], v[152:155]\n"
      "ds_read_b128 v[220:223], v233 offset:0\n"
      "global_load_lds_dwordx4 v237, s[52:53] offset:0 \n"
      "v_mfma_f32_16x16x32_bf16 v[156:159], a[204:207], v[224:227], v[156:159]\n"
      "v_mfma_f32_16x16x32_bf16 v[160:163], a[208:211], v[224:227], v[160:163]\n"
      "s_add_u32 m0, s17, 0x2f00\n"
      "v_mfma_f32_16x16x32_bf16 v[164:167], a[212:215], v[224:227], v[164:167]\n"
      "global_load_lds_dwordx4 v237, s[52:53] offset:256 \n"
      "s_waitcnt lgkmcnt(2)\n"
      "v_mfma_f32_16x16x32_bf16 v[168:171], a[192:195], v[228:231], v[168:171]\n"
      "v_mfma_f32_16x16x32_bf16 v[172:175], a[196:199], v[228:231], v[172:175]\n"
      "s_add_u32 m0, s17, 0x4e00\n"
      "v_mfma_f32_16x16x32_bf16 v[176:179], a[200:203], v[228:231], v[176:179]\n"
      "ds_read_b128 v[224:227], v233 offset:4096\n"
      "global_load_lds_dwordx4 v237, s[52:53] offset:512 \n"
      "v_mfma_f32_16x16x32_bf16 v[180:183], a[204:207], v[228:231], v[180:183]\n"
      "v_mfma_f32_16x16x32_bf16 v[184:187], a[208:211], v[228:231], v[184:187]\n"
      "v_mfma_f32_16x16x32_bf16 v[188:191], a[212:215], v[228:231], v[188:191]\n"
      "s_waitcnt lgkmcnt(2)\n"
      "v_mfma_f32_16x16x32_bf16 v[192:195], a[192:195], v[216:219], v[192:195]\n"
      "v_mfma_f32_16x16x32_bf16 v[196:199], a[196:199], v[216:219], v[196:199]\n"
      "v_mfma_f32_16x16x32_bf16 v[200:203], a[200:203], v[216:219], v[200:203]\n"
      "ds_read_b128 v[228:231], v233 offset:8192\n"
      "v_mfma_f32_16x16x32_bf16 v[204:207], a[204:207], v[216:219], v[204:207]\n"
      "v_mfma_f32_16x16x32_bf16 v[208:211], a[208:211], v[216:219], v[208:211]\n"
      "v_mfma_f32_16x16x32_bf16 v[212:215], a[212:215], v[216:219], v[212:215]\n"
      "s_waitcnt lgkmcnt(2)\n"
      "v_mfma_f32_16x16x32_bf16 v[0:3], a[216:219], v[220:223], v[0:3]\n"
      "s_add_u32 s58, s24, 0xa000\n"
      "s_addc_u32 s59, s25, 0\n"
      "global_load_dwordx4 a[192:195], v239, s[58:59] offset:0 sc1\n"
      "v_mfma_f32_16x16x32_bf16 v[4:7], a[220:223], v[220:223], v[4:7]\n"
      "v_mfma_f32_16x16x32_bf16 v[8:11], a[224:227], v[220:223], v[8:11]\n"
      "ds_read_b128 v[216:219], v233 offset:12288\n"
      "v_mfma_f32_16x16x32_bf16 v[12:15], a[228:231], v[220:223], v[12:15]\n"
      "v_mfma_f32_16x16x32_bf16 v[16:19], a[232:235], v[220:223], v[16:19]\n"
      "global_load_dwordx4 a[196:199], v239, s[58:59] offset:1024 sc1\n"
      "v_mfma_f32_16x16x32_bf16 v[20:23], a[236:239], v[220:223], v[20:23]\n"
      "s_waitcnt lgkmcnt(2)\n"
      "v_mfma_f32_16x16x32_bf16 v[24:27], a[216:219], v[224:227], v[24:27]\n"
      "v_mfma_f32_16x16x32_bf16 v[28:31], a[220:223], v[224:227], v[28:31]\n"
      "v_mfma_f32_16x16x32_bf16 v[32:35], a[224:227], v[224:227], v[32:35]\n"
      "ds_read_b128 v[220:223], v233 offset:16384\n"
      "global_load_dwordx4 a[200:203], v239, s[58:59] offset:2048 sc1\n"
      "v_mfma_f32_16x16x32_bf16 v[36:39], a[228:231], v[224:227], v[36:39]\n"
      "v_mfma_f32_16x16x32_bf16 v[40:43], a[232:235], v[224:227], v[40:43]\n"
      "v_mfma_f32_16x16x32_bf16 v[44:47], a[236:239], v[224:227], v[44:47]\n"
      "s_waitcnt lgkmcnt(2)\n"
      "v_mfma_f32_16x16x32_bf16 v[48:51], a[216:219], v[228:231], v[48:51]\n"
      "global_load_dwordx4 a[204:207], v239, s[58:59] offset:3072 sc1\n"
      "v_mfma_f32_16x16x32_bf16 v[52:55], a[220:223], v[228:231], v[52:55]\n"
      "v_mfma_f32_16x16x32_bf16 v[56:59], a[224:227], v[228:231], v[56:59]\n"
      "ds_read_b128 v[224:227], v233 offset:20480\n"
      "v_mfma_f32_16x16x32_bf16 v[60:63], a[228:231], v[228:231], v[60:63]\n"
      "v_mfma_f32_16x16x32_bf16 v[64:67], a[232:235], v[228:231], v[64:67]\n"
      "s_add_u32 s58, s24, 0xb000\n"
      "s_addc_u32 s59, s25, 0\n"
      "global_load_dwordx4 a[208:211], v239, s[58:59] offset:0 sc1\n"
      "v_mfma_f32_16x16x32_bf16 v[68:71], a[236:239], v[228:231], v[68:71]\n"
      "s_waitcnt lgkmcnt(2)\n"
      "v_mfma_f32_16x16x32_bf16 v[72:75], a[216:219], v[216:219], v[72:75]\n"
      "v_mfma_f32_16x16x32_bf16 v[76:79], a[220:223], v[216:219], v[76:79]\n"
      "v_mfma_f32_16x16x32_bf16 v[80:83], a[224:227], v[216:219], v[80:83]\n"
      "ds_read_b128 v[228:231], v233 offset:24576\n"
      "global_load_dwordx4 a[212:215], v239, s[58:59] offset:1024 sc1\n"
      "v_mfma_f32_16x16x32_bf16 v[84:87], a[228:231], v[216:219], v[84:87]\n"
      "v_mfma_f32_16x16x32_bf16 v[88:91], a[232:235], v[216:219], v[88:91]\n"
      "v_mfma_f32_16x16x32_bf16 v[92:95], a[236:239], v[216:219], v[92:95]\n"
      "s_waitcnt lgkmcnt(2)\n"
      "v_mfma_f32_16x16x32_bf16 v[96:99], a[216:219], v[220:223], v[96:99]\n"
      "v_mfma_f32_16x16x32_bf16 v[100:103], a[220:223], v[220:223], v[100:103]\n"
      "v_mfma_f32_16x16x32_bf16 v[104:107], a[224:227], v[220:223], v[104:107]\n"
      "ds_read_b128 v[216:219], v233 offset:28672\n"
      "v_mfma_f32_16x16x32_bf16 v[108:111], a[228:231], v[220:223], v[108:111]\n"
      "v_mfma_f32_16x16x32_bf16 v[112:115], a[232:235], v[220:223], v[112:115]\n"
      "v_mfma_f32_16x16x32_bf16 v[116:119], a[236:239], v[220:223], v[116:119]\n"
      "s_waitcnt lgkmcnt(2)\n"
      "v_mfma_f32_16x16x32_bf16 v[120:123], a[216:219], v[224:227], v[120:123]\n"
      "v_mfma_f32_16x16x32_bf16 v[124:127], a[220:223], v[224:227], v[124:127]\n"
      "v_mfma_f32_16x16x32_bf16 v[128:131], a[224:227], v[224:227], v[128:131]\n"
      "ds_read_b128 v[220:223], v233 offset:32768\n"
      "v_add_u32 v233, v249, v233\n"
      "v_mfma_f32_16x16x32_bf16 v[132:135], a[228:231], v[224:227], v[132:135]\n"
      "v_mfma_f32_16x16x32_bf16 v[136:139], a[232:235], v[224:227], v[136:139]\n"
      "v_mfma_f32_16x16x32_bf16 v[140:143], a[236:239], v[224:227], v[140:143]\n"
      "s_waitcnt lgkmcnt(2)\n"
      "v_mfma_f32_16x16x32_bf16 v[144:147], a[216:219], v[228:231], v[144:147]\n"
      "v_mfma_f32_16x16x32_bf16 v[148:151], a[220:223], v[228:231], v[148:151]\n"
      "v_mfma_f32_16x16x32_bf16 v[152:155], a[224:227], v[228:231], v[152:155]\n"
      "ds_read_b128 v[224:227], v234 offset:0\n"
      "v_mfma_f32_16x16x32_bf16 v[156:159], a[228:231], v[228:231], v[156:159]\n"
      "v_mfma_f32_16x16x32_bf16 v[160:163], a[232:235], v[228:231], v[160:163]\n"
      "v_mfma_f32_16x16x32_bf16 v[164:167], a[236:239], v[228:231], v[164:167]\n"
      "s_waitcnt lgkmcnt(2)\n"
      "v_mfma_f32_16x16x32_bf16 v[168:171], a[216:219], v[216:219], v[168:171]\n"
      "v_mfma_f32_16x16x32_bf16 v[172:175], a[220:223], v[216:219], v[172:175]\n"
      "v_mfma_f32_16x16x32_bf16 v[176:179], a[224:227], v[216:219], v[176:179]\n"
      "ds_read_b128 v[228:231], v234 offset:4096\n"
      "v_mfma_f32_16x16x32_bf16 v[180:183], a[228:231], v[216:219], v[180:183]\n"
      "v_mfma_f32_16x16x32_bf16 v[184:187], a[232:235], v[216:219], v[184:187]\n"
      "s_add_u32 s52, s42, s32\n"
      "s_addc_u32 s53, s43, 0\n"
      "s_add_u32 m0, s17, 0x6000\n"
      "v_mfma_f32_16x16x32_bf16 v[188:191], a[236:239], v[216:219], v[188:191]\n"
      "global_load_lds_dwordx4 v236, s[52:53] offset:0 \n"
      "s_waitcnt lgkmcnt(2)\n"
      "v_mfma_f32_16x16x32_bf16 v[192:195], a[216:219], v[220:223], v[192:195]\n"
      "v_mfma_f32_16x16x32_bf16 v[196:199], a[220:223], v[220:223], v[196:199]\n"
      "s_add_u32 m0, s17, 0x7f00\n"
      "v_mfma_f32_16x16x32_bf16 v[200:203], a[224:227], v[220:223], v[200:203]\n"
      "ds_read_b128 v[216:219], v234 offset:8192\n"
      "global_load_lds_dwordx4 v236, s[52:53] offset:256 \n"
      "v_mfma_f32_16x16x32_bf16 v[204:207], a[228:231], v[220:223], v[204:207]\n"
      "v_mfma_f32_16x16x32_bf16 v[208:211], a[232:235], v[220:223], v[208:211]\n"
      "s_add_u32 m0, s17, 0x9e00\n"
      "v_mfma_f32_16x16x32_bf16 v[212:215], a[236:239], v[220:223], v[212:215]\n"
      "global_load_lds_dwordx4 v236, s[52:53] offset:512 \n"
      "s_waitcnt lgkmcnt(2)\n"
      "v_mfma_f32_16x16x32_bf16 v[0:3], a[0:3], v[224:227], v[0:3]\n"
      "ds_read_b128 v[244:247], v250\n"
      "v_mfma_f32_16x16x32_bf16 v[4:7], a[4:7], v[224:227], v[4:7]\n"
      "s_add_u32 m0, s17, 0x7000\n"
      "ds_read_b64 v[252:253], v250 offset:4096\n"
      "v_mfma_f32_16x16x32_bf16 v[8:11], a[8:11], v[224:227], v[8:11]\n"
      "ds_read_b128 v[220:223], v234 offset:12288\n"
      "global_load_lds_dwordx4 v237, s[52:53] offset:0 \n"
      "v_mfma_f32_16x16x32_bf16 v[12:15], a[12:15], v[224:227], v[12:15]\n"
      "v_mfma_f32_16x16x32_bf16 v[16:19], a[16:19], v[224:227], v[16:19]\n"
      "s_add_u32 m0, s17, 0x8f00\n"
      "v_mfma_f32_16x16x32_bf16 v[20:23], a[20:23], v[224:227], v[20:23]\n"
      "global_load_lds_dwordx4 v237, s[52:53] offset:256 \n"
      "s_waitcnt lgkmcnt(4)\n"
      "v_mfma_f32_16x16x32_bf16 v[24:27], a[0:3], v[228:231], v[24:27]\n"
      "v_mfma_f32_16x16x32_bf16 v[28:31], a[4:7], v[228:231], v[28:31]\n"
      "s_add_u32 m0, s17, 0xae00\n"
      "v_mfma_f32_16x16x32_bf16 v[32:35], a[8:11], v[228:231], v[32:35]\n"
      "ds_read_b128 v[224:227], v234 offset:16384\n"
      "global_load_lds_dwordx4 v237, s[52:53] offset:512 \n"
      "v_mfma_f32_16x16x32_bf16 v[36:39], a[12:15], v[228:231], v[36:39]\n"
      "v_mfma_f32_16x16x32_bf16 v[40:43], a[16:19], v[228:231], v[40:43]\n"
      "v_mfma_f32_16x16x32_bf16 v[44:47], a[20:23], v[228:231], v[44:47]\n"
      "global_load_dwordx4 a[216:219], v239, s[58:59] offset:2048 sc1\n"
      "s_waitcnt lgkmcnt(4)\n"
      "v_mfma_f32_16x16x32_bf16 v[48:51], a[0:3], v[216:219], v[48:51]\n"
      "v_mfma_f32_16x16x32_bf16 v[52:55], a[4:7], v[216:219], v[52:55]\n"
      "v_mfma_f32_16x16x32_bf16 v[56:59], a[8:11], v[216:219], v[56:59]\n"
      "ds_read_b128 v[228:231], v234 offset:20480\n"
      "v_mfma_f32_16x16x32_bf16 v[60:63], a[12:15], v[216:219], v[60:63]\n"
      "global_load_dwordx4 a[220:223], v239, s[58:59] offset:3072 sc1\n"
      "v_mfma_f32_16x16x32_bf16 v[64:67], a[16:19], v[216:219], v[64:67]\n"
      "v_mfma_f32_16x16x32_bf16 v[68:71], a[20:23], v[216:219], v[68:71]\n"
      "s_waitcnt lgkmcnt(2)\n"
      "v_mfma_f32_16x16x32_bf16 v[72:75], a[0:3], v[220:223], v[72:75]\n"
      "v_mfma_f32_16x16x32_bf16 v[76:79], a[4:7], v[220:223], v[76:79]\n"
      "s_add_u32 s58, s24, 0xc000\n"
      "s_addc_u32 s59, s25, 0\n"
      "global_load_dwordx4 a[224:227], v239, s[58:59] offset:0 sc1\n"
      "v_mfma_f32_16x16x32_bf16 v[80:83], a[8:11], v[220:223], v[80:83]\n"
      "ds_read_b128 v[216:219], v234 offset:24576\n"
      "v_mfma_f32_16x16x32_bf16 v[84:87], a[12:15], v[220:223], v[84:87]\n"
      "v_mfma_f32_16x16x32_bf16 v[88:91], a[16:19], v[220:223], v[88:91]\n"
      "v_mfma_f32_16x16x32_bf16 v[92:95], a[20:23], v[220:223], v[92:95]\n"
      "global_load_dwordx4 a[228:231], v239, s[58:59] offset:1024 sc1\n"
      "s_waitcnt lgkmcnt(2)\n"
      "v_mfma_f32_16x16x32_bf16 v[96:99], a[0:3], v[224:227], v[96:99]\n"
      "v_mfma_f32_16x16x32_bf16 v[100:103], a[4:7], v[224:227], v[100:103]\n"
      "v_mfma_f32_16x16x32_bf16 v[104:107], a[8:11], v[224:227], v[104:107]\n"
      "ds_read_b128 v[220:223], v234 offset:28672\n"
      "v_mfma_f32_16x16x32_bf16 v[108:111], a[12:15], v[224:227], v[108:111]\n"
      "global_load_dwordx4 a[232:235], v239, s[58:59] offset:2048 sc1\n"
      "v_mfma_f32_16x16x32_bf16 v[112:115], a[16:19], v[224:227], v[112:115]\n"
      "v_mfma_f32_16x16x32_bf16 v[116:119], a[20:23], v[224:227], v[116:119]\n"
      "s_waitcnt lgkmcnt(2)\n"
      "v_mfma_f32_16x16x32_bf16 v[120:123], a[0:3], v[228:231], v[120:123]\n"
      "v_mfma_f32_16x16x32_bf16 v[124:127], a[4:7], v[228:231], v[124:127]\n"
      "global_load_dwordx4 a[236:239], v239, s[58:59] offset:3072 sc1\n"
      "v_mfma_f32_16x16x32_bf16 v[128:131], a[8:11], v[228:231], v[128:131]\n"
      "ds_read_b128 v[224:227], v234 offset:32768\n"
      "v_add_u32 v234, v249, v234\n"
      "v_mfma_f32_16x16x32_bf16 v[132:135], a[12:15], v[228:231], v[132:135]\n"
      "v_mfma_f32_16x16x32_bf16 v[136:139], a[16:19], v[228:231], v[136:139]\n"
      "v_mfma_f32_16x16x32_bf16 v[140:143], a[20:23], v[228:231], v[140:143]\n"
      "s_waitcnt lgkmcnt(2)\n"
      "v_mfma_f32_16x16x32_bf16 v[144:147], a[0:3], v[216:219], v[144:147]\n"
      "v_mfma_f32_16x16x32_bf16 v[148:151], a[4:7], v[216:219], v[148:151]\n"
      "v_mfma_f32_16x16x32_bf16 v[152:155], a[8:11], v[216:219], v[152:155]\n"
      "ds_read_b128 v[228:231], v235 offset:4096\n"
      "v_mfma_f32_16x16x32_bf16 v[156:159], a[12:15], v[216:219], v[156:159]\n"
      "v_mfma_f32_16x16x32_bf16 v[160:163], a[16:19], v[216:219], v[160:163]\n"
      "v_mfma_f32_16x16x32_bf16 v[164:167], a[20:23], v[216:219], v[164:167]\n"
      "s_waitcnt lgkmcnt(2)\n"
      "v_mfma_f32_16x16x32_bf16 v[168:171], a[0:3], v[220:223], v[168:171]\n"
      "v_mfma_f32_16x16x32_bf16 v[172:175], a[4:7], v[220:223], v[172:175]\n"
      "v_mfma_f32_16x16x32_bf16 v[176:179], a[8:11], v[220:223], v[176:179]\n"
      "ds_read_b128 v[216:219], v235 offset:8192\n"
      "v_mfma_f32_16x16x32_bf16 v[180:183], a[12:15], v[220:223], v[180:183]\n"
      "v_mfma_f32_16x16x32_bf16 v[184:187], a[16:19], v[220:223], v[184:187]\n"
      "v_mfma_f32_16x16x32_bf16 v[188:191], a[20:23], v[220:223], v[188:191]\n"
      "s_waitcnt lgkmcnt(2)\n"
      "v_mfma_f32_16x16x32_bf16 v[192:195], a[0:3], v[224:227], v[192:195]\n"
      "v_mfma_f32_16x16x32_bf16 v[196:199], a[4:7], v[224:227], v[196:199]\n"
      "v_mfma_f32_16x16x32_bf16 v[200:203], a[8:11], v[224:227], v[200:203]\n"
      "ds_read_b128 v[220:223], v235 offset:12288\n"
      "v_mfma_f32_16x16x32_bf16 v[204:207], a[12:15], v[224:227], v[204:207]\n"
      "v_mfma_f32_16x16x32_bf16 v[208:211], a[16:19], v[224:227], v[208:211]\n"
      "v_mfma_f32_16x16x32_bf16 v[212:215], a[20:23], v[224:227], v[212:215]\n"
      "s_waitcnt lgkmcnt(2)\n"
      "v_mfma_f32_16x16x32_bf16 v[24:27], a[24:27], v[228:231], v[24:27]\n"
      "s_add_u32 s58, s24, 0xd000\n"
      "s_addc_u32 s59, s25, 0\n"
      "global_load_dwordx4 a[0:3], v239, s[58:59] offset:0 sc1\n"
      "v_mfma_f32_16x16x32_bf16 v[28:31], a[28:31], v[228:231], v[28:31]\n"
      "v_mfma_f32_16x16x32_bf16 v[32:35], a[32:35], v[228:231], v[32:35]\n"
      "ds_read_b128 v[224:227], v235 offset:16384\n"
      "v_mfma_f32_16x16x32_bf16 v[36:39], a[36:39], v[228:231], v[36:39]\n"
      "v_mfma_f32_16x16x32_bf16 v[40:43], a[40:43], v[228:231], v[40:43]\n"
      "global_load_dwordx4 a[4:7], v239, s[58:59] offset:1024 sc1\n"
      "v_mfma_f32_16x16x32_bf16 v[44:47], a[44:47], v[228:231], v[44:47]\n"
      "s_waitcnt lgkmcnt(2)\n"
      "v_mfma_f32_16x16x32_bf16 v[48:51], a[24:27], v[216:219], v[48:51]\n"
      "v_cvt_pk_bf16_f32 v24, v24, v25\n"
      "v_cvt_pk_bf16_f32 v25, v26, v27\n"
      "v_mfma_f32_16x16x32_bf16 v[52:55], a[28:31], v[216:219], v[52:55]\n"
      "v_cvt_pk_bf16_f32 v26, v28, v29\n"
      "v_cvt_pk_bf16_f32 v27, v30, v31\n"
      "v_mfma_f32_16x16x32_bf16 v[56:59], a[32:35], v[216:219], v[56:59]\n"
      "ds_read_b128 v[228:231], v235 offset:20480\n"
      "v_cvt_pk_bf16_f32 v28, v32, v33\n"
      "v_cvt_pk_bf16_f32 v29, v34, v35\n"
      "global_load_dwordx4 a[8:11], v239, s[58:59] offset:2048 sc1\n"
      "v_mfma_f32_16x16x32_bf16 v[60:63], a[36:39], v[216:219], v[60:63]\n"
      "v_cvt_pk_bf16_f32 v30, v36, v37\n"
      "v_cvt_pk_bf16_f32 v31, v38, v39\n"
      "v_mfma_f32_16x16x32_bf16 v[64:67], a[40:43], v[216:219], v[64:67]\n"
      "v_cvt_pk_bf16_f32 v32, v40, v41\n"
      "v_cvt_pk_bf16_f32 v33, v42, v43\n"
      "v_mfma_f32_16x16x32_bf16 v[68:71], a[44:47], v[216:219], v[68:71]\n"
      "v_cvt_pk_bf16_f32 v34, v44, v45\n"
      "v_cvt_pk_bf16_f32 v35, v46, v47\n"
      "s_waitcnt lgkmcnt(2)\n"
      "v_mfma_f32_16x16x32_bf16 v[72:75], a[24:27], v[220:223], v[72:75]\n"
      "v_cvt_pk_bf16_f32 v48, v48, v49\n"
      "v_cvt_pk_bf16_f32 v49, v50, v51\n"
      "global_load_dwordx4 a[12:15], v239, s[58:59] offset:3072 sc1\n"
      "v_mfma_f32_16x16x32_bf16 v[76:79], a[28:31], v[220:223], v[76:79]\n"
      "v_cvt_pk_bf16_f32 v50, v52, v53\n"
      "v_cvt_pk_bf16_f32 v51, v54, v55\n"
      "v_mfma_f32_16x16x32_bf16 v[80:83], a[32:35], v[220:223], v[80:83]\n"
      "ds_read_b128 v[216:219], v235 offset:24576\n"
      "v_cvt_pk_bf16_f32 v52, v56, v57\n"
      "v_cvt_pk_bf16_f32 v53, v58, v59\n"
      "v_mfma_f32_16x16x32_bf16 v[84:87], a[36:39], v[220:223], v[84:87]\n"
      "v_cvt_pk_bf16_f32 v54, v60, v61\n"
      "v_cvt_pk_bf16_f32 v55, v62, v63\n"
      "v_mfma_f32_16x16x32_bf16 v[88:91], a[40:43], v[220:223], v[88:91]\n"
      "v_cvt_pk_bf16_f32 v56, v64, v65\n"
      "v_cvt_pk_bf16_f32 v57, v66, v67\n"
      "s_add_u32 s58, s24, 0xe000\n"
      "s_addc_u32 s59, s25, 0\n"
      "global_load_dwordx4 a[16:19], v239, s[58:59] offset:0 sc1\n"
      "v_mfma_f32_16x16x32_bf16 v[92:95], a[44:47], v[220:223], v[92:95]\n"
      "LPD6:\n"
      "s_cbranch_execz LPD7\n"
      "v_cvt_pk_bf16_f32 v58, v68, v69\n"
      "v_cvt_pk_bf16_f32 v59, v70, v71\n"
      "s_waitcnt lgkmcnt(2)\n"
      "v_mfma_f32_16x16x32_bf16 v[96:99], a[24:27], v[224:227], v[96:99]\n"
      "v_cvt_pk_bf16_f32 v72, v72, v73\n"
      "v_cvt_pk_bf16_f32 v73, v74, v75\n"
      "v_mfma_f32_16x16x32_bf16 v[100:103], a[28:31], v[224:227], v[100:103]\n"
      "v_cvt_pk_bf16_f32 v74, v76, v77\n"
      "v_cvt_pk_bf16_f32 v75, v78, v79\n"
      "v_mfma_f32_16x16x32_bf16 v[104:107], a[32:35], v[224:227], v[104:107]\n"
      "ds_read_b128 v[220:223], v235 offset:28672\n"
      "v_cvt_pk_bf16_f32 v76, v80, v81\n"
      "v_cvt_pk_bf16_f32 v77, v82, v83\n"
      "global_load_dwordx4 a[20:23], v239, s[58:59] offset:1024 sc1\n"
      "v_mfma_f32_16x16x32_bf16 v[108:111], a[36:39], v[224:227], v[108:111]\n"
      "v_cvt_pk_bf16_f32 v78, v84, v85\n"
      "v_cvt_pk_bf16_f32 v79, v86, v87\n"
      "v_mfma_f32_16x16x32_bf16 v[112:115], a[40:43], v[224:227], v[112:115]\n"
      "v_cvt_pk_bf16_f32 v80, v88, v89\n"
      "v_cvt_pk_bf16_f32 v81, v90, v91\n"
      "v_mfma_f32_16x16x32_bf16 v[116:119], a[44:47], v[224:227], v[116:119]\n"
      "v_cvt_pk_bf16_f32 v82, v92, v93\n"
      "v_cvt_pk_bf16_f32 v83, v94, v95\n"
      "s_waitcnt lgkmcnt(2)\n"
      "v_mfma_f32_16x16x32_bf16 v[120:123], a[24:27], v[228:231], v[120:123]\n"
      "v_cvt_pk_bf16_f32 v96, v96, v97\n"
      "v_cvt_pk_bf16_f32 v97, v98, v99\n"
      "v_mfma_f32_16x16x32_bf16 v[124:127], a[28:31], v[228:231], v[124:127]\n"
      "v_cvt_pk_bf16_f32 v98, v100, v101\n"
      "v_cvt_pk_bf16_f32 v99, v102, v103\n"
      "v_mfma_f32_16x16x32_bf16 v[128:131], a[32:35], v[228:231], v[128:131]\n"
      "ds_read_b128 v[224:227], v235 offset:32768\n"
      "v_cvt_pk_bf16_f32 v100, v104, v105\n"
      "v_cvt_pk_bf16_f32 v101, v106, v107\n"
      "v_mfma_f32_16x16x32_bf16 v[132:135], a[36:39], v[228:231], v[132:135]\n"
      "v_cvt_pk_bf16_f32 v102, v108, v109\n"
      "v_cvt_pk_bf16_f32 v103, v110, v111\n"
      "v_mfma_f32_16x16x32_bf16 v[136:139], a[40:43], v[228:231], v[136:139]\n"
      "v_cvt_pk_bf16_f32 v104, v112, v113\n"
      "v_cvt_pk_bf16_f32 v105, v114, v115\n"
      "v_mfma_f32_16x16x32_bf16 v[140:143], a[44:47], v[228:231], v[140:143]\n"
      "v_cvt_pk_bf16_f32 v106, v116, v117\n"
      "v_cvt_pk_bf16_f32 v107, v118, v119\n"
      "s_waitcnt lgkmcnt(2)\n"
      "v_mfma_f32_16x16x32_bf16 v[144:147], a[24:27], v[216:219], v[144:147]\n"
      "v_cvt_pk_bf16_f32 v120, v120, v121\n"
      "v_cvt_pk_bf16_f32 v121, v122, v123\n"
      "v_mfma_f32_16x16x32_bf16 v[148:151], a[28:31], v[216:219], v[148:151]\n"
      "v_cvt_pk_bf16_f32 v122, v124, v125\n"
      "v_cvt_pk_bf16_f32 v123, v126, v127\n"
      "v_mfma_f32_16x16x32_bf16 v[152:155], a[32:35], v[216:219], v[152:155]\n"
      "ds_read_b128 v[228:231], v235 offset:0\n"
      "v_add_u32 v235, v249, v235\n"
      "s_waitcnt vmcnt(24)\n"
      "s_barrier\n"
      "v_cvt_pk_bf16_f32 v124, v128, v129\n"
      "v_cvt_pk_bf16_f32 v125, v130, v131\n"
      "v_mfma_f32_16x16x32_bf16 v[156:159], a[36:39], v[216:219], v[156:159]\n"
      "ds_read_b128 v[36:39], v232 offset:0\n"
      "v_cvt_pk_bf16_f32 v126, v132, v133\n"
      "v_cvt_pk_bf16_f32 v127, v134, v135\n"
      "v_mfma_f32_16x16x32_bf16 v[160:163], a[40:43], v[216:219], v[160:163]\n"
      "v_cvt_pk_bf16_f32 v128, v136, v137\n"
      "v_cvt_pk_bf16_f32 v129, v138, v139\n"
      "v_mfma_f32_16x16x32_bf16 v[164:167], a[44:47], v[216:219], v[164:167]\n"
      "ds_read_b128 v[40:43], v233 offset:0\n"
      "v_cvt_pk_bf16_f32 v130, v140, v141\n"
      "v_cvt_pk_bf16_f32 v131, v142, v143\n"
      "s_waitcnt lgkmcnt(4)\n"
      "v_mfma_f32_16x16x32_bf16 v[168:171], a[24:27], v[220:223], v[168:171]\n"
      "v_cvt_pk_bf16_f32 v144, v144, v145\n"
      "v_cvt_pk_bf16_f32 v145, v146, v147\n"
      "global_load_dwordx4 v[140:143], v239, s[58:59] offset:2048 sc1\n"
      "v_mfma_f32_16x16x32_bf16 v[172:175], a[28:31], v[220:223], v[172:175]\n"
      "ds_read_b128 v[44:47], v234 offset:0\n"
      "v_cvt_pk_bf16_f32 v146, v148, v149\n"
      "v_cvt_pk_bf16_f32 v147, v150, v151\n"
      "v_mfma_f32_16x16x32_bf16 v[176:179], a[32:35], v[220:223], v[176:179]\n"
      "v_cvt_pk_bf16_f32 v148, v152, v153\n"
      "v_cvt_pk_bf16_f32 v149, v154, v155\n"
      "v_mfma_f32_16x16x32_bf16 v[180:183], a[36:39], v[220:223], v[180:183]\n"
      "ds_read_b128 v[60:63], v235 offset:0\n"
      "v_cvt_pk_bf16_f32 v150, v156, v157\n"
      "v_cvt_pk_bf16_f32 v151, v158, v159\n"
      "v_mfma_f32_16x16x32_bf16 v[184:187], a[40:43], v[220:223], v[184:187]\n"
      "v_cvt_pk_bf16_f32 v152, v160, v161\n"
      "v_cvt_pk_bf16_f32 v153, v162, v163\n"
      "v_mfma_f32_16x16x32_bf16 v[188:191], a[44:47], v[220:223], v[188:191]\n"
      "ds_read_b128 v[64:67], v232 offset:8192\n"
      "v_cvt_pk_bf16_f32 v154, v164, v165\n"
      "v_cvt_pk_bf16_f32 v155, v166, v167\n"
      "s_waitcnt lgkmcnt(6)\n"
      "v_mfma_f32_16x16x32_bf16 v[192:195], a[24:27], v[224:227], v[192:195]\n"
      "v_cvt_pk_bf16_f32 v168, v168, v169\n"
      "v_cvt_pk_bf16_f32 v169, v170, v171\n"
      "global_load_dwordx4 v[156:159], v239, s[58:59] offset:3072 sc1\n"
      "v_mfma_f32_16x16x32_bf16 v[196:199], a[28:31], v[224:227], v[196:199]\n"
      "ds_read_b128 v[68:71], v233 offset:8192\n"
      "v_cvt_pk_bf16_f32 v170, v172, v173\n"
      "v_cvt_pk_bf16_f32 v171, v174, v175\n"
      "v_mfma_f32_16x16x32_bf16 v[200:203], a[32:35], v[224:227], v[200:203]\n"
      "v_cvt_pk_bf16_f32 v172, v176, v177\n"
      "v_cvt_pk_bf16_f32 v173, v178, v179\n"
      "v_mfma_f32_16x16x32_bf16 v[204:207], a[36:39], v[224:227], v[204:207]\n"
      "ds_read_b128 v[84:87], v234 offset:8192\n"
      "v_cvt_pk_bf16_f32 v174, v180, v181\n"
      "v_cvt_pk_bf16_f32 v175, v182, v183\n"
      "v_mfma_f32_16x16x32_bf16 v[208:211], a[40:43], v[224:227], v[208:211]\n"
      "v_cvt_pk_bf16_f32 v176, v184, v185\n"
      "v_cvt_pk_bf16_f32 v177, v186, v187\n"
      "s_add_u32 s58, s24, 0xf000\n"
      "s_addc_u32 s59, s25, 0\n"
      "global_load_dwordx4 v[160:163], v239, s[58:59] offset:0 sc1\n"
      "v_mfma_f32_16x16x32_bf16 v[212:215], a[44:47], v[224:227], v[212:215]\n"
      "ds_read_b128 v[88:91], v235 offset:8192\n"
      "v_cvt_pk_bf16_f32 v178, v188, v189\n"
      "v_cvt_pk_bf16_f32 v179, v190, v191\n"
      "s_waitcnt lgkmcnt(8)\n"
      "v_mfma_f32_16x16x32_bf16 v[0:3], a[24:27], v[228:231], v[0:3]\n"
      "v_cvt_pk_bf16_f32 v192, v192, v193\n"
      "v_cvt_pk_bf16_f32 v193, v194, v195\n"
      "v_mfma_f32_16x16x32_bf16 v[4:7], a[28:31], v[228:231], v[4:7]\n"
      "ds_read_b128 v[92:95], v232 offset:16384\n"
      "v_cvt_pk_bf16_f32 v194, v196, v197\n"
      "v_cvt_pk_bf16_f32 v195, v198, v199\n"
      "v_mfma_f32_16x16x32_bf16 v[8:11], a[32:35], v[228:231], v[8:11]\n"
      "v_cvt_pk_bf16_f32 v196, v200, v201\n"
      "v_cvt_pk_bf16_f32 v197, v202, v203\n"
      "global_load_dwordx4 v[164:167], v239, s[58:59] offset:1024 sc1\n"
      "v_mfma_f32_16x16x32_bf16 v[12:15], a[36:39], v[228:231], v[12:15]\n"
      "ds_read_b128 v[108:111], v233 offset:16384\n"
      "v_cvt_pk_bf16_f32 v198, v204, v205\n"
      "v_cvt_pk_bf16_f32 v199, v206, v207\n"
      "v_mfma_f32_16x16x32_bf16 v[16:19], a[40:43], v[228:231], v[16:19]\n"
      "ds_read_b128 v[112:115], v234 offset:16384\n"
      "v_cvt_pk_bf16_f32 v200, v208, v209\n"
      "v_cvt_pk_bf16_f32 v201, v210, v211\n"
      "v_mfma_f32_16x16x32_bf16 v[20:23], a[44:47], v[228:231], v[20:23]\n"
      "ds_read_b128 v[116:119], v235 offset:16384\n"
      "v_cvt_pk_bf16_f32 v202, v212, v213\n"
      "v_cvt_pk_bf16_f32 v203, v214, v215\n"
      "s_waitcnt lgkmcnt(8)\n"
      "v_mfma_f32_16x16x32_bf16 v[132:135], a[240:243], v[36:39], v[0:3]\n"
      "global_load_dwordx4 v[180:183], v239, s[58:59] offset:2048 sc1\n"
      "s_mov_b64 vcc, 0\n"
      "v_mfma_f32_16x16x32_bf16 v[132:135], a[244:247], v[40:43], v[132:135]\n"
      "v_mfma_f32_16x16x32_bf16 v[132:135], a[248:251], v[44:47], v[132:135]\n"
      "v_mfma_f32_16x16x32_bf16 v[132:135], a[252:255], v[60:63], v[132:135]\n"
      "global_load_dwordx4 v[184:187], v239, s[58:59] offset:3072 sc1\n"
      "s_waitcnt lgkmcnt(4)\n"
      "v_mfma_f32_16x16x32_bf16 v[132:135], a[48:51], v[64:67], v[132:135]\n"
      "v_mfma_f32_16x16x32_bf16 v[132:135], a[52:55], v[68:71], v[132:135]\n"
      "v_mfma_f32_16x16x32_bf16 v[132:135], a[56:59], v[84:87], v[132:135]\n"
      "s_add_u32 s58, s24, 0x10000\n"
      "s_addc_u32 s59, s25, 0\n"
      "global_load_dwordx4 v[188:191], v239, s[58:59] offset:0 sc1\n"
      "v_mfma_f32_16x16x32_bf16 v[132:135], a[60:63], v[88:91], v[132:135]\n"
      "s_waitcnt lgkmcnt(0)\n"
      "v_mfma_f32_16x16x32_bf16 v[132:135], a[64:67], v[92:95], v[132:135]\n"
      "v_mfma_f32_16x16x32_bf16 v[132:135], a[68:71], v[108:111], v[132:135]\n"
      "global_load_dwordx4 v[204:207], v239, s[58:59] offset:1024 sc1\n"
      "v_mfma_f32_16x16x32_bf16 v[132:135], a[72:75], v[112:115], v[132:135]\n"
      "v_mfma_f32_16x16x32_bf16 v[132:135], a[76:79], v[116:119], v[132:135]\n"
      "v_mfma_f32_16x16x32_bf16 v[136:139], a[80:83], v[36:39], v[4:7]\n"
      "global_load_dwordx4 a[24:27], v239, s[58:59] offset:2048 sc1\n"
      "v_mfma_f32_16x16x32_bf16 v[136:139], a[84:87], v[40:43], v[136:139]\n"
      "v_mfma_f32_16x16x32_bf16 v[136:139], a[88:91], v[44:47], v[136:139]\n"
      "v_mfma_f32_16x16x32_bf16 v[136:139], a[92:95], v[60:63], v[136:139]\n"
      "global_load_dwordx4 a[28:31], v239, s[58:59] offset:3072 sc1\n"
      "v_mfma_f32_16x16x32_bf16 v[136:139], a[96:99], v[64:67], v[136:139]\n"
      "v_cvt_pk_bf16_f32 v208, v132, v133\n"
      "v_mfma_f32_16x16x32_bf16 v[136:139], a[100:103], v[68:71], v[136:139]\n"
      "v_cvt_pk_bf16_f32 v209, v134, v135\n"
      "v_mfma_f32_16x16x32_bf16 v[136:139], a[104:107], v[84:87], v[136:139]\n"
      "s_add_u32 s58, s24, 0x11000\n"
      "s_addc_u32 s59, s25, 0\n"
      "global_load_dwordx4 a[32:35], v239, s[58:59] offset:0 sc1\n"
      "v_mfma_f32_16x16x32_bf16 v[136:139], a[108:111], v[88:91], v[136:139]\n"
      "v_mfma_f32_16x16x32_bf16 v[136:139], a[112:115], v[92:95], v[136:139]\n"
      "s_add_u32 s68, s83, 1160\n"
      "v_mfma_f32_16x16x32_bf16 v[136:139], a[116:119], v[108:111], v[136:139]\n"
      "global_load_dwordx4 a[36:39], v239, s[58:59] offset:1024 sc1\n"
      "v_mfma_f32_16x16x32_bf16 v[136:139], a[120:123], v[112:115], v[136:139]\n"
      "ds_write_b64 v244, v[208:209] offset:12288\n"
      "s_add_u32 s67, s68, 150\n"
      "v_mfma_f32_16x16x32_bf16 v[136:139], a[124:127], v[116:119], v[136:139]\n"
      "v_mfma_f32_16x16x32_bf16 v[132:135], a[128:131], v[36:39], v[8:11]\n"
      "global_load_dwordx4 a[40:43], v239, s[58:59] offset:2048 sc1\n"
      "s_mov_b64 s[70:71], 0\n"
      "v_mfma_f32_16x16x32_bf16 v[132:135], a[132:135], v[40:43], v[132:135]\n"
      "v_mfma_f32_16x16x32_bf16 v[132:135], a[136:139], v[44:47], v[132:135]\n"
      "v_mfma_f32_16x16x32_bf16 v[132:135], a[140:143], v[60:63], v[132:135]\n"
      "global_load_dwordx4 a[44:47], v239, s[58:59] offset:3072 sc1\n"
      "v_mfma_f32_16x16x32_bf16 v[132:135], a[144:147], v[64:67], v[132:135]\n"
      "v_cvt_pk_bf16_f32 v210, v136, v137\n"
      "v_mfma_f32_16x16x32_bf16 v[132:135], a[148:151], v[68:71], v[132:135]\n"
      "v_cvt_pk_bf16_f32 v211, v138, v139\n"
      "v_mfma_f32_16x16x32_bf16 v[132:135], a[152:155], v[84:87], v[132:135]\n"
      "s_mul_i32 s78, s90, s19\n"
      "v_mfma_f32_16x16x32_bf16 v[132:135], a[156:159], v[88:91], v[132:135]\n"
      "v_add_u32 v236, s78, v236\n"
      "v_mfma_f32_16x16x32_bf16 v[132:135], a[160:163], v[92:95], v[132:135]\n"
      "s_mul_i32 s78, s78, s99\n"
      "v_mfma_f32_16x16x32_bf16 v[132:135], a[164:167], v[108:111], v[132:135]\n"
      "v_add_u32 v237, s78, v237\n"
      "v_mfma_f32_16x16x32_bf16 v[132:135], a[168:171], v[112:115], v[132:135]\n"
      "s_add_u32 s52, s20, s33\n"
      "s_addc_u32 s53, s21, 0\n"
      "s_add_u32 m0, s17, 0xc000\n"
      "v_mfma_f32_16x16x32_bf16 v[132:135], a[172:175], v[116:119], v[132:135]\n"
      "global_load_lds_dwordx4 v236, s[52:53] offset:0 \n"
      "v_mfma_f32_16x16x32_bf16 v[136:139], a[176:179], v[36:39], v[12:15]\n"
      "ds_write_b64 v244, v[210:211] offset:12296\n"
      "v_mfma_f32_16x16x32_bf16 v[136:139], a[180:183], v[40:43], v[136:139]\n"
      "v_mfma_f32_16x16x32_bf16 v[136:139], a[184:187], v[44:47], v[136:139]\n"
      "v_mfma_f32_16x16x32_bf16 v[136:139], a[188:191], v[60:63], v[136:139]\n"
      "v_cvt_pk_bf16_f32 v212, v132, v133\n"
      "s_waitcnt vmcnt(35)\n"
      "v_mfma_f32_16x16x32_bf16 v[136:139], a[192:195], v[64:67], v[136:139]\n"
      "s_add_u32 m0, s17, 0xdf00\n"
      "v_mfma_f32_16x16x32_bf16 v[136:139], a[196:199], v[68:71], v[136:139]\n"
      "global_load_lds_dwordx4 v236, s[52:53] offset:256 \n"
      "v_mfma_f32_16x16x32_bf16 v[136:139], a[200:203], v[84:87], v[136:139]\n"
      "v_cvt_pk_bf16_f32 v213, v134, v135\n"
      "v_mfma_f32_16x16x32_bf16 v[136:139], a[204:207], v[88:91], v[136:139]\n"
      "s_waitcnt vmcnt(26)\n"
      "v_mfma_f32_16x16x32_bf16 v[136:139], a[208:211], v[92:95], v[136:139]\n"
      "v_mfma_f32_16x16x32_bf16 v[136:139], a[212:215], v[108:111], v[136:139]\n"
      "v_mfma_f32_16x16x32_bf16 v[136:139], a[216:219], v[112:115], v[136:139]\n"
      "s_add_u32 m0, s17, 0xfe00\n"
      "v_mfma_f32_16x16x32_bf16 v[136:139], a[220:223], v[116:119], v[136:139]\n"
      "global_load_lds_dwordx4 v236, s[52:53] offset:512 \n"
      "s_waitcnt vmcnt(23)\n"
      "v_mfma_f32_16x16x32_bf16 v[132:135], a[224:227], v[36:39], v[16:19]\n"
      "ds_write_b64 v245, v[212:213] offset:12288\n"
      "v_mfma_f32_16x16x32_bf16 v[132:135], a[228:231], v[40:43], v[132:135]\n"
      "v_mfma_f32_16x16x32_bf16 v[132:135], a[232:235], v[44:47], v[132:135]\n"
      "v_mfma_f32_16x16x32_bf16 v[132:135], a[236:239], v[60:63], v[132:135]\n"
      "v_cvt_pk_bf16_f32 v214, v136, v137\n"
      "s_waitcnt vmcnt(19)\n"
      "v_mfma_f32_16x16x32_bf16 v[132:135], a[0:3], v[64:67], v[132:135]\n"
      "s_add_u32 m0, s17, 0xd000\n"
      "v_mfma_f32_16x16x32_bf16 v[132:135], a[4:7], v[68:71], v[132:135]\n"
      "global_load_lds_dwordx4 v237, s[52:53] offset:0 \n"
      "v_mfma_f32_16x16x32_bf16 v[132:135], a[8:11], v[84:87], v[132:135]\n"
      "v_cvt_pk_bf16_f32 v215, v138, v139\n"
      "v_mfma_f32_16x16x32_bf16 v[132:135], a[12:15], v[88:91], v[132:135]\n"
      "s_waitcnt vmcnt(16)\n"
      "v_mfma_f32_16x16x32_bf16 v[132:135], a[16:19], v[92:95], v[132:135]\n"
      "v_mfma_f32_16x16x32_bf16 v[132:135], a[20:23], v[108:111], v[132:135]\n"
      "v_mfma_f32_16x16x32_bf16 v[132:135], v[140:143], v[112:115], v[132:135]\n"
      "s_add_u32 m0, s17, 0xef00\n"
      "v_mfma_f32_16x16x32_bf16 v[132:135], v[156:159], v[116:119], v[132:135]\n"
      "s_barrier\n"
      "global_load_lds_dwordx4 v237, s[52:53] offset:256 \n"
      "s_waitcnt vmcnt(13)\n"
      "v_mfma_f32_16x16x32_bf16 v[136:139], v[160:163], v[36:39], v[20:23]\n"
      "ds_read_b128 v[36:39], v232 offset:256\n"
      "ds_write_b64 v245, v[214:215] offset:12296\n"
      "v_mfma_f32_16x16x32_bf16 v[136:139], v[164:167], v[40:43], v[136:139]\n"
      "ds_read_b128 v[40:43], v233 offset:256\n"
      "v_mfma_f32_16x16x32_bf16 v[136:139], v[180:183], v[44:47], v[136:139]\n"
      "ds_read_b128 v[44:47], v234 offset:256\n"
      "v_mfma_f32_16x16x32_bf16 v[136:139], v[184:187], v[60:63], v[136:139]\n"
      "ds_read_b128 v[60:63], v235 offset:256\n"
      "v_cvt_pk_bf16_f32 v208, v132, v133\n"
      "s_waitcnt vmcnt(9)\n"
      "v_mfma_f32_16x16x32_bf16 v[136:139], v[188:191], v[64:67], v[136:139]\n"
      "ds_read_b128 v[64:67], v232 offset:8448\n"
      "s_add_u32 m0, s17, 0x10e00\n"
      "v_mfma_f32_16x16x32_bf16 v[136:139], v[204:207], v[68:71], v[136:139]\n"
      "ds_read_b128 v[68:71], v233 offset:8448\n"
      "global_load_lds_dwordx4 v237, s[52:53] offset:512 \n"
      "v_mfma_f32_16x16x32_bf16 v[136:139], a[24:27], v[84:87], v[136:139]\n"
      "ds_read_b128 v[84:87], v234 offset:8448\n"
      "v_cvt_pk_bf16_f32 v209, v134, v135\n"
      "v_mfma_f32_16x16x32_bf16 v[136:139], a[28:31], v[88:91], v[136:139]\n"
      "ds_read_b128 v[88:91], v235 offset:8448\n"
      "s_waitcnt vmcnt(6)\n"
      "v_mfma_f32_16x16x32_bf16 v[136:139], a[32:35], v[92:95], v[136:139]\n"
      "ds_read_b128 v[92:95], v232 offset:16640\n"
      "v_mfma_f32_16x16x32_bf16 v[136:139], a[36:39], v[108:111], v[136:139]\n"
      "ds_read_b128 v[108:111], v233 offset:16640\n"
      "v_mfma_f32_16x16x32_bf16 v[136:139], a[40:43], v[112:115], v[136:139]\n"
      "ds_read_b128 v[112:115], v234 offset:16640\n"
      "v_mfma_f32_16x16x32_bf16 v[136:139], a[44:47], v[116:119], v[136:139]\n"
      "ds_read_b128 v[116:119], v235 offset:16640\n"
      "s_waitcnt lgkmcnt(8)\n"
      "v_mfma_f32_16x16x32_bf16 v[132:135], a[240:243], v[36:39], v[0:3]\n"
      "s_add_u32 s52, s42, s33\n"
      "s_addc_u32 s53, s43, 0\n"
      "s_add_u32 m0, s17, 0x12000\n"
      "v_mfma_f32_16x16x32_bf16 v[132:135], a[244:247], v[40:43], v[132:135]\n"
      "global_load_lds_dwordx4 v236, s[52:53] offset:0 \n"
      "v_mfma_f32_16x16x32_bf16 v[132:135], a[248:251], v[44:47], v[132:135]\n"
      "ds_write_b64 v244, v[208:209] offset:12416\n"
      "v_mfma_f32_16x16x32_bf16 v[132:135], a[252:255], v[60:63], v[132:135]\n"
      "v_cvt_pk_bf16_f32 v210, v136, v137\n"
      "s_waitcnt lgkmcnt(5)\n"
      "v_mfma_f32_16x16x32_bf16 v[132:135], a[48:51], v[64:67], v[132:135]\n"
      "v_cvt_pk_bf16_f32 v211, v138, v139\n"
      "v_mfma_f32_16x16x32_bf16 v[132:135], a[52:55], v[68:71], v[132:135]\n"
      "v_mfma_f32_16x16x32_bf16 v[132:135], a[56:59], v[84:87], v[132:135]\n"
      "s_add_u32 m0, s17, 0x13f00\n"
      "v_mfma_f32_16x16x32_bf16 v[132:135], a[60:63], v[88:91], v[132:135]\n"
      "global_load_lds_dwordx4 v236, s[52:53] offset:256 \n"
      "s_waitcnt lgkmcnt(1)\n"
      "v_mfma_f32_16x16x32_bf16 v[132:135], a[64:67], v[92:95], v[132:135]\n"
      "v_mfma_f32_16x16x32_bf16 v[132:135], a[68:71], v[108:111], v[132:135]\n"
      "ds_write_b64 v244, v[210:211] offset:12424\n"
      "v_mfma_f32_16x16x32_bf16 v[132:135], a[72:75], v[112:115], v[132:135]\n"
      "v_mfma_f32_16x16x32_bf16 v[132:135], a[76:79], v[116:119], v[132:135]\n"
      "v_mfma_f32_16x16x32_bf16 v[136:139], a[80:83], v[36:39], v[4:7]\n"
      "s_add_u32 m0, s17, 0x15e00\n"
      "v_mfma_f32_16x16x32_bf16 v[136:139], a[84:87], v[40:43], v[136:139]\n"
      "global_load_lds_dwordx4 v236, s[52:53] offset:512 \n"
      "v_mfma_f32_16x16x32_bf16 v[136:139], a[88:91], v[44:47], v[136:139]\n"
      "v_mfma_f32_16x16x32_bf16 v[136:139], a[92:95], v[60:63], v[136:139]\n"
      "v_cvt_pk_bf16_f32 v208, v132, v133\n"
      "v_mfma_f32_16x16x32_bf16 v[136:139], a[96:99], v[64:67], v[136:139]\n"
      "v_cvt_pk_bf16_f32 v209, v134, v135\n"
      "v_mfma_f32_16x16x32_bf16 v[136:139], a[100:103], v[68:71], v[136:139]\n"
      "v_mfma_f32_16x16x32_bf16 v[136:139], a[104:107], v[84:87], v[136:139]\n"
      "s_add_u32 m0, s17, 0x13000\n"
      "v_mfma_f32_16x16x32_bf16 v[136:139], a[108:111], v[88:91], v[136:139]\n"
      "global_load_lds_dwordx4 v237, s[52:53] offset:0 \n"
      "v_mfma_f32_16x16x32_bf16 v[136:139], a[112:115], v[92:95], v[136:139]\n"
      "v_mfma_f32_16x16x32_bf16 v[136:139], a[116:119], v[108:111], v[136:139]\n"
      "ds_write_b64 v244, v[208:209] offset:24576\n"
      "v_mfma_f32_16x16x32_bf16 v[136:139], a[120:123], v[112:115], v[136:139]\n"
      "v_mfma_f32_16x16x32_bf16 v[136:139], a[124:127], v[116:119], v[136:139]\n"
      "v_mfma_f32_16x16x32_bf16 v[132:135], a[128:131], v[36:39], v[8:11]\n"
      "s_add_u32 m0, s17, 0x14f00\n"
      "v_mfma_f32_16x16x32_bf16 v[132:135], a[132:135], v[40:43], v[132:135]\n"
      "global_load_lds_dwordx4 v237, s[52:53] offset:256 \n"
      "v_mfma_f32_16x16x32_bf16 v[132:135], a[136:139], v[44:47], v[132:135]\n"
      "v_mfma_f32_16x16x32_bf16 v[132:135], a[140:143], v[60:63], v[132:135]\n"
      "v_cvt_pk_bf16_f32 v210, v136, v137\n"
      "v_mfma_f32_16x16x32_bf16 v[132:135], a[144:147], v[64:67], v[132:135]\n"
      "v_cvt_pk_bf16_f32 v211, v138, v139\n"
      "v_mfma_f32_16x16x32_bf16 v[132:135], a[148:151], v[68:71], v[132:135]\n"
      "v_mfma_f32_16x16x32_bf16 v[132:135], a[152:155], v[84:87], v[132:135]\n"
      "s_add_u32 m0, s17, 0x16e00\n"
      "v_mfma_f32_16x16x32_bf16 v[132:135], a[156:159], v[88:91], v[132:135]\n"
      "global_load_lds_dwordx4 v237, s[52:53] offset:512 \n"
      "v_mfma_f32_16x16x32_bf16 v[132:135], a[160:163], v[92:95], v[132:135]\n"
      "v_mfma_f32_16x16x32_bf16 v[132:135], a[164:167], v[108:111], v[132:135]\n"
      "ds_write_b64 v244, v[210:211] offset:24584\n"
      "v_mfma_f32_16x16x32_bf16 v[132:135], a[168:171], v[112:115], v[132:135]\n"
      "v_mfma_f32_16x16x32_bf16 v[132:135], a[172:175], v[116:119], v[132:135]\n"
      "v_mfma_f32_16x16x32_bf16 v[136:139], a[176:179], v[36:39], v[12:15]\n"
      "v_mfma_f32_16x16x32_bf16 v[136:139], a[180:183], v[40:43], v[136:139]\n"
      "v_mfma_f32_16x16x32_bf16 v[136:139], a[184:187], v[44:47], v[136:139]\n"
      "v_mfma_f32_16x16x32_bf16 v[136:139], a[188:191], v[60:63], v[136:139]\n"
      "v_cvt_pk_bf16_f32 v212, v132, v133\n"
      "v_mfma_f32_16x16x32_bf16 v[136:139], a[192:195], v[64:67], v[136:139]\n"
      "v_cvt_pk_bf16_f32 v213, v134, v135\n"
      "v_mfma_f32_16x16x32_bf16 v[136:139], a[196:199], v[68:71], v[136:139]\n"
      "v_mfma_f32_16x16x32_bf16 v[136:139], a[200:203], v[84:87], v[136:139]\n"
      "v_mfma_f32_16x16x32_bf16 v[136:139], a[204:207], v[88:91], v[136:139]\n"
      "v_mfma_f32_16x16x32_bf16 v[136:139], a[208:211], v[92:95], v[136:139]\n"
      "v_mfma_f32_16x16x32_bf16 v[136:139], a[212:215], v[108:111], v[136:139]\n"
      "ds_write_b64 v245, v[212:213] offset:24576\n"
      "v_mfma_f32_16x16x32_bf16 v[136:139], a[216:219], v[112:115], v[136:139]\n"
      "v_mfma_f32_16x16x32_bf16 v[136:139], a[220:223], v[116:119], v[136:139]\n"
      "v_mfma_f32_16x16x32_bf16 v[132:135], a[224:227], v[36:39], v[16:19]\n"
      "v_mfma_f32_16x16x32_bf16 v[132:135], a[228:231], v[40:43], v[132:135]\n"
      "v_mfma_f32_16x16x32_bf16 v[132:135], a[232:235], v[44:47], v[132:135]\n"
      "v_mfma_f32_16x16x32_bf16 v[132:135], a[236:239], v[60:63], v[132:135]\n"
      "s_memrealtime s[84:85]\n"
      "v_mfma_f32_16x16x32_bf16 v[132:135], a[0:3], v[64:67], v[132:135]\n"
      "v_cvt_pk_bf16_f32 v214, v136, v137\n"
      "v_mfma_f32_16x16x32_bf16 v[132:135], a[4:7], v[68:71], v[132:135]\n"
      "v_cvt_pk_bf16_f32 v215, v138, v139\n"
      "v_mfma_f32_16x16x32_bf16 v[132:135], a[8:11], v[84:87], v[132:135]\n"
      "v_mfma_f32_16x16x32_bf16 v[132:135], a[12:15], v[88:91], v[132:135]\n"
      "v_mfma_f32_16x16x32_bf16 v[132:135], a[16:19], v[92:95], v[132:135]\n"
      "v_mfma_f32_16x16x32_bf16 v[132:135], a[20:23], v[108:111], v[132:135]\n"
      "v_mfma_f32_16x16x32_bf16 v[132:135], v[140:143], v[112:115], v[132:135]\n"
      "s_waitcnt lgkmcnt(0)\n"
      "s_mov_b32 s66, s84\n"
      "v_mfma_f32_16x16x32_bf16 v[132:135], v[156:159], v[116:119], v[132:135]\n"
      "s_barrier\n"
      "ds_write_b64 v245, v[214:215] offset:24584\n"
      "v_mfma_f32_16x16x32_bf16 v[136:139], v[160:163], v[36:39], v[20:23]\n"
      "ds_read_b128 v[36:39], v232 offset:24576\n"
      "s_lshl_b32 s79, s98, 1\n"
      "v_mfma_f32_16x16x32_bf16 v[136:139], v[164:167], v[40:43], v[136:139]\n"
      "ds_read_b128 v[40:43], v233 offset:24576\n"
      "v_mad_i32_i24 v252, v251, s79, v252\n"
      "v_mfma_f32_16x16x32_bf16 v[136:139], v[180:183], v[44:47], v[136:139]\n"
      "ds_read_b128 v[44:47], v234 offset:24576\n"
      "s_mul_i32 s79, s79, s100\n"
      "v_mfma_f32_16x16x32_bf16 v[136:139], v[184:187], v[60:63], v[136:139]\n"
      "ds_read_b128 v[60:63], v235 offset:24576\n"
      "v_cvt_pk_bf16_f32 v208, v132, v133\n"
      "v_mfma_f32_16x16x32_bf16 v[136:139], v[188:191], v[64:67], v[136:139]\n"
      "ds_read_b128 v[64:67], v232 offset:32768\n"
      "v_cvt_pk_bf16_f32 v209, v134, v135\n"
      "v_mfma_f32_16x16x32_bf16 v[136:139], v[204:207], v[68:71], v[136:139]\n"
      "ds_read_b128 v[68:71], v233 offset:32768\n"
      "v_add_u32 v247, s79, v247\n"
      "v_mfma_f32_16x16x32_bf16 v[136:139], a[24:27], v[84:87], v[136:139]\n"
      "ds_read_b128 v[84:87], v234 offset:32768\n"
      "s_mul_i32 s79, s79, s101\n"
      "v_mfma_f32_16x16x32_bf16 v[136:139], a[28:31], v[88:91], v[136:139]\n"
      "ds_read_b128 v[88:91], v235 offset:32768\n"
      "v_add_u32 v253, s79, v253\n"
      "v_mfma_f32_16x16x32_bf16 v[136:139], a[32:35], v[92:95], v[136:139]\n"
      "ds_read_b128 v[92:95], v232 offset:40960\n"
      "v_mfma_f32_16x16x32_bf16 v[136:139], a[36:39], v[108:111], v[136:139]\n"
      "ds_read_b128 v[108:111], v233 offset:40960\n"
      "ds_write_b64 v244, v[208:209] offset:24704\n"
      "v_mfma_f32_16x16x32_bf16 v[136:139], a[40:43], v[112:115], v[136:139]\n"
      "ds_read_b128 v[112:115], v234 offset:40960\n"
      "v_mfma_f32_16x16x32_bf16 v[136:139], a[44:47], v[116:119], v[136:139]\n"
      "ds_read_b128 v[116:119], v235 offset:40960\n"
      "LJOIN:\n"
      "LPD7:\n"
      "s_cbranch_execz LPD10\n"
      "s_waitcnt lgkmcnt(9)\n"
      "v_mfma_f32_16x16x32_bf16 v[132:135], a[240:243], v[36:39], v[0:3]\n"
      "s_sub_u32 s69, s66, s68\n"
      "v_mfma_f32_16x16x32_bf16 v[132:135], a[244:247], v[40:43], v[132:135]\n"
      "s_mov_b32 s68, s67\n"
      "v_mfma_f32_16x16x32_bf16 v[132:135], a[248:251], v[44:47], v[132:135]\n"
      "s_mov_b32 s67, s66\n"
      "v_mfma_f32_16x16x32_bf16 v[132:135], a[252:255], v[60:63], v[132:135]\n"
      "s_cmp_gt_i32 s69, 460\n"
      "s_cselect_b64 s[70:71], -1, s[70:71]\n"
      "s_waitcnt lgkmcnt(5)\n"
      "v_mfma_f32_16x16x32_bf16 v[132:135], a[48:51], v[64:67], v[132:135]\n"
      "s_or_b64 vcc, vcc, s[70:71]\n"
      "s_mul_i32 s78, s91, s19\n"
      "v_mfma_f32_16x16x32_bf16 v[132:135], a[52:55], v[68:71], v[132:135]\n"
      "v_cvt_pk_bf16_f32 v210, v136, v137\n"
      "v_add_u32 v236, s78, v236\n"
      "v_mfma_f32_16x16x32_bf16 v[132:135], a[56:59], v[84:87], v[132:135]\n"
      "v_cvt_pk_bf16_f32 v211, v138, v139\n"
      "s_mul_i32 s78, s78, s99\n"
      "v_mfma_f32_16x16x32_bf16 v[132:135], a[60:63], v[88:91], v[132:135]\n"
      "s_add_u32 s54, s30, s32\n"
      "s_addc_u32 s55, s31, 0\n"
      "s_waitcnt lgkmcnt(0)\n"
      "v_mfma_f32_16x16x32_bf16 v[132:135], a[64:67], v[92:95], v[132:135]\n"
      "ds_read_b128 v[216:219], v246 offset:12288\n"
      "v_add_u32 v237, s78, v237\n"
      "v_mfma_f32_16x16x32_bf16 v[132:135], a[68:71], v[108:111], v[132:135]\n"
      "ds_read_b128 v[220:223], v246 offset:13312\n"
      "s_cbranch_vccnz LT0\n"
      "LS0:\n"
      "v_mfma_f32_16x16x32_bf16 v[132:135], a[72:75], v[112:115], v[132:135]\n"
      "ds_read_b128 v[224:227], v246 offset:14336\n"
      "v_mfma_f32_16x16x32_bf16 v[132:135], a[76:79], v[116:119], v[132:135]\n"
      "ds_write_b64 v244, v[210:211] offset:24712\n"
      "v_mfma_f32_16x16x32_bf16 v[136:139], a[80:83], v[36:39], v[4:7]\n"
      "v_mfma_f32_16x16x32_bf16 v[136:139], a[84:87], v[40:43], v[136:139]\n"
      "v_mfma_f32_16x16x32_bf16 v[136:139], a[88:91], v[44:47], v[136:139]\n"
      "v_mfma_f32_16x16x32_bf16 v[136:139], a[92:95], v[60:63], v[136:139]\n"
      "v_cvt_pk_bf16_f32 v208, v132, v133\n"
      "v_mfma_f32_16x16x32_bf16 v[136:139], a[96:99], v[64:67], v[136:139]\n"
      "v_cvt_pk_bf16_f32 v209, v134, v135\n"
      "v_mfma_f32_16x16x32_bf16 v[136:139], a[100:103], v[68:71], v[136:139]\n"
      "s_waitcnt lgkmcnt(3)\n"
      "v_mfma_f32_16x16x32_bf16 v[136:139], a[104:107], v[84:87], v[136:139]\n"
      "global_store_dwordx4 v247, v[216:219], s[54:55] offset:0 sc1\n"
      "v_mfma_f32_16x16x32_bf16 v[136:139], a[108:111], v[88:91], v[136:139]\n"
      "s_add_u32 s52, s20, s34\n"
      "s_addc_u32 s53, s21, 0\n"
      "v_mfma_f32_16x16x32_bf16 v[136:139], a[112:115], v[92:95], v[136:139]\n"
      "s_add_u32 m0, s17, 0x0\n"
      "v_mfma_f32_16x16x32_bf16 v[136:139], a[116:119], v[108:111], v[136:139]\n"
      "ds_write_b64 v244, v[208:209] offset:0\n"
      "v_mfma_f32_16x16x32_bf16 v[136:139], a[120:123], v[112:115], v[136:139]\n"
      "global_load_lds_dwordx4 v236, s[52:53] offset:0 nt\n"
      "v_mfma_f32_16x16x32_bf16 v[136:139], a[124:127], v[116:119], v[136:139]\n"
      "v_mfma_f32_16x16x32_bf16 v[132:135], a[128:131], v[36:39], v[8:11]\n"
      "v_mfma_f32_16x16x32_bf16 v[132:135], a[132:135], v[40:43], v[132:135]\n"
      "v_mfma_f32_16x16x32_bf16 v[132:135], a[136:139], v[44:47], v[132:135]\n"
      "v_mfma_f32_16x16x32_bf16 v[132:135], a[140:143], v[60:63], v[132:135]\n"
      "v_cvt_pk_bf16_f32 v210, v136, v137\n"
      "v_mfma_f32_16x16x32_bf16 v[132:135], a[144:147], v[64:67], v[132:135]\n"
      "v_cvt_pk_bf16_f32 v211, v138, v139\n"
      "v_mfma_f32_16x16x32_bf16 v[132:135], a[148:151], v[68:71], v[132:135]\n"
      "v_mfma_f32_16x16x32_bf16 v[132:135], a[152:155], v[84:87], v[132:135]\n"
      "s_waitcnt lgkmcnt(3)\n"
      "v_mfma_f32_16x16x32_bf16 v[132:135], a[156:159], v[88:91], v[132:135]\n"
      "global_store_dwordx4 v252, v[220:223], s[54:55] offset:0 sc1\n"
      "v_mfma_f32_16x16x32_bf16 v[132:135], a[160:163], v[92:95], v[132:135]\n"
      "s_add_u32 m0, s17, 0x1f00\n"
      "v_mfma_f32_16x16x32_bf16 v[132:135], a[164:167], v[108:111], v[132:135]\n"
      "ds_write_b64 v244, v[210:211] offset:8\n"
      "v_mfma_f32_16x16x32_bf16 v[132:135], a[168:171], v[112:115], v[132:135]\n"
      "global_load_lds_dwordx4 v236, s[52:53] offset:256 nt\n"
      "v_mfma_f32_16x16x32_bf16 v[132:135], a[172:175], v[116:119], v[132:135]\n"
      "v_mfma_f32_16x16x32_bf16 v[136:139], a[176:179], v[36:39], v[12:15]\n"
      "v_mfma_f32_16x16x32_bf16 v[136:139], a[180:183], v[40:43], v[136:139]\n"
      "v_mfma_f32_16x16x32_bf16 v[136:139], a[184:187], v[44:47], v[136:139]\n"
      "v_mfma_f32_16x16x32_bf16 v[136:139], a[188:191], v[60:63], v[136:139]\n"
      "v_cvt_pk_bf16_f32 v212, v132, v133\n"
      "v_mfma_f32_16x16x32_bf16 v[136:139], a[192:195], v[64:67], v[136:139]\n"
      "v_cvt_pk_bf16_f32 v213, v134, v135\n"
      "v_mfma_f32_16x16x32_bf16 v[136:139], a[196:199], v[68:71], v[136:139]\n"
      "v_mfma_f32_16x16x32_bf16 v[136:139], a[200:203], v[84:87], v[136:139]\n"
      "v_mfma_f32_16x16x32_bf16 v[136:139], a[204:207], v[88:91], v[136:139]\n"
      "s_waitcnt lgkmcnt(3)\n"
      "v_mfma_f32_16x16x32_bf16 v[136:139], a[208:211], v[92:95], v[136:139]\n"
      "global_store_dwordx4 v253, v[224:227], s[54:55] offset:0 sc1\n"
      "s_cbranch_vccnz LT1\n"
      "LS1:\n"
      "v_mfma_f32_16x16x32_bf16 v[136:139], a[212:215], v[108:111], v[136:139]\n"
      "ds_write_b64 v245, v[212:213] offset:0\n"
      "v_mfma_f32_16x16x32_bf16 v[136:139], a[216:219], v[112:115], v[136:139]\n"
      "s_add_u32 m0, s17, 0x3e00\n"
      "v_mfma_f32_16x16x32_bf16 v[136:139], a[220:223], v[116:119], v[136:139]\n"
      "global_load_lds_dwordx4 v236, s[52:53] offset:512 nt\n"
      "v_mfma_f32_16x16x32_bf16 v[132:135], a[224:227], v[36:39], v[16:19]\n"
      "v_mfma_f32_16x16x32_bf16 v[132:135], a[228:231], v[40:43], v[132:135]\n"
      "v_mfma_f32_16x16x32_bf16 v[132:135], a[232:235], v[44:47], v[132:135]\n"
      "v_mfma_f32_16x16x32_bf16 v[132:135], a[236:239], v[60:63], v[132:135]\n"
      "v_cvt_pk_bf16_f32 v214, v136, v137\n"
      "v_mfma_f32_16x16x32_bf16 v[132:135], a[0:3], v[64:67], v[132:135]\n"
      "v_cvt_pk_bf16_f32 v215, v138, v139\n"
      "v_mfma_f32_16x16x32_bf16 v[132:135], a[4:7], v[68:71], v[132:135]\n"
      "v_mfma_f32_16x16x32_bf16 v[132:135], a[8:11], v[84:87], v[132:135]\n"
      "v_mfma_f32_16x16x32_bf16 v[132:135], a[12:15], v[88:91], v[132:135]\n"
      "v_mfma_f32_16x16x32_bf16 v[132:135], a[16:19], v[92:95], v[132:135]\n"
      "v_mfma_f32_16x16x32_bf16 v[132:135], a[20:23], v[108:111], v[132:135]\n"
      "ds_write_b64 v245, v[214:215] offset:8\n"
      "v_mfma_f32_16x16x32_bf16 v[132:135], v[140:143], v[112:115], v[132:135]\n"
      "v_mfma_f32_16x16x32_bf16 v[132:135], v[156:159], v[116:119], v[132:135]\n"
      "s_waitcnt lgkmcnt(4)\n"
      "s_barrier\n"
      "v_mfma_f32_16x16x32_bf16 v[136:139], v[160:163], v[36:39], v[20:23]\n"
      "ds_read_b128 v[36:39], v232 offset:24832\n"
      "v_mfma_f32_16x16x32_bf16 v[136:139], v[164:167], v[40:43], v[136:139]\n"
      "ds_read_b128 v[40:43], v233 offset:24832\n"
      "v_mfma_f32_16x16x32_bf16 v[136:139], v[180:183], v[44:47], v[136:139]\n"
      "ds_read_b128 v[44:47], v234 offset:24832\n"
      "v_mfma_f32_16x16x32_bf16 v[136:139], v[184:187], v[60:63], v[136:139]\n"
      "ds_read_b128 v[60:63], v235 offset:24832\n"
      "v_cvt_pk_bf16_f32 v208, v132, v133\n"
      "v_mfma_f32_16x16x32_bf16 v[136:139], v[188:191], v[64:67], v[136:139]\n"
      "ds_read_b128 v[64:67], v232 offset:33024\n"
      "v_cvt_pk_bf16_f32 v209, v134, v135\n"
      "v_mfma_f32_16x16x32_bf16 v[136:139], v[204:207], v[68:71], v[136:139]\n"
      "ds_read_b128 v[68:71], v233 offset:33024\n"
      "v_mfma_f32_16x16x32_bf16 v[136:139], a[24:27], v[84:87], v[136:139]\n"
      "ds_read_b128 v[84:87], v234 offset:33024\n"
      "v_mfma_f32_16x16x32_bf16 v[136:139], a[28:31], v[88:91], v[136:139]\n"
      "ds_read_b128 v[88:91], v235 offset:33024\n"
      "LPD8:\n"
      "s_cbranch_execz LPD11\n"
      "v_mfma_f32_16x16x32_bf16 v[136:139], a[32:35], v[92:95], v[136:139]\n"
      "ds_read_b128 v[92:95], v232 offset:41216\n"
      "v_mfma_f32_16x16x32_bf16 v[136:139], a[36:39], v[108:111], v[136:139]\n"
      "ds_read_b128 v[108:111], v233 offset:41216\n"
      "ds_write_b64 v244, v[208:209] offset:128\n"
      "v_mfma_f32_16x16x32_bf16 v[136:139], a[40:43], v[112:115], v[136:139]\n"
      "ds_read_b128 v[112:115], v234 offset:41216\n"
      "v_mfma_f32_16x16x32_bf16 v[136:139], a[44:47], v[116:119], v[136:139]\n"
      "ds_read_b128 v[116:119], v235 offset:41216\n"
      "s_waitcnt lgkmcnt(9)\n"
      "v_mfma_f32_16x16x32_bf16 v[132:135], a[240:243], v[36:39], v[0:3]\n"
      "v_add_u32 v232, 0xc000, v232\n"
      "v_mfma_f32_16x16x32_bf16 v[132:135], a[244:247], v[40:43], v[132:135]\n"
      "v_add_u32 v233, 0xc000, v233\n"
      "v_mfma_f32_16x16x32_bf16 v[132:135], a[248:251], v[44:47], v[132:135]\n"
      "v_add_u32 v234, 0xc000, v234\n"
      "v_mfma_f32_16x16x32_bf16 v[132:135], a[252:255], v[60:63], v[132:135]\n"
      "v_add_u32 v235, 0xc000, v235\n"
      "s_waitcnt lgkmcnt(5)\n"
      "v_mfma_f32_16x16x32_bf16 v[132:135], a[48:51], v[64:67], v[132:135]\n"
      "v_cvt_pk_bf16_f32 v210, v136, v137\n"
      "v_mfma_f32_16x16x32_bf16 v[132:135], a[52:55], v[68:71], v[132:135]\n"
      "v_cvt_pk_bf16_f32 v211, v138, v139\n"
      "v_mfma_f32_16x16x32_bf16 v[132:135], a[56:59], v[84:87], v[132:135]\n"
      "ds_read_b128 v[216:219], v246 offset:24576\n"
      "v_mfma_f32_16x16x32_bf16 v[132:135], a[60:63], v[88:91], v[132:135]\n"
      "ds_read_b128 v[220:223], v246 offset:25600\n"
      "s_waitcnt lgkmcnt(2)\n"
      "v_mfma_f32_16x16x32_bf16 v[132:135], a[64:67], v[92:95], v[132:135]\n"
      "ds_read_b128 v[224:227], v246 offset:26624\n"
      "s_cbranch_vccnz LT2\n"
      "LS2:\n"
      "v_mfma_f32_16x16x32_bf16 v[132:135], a[68:71], v[108:111], v[132:135]\n"
      "v_lshlrev_b32 v0, 16, v24\n"
      "v_mfma_f32_16x16x32_bf16 v[132:135], a[72:75], v[112:115], v[132:135]\n"
      "ds_write_b64 v244, v[210:211] offset:136\n"
      "v_mfma_f32_16x16x32_bf16 v[132:135], a[76:79], v[116:119], v[132:135]\n"
      "v_and_b32 v1, 0xffff0000, v24\n"
      "v_mfma_f32_16x16x32_bf16 v[136:139], a[80:83], v[36:39], v[4:7]\n"
      "v_lshlrev_b32 v2, 16, v25\n"
      "v_mfma_f32_16x16x32_bf16 v[136:139], a[84:87], v[40:43], v[136:139]\n"
      "v_and_b32 v3, 0xffff0000, v25\n"
      "v_mfma_f32_16x16x32_bf16 v[136:139], a[88:91], v[44:47], v[136:139]\n"
      "v_mfma_f32_16x16x32_bf16 v[136:139], a[92:95], v[60:63], v[136:139]\n"
      "v_cvt_pk_bf16_f32 v208, v132, v133\n"
      "v_mfma_f32_16x16x32_bf16 v[136:139], a[96:99], v[64:67], v[136:139]\n"
      "v_cvt_pk_bf16_f32 v209, v134, v135\n"
      "v_mfma_f32_16x16x32_bf16 v[136:139], a[100:103], v[68:71], v[136:139]\n"
      "s_waitcnt lgkmcnt(3)\n"
      "v_mfma_f32_16x16x32_bf16 v[136:139], a[104:107], v[84:87], v[136:139]\n"
      "global_store_dwordx4 v247, v[216:219], s[54:55] offset:768 sc1\n"
      "v_mfma_f32_16x16x32_bf16 v[136:139], a[108:111], v[88:91], v[136:139]\n"
      "s_add_u32 m0, s17, 0x1000\n"
      "v_mfma_f32_16x16x32_bf16 v[136:139], a[112:115], v[92:95], v[136:139]\n"
      "global_load_lds_dwordx4 v237, s[52:53] offset:0 nt\n"
      "v_mfma_f32_16x16x32_bf16 v[136:139], a[116:119], v[108:111], v[136:139]\n"
      "ds_write_b64 v244, v[208:209] offset:12288\n"
      "v_mfma_f32_16x16x32_bf16 v[136:139], a[120:123], v[112:115], v[136:139]\n"
      "v_lshlrev_b32 v4, 16, v26\n"
      "v_mfma_f32_16x16x32_bf16 v[136:139], a[124:127], v[116:119], v[136:139]\n"
      "v_and_b32 v5, 0xffff0000, v26\n"
      "v_mfma_f32_16x16x32_bf16 v[132:135], a[128:131], v[36:39], v[8:11]\n"
      "v_lshlrev_b32 v6, 16, v27\n"
      "v_mfma_f32_16x16x32_bf16 v[132:135], a[132:135], v[40:43], v[132:135]\n"
      "v_and_b32 v7, 0xffff0000, v27\n"
      "v_mfma_f32_16x16x32_bf16 v[132:135], a[136:139], v[44:47], v[132:135]\n"
      "v_mfma_f32_16x16x32_bf16 v[132:135], a[140:143], v[60:63], v[132:135]\n"
      "v_cvt_pk_bf16_f32 v210, v136, v137\n"
      "v_mfma_f32_16x16x32_bf16 v[132:135], a[144:147], v[64:67], v[132:135]\n"
      "v_cvt_pk_bf16_f32 v211, v138, v139\n"
      "v_mfma_f32_16x16x32_bf16 v[132:135], a[148:151], v[68:71], v[132:135]\n"
      "v_lshlrev_b32 v8, 16, v28\n"
      "v_mfma_f32_16x16x32_bf16 v[132:135], a[152:155], v[84:87], v[132:135]\n"
      "s_waitcnt lgkmcnt(3)\n"
      "v_mfma_f32_16x16x32_bf16 v[132:135], a[156:159], v[88:91], v[132:135]\n"
      "global_store_dwordx4 v252, v[220:223], s[54:55] offset:768 sc1\n"
      "v_mfma_f32_16x16x32_bf16 v[132:135], a[160:163], v[92:95], v[132:135]\n"
      "s_add_u32 m0, s17, 0x2f00\n"
      "v_mfma_f32_16x16x32_bf16 v[132:135], a[164:167], v[108:111], v[132:135]\n"
      "ds_write_b64 v244, v[210:211] offset:12296\n"
      "v_mfma_f32_16x16x32_bf16 v[132:135], a[168:171], v[112:115], v[132:135]\n"
      "global_load_lds_dwordx4 v237, s[52:53] offset:256 nt\n"
      "v_mfma_f32_16x16x32_bf16 v[132:135], a[172:175], v[116:119], v[132:135]\n"
      "v_and_b32 v9, 0xffff0000, v28\n"
      "v_mfma_f32_16x16x32_bf16 v[136:139], a[176:179], v[36:39], v[12:15]\n"
      "v_lshlrev_b32 v10, 16, v29\n"
      "v_mfma_f32_16x16x32_bf16 v[136:139], a[180:183], v[40:43], v[136:139]\n"
      "v_and_b32 v11, 0xffff0000, v29\n"
      "v_mfma_f32_16x16x32_bf16 v[136:139], a[184:187], v[44:47], v[136:139]\n"
      "v_mfma_f32_16x16x32_bf16 v[136:139], a[188:191], v[60:63], v[136:139]\n"
      "v_cvt_pk_bf16_f32 v212, v132, v133\n"
      "v_mfma_f32_16x16x32_bf16 v[136:139], a[192:195], v[64:67], v[136:139]\n"
      "v_cvt_pk_bf16_f32 v213, v134, v135\n"
      "v_mfma_f32_16x16x32_bf16 v[136:139], a[196:199], v[68:71], v[136:139]\n"
      "v_lshlrev_b32 v12, 16, v30\n"
      "v_mfma_f32_16x16x32_bf16 v[136:139], a[200:203], v[84:87], v[136:139]\n"
      "v_and_b32 v13, 0xffff0000, v30\n"
      "v_mfma_f32_16x16x32_bf16 v[136:139], a[204:207], v[88:91], v[136:139]\n"
      "s_waitcnt lgkmcnt(3)\n"
      "v_mfma_f32_16x16x32_bf16 v[136:139], a[208:211], v[92:95], v[136:139]\n"
      "global_store_dwordx4 v253, v[224:227], s[54:55] offset:768 sc1\n"
      "s_cbranch_vccnz LT3\n"
      "LS3:\n"
      "v_mfma_f32_16x16x32_bf16 v[136:139], a[212:215], v[108:111], v[136:139]\n"
      "ds_write_b64 v245, v[212:213] offset:12288\n"
      "v_mfma_f32_16x16x32_bf16 v[136:139], a[216:219], v[112:115], v[136:139]\n"
      "s_add_u32 m0, s17, 0x4e00\n"
      "v_mfma_f32_16x16x32_bf16 v[136:139], a[220:223], v[116:119], v[136:139]\n"
      "global_load_lds_dwordx4 v237, s[52:53] offset:512 nt\n"
      "v_mfma_f32_16x16x32_bf16 v[132:135], a[224:227], v[36:39], v[16:19]\n"
      "v_lshlrev_b32 v14, 16, v31\n"
      "v_mfma_f32_16x16x32_bf16 v[132:135], a[228:231], v[40:43], v[132:135]\n"
      "v_and_b32 v15, 0xffff0000, v31\n"
      "v_mfma_f32_16x16x32_bf16 v[132:135], a[232:235], v[44:47], v[132:135]\n"
      "v_mfma_f32_16x16x32_bf16 v[132:135], a[236:239], v[60:63], v[132:135]\n"
      "s_memrealtime s[84:85]\n"
      "v_mfma_f32_16x16x32_bf16 v[132:135], a[0:3], v[64:67], v[132:135]\n"
      "v_cvt_pk_bf16_f32 v214, v136, v137\n"
      "v_mfma_f32_16x16x32_bf16 v[132:135], a[4:7], v[68:71], v[132:135]\n"
      "v_cvt_pk_bf16_f32 v215, v138, v139\n"
      "v_mfma_f32_16x16x32_bf16 v[132:135], a[8:11], v[84:87], v[132:135]\n"
      "v_lshlrev_b32 v16, 16, v32\n"
      "v_mfma_f32_16x16x32_bf16 v[132:135], a[12:15], v[88:91], v[132:135]\n"
      "v_and_b32 v17, 0xffff0000, v32\n"
      "v_mfma_f32_16x16x32_bf16 v[132:135], a[16:19], v[92:95], v[132:135]\n"
      "v_lshlrev_b32 v18, 16, v33\n"
      "v_mfma_f32_16x16x32_bf16 v[132:135], a[20:23], v[108:111], v[132:135]\n"
      "v_and_b32 v19, 0xffff0000, v33\n"
      "v_mfma_f32_16x16x32_bf16 v[132:135], v[140:143], v[112:115], v[132:135]\n"
      "s_waitcnt lgkmcnt(0)\n"
      "s_mov_b32 s66, s84\n"
      "s_mov_b32 s48, s84\n"
      "v_mfma_f32_16x16x32_bf16 v[132:135], v[156:159], v[116:119], v[132:135]\n"
      "s_waitcnt vmcnt(18)\n"
      "s_barrier\n"
      "ds_write_b64 v245, v[214:215] offset:12296\n"
      "v_mfma_f32_16x16x32_bf16 v[136:139], v[160:163], v[36:39], v[20:23]\n"
      "ds_read_b128 v[36:39], v232 offset:0\n"
      "v_mfma_f32_16x16x32_bf16 v[136:139], v[164:167], v[40:43], v[136:139]\n"
      "ds_read_b128 v[40:43], v233 offset:0\n"
      "v_mfma_f32_16x16x32_bf16 v[136:139], v[180:183], v[44:47], v[136:139]\n"
      "ds_read_b128 v[44:47], v234 offset:0\n"
      "v_mfma_f32_16x16x32_bf16 v[136:139], v[184:187], v[60:63], v[136:139]\n"
      "ds_read_b128 v[60:63], v235 offset:0\n"
      "v_cvt_pk_bf16_f32 v208, v132, v133\n"
      "v_mfma_f32_16x16x32_bf16 v[136:139], v[188:191], v[64:67], v[136:139]\n"
      "ds_read_b128 v[64:67], v232 offset:8192\n"
      "v_cvt_pk_bf16_f32 v209, v134, v135\n"
      "v_mfma_f32_16x16x32_bf16 v[136:139], v[204:207], v[68:71], v[136:139]\n"
      "ds_read_b128 v[68:71], v233 offset:8192\n"
      "v_mfma_f32_16x16x32_bf16 v[136:139], a[24:27], v[84:87], v[136:139]\n"
      "ds_read_b128 v[84:87], v234 offset:8192\n"
      "v_mfma_f32_16x16x32_bf16 v[136:139], a[28:31], v[88:91], v[136:139]\n"
      "ds_read_b128 v[88:91], v235 offset:8192\n"
      "v_mfma_f32_16x16x32_bf16 v[136:139], a[32:35], v[92:95], v[136:139]\n"
      "ds_read_b128 v[92:95], v232 offset:16384\n"
      "v_mfma_f32_16x16x32_bf16 v[136:139], a[36:39], v[108:111], v[136:139]\n"
      "ds_read_b128 v[108:111], v233 offset:16384\n"
      "ds_write_b64 v244, v[208:209] offset:12416\n"
      "v_mfma_f32_16x16x32_bf16 v[136:139], a[40:43], v[112:115], v[136:139]\n"
      "ds_read_b128 v[112:115], v234 offset:16384\n"
      "v_mfma_f32_16x16x32_bf16 v[136:139], a[44:47], v[116:119], v[136:139]\n"
      "ds_read_b128 v[116:119], v235 offset:16384\n"
      "s_waitcnt lgkmcnt(9)\n"
      "v_mfma_f32_16x16x32_bf16 v[132:135], a[240:243], v[36:39], v[0:3]\n"
      "s_sub_u32 s69, s66, s68\n"
      "v_mfma_f32_16x16x32_bf16 v[132:135], a[244:247], v[40:43], v[132:135]\n"
      "s_mov_b32 s68, s67\n"
      "v_mfma_f32_16x16x32_bf16 v[132:135], a[248:251], v[44:47], v[132:135]\n"
      "s_mov_b32 s67, s66\n"
      "v_mfma_f32_16x16x32_bf16 v[132:135], a[252:255], v[60:63], v[132:135]\n"
      "s_cmp_gt_i32 s69, 460\n"
      "s_cselect_b64 s[70:71], -1, s[70:71]\n"
      "s_waitcnt lgkmcnt(5)\n"
      "v_mfma_f32_16x16x32_bf16 v[132:135], a[48:51], v[64:67], v[132:135]\n"
      "s_or_b64 vcc, vcc, s[70:71]\n"
      "v_cvt_pk_bf16_f32 v211, v138, v139\n"
      "v_mfma_f32_16x16x32_bf16 v[132:135], a[52:55], v[68:71], v[132:135]\n"
      "v_lshlrev_b32 v20, 16, v34\n"
      "v_mfma_f32_16x16x32_bf16 v[132:135], a[56:59], v[84:87], v[132:135]\n"
      "v_and_b32 v21, 0xffff0000, v34\n"
      "v_mfma_f32_16x16x32_bf16 v[132:135], a[60:63], v[88:91], v[132:135]\n"
      "v_lshlrev_b32 v22, 16, v35\n"
      "s_waitcnt lgkmcnt(0)\n"
      "v_mfma_f32_16x16x32_bf16 v[132:135], a[64:67], v[92:95], v[132:135]\n"
      "v_and_b32 v23, 0xffff0000, v35\n"
      "s_cbranch_vccnz LT4\n"
      "LS4:\n"
      "v_mfma_f32_16x16x32_bf16 v[132:135], a[68:71], v[108:111], v[132:135]\n"
      "v_cvt_pk_bf16_f32 v210, v136, v137\n"
      "v_mfma_f32_16x16x32_bf16 v[132:135], a[72:75], v[112:115], v[132:135]\n"
      "s_add_u32 s54, s44, s32\n"
      "s_addc_u32 s55, s45, 0\n"
      "v_mfma_f32_16x16x32_bf16 v[132:135], a[76:79], v[116:119], v[132:135]\n"
      "ds_read_b128 v[216:219], v246 offset:0\n"
      "v_mfma_f32_16x16x32_bf16 v[136:139], a[80:83], v[36:39], v[4:7]\n"
      "ds_read_b128 v[220:223], v246 offset:1024\n"
      "v_mfma_f32_16x16x32_bf16 v[136:139], a[84:87], v[40:43], v[136:139]\n"
      "ds_read_b128 v[224:227], v246 offset:2048\n"
      "v_mfma_f32_16x16x32_bf16 v[136:139], a[88:91], v[44:47], v[136:139]\n"
      "ds_write_b64 v244, v[210:211] offset:12424\n"
      "v_mfma_f32_16x16x32_bf16 v[136:139], a[92:95], v[60:63], v[136:139]\n"
      "v_cvt_pk_bf16_f32 v208, v132, v133\n"
      "v_mfma_f32_16x16x32_bf16 v[136:139], a[96:99], v[64:67], v[136:139]\n"
      "v_cvt_pk_bf16_f32 v209, v134, v135\n"
      "v_mfma_f32_16x16x32_bf16 v[136:139], a[100:103], v[68:71], v[136:139]\n"
      "s_waitcnt lgkmcnt(3)\n"
      "v_mfma_f32_16x16x32_bf16 v[136:139], a[104:107], v[84:87], v[136:139]\n"
      "global_store_dwordx4 v247, v[216:219], s[54:55] offset:0 sc1\n"
      "v_mfma_f32_16x16x32_bf16 v[136:139], a[108:111], v[88:91], v[136:139]\n"
      "s_add_u32 s52, s42, s34\n"
      "s_addc_u32 s53, s43, 0\n"
      "v_mfma_f32_16x16x32_bf16 v[136:139], a[112:115], v[92:95], v[136:139]\n"
      "s_add_u32 m0, s17, 0x6000\n"
      "v_mfma_f32_16x16x32_bf16 v[136:139], a[116:119], v[108:111], v[136:139]\n"
      "ds_write_b64 v244, v[208:209] offset:24576\n"
      "v_mfma_f32_16x16x32_bf16 v[136:139], a[120:123], v[112:115], v[136:139]\n"
      "global_load_lds_dwordx4 v236, s[52:53] offset:0 nt\n"
      "v_mfma_f32_16x16x32_bf16 v[136:139], a[124:127], v[116:119], v[136:139]\n"
      "v_mfma_f32_16x16x32_bf16 v[132:135], a[128:131], v[36:39], v[8:11]\n"
      "v_mfma_f32_16x16x32_bf16 v[132:135], a[132:135], v[40:43], v[132:135]\n"
      "v_mfma_f32_16x16x32_bf16 v[132:135], a[136:139], v[44:47], v[132:135]\n"
      "v_mfma_f32_16x16x32_bf16 v[132:135], a[140:143], v[60:63], v[132:135]\n"
      "v_cvt_pk_bf16_f32 v210, v136, v137\n"
      "v_mfma_f32_16x16x32_bf16 v[132:135], a[144:147], v[64:67], v[132:135]\n"
      "v_cvt_pk_bf16_f32 v211, v138, v139\n"
      "v_mfma_f32_16x16x32_bf16 v[132:135], a[148:151], v[68:71], v[132:135]\n"
      "v_mfma_f32_16x16x32_bf16 v[132:135], a[152:155], v[84:87], v[132:135]\n"
      "s_waitcnt lgkmcnt(3)\n"
      "v_mfma_f32_16x16x32_bf16 v[132:135], a[156:159], v[88:91], v[132:135]\n"
      "global_store_dwordx4 v252, v[220:223], s[54:55] offset:0 sc1\n"
      "v_mfma_f32_16x16x32_bf16 v[132:135], a[160:163], v[92:95], v[132:135]\n"
      "s_add_u32 m0, s17, 0x7f00\n"
      "v_mfma_f32_16x16x32_bf16 v[132:135], a[164:167], v[108:111], v[132:135]\n"
      "ds_write_b64 v244, v[210:211] offset:24584\n"
      "v_mfma_f32_16x16x32_bf16 v[132:135], a[168:171], v[112:115], v[132:135]\n"
      "global_load_lds_dwordx4 v236, s[52:53] offset:256 nt\n"
      "v_mfma_f32_16x16x32_bf16 v[132:135], a[172:175], v[116:119], v[132:135]\n"
      "v_mfma_f32_16x16x32_bf16 v[136:139], a[176:179], v[36:39], v[12:15]\n"
      "v_mfma_f32_16x16x32_bf16 v[136:139], a[180:183], v[40:43], v[136:139]\n"
      "v_mfma_f32_16x16x32_bf16 v[136:139], a[184:187], v[44:47], v[136:139]\n"
      "v_mfma_f32_16x16x32_bf16 v[136:139], a[188:191], v[60:63], v[136:139]\n"
      "v_cvt_pk_bf16_f32 v212, v132, v133\n"
      "v_mfma_f32_16x16x32_bf16 v[136:139], a[192:195], v[64:67], v[136:139]\n"
      "v_cvt_pk_bf16_f32 v213, v134, v135\n"
      "v_mfma_f32_16x16x32_bf16 v[136:139], a[196:199], v[68:71], v[136:139]\n"
      "v_mfma_f32_16x16x32_bf16 v[136:139], a[200:203], v[84:87], v[136:139]\n"
      "v_mfma_f32_16x16x32_bf16 v[136:139], a[204:207], v[88:91], v[136:139]\n"
      "s_waitcnt lgkmcnt(3)\n"
      "v_mfma_f32_16x16x32_bf16 v[136:139], a[208:211], v[92:95], v[136:139]\n"
      "global_store_dwordx4 v253, v[224:227], s[54:55] offset:0 sc1\n"
      "s_cbranch_vccnz LT5\n"
      "LS5:\n"
      "v_mfma_f32_16x16x32_bf16 v[136:139], a[212:215], v[108:111], v[136:139]\n"
      "ds_write_b64 v245, v[212:213] offset:24576\n"
      "v_mfma_f32_16x16x32_bf16 v[136:139], a[216:219], v[112:115], v[136:139]\n"
      "s_add_u32 m0, s17, 0x9e00\n"
      "v_mfma_f32_16x16x32_bf16 v[136:139], a[220:223], v[116:119], v[136:139]\n"
      "global_load_lds_dwordx4 v236, s[52:53] offset:512 nt\n"
      "v_mfma_f32_16x16x32_bf16 v[132:135], a[224:227], v[36:39], v[16:19]\n"
      "v_mfma_f32_16x16x32_bf16 v[132:135], a[228:231], v[40:43], v[132:135]\n"
      "v_mfma_f32_16x16x32_bf16 v[132:135], a[232:235], v[44:47], v[132:135]\n"
      "v_mfma_f32_16x16x32_bf16 v[132:135], a[236:239], v[60:63], v[132:135]\n"
      "v_cvt_pk_bf16_f32 v214, v136, v137\n"
      "v_mfma_f32_16x16x32_bf16 v[132:135], a[0:3], v[64:67], v[132:135]\n"
      "v_cvt_pk_bf16_f32 v215, v138, v139\n"
      "v_mfma_f32_16x16x32_bf16 v[132:135], a[4:7], v[68:71], v[132:135]\n"
      "v_mfma_f32_16x16x32_bf16 v[132:135], a[8:11], v[84:87], v[132:135]\n"
      "v_mfma_f32_16x16x32_bf16 v[132:135], a[12:15], v[88:91], v[132:135]\n"
      "v_mfma_f32_16x16x32_bf16 v[132:135], a[16:19], v[92:95], v[132:135]\n"
      "v_mfma_f32_16x16x32_bf16 v[132:135], a[20:23], v[108:111], v[132:135]\n"
      "ds_write_b64 v245, v[214:215] offset:24584\n"
      "v_mfma_f32_16x16x32_bf16 v[132:135], v[140:143], v[112:115], v[132:135]\n"
      "v_mfma_f32_16x16x32_bf16 v[132:135], v[156:159], v[116:119], v[132:135]\n"
      "s_waitcnt lgkmcnt(4)\n"
      "s_barrier\n"
      "v_mfma_f32_16x16x32_bf16 v[136:139], v[160:163], v[36:39], v[20:23]\n"
      "ds_read_b128 v[36:39], v232 offset:256\n"
      "v_mfma_f32_16x16x32_bf16 v[136:139], v[164:167], v[40:43], v[136:139]\n"
      "ds_read_b128 v[40:43], v233 offset:256\n"
      "v_mfma_f32_16x16x32_bf16 v[136:139], v[180:183], v[44:47], v[136:139]\n"
      "ds_read_b128 v[44:47], v234 offset:256\n"
      "v_mfma_f32_16x16x32_bf16 v[136:139], v[184:187], v[60:63], v[136:139]\n"
      "ds_read_b128 v[60:63], v235 offset:256\n"
      "v_cvt_pk_bf16_f32 v208, v132, v133\n"
      "v_mfma_f32_16x16x32_bf16 v[136:139], v[188:191], v[64:67], v[136:139]\n"
      "ds_read_b128 v[64:67], v232 offset:8448\n"
      "v_cvt_pk_bf16_f32 v209, v134, v135\n"
      "v_mfma_f32_16x16x32_bf16 v[136:139], v[204:207], v[68:71], v[136:139]\n"
      "ds_read_b128 v[68:71], v233 offset:8448\n"
      "v_mfma_f32_16x16x32_bf16 v[136:139], a[24:27], v[84:87], v[136:139]\n"
      "ds_read_b128 v[84:87], v234 offset:8448\n"
      "v_mfma_f32_16x16x32_bf16 v[136:139], a[28:31], v[88:91], v[136:139]\n"
      "ds_read_b128 v[88:91], v235 offset:8448\n"
      "v_mfma_f32_16x16x32_bf16 v[136:139], a[32:35], v[92:95], v[136:139]\n"
      "ds_read_b128 v[92:95], v232 offset:16640\n"
      "v_mfma_f32_16x16x32_bf16 v[136:139], a[36:39], v[108:111], v[136:139]\n"
      "ds_read_b128 v[108:111], v233 offset:16640\n"
      "v_mfma_f32_16x16x32_bf16 v[136:139], a[40:43], v[112:115], v[136:139]\n"
      "ds_read_b128 v[112:115], v234 offset:16640\n"
      "v_mfma_f32_16x16x32_bf16 v[136:139], a[44:47], v[116:119], v[136:139]\n"
      "ds_read_b128 v[116:119], v235 offset:16640\n"
      "s_waitcnt lgkmcnt(8)\n"
      "v_mfma_f32_16x16x32_bf16 v[132:135], a[240:243], v[36:39], v[0:3]\n"
      "ds_write_b64 v244, v[208:209] offset:24704\n"
      "v_mfma_f32_16x16x32_bf16 v[132:135], a[244:247], v[40:43], v[132:135]\n"
      "v_mfma_f32_16x16x32_bf16 v[132:135], a[248:251], v[44:47], v[132:135]\n"
      "v_mfma_f32_16x16x32_bf16 v[132:135], a[252:255], v[60:63], v[132:135]\n"
      "v_cvt_pk_bf16_f32 v210, v136, v137\n"
      "s_waitcnt lgkmcnt(5)\n"
      "v_mfma_f32_16x16x32_bf16 v[132:135], a[48:51], v[64:67], v[132:135]\n"
      "v_cvt_pk_bf16_f32 v211, v138, v139\n"
      "v_mfma_f32_16x16x32_bf16 v[132:135], a[52:55], v[68:71], v[132:135]\n"
      "v_mfma_f32_16x16x32_bf16 v[132:135], a[56:59], v[84:87], v[132:135]\n"
      "ds_read_b128 v[216:219], v246 offset:12288\n"
      "v_mfma_f32_16x16x32_bf16 v[132:135], a[60:63], v[88:91], v[132:135]\n"
      "ds_read_b128 v[220:223], v246 offset:13312\n"
      "s_waitcnt lgkmcnt(3)\n"
      "v_mfma_f32_16x16x32_bf16 v[132:135], a[64:67], v[92:95], v[132:135]\n"
      "ds_read_b128 v[224:227], v246 offset:14336\n"
      "v_mfma_f32_16x16x32_bf16 v[132:135], a[68:71], v[108:111], v[132:135]\n"
      "ds_write_b64 v244, v[210:211] offset:24712\n"
      "v_mfma_f32_16x16x32_bf16 v[132:135], a[72:75], v[112:115], v[132:135]\n"
      "s_cbranch_vccnz LT6\n"
      "LS6:\n"
      "v_mfma_f32_16x16x32_bf16 v[132:135], a[76:79], v[116:119], v[132:135]\n"
      "v_mfma_f32_16x16x32_bf16 v[136:139], a[80:83], v[36:39], v[4:7]\n"
      "v_mfma_f32_16x16x32_bf16 v[136:139], a[84:87], v[40:43], v[136:139]\n"
      "v_mfma_f32_16x16x32_bf16 v[136:139], a[88:91], v[44:47], v[136:139]\n"
      "v_mfma_f32_16x16x32_bf16 v[136:139], a[92:95], v[60:63], v[136:139]\n"
      "v_cvt_pk_bf16_f32 v208, v132, v133\n"
      "v_mfma_f32_16x16x32_bf16 v[136:139], a[96:99], v[64:67], v[136:139]\n"
      "v_cvt_pk_bf16_f32 v209, v134, v135\n"
      "v_mfma_f32_16x16x32_bf16 v[136:139], a[100:103], v[68:71], v[136:139]\n"
      "s_waitcnt lgkmcnt(3)\n"
      "v_mfma_f32_16x16x32_bf16 v[136:139], a[104:107], v[84:87], v[136:139]\n"
      "global_store_dwordx4 v247, v[216:219], s[54:55] offset:768 sc1\n"
      "v_mfma_f32_16x16x32_bf16 v[136:139], a[108:111], v[88:91], v[136:139]\n"
      "s_add_u32 m0, s17, 0x7000\n"
      "v_mfma_f32_16x16x32_bf16 v[136:139], a[112:115], v[92:95], v[136:139]\n"
      "global_load_lds_dwordx4 v237, s[52:53] offset:0 nt\n"
      "v_mfma_f32_16x16x32_bf16 v[136:139], a[116:119], v[108:111], v[136:139]\n"
      "ds_write_b64 v244, v[208:209] offset:0\n"
      "v_mfma_f32_16x16x32_bf16 v[136:139], a[120:123], v[112:115], v[136:139]\n"
      "v_mfma_f32_16x16x32_bf16 v[136:139], a[124:127], v[116:119], v[136:139]\n"
      "v_mfma_f32_16x16x32_bf16 v[132:135], a[128:131], v[36:39], v[8:11]\n"
      "v_mfma_f32_16x16x32_bf16 v[132:135], a[132:135], v[40:43], v[132:135]\n"
      "v_mfma_f32_16x16x32_bf16 v[132:135], a[136:139], v[44:47], v[132:135]\n"
      "v_mfma_f32_16x16x32_bf16 v[132:135], a[140:143], v[60:63], v[132:135]\n"
      "v_cvt_pk_bf16_f32 v210, v136, v137\n"
      "v_mfma_f32_16x16x32_bf16 v[132:135], a[144:147], v[64:67], v[132:135]\n"
      "v_cvt_pk_bf16_f32 v211, v138, v139\n"
      "v_mfma_f32_16x16x32_bf16 v[132:135], a[148:151], v[68:71], v[132:135]\n"
      "v_mfma_f32_16x16x32_bf16 v[132:135], a[152:155], v[84:87], v[132:135]\n"
      "s_waitcnt lgkmcnt(3)\n"
      "v_mfma_f32_16x16x32_bf16 v[132:135], a[156:159], v[88:91], v[132:135]\n"
      "global_store_dwordx4 v252, v[220:223], s[54:55] offset:768 sc1\n"
      "v_mfma_f32_16x16x32_bf16 v[132:135], a[160:163], v[92:95], v[132:135]\n"
      "s_add_u32 m0, s17, 0x8f00\n"
      "v_mfma_f32_16x16x32_bf16 v[132:135], a[164:167], v[108:111], v[132:135]\n"
      "ds_write_b64 v244, v[210:211] offset:8\n"
      "v_mfma_f32_16x16x32_bf16 v[132:135], a[168:171], v[112:115], v[132:135]\n"
      "global_load_lds_dwordx4 v237, s[52:53] offset:256 nt\n"
      "v_mfma_f32_16x16x32_bf16 v[132:135], a[172:175], v[116:119], v[132:135]\n"
      "v_mfma_f32_16x16x32_bf16 v[136:139], a[176:179], v[36:39], v[12:15]\n"
      "v_mfma_f32_16x16x32_bf16 v[136:139], a[180:183], v[40:43], v[136:139]\n"
      "v_mfma_f32_16x16x32_bf16 v[136:139], a[184:187], v[44:47], v[136:139]\n"
      "v_mfma_f32_16x16x32_bf16 v[136:139], a[188:191], v[60:63], v[136:139]\n"
      "v_cvt_pk_bf16_f32 v212, v132, v133\n"
      "v_mfma_f32_16x16x32_bf16 v[136:139], a[192:195], v[64:67], v[136:139]\n"
      "v_cvt_pk_bf16_f32 v213, v134, v135\n"
      "v_mfma_f32_16x16x32_bf16 v[136:139], a[196:199], v[68:71], v[136:139]\n"
      "v_mfma_f32_16x16x32_bf16 v[136:139], a[200:203], v[84:87], v[136:139]\n"
      "v_mfma_f32_16x16x32_bf16 v[136:139], a[204:207], v[88:91], v[136:139]\n"
      "s_waitcnt lgkmcnt(3)\n"
      "v_mfma_f32_16x16x32_bf16 v[136:139], a[208:211], v[92:95], v[136:139]\n"
      "global_store_dwordx4 v253, v[224:227], s[54:55] offset:768 sc1\n"
      "s_cbranch_vccnz LT7\n"
      "LS7:\n"
      "v_mfma_f32_16x16x32_bf16 v[136:139], a[212:215], v[108:111], v[136:139]\n"
      "ds_write_b64 v245, v[212:213] offset:0\n"
      "v_mfma_f32_16x16x32_bf16 v[136:139], a[216:219], v[112:115], v[136:139]\n"
      "s_add_u32 m0, s17, 0xae00\n"
      "v_mfma_f32_16x16x32_bf16 v[136:139], a[220:223], v[116:119], v[136:139]\n"
      "global_load_lds_dwordx4 v237, s[52:53] offset:512 nt\n"
      "v_mfma_f32_16x16x32_bf16 v[132:135], a[224:227], v[36:39], v[16:19]\n"
      "v_mfma_f32_16x16x32_bf16 v[132:135], a[228:231], v[40:43], v[132:135]\n"
      "v_mfma_f32_16x16x32_bf16 v[132:135], a[232:235], v[44:47], v[132:135]\n"
      "v_mfma_f32_16x16x32_bf16 v[132:135], a[236:239], v[60:63], v[132:135]\n"
      "s_memrealtime s[84:85]\n"
      "v_mfma_f32_16x16x32_bf16 v[132:135], a[0:3], v[64:67], v[132:135]\n"
      "v_cvt_pk_bf16_f32 v214, v136, v137\n"
      "v_mfma_f32_16x16x32_bf16 v[132:135], a[4:7], v[68:71], v[132:135]\n"
      "v_cvt_pk_bf16_f32 v215, v138, v139\n"
      "v_mfma_f32_16x16x32_bf16 v[132:135], a[8:11], v[84:87], v[132:135]\n"
      "v_mfma_f32_16x16x32_bf16 v[132:135], a[12:15], v[88:91], v[132:135]\n"
      "v_mfma_f32_16x16x32_bf16 v[132:135], a[16:19], v[92:95], v[132:135]\n"
      "v_mfma_f32_16x16x32_bf16 v[132:135], a[20:23], v[108:111], v[132:135]\n"
      "v_mfma_f32_16x16x32_bf16 v[132:135], v[140:143], v[112:115], v[132:135]\n"
      "s_waitcnt lgkmcnt(0)\n"
      "s_mov_b32 s66, s84\n"
      "v_mfma_f32_16x16x32_bf16 v[132:135], v[156:159], v[116:119], v[132:135]\n"
      "s_waitcnt vmcnt(24)\n"
      "s_barrier\n"
      "ds_write_b64 v245, v[214:215] offset:8\n"
      "v_mfma_f32_16x16x32_bf16 v[136:139], v[160:163], v[36:39], v[20:23]\n"
      "ds_read_b128 v[36:39], v232 offset:24576\n"
      "s_lshl_b32 s79, s90, 1\n"
      "v_mfma_f32_16x16x32_bf16 v[136:139], v[164:167], v[40:43], v[136:139]\n"
      "ds_read_b128 v[40:43], v233 offset:24576\n"
      "v_mad_i32_i24 v252, v251, s79, v252\n"
      "v_mfma_f32_16x16x32_bf16 v[136:139], v[180:183], v[44:47], v[136:139]\n"
      "ds_read_b128 v[44:47], v234 offset:24576\n"
      "s_mul_i32 s79, s79, s100\n"
      "v_mfma_f32_16x16x32_bf16 v[136:139], v[184:187], v[60:63], v[136:139]\n"
      "ds_read_b128 v[60:63], v235 offset:24576\n"
      "v_cvt_pk_bf16_f32 v208, v132, v133\n"
      "v_mfma_f32_16x16x32_bf16 v[136:139], v[188:191], v[64:67], v[136:139]\n"
      "ds_read_b128 v[64:67], v232 offset:32768\n"
      "v_cvt_pk_bf16_f32 v209, v134, v135\n"
      "v_mfma_f32_16x16x32_bf16 v[136:139], v[204:207], v[68:71], v[136:139]\n"
      "ds_read_b128 v[68:71], v233 offset:32768\n"
      "v_add_u32 v247, s79, v247\n"
      "v_mfma_f32_16x16x32_bf16 v[136:139], a[24:27], v[84:87], v[136:139]\n"
      "ds_read_b128 v[84:87], v234 offset:32768\n"
      "s_mul_i32 s79, s79, s101\n"
      "v_mfma_f32_16x16x32_bf16 v[136:139], a[28:31], v[88:91], v[136:139]\n"
      "ds_read_b128 v[88:91], v235 offset:32768\n"
      "v_add_u32 v253, s79, v253\n"
      "v_mfma_f32_16x16x32_bf16 v[136:139], a[32:35], v[92:95], v[136:139]\n"
      "ds_read_b128 v[92:95], v232 offset:40960\n"
      "v_mfma_f32_16x16x32_bf16 v[136:139], a[36:39], v[108:111], v[136:139]\n"
      "ds_read_b128 v[108:111], v233 offset:40960\n"
      "ds_write_b64 v244, v[208:209] offset:128\n"
      "v_mfma_f32_16x16x32_bf16 v[136:139], a[40:43], v[112:115], v[136:139]\n"
      "ds_read_b128 v[112:115], v234 offset:40960\n"
      "v_mfma_f32_16x16x32_bf16 v[136:139], a[44:47], v[116:119], v[136:139]\n"
      "ds_read_b128 v[116:119], v235 offset:40960\n"
      "s_waitcnt lgkmcnt(9)\n"
      "v_mfma_f32_16x16x32_bf16 v[132:135], a[240:243], v[36:39], v[0:3]\n"
      "s_sub_u32 s69, s66, s68\n"
      "v_mfma_f32_16x16x32_bf16 v[132:135], a[244:247], v[40:43], v[132:135]\n"
      "s_mov_b32 s68, s67\n"
      "v_mfma_f32_16x16x32_bf16 v[132:135], a[248:251], v[44:47], v[132:135]\n"
      "s_mov_b32 s67, s66\n"
      "v_mfma_f32_16x16x32_bf16 v[132:135], a[252:255], v[60:63], v[132:135]\n"
      "s_cmp_gt_i32 s69, 460\n"
      "s_cselect_b64 s[70:71], -1, s[70:71]\n"
      "s_waitcnt lgkmcnt(5)\n"
      "v_mfma_f32_16x16x32_bf16 v[132:135], a[48:51], v[64:67], v[132:135]\n"
      "s_or_b64 vcc, vcc, s[70:71]\n"
      "s_mul_i32 s78, s92, s19\n"
      "v_mfma_f32_16x16x32_bf16 v[132:135], a[52:55], v[68:71], v[132:135]\n"
      "v_cvt_pk_bf16_f32 v210, v136, v137\n"
      "v_add_u32 v236, s78, v236\n"
      "v_mfma_f32_16x16x32_bf16 v[132:135], a[56:59], v[84:87], v[132:135]\n"
      "v_cvt_pk_bf16_f32 v211, v138, v139\n"
      "s_mul_i32 s78, s78, s99\n"
      "v_mfma_f32_16x16x32_bf16 v[132:135], a[60:63], v[88:91], v[132:135]\n"
      "s_add_u32 s54, s30, s33\n"
      "s_addc_u32 s55, s31, 0\n"
      "s_waitcnt lgkmcnt(0)\n"
      "v_mfma_f32_16x16x32_bf16 v[132:135], a[64:67], v[92:95], v[132:135]\n"
      "ds_read_b128 v[216:219], v246 offset:24576\n"
      "v_add_u32 v237, s78, v237\n"
      "v_mfma_f32_16x16x32_bf16 v[132:135], a[68:71], v[108:111], v[132:135]\n"
      "ds_read_b128 v[220:223], v246 offset:25600\n"
      "s_cbranch_vccnz LT8\n"
      "LS8:\n"
      "v_mfma_f32_16x16x32_bf16 v[132:135], a[72:75], v[112:115], v[132:135]\n"
      "ds_read_b128 v[224:227], v246 offset:26624\n"
      "v_mfma_f32_16x16x32_bf16 v[132:135], a[76:79], v[116:119], v[132:135]\n"
      "ds_write_b64 v244, v[210:211] offset:136\n"
      "v_mfma_f32_16x16x32_bf16 v[136:139], a[80:83], v[36:39], v[4:7]\n"
      "v_mfma_f32_16x16x32_bf16 v[136:139], a[84:87], v[40:43], v[136:139]\n"
      "v_mfma_f32_16x16x32_bf16 v[136:139], a[88:91], v[44:47], v[136:139]\n"
      "v_mfma_f32_16x16x32_bf16 v[136:139], a[92:95], v[60:63], v[136:139]\n"
      "v_cvt_pk_bf16_f32 v208, v132, v133\n"
      "v_mfma_f32_16x16x32_bf16 v[136:139], a[96:99], v[64:67], v[136:139]\n"
      "v_cvt_pk_bf16_f32 v209, v134, v135\n"
      "v_mfma_f32_16x16x32_bf16 v[136:139], a[100:103], v[68:71], v[136:139]\n"
      "s_waitcnt lgkmcnt(3)\n"
      "v_mfma_f32_16x16x32_bf16 v[136:139], a[104:107], v[84:87], v[136:139]\n"
      "global_store_dwordx4 v247, v[216:219], s[54:55] offset:0 sc1\n"
      "v_mfma_f32_16x16x32_bf16 v[136:139], a[108:111], v[88:91], v[136:139]\n"
      "s_add_u32 s52, s20, s35\n"
      "s_addc_u32 s53, s21, 0\n"
      "v_mfma_f32_16x16x32_bf16 v[136:139], a[112:115], v[92:95], v[136:139]\n"
      "s_add_u32 m0, s17, 0xc000\n"
      "v_mfma_f32_16x16x32_bf16 v[136:139], a[116:119], v[108:111], v[136:139]\n"
      "ds_write_b64 v244, v[208:209] offset:12288\n"
      "v_mfma_f32_16x16x32_bf16 v[136:139], a[120:123], v[112:115], v[136:139]\n"
      "global_load_lds_dwordx4 v236, s[52:53] offset:0 nt\n"
      "v_mfma_f32_16x16x32_bf16 v[136:139], a[124:127], v[116:119], v[136:139]\n"
      "v_mfma_f32_16x16x32_bf16 v[132:135], a[128:131], v[36:39], v[8:11]\n"
      "v_min_u32 v28, 0x370, v239\n"
      "v_mfma_f32_16x16x32_bf16 v[132:135], a[132:135], v[40:43], v[132:135]\n"
      "v_mfma_f32_16x16x32_bf16 v[132:135], a[136:139], v[44:47], v[132:135]\n"
      "v_mfma_f32_16x16x32_bf16 v[132:135], a[140:143], v[60:63], v[132:135]\n"
      "v_cvt_pk_bf16_f32 v210, v136, v137\n"
      "v_mfma_f32_16x16x32_bf16 v[132:135], a[144:147], v[64:67], v[132:135]\n"
      "v_cvt_pk_bf16_f32 v211, v138, v139\n"
      "v_mfma_f32_16x16x32_bf16 v[132:135], a[148:151], v[68:71], v[132:135]\n"
      "v_add_u32 v28, 0x2480, v28\n"
      "v_mfma_f32_16x16x32_bf16 v[132:135], a[152:155], v[84:87], v[132:135]\n"
      "s_waitcnt lgkmcnt(3)\n"
      "global_load_dwordx4 v[24:27], v28, s[28:29] sc1\n"
      "v_mfma_f32_16x16x32_bf16 v[132:135], a[156:159], v[88:91], v[132:135]\n"
      "global_store_dwordx4 v252, v[220:223], s[54:55] offset:0 sc1\n"
      "v_mfma_f32_16x16x32_bf16 v[132:135], a[160:163], v[92:95], v[132:135]\n"
      "s_add_u32 m0, s17, 0xdf00\n"
      "v_mfma_f32_16x16x32_bf16 v[132:135], a[164:167], v[108:111], v[132:135]\n"
      "ds_write_b64 v244, v[210:211] offset:12296\n"
      "v_mfma_f32_16x16x32_bf16 v[132:135], a[168:171], v[112:115], v[132:135]\n"
      "global_load_lds_dwordx4 v236, s[52:53] offset:256 nt\n"
      "v_mfma_f32_16x16x32_bf16 v[132:135], a[172:175], v[116:119], v[132:135]\n"
      "v_mfma_f32_16x16x32_bf16 v[136:139], a[176:179], v[36:39], v[12:15]\n"
      "v_mfma_f32_16x16x32_bf16 v[136:139], a[180:183], v[40:43], v[136:139]\n"
      "v_mfma_f32_16x16x32_bf16 v[136:139], a[184:187], v[44:47], v[136:139]\n"
      "v_mfma_f32_16x16x32_bf16 v[136:139], a[188:191], v[60:63], v[136:139]\n"
      "v_cvt_pk_bf16_f32 v212, v132, v133\n"
      "v_mfma_f32_16x16x32_bf16 v[136:139], a[192:195], v[64:67], v[136:139]\n"
      "v_cvt_pk_bf16_f32 v213, v134, v135\n"
      "v_mfma_f32_16x16x32_bf16 v[136:139], a[196:199], v[68:71], v[136:139]\n"
      "v_mfma_f32_16x16x32_bf16 v[136:139], a[200:203], v[84:87], v[136:139]\n"
      "v_mfma_f32_16x16x32_bf16 v[136:139], a[204:207], v[88:91], v[136:139]\n"
      "s_waitcnt lgkmcnt(3)\n"
      "v_mfma_f32_16x16x32_bf16 v[136:139], a[208:211], v[92:95], v[136:139]\n"
      "global_store_dwordx4 v253, v[224:227], s[54:55] offset:0 sc1\n"
      "s_cbranch_vccnz LT9\n"
      "LS9:\n"
      "v_mfma_f32_16x16x32_bf16 v[136:139], a[212:215], v[108:111], v[136:139]\n"
      "ds_write_b64 v245, v[212:213] offset:12288\n"
      "v_mfma_f32_16x16x32_bf16 v[136:139], a[216:219], v[112:115], v[136:139]\n"
      "s_add_u32 m0, s17, 0xfe00\n"
      "v_mfma_f32_16x16x32_bf16 v[136:139], a[220:223], v[116:119], v[136:139]\n"
      "global_load_lds_dwordx4 v236, s[52:53] offset:512 nt\n"
      "v_mfma_f32_16x16x32_bf16 v[132:135], a[224:227], v[36:39], v[16:19]\n"
      "v_mfma_f32_16x16x32_bf16 v[132:135], a[228:231], v[40:43], v[132:135]\n"
      "v_mfma_f32_16x16x32_bf16 v[132:135], a[232:235], v[44:47], v[132:135]\n"
      "v_mfma_f32_16x16x32_bf16 v[132:135], a[236:239], v[60:63], v[132:135]\n"
      "v_cvt_pk_bf16_f32 v214, v136, v137\n"
      "v_mfma_f32_16x16x32_bf16 v[132:135], a[0:3], v[64:67], v[132:135]\n"
      "v_cvt_pk_bf16_f32 v215, v138, v139\n"
      "v_mfma_f32_16x16x32_bf16 v[132:135], a[4:7], v[68:71], v[132:135]\n"
      "v_mfma_f32_16x16x32_bf16 v[132:135], a[8:11], v[84:87], v[132:135]\n"
      "v_mfma_f32_16x16x32_bf16 v[132:135], a[12:15], v[88:91], v[132:135]\n"
      "v_mfma_f32_16x16x32_bf16 v[132:135], a[16:19], v[92:95], v[132:135]\n"
      "v_mfma_f32_16x16x32_bf16 v[132:135], a[20:23], v[108:111], v[132:135]\n"
      "ds_write_b64 v245, v[214:215] offset:12296\n"
      "v_mfma_f32_16x16x32_bf16 v[132:135], v[140:143], v[112:115], v[132:135]\n"
      "v_mfma_f32_16x16x32_bf16 v[132:135], v[156:159], v[116:119], v[132:135]\n"
      "s_waitcnt lgkmcnt(4)\n"
      "s_barrier\n"
      "v_mfma_f32_16x16x32_bf16 v[136:139], v[160:163], v[36:39], v[20:23]\n"
      "ds_read_b128 v[36:39], v232 offset:24832\n"
      "v_mfma_f32_16x16x32_bf16 v[136:139], v[164:167], v[40:43], v[136:139]\n"
      "ds_read_b128 v[40:43], v233 offset:24832\n"
      "v_mfma_f32_16x16x32_bf16 v[136:139], v[180:183], v[44:47], v[136:139]\n"
      "ds_read_b128 v[44:47], v234 offset:24832\n"
      "v_mfma_f32_16x16x32_bf16 v[136:139], v[184:187], v[60:63], v[136:139]\n"
      "LPD9:\n"
      "s_cbranch_execz LPD12\n"
      "ds_read_b128 v[60:63], v235 offset:24832\n"
      "v_cvt_pk_bf16_f32 v208, v132, v133\n"
      "v_mfma_f32_16x16x32_bf16 v[136:139], v[188:191], v[64:67], v[136:139]\n"
      "ds_read_b128 v[64:67], v232 offset:33024\n"
      "v_cvt_pk_bf16_f32 v209, v134, v135\n"
      "v_mfma_f32_16x16x32_bf16 v[136:139], v[204:207], v[68:71], v[136:139]\n"
      "ds_read_b128 v[68:71], v233 offset:33024\n"
      "v_mfma_f32_16x16x32_bf16 v[136:139], a[24:27], v[84:87], v[136:139]\n"
      "ds_read_b128 v[84:87], v234 offset:33024\n"
      "v_mfma_f32_16x16x32_bf16 v[136:139], a[28:31], v[88:91], v[136:139]\n"
      "ds_read_b128 v[88:91], v235 offset:33024\n"
      "v_mfma_f32_16x16x32_bf16 v[136:139], a[32:35], v[92:95], v[136:139]\n"
      "ds_read_b128 v[92:95], v232 offset:41216\n"
      "v_mfma_f32_16x16x32_bf16 v[136:139], a[36:39], v[108:111], v[136:139]\n"
      "ds_read_b128 v[108:111], v233 offset:41216\n"
      "ds_write_b64 v244, v[208:209] offset:12416\n"
      "v_mfma_f32_16x16x32_bf16 v[136:139], a[40:43], v[112:115], v[136:139]\n"
      "ds_read_b128 v[112:115], v234 offset:41216\n"
      "v_mfma_f32_16x16x32_bf16 v[136:139], a[44:47], v[116:119], v[136:139]\n"
      "ds_read_b128 v[116:119], v235 offset:41216\n"
      "s_waitcnt lgkmcnt(9)\n"
      "v_mfma_f32_16x16x32_bf16 v[132:135], a[240:243], v[36:39], v[0:3]\n"
      "v_add_u32 v232, 0xffff4000, v232\n"
      "v_mfma_f32_16x16x32_bf16 v[132:135], a[244:247], v[40:43], v[132:135]\n"
      "v_add_u32 v233, 0xffff4000, v233\n"
      "v_mfma_f32_16x16x32_bf16 v[132:135], a[248:251], v[44:47], v[132:135]\n"
      "v_add_u32 v234, 0xffff4000, v234\n"
      "v_mfma_f32_16x16x32_bf16 v[132:135], a[252:255], v[60:63], v[132:135]\n"
      "v_add_u32 v235, 0xffff4000, v235\n"
      "s_waitcnt lgkmcnt(5)\n"
      "v_mfma_f32_16x16x32_bf16 v[132:135], a[48:51], v[64:67], v[132:135]\n"
      "v_cvt_pk_bf16_f32 v210, v136, v137\n"
      "v_mfma_f32_16x16x32_bf16 v[132:135], a[52:55], v[68:71], v[132:135]\n"
      "v_cvt_pk_bf16_f32 v211, v138, v139\n"
      "v_mfma_f32_16x16x32_bf16 v[132:135], a[56:59], v[84:87], v[132:135]\n"
      "ds_read_b128 v[216:219], v246 offset:0\n"
      "v_mfma_f32_16x16x32_bf16 v[132:135], a[60:63], v[88:91], v[132:135]\n"
      "ds_read_b128 v[220:223], v246 offset:1024\n"
      "s_waitcnt lgkmcnt(2)\n"
      "v_mfma_f32_16x16x32_bf16 v[132:135], a[64:67], v[92:95], v[132:135]\n"
      "ds_read_b128 v[224:227], v246 offset:2048\n"
      "s_cbranch_vccnz LT10\n"
      "LS10:\n"
      "v_mfma_f32_16x16x32_bf16 v[132:135], a[68:71], v[108:111], v[132:135]\n"
      "v_lshlrev_b32 v0, 16, v48\n"
      "v_mfma_f32_16x16x32_bf16 v[132:135], a[72:75], v[112:115], v[132:135]\n"
      "ds_write_b64 v244, v[210:211] offset:12424\n"
      "v_mfma_f32_16x16x32_bf16 v[132:135], a[76:79], v[116:119], v[132:135]\n"
      "v_and_b32 v1, 0xffff0000, v48\n"
      "v_mfma_f32_16x16x32_bf16 v[136:139], a[80:83], v[36:39], v[4:7]\n"
      "v_lshlrev_b32 v2, 16, v49\n"
      "v_mfma_f32_16x16x32_bf16 v[136:139], a[84:87], v[40:43], v[136:139]\n"
      "v_and_b32 v3, 0xffff0000, v49\n"
      "v_mfma_f32_16x16x32_bf16 v[136:139], a[88:91], v[44:47], v[136:139]\n"
      "v_mfma_f32_16x16x32_bf16 v[136:139], a[92:95], v[60:63], v[136:139]\n"
      "v_cvt_pk_bf16_f32 v208, v132, v133\n"
      "v_mfma_f32_16x16x32_bf16 v[136:139], a[96:99], v[64:67], v[136:139]\n"
      "v_cvt_pk_bf16_f32 v209, v134, v135\n"
      "v_mfma_f32_16x16x32_bf16 v[136:139], a[100:103], v[68:71], v[136:139]\n"
      "s_waitcnt lgkmcnt(3)\n"
      "v_mfma_f32_16x16x32_bf16 v[136:139], a[104:107], v[84:87], v[136:139]\n"
      "global_store_dwordx4 v247, v[216:219], s[54:55] offset:768 sc1\n"
      "v_mfma_f32_16x16x32_bf16 v[136:139], a[108:111], v[88:91], v[136:139]\n"
      "s_add_u32 m0, s17, 0xd000\n"
      "v_mfma_f32_16x16x32_bf16 v[136:139], a[112:115], v[92:95], v[136:139]\n"
      "global_load_lds_dwordx4 v237, s[52:53] offset:0 nt\n"
      "v_mfma_f32_16x16x32_bf16 v[136:139], a[116:119], v[108:111], v[136:139]\n"
      "ds_write_b64 v244, v[208:209] offset:24576\n"
      "v_mfma_f32_16x16x32_bf16 v[136:139], a[120:123], v[112:115], v[136:139]\n"
      "v_lshlrev_b32 v4, 16, v50\n"
      "v_mfma_f32_16x16x32_bf16 v[136:139], a[124:127], v[116:119], v[136:139]\n"
      "v_and_b32 v5, 0xffff0000, v50\n"
      "v_mfma_f32_16x16x32_bf16 v[132:135], a[128:131], v[36:39], v[8:11]\n"
      "v_lshlrev_b32 v6, 16, v51\n"
      "v_mfma_f32_16x16x32_bf16 v[132:135], a[132:135], v[40:43], v[132:135]\n"
      "v_and_b32 v7, 0xffff0000, v51\n"
      "v_mfma_f32_16x16x32_bf16 v[132:135], a[136:139], v[44:47], v[132:135]\n"
      "v_mfma_f32_16x16x32_bf16 v[132:135], a[140:143], v[60:63], v[132:135]\n"
      "v_cvt_pk_bf16_f32 v210, v136, v137\n"
      "v_mfma_f32_16x16x32_bf16 v[132:135], a[144:147], v[64:67], v[132:135]\n"
      "v_cvt_pk_bf16_f32 v211, v138, v139\n"
      "v_mfma_f32_16x16x32_bf16 v[132:135], a[148:151], v[68:71], v[132:135]\n"
      "v_lshlrev_b32 v8, 16, v52\n"
      "v_mfma_f32_16x16x32_bf16 v[132:135], a[152:155], v[84:87], v[132:135]\n"
      "s_waitcnt lgkmcnt(3)\n"
      "v_mfma_f32_16x16x32_bf16 v[132:135], a[156:159], v[88:91], v[132:135]\n"
      "global_store_dwordx4 v252, v[220:223], s[54:55] offset:768 sc1\n"
      "v_mfma_f32_16x16x32_bf16 v[132:135], a[160:163], v[92:95], v[132:135]\n"
      "s_add_u32 m0, s17, 0xef00\n"
      "v_mfma_f32_16x16x32_bf16 v[132:135], a[164:167], v[108:111], v[132:135]\n"
      "ds_write_b64 v244, v[210:211] offset:24584\n"
      "v_mfma_f32_16x16x32_bf16 v[132:135], a[168:171], v[112:115], v[132:135]\n"
      "global_load_lds_dwordx4 v237, s[52:53] offset:256 nt\n"
      "v_mfma_f32_16x16x32_bf16 v[132:135], a[172:175], v[116:119], v[132:135]\n"
      "v_and_b32 v9, 0xffff0000, v52\n"
      "v_mfma_f32_16x16x32_bf16 v[136:139], a[176:179], v[36:39], v[12:15]\n"
      "v_lshlrev_b32 v10, 16, v53\n"
      "v_mfma_f32_16x16x32_bf16 v[136:139], a[180:183], v[40:43], v[136:139]\n"
      "v_and_b32 v11, 0xffff0000, v53\n"
      "v_mfma_f32_16x16x32_bf16 v[136:139], a[184:187], v[44:47], v[136:139]\n"
      "v_mfma_f32_16x16x32_bf16 v[136:139], a[188:191], v[60:63], v[136:139]\n"
      "v_cvt_pk_bf16_f32 v212, v132, v133\n"
      "v_mfma_f32_16x16x32_bf16 v[136:139], a[192:195], v[64:67], v[136:139]\n"
      "v_cvt_pk_bf16_f32 v213, v134, v135\n"
      "v_mfma_f32_16x16x32_bf16 v[136:139], a[196:199], v[68:71], v[136:139]\n"
      "v_lshlrev_b32 v12, 16, v54\n"
      "v_mfma_f32_16x16x32_bf16 v[136:139], a[200:203], v[84:87], v[136:139]\n"
      "v_and_b32 v13, 0xffff0000, v54\n"
      "v_mfma_f32_16x16x32_bf16 v[136:139], a[204:207], v[88:91], v[136:139]\n"
      "s_waitcnt lgkmcnt(3)\n"
      "v_mfma_f32_16x16x32_bf16 v[136:139], a[208:211], v[92:95], v[136:139]\n"
      "global_store_dwordx4 v253, v[224:227], s[54:55] offset:768 sc1\n"
      "s_cbranch_vccnz LT11\n"
      "LS11:\n"
      "v_mfma_f32_16x16x32_bf16 v[136:139], a[212:215], v[108:111], v[136:139]\n"
      "ds_write_b64 v245, v[212:213] offset:24576\n"
      "v_mfma_f32_16x16x32_bf16 v[136:139], a[216:219], v[112:115], v[136:139]\n"
      "s_add_u32 m0, s17, 0x10e00\n"
      "v_mfma_f32_16x16x32_bf16 v[136:139], a[220:223], v[116:119], v[136:139]\n"
      "global_load_lds_dwordx4 v237, s[52:53] offset:512 nt\n"
      "v_mfma_f32_16x16x32_bf16 v[132:135], a[224:227], v[36:39], v[16:19]\n"
      "v_lshlrev_b32 v14, 16, v55\n"
      "v_mfma_f32_16x16x32_bf16 v[132:135], a[228:231], v[40:43], v[132:135]\n"
      "v_and_b32 v15, 0xffff0000, v55\n"
      "v_mfma_f32_16x16x32_bf16 v[132:135], a[232:235], v[44:47], v[132:135]\n"
      "v_mfma_f32_16x16x32_bf16 v[132:135], a[236:239], v[60:63], v[132:135]\n"
      "s_memrealtime s[84:85]\n"
      "v_mfma_f32_16x16x32_bf16 v[132:135], a[0:3], v[64:67], v[132:135]\n"
      "v_cvt_pk_bf16_f32 v214, v136, v137\n"
      "v_mfma_f32_16x16x32_bf16 v[132:135], a[4:7], v[68:71], v[132:135]\n"
      "v_cvt_pk_bf16_f32 v215, v138, v139\n"
      "v_mfma_f32_16x16x32_bf16 v[132:135], a[8:11], v[84:87], v[132:135]\n"
      "v_lshlrev_b32 v16, 16, v56\n"
      "v_mfma_f32_16x16x32_bf16 v[132:135], a[12:15], v[88:91], v[132:135]\n"
      "v_and_b32 v17, 0xffff0000, v56\n"
      "v_mfma_f32_16x16x32_bf16 v[132:135], a[16:19], v[92:95], v[132:135]\n"
      "v_lshlrev_b32 v18, 16, v57\n"
      "v_mfma_f32_16x16x32_bf16 v[132:135], a[20:23], v[108:111], v[132:135]\n"
      "v_and_b32 v19, 0xffff0000, v57\n"
      "v_mfma_f32_16x16x32_bf16 v[132:135], v[140:143], v[112:115], v[132:135]\n"
      "s_waitcnt lgkmcnt(0)\n"
      "s_mov_b32 s66, s84\n"
      "v_mfma_f32_16x16x32_bf16 v[132:135], v[156:159], v[116:119], v[132:135]\n"
      "s_waitcnt vmcnt(25)\n"
      "s_barrier\n"
      "ds_write_b64 v245, v[214:215] offset:24584\n"
      "v_mfma_f32_16x16x32_bf16 v[136:139], v[160:163], v[36:39], v[20:23]\n"
      "ds_read_b128 v[36:39], v232 offset:0\n"
      "v_mfma_f32_16x16x32_bf16 v[136:139], v[164:167], v[40:43], v[136:139]\n"
      "ds_read_b128 v[40:43], v233 offset:0\n"
      "v_mfma_f32_16x16x32_bf16 v[136:139], v[180:183], v[44:47], v[136:139]\n"
      "ds_read_b128 v[44:47], v234 offset:0\n"
      "v_mfma_f32_16x16x32_bf16 v[136:139], v[184:187], v[60:63], v[136:139]\n"
      "ds_read_b128 v[60:63], v235 offset:0\n"
      "v_cvt_pk_bf16_f32 v208, v132, v133\n"
      "v_mfma_f32_16x16x32_bf16 v[136:139], v[188:191], v[64:67], v[136:139]\n"
      "ds_read_b128 v[64:67], v232 offset:8192\n"
      "v_cvt_pk_bf16_f32 v209, v134, v135\n"
      "v_mfma_f32_16x16x32_bf16 v[136:139], v[204:207], v[68:71], v[136:139]\n"
      "ds_read_b128 v[68:71], v233 offset:8192\n"
      "v_mfma_f32_16x16x32_bf16 v[136:139], a[24:27], v[84:87], v[136:139]\n"
      "ds_read_b128 v[84:87], v234 offset:8192\n"
      "v_mfma_f32_16x16x32_bf16 v[136:139], a[28:31], v[88:91], v[136:139]\n"
      "ds_read_b128 v[88:91], v235 offset:8192\n"
      "v_mfma_f32_16x16x32_bf16 v[136:139], a[32:35], v[92:95], v[136:139]\n"
      "ds_read_b128 v[92:95], v232 offset:16384\n"
      "v_mfma_f32_16x16x32_bf16 v[136:139], a[36:39], v[108:111], v[136:139]\n"
      "ds_read_b128 v[108:111], v233 offset:16384\n"
      "ds_write_b64 v244, v[208:209] offset:24704\n"
      "v_mfma_f32_16x16x32_bf16 v[136:139], a[40:43], v[112:115], v[136:139]\n"
      "ds_read_b128 v[112:115], v234 offset:16384\n"
      "v_mfma_f32_16x16x32_bf16 v[136:139], a[44:47], v[116:119], v[136:139]\n"
      "ds_read_b128 v[116:119], v235 offset:16384\n"
      "s_waitcnt lgkmcnt(9)\n"
      "v_mfma_f32_16x16x32_bf16 v[132:135], a[240:243], v[36:39], v[0:3]\n"
      "s_sub_u32 s69, s66, s68\n"
      "v_mfma_f32_16x16x32_bf16 v[132:135], a[244:247], v[40:43], v[132:135]\n"
      "s_mov_b32 s68, s67\n"
      "v_mfma_f32_16x16x32_bf16 v[132:135], a[248:251], v[44:47], v[132:135]\n"
      "s_mov_b32 s67, s66\n"
      "v_mfma_f32_16x16x32_bf16 v[132:135], a[252:255], v[60:63], v[132:135]\n"
      "s_cmp_gt_i32 s69, 460\n"
      "s_cselect_b64 s[70:71], -1, s[70:71]\n"
      "s_waitcnt lgkmcnt(5)\n"
      "v_mfma_f32_16x16x32_bf16 v[132:135], a[48:51], v[64:67], v[132:135]\n"
      "s_or_b64 vcc, vcc, s[70:71]\n"
      "v_cvt_pk_bf16_f32 v211, v138, v139\n"
      "v_mfma_f32_16x16x32_bf16 v[132:135], a[52:55], v[68:71], v[132:135]\n"
      "v_lshlrev_b32 v20, 16, v58\n"
      "v_mfma_f32_16x16x32_bf16 v[132:135], a[56:59], v[84:87], v[132:135]\n"
      "v_and_b32 v21, 0xffff0000, v58\n"
      "v_mfma_f32_16x16x32_bf16 v[132:135], a[60:63], v[88:91], v[132:135]\n"
      "v_lshlrev_b32 v22, 16, v59\n"
      "s_waitcnt lgkmcnt(0)\n"
      "v_mfma_f32_16x16x32_bf16 v[132:135], a[64:67], v[92:95], v[132:135]\n"
      "v_and_b32 v23, 0xffff0000, v59\n"
      "s_cbranch_vccnz LT12\n"
      "LS12:\n"
      "v_mfma_f32_16x16x32_bf16 v[132:135], a[68:71], v[108:111], v[132:135]\n"
      "v_cvt_pk_bf16_f32 v210, v136, v137\n"
      "v_mfma_f32_16x16x32_bf16 v[132:135], a[72:75], v[112:115], v[132:135]\n"
      "s_add_u32 s54, s44, s33\n"
      "s_addc_u32 s55, s45, 0\n"
      "v_mfma_f32_16x16x32_bf16 v[132:135], a[76:79], v[116:119], v[132:135]\n"
      "ds_read_b128 v[216:219], v246 offset:12288\n"
      "v_mfma_f32_16x16x32_bf16 v[136:139], a[80:83], v[36:39], v[4:7]\n"
      "ds_read_b128 v[220:223], v246 offset:13312\n"
      "v_mfma_f32_16x16x32_bf16 v[136:139], a[84:87], v[40:43], v[136:139]\n"
      "ds_read_b128 v[224:227], v246 offset:14336\n"
      "v_mfma_f32_16x16x32_bf16 v[136:139], a[88:91], v[44:47], v[136:139]\n"
      "ds_write_b64 v244, v[210:211] offset:24712\n"
      "v_mfma_f32_16x16x32_bf16 v[136:139], a[92:95], v[60:63], v[136:139]\n"
      "v_cvt_pk_bf16_f32 v208, v132, v133\n"
      "v_mfma_f32_16x16x32_bf16 v[136:139], a[96:99], v[64:67], v[136:139]\n"
      "v_cvt_pk_bf16_f32 v209, v134, v135\n"
      "v_mfma_f32_16x16x32_bf16 v[136:139], a[100:103], v[68:71], v[136:139]\n"
      "s_waitcnt lgkmcnt(3)\n"
      "v_mfma_f32_16x16x32_bf16 v[136:139], a[104:107], v[84:87], v[136:139]\n"
      "global_store_dwordx4 v247, v[216:219], s[54:55] offset:0 sc1\n"
      "v_mfma_f32_16x16x32_bf16 v[136:139], a[108:111], v[88:91], v[136:139]\n"
      "s_add_u32 s52, s42, s35\n"
      "s_addc_u32 s53, s43, 0\n"
      "v_mfma_f32_16x16x32_bf16 v[136:139], a[112:115], v[92:95], v[136:139]\n"
      "s_add_u32 m0, s17, 0x12000\n"
      "v_mfma_f32_16x16x32_bf16 v[136:139], a[116:119], v[108:111], v[136:139]\n"
      "ds_write_b64 v244, v[208:209] offset:0\n"
      "v_mfma_f32_16x16x32_bf16 v[136:139], a[120:123], v[112:115], v[136:139]\n"
      "global_load_lds_dwordx4 v236, s[52:53] offset:0 nt\n"
      "v_mfma_f32_16x16x32_bf16 v[136:139], a[124:127], v[116:119], v[136:139]\n"
      "v_mfma_f32_16x16x32_bf16 v[132:135], a[128:131], v[36:39], v[8:11]\n"
      "v_mfma_f32_16x16x32_bf16 v[132:135], a[132:135], v[40:43], v[132:135]\n"
      "v_mfma_f32_16x16x32_bf16 v[132:135], a[136:139], v[44:47], v[132:135]\n"
      "v_mfma_f32_16x16x32_bf16 v[132:135], a[140:143], v[60:63], v[132:135]\n"
      "v_cvt_pk_bf16_f32 v210, v136, v137\n"
      "v_mfma_f32_16x16x32_bf16 v[132:135], a[144:147], v[64:67], v[132:135]\n"
      "v_cvt_pk_bf16_f32 v211, v138, v139\n"
      "v_mfma_f32_16x16x32_bf16 v[132:135], a[148:151], v[68:71], v[132:135]\n"
      "v_mfma_f32_16x16x32_bf16 v[132:135], a[152:155], v[84:87], v[132:135]\n"
      "s_waitcnt lgkmcnt(3)\n"
      "v_mfma_f32_16x16x32_bf16 v[132:135], a[156:159], v[88:91], v[132:135]\n"
      "global_store_dwordx4 v252, v[220:223], s[54:55] offset:0 sc1\n"
      "v_mfma_f32_16x16x32_bf16 v[132:135], a[160:163], v[92:95], v[132:135]\n"
      "s_add_u32 m0, s17, 0x13f00\n"
      "v_mfma_f32_16x16x32_bf16 v[132:135], a[164:167], v[108:111], v[132:135]\n"
      "ds_write_b64 v244, v[210:211] offset:8\n"
      "v_mfma_f32_16x16x32_bf16 v[132:135], a[168:171], v[112:115], v[132:135]\n"
      "global_load_lds_dwordx4 v236, s[52:53] offset:256 nt\n"
      "v_mfma_f32_16x16x32_bf16 v[132:135], a[172:175], v[116:119], v[132:135]\n"
      "v_mfma_f32_16x16x32_bf16 v[136:139], a[176:179], v[36:39], v[12:15]\n"
      "v_mfma_f32_16x16x32_bf16 v[136:139], a[180:183], v[40:43], v[136:139]\n"
      "v_mfma_f32_16x16x32_bf16 v[136:139], a[184:187], v[44:47], v[136:139]\n"
      "v_mfma_f32_16x16x32_bf16 v[136:139], a[188:191], v[60:63], v[136:139]\n"
      "v_cvt_pk_bf16_f32 v212, v132, v133\n"
      "v_mfma_f32_16x16x32_bf16 v[136:139], a[192:195], v[64:67], v[136:139]\n"
      "v_cvt_pk_bf16_f32 v213, v134, v135\n"
      "v_mfma_f32_16x16x32_bf16 v[136:139], a[196:199], v[68:71], v[136:139]\n"
      "v_mfma_f32_16x16x32_bf16 v[136:139], a[200:203], v[84:87], v[136:139]\n"
      "v_mfma_f32_16x16x32_bf16 v[136:139], a[204:207], v[88:91], v[136:139]\n"
      "s_waitcnt lgkmcnt(3)\n"
      "v_mfma_f32_16x16x32_bf16 v[136:139], a[208:211], v[92:95], v[136:139]\n"
      "global_store_dwordx4 v253, v[224:227], s[54:55] offset:0 sc1\n"
      "s_cbranch_vccnz LT13\n"
      "LS13:\n"
      "v_mfma_f32_16x16x32_bf16 v[136:139], a[212:215], v[108:111], v[136:139]\n"
      "ds_write_b64 v245, v[212:213] offset:0\n"
      "v_mfma_f32_16x16x32_bf16 v[136:139], a[216:219], v[112:115], v[136:139]\n"
      "s_add_u32 m0, s17, 0x15e00\n"
      "v_mfma_f32_16x16x32_bf16 v[136:139], a[220:223], v[116:119], v[136:139]\n"
      "global_load_lds_dwordx4 v236, s[52:53] offset:512 nt\n"
      "v_mfma_f32_16x16x32_bf16 v[132:135], a[224:227], v[36:39], v[16:19]\n"
      "v_mfma_f32_16x16x32_bf16 v[132:135], a[228:231], v[40:43], v[132:135]\n"
      "v_mfma_f32_16x16x32_bf16 v[132:135], a[232:235], v[44:47], v[132:135]\n"
      "v_mfma_f32_16x16x32_bf16 v[132:135], a[236:239], v[60:63], v[132:135]\n"
      "v_cvt_pk_bf16_f32 v214, v136, v137\n"
      "v_mfma_f32_16x16x32_bf16 v[132:135], a[0:3], v[64:67], v[132:135]\n"
      "v_cvt_pk_bf16_f32 v215, v138, v139\n"
      "v_mfma_f32_16x16x32_bf16 v[132:135], a[4:7], v[68:71], v[132:135]\n"
      "v_mfma_f32_16x16x32_bf16 v[132:135], a[8:11], v[84:87], v[132:135]\n"
      "v_mfma_f32_16x16x32_bf16 v[132:135], a[12:15], v[88:91], v[132:135]\n"
      "v_mfma_f32_16x16x32_bf16 v[132:135], a[16:19], v[92:95], v[132:135]\n"
      "v_mfma_f32_16x16x32_bf16 v[132:135], a[20:23], v[108:111], v[132:135]\n"
      "ds_write_b64 v245, v[214:215] offset:8\n"
      "v_mfma_f32_16x16x32_bf16 v[132:135], v[140:143], v[112:115], v[132:135]\n"
      "v_mfma_f32_16x16x32_bf16 v[132:135], v[156:159], v[116:119], v[132:135]\n"
      "s_waitcnt lgkmcnt(4)\n"
      "s_barrier\n"
      "v_mfma_f32_16x16x32_bf16 v[136:139], v[160:163], v[36:39], v[20:23]\n"
      "ds_read_b128 v[36:39], v232 offset:256\n"
      "v_mfma_f32_16x16x32_bf16 v[136:139], v[164:167], v[40:43], v[136:139]\n"
      "ds_read_b128 v[40:43], v233 offset:256\n"
      "v_mfma_f32_16x16x32_bf16 v[136:139], v[180:183], v[44:47], v[136:139]\n"
      "ds_read_b128 v[44:47], v234 offset:256\n"
      "v_mfma_f32_16x16x32_bf16 v[136:139], v[184:187], v[60:63], v[136:139]\n"
      "ds_read_b128 v[60:63], v235 offset:256\n"
      "v_cvt_pk_bf16_f32 v208, v132, v133\n"
      "v_mfma_f32_16x16x32_bf16 v[136:139], v[188:191], v[64:67], v[136:139]\n"
      "ds_read_b128 v[64:67], v232 offset:8448\n"
      "v_cvt_pk_bf16_f32 v209, v134, v135\n"
      "v_mfma_f32_16x16x32_bf16 v[136:139], v[204:207], v[68:71], v[136:139]\n"
      "ds_read_b128 v[68:71], v233 offset:8448\n"
      "v_mfma_f32_16x16x32_bf16 v[136:139], a[24:27], v[84:87], v[136:139]\n"
      "ds_read_b128 v[84:87], v234 offset:8448\n"
      "v_mfma_f32_16x16x32_bf16 v[136:139], a[28:31], v[88:91], v[136:139]\n"
      "ds_read_b128 v[88:91], v235 offset:8448\n"
      "v_mfma_f32_16x16x32_bf16 v[136:139], a[32:35], v[92:95], v[136:139]\n"
      "ds_read_b128 v[92:95], v232 offset:16640\n"
      "v_mfma_f32_16x16x32_bf16 v[136:139], a[36:39], v[108:111], v[136:139]\n"
      "ds_read_b128 v[108:111], v233 offset:16640\n"
      "v_mfma_f32_16x16x32_bf16 v[136:139], a[40:43], v[112:115], v[136:139]\n"
      "ds_read_b128 v[112:115], v234 offset:16640\n"
      "v_mfma_f32_16x16x32_bf16 v[136:139], a[44:47], v[116:119], v[136:139]\n"
      "ds_read_b128 v[116:119], v235 offset:16640\n"
      "s_waitcnt lgkmcnt(8)\n"
      "v_mfma_f32_16x16x32_bf16 v[132:135], a[240:243], v[36:39], v[0:3]\n"
      "ds_write_b64 v244, v[208:209] offset:128\n"
      "v_mfma_f32_16x16x32_bf16 v[132:135], a[244:247], v[40:43], v[132:135]\n"
      "v_mfma_f32_16x16x32_bf16 v[132:135], a[248:251], v[44:47], v[132:135]\n"
      "v_mfma_f32_16x16x32_bf16 v[132:135], a[252:255], v[60:63], v[132:135]\n"
      "v_cvt_pk_bf16_f32 v210, v136, v137\n"
      "s_waitcnt lgkmcnt(5)\n"
      "v_mfma_f32_16x16x32_bf16 v[132:135], a[48:51], v[64:67], v[132:135]\n"
      "v_cvt_pk_bf16_f32 v211, v138, v139\n"
      "v_mfma_f32_16x16x32_bf16 v[132:135], a[52:55], v[68:71], v[132:135]\n"
      "v_mfma_f32_16x16x32_bf16 v[132:135], a[56:59], v[84:87], v[132:135]\n"
      "ds_read_b128 v[216:219], v246 offset:24576\n"
      "v_mfma_f32_16x16x32_bf16 v[132:135], a[60:63], v[88:91], v[132:135]\n"
      "ds_read_b128 v[220:223], v246 offset:25600\n"
      "s_waitcnt lgkmcnt(3)\n"
      "v_mfma_f32_16x16x32_bf16 v[132:135], a[64:67], v[92:95], v[132:135]\n"
      "ds_read_b128 v[224:227], v246 offset:26624\n"
      "v_mfma_f32_16x16x32_bf16 v[132:135], a[68:71], v[108:111], v[132:135]\n"
      "ds_write_b64 v244, v[210:211] offset:136\n"
      "v_mfma_f32_16x16x32_bf16 v[132:135], a[72:75], v[112:115], v[132:135]\n"
      "s_cbranch_vccnz LT14\n"
      "LS14:\n"
      "v_mfma_f32_16x16x32_bf16 v[132:135], a[76:79], v[116:119], v[132:135]\n"
      "v_mfma_f32_16x16x32_bf16 v[136:139], a[80:83], v[36:39], v[4:7]\n"
      "v_mfma_f32_16x16x32_bf16 v[136:139], a[84:87], v[40:43], v[136:139]\n"
      "v_mfma_f32_16x16x32_bf16 v[136:139], a[88:91], v[44:47], v[136:139]\n"
      "v_mfma_f32_16x16x32_bf16 v[136:139], a[92:95], v[60:63], v[136:139]\n"
      "v_cvt_pk_bf16_f32 v208, v132, v133\n"
      "v_mfma_f32_16x16x32_bf16 v[136:139], a[96:99], v[64:67], v[136:139]\n"
      "v_cvt_pk_bf16_f32 v209, v134, v135\n"
      "v_mfma_f32_16x16x32_bf16 v[136:139], a[100:103], v[68:71], v[136:139]\n"
      "s_waitcnt lgkmcnt(3)\n"
      "v_mfma_f32_16x16x32_bf16 v[136:139], a[104:107], v[84:87], v[136:139]\n"
      "global_store_dwordx4 v247, v[216:219], s[54:55] offset:768 sc1\n"
      "v_mfma_f32_16x16x32_bf16 v[136:139], a[108:111], v[88:91], v[136:139]\n"
      "s_add_u32 m0, s17, 0x13000\n"
      "v_mfma_f32_16x16x32_bf16 v[136:139], a[112:115], v[92:95], v[136:139]\n"
      "global_load_lds_dwordx4 v237, s[52:53] offset:0 nt\n"
      "v_mfma_f32_16x16x32_bf16 v[136:139], a[116:119], v[108:111], v[136:139]\n"
      "ds_write_b64 v244, v[208:209] offset:12288\n"
      "v_mfma_f32_16x16x32_bf16 v[136:139], a[120:123], v[112:115], v[136:139]\n"
      "v_mfma_f32_16x16x32_bf16 v[136:139], a[124:127], v[116:119], v[136:139]\n"
      "v_mfma_f32_16x16x32_bf16 v[132:135], a[128:131], v[36:39], v[8:11]\n"
      "v_mfma_f32_16x16x32_bf16 v[132:135], a[132:135], v[40:43], v[132:135]\n"
      "v_mfma_f32_16x16x32_bf16 v[132:135], a[136:139], v[44:47], v[132:135]\n"
      "v_mfma_f32_16x16x32_bf16 v[132:135], a[140:143], v[60:63], v[132:135]\n"
      "v_cvt_pk_bf16_f32 v210, v136, v137\n"
      "v_mfma_f32_16x16x32_bf16 v[132:135], a[144:147], v[64:67], v[132:135]\n"
      "v_cvt_pk_bf16_f32 v211, v138, v139\n"
      "v_mfma_f32_16x16x32_bf16 v[132:135], a[148:151], v[68:71], v[132:135]\n"
      "v_mfma_f32_16x16x32_bf16 v[132:135], a[152:155], v[84:87], v[132:135]\n"
      "s_waitcnt lgkmcnt(3)\n"
      "s_waitcnt vmcnt(18)\n"
      "s_mov_b32 s87, 1\n"
      "s_or_b32 s56, s51, s76\n"
      "s_cmp_lg_u32 s56, 0\n"
      "s_cbranch_scc1 LG1\n"
      "v_cmp_ne_u64 s[58:59], s[80:81], v[24:25]\n"
      "v_cmp_ne_u64 s[56:57], s[80:81], v[26:27]\n"
      "s_mov_b32 s87, 0\n"
      "s_nop 3\n"
      "s_or_b64 s[56:57], s[56:57], s[58:59]\n"
      "s_cbranch_scc1 LG1\n"
      "v_mov_b32 v28, 0\n"
      "v_mov_b32 v30, s80\n"
      "v_mov_b32 v31, s81\n"
      "s_mov_b32 s87, 1\n"
      "s_mov_b64 exec, 1\n"
      "global_store_dwordx2 v28, v[30:31], s[28:29] sc1\n"
      "s_mov_b64 exec, -1\n"
      "s_nop 4\n"
      "LG1:\n"
      "v_mfma_f32_16x16x32_bf16 v[132:135], a[156:159], v[88:91], v[132:135]\n"
      "global_store_dwordx4 v252, v[220:223], s[54:55] offset:768 sc1\n"
      "v_mfma_f32_16x16x32_bf16 v[132:135], a[160:163], v[92:95], v[132:135]\n"
      "s_add_u32 m0, s17, 0x14f00\n"
      "v_mfma_f32_16x16x32_bf16 v[132:135], a[164:167], v[108:111], v[132:135]\n"
      "ds_write_b64 v244, v[210:211] offset:12296\n"
      "v_mfma_f32_16x16x32_bf16 v[132:135], a[168:171], v[112:115], v[132:135]\n"
      "global_load_lds_dwordx4 v237, s[52:53] offset:256 nt\n"
      "v_mfma_f32_16x16x32_bf16 v[132:135], a[172:175], v[116:119], v[132:135]\n"
      "v_mfma_f32_16x16x32_bf16 v[136:139], a[176:179], v[36:39], v[12:15]\n"
      "v_mfma_f32_16x16x32_bf16 v[136:139], a[180:183], v[40:43], v[136:139]\n"
      "v_mfma_f32_16x16x32_bf16 v[136:139], a[184:187], v[44:47], v[136:139]\n"
      "v_mfma_f32_16x16x32_bf16 v[136:139], a[188:191], v[60:63], v[136:139]\n"
      "v_cvt_pk_bf16_f32 v212, v132, v133\n"
      "v_mfma_f32_16x16x32_bf16 v[136:139], a[192:195], v[64:67], v[136:139]\n"
      "v_cvt_pk_bf16_f32 v213, v134, v135\n"
      "v_mfma_f32_16x16x32_bf16 v[136:139], a[196:199], v[68:71], v[136:139]\n"
      "v_mfma_f32_16x16x32_bf16 v[136:139], a[200:203], v[84:87], v[136:139]\n"
      "v_mfma_f32_16x16x32_bf16 v[136:139], a[204:207], v[88:91], v[136:139]\n"
      "s_waitcnt lgkmcnt(3)\n"
      "v_mfma_f32_16x16x32_bf16 v[136:139], a[208:211], v[92:95], v[136:139]\n"
      "global_store_dwordx4 v253, v[224:227], s[54:55] offset:768 sc1\n"
      "s_cbranch_vccnz LT15\n"
      "LS15:\n"
      "v_mfma_f32_16x16x32_bf16 v[136:139], a[212:215], v[108:111], v[136:139]\n"
      "ds_write_b64 v245, v[212:213] offset:12288\n"
      "v_mfma_f32_16x16x32_bf16 v[136:139], a[216:219], v[112:115], v[136:139]\n"
      "s_add_u32 m0, s17, 0x16e00\n"
      "v_mfma_f32_16x16x32_bf16 v[136:139], a[220:223], v[116:119], v[136:139]\n"
      "global_load_lds_dwordx4 v237, s[52:53] offset:512 nt\n"
      "v_mfma_f32_16x16x32_bf16 v[132:135], a[224:227], v[36:39], v[16:19]\n"
      "v_mfma_f32_16x16x32_bf16 v[132:135], a[228:231], v[40:43], v[132:135]\n"
      "v_mfma_f32_16x16x32_bf16 v[132:135], a[232:235], v[44:47], v[132:135]\n"
      "v_mfma_f32_16x16x32_bf16 v[132:135], a[236:239], v[60:63], v[132:135]\n"
      "s_memrealtime s[84:85]\n"
      "v_mfma_f32_16x16x32_bf16 v[132:135], a[0:3], v[64:67], v[132:135]\n"
      "v_cvt_pk_bf16_f32 v214, v136, v137\n"
      "v_mfma_f32_16x16x32_bf16 v[132:135], a[4:7], v[68:71], v[132:135]\n"
      "v_cvt_pk_bf16_f32 v215, v138, v139\n"
      "v_mfma_f32_16x16x32_bf16 v[132:135], a[8:11], v[84:87], v[132:135]\n"
      "v_mfma_f32_16x16x32_bf16 v[132:135], a[12:15], v[88:91], v[132:135]\n"
      "v_mfma_f32_16x16x32_bf16 v[132:135], a[16:19], v[92:95], v[132:135]\n"
      "v_mfma_f32_16x16x32_bf16 v[132:135], a[20:23], v[108:111], v[132:135]\n"
      "v_mfma_f32_16x16x32_bf16 v[132:135], v[140:143], v[112:115], v[132:135]\n"
      "s_waitcnt lgkmcnt(0)\n"
      "s_mov_b32 s66, s84\n"
      "s_sub_u32 s48, s84, s48\n"
      "v_mfma_f32_16x16x32_bf16 v[132:135], v[156:159], v[116:119], v[132:135]\n"
      "s_barrier\n"
      "ds_write_b64 v245, v[214:215] offset:12296\n"
      "v_mfma_f32_16x16x32_bf16 v[136:139], v[160:163], v[36:39], v[20:23]\n"
      "ds_read_b128 v[36:39], v232 offset:24576\n"
      "s_lshl_b32 s79, s91, 1\n"
      "v_mfma_f32_16x16x32_bf16 v[136:139], v[164:167], v[40:43], v[136:139]\n"
      "ds_read_b128 v[40:43], v233 offset:24576\n"
      "v_mad_i32_i24 v252, v251, s79, v252\n"
      "v_mfma_f32_16x16x32_bf16 v[136:139], v[180:183], v[44:47], v[136:139]\n"
      "ds_read_b128 v[44:47], v234 offset:24576\n"
      "s_mul_i32 s79, s79, s100\n"
      "v_mfma_f32_16x16x32_bf16 v[136:139], v[184:187], v[60:63], v[136:139]\n"
      "ds_read_b128 v[60:63], v235 offset:24576\n"
      "v_cvt_pk_bf16_f32 v208, v132, v133\n"
      "v_mfma_f32_16x16x32_bf16 v[136:139], v[188:191], v[64:67], v[136:139]\n"
      "ds_read_b128 v[64:67], v232 offset:32768\n"
      "v_cvt_pk_bf16_f32 v209, v134, v135\n"
      "v_mfma_f32_16x16x32_bf16 v[136:139], v[204:207], v[68:71], v[136:139]\n"
      "ds_read_b128 v[68:71], v233 offset:32768\n"
      "v_add_u32 v247, s79, v247\n"
      "v_mfma_f32_16x16x32_bf16 v[136:139], a[24:27], v[84:87], v[136:139]\n"
      "ds_read_b128 v[84:87], v234 offset:32768\n"
      "s_mul_i32 s79, s79, s101\n"
      "v_mfma_f32_16x16x32_bf16 v[136:139], a[28:31], v[88:91], v[136:139]\n"
      "ds_read_b128 v[88:91], v235 offset:32768\n"
      "v_add_u32 v253, s79, v253\n"
      "v_mfma_f32_16x16x32_bf16 v[136:139], a[32:35], v[92:95], v[136:139]\n"
      "ds_read_b128 v[92:95], v232 offset:40960\n"
      "v_mfma_f32_16x16x32_bf16 v[136:139], a[36:39], v[108:111], v[136:139]\n"
      "ds_read_b128 v[108:111], v233 offset:40960\n"
      "ds_write_b64 v244, v[208:209] offset:12416\n"
      "v_mfma_f32_16x16x32_bf16 v[136:139], a[40:43], v[112:115], v[136:139]\n"
      "ds_read_b128 v[112:115], v234 offset:40960\n"
      "v_mfma_f32_16x16x32_bf16 v[136:139], a[44:47], v[116:119], v[136:139]\n"
      "ds_read_b128 v[116:119], v235 offset:40960\n"
      "s_waitcnt lgkmcnt(9)\n"
      "v_mfma_f32_16x16x32_bf16 v[132:135], a[240:243], v[36:39], v[0:3]\n"
      "s_sub_u32 s69, s66, s68\n"
      "v_mfma_f32_16x16x32_bf16 v[132:135], a[244:247], v[40:43], v[132:135]\n"
      "s_mov_b32 s68, s67\n"
      "v_mfma_f32_16x16x32_bf16 v[132:135], a[248:251], v[44:47], v[132:135]\n"
      "s_mul_i32 s48, s48, 939\n"
      "s_mov_b32 s67, s66\n"
      "v_mfma_f32_16x16x32_bf16 v[132:135], a[252:255], v[60:63], v[132:135]\n"
      "s_lshr_b32 s48, s48, 8\n"
      "s_cmp_gt_i32 s69, 460\n"
      "s_cselect_b64 s[70:71], -1, s[70:71]\n"
      "s_waitcnt lgkmcnt(5)\n"
      "v_mfma_f32_16x16x32_bf16 v[132:135], a[48:51], v[64:67], v[132:135]\n"
      "s_add_u32 s48, s48, s84\n"
      "s_or_b64 vcc, vcc, s[70:71]\n"
      "s_mul_i32 s78, s93, s19\n"
      "v_mfma_f32_16x16x32_bf16 v[132:135], a[52:55], v[68:71], v[132:135]\n"
      "s_xor_b32 s49, s48, s80\n"
      "v_cvt_pk_bf16_f32 v210, v136, v137\n"
      "v_mfma_f32_16x16x32_bf16 v[132:135], a[56:59], v[84:87], v[132:135]\n"
      "v_mov_b32 v230, s49\n"
      "v_cvt_pk_bf16_f32 v211, v138, v139\n"
      "v_mfma_f32_16x16x32_bf16 v[132:135], a[60:63], v[88:91], v[132:135]\n"
      "s_mul_i32 s49, s51, 0x1100\n"
      "v_add_u32 v236, s78, v236\n"
      "s_waitcnt lgkmcnt(0)\n"
      "v_mfma_f32_16x16x32_bf16 v[132:135], a[64:67], v[92:95], v[132:135]\n"
      "s_and_b32 s49, s49, 0xf000\n"
      "s_mul_i32 s78, s78, s99\n"
      "v_mfma_f32_16x16x32_bf16 v[132:135], a[68:71], v[108:111], v[132:135]\n"
      "s_lshr_b32 s49, s49, 0\n"
      "v_add_u32 v237, s78, v237\n"
      "v_mfma_f32_16x16x32_bf16 v[132:135], a[72:75], v[112:115], v[132:135]\n"
      "s_add_u32 s49, s49, 1114112\n"
      "s_cbranch_vccnz LT16\n"
      "LS16:\n"
      "v_mfma_f32_16x16x32_bf16 v[132:135], a[76:79], v[116:119], v[132:135]\n"
      "v_mov_b32 v229, s49\n"
      "ds_write_b64 v244, v[210:211] offset:12424\n"
      "v_mfma_f32_16x16x32_bf16 v[136:139], a[80:83], v[36:39], v[4:7]\n"
      "s_and_b32 s49, s51, 0x78\n"
      "v_mfma_f32_16x16x32_bf16 v[136:139], a[84:87], v[40:43], v[136:139]\n"
      "s_xor_b32 s49, s49, 0x40\n"
      "v_mfma_f32_16x16x32_bf16 v[136:139], a[88:91], v[44:47], v[136:139]\n"
      "s_or_b32 s49, s49, s76\n"
      "v_mfma_f32_16x16x32_bf16 v[136:139], a[92:95], v[60:63], v[136:139]\n"
      "v_cvt_pk_bf16_f32 v208, v132, v133\n"
      "v_mfma_f32_16x16x32_bf16 v[136:139], a[96:99], v[64:67], v[136:139]\n"
      "s_cmp_lg_u32 s49, 0\n"
      "s_cbranch_scc0 LPS\n"
      "LPR:\n"
      "v_mfma_f32_16x16x32_bf16 v[136:139], a[100:103], v[68:71], v[136:139]\n"
      "v_cvt_pk_bf16_f32 v209, v134, v135\n"
      "v_mfma_f32_16x16x32_bf16 v[136:139], a[104:107], v[84:87], v[136:139]\n"
      "s_add_u32 s54, s30, s34\n"
      "s_addc_u32 s55, s31, 0\n"
      "v_mfma_f32_16x16x32_bf16 v[136:139], a[108:111], v[88:91], v[136:139]\n"
      "ds_read_b128 v[216:219], v246 offset:0\n"
      "v_mfma_f32_16x16x32_bf16 v[136:139], a[112:115], v[92:95], v[136:139]\n"
      "ds_read_b128 v[220:223], v246 offset:1024\n"
      "v_mfma_f32_16x16x32_bf16 v[136:139], a[116:119], v[108:111], v[136:139]\n"
      "ds_read_b128 v[224:227], v246 offset:2048\n"
      "v_mfma_f32_16x16x32_bf16 v[136:139], a[120:123], v[112:115], v[136:139]\n"
      "ds_write_b64 v244, v[208:209] offset:24576\n"
      "v_mfma_f32_16x16x32_bf16 v[136:139], a[124:127], v[116:119], v[136:139]\n"
      "s_waitcnt lgkmcnt(3)\n"
      "v_mfma_f32_16x16x32_bf16 v[132:135], a[128:131], v[36:39], v[8:11]\n"
      "global_store_dwordx4 v247, v[216:219], s[54:55] offset:0 sc1\n"
      "v_mfma_f32_16x16x32_bf16 v[132:135], a[132:135], v[40:43], v[132:135]\n"
      "s_add_u32 s52, s20, s36\n"
      "s_addc_u32 s53, s21, 0\n"
      "v_mfma_f32_16x16x32_bf16 v[132:135], a[136:139], v[44:47], v[132:135]\n"
      "s_add_u32 m0, s17, 0x0\n"
      "v_mfma_f32_16x16x32_bf16 v[132:135], a[140:143], v[60:63], v[132:135]\n"
      "v_cvt_pk_bf16_f32 v210, v136, v137\n"
      "v_mfma_f32_16x16x32_bf16 v[132:135], a[144:147], v[64:67], v[132:135]\n"
      "v_cvt_pk_bf16_f32 v211, v138, v139\n"
      "v_mfma_f32_16x16x32_bf16 v[132:135], a[148:151], v[68:71], v[132:135]\n"
      "global_load_lds_dwordx4 v236, s[52:53] offset:0 nt\n"
      "v_mfma_f32_16x16x32_bf16 v[132:135], a[152:155], v[84:87], v[132:135]\n"
      "s_waitcnt lgkmcnt(2)\n"
      "v_mfma_f32_16x16x32_bf16 v[132:135], a[156:159], v[88:91], v[132:135]\n"
      "global_store_dwordx4 v252, v[220:223], s[54:55] offset:0 sc1\n"
      "v_mfma_f32_16x16x32_bf16 v[132:135], a[160:163], v[92:95], v[132:135]\n"
      "v_mfma_f32_16x16x32_bf16 v[132:135], a[164:167], v[108:111], v[132:135]\n"
      "ds_write_b64 v244, v[210:211] offset:24584\n"
      "v_mfma_f32_16x16x32_bf16 v[132:135], a[168:171], v[112:115], v[132:135]\n"
      "v_mfma_f32_16x16x32_bf16 v[132:135], a[172:175], v[116:119], v[132:135]\n"
      "v_mfma_f32_16x16x32_bf16 v[136:139], a[176:179], v[36:39], v[12:15]\n"
      "v_mfma_f32_16x16x32_bf16 v[136:139], a[180:183], v[40:43], v[136:139]\n"
      "v_mfma_f32_16x16x32_bf16 v[136:139], a[184:187], v[44:47], v[136:139]\n"
      "v_mfma_f32_16x16x32_bf16 v[136:139], a[188:191], v[60:63], v[136:139]\n"
      "v_cvt_pk_bf16_f32 v212, v132, v133\n"
      "v_mfma_f32_16x16x32_bf16 v[136:139], a[192:195], v[64:67], v[136:139]\n"
      "v_cvt_pk_bf16_f32 v213, v134, v135\n"
      "v_mfma_f32_16x16x32_bf16 v[136:139], a[196:199], v[68:71], v[136:139]\n"
      "s_add_u32 m0, s17, 0x1f00\n"
      "v_mfma_f32_16x16x32_bf16 v[136:139], a[200:203], v[84:87], v[136:139]\n"
      "global_load_lds_dwordx4 v236, s[52:53] offset:256 nt\n"
      "v_mfma_f32_16x16x32_bf16 v[136:139], a[204:207], v[88:91], v[136:139]\n"
      "s_waitcnt lgkmcnt(2)\n"
      "v_mfma_f32_16x16x32_bf16 v[136:139], a[208:211], v[92:95], v[136:139]\n"
      "global_store_dwordx4 v253, v[224:227], s[54:55] offset:0 sc1\n"
      "v_mfma_f32_16x16x32_bf16 v[136:139], a[212:215], v[108:111], v[136:139]\n"
      "ds_write_b64 v245, v[212:213] offset:24576\n"
      "LPD10:\n"
      "s_cbranch_execz LPD13\n"
      "v_mfma_f32_16x16x32_bf16 v[136:139], a[216:219], v[112:115], v[136:139]\n"
      "s_cbranch_vccnz LT17\n"
      "LS17:\n"
      "v_mfma_f32_16x16x32_bf16 v[136:139], a[220:223], v[116:119], v[136:139]\n"
      "v_mfma_f32_16x16x32_bf16 v[132:135], a[224:227], v[36:39], v[16:19]\n"
      "v_mfma_f32_16x16x32_bf16 v[132:135], a[228:231], v[40:43], v[132:135]\n"
      "v_mfma_f32_16x16x32_bf16 v[132:135], a[232:235], v[44:47], v[132:135]\n"
      "v_mfma_f32_16x16x32_bf16 v[132:135], a[236:239], v[60:63], v[132:135]\n"
      "v_cvt_pk_bf16_f32 v214, v136, v137\n"
      "v_mfma_f32_16x16x32_bf16 v[132:135], a[0:3], v[64:67], v[132:135]\n"
      "v_cvt_pk_bf16_f32 v215, v138, v139\n"
      "v_mfma_f32_16x16x32_bf16 v[132:135], a[4:7], v[68:71], v[132:135]\n"
      "s_add_u32 m0, s17, 0x3e00\n"
      "v_mfma_f32_16x16x32_bf16 v[132:135], a[8:11], v[84:87], v[132:135]\n"
      "global_load_lds_dwordx4 v236, s[52:53] offset:512 nt\n"
      "v_mfma_f32_16x16x32_bf16 v[132:135], a[12:15], v[88:91], v[132:135]\n"
      "v_mfma_f32_16x16x32_bf16 v[132:135], a[16:19], v[92:95], v[132:135]\n"
      "v_mfma_f32_16x16x32_bf16 v[132:135], a[20:23], v[108:111], v[132:135]\n"
      "ds_write_b64 v245, v[214:215] offset:24584\n"
      "v_mfma_f32_16x16x32_bf16 v[132:135], v[140:143], v[112:115], v[132:135]\n"
      "v_mfma_f32_16x16x32_bf16 v[132:135], v[156:159], v[116:119], v[132:135]\n"
      "s_barrier\n"
      "v_mfma_f32_16x16x32_bf16 v[136:139], v[160:163], v[36:39], v[20:23]\n"
      "ds_read_b128 v[36:39], v232 offset:24832\n"
      "v_mfma_f32_16x16x32_bf16 v[136:139], v[164:167], v[40:43], v[136:139]\n"
      "ds_read_b128 v[40:43], v233 offset:24832\n"
      "v_mfma_f32_16x16x32_bf16 v[136:139], v[180:183], v[44:47], v[136:139]\n"
      "ds_read_b128 v[44:47], v234 offset:24832\n"
      "v_mfma_f32_16x16x32_bf16 v[136:139], v[184:187], v[60:63], v[136:139]\n"
      "ds_read_b128 v[60:63], v235 offset:24832\n"
      "v_cvt_pk_bf16_f32 v208, v132, v133\n"
      "v_mfma_f32_16x16x32_bf16 v[136:139], v[188:191], v[64:67], v[136:139]\n"
      "ds_read_b128 v[64:67], v232 offset:33024\n"
      "v_cvt_pk_bf16_f32 v209, v134, v135\n"
      "v_mfma_f32_16x16x32_bf16 v[136:139], v[204:207], v[68:71], v[136:139]\n"
      "ds_read_b128 v[68:71], v233 offset:33024\n"
      "v_mfma_f32_16x16x32_bf16 v[136:139], a[24:27], v[84:87], v[136:139]\n"
      "ds_read_b128 v[84:87], v234 offset:33024\n"
      "v_mfma_f32_16x16x32_bf16 v[136:139], a[28:31], v[88:91], v[136:139]\n"
      "ds_read_b128 v[88:91], v235 offset:33024\n"
      "v_mfma_f32_16x16x32_bf16 v[136:139], a[32:35], v[92:95], v[136:139]\n"
      "ds_read_b128 v[92:95], v232 offset:41216\n"
      "v_mfma_f32_16x16x32_bf16 v[136:139], a[36:39], v[108:111], v[136:139]\n"
      "ds_read_b128 v[108:111], v233 offset:41216\n"
      "ds_write_b64 v244, v[208:209] offset:24704\n"
      "v_mfma_f32_16x16x32_bf16 v[136:139], a[40:43], v[112:115], v[136:139]\n"
      "ds_read_b128 v[112:115], v234 offset:41216\n"
      "v_mfma_f32_16x16x32_bf16 v[136:139], a[44:47], v[116:119], v[136:139]\n"
      "ds_read_b128 v[116:119], v235 offset:41216\n"
      "s_waitcnt lgkmcnt(9)\n"
      "v_mfma_f32_16x16x32_bf16 v[132:135], a[240:243], v[36:39], v[0:3]\n"
      "v_add_u32 v232, 0xc000, v232\n"
      "v_mfma_f32_16x16x32_bf16 v[132:135], a[244:247], v[40:43], v[132:135]\n"
      "v_add_u32 v233, 0xc000, v233\n"
      "v_mfma_f32_16x16x32_bf16 v[132:135], a[248:251], v[44:47], v[132:135]\n"
      "v_add_u32 v234, 0xc000, v234\n"
      "v_mfma_f32_16x16x32_bf16 v[132:135], a[252:255], v[60:63], v[132:135]\n"
      "v_add_u32 v235, 0xc000, v235\n"
      "s_waitcnt lgkmcnt(5)\n"
      "v_mfma_f32_16x16x32_bf16 v[132:135], a[48:51], v[64:67], v[132:135]\n"
      "v_cvt_pk_bf16_f32 v210, v136, v137\n"
      "v_mfma_f32_16x16x32_bf16 v[132:135], a[52:55], v[68:71], v[132:135]\n"
      "v_cvt_pk_bf16_f32 v211, v138, v139\n"
      "v_mfma_f32_16x16x32_bf16 v[132:135], a[56:59], v[84:87], v[132:135]\n"
      "ds_read_b128 v[216:219], v246 offset:12288\n"
      "v_mfma_f32_16x16x32_bf16 v[132:135], a[60:63], v[88:91], v[132:135]\n"
      "ds_read_b128 v[220:223], v246 offset:13312\n"
      "s_waitcnt lgkmcnt(2)\n"
      "v_mfma_f32_16x16x32_bf16 v[132:135], a[64:67], v[92:95], v[132:135]\n"
      "ds_read_b128 v[224:227], v246 offset:14336\n"
      "s_cbranch_vccnz LT18\n"
      "LS18:\n"
      "v_mfma_f32_16x16x32_bf16 v[132:135], a[68:71], v[108:111], v[132:135]\n"
      "v_lshlrev_b32 v0, 16, v72\n"
      "v_mfma_f32_16x16x32_bf16 v[132:135], a[72:75], v[112:115], v[132:135]\n"
      "ds_write_b64 v244, v[210:211] offset:24712\n"
      "v_mfma_f32_16x16x32_bf16 v[132:135], a[76:79], v[116:119], v[132:135]\n"
      "v_and_b32 v1, 0xffff0000, v72\n"
      "v_mfma_f32_16x16x32_bf16 v[136:139], a[80:83], v[36:39], v[4:7]\n"
      "v_lshlrev_b32 v2, 16, v73\n"
      "v_mfma_f32_16x16x32_bf16 v[136:139], a[84:87], v[40:43], v[136:139]\n"
      "v_and_b32 v3, 0xffff0000, v73\n"
      "v_mfma_f32_16x16x32_bf16 v[136:139], a[88:91], v[44:47], v[136:139]\n"
      "v_mfma_f32_16x16x32_bf16 v[136:139], a[92:95], v[60:63], v[136:139]\n"
      "v_cvt_pk_bf16_f32 v208, v132, v133\n"
      "v_mfma_f32_16x16x32_bf16 v[136:139], a[96:99], v[64:67], v[136:139]\n"
      "v_cvt_pk_bf16_f32 v209, v134, v135\n"
      "v_mfma_f32_16x16x32_bf16 v[136:139], a[100:103], v[68:71], v[136:139]\n"
      "s_waitcnt lgkmcnt(3)\n"
      "v_mfma_f32_16x16x32_bf16 v[136:139], a[104:107], v[84:87], v[136:139]\n"
      "global_store_dwordx4 v247, v[216:219], s[54:55] offset:768 sc1\n"
      "v_mfma_f32_16x16x32_bf16 v[136:139], a[108:111], v[88:91], v[136:139]\n"
      "s_add_u32 m0, s17, 0x1000\n"
      "v_mfma_f32_16x16x32_bf16 v[136:139], a[112:115], v[92:95], v[136:139]\n"
      "global_load_lds_dwordx4 v237, s[52:53] offset:0 nt\n"
      "v_mfma_f32_16x16x32_bf16 v[136:139], a[116:119], v[108:111], v[136:139]\n"
      "ds_write_b64 v244, v[208:209] offset:0\n"
      "v_mfma_f32_16x16x32_bf16 v[136:139], a[120:123], v[112:115], v[136:139]\n"
      "v_lshlrev_b32 v4, 16, v74\n"
      "v_mfma_f32_16x16x32_bf16 v[136:139], a[124:127], v[116:119], v[136:139]\n"
      "v_and_b32 v5, 0xffff0000, v74\n"
      "v_mfma_f32_16x16x32_bf16 v[132:135], a[128:131], v[36:39], v[8:11]\n"
      "v_lshlrev_b32 v6, 16, v75\n"
      "v_mfma_f32_16x16x32_bf16 v[132:135], a[132:135], v[40:43], v[132:135]\n"
      "v_and_b32 v7, 0xffff0000, v75\n"
      "v_mfma_f32_16x16x32_bf16 v[132:135], a[136:139], v[44:47], v[132:135]\n"
      "v_mfma_f32_16x16x32_bf16 v[132:135], a[140:143], v[60:63], v[132:135]\n"
      "v_cvt_pk_bf16_f32 v210, v136, v137\n"
      "v_mfma_f32_16x16x32_bf16 v[132:135], a[144:147], v[64:67], v[132:135]\n"
      "v_cvt_pk_bf16_f32 v211, v138, v139\n"
      "v_mfma_f32_16x16x32_bf16 v[132:135], a[148:151], v[68:71], v[132:135]\n"
      "v_lshlrev_b32 v8, 16, v76\n"
      "v_mfma_f32_16x16x32_bf16 v[132:135], a[152:155], v[84:87], v[132:135]\n"
      "s_waitcnt lgkmcnt(3)\n"
      "v_mfma_f32_16x16x32_bf16 v[132:135], a[156:159], v[88:91], v[132:135]\n"
      "global_store_dwordx4 v252, v[220:223], s[54:55] offset:768 sc1\n"
      "v_mfma_f32_16x16x32_bf16 v[132:135], a[160:163], v[92:95], v[132:135]\n"
      "s_add_u32 m0, s17, 0x2f00\n"
      "v_mfma_f32_16x16x32_bf16 v[132:135], a[164:167], v[108:111], v[132:135]\n"
      "ds_write_b64 v244, v[210:211] offset:8\n"
      "v_mfma_f32_16x16x32_bf16 v[132:135], a[168:171], v[112:115], v[132:135]\n"
      "global_load_lds_dwordx4 v237, s[52:53] offset:256 nt\n"
      "v_mfma_f32_16x16x32_bf16 v[132:135], a[172:175], v[116:119], v[132:135]\n"
      "v_and_b32 v9, 0xffff0000, v76\n"
      "v_mfma_f32_16x16x32_bf16 v[136:139], a[176:179], v[36:39], v[12:15]\n"
      "v_lshlrev_b32 v10, 16, v77\n"
      "v_mfma_f32_16x16x32_bf16 v[136:139], a[180:183], v[40:43], v[136:139]\n"
      "v_and_b32 v11, 0xffff0000, v77\n"
      "v_mfma_f32_16x16x32_bf16 v[136:139], a[184:187], v[44:47], v[136:139]\n"
      "v_mfma_f32_16x16x32_bf16 v[136:139], a[188:191], v[60:63], v[136:139]\n"
      "v_cvt_pk_bf16_f32 v212, v132, v133\n"
      "v_mfma_f32_16x16x32_bf16 v[136:139], a[192:195], v[64:67], v[136:139]\n"
      "v_cvt_pk_bf16_f32 v213, v134, v135\n"
      "v_mfma_f32_16x16x32_bf16 v[136:139], a[196:199], v[68:71], v[136:139]\n"
      "v_lshlrev_b32 v12, 16, v78\n"
      "v_mfma_f32_16x16x32_bf16 v[136:139], a[200:203], v[84:87], v[136:139]\n"
      "v_and_b32 v13, 0xffff0000, v78\n"
      "v_mfma_f32_16x16x32_bf16 v[136:139], a[204:207], v[88:91], v[136:139]\n"
      "s_waitcnt lgkmcnt(3)\n"
      "v_mfma_f32_16x16x32_bf16 v[136:139], a[208:211], v[92:95], v[136:139]\n"
      "global_store_dwordx4 v253, v[224:227], s[54:55] offset:768 sc1\n"
      "s_cbranch_vccnz LT19\n"
      "LS19:\n"
      "v_mfma_f32_16x16x32_bf16 v[136:139], a[212:215], v[108:111], v[136:139]\n"
      "ds_write_b64 v245, v[212:213] offset:0\n"
      "v_mfma_f32_16x16x32_bf16 v[136:139], a[216:219], v[112:115], v[136:139]\n"
      "s_add_u32 m0, s17, 0x4e00\n"
      "v_mfma_f32_16x16x32_bf16 v[136:139], a[220:223], v[116:119], v[136:139]\n"
      "global_load_lds_dwordx4 v237, s[52:53] offset:512 nt\n"
      "v_mfma_f32_16x16x32_bf16 v[132:135], a[224:227], v[36:39], v[16:19]\n"
      "v_lshlrev_b32 v14, 16, v79\n"
      "v_mfma_f32_16x16x32_bf16 v[132:135], a[228:231], v[40:43], v[132:135]\n"
      "v_and_b32 v15, 0xffff0000, v79\n"
      "v_mfma_f32_16x16x32_bf16 v[132:135], a[232:235], v[44:47], v[132:135]\n"
      "v_mfma_f32_16x16x32_bf16 v[132:135], a[236:239], v[60:63], v[132:135]\n"
      "s_memrealtime s[84:85]\n"
      "v_mfma_f32_16x16x32_bf16 v[132:135], a[0:3], v[64:67], v[132:135]\n"
      "v_cvt_pk_bf16_f32 v214, v136, v137\n"
      "v_mfma_f32_16x16x32_bf16 v[132:135], a[4:7], v[68:71], v[132:135]\n"
      "v_cvt_pk_bf16_f32 v215, v138, v139\n"
      "v_mfma_f32_16x16x32_bf16 v[132:135], a[8:11], v[84:87], v[132:135]\n"
      "v_lshlrev_b32 v16, 16, v80\n"
      "v_mfma_f32_16x16x32_bf16 v[132:135], a[12:15], v[88:91], v[132:135]\n"
      "v_and_b32 v17, 0xffff0000, v80\n"
      "v_mfma_f32_16x16x32_bf16 v[132:135], a[16:19], v[92:95], v[132:135]\n"
      "v_lshlrev_b32 v18, 16, v81\n"
      "v_mfma_f32_16x16x32_bf16 v[132:135], a[20:23], v[108:111], v[132:135]\n"
      "v_and_b32 v19, 0xffff0000, v81\n"
      "v_mfma_f32_16x16x32_bf16 v[132:135], v[140:143], v[112:115], v[132:135]\n"
      "s_waitcnt lgkmcnt(0)\n"
      "s_mov_b32 s66, s84\n"
      "v_mfma_f32_16x16x32_bf16 v[132:135], v[156:159], v[116:119], v[132:135]\n"
      "s_waitcnt vmcnt(24)\n"
      "s_barrier\n"
      "ds_write_b64 v245, v[214:215] offset:8\n"
      "v_mfma_f32_16x16x32_bf16 v[136:139], v[160:163], v[36:39], v[20:23]\n"
      "ds_read_b128 v[36:39], v232 offset:0\n"
      "v_mfma_f32_16x16x32_bf16 v[136:139], v[164:167], v[40:43], v[136:139]\n"
      "ds_read_b128 v[40:43], v233 offset:0\n"
      "v_mfma_f32_16x16x32_bf16 v[136:139], v[180:183], v[44:47], v[136:139]\n"
      "ds_read_b128 v[44:47], v234 offset:0\n"
      "v_mfma_f32_16x16x32_bf16 v[136:139], v[184:187], v[60:63], v[136:139]\n"
      "ds_read_b128 v[60:63], v235 offset:0\n"
      "v_cvt_pk_bf16_f32 v208, v132, v133\n"
      "v_mfma_f32_16x16x32_bf16 v[136:139], v[188:191], v[64:67], v[136:139]\n"
      "ds_read_b128 v[64:67], v232 offset:8192\n"
      "v_cvt_pk_bf16_f32 v209, v134, v135\n"
      "v_mfma_f32_16x16x32_bf16 v[136:139], v[204:207], v[68:71], v[136:139]\n"
      "ds_read_b128 v[68:71], v233 offset:8192\n"
      "v_mfma_f32_16x16x32_bf16 v[136:139], a[24:27], v[84:87], v[136:139]\n"
      "ds_read_b128 v[84:87], v234 offset:8192\n"
      "v_mfma_f32_16x16x32_bf16 v[136:139], a[28:31], v[88:91], v[136:139]\n"
      "ds_read_b128 v[88:91], v235 offset:8192\n"
      "v_mfma_f32_16x16x32_bf16 v[136:139], a[32:35], v[92:95], v[136:139]\n"
      "ds_read_b128 v[92:95], v232 offset:16384\n"
      "v_mfma_f32_16x16x32_bf16 v[136:139], a[36:39], v[108:111], v[136:139]\n"
      "ds_read_b128 v[108:111], v233 offset:16384\n"
      "ds_write_b64 v244, v[208:209] offset:128\n"
      "v_mfma_f32_16x16x32_bf16 v[136:139], a[40:43], v[112:115], v[136:139]\n"
      "ds_read_b128 v[112:115], v234 offset:16384\n"
      "v_mfma_f32_16x16x32_bf16 v[136:139], a[44:47], v[116:119], v[136:139]\n"
      "ds_read_b128 v[116:119], v235 offset:16384\n"
      "s_waitcnt lgkmcnt(9)\n"
      "v_mfma_f32_16x16x32_bf16 v[132:135], a[240:243], v[36:39], v[0:3]\n"
      "s_sub_u32 s69, s66, s68\n"
      "v_mfma_f32_16x16x32_bf16 v[132:135], a[244:247], v[40:43], v[132:135]\n"
      "s_mov_b32 s68, s67\n"
      "v_mfma_f32_16x16x32_bf16 v[132:135], a[248:251], v[44:47], v[132:135]\n"
      "s_mov_b32 s67, s66\n"
      "v_mfma_f32_16x16x32_bf16 v[132:135], a[252:255], v[60:63], v[132:135]\n"
      "s_cmp_gt_i32 s69, 460\n"
      "s_cselect_b64 s[70:71], -1, s[70:71]\n"
      "s_waitcnt lgkmcnt(5)\n"
      "v_mfma_f32_16x16x32_bf16 v[132:135], a[48:51], v[64:67], v[132:135]\n"
      "s_or_b64 vcc, vcc, s[70:71]\n"
      "v_cvt_pk_bf16_f32 v211, v138, v139\n"
      "v_mfma_f32_16x16x32_bf16 v[132:135], a[52:55], v[68:71], v[132:135]\n"
      "v_lshlrev_b32 v20, 16, v82\n"
      "v_mfma_f32_16x16x32_bf16 v[132:135], a[56:59], v[84:87], v[132:135]\n"
      "v_and_b32 v21, 0xffff0000, v82\n"
      "v_mfma_f32_16x16x32_bf16 v[132:135], a[60:63], v[88:91], v[132:135]\n"
      "v_lshlrev_b32 v22, 16, v83\n"
      "s_waitcnt lgkmcnt(0)\n"
      "v_mfma_f32_16x16x32_bf16 v[132:135], a[64:67], v[92:95], v[132:135]\n"
      "v_and_b32 v23, 0xffff0000, v83\n"
      "s_cbranch_vccnz LT20\n"
      "LS20:\n"
      "v_mfma_f32_16x16x32_bf16 v[132:135], a[68:71], v[108:111], v[132:135]\n"
      "v_cvt_pk_bf16_f32 v210, v136, v137\n"
      "v_mfma_f32_16x16x32_bf16 v[132:135], a[72:75], v[112:115], v[132:135]\n"
      "s_add_u32 s54, s44, s34\n"
      "s_addc_u32 s55, s45, 0\n"
      "v_mfma_f32_16x16x32_bf16 v[132:135], a[76:79], v[116:119], v[132:135]\n"
      "ds_read_b128 v[216:219], v246 offset:24576\n"
      "v_mfma_f32_16x16x32_bf16 v[136:139], a[80:83], v[36:39], v[4:7]\n"
      "ds_read_b128 v[220:223], v246 offset:25600\n"
      "v_mfma_f32_16x16x32_bf16 v[136:139], a[84:87], v[40:43], v[136:139]\n"
      "ds_read_b128 v[224:227], v246 offset:26624\n"
      "v_mfma_f32_16x16x32_bf16 v[136:139], a[88:91], v[44:47], v[136:139]\n"
      "ds_write_b64 v244, v[210:211] offset:136\n"
      "v_mfma_f32_16x16x32_bf16 v[136:139], a[92:95], v[60:63], v[136:139]\n"
      "v_cvt_pk_bf16_f32 v208, v132, v133\n"
      "v_mfma_f32_16x16x32_bf16 v[136:139], a[96:99], v[64:67], v[136:139]\n"
      "v_cvt_pk_bf16_f32 v209, v134, v135\n"
      "v_mfma_f32_16x16x32_bf16 v[136:139], a[100:103], v[68:71], v[136:139]\n"
      "s_waitcnt lgkmcnt(3)\n"
      "v_mfma_f32_16x16x32_bf16 v[136:139], a[104:107], v[84:87], v[136:139]\n"
      "global_store_dwordx4 v247, v[216:219], s[54:55] offset:0 sc1\n"
      "v_mfma_f32_16x16x32_bf16 v[136:139], a[108:111], v[88:91], v[136:139]\n"
      "s_add_u32 s52, s42, s36\n"
      "s_addc_u32 s53, s43, 0\n"
      "v_mfma_f32_16x16x32_bf16 v[136:139], a[112:115], v[92:95], v[136:139]\n"
      "s_add_u32 m0, s17, 0x6000\n"
      "v_mfma_f32_16x16x32_bf16 v[136:139], a[116:119], v[108:111], v[136:139]\n"
      "ds_write_b64 v244, v[208:209] offset:12288\n"
      "v_mfma_f32_16x16x32_bf16 v[136:139], a[120:123], v[112:115], v[136:139]\n"
      "global_load_lds_dwordx4 v236, s[52:53] offset:0 nt\n"
      "v_mfma_f32_16x16x32_bf16 v[136:139], a[124:127], v[116:119], v[136:139]\n"
      "v_mfma_f32_16x16x32_bf16 v[132:135], a[128:131], v[36:39], v[8:11]\n"
      "v_mfma_f32_16x16x32_bf16 v[132:135], a[132:135], v[40:43], v[132:135]\n"
      "v_mfma_f32_16x16x32_bf16 v[132:135], a[136:139], v[44:47], v[132:135]\n"
      "v_mfma_f32_16x16x32_bf16 v[132:135], a[140:143], v[60:63], v[132:135]\n"
      "v_cvt_pk_bf16_f32 v210, v136, v137\n"
      "v_mfma_f32_16x16x32_bf16 v[132:135], a[144:147], v[64:67], v[132:135]\n"
      "v_cvt_pk_bf16_f32 v211, v138, v139\n"
      "v_mfma_f32_16x16x32_bf16 v[132:135], a[148:151], v[68:71], v[132:135]\n"
      "v_mfma_f32_16x16x32_bf16 v[132:135], a[152:155], v[84:87], v[132:135]\n"
      "s_waitcnt lgkmcnt(3)\n"
      "v_mfma_f32_16x16x32_bf16 v[132:135], a[156:159], v[88:91], v[132:135]\n"
      "global_store_dwordx4 v252, v[220:223], s[54:55] offset:0 sc1\n"
      "v_mfma_f32_16x16x32_bf16 v[132:135], a[160:163], v[92:95], v[132:135]\n"
      "s_add_u32 m0, s17, 0x7f00\n"
      "v_mfma_f32_16x16x32_bf16 v[132:135], a[164:167], v[108:111], v[132:135]\n"
      "ds_write_b64 v244, v[210:211] offset:12296\n"
      "v_mfma_f32_16x16x32_bf16 v[132:135], a[168:171], v[112:115], v[132:135]\n"
      "global_load_lds_dwordx4 v236, s[52:53] offset:256 nt\n"
      "v_mfma_f32_16x16x32_bf16 v[132:135], a[172:175], v[116:119], v[132:135]\n"
      "v_mfma_f32_16x16x32_bf16 v[136:139], a[176:179], v[36:39], v[12:15]\n"
      "v_mfma_f32_16x16x32_bf16 v[136:139], a[180:183], v[40:43], v[136:139]\n"
      "v_mfma_f32_16x16x32_bf16 v[136:139], a[184:187], v[44:47], v[136:139]\n"
      "v_mfma_f32_16x16x32_bf16 v[136:139], a[188:191], v[60:63], v[136:139]\n"
      "v_cvt_pk_bf16_f32 v212, v132, v133\n"
      "v_mfma_f32_16x16x32_bf16 v[136:139], a[192:195], v[64:67], v[136:139]\n"
      "v_cvt_pk_bf16_f32 v213, v134, v135\n"
      "v_mfma_f32_16x16x32_bf16 v[136:139], a[196:199], v[68:71], v[136:139]\n"
      "v_mfma_f32_16x16x32_bf16 v[136:139], a[200:203], v[84:87], v[136:139]\n"
      "v_mfma_f32_16x16x32_bf16 v[136:139], a[204:207], v[88:91], v[136:139]\n"
      "s_waitcnt lgkmcnt(3)\n"
      "v_mfma_f32_16x16x32_bf16 v[136:139], a[208:211], v[92:95], v[136:139]\n"
      "global_store_dwordx4 v253, v[224:227], s[54:55] offset:0 sc1\n"
      "s_cbranch_vccnz LT21\n"
      "LS21:\n"
      "v_mfma_f32_16x16x32_bf16 v[136:139], a[212:215], v[108:111], v[136:139]\n"
      "ds_write_b64 v245, v[212:213] offset:12288\n"
      "v_mfma_f32_16x16x32_bf16 v[136:139], a[216:219], v[112:115], v[136:139]\n"
      "s_add_u32 m0, s17, 0x9e00\n"
      "v_mfma_f32_16x16x32_bf16 v[136:139], a[220:223], v[116:119], v[136:139]\n"
      "global_load_lds_dwordx4 v236, s[52:53] offset:512 nt\n"
      "v_mfma_f32_16x16x32_bf16 v[132:135], a[224:227], v[36:39], v[16:19]\n"
      "v_mfma_f32_16x16x32_bf16 v[132:135], a[228:231], v[40:43], v[132:135]\n"
      "v_mfma_f32_16x16x32_bf16 v[132:135], a[232:235], v[44:47], v[132:135]\n"
      "v_mfma_f32_16x16x32_bf16 v[132:135], a[236:239], v[60:63], v[132:135]\n"
      "v_cvt_pk_bf16_f32 v214, v136, v137\n"
      "v_mfma_f32_16x16x32_bf16 v[132:135], a[0:3], v[64:67], v[132:135]\n"
      "v_cvt_pk_bf16_f32 v215, v138, v139\n"
      "v_mfma_f32_16x16x32_bf16 v[132:135], a[4:7], v[68:71], v[132:135]\n"
      "v_mfma_f32_16x16x32_bf16 v[132:135], a[8:11], v[84:87], v[132:135]\n"
      "v_mfma_f32_16x16x32_bf16 v[132:135], a[12:15], v[88:91], v[132:135]\n"
      "v_mfma_f32_16x16x32_bf16 v[132:135], a[16:19], v[92:95], v[132:135]\n"
      "v_mfma_f32_16x16x32_bf16 v[132:135], a[20:23], v[108:111], v[132:135]\n"
      "ds_write_b64 v245, v[214:215] offset:12296\n"
      "v_mfma_f32_16x16x32_bf16 v[132:135], v[140:143], v[112:115], v[132:135]\n"
      "v_mfma_f32_16x16x32_bf16 v[132:135], v[156:159], v[116:119], v[132:135]\n"
      "s_waitcnt lgkmcnt(4)\n"
      "s_barrier\n"
      "v_mfma_f32_16x16x32_bf16 v[136:139], v[160:163], v[36:39], v[20:23]\n"
      "ds_read_b128 v[36:39], v232 offset:256\n"
      "v_mfma_f32_16x16x32_bf16 v[136:139], v[164:167], v[40:43], v[136:139]\n"
      "ds_read_b128 v[40:43], v233 offset:256\n"
      "v_mfma_f32_16x16x32_bf16 v[136:139], v[180:183], v[44:47], v[136:139]\n"
      "ds_read_b128 v[44:47], v234 offset:256\n"
      "v_mfma_f32_16x16x32_bf16 v[136:139], v[184:187], v[60:63], v[136:139]\n"
      "ds_read_b128 v[60:63], v235 offset:256\n"
      "v_cvt_pk_bf16_f32 v208, v132, v133\n"
      "v_mfma_f32_16x16x32_bf16 v[136:139], v[188:191], v[64:67], v[136:139]\n"
      "ds_read_b128 v[64:67], v232 offset:8448\n"
      "v_cvt_pk_bf16_f32 v209, v134, v135\n"
      "v_mfma_f32_16x16x32_bf16 v[136:139], v[204:207], v[68:71], v[136:139]\n"
      "ds_read_b128 v[68:71], v233 offset:8448\n"
      "v_mfma_f32_16x16x32_bf16 v[136:139], a[24:27], v[84:87], v[136:139]\n"
      "ds_read_b128 v[84:87], v234 offset:8448\n"
      "v_mfma_f32_16x16x32_bf16 v[136:139], a[28:31], v[88:91], v[136:139]\n"
      "ds_read_b128 v[88:91], v235 offset:8448\n"
      "v_mfma_f32_16x16x32_bf16 v[136:139], a[32:35], v[92:95], v[136:139]\n"
      "ds_read_b128 v[92:95], v232 offset:16640\n"
      "v_mfma_f32_16x16x32_bf16 v[136:139], a[36:39], v[108:111], v[136:139]\n"
      "ds_read_b128 v[108:111], v233 offset:16640\n"
      "v_mfma_f32_16x16x32_bf16 v[136:139], a[40:43], v[112:115], v[136:139]\n"
      "ds_read_b128 v[112:115], v234 offset:16640\n"
      "v_mfma_f32_16x16x32_bf16 v[136:139], a[44:47], v[116:119], v[136:139]\n"
      "ds_read_b128 v[116:119], v235 offset:16640\n"
      "s_waitcnt lgkmcnt(8)\n"
      "v_mfma_f32_16x16x32_bf16 v[132:135], a[240:243], v[36:39], v[0:3]\n"
      "ds_write_b64 v244, v[208:209] offset:12416\n"
      "v_mfma_f32_16x16x32_bf16 v[132:135], a[244:247], v[40:43], v[132:135]\n"
      "v_mfma_f32_16x16x32_bf16 v[132:135], a[248:251], v[44:47], v[132:135]\n"
      "v_mfma_f32_16x16x32_bf16 v[132:135], a[252:255], v[60:63], v[132:135]\n"
      "v_cvt_pk_bf16_f32 v210, v136, v137\n"
      "s_waitcnt lgkmcnt(5)\n"
      "v_mfma_f32_16x16x32_bf16 v[132:135], a[48:51], v[64:67], v[132:135]\n"
      "v_lshlrev_b32 v229, 8, v239\n"
      "v_mfma_f32_16x16x32_bf16 v[132:135], a[52:55], v[68:71], v[132:135]\n"
      "v_add_u32 v229, 1114112, v229\n"
      "v_mfma_f32_16x16x32_bf16 v[132:135], a[56:59], v[84:87], v[132:135]\n"
      "v_cvt_pk_bf16_f32 v211, v138, v139\n"
      "v_mfma_f32_16x16x32_bf16 v[132:135], a[60:63], v[88:91], v[132:135]\n"
      "s_mov_b64 exec, 0xffff\n"
      "global_load_dword v228, v229, s[28:29] sc1\n"
      "s_mov_b64 exec, -1\n"
      "s_nop 1\n"
      "s_waitcnt lgkmcnt(1)\n"
      "v_mfma_f32_16x16x32_bf16 v[132:135], a[64:67], v[92:95], v[132:135]\n"
      "ds_read_b128 v[216:219], v246 offset:0\n"
      "s_cbranch_vccnz LT22\n"
      "LS22:\n"
      "v_mfma_f32_16x16x32_bf16 v[132:135], a[68:71], v[108:111], v[132:135]\n"
      "ds_read_b128 v[220:223], v246 offset:1024\n"
      "v_mfma_f32_16x16x32_bf16 v[132:135], a[72:75], v[112:115], v[132:135]\n"
      "ds_read_b128 v[224:227], v246 offset:2048\n"
      "v_mfma_f32_16x16x32_bf16 v[132:135], a[76:79], v[116:119], v[132:135]\n"
      "ds_write_b64 v244, v[210:211] offset:12424\n"
      "v_mfma_f32_16x16x32_bf16 v[136:139], a[80:83], v[36:39], v[4:7]\n"
      "v_mfma_f32_16x16x32_bf16 v[136:139], a[84:87], v[40:43], v[136:139]\n"
      "v_mfma_f32_16x16x32_bf16 v[136:139], a[88:91], v[44:47], v[136:139]\n"
      "v_mfma_f32_16x16x32_bf16 v[136:139], a[92:95], v[60:63], v[136:139]\n"
      "v_cvt_pk_bf16_f32 v208, v132, v133\n"
      "v_mfma_f32_16x16x32_bf16 v[136:139], a[96:99], v[64:67], v[136:139]\n"
      "v_cvt_pk_bf16_f32 v209, v134, v135\n"
      "v_mfma_f32_16x16x32_bf16 v[136:139], a[100:103], v[68:71], v[136:139]\n"
      "s_waitcnt lgkmcnt(3)\n"
      "v_mfma_f32_16x16x32_bf16 v[136:139], a[104:107], v[84:87], v[136:139]\n"
      "global_store_dwordx4 v247, v[216:219], s[54:55] offset:768 sc1\n"
      "v_mfma_f32_16x16x32_bf16 v[136:139], a[108:111], v[88:91], v[136:139]\n"
      "s_add_u32 m0, s17, 0x7000\n"
      "v_mfma_f32_16x16x32_bf16 v[136:139], a[112:115], v[92:95], v[136:139]\n"
      "global_load_lds_dwordx4 v237, s[52:53] offset:0 nt\n"
      "v_mfma_f32_16x16x32_bf16 v[136:139], a[116:119], v[108:111], v[136:139]\n"
      "ds_write_b64 v244, v[208:209] offset:24576\n"
      "v_mfma_f32_16x16x32_bf16 v[136:139], a[120:123], v[112:115], v[136:139]\n"
      "v_mfma_f32_16x16x32_bf16 v[136:139], a[124:127], v[116:119], v[136:139]\n"
      "v_mfma_f32_16x16x32_bf16 v[132:135], a[128:131], v[36:39], v[8:11]\n"
      "v_mfma_f32_16x16x32_bf16 v[132:135], a[132:135], v[40:43], v[132:135]\n"
      "v_mfma_f32_16x16x32_bf16 v[132:135], a[136:139], v[44:47], v[132:135]\n"
      "v_mfma_f32_16x16x32_bf16 v[132:135], a[140:143], v[60:63], v[132:135]\n"
      "v_cvt_pk_bf16_f32 v210, v136, v137\n"
      "v_mfma_f32_16x16x32_bf16 v[132:135], a[144:147], v[64:67], v[132:135]\n"
      "v_cvt_pk_bf16_f32 v211, v138, v139\n"
      "v_mfma_f32_16x16x32_bf16 v[132:135], a[148:151], v[68:71], v[132:135]\n"
      "v_mfma_f32_16x16x32_bf16 v[132:135], a[152:155], v[84:87], v[132:135]\n"
      "s_waitcnt lgkmcnt(3)\n"
      "v_mfma_f32_16x16x32_bf16 v[132:135], a[156:159], v[88:91], v[132:135]\n"
      "global_store_dwordx4 v252, v[220:223], s[54:55] offset:768 sc1\n"
      "v_mfma_f32_16x16x32_bf16 v[132:135], a[160:163], v[92:95], v[132:135]\n"
      "s_add_u32 m0, s17, 0x8f00\n"
      "v_mfma_f32_16x16x32_bf16 v[132:135], a[164:167], v[108:111], v[132:135]\n"
      "ds_write_b64 v244, v[210:211] offset:24584\n"
      "v_mfma_f32_16x16x32_bf16 v[132:135], a[168:171], v[112:115], v[132:135]\n"
      "global_load_lds_dwordx4 v237, s[52:53] offset:256 nt\n"
      "v_mfma_f32_16x16x32_bf16 v[132:135], a[172:175], v[116:119], v[132:135]\n"
      "v_mfma_f32_16x16x32_bf16 v[136:139], a[176:179], v[36:39], v[12:15]\n"
      "v_mfma_f32_16x16x32_bf16 v[136:139], a[180:183], v[40:43], v[136:139]\n"
      "v_mfma_f32_16x16x32_bf16 v[136:139], a[184:187], v[44:47], v[136:139]\n"
      "v_mfma_f32_16x16x32_bf16 v[136:139], a[188:191], v[60:63], v[136:139]\n"
      "v_cvt_pk_bf16_f32 v212, v132, v133\n"
      "v_mfma_f32_16x16x32_bf16 v[136:139], a[192:195], v[64:67], v[136:139]\n"
      "v_cvt_pk_bf16_f32 v213, v134, v135\n"
      "v_mfma_f32_16x16x32_bf16 v[136:139], a[196:199], v[68:71], v[136:139]\n"
      "v_mfma_f32_16x16x32_bf16 v[136:139], a[200:203], v[84:87], v[136:139]\n"
      "v_mfma_f32_16x16x32_bf16 v[136:139], a[204:207], v[88:91], v[136:139]\n"
      "s_waitcnt lgkmcnt(3)\n"
      "v_mfma_f32_16x16x32_bf16 v[136:139], a[208:211], v[92:95], v[136:139]\n"
      "global_store_dwordx4 v253, v[224:227], s[54:55] offset:768 sc1\n"
      "s_cbranch_vccnz LT23\n"
      "LS23:\n"
      "v_mfma_f32_16x16x32_bf16 v[136:139], a[212:215], v[108:111], v[136:139]\n"
      "ds_write_b64 v245, v[212:213] offset:24576\n"
      "v_mfma_f32_16x16x32_bf16 v[136:139], a[216:219], v[112:115], v[136:139]\n"
      "s_add_u32 m0, s17, 0xae00\n"
      "v_mfma_f32_16x16x32_bf16 v[136:139], a[220:223], v[116:119], v[136:139]\n"
      "global_load_lds_dwordx4 v237, s[52:53] offset:512 nt\n"
      "v_mfma_f32_16x16x32_bf16 v[132:135], a[224:227], v[36:39], v[16:19]\n"
      "v_mfma_f32_16x16x32_bf16 v[132:135], a[228:231], v[40:43], v[132:135]\n"
      "v_mfma_f32_16x16x32_bf16 v[132:135], a[232:235], v[44:47], v[132:135]\n"
      "v_mfma_f32_16x16x32_bf16 v[132:135], a[236:239], v[60:63], v[132:135]\n"
      "s_memrealtime s[84:85]\n"
      "v_mfma_f32_16x16x32_bf16 v[132:135], a[0:3], v[64:67], v[132:135]\n"
      "v_cvt_pk_bf16_f32 v214, v136, v137\n"
      "v_mfma_f32_16x16x32_bf16 v[132:135], a[4:7], v[68:71], v[132:135]\n"
      "v_cvt_pk_bf16_f32 v215, v138, v139\n"
      "v_mfma_f32_16x16x32_bf16 v[132:135], a[8:11], v[84:87], v[132:135]\n"
      "v_mfma_f32_16x16x32_bf16 v[132:135], a[12:15], v[88:91], v[132:135]\n"
      "v_mfma_f32_16x16x32_bf16 v[132:135], a[16:19], v[92:95], v[132:135]\n"
      "v_mfma_f32_16x16x32_bf16 v[132:135], a[20:23], v[108:111], v[132:135]\n"
      "v_mfma_f32_16x16x32_bf16 v[132:135], v[140:143], v[112:115], v[132:135]\n"
      "s_waitcnt lgkmcnt(0)\n"
      "s_mov_b32 s66, s84\n"
      "v_mfma_f32_16x16x32_bf16 v[132:135], v[156:159], v[116:119], v[132:135]\n"
      "s_waitcnt vmcnt(25)\n"
      "s_barrier\n"
      "ds_write_b64 v245, v[214:215] offset:24584\n"
      "v_mfma_f32_16x16x32_bf16 v[136:139], v[160:163], v[36:39], v[20:23]\n"
      "ds_read_b128 v[36:39], v232 offset:24576\n"
      "s_lshl_b32 s79, s92, 1\n"
      "v_mfma_f32_16x16x32_bf16 v[136:139], v[164:167], v[40:43], v[136:139]\n"
      "ds_read_b128 v[40:43], v233 offset:24576\n"
      "v_mad_i32_i24 v252, v251, s79, v252\n"
      "v_mfma_f32_16x16x32_bf16 v[136:139], v[180:183], v[44:47], v[136:139]\n"
      "ds_read_b128 v[44:47], v234 offset:24576\n"
      "s_mul_i32 s79, s79, s100\n"
      "v_mfma_f32_16x16x32_bf16 v[136:139], v[184:187], v[60:63], v[136:139]\n"
      "ds_read_b128 v[60:63], v235 offset:24576\n"
      "v_cvt_pk_bf16_f32 v208, v132, v133\n"
      "v_mfma_f32_16x16x32_bf16 v[136:139], v[188:191], v[64:67], v[136:139]\n"
      "ds_read_b128 v[64:67], v232 offset:32768\n"
      "v_cvt_pk_bf16_f32 v209, v134, v135\n"
      "v_mfma_f32_16x16x32_bf16 v[136:139], v[204:207], v[68:71], v[136:139]\n"
      "ds_read_b128 v[68:71], v233 offset:32768\n"
      "v_add_u32 v247, s79, v247\n"
      "v_mfma_f32_16x16x32_bf16 v[136:139], a[24:27], v[84:87], v[136:139]\n"
      "ds_read_b128 v[84:87], v234 offset:32768\n"
      "s_mul_i32 s79, s79, s101\n"
      "v_mfma_f32_16x16x32_bf16 v[136:139], a[28:31], v[88:91], v[136:139]\n"
      "ds_read_b128 v[88:91], v235 offset:32768\n"
      "v_add_u32 v253, s79, v253\n"
      "v_mfma_f32_16x16x32_bf16 v[136:139], a[32:35], v[92:95], v[136:139]\n"
      "ds_read_b128 v[92:95], v232 offset:40960\n"
      "v_mfma_f32_16x16x32_bf16 v[136:139], a[36:39], v[108:111], v[136:139]\n"
      "ds_read_b128 v[108:111], v233 offset:40960\n"
      "ds_write_b64 v244, v[208:209] offset:24704\n"
      "v_mfma_f32_16x16x32_bf16 v[136:139], a[40:43], v[112:115], v[136:139]\n"
      "ds_read_b128 v[112:115], v234 offset:40960\n"
      "v_mfma_f32_16x16x32_bf16 v[136:139], a[44:47], v[116:119], v[136:139]\n"
      "ds_read_b128 v[116:119], v235 offset:40960\n"
      "s_waitcnt lgkmcnt(9)\n"
      "v_mfma_f32_16x16x32_bf16 v[132:135], a[240:243], v[36:39], v[0:3]\n"
      "s_sub_u32 s69, s66, s68\n"
      "v_mfma_f32_16x16x32_bf16 v[132:135], a[244:247], v[40:43], v[132:135]\n"
      "s_mov_b32 s68, s67\n"
      "v_mfma_f32_16x16x32_bf16 v[132:135], a[248:251], v[44:47], v[132:135]\n"
      "s_mov_b32 s67, s66\n"
      "v_mfma_f32_16x16x32_bf16 v[132:135], a[252:255], v[60:63], v[132:135]\n"
      "s_cmp_gt_i32 s69, 460\n"
      "s_cselect_b64 s[70:71], -1, s[70:71]\n"
      "s_waitcnt lgkmcnt(5)\n"
      "v_mfma_f32_16x16x32_bf16 v[132:135], a[48:51], v[64:67], v[132:135]\n"
      "s_or_b64 vcc, vcc, s[70:71]\n"
      "s_mul_i32 s78, s94, s19\n"
      "v_mfma_f32_16x16x32_bf16 v[132:135], a[52:55], v[68:71], v[132:135]\n"
      "v_cvt_pk_bf16_f32 v210, v136, v137\n"
      "v_add_u32 v236, s78, v236\n"
      "v_mfma_f32_16x16x32_bf16 v[132:135], a[56:59], v[84:87], v[132:135]\n"
      "v_cvt_pk_bf16_f32 v211, v138, v139\n"
      "s_mul_i32 s78, s78, s99\n"
      "v_mfma_f32_16x16x32_bf16 v[132:135], a[60:63], v[88:91], v[132:135]\n"
      "s_add_u32 s54, s30, s35\n"
      "s_addc_u32 s55, s31, 0\n"
      "s_waitcnt lgkmcnt(0)\n"
      "v_mfma_f32_16x16x32_bf16 v[132:135], a[64:67], v[92:95], v[132:135]\n"
      "ds_read_b128 v[216:219], v246 offset:12288\n"
      "v_add_u32 v237, s78, v237\n"
      "v_mfma_f32_16x16x32_bf16 v[132:135], a[68:71], v[108:111], v[132:135]\n"
      "ds_read_b128 v[220:223], v246 offset:13312\n"
      "s_cbranch_vccnz LT24\n"
      "LS24:\n"
      "v_mfma_f32_16x16x32_bf16 v[132:135], a[72:75], v[112:115], v[132:135]\n"
      "ds_read_b128 v[224:227], v246 offset:14336\n"
      "v_mfma_f32_16x16x32_bf16 v[132:135], a[76:79], v[116:119], v[132:135]\n"
      "ds_write_b64 v244, v[210:211] offset:24712\n"
      "v_mfma_f32_16x16x32_bf16 v[136:139], a[80:83], v[36:39], v[4:7]\n"
      "v_mfma_f32_16x16x32_bf16 v[136:139], a[84:87], v[40:43], v[136:139]\n"
      "v_mfma_f32_16x16x32_bf16 v[136:139], a[88:91], v[44:47], v[136:139]\n"
      "v_mfma_f32_16x16x32_bf16 v[136:139], a[92:95], v[60:63], v[136:139]\n"
      "v_cvt_pk_bf16_f32 v208, v132, v133\n"
      "v_mfma_f32_16x16x32_bf16 v[136:139], a[96:99], v[64:67], v[136:139]\n"
      "v_cvt_pk_bf16_f32 v209, v134, v135\n"
      "v_mfma_f32_16x16x32_bf16 v[136:139], a[100:103], v[68:71], v[136:139]\n"
      "s_waitcnt lgkmcnt(3)\n"
      "v_mfma_f32_16x16x32_bf16 v[136:139], a[104:107], v[84:87], v[136:139]\n"
      "global_store_dwordx4 v247, v[216:219], s[54:55] offset:0 sc1\n"
      "v_mfma_f32_16x16x32_bf16 v[136:139], a[108:111], v[88:91], v[136:139]\n"
      "s_add_u32 s52, s20, s37\n"
      "s_addc_u32 s53, s21, 0\n"
      "v_mfma_f32_16x16x32_bf16 v[136:139], a[112:115], v[92:95], v[136:139]\n"
      "s_add_u32 m0, s17, 0xc000\n"
      "v_mfma_f32_16x16x32_bf16 v[136:139], a[116:119], v[108:111], v[136:139]\n"
      "ds_write_b64 v244, v[208:209] offset:0\n"
      "v_mfma_f32_16x16x32_bf16 v[136:139], a[120:123], v[112:115], v[136:139]\n"
      "global_load_lds_dwordx4 v236, s[52:53] offset:0 nt\n"
      "v_mfma_f32_16x16x32_bf16 v[136:139], a[124:127], v[116:119], v[136:139]\n"
      "v_mfma_f32_16x16x32_bf16 v[132:135], a[128:131], v[36:39], v[8:11]\n"
      "v_mfma_f32_16x16x32_bf16 v[132:135], a[132:135], v[40:43], v[132:135]\n"
      "v_mfma_f32_16x16x32_bf16 v[132:135], a[136:139], v[44:47], v[132:135]\n"
      "v_mfma_f32_16x16x32_bf16 v[132:135], a[140:143], v[60:63], v[132:135]\n"
      "v_cvt_pk_bf16_f32 v210, v136, v137\n"
      "v_mfma_f32_16x16x32_bf16 v[132:135], a[144:147], v[64:67], v[132:135]\n"
      "v_cvt_pk_bf16_f32 v211, v138, v139\n"
      "v_mfma_f32_16x16x32_bf16 v[132:135], a[148:151], v[68:71], v[132:135]\n"
      "v_mfma_f32_16x16x32_bf16 v[132:135], a[152:155], v[84:87], v[132:135]\n"
      "s_waitcnt lgkmcnt(3)\n"
      "v_mfma_f32_16x16x32_bf16 v[132:135], a[156:159], v[88:91], v[132:135]\n"
      "global_store_dwordx4 v252, v[220:223], s[54:55] offset:0 sc1\n"
      "v_mfma_f32_16x16x32_bf16 v[132:135], a[160:163], v[92:95], v[132:135]\n"
      "s_add_u32 m0, s17, 0xdf00\n"
      "v_mfma_f32_16x16x32_bf16 v[132:135], a[164:167], v[108:111], v[132:135]\n"
      "ds_write_b64 v244, v[210:211] offset:8\n"
      "v_mfma_f32_16x16x32_bf16 v[132:135], a[168:171], v[112:115], v[132:135]\n"
      "global_load_lds_dwordx4 v236, s[52:53] offset:256 nt\n"
      "v_mfma_f32_16x16x32_bf16 v[132:135], a[172:175], v[116:119], v[132:135]\n"
      "v_mfma_f32_16x16x32_bf16 v[136:139], a[176:179], v[36:39], v[12:15]\n"
      "v_mfma_f32_16x16x32_bf16 v[136:139], a[180:183], v[40:43], v[136:139]\n"
      "v_mfma_f32_16x16x32_bf16 v[136:139], a[184:187], v[44:47], v[136:139]\n"
      "v_mfma_f32_16x16x32_bf16 v[136:139], a[188:191], v[60:63], v[136:139]\n"
      "LPD11:\n"
      "s_cbranch_execz LPD14\n"
      "v_cvt_pk_bf16_f32 v212, v132, v133\n"
      "v_mfma_f32_16x16x32_bf16 v[136:139], a[192:195], v[64:67], v[136:139]\n"
      "v_cvt_pk_bf16_f32 v213, v134, v135\n"
      "v_mfma_f32_16x16x32_bf16 v[136:139], a[196:199], v[68:71], v[136:139]\n"
      "v_mfma_f32_16x16x32_bf16 v[136:139], a[200:203], v[84:87], v[136:139]\n"
      "v_mfma_f32_16x16x32_bf16 v[136:139], a[204:207], v[88:91], v[136:139]\n"
      "s_waitcnt lgkmcnt(3)\n"
      "v_mfma_f32_16x16x32_bf16 v[136:139], a[208:211], v[92:95], v[136:139]\n"
      "global_store_dwordx4 v253, v[224:227], s[54:55] offset:0 sc1\n"
      "s_cbranch_vccnz LT25\n"
      "LS25:\n"
      "v_mfma_f32_16x16x32_bf16 v[136:139], a[212:215], v[108:111], v[136:139]\n"
      "ds_write_b64 v245, v[212:213] offset:0\n"
      "v_mfma_f32_16x16x32_bf16 v[136:139], a[216:219], v[112:115], v[136:139]\n"
      "s_add_u32 m0, s17, 0xfe00\n"
      "v_mfma_f32_16x16x32_bf16 v[136:139], a[220:223], v[116:119], v[136:139]\n"
      "global_load_lds_dwordx4 v236, s[52:53] offset:512 nt\n"
      "v_mfma_f32_16x16x32_bf16 v[132:135], a[224:227], v[36:39], v[16:19]\n"
      "v_mfma_f32_16x16x32_bf16 v[132:135], a[228:231], v[40:43], v[132:135]\n"
      "v_mfma_f32_16x16x32_bf16 v[132:135], a[232:235], v[44:47], v[132:135]\n"
      "v_mfma_f32_16x16x32_bf16 v[132:135], a[236:239], v[60:63], v[132:135]\n"
      "v_cvt_pk_bf16_f32 v214, v136, v137\n"
      "v_mfma_f32_16x16x32_bf16 v[132:135], a[0:3], v[64:67], v[132:135]\n"
      "v_cvt_pk_bf16_f32 v215, v138, v139\n"
      "v_mfma_f32_16x16x32_bf16 v[132:135], a[4:7], v[68:71], v[132:135]\n"
      "v_mfma_f32_16x16x32_bf16 v[132:135], a[8:11], v[84:87], v[132:135]\n"
      "v_mfma_f32_16x16x32_bf16 v[132:135], a[12:15], v[88:91], v[132:135]\n"
      "v_mfma_f32_16x16x32_bf16 v[132:135], a[16:19], v[92:95], v[132:135]\n"
      "v_mfma_f32_16x16x32_bf16 v[132:135], a[20:23], v[108:111], v[132:135]\n"
      "ds_write_b64 v245, v[214:215] offset:8\n"
      "v_mfma_f32_16x16x32_bf16 v[132:135], v[140:143], v[112:115], v[132:135]\n"
      "v_mfma_f32_16x16x32_bf16 v[132:135], v[156:159], v[116:119], v[132:135]\n"
      "s_waitcnt lgkmcnt(4)\n"
      "s_barrier\n"
      "v_mfma_f32_16x16x32_bf16 v[136:139], v[160:163], v[36:39], v[20:23]\n"
      "ds_read_b128 v[36:39], v232 offset:24832\n"
      "v_mfma_f32_16x16x32_bf16 v[136:139], v[164:167], v[40:43], v[136:139]\n"
      "ds_read_b128 v[40:43], v233 offset:24832\n"
      "v_mfma_f32_16x16x32_bf16 v[136:139], v[180:183], v[44:47], v[136:139]\n"
      "ds_read_b128 v[44:47], v234 offset:24832\n"
      "v_mfma_f32_16x16x32_bf16 v[136:139], v[184:187], v[60:63], v[136:139]\n"
      "ds_read_b128 v[60:63], v235 offset:24832\n"
      "v_cvt_pk_bf16_f32 v208, v132, v133\n"
      "v_mfma_f32_16x16x32_bf16 v[136:139], v[188:191], v[64:67], v[136:139]\n"
      "ds_read_b128 v[64:67], v232 offset:33024\n"
      "v_cvt_pk_bf16_f32 v209, v134, v135\n"
      "v_mfma_f32_16x16x32_bf16 v[136:139], v[204:207], v[68:71], v[136:139]\n"
      "ds_read_b128 v[68:71], v233 offset:33024\n"
      "v_mfma_f32_16x16x32_bf16 v[136:139], a[24:27], v[84:87], v[136:139]\n"
      "ds_read_b128 v[84:87], v234 offset:33024\n"
      "v_mfma_f32_16x16x32_bf16 v[136:139], a[28:31], v[88:91], v[136:139]\n"
      "ds_read_b128 v[88:91], v235 offset:33024\n"
      "v_mfma_f32_16x16x32_bf16 v[136:139], a[32:35], v[92:95], v[136:139]\n"
      "ds_read_b128 v[92:95], v232 offset:41216\n"
      "v_mfma_f32_16x16x32_bf16 v[136:139], a[36:39], v[108:111], v[136:139]\n"
      "ds_read_b128 v[108:111], v233 offset:41216\n"
      "ds_write_b64 v244, v[208:209] offset:128\n"
      "v_mfma_f32_16x16x32_bf16 v[136:139], a[40:43], v[112:115], v[136:139]\n"
      "ds_read_b128 v[112:115], v234 offset:41216\n"
      "v_mfma_f32_16x16x32_bf16 v[136:139], a[44:47], v[116:119], v[136:139]\n"
      "ds_read_b128 v[116:119], v235 offset:41216\n"
      "s_waitcnt lgkmcnt(9)\n"
      "v_mfma_f32_16x16x32_bf16 v[132:135], a[240:243], v[36:39], v[0:3]\n"
      "v_add_u32 v232, 0xffff4000, v232\n"
      "v_mfma_f32_16x16x32_bf16 v[132:135], a[244:247], v[40:43], v[132:135]\n"
      "v_add_u32 v233, 0xffff4000, v233\n"
      "v_mfma_f32_16x16x32_bf16 v[132:135], a[248:251], v[44:47], v[132:135]\n"
      "v_add_u32 v234, 0xffff4000, v234\n"
      "v_mfma_f32_16x16x32_bf16 v[132:135], a[252:255], v[60:63], v[132:135]\n"
      "v_add_u32 v235, 0xffff4000, v235\n"
      "s_waitcnt lgkmcnt(5)\n"
      "v_mfma_f32_16x16x32_bf16 v[132:135], a[48:51], v[64:67], v[132:135]\n"
      "v_cvt_pk_bf16_f32 v210, v136, v137\n"
      "v_mfma_f32_16x16x32_bf16 v[132:135], a[52:55], v[68:71], v[132:135]\n"
      "v_cvt_pk_bf16_f32 v211, v138, v139\n"
      "v_mfma_f32_16x16x32_bf16 v[132:135], a[56:59], v[84:87], v[132:135]\n"
      "ds_read_b128 v[216:219], v246 offset:24576\n"
      "v_mfma_f32_16x16x32_bf16 v[132:135], a[60:63], v[88:91], v[132:135]\n"
      "ds_read_b128 v[220:223], v246 offset:25600\n"
      "s_waitcnt lgkmcnt(2)\n"
      "v_mfma_f32_16x16x32_bf16 v[132:135], a[64:67], v[92:95], v[132:135]\n"
      "ds_read_b128 v[224:227], v246 offset:26624\n"
      "s_cbranch_vccnz LT26\n"
      "LS26:\n"
      "v_mfma_f32_16x16x32_bf16 v[132:135], a[68:71], v[108:111], v[132:135]\n"
      "v_lshlrev_b32 v0, 16, v96\n"
      "v_mfma_f32_16x16x32_bf16 v[132:135], a[72:75], v[112:115], v[132:135]\n"
      "ds_write_b64 v244, v[210:211] offset:136\n"
      "v_mfma_f32_16x16x32_bf16 v[132:135], a[76:79], v[116:119], v[132:135]\n"
      "v_and_b32 v1, 0xffff0000, v96\n"
      "v_mfma_f32_16x16x32_bf16 v[136:139], a[80:83], v[36:39], v[4:7]\n"
      "v_lshlrev_b32 v2, 16, v97\n"
      "v_mfma_f32_16x16x32_bf16 v[136:139], a[84:87], v[40:43], v[136:139]\n"
      "v_and_b32 v3, 0xffff0000, v97\n"
      "v_mfma_f32_16x16x32_bf16 v[136:139], a[88:91], v[44:47], v[136:139]\n"
      "v_mfma_f32_16x16x32_bf16 v[136:139], a[92:95], v[60:63], v[136:139]\n"
      "v_cvt_pk_bf16_f32 v208, v132, v133\n"
      "v_mfma_f32_16x16x32_bf16 v[136:139], a[96:99], v[64:67], v[136:139]\n"
      "v_cvt_pk_bf16_f32 v209, v134, v135\n"
      "v_mfma_f32_16x16x32_bf16 v[136:139], a[100:103], v[68:71], v[136:139]\n"
      "s_waitcnt lgkmcnt(3)\n"
      "v_mfma_f32_16x16x32_bf16 v[136:139], a[104:107], v[84:87], v[136:139]\n"
      "global_store_dwordx4 v247, v[216:219], s[54:55] offset:768 sc1\n"
      "v_mfma_f32_16x16x32_bf16 v[136:139], a[108:111], v[88:91], v[136:139]\n"
      "s_add_u32 m0, s17, 0xd000\n"
      "v_mfma_f32_16x16x32_bf16 v[136:139], a[112:115], v[92:95], v[136:139]\n"
      "s_waitcnt vmcnt(13)\n"
      "s_sub_u32 s49, s48, 1000\n"
      "v_mfma_f32_16x16x32_bf16 v[136:139], a[116:119], v[108:111], v[136:139]\n"
      "v_mov_b32 v230, 1100\n"
      "ds_write_b64 v244, v[208:209] offset:12288\n"
      "v_mfma_f32_16x16x32_bf16 v[136:139], a[120:123], v[112:115], v[136:139]\n"
      "v_xor_b32 v228, s80, v228\n"
      "v_mfma_f32_16x16x32_bf16 v[136:139], a[124:127], v[116:119], v[136:139]\n"
      "v_subrev_u32 v228, s49, v228\n"
      "v_mfma_f32_16x16x32_bf16 v[132:135], a[128:131], v[36:39], v[8:11]\n"
      "v_mov_b32 v231, 2000\n"
      "v_mfma_f32_16x16x32_bf16 v[132:135], a[132:135], v[40:43], v[132:135]\n"
      "v_cmp_gt_u32 s[58:59], v231, v228\n"
      "v_mfma_f32_16x16x32_bf16 v[132:135], a[136:139], v[44:47], v[132:135]\n"
      "v_cndmask_b32 v228, v230, v228, s[58:59]\n"
      "v_mfma_f32_16x16x32_bf16 v[132:135], a[140:143], v[60:63], v[132:135]\n"
      "v_cvt_pk_bf16_f32 v210, v136, v137\n"
      "v_mfma_f32_16x16x32_bf16 v[132:135], a[144:147], v[64:67], v[132:135]\n"
      "v_add_u32_dpp v228, v228, v228 row_shr:1 row_mask:0xf bank_mask:0xf bound_ctrl:0\n"
      "v_mfma_f32_16x16x32_bf16 v[132:135], a[148:151], v[68:71], v[132:135]\n"
      "v_cvt_pk_bf16_f32 v211, v138, v139\n"
      "v_mfma_f32_16x16x32_bf16 v[132:135], a[152:155], v[84:87], v[132:135]\n"
      "v_add_u32_dpp v228, v228, v228 row_shr:2 row_mask:0xf bank_mask:0xf bound_ctrl:0\n"
      "v_mfma_f32_16x16x32_bf16 v[132:135], a[156:159], v[88:91], v[132:135]\n"
      "s_waitcnt lgkmcnt(3)\n"
      "v_mfma_f32_16x16x32_bf16 v[132:135], a[160:163], v[92:95], v[132:135]\n"
      "v_add_u32_dpp v228, v228, v228 row_shr:4 row_mask:0xf bank_mask:0xf bound_ctrl:0\n"
      "v_mfma_f32_16x16x32_bf16 v[132:135], a[164:167], v[108:111], v[132:135]\n"
      "global_store_dwordx4 v252, v[220:223], s[54:55] offset:768 sc1\n"
      "v_mfma_f32_16x16x32_bf16 v[132:135], a[168:171], v[112:115], v[132:135]\n"
      "v_add_u32_dpp v228, v228, v228 row_shr:8 row_mask:0xf bank_mask:0xf bound_ctrl:0\n"
      "v_mfma_f32_16x16x32_bf16 v[132:135], a[172:175], v[116:119], v[132:135]\n"
      "ds_write_b64 v244, v[210:211] offset:12296\n"
      "v_mfma_f32_16x16x32_bf16 v[136:139], a[176:179], v[36:39], v[12:15]\n"
      "v_readlane_b32 s62, v228, 15\n"
      "v_mfma_f32_16x16x32_bf16 v[136:139], a[180:183], v[40:43], v[136:139]\n"
      "global_load_lds_dwordx4 v237, s[52:53] offset:0 nt\n"
      "v_mfma_f32_16x16x32_bf16 v[136:139], a[184:187], v[44:47], v[136:139]\n"
      "v_lshlrev_b32 v4, 16, v98\n"
      "v_mfma_f32_16x16x32_bf16 v[136:139], a[188:191], v[60:63], v[136:139]\n"
      "s_lshr_b32 s62, s62, 4\n"
      "v_mfma_f32_16x16x32_bf16 v[136:139], a[192:195], v[64:67], v[136:139]\n"
      "s_add_u32 s62, s62, 20\n"
      "v_mfma_f32_16x16x32_bf16 v[136:139], a[196:199], v[68:71], v[136:139]\n"
      "s_add_u32 s49, s49, s62\n"
      "v_mfma_f32_16x16x32_bf16 v[136:139], a[200:203], v[84:87], v[136:139]\n"
      "v_cvt_pk_bf16_f32 v212, v132, v133\n"
      "v_mfma_f32_16x16x32_bf16 v[136:139], a[204:207], v[88:91], v[136:139]\n"
      "v_cvt_pk_bf16_f32 v213, v134, v135\n"
      "v_mfma_f32_16x16x32_bf16 v[136:139], a[208:211], v[92:95], v[136:139]\n"
      "s_waitcnt lgkmcnt(3)\n"
      "s_cbranch_vccnz LT27\n"
      "LS27:\n"
      "v_mfma_f32_16x16x32_bf16 v[136:139], a[212:215], v[108:111], v[136:139]\n"
      "global_store_dwordx4 v253, v[224:227], s[54:55] offset:768 sc1\n"
      "v_mfma_f32_16x16x32_bf16 v[136:139], a[216:219], v[112:115], v[136:139]\n"
      "v_and_b32 v5, 0xffff0000, v98\n"
      "v_mfma_f32_16x16x32_bf16 v[136:139], a[220:223], v[116:119], v[136:139]\n"
      "s_add_u32 m0, s17, 0xef00\n"
      "v_mfma_f32_16x16x32_bf16 v[132:135], a[224:227], v[36:39], v[16:19]\n"
      "ds_write_b64 v245, v[212:213] offset:12288\n"
      "v_mfma_f32_16x16x32_bf16 v[132:135], a[228:231], v[40:43], v[132:135]\n"
      "global_load_lds_dwordx4 v237, s[52:53] offset:256 nt\n"
      "v_mfma_f32_16x16x32_bf16 v[132:135], a[232:235], v[44:47], v[132:135]\n"
      "v_lshlrev_b32 v6, 16, v99\n"
      "v_mfma_f32_16x16x32_bf16 v[132:135], a[236:239], v[60:63], v[132:135]\n"
      "s_memrealtime s[84:85]\n"
      "v_mfma_f32_16x16x32_bf16 v[132:135], a[0:3], v[64:67], v[132:135]\n"
      "v_cvt_pk_bf16_f32 v214, v136, v137\n"
      "v_mfma_f32_16x16x32_bf16 v[132:135], a[4:7], v[68:71], v[132:135]\n"
      "v_cvt_pk_bf16_f32 v215, v138, v139\n"
      "v_mfma_f32_16x16x32_bf16 v[132:135], a[8:11], v[84:87], v[132:135]\n"
      "v_and_b32 v7, 0xffff0000, v99\n"
      "v_mfma_f32_16x16x32_bf16 v[132:135], a[12:15], v[88:91], v[132:135]\n"
      "v_lshlrev_b32 v8, 16, v100\n"
      "v_mfma_f32_16x16x32_bf16 v[132:135], a[16:19], v[92:95], v[132:135]\n"
      "v_and_b32 v9, 0xffff0000, v100\n"
      "v_mfma_f32_16x16x32_bf16 v[132:135], a[20:23], v[108:111], v[132:135]\n"
      "v_lshlrev_b32 v10, 16, v101\n"
      "v_mfma_f32_16x16x32_bf16 v[132:135], v[140:143], v[112:115], v[132:135]\n"
      "s_waitcnt lgkmcnt(0)\n"
      "s_mov_b32 s66, s84\n"
      "s_sub_u32 s86, s49, s84\n"
      "s_mov_b32 s83, s84\n"
      "v_mfma_f32_16x16x32_bf16 v[132:135], v[156:159], v[116:119], v[132:135]\n"
      "s_cmp_gt_u32 s62, 1080\n"
      "s_cselect_b64 vcc, -1, 0\n"
      "s_barrier\n"
      "v_mfma_f32_16x16x32_bf16 v[136:139], v[160:163], v[36:39], v[20:23]\n"
      "ds_read_b128 v[36:39], v232 offset:0\n"
      "ds_write_b64 v245, v[214:215] offset:12296\n"
      "v_mfma_f32_16x16x32_bf16 v[136:139], v[164:167], v[40:43], v[136:139]\n"
      "ds_read_b128 v[40:43], v233 offset:0\n"
      "v_mfma_f32_16x16x32_bf16 v[136:139], v[180:183], v[44:47], v[136:139]\n"
      "ds_read_b128 v[44:47], v234 offset:0\n"
      "v_mfma_f32_16x16x32_bf16 v[136:139], v[184:187], v[60:63], v[136:139]\n"
      "ds_read_b128 v[60:63], v235 offset:0\n"
      "v_cvt_pk_bf16_f32 v208, v132, v133\n"
      "v_mfma_f32_16x16x32_bf16 v[136:139], v[188:191], v[64:67], v[136:139]\n"
      "ds_read_b128 v[64:67], v232 offset:8192\n"
      "v_cvt_pk_bf16_f32 v209, v134, v135\n"
      "v_mfma_f32_16x16x32_bf16 v[136:139], v[204:207], v[68:71], v[136:139]\n"
      "ds_read_b128 v[68:71], v233 offset:8192\n"
      "v_mfma_f32_16x16x32_bf16 v[136:139], a[24:27], v[84:87], v[136:139]\n"
      "ds_read_b128 v[84:87], v234 offset:8192\n"
      "v_mfma_f32_16x16x32_bf16 v[136:139], a[28:31], v[88:91], v[136:139]\n"
      "ds_read_b128 v[88:91], v235 offset:8192\n"
      "v_mfma_f32_16x16x32_bf16 v[136:139], a[32:35], v[92:95], v[136:139]\n"
      "ds_read_b128 v[92:95], v232 offset:16384\n"
      "v_mfma_f32_16x16x32_bf16 v[136:139], a[36:39], v[108:111], v[136:139]\n"
      "ds_read_b128 v[108:111], v233 offset:16384\n"
      "ds_write_b64 v244, v[208:209] offset:12416\n"
      "v_mfma_f32_16x16x32_bf16 v[136:139], a[40:43], v[112:115], v[136:139]\n"
      "ds_read_b128 v[112:115], v234 offset:16384\n"
      "v_mfma_f32_16x16x32_bf16 v[136:139], a[44:47], v[116:119], v[136:139]\n"
      "ds_read_b128 v[116:119], v235 offset:16384\n"
      "s_waitcnt lgkmcnt(9)\n"
      "v_mfma_f32_16x16x32_bf16 v[132:135], a[240:243], v[36:39], v[0:3]\n"
      "s_sub_u32 s69, s66, s68\n"
      "v_mfma_f32_16x16x32_bf16 v[132:135], a[244:247], v[40:43], v[132:135]\n"
      "s_mov_b32 s68, s67\n"
      "v_mfma_f32_16x16x32_bf16 v[132:135], a[248:251], v[44:47], v[132:135]\n"
      "s_mul_i32 s86, s86, 128\n"
      "s_mov_b32 s67, s66\n"
      "v_mfma_f32_16x16x32_bf16 v[132:135], a[252:255], v[60:63], v[132:135]\n"
      "s_cmp_gt_i32 s69, 460\n"
      "s_cselect_b64 s[70:71], -1, s[70:71]\n"
      "s_waitcnt lgkmcnt(5)\n"
      "v_mfma_f32_16x16x32_bf16 v[132:135], a[48:51], v[64:67], v[132:135]\n"
      "s_ashr_i32 s86, s86, 10\n"
      "s_or_b64 vcc, vcc, s[70:71]\n"
      "v_mfma_f32_16x16x32_bf16 v[132:135], a[52:55], v[68:71], v[132:135]\n"
      "s_add_u32 m0, s17, 0x10e00\n"
      "v_cvt_pk_bf16_f32 v210, v136, v137\n"
      "v_mfma_f32_16x16x32_bf16 v[132:135], a[56:59], v[84:87], v[132:135]\n"
      "global_load_lds_dwordx4 v237, s[52:53] offset:512 nt\n"
      "v_cvt_pk_bf16_f32 v211, v138, v139\n"
      "v_mfma_f32_16x16x32_bf16 v[132:135], a[60:63], v[88:91], v[132:135]\n"
      "v_and_b32 v11, 0xffff0000, v101\n"
      "s_waitcnt lgkmcnt(0)\n"
      "v_mfma_f32_16x16x32_bf16 v[132:135], a[64:67], v[92:95], v[132:135]\n"
      "v_lshlrev_b32 v12, 16, v102\n"
      "s_cbranch_vccnz LT28\n"
      "LS28:\n"
      "v_mfma_f32_16x16x32_bf16 v[132:135], a[68:71], v[108:111], v[132:135]\n"
      "v_and_b32 v13, 0xffff0000, v102\n"
      "v_mfma_f32_16x16x32_bf16 v[132:135], a[72:75], v[112:115], v[132:135]\n"
      "v_lshlrev_b32 v14, 16, v103\n"
      "v_mfma_f32_16x16x32_bf16 v[132:135], a[76:79], v[116:119], v[132:135]\n"
      "v_and_b32 v15, 0xffff0000, v103\n"
      "ds_write_b64 v244, v[210:211] offset:12424\n"
      "v_mfma_f32_16x16x32_bf16 v[136:139], a[80:83], v[36:39], v[4:7]\n"
      "v_lshlrev_b32 v16, 16, v104\n"
      "v_mfma_f32_16x16x32_bf16 v[136:139], a[84:87], v[40:43], v[136:139]\n"
      "v_and_b32 v17, 0xffff0000, v104\n"
      "v_mfma_f32_16x16x32_bf16 v[136:139], a[88:91], v[44:47], v[136:139]\n"
      "v_lshlrev_b32 v18, 16, v105\n"
      "v_mfma_f32_16x16x32_bf16 v[136:139], a[92:95], v[60:63], v[136:139]\n"
      "v_and_b32 v19, 0xffff0000, v105\n"
      "v_mfma_f32_16x16x32_bf16 v[136:139], a[96:99], v[64:67], v[136:139]\n"
      "v_lshlrev_b32 v20, 16, v106\n"
      "v_mfma_f32_16x16x32_bf16 v[136:139], a[100:103], v[68:71], v[136:139]\n"
      "v_and_b32 v21, 0xffff0000, v106\n"
      "v_mfma_f32_16x16x32_bf16 v[136:139], a[104:107], v[84:87], v[136:139]\n"
      "v_lshlrev_b32 v22, 16, v107\n"
      "v_mfma_f32_16x16x32_bf16 v[136:139], a[108:111], v[88:91], v[136:139]\n"
      "v_and_b32 v23, 0xffff0000, v107\n"
      "v_mfma_f32_16x16x32_bf16 v[136:139], a[112:115], v[92:95], v[136:139]\n"
      "v_cvt_pk_bf16_f32 v208, v132, v133\n"
      "v_mfma_f32_16x16x32_bf16 v[136:139], a[116:119], v[108:111], v[136:139]\n"
      "v_cvt_pk_bf16_f32 v209, v134, v135\n"
      "v_mfma_f32_16x16x32_bf16 v[136:139], a[120:123], v[112:115], v[136:139]\n"
      "s_add_u32 s54, s44, s35\n"
      "s_addc_u32 s55, s45, 0\n"
      "v_mfma_f32_16x16x32_bf16 v[136:139], a[124:127], v[116:119], v[136:139]\n"
      "ds_read_b128 v[216:219], v246 offset:0\n"
      "v_mfma_f32_16x16x32_bf16 v[132:135], a[128:131], v[36:39], v[8:11]\n"
      "ds_read_b128 v[220:223], v246 offset:1024\n"
      "v_mfma_f32_16x16x32_bf16 v[132:135], a[132:135], v[40:43], v[132:135]\n"
      "ds_read_b128 v[224:227], v246 offset:2048\n"
      "v_mfma_f32_16x16x32_bf16 v[132:135], a[136:139], v[44:47], v[132:135]\n"
      "ds_write_b64 v244, v[208:209] offset:24576\n"
      "v_mfma_f32_16x16x32_bf16 v[132:135], a[140:143], v[60:63], v[132:135]\n"
      "v_cvt_pk_bf16_f32 v210, v136, v137\n"
      "v_mfma_f32_16x16x32_bf16 v[132:135], a[144:147], v[64:67], v[132:135]\n"
      "v_cvt_pk_bf16_f32 v211, v138, v139\n"
      "v_mfma_f32_16x16x32_bf16 v[132:135], a[148:151], v[68:71], v[132:135]\n"
      "s_waitcnt lgkmcnt(3)\n"
      "v_mfma_f32_16x16x32_bf16 v[132:135], a[152:155], v[84:87], v[132:135]\n"
      "global_store_dwordx4 v247, v[216:219], s[54:55] offset:0 sc1\n"
      "v_mfma_f32_16x16x32_bf16 v[132:135], a[156:159], v[88:91], v[132:135]\n"
      "s_waitcnt lgkmcnt(2)\n"
      "v_mfma_f32_16x16x32_bf16 v[132:135], a[160:163], v[92:95], v[132:135]\n"
      "global_store_dwordx4 v252, v[220:223], s[54:55] offset:0 sc1\n"
      "v_mfma_f32_16x16x32_bf16 v[132:135], a[164:167], v[108:111], v[132:135]\n"
      "ds_write_b64 v244, v[210:211] offset:24584\n"
      "v_mfma_f32_16x16x32_bf16 v[132:135], a[168:171], v[112:115], v[132:135]\n"
      "s_add_u32 s52, s42, s37\n"
      "s_addc_u32 s53, s43, 0\n"
      "v_mfma_f32_16x16x32_bf16 v[132:135], a[172:175], v[116:119], v[132:135]\n"
      "s_add_u32 m0, s17, 0x12000\n"
      "v_mfma_f32_16x16x32_bf16 v[136:139], a[176:179], v[36:39], v[12:15]\n"
      "global_load_lds_dwordx4 v236, s[52:53] offset:0 nt\n"
      "v_mfma_f32_16x16x32_bf16 v[136:139], a[180:183], v[40:43], v[136:139]\n"
      "v_mfma_f32_16x16x32_bf16 v[136:139], a[184:187], v[44:47], v[136:139]\n"
      "v_mfma_f32_16x16x32_bf16 v[136:139], a[188:191], v[60:63], v[136:139]\n"
      "v_cvt_pk_bf16_f32 v212, v132, v133\n"
      "v_mfma_f32_16x16x32_bf16 v[136:139], a[192:195], v[64:67], v[136:139]\n"
      "v_cvt_pk_bf16_f32 v213, v134, v135\n"
      "v_mfma_f32_16x16x32_bf16 v[136:139], a[196:199], v[68:71], v[136:139]\n"
      "v_mfma_f32_16x16x32_bf16 v[136:139], a[200:203], v[84:87], v[136:139]\n"
      "v_mfma_f32_16x16x32_bf16 v[136:139], a[204:207], v[88:91], v[136:139]\n"
      "s_waitcnt lgkmcnt(2)\n"
      "v_mfma_f32_16x16x32_bf16 v[136:139], a[208:211], v[92:95], v[136:139]\n"
      "global_store_dwordx4 v253, v[224:227], s[54:55] offset:0 sc1\n"
      "s_cbranch_vccnz LT29\n"
      "LS29:\n"
      "v_mfma_f32_16x16x32_bf16 v[136:139], a[212:215], v[108:111], v[136:139]\n"
      "ds_write_b64 v245, v[212:213] offset:24576\n"
      "v_mfma_f32_16x16x32_bf16 v[136:139], a[216:219], v[112:115], v[136:139]\n"
      "s_add_u32 m0, s17, 0x13f00\n"
      "v_mfma_f32_16x16x32_bf16 v[136:139], a[220:223], v[116:119], v[136:139]\n"
      "global_load_lds_dwordx4 v236, s[52:53] offset:256 nt\n"
      "v_mfma_f32_16x16x32_bf16 v[132:135], a[224:227], v[36:39], v[16:19]\n"
      "v_mfma_f32_16x16x32_bf16 v[132:135], a[228:231], v[40:43], v[132:135]\n"
      "v_mfma_f32_16x16x32_bf16 v[132:135], a[232:235], v[44:47], v[132:135]\n"
      "v_mfma_f32_16x16x32_bf16 v[132:135], a[236:239], v[60:63], v[132:135]\n"
      "v_cvt_pk_bf16_f32 v214, v136, v137\n"
      "v_mfma_f32_16x16x32_bf16 v[132:135], a[0:3], v[64:67], v[132:135]\n"
      "v_cvt_pk_bf16_f32 v215, v138, v139\n"
      "v_mfma_f32_16x16x32_bf16 v[132:135], a[4:7], v[68:71], v[132:135]\n"
      "v_mfma_f32_16x16x32_bf16 v[132:135], a[8:11], v[84:87], v[132:135]\n"
      "v_mfma_f32_16x16x32_bf16 v[132:135], a[12:15], v[88:91], v[132:135]\n"
      "v_mfma_f32_16x16x32_bf16 v[132:135], a[16:19], v[92:95], v[132:135]\n"
      "v_mfma_f32_16x16x32_bf16 v[132:135], a[20:23], v[108:111], v[132:135]\n"
      "ds_write_b64 v245, v[214:215] offset:24584\n"
      "v_mfma_f32_16x16x32_bf16 v[132:135], v[140:143], v[112:115], v[132:135]\n"
      "s_add_u32 m0, s17, 0x15e00\n"
      "v_mfma_f32_16x16x32_bf16 v[132:135], v[156:159], v[116:119], v[132:135]\n"
      "s_barrier\n"
      "v_mfma_f32_16x16x32_bf16 v[136:139], v[160:163], v[36:39], v[20:23]\n"
      "ds_read_b128 v[36:39], v232 offset:256\n"
      "v_mfma_f32_16x16x32_bf16 v[136:139], v[164:167], v[40:43], v[136:139]\n"
      "ds_read_b128 v[40:43], v233 offset:256\n"
      "v_mfma_f32_16x16x32_bf16 v[136:139], v[180:183], v[44:47], v[136:139]\n"
      "ds_read_b128 v[44:47], v234 offset:256\n"
      "v_mfma_f32_16x16x32_bf16 v[136:139], v[184:187], v[60:63], v[136:139]\n"
      "ds_read_b128 v[60:63], v235 offset:256\n"
      "v_cvt_pk_bf16_f32 v208, v132, v133\n"
      "v_mfma_f32_16x16x32_bf16 v[136:139], v[188:191], v[64:67], v[136:139]\n"
      "ds_read_b128 v[64:67], v232 offset:8448\n"
      "v_cvt_pk_bf16_f32 v209, v134, v135\n"
      "v_mfma_f32_16x16x32_bf16 v[136:139], v[204:207], v[68:71], v[136:139]\n"
      "ds_read_b128 v[68:71], v233 offset:8448\n"
      "v_mfma_f32_16x16x32_bf16 v[136:139], a[24:27], v[84:87], v[136:139]\n"
      "ds_read_b128 v[84:87], v234 offset:8448\n"
      "v_mfma_f32_16x16x32_bf16 v[136:139], a[28:31], v[88:91], v[136:139]\n"
      "ds_read_b128 v[88:91], v235 offset:8448\n"
      "v_mfma_f32_16x16x32_bf16 v[136:139], a[32:35], v[92:95], v[136:139]\n"
      "ds_read_b128 v[92:95], v232 offset:16640\n"
      "v_mfma_f32_16x16x32_bf16 v[136:139], a[36:39], v[108:111], v[136:139]\n"
      "ds_read_b128 v[108:111], v233 offset:16640\n"
      "v_mfma_f32_16x16x32_bf16 v[136:139], a[40:43], v[112:115], v[136:139]\n"
      "ds_read_b128 v[112:115], v234 offset:16640\n"
      "v_mfma_f32_16x16x32_bf16 v[136:139], a[44:47], v[116:119], v[136:139]\n"
      "ds_read_b128 v[116:119], v235 offset:16640\n"
      "s_waitcnt lgkmcnt(8)\n"
      "v_mfma_f32_16x16x32_bf16 v[132:135], a[240:243], v[36:39], v[0:3]\n"
      "ds_write_b64 v244, v[208:209] offset:24704\n"
      "v_mfma_f32_16x16x32_bf16 v[132:135], a[244:247], v[40:43], v[132:135]\n"
      "global_load_lds_dwordx4 v236, s[52:53] offset:512 nt\n"
      "v_mfma_f32_16x16x32_bf16 v[132:135], a[248:251], v[44:47], v[132:135]\n"
      "v_mfma_f32_16x16x32_bf16 v[132:135], a[252:255], v[60:63], v[132:135]\n"
      "v_cvt_pk_bf16_f32 v210, v136, v137\n"
      "s_waitcnt lgkmcnt(5)\n"
      "v_mfma_f32_16x16x32_bf16 v[132:135], a[48:51], v[64:67], v[132:135]\n"
      "v_cvt_pk_bf16_f32 v211, v138, v139\n"
      "v_mfma_f32_16x16x32_bf16 v[132:135], a[52:55], v[68:71], v[132:135]\n"
      "v_mfma_f32_16x16x32_bf16 v[132:135], a[56:59], v[84:87], v[132:135]\n"
      "ds_read_b128 v[216:219], v246 offset:12288\n"
      "v_mfma_f32_16x16x32_bf16 v[132:135], a[60:63], v[88:91], v[132:135]\n"
      "ds_read_b128 v[220:223], v246 offset:13312\n"
      "s_waitcnt lgkmcnt(3)\n"
      "v_mfma_f32_16x16x32_bf16 v[132:135], a[64:67], v[92:95], v[132:135]\n"
      "ds_read_b128 v[224:227], v246 offset:14336\n"
      "v_mfma_f32_16x16x32_bf16 v[132:135], a[68:71], v[108:111], v[132:135]\n"
      "ds_write_b64 v244, v[210:211] offset:24712\n"
      "v_mfma_f32_16x16x32_bf16 v[132:135], a[72:75], v[112:115], v[132:135]\n"
      "s_cbranch_vccnz LT30\n"
      "LS30:\n"
      "v_mfma_f32_16x16x32_bf16 v[132:135], a[76:79], v[116:119], v[132:135]\n"
      "v_mfma_f32_16x16x32_bf16 v[136:139], a[80:83], v[36:39], v[4:7]\n"
      "v_mfma_f32_16x16x32_bf16 v[136:139], a[84:87], v[40:43], v[136:139]\n"
      "v_mfma_f32_16x16x32_bf16 v[136:139], a[88:91], v[44:47], v[136:139]\n"
      "v_mfma_f32_16x16x32_bf16 v[136:139], a[92:95], v[60:63], v[136:139]\n"
      "v_cvt_pk_bf16_f32 v208, v132, v133\n"
      "v_mfma_f32_16x16x32_bf16 v[136:139], a[96:99], v[64:67], v[136:139]\n"
      "v_cvt_pk_bf16_f32 v209, v134, v135\n"
      "v_mfma_f32_16x16x32_bf16 v[136:139], a[100:103], v[68:71], v[136:139]\n"
      "s_waitcnt lgkmcnt(3)\n"
      "v_mfma_f32_16x16x32_bf16 v[136:139], a[104:107], v[84:87], v[136:139]\n"
      "global_store_dwordx4 v247, v[216:219], s[54:55] offset:768 sc1\n"
      "v_mfma_f32_16x16x32_bf16 v[136:139], a[108:111], v[88:91], v[136:139]\n"
      "s_add_u32 m0, s17, 0x13000\n"
      "v_mfma_f32_16x16x32_bf16 v[136:139], a[112:115], v[92:95], v[136:139]\n"
      "global_load_lds_dwordx4 v237, s[52:53] offset:0 nt\n"
      "v_mfma_f32_16x16x32_bf16 v[136:139], a[116:119], v[108:111], v[136:139]\n"
      "ds_write_b64 v244, v[208:209] offset:0\n"
      "v_mfma_f32_16x16x32_bf16 v[136:139], a[120:123], v[112:115], v[136:139]\n"
      "v_mfma_f32_16x16x32_bf16 v[136:139], a[124:127], v[116:119], v[136:139]\n"
      "v_mfma_f32_16x16x32_bf16 v[132:135], a[128:131], v[36:39], v[8:11]\n"
      "v_mfma_f32_16x16x32_bf16 v[132:135], a[132:135], v[40:43], v[132:135]\n"
      "v_mfma_f32_16x16x32_bf16 v[132:135], a[136:139], v[44:47], v[132:135]\n"
      "v_mfma_f32_16x16x32_bf16 v[132:135], a[140:143], v[60:63], v[132:135]\n"
      "v_cvt_pk_bf16_f32 v210, v136, v137\n"
      "v_mfma_f32_16x16x32_bf16 v[132:135], a[144:147], v[64:67], v[132:135]\n"
      "v_cvt_pk_bf16_f32 v211, v138, v139\n"
      "v_mfma_f32_16x16x32_bf16 v[132:135], a[148:151], v[68:71], v[132:135]\n"
      "v_mfma_f32_16x16x32_bf16 v[132:135], a[152:155], v[84:87], v[132:135]\n"
      "s_waitcnt lgkmcnt(3)\n"
      "v_mfma_f32_16x16x32_bf16 v[132:135], a[156:159], v[88:91], v[132:135]\n"
      "global_store_dwordx4 v252, v[220:223], s[54:55] offset:768 sc1\n"
      "v_mfma_f32_16x16x32_bf16 v[132:135], a[160:163], v[92:95], v[132:135]\n"
      "s_add_u32 m0, s17, 0x14f00\n"
      "v_mfma_f32_16x16x32_bf16 v[132:135], a[164:167], v[108:111], v[132:135]\n"
      "ds_write_b64 v244, v[210:211] offset:8\n"
      "v_mfma_f32_16x16x32_bf16 v[132:135], a[168:171], v[112:115], v[132:135]\n"
      "global_load_lds_dwordx4 v237, s[52:53] offset:256 nt\n"
      "v_mfma_f32_16x16x32_bf16 v[132:135], a[172:175], v[116:119], v[132:135]\n"
      "v_mfma_f32_16x16x32_bf16 v[136:139], a[176:179], v[36:39], v[12:15]\n"
      "v_mfma_f32_16x16x32_bf16 v[136:139], a[180:183], v[40:43], v[136:139]\n"
      "v_mfma_f32_16x16x32_bf16 v[136:139], a[184:187], v[44:47], v[136:139]\n"
      "v_mfma_f32_16x16x32_bf16 v[136:139], a[188:191], v[60:63], v[136:139]\n"
      "v_cvt_pk_bf16_f32 v212, v132, v133\n"
      "v_mfma_f32_16x16x32_bf16 v[136:139], a[192:195], v[64:67], v[136:139]\n"
      "v_cvt_pk_bf16_f32 v213, v134, v135\n"
      "v_mfma_f32_16x16x32_bf16 v[136:139], a[196:199], v[68:71], v[136:139]\n"
      "v_mfma_f32_16x16x32_bf16 v[136:139], a[200:203], v[84:87], v[136:139]\n"
      "v_mfma_f32_16x16x32_bf16 v[136:139], a[204:207], v[88:91], v[136:139]\n"
      "s_waitcnt lgkmcnt(3)\n"
      "v_mfma_f32_16x16x32_bf16 v[136:139], a[208:211], v[92:95], v[136:139]\n"
      "global_store_dwordx4 v253, v[224:227], s[54:55] offset:768 sc1\n"
      "s_cbranch_vccnz LT31\n"
      "LS31:\n"
      "v_mfma_f32_16x16x32_bf16 v[136:139], a[212:215], v[108:111], v[136:139]\n"
      "ds_write_b64 v245, v[212:213] offset:0\n"
      "v_mfma_f32_16x16x32_bf16 v[136:139], a[216:219], v[112:115], v[136:139]\n"
      "s_add_u32 m0, s17, 0x16e00\n"
      "v_mfma_f32_16x16x32_bf16 v[136:139], a[220:223], v[116:119], v[136:139]\n"
      "global_load_lds_dwordx4 v237, s[52:53] offset:512 nt\n"
      "v_mfma_f32_16x16x32_bf16 v[132:135], a[224:227], v[36:39], v[16:19]\n"
      "v_mfma_f32_16x16x32_bf16 v[132:135], a[228:231], v[40:43], v[132:135]\n"
      "v_mfma_f32_16x16x32_bf16 v[132:135], a[232:235], v[44:47], v[132:135]\n"
      "v_mfma_f32_16x16x32_bf16 v[132:135], a[236:239], v[60:63], v[132:135]\n"
      "s_memrealtime s[84:85]\n"
      "v_mfma_f32_16x16x32_bf16 v[132:135], a[0:3], v[64:67], v[132:135]\n"
      "v_cvt_pk_bf16_f32 v214, v136, v137\n"
      "v_mfma_f32_16x16x32_bf16 v[132:135], a[4:7], v[68:71], v[132:135]\n"
      "v_cvt_pk_bf16_f32 v215, v138, v139\n"
      "v_mfma_f32_16x16x32_bf16 v[132:135], a[8:11], v[84:87], v[132:135]\n"
      "v_mfma_f32_16x16x32_bf16 v[132:135], a[12:15], v[88:91], v[132:135]\n"
      "s_add_u32 s83, s83, s86\n"
      "v_mfma_f32_16x16x32_bf16 v[132:135], a[16:19], v[92:95], v[132:135]\n"
      "v_mfma_f32_16x16x32_bf16 v[132:135], a[20:23], v[108:111], v[132:135]\n"
      "v_mfma_f32_16x16x32_bf16 v[132:135], v[140:143], v[112:115], v[132:135]\n"
      "s_waitcnt lgkmcnt(0)\n"
      "s_mov_b32 s66, s84\n"
      "s_sub_u32 s84, s84, s83\n"
      "v_mfma_f32_16x16x32_bf16 v[132:135], v[156:159], v[116:119], v[132:135]\n"
      "s_cmp_lt_i32 s84, 0\n"
      "s_cselect_b64 vcc, -1, 0\n"
      "s_waitcnt vmcnt(24)\n"
      "s_barrier\n"
      "v_mfma_f32_16x16x32_bf16 v[136:139], v[160:163], v[36:39], v[20:23]\n"
      "ds_read_b128 v[36:39], v232 offset:24576\n"
      "ds_write_b64 v245, v[214:215] offset:8\n"
      "v_mfma_f32_16x16x32_bf16 v[136:139], v[164:167], v[40:43], v[136:139]\n"
      "ds_read_b128 v[40:43], v233 offset:24576\n"
      "s_lshl_b32 s79, s93, 1\n"
      "v_mfma_f32_16x16x32_bf16 v[136:139], v[180:183], v[44:47], v[136:139]\n"
      "ds_read_b128 v[44:47], v234 offset:24576\n"
      "v_mad_i32_i24 v252, v251, s79, v252\n"
      "v_mfma_f32_16x16x32_bf16 v[136:139], v[184:187], v[60:63], v[136:139]\n"
      "ds_read_b128 v[60:63], v235 offset:24576\n"
      "v_cvt_pk_bf16_f32 v208, v132, v133\n"
      "v_mfma_f32_16x16x32_bf16 v[136:139], v[188:191], v[64:67], v[136:139]\n"
      "ds_read_b128 v[64:67], v232 offset:32768\n"
      "v_cvt_pk_bf16_f32 v209, v134, v135\n"
      "v_mfma_f32_16x16x32_bf16 v[136:139], v[204:207], v[68:71], v[136:139]\n"
      "ds_read_b128 v[68:71], v233 offset:32768\n"
      "s_mul_i32 s79, s79, s100\n"
      "v_mfma_f32_16x16x32_bf16 v[136:139], a[24:27], v[84:87], v[136:139]\n"
      "ds_read_b128 v[84:87], v234 offset:32768\n"
      "v_add_u32 v247, s79, v247\n"
      "v_mfma_f32_16x16x32_bf16 v[136:139], a[28:31], v[88:91], v[136:139]\n"
      "ds_read_b128 v[88:91], v235 offset:32768\n"
      "s_mul_i32 s79, s79, s101\n"
      "v_mfma_f32_16x16x32_bf16 v[136:139], a[32:35], v[92:95], v[136:139]\n"
      "ds_read_b128 v[92:95], v232 offset:40960\n"
      "v_add_u32 v253, s79, v253\n"
      "v_mfma_f32_16x16x32_bf16 v[136:139], a[36:39], v[108:111], v[136:139]\n"
      "ds_read_b128 v[108:111], v233 offset:40960\n"
      "ds_write_b64 v244, v[208:209] offset:128\n"
      "v_mfma_f32_16x16x32_bf16 v[136:139], a[40:43], v[112:115], v[136:139]\n"
      "ds_read_b128 v[112:115], v234 offset:40960\n"
      "v_mfma_f32_16x16x32_bf16 v[136:139], a[44:47], v[116:119], v[136:139]\n"
      "ds_read_b128 v[116:119], v235 offset:40960\n"
      "s_waitcnt lgkmcnt(9)\n"
      "v_mfma_f32_16x16x32_bf16 v[132:135], a[240:243], v[36:39], v[0:3]\n"
      "s_sub_u32 s69, s66, s68\n"
      "v_mfma_f32_16x16x32_bf16 v[132:135], a[244:247], v[40:43], v[132:135]\n"
      "s_mov_b32 s68, s67\n"
      "v_mfma_f32_16x16x32_bf16 v[132:135], a[248:251], v[44:47], v[132:135]\n"
      "s_mov_b32 s67, s66\n"
      "v_mfma_f32_16x16x32_bf16 v[132:135], a[252:255], v[60:63], v[132:135]\n"
      "s_cmp_gt_i32 s69, 460\n"
      "s_cselect_b64 s[70:71], -1, s[70:71]\n"
      "s_waitcnt lgkmcnt(5)\n"
      "v_mfma_f32_16x16x32_bf16 v[132:135], a[48:51], v[64:67], v[132:135]\n"
      "s_or_b64 vcc, vcc, s[70:71]\n"
      "s_mul_i32 s78, s95, s19\n"
      "v_mfma_f32_16x16x32_bf16 v[132:135], a[52:55], v[68:71], v[132:135]\n"
      "v_cvt_pk_bf16_f32 v210, v136, v137\n"
      "v_add_u32 v236, s78, v236\n"
      "v_mfma_f32_16x16x32_bf16 v[132:135], a[56:59], v[84:87], v[132:135]\n"
      "v_cvt_pk_bf16_f32 v211, v138, v139\n"
      "s_mul_i32 s78, s78, s99\n"
      "v_mfma_f32_16x16x32_bf16 v[132:135], a[60:63], v[88:91], v[132:135]\n"
      "s_add_u32 s54, s30, s36\n"
      "s_addc_u32 s55, s31, 0\n"
      "s_waitcnt lgkmcnt(0)\n"
      "v_mfma_f32_16x16x32_bf16 v[132:135], a[64:67], v[92:95], v[132:135]\n"
      "ds_read_b128 v[216:219], v246 offset:24576\n"
      "v_add_u32 v237, s78, v237\n"
      "v_mfma_f32_16x16x32_bf16 v[132:135], a[68:71], v[108:111], v[132:135]\n"
      "ds_read_b128 v[220:223], v246 offset:25600\n"
      "s_cbranch_vccnz LT32\n"
      "LS32:\n"
      "v_mfma_f32_16x16x32_bf16 v[132:135], a[72:75], v[112:115], v[132:135]\n"
      "ds_read_b128 v[224:227], v246 offset:26624\n"
      "v_mfma_f32_16x16x32_bf16 v[132:135], a[76:79], v[116:119], v[132:135]\n"
      "ds_write_b64 v244, v[210:211] offset:136\n"
      "v_mfma_f32_16x16x32_bf16 v[136:139], a[80:83], v[36:39], v[4:7]\n"
      "v_mfma_f32_16x16x32_bf16 v[136:139], a[84:87], v[40:43], v[136:139]\n"
      "v_mfma_f32_16x16x32_bf16 v[136:139], a[88:91], v[44:47], v[136:139]\n"
      "v_mfma_f32_16x16x32_bf16 v[136:139], a[92:95], v[60:63], v[136:139]\n"
      "v_cvt_pk_bf16_f32 v208, v132, v133\n"
      "v_mfma_f32_16x16x32_bf16 v[136:139], a[96:99], v[64:67], v[136:139]\n"
      "v_cvt_pk_bf16_f32 v209, v134, v135\n"
      "v_mfma_f32_16x16x32_bf16 v[136:139], a[100:103], v[68:71], v[136:139]\n"
      "s_waitcnt lgkmcnt(3)\n"
      "v_mfma_f32_16x16x32_bf16 v[136:139], a[104:107], v[84:87], v[136:139]\n"
      "global_store_dwordx4 v247, v[216:219], s[54:55] offset:0 sc1\n"
      "v_mfma_f32_16x16x32_bf16 v[136:139], a[108:111], v[88:91], v[136:139]\n"
      "s_add_u32 s52, s20, s38\n"
      "s_addc_u32 s53, s21, 0\n"
      "v_mfma_f32_16x16x32_bf16 v[136:139], a[112:115], v[92:95], v[136:139]\n"
      "s_add_u32 m0, s17, 0x0\n"
      "v_mfma_f32_16x16x32_bf16 v[136:139], a[116:119], v[108:111], v[136:139]\n"
      "ds_write_b64 v244, v[208:209] offset:12288\n"
      "v_mfma_f32_16x16x32_bf16 v[136:139], a[120:123], v[112:115], v[136:139]\n"
      "LPD12:\n"
      "s_cbranch_execz LPD15\n"
      "global_load_lds_dwordx4 v236, s[52:53] offset:0 nt\n"
      "v_mfma_f32_16x16x32_bf16 v[136:139], a[124:127], v[116:119], v[136:139]\n"
      "v_mfma_f32_16x16x32_bf16 v[132:135], a[128:131], v[36:39], v[8:11]\n"
      "v_mfma_f32_16x16x32_bf16 v[132:135], a[132:135], v[40:43], v[132:135]\n"
      "v_mfma_f32_16x16x32_bf16 v[132:135], a[136:139], v[44:47], v[132:135]\n"
      "v_mfma_f32_16x16x32_bf16 v[132:135], a[140:143], v[60:63], v[132:135]\n"
      "v_cvt_pk_bf16_f32 v210, v136, v137\n"
      "v_mfma_f32_16x16x32_bf16 v[132:135], a[144:147], v[64:67], v[132:135]\n"
      "v_cvt_pk_bf16_f32 v211, v138, v139\n"
      "v_mfma_f32_16x16x32_bf16 v[132:135], a[148:151], v[68:71], v[132:135]\n"
      "v_mfma_f32_16x16x32_bf16 v[132:135], a[152:155], v[84:87], v[132:135]\n"
      "s_waitcnt lgkmcnt(3)\n"
      "v_mfma_f32_16x16x32_bf16 v[132:135], a[156:159], v[88:91], v[132:135]\n"
      "global_store_dwordx4 v252, v[220:223], s[54:55] offset:0 sc1\n"
      "v_mfma_f32_16x16x32_bf16 v[132:135], a[160:163], v[92:95], v[132:135]\n"
      "s_add_u32 m0, s17, 0x1f00\n"
      "v_mfma_f32_16x16x32_bf16 v[132:135], a[164:167], v[108:111], v[132:135]\n"
      "ds_write_b64 v244, v[210:211] offset:12296\n"
      "v_mfma_f32_16x16x32_bf16 v[132:135], a[168:171], v[112:115], v[132:135]\n"
      "global_load_lds_dwordx4 v236, s[52:53] offset:256 nt\n"
      "v_mfma_f32_16x16x32_bf16 v[132:135], a[172:175], v[116:119], v[132:135]\n"
      "v_mfma_f32_16x16x32_bf16 v[136:139], a[176:179], v[36:39], v[12:15]\n"
      "v_mfma_f32_16x16x32_bf16 v[136:139], a[180:183], v[40:43], v[136:139]\n"
      "v_mfma_f32_16x16x32_bf16 v[136:139], a[184:187], v[44:47], v[136:139]\n"
      "v_mfma_f32_16x16x32_bf16 v[136:139], a[188:191], v[60:63], v[136:139]\n"
      "v_cvt_pk_bf16_f32 v212, v132, v133\n"
      "v_mfma_f32_16x16x32_bf16 v[136:139], a[192:195], v[64:67], v[136:139]\n"
      "v_cvt_pk_bf16_f32 v213, v134, v135\n"
      "v_mfma_f32_16x16x32_bf16 v[136:139], a[196:199], v[68:71], v[136:139]\n"
      "v_mfma_f32_16x16x32_bf16 v[136:139], a[200:203], v[84:87], v[136:139]\n"
      "v_mfma_f32_16x16x32_bf16 v[136:139], a[204:207], v[88:91], v[136:139]\n"
      "s_waitcnt lgkmcnt(3)\n"
      "v_mfma_f32_16x16x32_bf16 v[136:139], a[208:211], v[92:95], v[136:139]\n"
      "global_store_dwordx4 v253, v[224:227], s[54:55] offset:0 sc1\n"
      "s_cbranch_vccnz LT33\n"
      "LS33:\n"
      "v_mfma_f32_16x16x32_bf16 v[136:139], a[212:215], v[108:111], v[136:139]\n"
      "ds_write_b64 v245, v[212:213] offset:12288\n"
      "v_mfma_f32_16x16x32_bf16 v[136:139], a[216:219], v[112:115], v[136:139]\n"
      "s_add_u32 m0, s17, 0x3e00\n"
      "v_mfma_f32_16x16x32_bf16 v[136:139], a[220:223], v[116:119], v[136:139]\n"
      "global_load_lds_dwordx4 v236, s[52:53] offset:512 nt\n"
      "v_mfma_f32_16x16x32_bf16 v[132:135], a[224:227], v[36:39], v[16:19]\n"
      "v_mfma_f32_16x16x32_bf16 v[132:135], a[228:231], v[40:43], v[132:135]\n"
      "v_mfma_f32_16x16x32_bf16 v[132:135], a[232:235], v[44:47], v[132:135]\n"
      "v_mfma_f32_16x16x32_bf16 v[132:135], a[236:239], v[60:63], v[132:135]\n"
      "v_cvt_pk_bf16_f32 v214, v136, v137\n"
      "v_mfma_f32_16x16x32_bf16 v[132:135], a[0:3], v[64:67], v[132:135]\n"
      "v_cvt_pk_bf16_f32 v215, v138, v139\n"
      "v_mfma_f32_16x16x32_bf16 v[132:135], a[4:7], v[68:71], v[132:135]\n"
      "v_mfma_f32_16x16x32_bf16 v[132:135], a[8:11], v[84:87], v[132:135]\n"
      "v_mfma_f32_16x16x32_bf16 v[132:135], a[12:15], v[88:91], v[132:135]\n"
      "v_mfma_f32_16x16x32_bf16 v[132:135], a[16:19], v[92:95], v[132:135]\n"
      "v_mfma_f32_16x16x32_bf16 v[132:135], a[20:23], v[108:111], v[132:135]\n"
      "ds_write_b64 v245, v[214:215] offset:12296\n"
      "v_mfma_f32_16x16x32_bf16 v[132:135], v[140:143], v[112:115], v[132:135]\n"
      "v_mfma_f32_16x16x32_bf16 v[132:135], v[156:159], v[116:119], v[132:135]\n"
      "s_waitcnt lgkmcnt(4)\n"
      "s_barrier\n"
      "v_mfma_f32_16x16x32_bf16 v[136:139], v[160:163], v[36:39], v[20:23]\n"
      "ds_read_b128 v[36:39], v232 offset:24832\n"
      "v_mfma_f32_16x16x32_bf16 v[136:139], v[164:167], v[40:43], v[136:139]\n"
      "ds_read_b128 v[40:43], v233 offset:24832\n"
      "v_mfma_f32_16x16x32_bf16 v[136:139], v[180:183], v[44:47], v[136:139]\n"
      "ds_read_b128 v[44:47], v234 offset:24832\n"
      "v_mfma_f32_16x16x32_bf16 v[136:139], v[184:187], v[60:63], v[136:139]\n"
      "ds_read_b128 v[60:63], v235 offset:24832\n"
      "v_cvt_pk_bf16_f32 v208, v132, v133\n"
      "v_mfma_f32_16x16x32_bf16 v[136:139], v[188:191], v[64:67], v[136:139]\n"
      "ds_read_b128 v[64:67], v232 offset:33024\n"
      "v_cvt_pk_bf16_f32 v209, v134, v135\n"
      "v_mfma_f32_16x16x32_bf16 v[136:139], v[204:207], v[68:71], v[136:139]\n"
      "ds_read_b128 v[68:71], v233 offset:33024\n"
      "v_mfma_f32_16x16x32_bf16 v[136:139], a[24:27], v[84:87], v[136:139]\n"
      "ds_read_b128 v[84:87], v234 offset:33024\n"
      "v_mfma_f32_16x16x32_bf16 v[136:139], a[28:31], v[88:91], v[136:139]\n"
      "ds_read_b128 v[88:91], v235 offset:33024\n"
      "v_mfma_f32_16x16x32_bf16 v[136:139], a[32:35], v[92:95], v[136:139]\n"
      "ds_read_b128 v[92:95], v232 offset:41216\n"
      "v_mfma_f32_16x16x32_bf16 v[136:139], a[36:39], v[108:111], v[136:139]\n"
      "ds_read_b128 v[108:111], v233 offset:41216\n"
      "ds_write_b64 v244, v[208:209] offset:12416\n"
      "v_mfma_f32_16x16x32_bf16 v[136:139], a[40:43], v[112:115], v[136:139]\n"
      "ds_read_b128 v[112:115], v234 offset:41216\n"
      "v_mfma_f32_16x16x32_bf16 v[136:139], a[44:47], v[116:119], v[136:139]\n"
      "ds_read_b128 v[116:119], v235 offset:41216\n"
      "s_waitcnt lgkmcnt(9)\n"
      "v_mfma_f32_16x16x32_bf16 v[132:135], a[240:243], v[36:39], v[0:3]\n"
      "v_add_u32 v232, 0xc000, v232\n"
      "v_mfma_f32_16x16x32_bf16 v[132:135], a[244:247], v[40:43], v[132:135]\n"
      "v_add_u32 v233, 0xc000, v233\n"
      "v_mfma_f32_16x16x32_bf16 v[132:135], a[248:251], v[44:47], v[132:135]\n"
      "v_add_u32 v234, 0xc000, v234\n"
      "v_mfma_f32_16x16x32_bf16 v[132:135], a[252:255], v[60:63], v[132:135]\n"
      "v_add_u32 v235, 0xc000, v235\n"
      "s_waitcnt lgkmcnt(5)\n"
      "v_mfma_f32_16x16x32_bf16 v[132:135], a[48:51], v[64:67], v[132:135]\n"
      "v_cvt_pk_bf16_f32 v210, v136, v137\n"
      "v_mfma_f32_16x16x32_bf16 v[132:135], a[52:55], v[68:71], v[132:135]\n"
      "v_cvt_pk_bf16_f32 v211, v138, v139\n"
      "v_mfma_f32_16x16x32_bf16 v[132:135], a[56:59], v[84:87], v[132:135]\n"
      "ds_read_b128 v[216:219], v246 offset:0\n"
      "v_mfma_f32_16x16x32_bf16 v[132:135], a[60:63], v[88:91], v[132:135]\n"
      "ds_read_b128 v[220:223], v246 offset:1024\n"
      "s_waitcnt lgkmcnt(2)\n"
      "v_mfma_f32_16x16x32_bf16 v[132:135], a[64:67], v[92:95], v[132:135]\n"
      "ds_read_b128 v[224:227], v246 offset:2048\n"
      "s_cbranch_vccnz LT34\n"
      "LS34:\n"
      "v_mfma_f32_16x16x32_bf16 v[132:135], a[68:71], v[108:111], v[132:135]\n"
      "v_lshlrev_b32 v0, 16, v120\n"
      "v_mfma_f32_16x16x32_bf16 v[132:135], a[72:75], v[112:115], v[132:135]\n"
      "ds_write_b64 v244, v[210:211] offset:12424\n"
      "v_mfma_f32_16x16x32_bf16 v[132:135], a[76:79], v[116:119], v[132:135]\n"
      "v_and_b32 v1, 0xffff0000, v120\n"
      "v_mfma_f32_16x16x32_bf16 v[136:139], a[80:83], v[36:39], v[4:7]\n"
      "v_lshlrev_b32 v2, 16, v121\n"
      "v_mfma_f32_16x16x32_bf16 v[136:139], a[84:87], v[40:43], v[136:139]\n"
      "v_and_b32 v3, 0xffff0000, v121\n"
      "v_mfma_f32_16x16x32_bf16 v[136:139], a[88:91], v[44:47], v[136:139]\n"
      "v_mfma_f32_16x16x32_bf16 v[136:139], a[92:95], v[60:63], v[136:139]\n"
      "v_cvt_pk_bf16_f32 v208, v132, v133\n"
      "v_mfma_f32_16x16x32_bf16 v[136:139], a[96:99], v[64:67], v[136:139]\n"
      "v_cvt_pk_bf16_f32 v209, v134, v135\n"
      "v_mfma_f32_16x16x32_bf16 v[136:139], a[100:103], v[68:71], v[136:139]\n"
      "s_waitcnt lgkmcnt(3)\n"
      "v_mfma_f32_16x16x32_bf16 v[136:139], a[104:107], v[84:87], v[136:139]\n"
      "global_store_dwordx4 v247, v[216:219], s[54:55] offset:768 sc1\n"
      "v_mfma_f32_16x16x32_bf16 v[136:139], a[108:111], v[88:91], v[136:139]\n"
      "s_add_u32 m0, s17, 0x1000\n"
      "v_mfma_f32_16x16x32_bf16 v[136:139], a[112:115], v[92:95], v[136:139]\n"
      "global_load_lds_dwordx4 v237, s[52:53] offset:0 nt\n"
      "v_mfma_f32_16x16x32_bf16 v[136:139], a[116:119], v[108:111], v[136:139]\n"
      "ds_write_b64 v244, v[208:209] offset:24576\n"
      "v_mfma_f32_16x16x32_bf16 v[136:139], a[120:123], v[112:115], v[136:139]\n"
      "v_lshlrev_b32 v4, 16, v122\n"
      "v_mfma_f32_16x16x32_bf16 v[136:139], a[124:127], v[116:119], v[136:139]\n"
      "v_and_b32 v5, 0xffff0000, v122\n"
      "v_mfma_f32_16x16x32_bf16 v[132:135], a[128:131], v[36:39], v[8:11]\n"
      "v_lshlrev_b32 v6, 16, v123\n"
      "v_mfma_f32_16x16x32_bf16 v[132:135], a[132:135], v[40:43], v[132:135]\n"
      "v_and_b32 v7, 0xffff0000, v123\n"
      "v_mfma_f32_16x16x32_bf16 v[132:135], a[136:139], v[44:47], v[132:135]\n"
      "v_mfma_f32_16x16x32_bf16 v[132:135], a[140:143], v[60:63], v[132:135]\n"
      "v_cvt_pk_bf16_f32 v210, v136, v137\n"
      "v_mfma_f32_16x16x32_bf16 v[132:135], a[144:147], v[64:67], v[132:135]\n"
      "v_cvt_pk_bf16_f32 v211, v138, v139\n"
      "v_mfma_f32_16x16x32_bf16 v[132:135], a[148:151], v[68:71], v[132:135]\n"
      "v_lshlrev_b32 v8, 16, v124\n"
      "v_mfma_f32_16x16x32_bf16 v[132:135], a[152:155], v[84:87], v[132:135]\n"
      "s_waitcnt lgkmcnt(3)\n"
      "v_mfma_f32_16x16x32_bf16 v[132:135], a[156:159], v[88:91], v[132:135]\n"
      "global_store_dwordx4 v252, v[220:223], s[54:55] offset:768 sc1\n"
      "v_mfma_f32_16x16x32_bf16 v[132:135], a[160:163], v[92:95], v[132:135]\n"
      "s_add_u32 m0, s17, 0x2f00\n"
      "v_mfma_f32_16x16x32_bf16 v[132:135], a[164:167], v[108:111], v[132:135]\n"
      "ds_write_b64 v244, v[210:211] offset:24584\n"
      "v_mfma_f32_16x16x32_bf16 v[132:135], a[168:171], v[112:115], v[132:135]\n"
      "global_load_lds_dwordx4 v237, s[52:53] offset:256 nt\n"
      "v_mfma_f32_16x16x32_bf16 v[132:135], a[172:175], v[116:119], v[132:135]\n"
      "v_and_b32 v9, 0xffff0000, v124\n"
      "v_mfma_f32_16x16x32_bf16 v[136:139], a[176:179], v[36:39], v[12:15]\n"
      "v_lshlrev_b32 v10, 16, v125\n"
      "v_mfma_f32_16x16x32_bf16 v[136:139], a[180:183], v[40:43], v[136:139]\n"
      "v_and_b32 v11, 0xffff0000, v125\n"
      "v_mfma_f32_16x16x32_bf16 v[136:139], a[184:187], v[44:47], v[136:139]\n"
      "v_mfma_f32_16x16x32_bf16 v[136:139], a[188:191], v[60:63], v[136:139]\n"
      "v_cvt_pk_bf16_f32 v212, v132, v133\n"
      "v_mfma_f32_16x16x32_bf16 v[136:139], a[192:195], v[64:67], v[136:139]\n"
      "v_cvt_pk_bf16_f32 v213, v134, v135\n"
      "v_mfma_f32_16x16x32_bf16 v[136:139], a[196:199], v[68:71], v[136:139]\n"
      "v_lshlrev_b32 v12, 16, v126\n"
      "v_mfma_f32_16x16x32_bf16 v[136:139], a[200:203], v[84:87], v[136:139]\n"
      "v_and_b32 v13, 0xffff0000, v126\n"
      "v_mfma_f32_16x16x32_bf16 v[136:139], a[204:207], v[88:91], v[136:139]\n"
      "s_waitcnt lgkmcnt(3)\n"
      "v_mfma_f32_16x16x32_bf16 v[136:139], a[208:211], v[92:95], v[136:139]\n"
      "global_store_dwordx4 v253, v[224:227], s[54:55] offset:768 sc1\n"
      "s_cbranch_vccnz LT35\n"
      "LS35:\n"
      "v_mfma_f32_16x16x32_bf16 v[136:139], a[212:215], v[108:111], v[136:139]\n"
      "ds_write_b64 v245, v[212:213] offset:24576\n"
      "v_mfma_f32_16x16x32_bf16 v[136:139], a[216:219], v[112:115], v[136:139]\n"
      "s_add_u32 m0, s17, 0x4e00\n"
      "v_mfma_f32_16x16x32_bf16 v[136:139], a[220:223], v[116:119], v[136:139]\n"
      "global_load_lds_dwordx4 v237, s[52:53] offset:512 nt\n"
      "v_mfma_f32_16x16x32_bf16 v[132:135], a[224:227], v[36:39], v[16:19]\n"
      "v_lshlrev_b32 v14, 16, v127\n"
      "v_mfma_f32_16x16x32_bf16 v[132:135], a[228:231], v[40:43], v[132:135]\n"
      "v_and_b32 v15, 0xffff0000, v127\n"
      "v_mfma_f32_16x16x32_bf16 v[132:135], a[232:235], v[44:47], v[132:135]\n"
      "v_mfma_f32_16x16x32_bf16 v[132:135], a[236:239], v[60:63], v[132:135]\n"
      "s_memrealtime s[84:85]\n"
      "v_mfma_f32_16x16x32_bf16 v[132:135], a[0:3], v[64:67], v[132:135]\n"
      "v_cvt_pk_bf16_f32 v214, v136, v137\n"
      "v_mfma_f32_16x16x32_bf16 v[132:135], a[4:7], v[68:71], v[132:135]\n"
      "v_cvt_pk_bf16_f32 v215, v138, v139\n"
      "v_mfma_f32_16x16x32_bf16 v[132:135], a[8:11], v[84:87], v[132:135]\n"
      "v_lshlrev_b32 v16, 16, v128\n"
      "v_mfma_f32_16x16x32_bf16 v[132:135], a[12:15], v[88:91], v[132:135]\n"
      "s_add_u32 s83, s83, s86\n"
      "v_mfma_f32_16x16x32_bf16 v[132:135], a[16:19], v[92:95], v[132:135]\n"
      "v_and_b32 v17, 0xffff0000, v128\n"
      "v_mfma_f32_16x16x32_bf16 v[132:135], a[20:23], v[108:111], v[132:135]\n"
      "v_lshlrev_b32 v18, 16, v129\n"
      "v_mfma_f32_16x16x32_bf16 v[132:135], v[140:143], v[112:115], v[132:135]\n"
      "s_waitcnt lgkmcnt(0)\n"
      "s_mov_b32 s66, s84\n"
      "s_sub_u32 s84, s84, s83\n"
      "v_mfma_f32_16x16x32_bf16 v[132:135], v[156:159], v[116:119], v[132:135]\n"
      "s_cmp_lt_i32 s84, 0\n"
      "s_cselect_b64 vcc, -1, 0\n"
      "s_waitcnt vmcnt(24)\n"
      "s_barrier\n"
      "v_mfma_f32_16x16x32_bf16 v[136:139], v[160:163], v[36:39], v[20:23]\n"
      "ds_read_b128 v[36:39], v232 offset:0\n"
      "ds_write_b64 v245, v[214:215] offset:24584\n"
      "v_mfma_f32_16x16x32_bf16 v[136:139], v[164:167], v[40:43], v[136:139]\n"
      "ds_read_b128 v[40:43], v233 offset:0\n"
      "v_mfma_f32_16x16x32_bf16 v[136:139], v[180:183], v[44:47], v[136:139]\n"
      "ds_read_b128 v[44:47], v234 offset:0\n"
      "v_mfma_f32_16x16x32_bf16 v[136:139], v[184:187], v[60:63], v[136:139]\n"
      "ds_read_b128 v[60:63], v235 offset:0\n"
      "v_cvt_pk_bf16_f32 v208, v132, v133\n"
      "v_mfma_f32_16x16x32_bf16 v[136:139], v[188:191], v[64:67], v[136:139]\n"
      "ds_read_b128 v[64:67], v232 offset:8192\n"
      "v_cvt_pk_bf16_f32 v209, v134, v135\n"
      "v_mfma_f32_16x16x32_bf16 v[136:139], v[204:207], v[68:71], v[136:139]\n"
      "ds_read_b128 v[68:71], v233 offset:8192\n"
      "v_mfma_f32_16x16x32_bf16 v[136:139], a[24:27], v[84:87], v[136:139]\n"
      "ds_read_b128 v[84:87], v234 offset:8192\n"
      "v_mfma_f32_16x16x32_bf16 v[136:139], a[28:31], v[88:91], v[136:139]\n"
      "ds_read_b128 v[88:91], v235 offset:8192\n"
      "v_mfma_f32_16x16x32_bf16 v[136:139], a[32:35], v[92:95], v[136:139]\n"
      "ds_read_b128 v[92:95], v232 offset:16384\n"
      "v_mfma_f32_16x16x32_bf16 v[136:139], a[36:39], v[108:111], v[136:139]\n"
      "ds_read_b128 v[108:111], v233 offset:16384\n"
      "ds_write_b64 v244, v[208:209] offset:24704\n"
      "v_mfma_f32_16x16x32_bf16 v[136:139], a[40:43], v[112:115], v[136:139]\n"
      "ds_read_b128 v[112:115], v234 offset:16384\n"
      "v_mfma_f32_16x16x32_bf16 v[136:139], a[44:47], v[116:119], v[136:139]\n"
      "ds_read_b128 v[116:119], v235 offset:16384\n"
      "s_waitcnt lgkmcnt(9)\n"
      "v_mfma_f32_16x16x32_bf16 v[132:135], a[240:243], v[36:39], v[0:3]\n"
      "s_sub_u32 s69, s66, s68\n"
      "v_mfma_f32_16x16x32_bf16 v[132:135], a[244:247], v[40:43], v[132:135]\n"
      "s_mov_b32 s68, s67\n"
      "v_mfma_f32_16x16x32_bf16 v[132:135], a[248:251], v[44:47], v[132:135]\n"
      "s_mov_b32 s67, s66\n"
      "v_mfma_f32_16x16x32_bf16 v[132:135], a[252:255], v[60:63], v[132:135]\n"
      "s_cmp_gt_i32 s69, 460\n"
      "s_cselect_b64 s[70:71], -1, s[70:71]\n"
      "s_waitcnt lgkmcnt(5)\n"
      "v_mfma_f32_16x16x32_bf16 v[132:135], a[48:51], v[64:67], v[132:135]\n"
      "s_or_b64 vcc, vcc, s[70:71]\n"
      "v_cvt_pk_bf16_f32 v210, v136, v137\n"
      "v_mfma_f32_16x16x32_bf16 v[132:135], a[52:55], v[68:71], v[132:135]\n"
      "v_and_b32 v19, 0xffff0000, v129\n"
      "v_cvt_pk_bf16_f32 v211, v138, v139\n"
      "v_mfma_f32_16x16x32_bf16 v[132:135], a[56:59], v[84:87], v[132:135]\n"
      "v_lshlrev_b32 v20, 16, v130\n"
      "v_mfma_f32_16x16x32_bf16 v[132:135], a[60:63], v[88:91], v[132:135]\n"
      "v_and_b32 v21, 0xffff0000, v130\n"
      "s_waitcnt lgkmcnt(0)\n"
      "v_mfma_f32_16x16x32_bf16 v[132:135], a[64:67], v[92:95], v[132:135]\n"
      "v_lshlrev_b32 v22, 16, v131\n"
      "s_cbranch_vccnz LT36\n"
      "LS36:\n"
      "v_mfma_f32_16x16x32_bf16 v[132:135], a[68:71], v[108:111], v[132:135]\n"
      "v_and_b32 v23, 0xffff0000, v131\n"
      "v_mfma_f32_16x16x32_bf16 v[132:135], a[72:75], v[112:115], v[132:135]\n"
      "ds_write_b64 v244, v[210:211] offset:24712\n"
      "v_mfma_f32_16x16x32_bf16 v[132:135], a[76:79], v[116:119], v[132:135]\n"
      "s_add_u32 s54, s44, s36\n"
      "s_addc_u32 s55, s45, 0\n"
      "v_mfma_f32_16x16x32_bf16 v[136:139], a[80:83], v[36:39], v[4:7]\n"
      "ds_read_b128 v[216:219], v246 offset:12288\n"
      "v_mfma_f32_16x16x32_bf16 v[136:139], a[84:87], v[40:43], v[136:139]\n"
      "ds_read_b128 v[220:223], v246 offset:13312\n"
      "v_mfma_f32_16x16x32_bf16 v[136:139], a[88:91], v[44:47], v[136:139]\n"
      "ds_read_b128 v[224:227], v246 offset:14336\n"
      "v_mfma_f32_16x16x32_bf16 v[136:139], a[92:95], v[60:63], v[136:139]\n"
      "v_cvt_pk_bf16_f32 v208, v132, v133\n"
      "v_mfma_f32_16x16x32_bf16 v[136:139], a[96:99], v[64:67], v[136:139]\n"
      "v_cvt_pk_bf16_f32 v209, v134, v135\n"
      "v_mfma_f32_16x16x32_bf16 v[136:139], a[100:103], v[68:71], v[136:139]\n"
      "s_waitcnt lgkmcnt(2)\n"
      "v_mfma_f32_16x16x32_bf16 v[136:139], a[104:107], v[84:87], v[136:139]\n"
      "global_store_dwordx4 v247, v[216:219], s[54:55] offset:0 sc1\n"
      "v_mfma_f32_16x16x32_bf16 v[136:139], a[108:111], v[88:91], v[136:139]\n"
      "s_add_u32 s52, s42, s38\n"
      "s_addc_u32 s53, s43, 0\n"
      "v_mfma_f32_16x16x32_bf16 v[136:139], a[112:115], v[92:95], v[136:139]\n"
      "s_add_u32 m0, s17, 0x6000\n"
      "v_mfma_f32_16x16x32_bf16 v[136:139], a[116:119], v[108:111], v[136:139]\n"
      "ds_write_b64 v244, v[208:209] offset:0\n"
      "v_mfma_f32_16x16x32_bf16 v[136:139], a[120:123], v[112:115], v[136:139]\n"
      "global_load_lds_dwordx4 v236, s[52:53] offset:0 nt\n"
      "v_mfma_f32_16x16x32_bf16 v[136:139], a[124:127], v[116:119], v[136:139]\n"
      "v_mfma_f32_16x16x32_bf16 v[132:135], a[128:131], v[36:39], v[8:11]\n"
      "v_mfma_f32_16x16x32_bf16 v[132:135], a[132:135], v[40:43], v[132:135]\n"
      "v_mfma_f32_16x16x32_bf16 v[132:135], a[136:139], v[44:47], v[132:135]\n"
      "v_mfma_f32_16x16x32_bf16 v[132:135], a[140:143], v[60:63], v[132:135]\n"
      "v_cvt_pk_bf16_f32 v210, v136, v137\n"
      "v_mfma_f32_16x16x32_bf16 v[132:135], a[144:147], v[64:67], v[132:135]\n"
      "v_cvt_pk_bf16_f32 v211, v138, v139\n"
      "v_mfma_f32_16x16x32_bf16 v[132:135], a[148:151], v[68:71], v[132:135]\n"
      "v_mfma_f32_16x16x32_bf16 v[132:135], a[152:155], v[84:87], v[132:135]\n"
      "s_waitcnt lgkmcnt(2)\n"
      "v_mfma_f32_16x16x32_bf16 v[132:135], a[156:159], v[88:91], v[132:135]\n"
      "global_store_dwordx4 v252, v[220:223], s[54:55] offset:0 sc1\n"
      "v_mfma_f32_16x16x32_bf16 v[132:135], a[160:163], v[92:95], v[132:135]\n"
      "s_add_u32 m0, s17, 0x7f00\n"
      "v_mfma_f32_16x16x32_bf16 v[132:135], a[164:167], v[108:111], v[132:135]\n"
      "ds_write_b64 v244, v[210:211] offset:8\n"
      "v_mfma_f32_16x16x32_bf16 v[132:135], a[168:171], v[112:115], v[132:135]\n"
      "global_load_lds_dwordx4 v236, s[52:53] offset:256 nt\n"
      "v_mfma_f32_16x16x32_bf16 v[132:135], a[172:175], v[116:119], v[132:135]\n"
      "v_mfma_f32_16x16x32_bf16 v[136:139], a[176:179], v[36:39], v[12:15]\n"
      "v_mfma_f32_16x16x32_bf16 v[136:139], a[180:183], v[40:43], v[136:139]\n"
      "v_mfma_f32_16x16x32_bf16 v[136:139], a[184:187], v[44:47], v[136:139]\n"
      "v_mfma_f32_16x16x32_bf16 v[136:139], a[188:191], v[60:63], v[136:139]\n"
      "v_cvt_pk_bf16_f32 v212, v132, v133\n"
      "v_mfma_f32_16x16x32_bf16 v[136:139], a[192:195], v[64:67], v[136:139]\n"
      "v_cvt_pk_bf16_f32 v213, v134, v135\n"
      "v_mfma_f32_16x16x32_bf16 v[136:139], a[196:199], v[68:71], v[136:139]\n"
      "v_mfma_f32_16x16x32_bf16 v[136:139], a[200:203], v[84:87], v[136:139]\n"
      "v_mfma_f32_16x16x32_bf16 v[136:139], a[204:207], v[88:91], v[136:139]\n"
      "s_waitcnt lgkmcnt(2)\n"
      "v_mfma_f32_16x16x32_bf16 v[136:139], a[208:211], v[92:95], v[136:139]\n"
      "global_store_dwordx4 v253, v[224:227], s[54:55] offset:0 sc1\n"
      "s_cbranch_vccnz LT37\n"
      "LS37:\n"
      "v_mfma_f32_16x16x32_bf16 v[136:139], a[212:215], v[108:111], v[136:139]\n"
      "ds_write_b64 v245, v[212:213] offset:0\n"
      "v_mfma_f32_16x16x32_bf16 v[136:139], a[216:219], v[112:115], v[136:139]\n"
      "s_add_u32 m0, s17, 0x9e00\n"
      "v_mfma_f32_16x16x32_bf16 v[136:139], a[220:223], v[116:119], v[136:139]\n"
      "global_load_lds_dwordx4 v236, s[52:53] offset:512 nt\n"
      "v_mfma_f32_16x16x32_bf16 v[132:135], a[224:227], v[36:39], v[16:19]\n"
      "v_mfma_f32_16x16x32_bf16 v[132:135], a[228:231], v[40:43], v[132:135]\n"
      "v_mfma_f32_16x16x32_bf16 v[132:135], a[232:235], v[44:47], v[132:135]\n"
      "v_mfma_f32_16x16x32_bf16 v[132:135], a[236:239], v[60:63], v[132:135]\n"
      "v_cvt_pk_bf16_f32 v214, v136, v137\n"
      "v_mfma_f32_16x16x32_bf16 v[132:135], a[0:3], v[64:67], v[132:135]\n"
      "v_cvt_pk_bf16_f32 v215, v138, v139\n"
      "v_mfma_f32_16x16x32_bf16 v[132:135], a[4:7], v[68:71], v[132:135]\n"
      "v_mfma_f32_16x16x32_bf16 v[132:135], a[8:11], v[84:87], v[132:135]\n"
      "v_mfma_f32_16x16x32_bf16 v[132:135], a[12:15], v[88:91], v[132:135]\n"
      "v_mfma_f32_16x16x32_bf16 v[132:135], a[16:19], v[92:95], v[132:135]\n"
      "v_mfma_f32_16x16x32_bf16 v[132:135], a[20:23], v[108:111], v[132:135]\n"
      "ds_write_b64 v245, v[214:215] offset:8\n"
      "v_mfma_f32_16x16x32_bf16 v[132:135], v[140:143], v[112:115], v[132:135]\n"
      "v_mfma_f32_16x16x32_bf16 v[132:135], v[156:159], v[116:119], v[132:135]\n"
      "s_barrier\n"
      "v_mfma_f32_16x16x32_bf16 v[136:139], v[160:163], v[36:39], v[20:23]\n"
      "ds_read_b128 v[36:39], v232 offset:256\n"
      "v_mfma_f32_16x16x32_bf16 v[136:139], v[164:167], v[40:43], v[136:139]\n"
      "ds_read_b128 v[40:43], v233 offset:256\n"
      "v_mfma_f32_16x16x32_bf16 v[136:139], v[180:183], v[44:47], v[136:139]\n"
      "ds_read_b128 v[44:47], v234 offset:256\n"
      "v_mfma_f32_16x16x32_bf16 v[136:139], v[184:187], v[60:63], v[136:139]\n"
      "ds_read_b128 v[60:63], v235 offset:256\n"
      "v_cvt_pk_bf16_f32 v208, v132, v133\n"
      "v_mfma_f32_16x16x32_bf16 v[136:139], v[188:191], v[64:67], v[136:139]\n"
      "ds_read_b128 v[64:67], v232 offset:8448\n"
      "v_cvt_pk_bf16_f32 v209, v134, v135\n"
      "v_mfma_f32_16x16x32_bf16 v[136:139], v[204:207], v[68:71], v[136:139]\n"
      "ds_read_b128 v[68:71], v233 offset:8448\n"
      "v_mfma_f32_16x16x32_bf16 v[136:139], a[24:27], v[84:87], v[136:139]\n"
      "ds_read_b128 v[84:87], v234 offset:8448\n"
      "v_mfma_f32_16x16x32_bf16 v[136:139], a[28:31], v[88:91], v[136:139]\n"
      "ds_read_b128 v[88:91], v235 offset:8448\n"
      "v_mfma_f32_16x16x32_bf16 v[136:139], a[32:35], v[92:95], v[136:139]\n"
      "ds_read_b128 v[92:95], v232 offset:16640\n"
      "v_mfma_f32_16x16x32_bf16 v[136:139], a[36:39], v[108:111], v[136:139]\n"
      "ds_read_b128 v[108:111], v233 offset:16640\n"
      "v_mfma_f32_16x16x32_bf16 v[136:139], a[40:43], v[112:115], v[136:139]\n"
      "ds_read_b128 v[112:115], v234 offset:16640\n"
      "v_mfma_f32_16x16x32_bf16 v[136:139], a[44:47], v[116:119], v[136:139]\n"
      "ds_read_b128 v[116:119], v235 offset:16640\n"
      "s_waitcnt lgkmcnt(8)\n"
      "v_mfma_f32_16x16x32_bf16 v[132:135], a[240:243], v[36:39], v[0:3]\n"
      "ds_write_b64 v244, v[208:209] offset:128\n"
      "v_mfma_f32_16x16x32_bf16 v[132:135], a[244:247], v[40:43], v[132:135]\n"
      "v_mfma_f32_16x16x32_bf16 v[132:135], a[248:251], v[44:47], v[132:135]\n"
      "v_mfma_f32_16x16x32_bf16 v[132:135], a[252:255], v[60:63], v[132:135]\n"
      "v_cvt_pk_bf16_f32 v210, v136, v137\n"
      "s_waitcnt lgkmcnt(5)\n"
      "v_mfma_f32_16x16x32_bf16 v[132:135], a[48:51], v[64:67], v[132:135]\n"
      "v_cvt_pk_bf16_f32 v211, v138, v139\n"
      "v_mfma_f32_16x16x32_bf16 v[132:135], a[52:55], v[68:71], v[132:135]\n"
      "v_mfma_f32_16x16x32_bf16 v[132:135], a[56:59], v[84:87], v[132:135]\n"
      "ds_read_b128 v[216:219], v246 offset:24576\n"
      "v_mfma_f32_16x16x32_bf16 v[132:135], a[60:63], v[88:91], v[132:135]\n"
      "ds_read_b128 v[220:223], v246 offset:25600\n"
      "s_waitcnt lgkmcnt(3)\n"
      "v_mfma_f32_16x16x32_bf16 v[132:135], a[64:67], v[92:95], v[132:135]\n"
      "ds_read_b128 v[224:227], v246 offset:26624\n"
      "v_mfma_f32_16x16x32_bf16 v[132:135], a[68:71], v[108:111], v[132:135]\n"
      "ds_write_b64 v244, v[210:211] offset:136\n"
      "v_mfma_f32_16x16x32_bf16 v[132:135], a[72:75], v[112:115], v[132:135]\n"
      "s_cbranch_vccnz LT38\n"
      "LS38:\n"
      "v_mfma_f32_16x16x32_bf16 v[132:135], a[76:79], v[116:119], v[132:135]\n"
      "v_mfma_f32_16x16x32_bf16 v[136:139], a[80:83], v[36:39], v[4:7]\n"
      "v_mfma_f32_16x16x32_bf16 v[136:139], a[84:87], v[40:43], v[136:139]\n"
      "v_mfma_f32_16x16x32_bf16 v[136:139], a[88:91], v[44:47], v[136:139]\n"
      "v_mfma_f32_16x16x32_bf16 v[136:139], a[92:95], v[60:63], v[136:139]\n"
      "v_cvt_pk_bf16_f32 v208, v132, v133\n"
      "v_mfma_f32_16x16x32_bf16 v[136:139], a[96:99], v[64:67], v[136:139]\n"
      "v_cvt_pk_bf16_f32 v209, v134, v135\n"
      "v_mfma_f32_16x16x32_bf16 v[136:139], a[100:103], v[68:71], v[136:139]\n"
      "s_waitcnt lgkmcnt(3)\n"
      "v_mfma_f32_16x16x32_bf16 v[136:139], a[104:107], v[84:87], v[136:139]\n"
      "global_store_dwordx4 v247, v[216:219], s[54:55] offset:768 sc1\n"
      "v_mfma_f32_16x16x32_bf16 v[136:139], a[108:111], v[88:91], v[136:139]\n"
      "s_add_u32 m0, s17, 0x7000\n"
      "v_mfma_f32_16x16x32_bf16 v[136:139], a[112:115], v[92:95], v[136:139]\n"
      "global_load_lds_dwordx4 v237, s[52:53] offset:0 nt\n"
      "v_mfma_f32_16x16x32_bf16 v[136:139], a[116:119], v[108:111], v[136:139]\n"
      "ds_write_b64 v244, v[208:209] offset:12288\n"
      "v_mfma_f32_16x16x32_bf16 v[136:139], a[120:123], v[112:115], v[136:139]\n"
      "v_mfma_f32_16x16x32_bf16 v[136:139], a[124:127], v[116:119], v[136:139]\n"
      "v_mfma_f32_16x16x32_bf16 v[132:135], a[128:131], v[36:39], v[8:11]\n"
      "v_mfma_f32_16x16x32_bf16 v[132:135], a[132:135], v[40:43], v[132:135]\n"
      "v_mfma_f32_16x16x32_bf16 v[132:135], a[136:139], v[44:47], v[132:135]\n"
      "v_mfma_f32_16x16x32_bf16 v[132:135], a[140:143], v[60:63], v[132:135]\n"
      "v_cvt_pk_bf16_f32 v210, v136, v137\n"
      "v_mfma_f32_16x16x32_bf16 v[132:135], a[144:147], v[64:67], v[132:135]\n"
      "v_cvt_pk_bf16_f32 v211, v138, v139\n"
      "v_mfma_f32_16x16x32_bf16 v[132:135], a[148:151], v[68:71], v[132:135]\n"
      "v_mfma_f32_16x16x32_bf16 v[132:135], a[152:155], v[84:87], v[132:135]\n"
      "s_waitcnt lgkmcnt(3)\n"
      "v_mfma_f32_16x16x32_bf16 v[132:135], a[156:159], v[88:91], v[132:135]\n"
      "global_store_dwordx4 v252, v[220:223], s[54:55] offset:768 sc1\n"
      "v_mfma_f32_16x16x32_bf16 v[132:135], a[160:163], v[92:95], v[132:135]\n"
      "s_add_u32 m0, s17, 0x8f00\n"
      "v_mfma_f32_16x16x32_bf16 v[132:135], a[164:167], v[108:111], v[132:135]\n"
      "ds_write_b64 v244, v[210:211] offset:12296\n"
      "v_mfma_f32_16x16x32_bf16 v[132:135], a[168:171], v[112:115], v[132:135]\n"
      "global_load_lds_dwordx4 v237, s[52:53] offset:256 nt\n"
      "v_mfma_f32_16x16x32_bf16 v[132:135], a[172:175], v[116:119], v[132:135]\n"
      "v_mfma_f32_16x16x32_bf16 v[136:139], a[176:179], v[36:39], v[12:15]\n"
      "v_mfma_f32_16x16x32_bf16 v[136:139], a[180:183], v[40:43], v[136:139]\n"
      "v_mfma_f32_16x16x32_bf16 v[136:139], a[184:187], v[44:47], v[136:139]\n"
      "v_mfma_f32_16x16x32_bf16 v[136:139], a[188:191], v[60:63], v[136:139]\n"
      "v_cvt_pk_bf16_f32 v212, v132, v133\n"
      "v_mfma_f32_16x16x32_bf16 v[136:139], a[192:195], v[64:67], v[136:139]\n"
      "v_cvt_pk_bf16_f32 v213, v134, v135\n"
      "v_mfma_f32_16x16x32_bf16 v[136:139], a[196:199], v[68:71], v[136:139]\n"
      "v_mfma_f32_16x16x32_bf16 v[136:139], a[200:203], v[84:87], v[136:139]\n"
      "v_mfma_f32_16x16x32_bf16 v[136:139], a[204:207], v[88:91], v[136:139]\n"
      "s_waitcnt lgkmcnt(3)\n"
      "v_mfma_f32_16x16x32_bf16 v[136:139], a[208:211], v[92:95], v[136:139]\n"
      "global_store_dwordx4 v253, v[224:227], s[54:55] offset:768 sc1\n"
      "s_cbranch_vccnz LT39\n"
      "LS39:\n"
      "v_mfma_f32_16x16x32_bf16 v[136:139], a[212:215], v[108:111], v[136:139]\n"
      "ds_write_b64 v245, v[212:213] offset:12288\n"
      "v_mfma_f32_16x16x32_bf16 v[136:139], a[216:219], v[112:115], v[136:139]\n"
      "s_add_u32 m0, s17, 0xae00\n"
      "v_mfma_f32_16x16x32_bf16 v[136:139], a[220:223], v[116:119], v[136:139]\n"
      "global_load_lds_dwordx4 v237, s[52:53] offset:512 nt\n"
      "v_mfma_f32_16x16x32_bf16 v[132:135], a[224:227], v[36:39], v[16:19]\n"
      "v_mfma_f32_16x16x32_bf16 v[132:135], a[228:231], v[40:43], v[132:135]\n"
      "v_mfma_f32_16x16x32_bf16 v[132:135], a[232:235], v[44:47], v[132:135]\n"
      "v_mfma_f32_16x16x32_bf16 v[132:135], a[236:239], v[60:63], v[132:135]\n"
      "s_memrealtime s[84:85]\n"
      "v_mfma_f32_16x16x32_bf16 v[132:135], a[0:3], v[64:67], v[132:135]\n"
      "v_cvt_pk_bf16_f32 v214, v136, v137\n"
      "v_mfma_f32_16x16x32_bf16 v[132:135], a[4:7], v[68:71], v[132:135]\n"
      "v_cvt_pk_bf16_f32 v215, v138, v139\n"
      "v_mfma_f32_16x16x32_bf16 v[132:135], a[8:11], v[84:87], v[132:135]\n"
      "v_mfma_f32_16x16x32_bf16 v[132:135], a[12:15], v[88:91], v[132:135]\n"
      "s_add_u32 s83, s83, s86\n"
      "v_mfma_f32_16x16x32_bf16 v[132:135], a[16:19], v[92:95], v[132:135]\n"
      "v_mfma_f32_16x16x32_bf16 v[132:135], a[20:23], v[108:111], v[132:135]\n"
      "v_mfma_f32_16x16x32_bf16 v[132:135], v[140:143], v[112:115], v[132:135]\n"
      "s_waitcnt lgkmcnt(0)\n"
      "s_mov_b32 s66, s84\n"
      "s_sub_u32 s84, s84, s83\n"
      "v_mfma_f32_16x16x32_bf16 v[132:135], v[156:159], v[116:119], v[132:135]\n"
      "s_cmp_lt_i32 s84, 0\n"
      "s_cselect_b64 vcc, -1, 0\n"
      "s_waitcnt vmcnt(24)\n"
      "s_barrier\n"
      "v_mfma_f32_16x16x32_bf16 v[136:139], v[160:163], v[36:39], v[20:23]\n"
      "ds_read_b128 v[36:39], v232 offset:24576\n"
      "ds_write_b64 v245, v[214:215] offset:12296\n"
      "v_mfma_f32_16x16x32_bf16 v[136:139], v[164:167], v[40:43], v[136:139]\n"
      "ds_read_b128 v[40:43], v233 offset:24576\n"
      "s_lshl_b32 s79, s94, 1\n"
      "v_mfma_f32_16x16x32_bf16 v[136:139], v[180:183], v[44:47], v[136:139]\n"
      "ds_read_b128 v[44:47], v234 offset:24576\n"
      "v_mad_i32_i24 v252, v251, s79, v252\n"
      "v_mfma_f32_16x16x32_bf16 v[136:139], v[184:187], v[60:63], v[136:139]\n"
      "ds_read_b128 v[60:63], v235 offset:24576\n"
      "v_cvt_pk_bf16_f32 v208, v132, v133\n"
      "v_mfma_f32_16x16x32_bf16 v[136:139], v[188:191], v[64:67], v[136:139]\n"
      "ds_read_b128 v[64:67], v232 offset:32768\n"
      "v_cvt_pk_bf16_f32 v209, v134, v135\n"
      "v_mfma_f32_16x16x32_bf16 v[136:139], v[204:207], v[68:71], v[136:139]\n"
      "ds_read_b128 v[68:71], v233 offset:32768\n"
      "s_mul_i32 s79, s79, s100\n"
      "v_mfma_f32_16x16x32_bf16 v[136:139], a[24:27], v[84:87], v[136:139]\n"
      "ds_read_b128 v[84:87], v234 offset:32768\n"
      "v_add_u32 v247, s79, v247\n"
      "v_mfma_f32_16x16x32_bf16 v[136:139], a[28:31], v[88:91], v[136:139]\n"
      "ds_read_b128 v[88:91], v235 offset:32768\n"
      "s_mul_i32 s79, s79, s101\n"
      "v_mfma_f32_16x16x32_bf16 v[136:139], a[32:35], v[92:95], v[136:139]\n"
      "ds_read_b128 v[92:95], v232 offset:40960\n"
      "v_add_u32 v253, s79, v253\n"
      "v_mfma_f32_16x16x32_bf16 v[136:139], a[36:39], v[108:111], v[136:139]\n"
      "ds_read_b128 v[108:111], v233 offset:40960\n"
      "ds_write_b64 v244, v[208:209] offset:12416\n"
      "v_mfma_f32_16x16x32_bf16 v[136:139], a[40:43], v[112:115], v[136:139]\n"
      "ds_read_b128 v[112:115], v234 offset:40960\n"
      "v_mfma_f32_16x16x32_bf16 v[136:139], a[44:47], v[116:119], v[136:139]\n"
      "ds_read_b128 v[116:119], v235 offset:40960\n"
      "s_waitcnt lgkmcnt(9)\n"
      "v_mfma_f32_16x16x32_bf16 v[132:135], a[240:243], v[36:39], v[0:3]\n"
      "s_sub_u32 s69, s66, s68\n"
      "v_mfma_f32_16x16x32_bf16 v[132:135], a[244:247], v[40:43], v[132:135]\n"
      "s_mov_b32 s68, s67\n"
      "v_mfma_f32_16x16x32_bf16 v[132:135], a[248:251], v[44:47], v[132:135]\n"
      "s_mov_b32 s67, s66\n"
      "v_mfma_f32_16x16x32_bf16 v[132:135], a[252:255], v[60:63], v[132:135]\n"
      "s_cmp_gt_i32 s69, 460\n"
      "s_cselect_b64 s[70:71], -1, s[70:71]\n"
      "s_waitcnt lgkmcnt(5)\n"
      "v_mfma_f32_16x16x32_bf16 v[132:135], a[48:51], v[64:67], v[132:135]\n"
      "s_or_b64 vcc, vcc, s[70:71]\n"
      "s_mul_i32 s78, s96, s19\n"
      "v_mfma_f32_16x16x32_bf16 v[132:135], a[52:55], v[68:71], v[132:135]\n"
      "v_cvt_pk_bf16_f32 v210, v136, v137\n"
      "v_add_u32 v236, s78, v236\n"
      "v_mfma_f32_16x16x32_bf16 v[132:135], a[56:59], v[84:87], v[132:135]\n"
      "v_cvt_pk_bf16_f32 v211, v138, v139\n"
      "s_mul_i32 s78, s78, s99\n"
      "v_mfma_f32_16x16x32_bf16 v[132:135], a[60:63], v[88:91], v[132:135]\n"
      "s_add_u32 s54, s30, s37\n"
      "s_addc_u32 s55, s31, 0\n"
      "s_waitcnt lgkmcnt(0)\n"
      "v_mfma_f32_16x16x32_bf16 v[132:135], a[64:67], v[92:95], v[132:135]\n"
      "ds_read_b128 v[216:219], v246 offset:0\n"
      "v_add_u32 v237, s78, v237\n"
      "v_mfma_f32_16x16x32_bf16 v[132:135], a[68:71], v[108:111], v[132:135]\n"
      "ds_read_b128 v[220:223], v246 offset:1024\n"
      "s_cbranch_vccnz LT40\n"
      "LS40:\n"
      "v_mfma_f32_16x16x32_bf16 v[132:135], a[72:75], v[112:115], v[132:135]\n"
      "ds_read_b128 v[224:227], v246 offset:2048\n"
      "v_mfma_f32_16x16x32_bf16 v[132:135], a[76:79], v[116:119], v[132:135]\n"
      "ds_write_b64 v244, v[210:211] offset:12424\n"
      "v_mfma_f32_16x16x32_bf16 v[136:139], a[80:83], v[36:39], v[4:7]\n"
      "v_mfma_f32_16x16x32_bf16 v[136:139], a[84:87], v[40:43], v[136:139]\n"
      "v_mfma_f32_16x16x32_bf16 v[136:139], a[88:91], v[44:47], v[136:139]\n"
      "v_mfma_f32_16x16x32_bf16 v[136:139], a[92:95], v[60:63], v[136:139]\n"
      "v_cvt_pk_bf16_f32 v208, v132, v133\n"
      "v_mfma_f32_16x16x32_bf16 v[136:139], a[96:99], v[64:67], v[136:139]\n"
      "v_cvt_pk_bf16_f32 v209, v134, v135\n"
      "LPD13:\n"
      "s_cbranch_execz LPD16\n"
      "v_mfma_f32_16x16x32_bf16 v[136:139], a[100:103], v[68:71], v[136:139]\n"
      "s_waitcnt lgkmcnt(3)\n"
      "v_mfma_f32_16x16x32_bf16 v[136:139], a[104:107], v[84:87], v[136:139]\n"
      "global_store_dwordx4 v247, v[216:219], s[54:55] offset:0 sc1\n"
      "v_mfma_f32_16x16x32_bf16 v[136:139], a[108:111], v[88:91], v[136:139]\n"
      "s_add_u32 s52, s20, s39\n"
      "s_addc_u32 s53, s21, 0\n"
      "v_mfma_f32_16x16x32_bf16 v[136:139], a[112:115], v[92:95], v[136:139]\n"
      "s_add_u32 m0, s17, 0xc000\n"
      "v_mfma_f32_16x16x32_bf16 v[136:139], a[116:119], v[108:111], v[136:139]\n"
      "ds_write_b64 v244, v[208:209] offset:24576\n"
      "v_mfma_f32_16x16x32_bf16 v[136:139], a[120:123], v[112:115], v[136:139]\n"
      "global_load_lds_dwordx4 v236, s[52:53] offset:0 nt\n"
      "v_mfma_f32_16x16x32_bf16 v[136:139], a[124:127], v[116:119], v[136:139]\n"
      "v_mfma_f32_16x16x32_bf16 v[132:135], a[128:131], v[36:39], v[8:11]\n"
      "v_mfma_f32_16x16x32_bf16 v[132:135], a[132:135], v[40:43], v[132:135]\n"
      "v_mfma_f32_16x16x32_bf16 v[132:135], a[136:139], v[44:47], v[132:135]\n"
      "v_mfma_f32_16x16x32_bf16 v[132:135], a[140:143], v[60:63], v[132:135]\n"
      "v_cvt_pk_bf16_f32 v210, v136, v137\n"
      "v_mfma_f32_16x16x32_bf16 v[132:135], a[144:147], v[64:67], v[132:135]\n"
      "v_cvt_pk_bf16_f32 v211, v138, v139\n"
      "v_mfma_f32_16x16x32_bf16 v[132:135], a[148:151], v[68:71], v[132:135]\n"
      "v_mfma_f32_16x16x32_bf16 v[132:135], a[152:155], v[84:87], v[132:135]\n"
      "s_waitcnt lgkmcnt(3)\n"
      "v_mfma_f32_16x16x32_bf16 v[132:135], a[156:159], v[88:91], v[132:135]\n"
      "global_store_dwordx4 v252, v[220:223], s[54:55] offset:0 sc1\n"
      "v_mfma_f32_16x16x32_bf16 v[132:135], a[160:163], v[92:95], v[132:135]\n"
      "s_add_u32 m0, s17, 0xdf00\n"
      "v_mfma_f32_16x16x32_bf16 v[132:135], a[164:167], v[108:111], v[132:135]\n"
      "ds_write_b64 v244, v[210:211] offset:24584\n"
      "v_mfma_f32_16x16x32_bf16 v[132:135], a[168:171], v[112:115], v[132:135]\n"
      "global_load_lds_dwordx4 v236, s[52:53] offset:256 nt\n"
      "v_mfma_f32_16x16x32_bf16 v[132:135], a[172:175], v[116:119], v[132:135]\n"
      "v_mfma_f32_16x16x32_bf16 v[136:139], a[176:179], v[36:39], v[12:15]\n"
      "v_mfma_f32_16x16x32_bf16 v[136:139], a[180:183], v[40:43], v[136:139]\n"
      "v_mfma_f32_16x16x32_bf16 v[136:139], a[184:187], v[44:47], v[136:139]\n"
      "v_mfma_f32_16x16x32_bf16 v[136:139], a[188:191], v[60:63], v[136:139]\n"
      "v_cvt_pk_bf16_f32 v212, v132, v133\n"
      "v_mfma_f32_16x16x32_bf16 v[136:139], a[192:195], v[64:67], v[136:139]\n"
      "v_cvt_pk_bf16_f32 v213, v134, v135\n"
      "v_mfma_f32_16x16x32_bf16 v[136:139], a[196:199], v[68:71], v[136:139]\n"
      "v_mfma_f32_16x16x32_bf16 v[136:139], a[200:203], v[84:87], v[136:139]\n"
      "v_mfma_f32_16x16x32_bf16 v[136:139], a[204:207], v[88:91], v[136:139]\n"
      "s_waitcnt lgkmcnt(3)\n"
      "v_mfma_f32_16x16x32_bf16 v[136:139], a[208:211], v[92:95], v[136:139]\n"
      "global_store_dwordx4 v253, v[224:227], s[54:55] offset:0 sc1\n"
      "s_cbranch_vccnz LT41\n"
      "LS41:\n"
      "v_mfma_f32_16x16x32_bf16 v[136:139], a[212:215], v[108:111], v[136:139]\n"
      "ds_write_b64 v245, v[212:213] offset:24576\n"
      "v_mfma_f32_16x16x32_bf16 v[136:139], a[216:219], v[112:115], v[136:139]\n"
      "s_add_u32 m0, s17, 0xfe00\n"
      "v_mfma_f32_16x16x32_bf16 v[136:139], a[220:223], v[116:119], v[136:139]\n"
      "global_load_lds_dwordx4 v236, s[52:53] offset:512 nt\n"
      "v_mfma_f32_16x16x32_bf16 v[132:135], a[224:227], v[36:39], v[16:19]\n"
      "v_mfma_f32_16x16x32_bf16 v[132:135], a[228:231], v[40:43], v[132:135]\n"
      "v_mfma_f32_16x16x32_bf16 v[132:135], a[232:235], v[44:47], v[132:135]\n"
      "v_mfma_f32_16x16x32_bf16 v[132:135], a[236:239], v[60:63], v[132:135]\n"
      "v_cvt_pk_bf16_f32 v214, v136, v137\n"
      "v_mfma_f32_16x16x32_bf16 v[132:135], a[0:3], v[64:67], v[132:135]\n"
      "v_cvt_pk_bf16_f32 v215, v138, v139\n"
      "v_mfma_f32_16x16x32_bf16 v[132:135], a[4:7], v[68:71], v[132:135]\n"
      "v_mfma_f32_16x16x32_bf16 v[132:135], a[8:11], v[84:87], v[132:135]\n"
      "v_mfma_f32_16x16x32_bf16 v[132:135], a[12:15], v[88:91], v[132:135]\n"
      "v_mfma_f32_16x16x32_bf16 v[132:135], a[16:19], v[92:95], v[132:135]\n"
      "v_mfma_f32_16x16x32_bf16 v[132:135], a[20:23], v[108:111], v[132:135]\n"
      "ds_write_b64 v245, v[214:215] offset:24584\n"
      "v_mfma_f32_16x16x32_bf16 v[132:135], v[140:143], v[112:115], v[132:135]\n"
      "v_mfma_f32_16x16x32_bf16 v[132:135], v[156:159], v[116:119], v[132:135]\n"
      "s_waitcnt lgkmcnt(4)\n"
      "s_barrier\n"
      "v_mfma_f32_16x16x32_bf16 v[136:139], v[160:163], v[36:39], v[20:23]\n"
      "ds_read_b128 v[36:39], v232 offset:24832\n"
      "v_mfma_f32_16x16x32_bf16 v[136:139], v[164:167], v[40:43], v[136:139]\n"
      "ds_read_b128 v[40:43], v233 offset:24832\n"
      "v_mfma_f32_16x16x32_bf16 v[136:139], v[180:183], v[44:47], v[136:139]\n"
      "ds_read_b128 v[44:47], v234 offset:24832\n"
      "v_mfma_f32_16x16x32_bf16 v[136:139], v[184:187], v[60:63], v[136:139]\n"
      "ds_read_b128 v[60:63], v235 offset:24832\n"
      "v_cvt_pk_bf16_f32 v208, v132, v133\n"
      "v_mfma_f32_16x16x32_bf16 v[136:139], v[188:191], v[64:67], v[136:139]\n"
      "ds_read_b128 v[64:67], v232 offset:33024\n"
      "v_cvt_pk_bf16_f32 v209, v134, v135\n"
      "v_mfma_f32_16x16x32_bf16 v[136:139], v[204:207], v[68:71], v[136:139]\n"
      "ds_read_b128 v[68:71], v233 offset:33024\n"
      "v_mfma_f32_16x16x32_bf16 v[136:139], a[24:27], v[84:87], v[136:139]\n"
      "ds_read_b128 v[84:87], v234 offset:33024\n"
      "v_mfma_f32_16x16x32_bf16 v[136:139], a[28:31], v[88:91], v[136:139]\n"
      "ds_read_b128 v[88:91], v235 offset:33024\n"
      "v_mfma_f32_16x16x32_bf16 v[136:139], a[32:35], v[92:95], v[136:139]\n"
      "ds_read_b128 v[92:95], v232 offset:41216\n"
      "v_mfma_f32_16x16x32_bf16 v[136:139], a[36:39], v[108:111], v[136:139]\n"
      "ds_read_b128 v[108:111], v233 offset:41216\n"
      "ds_write_b64 v244, v[208:209] offset:24704\n"
      "v_mfma_f32_16x16x32_bf16 v[136:139], a[40:43], v[112:115], v[136:139]\n"
      "ds_read_b128 v[112:115], v234 offset:41216\n"
      "v_mfma_f32_16x16x32_bf16 v[136:139], a[44:47], v[116:119], v[136:139]\n"
      "ds_read_b128 v[116:119], v235 offset:41216\n"
      "s_waitcnt lgkmcnt(9)\n"
      "v_mfma_f32_16x16x32_bf16 v[132:135], a[240:243], v[36:39], v[0:3]\n"
      "v_add_u32 v232, 0xffff4000, v232\n"
      "v_mfma_f32_16x16x32_bf16 v[132:135], a[244:247], v[40:43], v[132:135]\n"
      "v_add_u32 v233, 0xffff4000, v233\n"
      "v_mfma_f32_16x16x32_bf16 v[132:135], a[248:251], v[44:47], v[132:135]\n"
      "v_add_u32 v234, 0xffff4000, v234\n"
      "v_mfma_f32_16x16x32_bf16 v[132:135], a[252:255], v[60:63], v[132:135]\n"
      "v_add_u32 v235, 0xffff4000, v235\n"
      "s_waitcnt lgkmcnt(5)\n"
      "v_mfma_f32_16x16x32_bf16 v[132:135], a[48:51], v[64:67], v[132:135]\n"
      "v_cvt_pk_bf16_f32 v210, v136, v137\n"
      "v_mfma_f32_16x16x32_bf16 v[132:135], a[52:55], v[68:71], v[132:135]\n"
      "v_cvt_pk_bf16_f32 v211, v138, v139\n"
      "v_mfma_f32_16x16x32_bf16 v[132:135], a[56:59], v[84:87], v[132:135]\n"
      "ds_read_b128 v[216:219], v246 offset:12288\n"
      "v_mfma_f32_16x16x32_bf16 v[132:135], a[60:63], v[88:91], v[132:135]\n"
      "ds_read_b128 v[220:223], v246 offset:13312\n"
      "s_waitcnt lgkmcnt(2)\n"
      "v_mfma_f32_16x16x32_bf16 v[132:135], a[64:67], v[92:95], v[132:135]\n"
      "ds_read_b128 v[224:227], v246 offset:14336\n"
      "s_cbranch_vccnz LT42\n"
      "LS42:\n"
      "v_mfma_f32_16x16x32_bf16 v[132:135], a[68:71], v[108:111], v[132:135]\n"
      "v_lshlrev_b32 v0, 16, v144\n"
      "v_mfma_f32_16x16x32_bf16 v[132:135], a[72:75], v[112:115], v[132:135]\n"
      "ds_write_b64 v244, v[210:211] offset:24712\n"
      "v_mfma_f32_16x16x32_bf16 v[132:135], a[76:79], v[116:119], v[132:135]\n"
      "v_and_b32 v1, 0xffff0000, v144\n"
      "v_mfma_f32_16x16x32_bf16 v[136:139], a[80:83], v[36:39], v[4:7]\n"
      "v_lshlrev_b32 v2, 16, v145\n"
      "v_mfma_f32_16x16x32_bf16 v[136:139], a[84:87], v[40:43], v[136:139]\n"
      "v_and_b32 v3, 0xffff0000, v145\n"
      "v_mfma_f32_16x16x32_bf16 v[136:139], a[88:91], v[44:47], v[136:139]\n"
      "v_mfma_f32_16x16x32_bf16 v[136:139], a[92:95], v[60:63], v[136:139]\n"
      "v_cvt_pk_bf16_f32 v208, v132, v133\n"
      "v_mfma_f32_16x16x32_bf16 v[136:139], a[96:99], v[64:67], v[136:139]\n"
      "v_cvt_pk_bf16_f32 v209, v134, v135\n"
      "v_mfma_f32_16x16x32_bf16 v[136:139], a[100:103], v[68:71], v[136:139]\n"
      "s_waitcnt lgkmcnt(3)\n"
      "v_mfma_f32_16x16x32_bf16 v[136:139], a[104:107], v[84:87], v[136:139]\n"
      "global_store_dwordx4 v247, v[216:219], s[54:55] offset:768 sc1\n"
      "v_mfma_f32_16x16x32_bf16 v[136:139], a[108:111], v[88:91], v[136:139]\n"
      "s_add_u32 m0, s17, 0xd000\n"
      "v_mfma_f32_16x16x32_bf16 v[136:139], a[112:115], v[92:95], v[136:139]\n"
      "global_load_lds_dwordx4 v237, s[52:53] offset:0 nt\n"
      "v_mfma_f32_16x16x32_bf16 v[136:139], a[116:119], v[108:111], v[136:139]\n"
      "ds_write_b64 v244, v[208:209] offset:0\n"
      "v_mfma_f32_16x16x32_bf16 v[136:139], a[120:123], v[112:115], v[136:139]\n"
      "v_lshlrev_b32 v4, 16, v146\n"
      "v_mfma_f32_16x16x32_bf16 v[136:139], a[124:127], v[116:119], v[136:139]\n"
      "v_and_b32 v5, 0xffff0000, v146\n"
      "v_mfma_f32_16x16x32_bf16 v[132:135], a[128:131], v[36:39], v[8:11]\n"
      "v_lshlrev_b32 v6, 16, v147\n"
      "v_mfma_f32_16x16x32_bf16 v[132:135], a[132:135], v[40:43], v[132:135]\n"
      "v_and_b32 v7, 0xffff0000, v147\n"
      "v_mfma_f32_16x16x32_bf16 v[132:135], a[136:139], v[44:47], v[132:135]\n"
      "v_mfma_f32_16x16x32_bf16 v[132:135], a[140:143], v[60:63], v[132:135]\n"
      "v_cvt_pk_bf16_f32 v210, v136, v137\n"
      "v_mfma_f32_16x16x32_bf16 v[132:135], a[144:147], v[64:67], v[132:135]\n"
      "v_cvt_pk_bf16_f32 v211, v138, v139\n"
      "v_mfma_f32_16x16x32_bf16 v[132:135], a[148:151], v[68:71], v[132:135]\n"
      "v_lshlrev_b32 v8, 16, v148\n"
      "v_mfma_f32_16x16x32_bf16 v[132:135], a[152:155], v[84:87], v[132:135]\n"
      "s_waitcnt lgkmcnt(3)\n"
      "v_mfma_f32_16x16x32_bf16 v[132:135], a[156:159], v[88:91], v[132:135]\n"
      "global_store_dwordx4 v252, v[220:223], s[54:55] offset:768 sc1\n"
      "v_mfma_f32_16x16x32_bf16 v[132:135], a[160:163], v[92:95], v[132:135]\n"
      "s_add_u32 m0, s17, 0xef00\n"
      "v_mfma_f32_16x16x32_bf16 v[132:135], a[164:167], v[108:111], v[132:135]\n"
      "ds_write_b64 v244, v[210:211] offset:8\n"
      "v_mfma_f32_16x16x32_bf16 v[132:135], a[168:171], v[112:115], v[132:135]\n"
      "global_load_lds_dwordx4 v237, s[52:53] offset:256 nt\n"
      "v_mfma_f32_16x16x32_bf16 v[132:135], a[172:175], v[116:119], v[132:135]\n"
      "v_and_b32 v9, 0xffff0000, v148\n"
      "v_mfma_f32_16x16x32_bf16 v[136:139], a[176:179], v[36:39], v[12:15]\n"
      "v_lshlrev_b32 v10, 16, v149\n"
      "v_mfma_f32_16x16x32_bf16 v[136:139], a[180:183], v[40:43], v[136:139]\n"
      "v_and_b32 v11, 0xffff0000, v149\n"
      "v_mfma_f32_16x16x32_bf16 v[136:139], a[184:187], v[44:47], v[136:139]\n"
      "v_mfma_f32_16x16x32_bf16 v[136:139], a[188:191], v[60:63], v[136:139]\n"
      "v_cvt_pk_bf16_f32 v212, v132, v133\n"
      "v_mfma_f32_16x16x32_bf16 v[136:139], a[192:195], v[64:67], v[136:139]\n"
      "v_cvt_pk_bf16_f32 v213, v134, v135\n"
      "v_mfma_f32_16x16x32_bf16 v[136:139], a[196:199], v[68:71], v[136:139]\n"
      "v_lshlrev_b32 v12, 16, v150\n"
      "v_mfma_f32_16x16x32_bf16 v[136:139], a[200:203], v[84:87], v[136:139]\n"
      "v_and_b32 v13, 0xffff0000, v150\n"
      "v_mfma_f32_16x16x32_bf16 v[136:139], a[204:207], v[88:91], v[136:139]\n"
      "s_waitcnt lgkmcnt(3)\n"
      "v_mfma_f32_16x16x32_bf16 v[136:139], a[208:211], v[92:95], v[136:139]\n"
      "global_store_dwordx4 v253, v[224:227], s[54:55] offset:768 sc1\n"
      "s_cbranch_vccnz LT43\n"
      "LS43:\n"
      "v_mfma_f32_16x16x32_bf16 v[136:139], a[212:215], v[108:111], v[136:139]\n"
      "ds_write_b64 v245, v[212:213] offset:0\n"
      "v_mfma_f32_16x16x32_bf16 v[136:139], a[216:219], v[112:115], v[136:139]\n"
      "s_add_u32 m0, s17, 0x10e00\n"
      "v_mfma_f32_16x16x32_bf16 v[136:139], a[220:223], v[116:119], v[136:139]\n"
      "global_load_lds_dwordx4 v237, s[52:53] offset:512 nt\n"
      "v_mfma_f32_16x16x32_bf16 v[132:135], a[224:227], v[36:39], v[16:19]\n"
      "v_lshlrev_b32 v14, 16, v151\n"
      "v_mfma_f32_16x16x32_bf16 v[132:135], a[228:231], v[40:43], v[132:135]\n"
      "v_and_b32 v15, 0xffff0000, v151\n"
      "v_mfma_f32_16x16x32_bf16 v[132:135], a[232:235], v[44:47], v[132:135]\n"
      "v_mfma_f32_16x16x32_bf16 v[132:135], a[236:239], v[60:63], v[132:135]\n"
      "s_memrealtime s[84:85]\n"
      "v_mfma_f32_16x16x32_bf16 v[132:135], a[0:3], v[64:67], v[132:135]\n"
      "v_cvt_pk_bf16_f32 v214, v136, v137\n"
      "v_mfma_f32_16x16x32_bf16 v[132:135], a[4:7], v[68:71], v[132:135]\n"
      "v_cvt_pk_bf16_f32 v215, v138, v139\n"
      "v_mfma_f32_16x16x32_bf16 v[132:135], a[8:11], v[84:87], v[132:135]\n"
      "v_lshlrev_b32 v16, 16, v152\n"
      "v_mfma_f32_16x16x32_bf16 v[132:135], a[12:15], v[88:91], v[132:135]\n"
      "s_add_u32 s83, s83, s86\n"
      "v_mfma_f32_16x16x32_bf16 v[132:135], a[16:19], v[92:95], v[132:135]\n"
      "v_and_b32 v17, 0xffff0000, v152\n"
      "v_mfma_f32_16x16x32_bf16 v[132:135], a[20:23], v[108:111], v[132:135]\n"
      "v_lshlrev_b32 v18, 16, v153\n"
      "v_mfma_f32_16x16x32_bf16 v[132:135], v[140:143], v[112:115], v[132:135]\n"
      "s_waitcnt lgkmcnt(0)\n"
      "s_mov_b32 s66, s84\n"
      "s_sub_u32 s84, s84, s83\n"
      "v_mfma_f32_16x16x32_bf16 v[132:135], v[156:159], v[116:119], v[132:135]\n"
      "s_cmp_lt_i32 s84, 0\n"
      "s_cselect_b64 vcc, -1, 0\n"
      "s_waitcnt vmcnt(24)\n"
      "s_barrier\n"
      "v_mfma_f32_16x16x32_bf16 v[136:139], v[160:163], v[36:39], v[20:23]\n"
      "ds_read_b128 v[36:39], v232 offset:0\n"
      "ds_write_b64 v245, v[214:215] offset:8\n"
      "v_mfma_f32_16x16x32_bf16 v[136:139], v[164:167], v[40:43], v[136:139]\n"
      "ds_read_b128 v[40:43], v233 offset:0\n"
      "v_mfma_f32_16x16x32_bf16 v[136:139], v[180:183], v[44:47], v[136:139]\n"
      "ds_read_b128 v[44:47], v234 offset:0\n"
      "v_mfma_f32_16x16x32_bf16 v[136:139], v[184:187], v[60:63], v[136:139]\n"
      "ds_read_b128 v[60:63], v235 offset:0\n"
      "v_cvt_pk_bf16_f32 v208, v132, v133\n"
      "v_mfma_f32_16x16x32_bf16 v[136:139], v[188:191], v[64:67], v[136:139]\n"
      "ds_read_b128 v[64:67], v232 offset:8192\n"
      "v_cvt_pk_bf16_f32 v209, v134, v135\n"
      "v_mfma_f32_16x16x32_bf16 v[136:139], v[204:207], v[68:71], v[136:139]\n"
      "ds_read_b128 v[68:71], v233 offset:8192\n"
      "v_mfma_f32_16x16x32_bf16 v[136:139], a[24:27], v[84:87], v[136:139]\n"
      "ds_read_b128 v[84:87], v234 offset:8192\n"
      "v_mfma_f32_16x16x32_bf16 v[136:139], a[28:31], v[88:91], v[136:139]\n"
      "ds_read_b128 v[88:91], v235 offset:8192\n"
      "v_mfma_f32_16x16x32_bf16 v[136:139], a[32:35], v[92:95], v[136:139]\n"
      "ds_read_b128 v[92:95], v232 offset:16384\n"
      "v_mfma_f32_16x16x32_bf16 v[136:139], a[36:39], v[108:111], v[136:139]\n"
      "ds_read_b128 v[108:111], v233 offset:16384\n"
      "ds_write_b64 v244, v[208:209] offset:128\n"
      "v_mfma_f32_16x16x32_bf16 v[136:139], a[40:43], v[112:115], v[136:139]\n"
      "ds_read_b128 v[112:115], v234 offset:16384\n"
      "v_mfma_f32_16x16x32_bf16 v[136:139], a[44:47], v[116:119], v[136:139]\n"
      "ds_read_b128 v[116:119], v235 offset:16384\n"
      "s_waitcnt lgkmcnt(9)\n"
      "v_mfma_f32_16x16x32_bf16 v[132:135], a[240:243], v[36:39], v[0:3]\n"
      "s_sub_u32 s69, s66, s68\n"
      "v_mfma_f32_16x16x32_bf16 v[132:135], a[244:247], v[40:43], v[132:135]\n"
      "s_mov_b32 s68, s67\n"
      "v_mfma_f32_16x16x32_bf16 v[132:135], a[248:251], v[44:47], v[132:135]\n"
      "s_mov_b32 s67, s66\n"
      "v_mfma_f32_16x16x32_bf16 v[132:135], a[252:255], v[60:63], v[132:135]\n"
      "s_cmp_gt_i32 s69, 460\n"
      "s_cselect_b64 s[70:71], -1, s[70:71]\n"
      "s_waitcnt lgkmcnt(5)\n"
      "v_mfma_f32_16x16x32_bf16 v[132:135], a[48:51], v[64:67], v[132:135]\n"
      "s_or_b64 vcc, vcc, s[70:71]\n"
      "v_cvt_pk_bf16_f32 v210, v136, v137\n"
      "v_mfma_f32_16x16x32_bf16 v[132:135], a[52:55], v[68:71], v[132:135]\n"
      "v_and_b32 v19, 0xffff0000, v153\n"
      "v_cvt_pk_bf16_f32 v211, v138, v139\n"
      "v_mfma_f32_16x16x32_bf16 v[132:135], a[56:59], v[84:87], v[132:135]\n"
      "v_lshlrev_b32 v20, 16, v154\n"
      "v_mfma_f32_16x16x32_bf16 v[132:135], a[60:63], v[88:91], v[132:135]\n"
      "v_and_b32 v21, 0xffff0000, v154\n"
      "s_waitcnt lgkmcnt(0)\n"
      "v_mfma_f32_16x16x32_bf16 v[132:135], a[64:67], v[92:95], v[132:135]\n"
      "v_lshlrev_b32 v22, 16, v155\n"
      "s_cbranch_vccnz LT44\n"
      "LS44:\n"
      "v_mfma_f32_16x16x32_bf16 v[132:135], a[68:71], v[108:111], v[132:135]\n"
      "v_and_b32 v23, 0xffff0000, v155\n"
      "v_mfma_f32_16x16x32_bf16 v[132:135], a[72:75], v[112:115], v[132:135]\n"
      "ds_write_b64 v244, v[210:211] offset:136\n"
      "v_mfma_f32_16x16x32_bf16 v[132:135], a[76:79], v[116:119], v[132:135]\n"
      "s_add_u32 s54, s44, s37\n"
      "s_addc_u32 s55, s45, 0\n"
      "v_mfma_f32_16x16x32_bf16 v[136:139], a[80:83], v[36:39], v[4:7]\n"
      "ds_read_b128 v[216:219], v246 offset:24576\n"
      "v_mfma_f32_16x16x32_bf16 v[136:139], a[84:87], v[40:43], v[136:139]\n"
      "ds_read_b128 v[220:223], v246 offset:25600\n"
      "v_mfma_f32_16x16x32_bf16 v[136:139], a[88:91], v[44:47], v[136:139]\n"
      "ds_read_b128 v[224:227], v246 offset:26624\n"
      "v_mfma_f32_16x16x32_bf16 v[136:139], a[92:95], v[60:63], v[136:139]\n"
      "v_cvt_pk_bf16_f32 v208, v132, v133\n"
      "v_mfma_f32_16x16x32_bf16 v[136:139], a[96:99], v[64:67], v[136:139]\n"
      "v_cvt_pk_bf16_f32 v209, v134, v135\n"
      "v_mfma_f32_16x16x32_bf16 v[136:139], a[100:103], v[68:71], v[136:139]\n"
      "s_waitcnt lgkmcnt(2)\n"
      "v_mfma_f32_16x16x32_bf16 v[136:139], a[104:107], v[84:87], v[136:139]\n"
      "global_store_dwordx4 v247, v[216:219], s[54:55] offset:0 sc1\n"
      "v_mfma_f32_16x16x32_bf16 v[136:139], a[108:111], v[88:91], v[136:139]\n"
      "s_add_u32 s52, s42, s39\n"
      "s_addc_u32 s53, s43, 0\n"
      "v_mfma_f32_16x16x32_bf16 v[136:139], a[112:115], v[92:95], v[136:139]\n"
      "s_add_u32 m0, s17, 0x12000\n"
      "v_mfma_f32_16x16x32_bf16 v[136:139], a[116:119], v[108:111], v[136:139]\n"
      "ds_write_b64 v244, v[208:209] offset:12288\n"
      "v_mfma_f32_16x16x32_bf16 v[136:139], a[120:123], v[112:115], v[136:139]\n"
      "global_load_lds_dwordx4 v236, s[52:53] offset:0 nt\n"
      "v_mfma_f32_16x16x32_bf16 v[136:139], a[124:127], v[116:119], v[136:139]\n"
      "v_mfma_f32_16x16x32_bf16 v[132:135], a[128:131], v[36:39], v[8:11]\n"
      "v_mfma_f32_16x16x32_bf16 v[132:135], a[132:135], v[40:43], v[132:135]\n"
      "v_mfma_f32_16x16x32_bf16 v[132:135], a[136:139], v[44:47], v[132:135]\n"
      "v_mfma_f32_16x16x32_bf16 v[132:135], a[140:143], v[60:63], v[132:135]\n"
      "v_cvt_pk_bf16_f32 v210, v136, v137\n"
      "v_mfma_f32_16x16x32_bf16 v[132:135], a[144:147], v[64:67], v[132:135]\n"
      "v_cvt_pk_bf16_f32 v211, v138, v139\n"
      "v_mfma_f32_16x16x32_bf16 v[132:135], a[148:151], v[68:71], v[132:135]\n"
      "v_mfma_f32_16x16x32_bf16 v[132:135], a[152:155], v[84:87], v[132:135]\n"
      "s_waitcnt lgkmcnt(2)\n"
      "v_mfma_f32_16x16x32_bf16 v[132:135], a[156:159], v[88:91], v[132:135]\n"
      "global_store_dwordx4 v252, v[220:223], s[54:55] offset:0 sc1\n"
      "v_mfma_f32_16x16x32_bf16 v[132:135], a[160:163], v[92:95], v[132:135]\n"
      "s_add_u32 m0, s17, 0x13f00\n"
      "v_mfma_f32_16x16x32_bf16 v[132:135], a[164:167], v[108:111], v[132:135]\n"
      "ds_write_b64 v244, v[210:211] offset:12296\n"
      "v_mfma_f32_16x16x32_bf16 v[132:135], a[168:171], v[112:115], v[132:135]\n"
      "global_load_lds_dwordx4 v236, s[52:53] offset:256 nt\n"
      "v_mfma_f32_16x16x32_bf16 v[132:135], a[172:175], v[116:119], v[132:135]\n"
      "v_mfma_f32_16x16x32_bf16 v[136:139], a[176:179], v[36:39], v[12:15]\n"
      "v_mfma_f32_16x16x32_bf16 v[136:139], a[180:183], v[40:43], v[136:139]\n"
      "v_mfma_f32_16x16x32_bf16 v[136:139], a[184:187], v[44:47], v[136:139]\n"
      "v_mfma_f32_16x16x32_bf16 v[136:139], a[188:191], v[60:63], v[136:139]\n"
      "v_cvt_pk_bf16_f32 v212, v132, v133\n"
      "v_mfma_f32_16x16x32_bf16 v[136:139], a[192:195], v[64:67], v[136:139]\n"
      "v_cvt_pk_bf16_f32 v213, v134, v135\n"
      "v_mfma_f32_16x16x32_bf16 v[136:139], a[196:199], v[68:71], v[136:139]\n"
      "v_mfma_f32_16x16x32_bf16 v[136:139], a[200:203], v[84:87], v[136:139]\n"
      "v_mfma_f32_16x16x32_bf16 v[136:139], a[204:207], v[88:91], v[136:139]\n"
      "s_waitcnt lgkmcnt(2)\n"
      "v_mfma_f32_16x16x32_bf16 v[136:139], a[208:211], v[92:95], v[136:139]\n"
      "global_store_dwordx4 v253, v[224:227], s[54:55] offset:0 sc1\n"
      "s_cbranch_vccnz LT45\n"
      "LS45:\n"
      "v_mfma_f32_16x16x32_bf16 v[136:139], a[212:215], v[108:111], v[136:139]\n"
      "ds_write_b64 v245, v[212:213] offset:12288\n"
      "v_mfma_f32_16x16x32_bf16 v[136:139], a[216:219], v[112:115], v[136:139]\n"
      "s_add_u32 m0, s17, 0x15e00\n"
      "v_mfma_f32_16x16x32_bf16 v[136:139], a[220:223], v[116:119], v[136:139]\n"
      "global_load_lds_dwordx4 v236, s[52:53] offset:512 nt\n"
      "v_mfma_f32_16x16x32_bf16 v[132:135], a[224:227], v[36:39], v[16:19]\n"
      "v_mfma_f32_16x16x32_bf16 v[132:135], a[228:231], v[40:43], v[132:135]\n"
      "v_mfma_f32_16x16x32_bf16 v[132:135], a[232:235], v[44:47], v[132:135]\n"
      "v_mfma_f32_16x16x32_bf16 v[132:135], a[236:239], v[60:63], v[132:135]\n"
      "v_cvt_pk_bf16_f32 v214, v136, v137\n"
      "v_mfma_f32_16x16x32_bf16 v[132:135], a[0:3], v[64:67], v[132:135]\n"
      "v_cvt_pk_bf16_f32 v215, v138, v139\n"
      "v_mfma_f32_16x16x32_bf16 v[132:135], a[4:7], v[68:71], v[132:135]\n"
      "v_mfma_f32_16x16x32_bf16 v[132:135], a[8:11], v[84:87], v[132:135]\n"
      "v_mfma_f32_16x16x32_bf16 v[132:135], a[12:15], v[88:91], v[132:135]\n"
      "v_mfma_f32_16x16x32_bf16 v[132:135], a[16:19], v[92:95], v[132:135]\n"
      "v_mfma_f32_16x16x32_bf16 v[132:135], a[20:23], v[108:111], v[132:135]\n"
      "ds_write_b64 v245, v[214:215] offset:12296\n"
      "v_mfma_f32_16x16x32_bf16 v[132:135], v[140:143], v[112:115], v[132:135]\n"
      "v_mfma_f32_16x16x32_bf16 v[132:135], v[156:159], v[116:119], v[132:135]\n"
      "s_barrier\n"
      "v_mfma_f32_16x16x32_bf16 v[136:139], v[160:163], v[36:39], v[20:23]\n"
      "ds_read_b128 v[36:39], v232 offset:256\n"
      "v_mfma_f32_16x16x32_bf16 v[136:139], v[164:167], v[40:43], v[136:139]\n"
      "ds_read_b128 v[40:43], v233 offset:256\n"
      "v_mfma_f32_16x16x32_bf16 v[136:139], v[180:183], v[44:47], v[136:139]\n"
      "ds_read_b128 v[44:47], v234 offset:256\n"
      "v_mfma_f32_16x16x32_bf16 v[136:139], v[184:187], v[60:63], v[136:139]\n"
      "ds_read_b128 v[60:63], v235 offset:256\n"
      "v_cvt_pk_bf16_f32 v208, v132, v133\n"
      "v_mfma_f32_16x16x32_bf16 v[136:139], v[188:191], v[64:67], v[136:139]\n"
      "ds_read_b128 v[64:67], v232 offset:8448\n"
      "v_cvt_pk_bf16_f32 v209, v134, v135\n"
      "v_mfma_f32_16x16x32_bf16 v[136:139], v[204:207], v[68:71], v[136:139]\n"
      "ds_read_b128 v[68:71], v233 offset:8448\n"
      "v_mfma_f32_16x16x32_bf16 v[136:139], a[24:27], v[84:87], v[136:139]\n"
      "ds_read_b128 v[84:87], v234 offset:8448\n"
      "v_mfma_f32_16x16x32_bf16 v[136:139], a[28:31], v[88:91], v[136:139]\n"
      "ds_read_b128 v[88:91], v235 offset:8448\n"
      "v_mfma_f32_16x16x32_bf16 v[136:139], a[32:35], v[92:95], v[136:139]\n"
      "ds_read_b128 v[92:95], v232 offset:16640\n"
      "v_mfma_f32_16x16x32_bf16 v[136:139], a[36:39], v[108:111], v[136:139]\n"
      "ds_read_b128 v[108:111], v233 offset:16640\n"
      "v_mfma_f32_16x16x32_bf16 v[136:139], a[40:43], v[112:115], v[136:139]\n"
      "ds_read_b128 v[112:115], v234 offset:16640\n"
      "v_mfma_f32_16x16x32_bf16 v[136:139], a[44:47], v[116:119], v[136:139]\n"
      "ds_read_b128 v[116:119], v235 offset:16640\n"
      "s_waitcnt lgkmcnt(8)\n"
      "v_mfma_f32_16x16x32_bf16 v[132:135], a[240:243], v[36:39], v[0:3]\n"
      "ds_write_b64 v244, v[208:209] offset:12416\n"
      "v_mfma_f32_16x16x32_bf16 v[132:135], a[244:247], v[40:43], v[132:135]\n"
      "v_mfma_f32_16x16x32_bf16 v[132:135], a[248:251], v[44:47], v[132:135]\n"
      "v_mfma_f32_16x16x32_bf16 v[132:135], a[252:255], v[60:63], v[132:135]\n"
      "v_cvt_pk_bf16_f32 v210, v136, v137\n"
      "s_waitcnt lgkmcnt(5)\n"
      "v_mfma_f32_16x16x32_bf16 v[132:135], a[48:51], v[64:67], v[132:135]\n"
      "v_cvt_pk_bf16_f32 v211, v138, v139\n"
      "v_mfma_f32_16x16x32_bf16 v[132:135], a[52:55], v[68:71], v[132:135]\n"
      "v_mfma_f32_16x16x32_bf16 v[132:135], a[56:59], v[84:87], v[132:135]\n"
      "ds_read_b128 v[216:219], v246 offset:0\n"
      "v_mfma_f32_16x16x32_bf16 v[132:135], a[60:63], v[88:91], v[132:135]\n"
      "ds_read_b128 v[220:223], v246 offset:1024\n"
      "s_waitcnt lgkmcnt(3)\n"
      "v_mfma_f32_16x16x32_bf16 v[132:135], a[64:67], v[92:95], v[132:135]\n"
      "ds_read_b128 v[224:227], v246 offset:2048\n"
      "v_mfma_f32_16x16x32_bf16 v[132:135], a[68:71], v[108:111], v[132:135]\n"
      "ds_write_b64 v244, v[210:211] offset:12424\n"
      "v_mfma_f32_16x16x32_bf16 v[132:135], a[72:75], v[112:115], v[132:135]\n"
      "s_cbranch_vccnz LT46\n"
      "LS46:\n"
      "v_mfma_f32_16x16x32_bf16 v[132:135], a[76:79], v[116:119], v[132:135]\n"
      "v_mfma_f32_16x16x32_bf16 v[136:139], a[80:83], v[36:39], v[4:7]\n"
      "v_mfma_f32_16x16x32_bf16 v[136:139], a[84:87], v[40:43], v[136:139]\n"
      "v_mfma_f32_16x16x32_bf16 v[136:139], a[88:91], v[44:47], v[136:139]\n"
      "v_mfma_f32_16x16x32_bf16 v[136:139], a[92:95], v[60:63], v[136:139]\n"
      "v_cvt_pk_bf16_f32 v208, v132, v133\n"
      "v_mfma_f32_16x16x32_bf16 v[136:139], a[96:99], v[64:67], v[136:139]\n"
      "v_cvt_pk_bf16_f32 v209, v134, v135\n"
      "v_mfma_f32_16x16x32_bf16 v[136:139], a[100:103], v[68:71], v[136:139]\n"
      "s_waitcnt lgkmcnt(3)\n"
      "v_mfma_f32_16x16x32_bf16 v[136:139], a[104:107], v[84:87], v[136:139]\n"
      "global_store_dwordx4 v247, v[216:219], s[54:55] offset:768 sc1\n"
      "v_mfma_f32_16x16x32_bf16 v[136:139], a[108:111], v[88:91], v[136:139]\n"
      "s_add_u32 m0, s17, 0x13000\n"
      "v_mfma_f32_16x16x32_bf16 v[136:139], a[112:115], v[92:95], v[136:139]\n"
      "global_load_lds_dwordx4 v237, s[52:53] offset:0 nt\n"
      "v_mfma_f32_16x16x32_bf16 v[136:139], a[116:119], v[108:111], v[136:139]\n"
      "ds_write_b64 v244, v[208:209] offset:24576\n"
      "v_mfma_f32_16x16x32_bf16 v[136:139], a[120:123], v[112:115], v[136:139]\n"
      "v_mfma_f32_16x16x32_bf16 v[136:139], a[124:127], v[116:119], v[136:139]\n"
      "v_mfma_f32_16x16x32_bf16 v[132:135], a[128:131], v[36:39], v[8:11]\n"
      "v_mfma_f32_16x16x32_bf16 v[132:135], a[132:135], v[40:43], v[132:135]\n"
      "v_mfma_f32_16x16x32_bf16 v[132:135], a[136:139], v[44:47], v[132:135]\n"
      "v_mfma_f32_16x16x32_bf16 v[132:135], a[140:143], v[60:63], v[132:135]\n"
      "v_cvt_pk_bf16_f32 v210, v136, v137\n"
      "v_mfma_f32_16x16x32_bf16 v[132:135], a[144:147], v[64:67], v[132:135]\n"
      "v_cvt_pk_bf16_f32 v211, v138, v139\n"
      "v_mfma_f32_16x16x32_bf16 v[132:135], a[148:151], v[68:71], v[132:135]\n"
      "v_mfma_f32_16x16x32_bf16 v[132:135], a[152:155], v[84:87], v[132:135]\n"
      "s_waitcnt lgkmcnt(3)\n"
      "v_mfma_f32_16x16x32_bf16 v[132:135], a[156:159], v[88:91], v[132:135]\n"
      "global_store_dwordx4 v252, v[220:223], s[54:55] offset:768 sc1\n"
      "v_mfma_f32_16x16x32_bf16 v[132:135], a[160:163], v[92:95], v[132:135]\n"
      "s_add_u32 m0, s17, 0x14f00\n"
      "v_mfma_f32_16x16x32_bf16 v[132:135], a[164:167], v[108:111], v[132:135]\n"
      "ds_write_b64 v244, v[210:211] offset:24584\n"
      "v_mfma_f32_16x16x32_bf16 v[132:135], a[168:171], v[112:115], v[132:135]\n"
      "global_load_lds_dwordx4 v237, s[52:53] offset:256 nt\n"
      "v_mfma_f32_16x16x32_bf16 v[132:135], a[172:175], v[116:119], v[132:135]\n"
      "v_mfma_f32_16x16x32_bf16 v[136:139], a[176:179], v[36:39], v[12:15]\n"
      "v_mfma_f32_16x16x32_bf16 v[136:139], a[180:183], v[40:43], v[136:139]\n"
      "v_mfma_f32_16x16x32_bf16 v[136:139], a[184:187], v[44:47], v[136:139]\n"
      "v_mfma_f32_16x16x32_bf16 v[136:139], a[188:191], v[60:63], v[136:139]\n"
      "v_cvt_pk_bf16_f32 v212, v132, v133\n"
      "v_mfma_f32_16x16x32_bf16 v[136:139], a[192:195], v[64:67], v[136:139]\n"
      "v_cvt_pk_bf16_f32 v213, v134, v135\n"
      "v_mfma_f32_16x16x32_bf16 v[136:139], a[196:199], v[68:71], v[136:139]\n"
      "v_mfma_f32_16x16x32_bf16 v[136:139], a[200:203], v[84:87], v[136:139]\n"
      "v_mfma_f32_16x16x32_bf16 v[136:139], a[204:207], v[88:91], v[136:139]\n"
      "s_waitcnt lgkmcnt(3)\n"
      "v_mfma_f32_16x16x32_bf16 v[136:139], a[208:211], v[92:95], v[136:139]\n"
      "global_store_dwordx4 v253, v[224:227], s[54:55] offset:768 sc1\n"
      "s_cbranch_vccnz LT47\n"
      "LS47:\n"
      "v_mfma_f32_16x16x32_bf16 v[136:139], a[212:215], v[108:111], v[136:139]\n"
      "ds_write_b64 v245, v[212:213] offset:24576\n"
      "v_mfma_f32_16x16x32_bf16 v[136:139], a[216:219], v[112:115], v[136:139]\n"
      "s_add_u32 m0, s17, 0x16e00\n"
      "v_mfma_f32_16x16x32_bf16 v[136:139], a[220:223], v[116:119], v[136:139]\n"
      "global_load_lds_dwordx4 v237, s[52:53] offset:512 nt\n"
      "v_mfma_f32_16x16x32_bf16 v[132:135], a[224:227], v[36:39], v[16:19]\n"
      "v_mfma_f32_16x16x32_bf16 v[132:135], a[228:231], v[40:43], v[132:135]\n"
      "v_mfma_f32_16x16x32_bf16 v[132:135], a[232:235], v[44:47], v[132:135]\n"
      "v_mfma_f32_16x16x32_bf16 v[132:135], a[236:239], v[60:63], v[132:135]\n"
      "s_memrealtime s[84:85]\n"
      "v_mfma_f32_16x16x32_bf16 v[132:135], a[0:3], v[64:67], v[132:135]\n"
      "v_cvt_pk_bf16_f32 v214, v136, v137\n"
      "v_mfma_f32_16x16x32_bf16 v[132:135], a[4:7], v[68:71], v[132:135]\n"
      "v_cvt_pk_bf16_f32 v215, v138, v139\n"
      "v_mfma_f32_16x16x32_bf16 v[132:135], a[8:11], v[84:87], v[132:135]\n"
      "v_mfma_f32_16x16x32_bf16 v[132:135], a[12:15], v[88:91], v[132:135]\n"
      "s_add_u32 s83, s83, s86\n"
      "v_mfma_f32_16x16x32_bf16 v[132:135], a[16:19], v[92:95], v[132:135]\n"
      "v_mfma_f32_16x16x32_bf16 v[132:135], a[20:23], v[108:111], v[132:135]\n"
      "v_mfma_f32_16x16x32_bf16 v[132:135], v[140:143], v[112:115], v[132:135]\n"
      "s_waitcnt lgkmcnt(0)\n"
      "s_mov_b32 s66, s84\n"
      "s_sub_u32 s84, s84, s83\n"
      "v_mfma_f32_16x16x32_bf16 v[132:135], v[156:159], v[116:119], v[132:135]\n"
      "s_cmp_lt_i32 s84, 0\n"
      "s_cselect_b64 vcc, -1, 0\n"
      "s_waitcnt vmcnt(24)\n"
      "s_barrier\n"
      "v_mfma_f32_16x16x32_bf16 v[136:139], v[160:163], v[36:39], v[20:23]\n"
      "ds_read_b128 v[36:39], v232 offset:24576\n"
      "ds_write_b64 v245, v[214:215] offset:24584\n"
      "v_mfma_f32_16x16x32_bf16 v[136:139], v[164:167], v[40:43], v[136:139]\n"
      "ds_read_b128 v[40:43], v233 offset:24576\n"
      "v_mfma_f32_16x16x32_bf16 v[136:139], v[180:183], v[44:47], v[136:139]\n"
      "ds_read_b128 v[44:47], v234 offset:24576\n"
      "v_mfma_f32_16x16x32_bf16 v[136:139], v[184:187], v[60:63], v[136:139]\n"
      "ds_read_b128 v[60:63], v235 offset:24576\n"
      "v_cvt_pk_bf16_f32 v208, v132, v133\n"
      "v_mfma_f32_16x16x32_bf16 v[136:139], v[188:191], v[64:67], v[136:139]\n"
      "ds_read_b128 v[64:67], v232 offset:32768\n"
      "v_cvt_pk_bf16_f32 v209, v134, v135\n"
      "v_mfma_f32_16x16x32_bf16 v[136:139], v[204:207], v[68:71], v[136:139]\n"
      "ds_read_b128 v[68:71], v233 offset:32768\n"
      "v_mfma_f32_16x16x32_bf16 v[136:139], a[24:27], v[84:87], v[136:139]\n"
      "ds_read_b128 v[84:87], v234 offset:32768\n"
      "v_mfma_f32_16x16x32_bf16 v[136:139], a[28:31], v[88:91], v[136:139]\n"
      "ds_read_b128 v[88:91], v235 offset:32768\n"
      "v_mfma_f32_16x16x32_bf16 v[136:139], a[32:35], v[92:95], v[136:139]\n"
      "ds_read_b128 v[92:95], v232 offset:40960\n"
      "v_mfma_f32_16x16x32_bf16 v[136:139], a[36:39], v[108:111], v[136:139]\n"
      "ds_read_b128 v[108:111], v233 offset:40960\n"
      "ds_write_b64 v244, v[208:209] offset:24704\n"
      "v_mfma_f32_16x16x32_bf16 v[136:139], a[40:43], v[112:115], v[136:139]\n"
      "ds_read_b128 v[112:115], v234 offset:40960\n"
      "v_mfma_f32_16x16x32_bf16 v[136:139], a[44:47], v[116:119], v[136:139]\n"
      "ds_read_b128 v[116:119], v235 offset:40960\n"
      "s_waitcnt lgkmcnt(9)\n"
      "v_mfma_f32_16x16x32_bf16 v[132:135], a[240:243], v[36:39], v[0:3]\n"
      "s_sub_u32 s69, s66, s68\n"
      "v_mfma_f32_16x16x32_bf16 v[132:135], a[244:247], v[40:43], v[132:135]\n"
      "s_mov_b32 s68, s67\n"
      "v_mfma_f32_16x16x32_bf16 v[132:135], a[248:251], v[44:47], v[132:135]\n"
      "s_mov_b32 s67, s66\n"
      "v_mfma_f32_16x16x32_bf16 v[132:135], a[252:255], v[60:63], v[132:135]\n"
      "s_cmp_gt_i32 s69, 460\n"
      "s_cselect_b64 s[70:71], -1, s[70:71]\n"
      "s_mul_i32 s78, s97, s19\n"
      "s_waitcnt lgkmcnt(5)\n"
      "v_mfma_f32_16x16x32_bf16 v[132:135], a[48:51], v[64:67], v[132:135]\n"
      "s_or_b64 vcc, vcc, s[70:71]\n"
      "s_lshl_b32 s79, s95, 1\n"
      "v_add_u32 v236, s78, v236\n"
      "v_mfma_f32_16x16x32_bf16 v[132:135], a[52:55], v[68:71], v[132:135]\n"
      "v_cvt_pk_bf16_f32 v210, v136, v137\n"
      "v_mad_i32_i24 v252, v251, s79, v252\n"
      "s_mul_i32 s78, s78, s99\n"
      "v_mfma_f32_16x16x32_bf16 v[132:135], a[56:59], v[84:87], v[132:135]\n"
      "v_cvt_pk_bf16_f32 v211, v138, v139\n"
      "s_mul_i32 s79, s79, s100\n"
      "v_mfma_f32_16x16x32_bf16 v[132:135], a[60:63], v[88:91], v[132:135]\n"
      "ds_read_b128 v[24:27], v246 offset:12288\n"
      "v_add_u32 v247, s79, v247\n"
      "s_waitcnt lgkmcnt(1)\n"
      "v_mfma_f32_16x16x32_bf16 v[132:135], a[64:67], v[92:95], v[132:135]\n"
      "ds_read_b128 v[28:31], v246 offset:13312\n"
      "s_mul_i32 s79, s79, s101\n"
      "v_mfma_f32_16x16x32_bf16 v[132:135], a[68:71], v[108:111], v[132:135]\n"
      "ds_read_b128 v[32:35], v246 offset:14336\n"
      "v_add_u32 v237, s78, v237\n"
      "v_mfma_f32_16x16x32_bf16 v[132:135], a[72:75], v[112:115], v[132:135]\n"
      "LPD14:\n"
      "s_cbranch_execz LRET\n"
      "v_add_u32 v253, s79, v253\n"
      "s_cbranch_vccnz LT48\n"
      "LS48:\n"
      "v_mfma_f32_16x16x32_bf16 v[132:135], a[76:79], v[116:119], v[132:135]\n"
      "ds_write_b64 v244, v[210:211] offset:24712\n"
      "v_mfma_f32_16x16x32_bf16 v[136:139], a[80:83], v[36:39], v[4:7]\n"
      "s_add_u32 s54, s30, s38\n"
      "s_addc_u32 s55, s31, 0\n"
      "v_mfma_f32_16x16x32_bf16 v[136:139], a[84:87], v[40:43], v[136:139]\n"
      "s_waitcnt lgkmcnt(3)\n"
      "v_mfma_f32_16x16x32_bf16 v[136:139], a[88:91], v[44:47], v[136:139]\n"
      "global_store_dwordx4 v247, v[24:27], s[54:55] offset:0 sc1\n"
      "v_mfma_f32_16x16x32_bf16 v[136:139], a[92:95], v[60:63], v[136:139]\n"
      "v_cvt_pk_bf16_f32 v208, v132, v133\n"
      "v_mfma_f32_16x16x32_bf16 v[136:139], a[96:99], v[64:67], v[136:139]\n"
      "v_cvt_pk_bf16_f32 v209, v134, v135\n"
      "v_mfma_f32_16x16x32_bf16 v[136:139], a[100:103], v[68:71], v[136:139]\n"
      "v_mfma_f32_16x16x32_bf16 v[136:139], a[104:107], v[84:87], v[136:139]\n"
      "v_mfma_f32_16x16x32_bf16 v[136:139], a[108:111], v[88:91], v[136:139]\n"
      "s_add_u32 s52, s20, s40\n"
      "s_addc_u32 s53, s21, 0\n"
      "v_mfma_f32_16x16x32_bf16 v[136:139], a[112:115], v[92:95], v[136:139]\n"
      "s_add_u32 m0, s17, 0x0\n"
      "v_mfma_f32_16x16x32_bf16 v[136:139], a[116:119], v[108:111], v[136:139]\n"
      "ds_write_b64 v244, v[208:209] offset:0\n"
      "v_mfma_f32_16x16x32_bf16 v[136:139], a[120:123], v[112:115], v[136:139]\n"
      "global_load_lds_dwordx4 v236, s[52:53] offset:0 nt\n"
      "v_mfma_f32_16x16x32_bf16 v[136:139], a[124:127], v[116:119], v[136:139]\n"
      "v_mfma_f32_16x16x32_bf16 v[132:135], a[128:131], v[36:39], v[8:11]\n"
      "v_mfma_f32_16x16x32_bf16 v[132:135], a[132:135], v[40:43], v[132:135]\n"
      "v_mfma_f32_16x16x32_bf16 v[132:135], a[136:139], v[44:47], v[132:135]\n"
      "v_mfma_f32_16x16x32_bf16 v[132:135], a[140:143], v[60:63], v[132:135]\n"
      "v_cvt_pk_bf16_f32 v210, v136, v137\n"
      "v_mfma_f32_16x16x32_bf16 v[132:135], a[144:147], v[64:67], v[132:135]\n"
      "v_cvt_pk_bf16_f32 v211, v138, v139\n"
      "v_mfma_f32_16x16x32_bf16 v[132:135], a[148:151], v[68:71], v[132:135]\n"
      "v_mfma_f32_16x16x32_bf16 v[132:135], a[152:155], v[84:87], v[132:135]\n"
      "s_waitcnt lgkmcnt(3)\n"
      "v_mfma_f32_16x16x32_bf16 v[132:135], a[156:159], v[88:91], v[132:135]\n"
      "global_store_dwordx4 v252, v[28:31], s[54:55] offset:0 sc1\n"
      "v_mfma_f32_16x16x32_bf16 v[132:135], a[160:163], v[92:95], v[132:135]\n"
      "s_add_u32 m0, s17, 0x1f00\n"
      "v_mfma_f32_16x16x32_bf16 v[132:135], a[164:167], v[108:111], v[132:135]\n"
      "ds_write_b64 v244, v[210:211] offset:8\n"
      "v_mfma_f32_16x16x32_bf16 v[132:135], a[168:171], v[112:115], v[132:135]\n"
      "global_load_lds_dwordx4 v236, s[52:53] offset:256 nt\n"
      "v_mfma_f32_16x16x32_bf16 v[132:135], a[172:175], v[116:119], v[132:135]\n"
      "v_mfma_f32_16x16x32_bf16 v[136:139], a[176:179], v[36:39], v[12:15]\n"
      "v_mfma_f32_16x16x32_bf16 v[136:139], a[180:183], v[40:43], v[136:139]\n"
      "v_mfma_f32_16x16x32_bf16 v[136:139], a[184:187], v[44:47], v[136:139]\n"
      "v_mfma_f32_16x16x32_bf16 v[136:139], a[188:191], v[60:63], v[136:139]\n"
      "v_cvt_pk_bf16_f32 v212, v132, v133\n"
      "v_mfma_f32_16x16x32_bf16 v[136:139], a[192:195], v[64:67], v[136:139]\n"
      "v_cvt_pk_bf16_f32 v213, v134, v135\n"
      "v_mfma_f32_16x16x32_bf16 v[136:139], a[196:199], v[68:71], v[136:139]\n"
      "v_mfma_f32_16x16x32_bf16 v[136:139], a[200:203], v[84:87], v[136:139]\n"
      "v_mfma_f32_16x16x32_bf16 v[136:139], a[204:207], v[88:91], v[136:139]\n"
      "s_waitcnt lgkmcnt(3)\n"
      "v_mfma_f32_16x16x32_bf16 v[136:139], a[208:211], v[92:95], v[136:139]\n"
      "global_store_dwordx4 v253, v[32:35], s[54:55] offset:0 sc1\n"
      "s_cbranch_vccnz LT49\n"
      "LS49:\n"
      "v_mfma_f32_16x16x32_bf16 v[136:139], a[212:215], v[108:111], v[136:139]\n"
      "ds_write_b64 v245, v[212:213] offset:0\n"
      "v_mfma_f32_16x16x32_bf16 v[136:139], a[216:219], v[112:115], v[136:139]\n"
      "s_add_u32 m0, s17, 0x3e00\n"
      "v_mfma_f32_16x16x32_bf16 v[136:139], a[220:223], v[116:119], v[136:139]\n"
      "global_load_lds_dwordx4 v236, s[52:53] offset:512 nt\n"
      "v_mfma_f32_16x16x32_bf16 v[132:135], a[224:227], v[36:39], v[16:19]\n"
      "v_mfma_f32_16x16x32_bf16 v[132:135], a[228:231], v[40:43], v[132:135]\n"
      "v_mfma_f32_16x16x32_bf16 v[132:135], a[232:235], v[44:47], v[132:135]\n"
      "v_mfma_f32_16x16x32_bf16 v[132:135], a[236:239], v[60:63], v[132:135]\n"
      "v_cvt_pk_bf16_f32 v214, v136, v137\n"
      "v_mfma_f32_16x16x32_bf16 v[132:135], a[0:3], v[64:67], v[132:135]\n"
      "v_cvt_pk_bf16_f32 v215, v138, v139\n"
      "v_mfma_f32_16x16x32_bf16 v[132:135], a[4:7], v[68:71], v[132:135]\n"
      "v_mfma_f32_16x16x32_bf16 v[132:135], a[8:11], v[84:87], v[132:135]\n"
      "v_mfma_f32_16x16x32_bf16 v[132:135], a[12:15], v[88:91], v[132:135]\n"
      "v_mfma_f32_16x16x32_bf16 v[132:135], a[16:19], v[92:95], v[132:135]\n"
      "v_mfma_f32_16x16x32_bf16 v[132:135], a[20:23], v[108:111], v[132:135]\n"
      "ds_write_b64 v245, v[214:215] offset:8\n"
      "v_mfma_f32_16x16x32_bf16 v[132:135], v[140:143], v[112:115], v[132:135]\n"
      "v_mfma_f32_16x16x32_bf16 v[132:135], v[156:159], v[116:119], v[132:135]\n"
      "s_waitcnt lgkmcnt(4)\n"
      "s_barrier\n"
      "v_mfma_f32_16x16x32_bf16 v[136:139], v[160:163], v[36:39], v[20:23]\n"
      "ds_read_b128 v[36:39], v232 offset:24832\n"
      "v_mfma_f32_16x16x32_bf16 v[136:139], v[164:167], v[40:43], v[136:139]\n"
      "ds_read_b128 v[40:43], v233 offset:24832\n"
      "v_mfma_f32_16x16x32_bf16 v[136:139], v[180:183], v[44:47], v[136:139]\n"
      "ds_read_b128 v[44:47], v234 offset:24832\n"
      "v_mfma_f32_16x16x32_bf16 v[136:139], v[184:187], v[60:63], v[136:139]\n"
      "ds_read_b128 v[60:63], v235 offset:24832\n"
      "v_cvt_pk_bf16_f32 v208, v132, v133\n"
      "v_mfma_f32_16x16x32_bf16 v[136:139], v[188:191], v[64:67], v[136:139]\n"
      "ds_read_b128 v[64:67], v232 offset:33024\n"
      "v_cvt_pk_bf16_f32 v209, v134, v135\n"
      "v_mfma_f32_16x16x32_bf16 v[136:139], v[204:207], v[68:71], v[136:139]\n"
      "ds_read_b128 v[68:71], v233 offset:33024\n"
      "v_mfma_f32_16x16x32_bf16 v[136:139], a[24:27], v[84:87], v[136:139]\n"
      "ds_read_b128 v[84:87], v234 offset:33024\n"
      "v_mfma_f32_16x16x32_bf16 v[136:139], a[28:31], v[88:91], v[136:139]\n"
      "ds_read_b128 v[88:91], v235 offset:33024\n"
      "v_mfma_f32_16x16x32_bf16 v[136:139], a[32:35], v[92:95], v[136:139]\n"
      "ds_read_b128 v[92:95], v232 offset:41216\n"
      "v_mfma_f32_16x16x32_bf16 v[136:139], a[36:39], v[108:111], v[136:139]\n"
      "ds_read_b128 v[108:111], v233 offset:41216\n"
      "ds_write_b64 v244, v[208:209] offset:128\n"
      "v_mfma_f32_16x16x32_bf16 v[136:139], a[40:43], v[112:115], v[136:139]\n"
      "ds_read_b128 v[112:115], v234 offset:41216\n"
      "v_mfma_f32_16x16x32_bf16 v[136:139], a[44:47], v[116:119], v[136:139]\n"
      "ds_read_b128 v[116:119], v235 offset:41216\n"
      "s_waitcnt lgkmcnt(9)\n"
      "v_mfma_f32_16x16x32_bf16 v[132:135], a[240:243], v[36:39], v[0:3]\n"
      "v_add_u32 v232, 0xc000, v232\n"
      "v_mfma_f32_16x16x32_bf16 v[132:135], a[244:247], v[40:43], v[132:135]\n"
      "v_add_u32 v233, 0xc000, v233\n"
      "v_mfma_f32_16x16x32_bf16 v[132:135], a[248:251], v[44:47], v[132:135]\n"
      "v_add_u32 v234, 0xc000, v234\n"
      "v_mfma_f32_16x16x32_bf16 v[132:135], a[252:255], v[60:63], v[132:135]\n"
      "v_add_u32 v235, 0xc000, v235\n"
      "s_waitcnt lgkmcnt(5)\n"
      "v_mfma_f32_16x16x32_bf16 v[132:135], a[48:51], v[64:67], v[132:135]\n"
      "v_cvt_pk_bf16_f32 v210, v136, v137\n"
      "v_mfma_f32_16x16x32_bf16 v[132:135], a[52:55], v[68:71], v[132:135]\n"
      "v_cvt_pk_bf16_f32 v211, v138, v139\n"
      "v_mfma_f32_16x16x32_bf16 v[132:135], a[56:59], v[84:87], v[132:135]\n"
      "ds_read_b128 v[48:51], v246 offset:24576\n"
      "v_mfma_f32_16x16x32_bf16 v[132:135], a[60:63], v[88:91], v[132:135]\n"
      "ds_read_b128 v[52:55], v246 offset:25600\n"
      "s_waitcnt lgkmcnt(2)\n"
      "v_mfma_f32_16x16x32_bf16 v[132:135], a[64:67], v[92:95], v[132:135]\n"
      "ds_read_b128 v[56:59], v246 offset:26624\n"
      "s_cbranch_vccnz LT50\n"
      "LS50:\n"
      "v_mfma_f32_16x16x32_bf16 v[132:135], a[68:71], v[108:111], v[132:135]\n"
      "v_lshlrev_b32 v0, 16, v168\n"
      "v_mfma_f32_16x16x32_bf16 v[132:135], a[72:75], v[112:115], v[132:135]\n"
      "ds_write_b64 v244, v[210:211] offset:136\n"
      "v_mfma_f32_16x16x32_bf16 v[132:135], a[76:79], v[116:119], v[132:135]\n"
      "v_and_b32 v1, 0xffff0000, v168\n"
      "v_mfma_f32_16x16x32_bf16 v[136:139], a[80:83], v[36:39], v[4:7]\n"
      "v_lshlrev_b32 v2, 16, v169\n"
      "v_mfma_f32_16x16x32_bf16 v[136:139], a[84:87], v[40:43], v[136:139]\n"
      "v_and_b32 v3, 0xffff0000, v169\n"
      "v_mfma_f32_16x16x32_bf16 v[136:139], a[88:91], v[44:47], v[136:139]\n"
      "v_mfma_f32_16x16x32_bf16 v[136:139], a[92:95], v[60:63], v[136:139]\n"
      "v_cvt_pk_bf16_f32 v208, v132, v133\n"
      "v_mfma_f32_16x16x32_bf16 v[136:139], a[96:99], v[64:67], v[136:139]\n"
      "v_cvt_pk_bf16_f32 v209, v134, v135\n"
      "v_mfma_f32_16x16x32_bf16 v[136:139], a[100:103], v[68:71], v[136:139]\n"
      "s_waitcnt lgkmcnt(3)\n"
      "v_mfma_f32_16x16x32_bf16 v[136:139], a[104:107], v[84:87], v[136:139]\n"
      "global_store_dwordx4 v247, v[48:51], s[54:55] offset:768 sc1\n"
      "v_mfma_f32_16x16x32_bf16 v[136:139], a[108:111], v[88:91], v[136:139]\n"
      "s_add_u32 m0, s17, 0x1000\n"
      "v_mfma_f32_16x16x32_bf16 v[136:139], a[112:115], v[92:95], v[136:139]\n"
      "global_load_lds_dwordx4 v237, s[52:53] offset:0 nt\n"
      "v_mfma_f32_16x16x32_bf16 v[136:139], a[116:119], v[108:111], v[136:139]\n"
      "ds_write_b64 v244, v[208:209] offset:12288\n"
      "v_mfma_f32_16x16x32_bf16 v[136:139], a[120:123], v[112:115], v[136:139]\n"
      "v_lshlrev_b32 v4, 16, v170\n"
      "v_mfma_f32_16x16x32_bf16 v[136:139], a[124:127], v[116:119], v[136:139]\n"
      "v_and_b32 v5, 0xffff0000, v170\n"
      "v_mfma_f32_16x16x32_bf16 v[132:135], a[128:131], v[36:39], v[8:11]\n"
      "v_lshlrev_b32 v6, 16, v171\n"
      "v_mfma_f32_16x16x32_bf16 v[132:135], a[132:135], v[40:43], v[132:135]\n"
      "v_and_b32 v7, 0xffff0000, v171\n"
      "v_mfma_f32_16x16x32_bf16 v[132:135], a[136:139], v[44:47], v[132:135]\n"
      "v_mfma_f32_16x16x32_bf16 v[132:135], a[140:143], v[60:63], v[132:135]\n"
      "v_cvt_pk_bf16_f32 v210, v136, v137\n"
      "v_mfma_f32_16x16x32_bf16 v[132:135], a[144:147], v[64:67], v[132:135]\n"
      "v_cvt_pk_bf16_f32 v211, v138, v139\n"
      "v_mfma_f32_16x16x32_bf16 v[132:135], a[148:151], v[68:71], v[132:135]\n"
      "v_lshlrev_b32 v8, 16, v172\n"
      "v_mfma_f32_16x16x32_bf16 v[132:135], a[152:155], v[84:87], v[132:135]\n"
      "s_waitcnt lgkmcnt(3)\n"
      "v_mfma_f32_16x16x32_bf16 v[132:135], a[156:159], v[88:91], v[132:135]\n"
      "global_store_dwordx4 v252, v[52:55], s[54:55] offset:768 sc1\n"
      "v_mfma_f32_16x16x32_bf16 v[132:135], a[160:163], v[92:95], v[132:135]\n"
      "s_add_u32 m0, s17, 0x2f00\n"
      "v_mfma_f32_16x16x32_bf16 v[132:135], a[164:167], v[108:111], v[132:135]\n"
      "ds_write_b64 v244, v[210:211] offset:12296\n"
      "v_mfma_f32_16x16x32_bf16 v[132:135], a[168:171], v[112:115], v[132:135]\n"
      "global_load_lds_dwordx4 v237, s[52:53] offset:256 nt\n"
      "v_mfma_f32_16x16x32_bf16 v[132:135], a[172:175], v[116:119], v[132:135]\n"
      "v_and_b32 v9, 0xffff0000, v172\n"
      "v_mfma_f32_16x16x32_bf16 v[136:139], a[176:179], v[36:39], v[12:15]\n"
      "v_lshlrev_b32 v10, 16, v173\n"
      "v_mfma_f32_16x16x32_bf16 v[136:139], a[180:183], v[40:43], v[136:139]\n"
      "v_and_b32 v11, 0xffff0000, v173\n"
      "v_mfma_f32_16x16x32_bf16 v[136:139], a[184:187], v[44:47], v[136:139]\n"
      "v_mfma_f32_16x16x32_bf16 v[136:139], a[188:191], v[60:63], v[136:139]\n"
      "v_cvt_pk_bf16_f32 v212, v132, v133\n"
      "v_mfma_f32_16x16x32_bf16 v[136:139], a[192:195], v[64:67], v[136:139]\n"
      "v_cvt_pk_bf16_f32 v213, v134, v135\n"
      "v_mfma_f32_16x16x32_bf16 v[136:139], a[196:199], v[68:71], v[136:139]\n"
      "v_lshlrev_b32 v12, 16, v174\n"
      "v_mfma_f32_16x16x32_bf16 v[136:139], a[200:203], v[84:87], v[136:139]\n"
      "v_and_b32 v13, 0xffff0000, v174\n"
      "v_mfma_f32_16x16x32_bf16 v[136:139], a[204:207], v[88:91], v[136:139]\n"
      "s_waitcnt lgkmcnt(3)\n"
      "v_mfma_f32_16x16x32_bf16 v[136:139], a[208:211], v[92:95], v[136:139]\n"
      "global_store_dwordx4 v253, v[56:59], s[54:55] offset:768 sc1\n"
      "s_cbranch_vccnz LT51\n"
      "LS51:\n"
      "v_mfma_f32_16x16x32_bf16 v[136:139], a[212:215], v[108:111], v[136:139]\n"
      "ds_write_b64 v245, v[212:213] offset:12288\n"
      "v_mfma_f32_16x16x32_bf16 v[136:139], a[216:219], v[112:115], v[136:139]\n"
      "s_add_u32 m0, s17, 0x4e00\n"
      "v_mfma_f32_16x16x32_bf16 v[136:139], a[220:223], v[116:119], v[136:139]\n"
      "global_load_lds_dwordx4 v237, s[52:53] offset:512 nt\n"
      "v_mfma_f32_16x16x32_bf16 v[132:135], a[224:227], v[36:39], v[16:19]\n"
      "v_lshlrev_b32 v14, 16, v175\n"
      "v_mfma_f32_16x16x32_bf16 v[132:135], a[228:231], v[40:43], v[132:135]\n"
      "v_and_b32 v15, 0xffff0000, v175\n"
      "v_mfma_f32_16x16x32_bf16 v[132:135], a[232:235], v[44:47], v[132:135]\n"
      "v_mfma_f32_16x16x32_bf16 v[132:135], a[236:239], v[60:63], v[132:135]\n"
      "s_memrealtime s[84:85]\n"
      "v_mfma_f32_16x16x32_bf16 v[132:135], a[0:3], v[64:67], v[132:135]\n"
      "v_cvt_pk_bf16_f32 v214, v136, v137\n"
      "v_mfma_f32_16x16x32_bf16 v[132:135], a[4:7], v[68:71], v[132:135]\n"
      "v_cvt_pk_bf16_f32 v215, v138, v139\n"
      "v_mfma_f32_16x16x32_bf16 v[132:135], a[8:11], v[84:87], v[132:135]\n"
      "v_lshlrev_b32 v16, 16, v176\n"
      "v_mfma_f32_16x16x32_bf16 v[132:135], a[12:15], v[88:91], v[132:135]\n"
      "s_add_u32 s83, s83, s86\n"
      "v_mfma_f32_16x16x32_bf16 v[132:135], a[16:19], v[92:95], v[132:135]\n"
      "v_and_b32 v17, 0xffff0000, v176\n"
      "v_mfma_f32_16x16x32_bf16 v[132:135], a[20:23], v[108:111], v[132:135]\n"
      "v_lshlrev_b32 v18, 16, v177\n"
      "v_mfma_f32_16x16x32_bf16 v[132:135], v[140:143], v[112:115], v[132:135]\n"
      "s_waitcnt lgkmcnt(0)\n"
      "s_mov_b32 s66, s84\n"
      "s_sub_u32 s84, s84, s83\n"
      "v_mfma_f32_16x16x32_bf16 v[132:135], v[156:159], v[116:119], v[132:135]\n"
      "s_cmp_lt_i32 s84, 0\n"
      "s_cselect_b64 vcc, -1, 0\n"
      "s_waitcnt vmcnt(24)\n"
      "s_barrier\n"
      "v_mfma_f32_16x16x32_bf16 v[136:139], v[160:163], v[36:39], v[20:23]\n"
      "ds_read_b128 v[36:39], v232 offset:0\n"
      "ds_write_b64 v245, v[214:215] offset:12296\n"
      "v_mfma_f32_16x16x32_bf16 v[136:139], v[164:167], v[40:43], v[136:139]\n"
      "ds_read_b128 v[40:43], v233 offset:0\n"
      "v_mfma_f32_16x16x32_bf16 v[136:139], v[180:183], v[44:47], v[136:139]\n"
      "ds_read_b128 v[44:47], v234 offset:0\n"
      "v_mfma_f32_16x16x32_bf16 v[136:139], v[184:187], v[60:63], v[136:139]\n"
      "ds_read_b128 v[60:63], v235 offset:0\n"
      "v_cvt_pk_bf16_f32 v208, v132, v133\n"
      "v_mfma_f32_16x16x32_bf16 v[136:139], v[188:191], v[64:67], v[136:139]\n"
      "ds_read_b128 v[64:67], v232 offset:8192\n"
      "v_cvt_pk_bf16_f32 v209, v134, v135\n"
      "v_mfma_f32_16x16x32_bf16 v[136:139], v[204:207], v[68:71], v[136:139]\n"
      "ds_read_b128 v[68:71], v233 offset:8192\n"
      "v_mfma_f32_16x16x32_bf16 v[136:139], a[24:27], v[84:87], v[136:139]\n"
      "ds_read_b128 v[84:87], v234 offset:8192\n"
      "v_mfma_f32_16x16x32_bf16 v[136:139], a[28:31], v[88:91], v[136:139]\n"
      "ds_read_b128 v[88:91], v235 offset:8192\n"
      "v_mfma_f32_16x16x32_bf16 v[136:139], a[32:35], v[92:95], v[136:139]\n"
      "ds_read_b128 v[92:95], v232 offset:16384\n"
      "v_mfma_f32_16x16x32_bf16 v[136:139], a[36:39], v[108:111], v[136:139]\n"
      "ds_read_b128 v[108:111], v233 offset:16384\n"
      "ds_write_b64 v244, v[208:209] offset:12416\n"
      "v_mfma_f32_16x16x32_bf16 v[136:139], a[40:43], v[112:115], v[136:139]\n"
      "ds_read_b128 v[112:115], v234 offset:16384\n"
      "v_mfma_f32_16x16x32_bf16 v[136:139], a[44:47], v[116:119], v[136:139]\n"
      "ds_read_b128 v[116:119], v235 offset:16384\n"
      "s_waitcnt lgkmcnt(9)\n"
      "v_mfma_f32_16x16x32_bf16 v[132:135], a[240:243], v[36:39], v[0:3]\n"
      "s_sub_u32 s69, s66, s68\n"
      "v_mfma_f32_16x16x32_bf16 v[132:135], a[244:247], v[40:43], v[132:135]\n"
      "s_mov_b32 s68, s67\n"
      "v_mfma_f32_16x16x32_bf16 v[132:135], a[248:251], v[44:47], v[132:135]\n"
      "s_mov_b32 s67, s66\n"
      "v_mfma_f32_16x16x32_bf16 v[132:135], a[252:255], v[60:63], v[132:135]\n"
      "s_cmp_gt_i32 s69, 460\n"
      "s_cselect_b64 s[70:71], -1, s[70:71]\n"
      "s_waitcnt lgkmcnt(5)\n"
      "v_mfma_f32_16x16x32_bf16 v[132:135], a[48:51], v[64:67], v[132:135]\n"
      "s_or_b64 vcc, vcc, s[70:71]\n"
      "v_cvt_pk_bf16_f32 v210, v136, v137\n"
      "v_mfma_f32_16x16x32_bf16 v[132:135], a[52:55], v[68:71], v[132:135]\n"
      "v_and_b32 v19, 0xffff0000, v177\n"
      "v_cvt_pk_bf16_f32 v211, v138, v139\n"
      "v_mfma_f32_16x16x32_bf16 v[132:135], a[56:59], v[84:87], v[132:135]\n"
      "v_lshlrev_b32 v20, 16, v178\n"
      "v_mfma_f32_16x16x32_bf16 v[132:135], a[60:63], v[88:91], v[132:135]\n"
      "v_and_b32 v21, 0xffff0000, v178\n"
      "s_waitcnt lgkmcnt(0)\n"
      "v_mfma_f32_16x16x32_bf16 v[132:135], a[64:67], v[92:95], v[132:135]\n"
      "v_lshlrev_b32 v22, 16, v179\n"
      "s_cbranch_vccnz LT52\n"
      "LS52:\n"
      "v_mfma_f32_16x16x32_bf16 v[132:135], a[68:71], v[108:111], v[132:135]\n"
      "v_and_b32 v23, 0xffff0000, v179\n"
      "v_mfma_f32_16x16x32_bf16 v[132:135], a[72:75], v[112:115], v[132:135]\n"
      "ds_write_b64 v244, v[210:211] offset:12424\n"
      "v_mfma_f32_16x16x32_bf16 v[132:135], a[76:79], v[116:119], v[132:135]\n"
      "ds_read_b128 v[72:75], v246 offset:0\n"
      "v_mfma_f32_16x16x32_bf16 v[136:139], a[80:83], v[36:39], v[4:7]\n"
      "ds_read_b128 v[76:79], v246 offset:1024\n"
      "v_mfma_f32_16x16x32_bf16 v[136:139], a[84:87], v[40:43], v[136:139]\n"
      "ds_read_b128 v[80:83], v246 offset:2048\n"
      "v_mfma_f32_16x16x32_bf16 v[136:139], a[88:91], v[44:47], v[136:139]\n"
      "v_mfma_f32_16x16x32_bf16 v[136:139], a[92:95], v[60:63], v[136:139]\n"
      "v_cvt_pk_bf16_f32 v208, v132, v133\n"
      "s_add_u32 s54, s44, s38\n"
      "s_addc_u32 s55, s45, 0\n"
      "v_mfma_f32_16x16x32_bf16 v[136:139], a[96:99], v[64:67], v[136:139]\n"
      "v_cvt_pk_bf16_f32 v209, v134, v135\n"
      "v_mfma_f32_16x16x32_bf16 v[136:139], a[100:103], v[68:71], v[136:139]\n"
      "s_waitcnt lgkmcnt(2)\n"
      "v_mfma_f32_16x16x32_bf16 v[136:139], a[104:107], v[84:87], v[136:139]\n"
      "global_store_dwordx4 v247, v[72:75], s[54:55] offset:0 sc1\n"
      "v_mfma_f32_16x16x32_bf16 v[136:139], a[108:111], v[88:91], v[136:139]\n"
      "s_add_u32 s52, s42, s40\n"
      "s_addc_u32 s53, s43, 0\n"
      "v_mfma_f32_16x16x32_bf16 v[136:139], a[112:115], v[92:95], v[136:139]\n"
      "s_add_u32 m0, s17, 0x6000\n"
      "v_mfma_f32_16x16x32_bf16 v[136:139], a[116:119], v[108:111], v[136:139]\n"
      "ds_write_b64 v244, v[208:209] offset:24576\n"
      "v_mfma_f32_16x16x32_bf16 v[136:139], a[120:123], v[112:115], v[136:139]\n"
      "global_load_lds_dwordx4 v236, s[52:53] offset:0 nt\n"
      "v_mfma_f32_16x16x32_bf16 v[136:139], a[124:127], v[116:119], v[136:139]\n"
      "v_mfma_f32_16x16x32_bf16 v[132:135], a[128:131], v[36:39], v[8:11]\n"
      "v_mfma_f32_16x16x32_bf16 v[132:135], a[132:135], v[40:43], v[132:135]\n"
      "v_mfma_f32_16x16x32_bf16 v[132:135], a[136:139], v[44:47], v[132:135]\n"
      "v_mfma_f32_16x16x32_bf16 v[132:135], a[140:143], v[60:63], v[132:135]\n"
      "v_cvt_pk_bf16_f32 v210, v136, v137\n"
      "v_mfma_f32_16x16x32_bf16 v[132:135], a[144:147], v[64:67], v[132:135]\n"
      "v_cvt_pk_bf16_f32 v211, v138, v139\n"
      "v_mfma_f32_16x16x32_bf16 v[132:135], a[148:151], v[68:71], v[132:135]\n"
      "v_mfma_f32_16x16x32_bf16 v[132:135], a[152:155], v[84:87], v[132:135]\n"
      "s_waitcnt lgkmcnt(2)\n"
      "v_mfma_f32_16x16x32_bf16 v[132:135], a[156:159], v[88:91], v[132:135]\n"
      "global_store_dwordx4 v252, v[76:79], s[54:55] offset:0 sc1\n"
      "v_mfma_f32_16x16x32_bf16 v[132:135], a[160:163], v[92:95], v[132:135]\n"
      "s_add_u32 m0, s17, 0x7f00\n"
      "v_mfma_f32_16x16x32_bf16 v[132:135], a[164:167], v[108:111], v[132:135]\n"
      "ds_write_b64 v244, v[210:211] offset:24584\n"
      "v_mfma_f32_16x16x32_bf16 v[132:135], a[168:171], v[112:115], v[132:135]\n"
      "global_load_lds_dwordx4 v236, s[52:53] offset:256 nt\n"
      "v_mfma_f32_16x16x32_bf16 v[132:135], a[172:175], v[116:119], v[132:135]\n"
      "v_mfma_f32_16x16x32_bf16 v[136:139], a[176:179], v[36:39], v[12:15]\n"
      "v_mfma_f32_16x16x32_bf16 v[136:139], a[180:183], v[40:43], v[136:139]\n"
      "v_mfma_f32_16x16x32_bf16 v[136:139], a[184:187], v[44:47], v[136:139]\n"
      "v_mfma_f32_16x16x32_bf16 v[136:139], a[188:191], v[60:63], v[136:139]\n"
      "v_cvt_pk_bf16_f32 v212, v132, v133\n"
      "v_mfma_f32_16x16x32_bf16 v[136:139], a[192:195], v[64:67], v[136:139]\n"
      "v_cvt_pk_bf16_f32 v213, v134, v135\n"
      "v_mfma_f32_16x16x32_bf16 v[136:139], a[196:199], v[68:71], v[136:139]\n"
      "v_mfma_f32_16x16x32_bf16 v[136:139], a[200:203], v[84:87], v[136:139]\n"
      "v_mfma_f32_16x16x32_bf16 v[136:139], a[204:207], v[88:91], v[136:139]\n"
      "s_waitcnt lgkmcnt(2)\n"
      "v_mfma_f32_16x16x32_bf16 v[136:139], a[208:211], v[92:95], v[136:139]\n"
      "global_store_dwordx4 v253, v[80:83], s[54:55] offset:0 sc1\n"
      "s_cbranch_vccnz LT53\n"
      "LS53:\n"
      "v_mfma_f32_16x16x32_bf16 v[136:139], a[212:215], v[108:111], v[136:139]\n"
      "ds_write_b64 v245, v[212:213] offset:24576\n"
      "v_mfma_f32_16x16x32_bf16 v[136:139], a[216:219], v[112:115], v[136:139]\n"
      "s_add_u32 m0, s17, 0x9e00\n"
      "v_mfma_f32_16x16x32_bf16 v[136:139], a[220:223], v[116:119], v[136:139]\n"
      "global_load_lds_dwordx4 v236, s[52:53] offset:512 nt\n"
      "v_mfma_f32_16x16x32_bf16 v[132:135], a[224:227], v[36:39], v[16:19]\n"
      "v_mfma_f32_16x16x32_bf16 v[132:135], a[228:231], v[40:43], v[132:135]\n"
      "v_mfma_f32_16x16x32_bf16 v[132:135], a[232:235], v[44:47], v[132:135]\n"
      "v_mfma_f32_16x16x32_bf16 v[132:135], a[236:239], v[60:63], v[132:135]\n"
      "v_cvt_pk_bf16_f32 v214, v136, v137\n"
      "v_mfma_f32_16x16x32_bf16 v[132:135], a[0:3], v[64:67], v[132:135]\n"
      "v_cvt_pk_bf16_f32 v215, v138, v139\n"
      "v_mfma_f32_16x16x32_bf16 v[132:135], a[4:7], v[68:71], v[132:135]\n"
      "v_mfma_f32_16x16x32_bf16 v[132:135], a[8:11], v[84:87], v[132:135]\n"
      "v_mfma_f32_16x16x32_bf16 v[132:135], a[12:15], v[88:91], v[132:135]\n"
      "v_mfma_f32_16x16x32_bf16 v[132:135], a[16:19], v[92:95], v[132:135]\n"
      "v_mfma_f32_16x16x32_bf16 v[132:135], a[20:23], v[108:111], v[132:135]\n"
      "ds_write_b64 v245, v[214:215] offset:24584\n"
      "v_mfma_f32_16x16x32_bf16 v[132:135], v[140:143], v[112:115], v[132:135]\n"
      "v_mfma_f32_16x16x32_bf16 v[132:135], v[156:159], v[116:119], v[132:135]\n"
      "s_barrier\n"
      "v_mfma_f32_16x16x32_bf16 v[136:139], v[160:163], v[36:39], v[20:23]\n"
      "ds_read_b128 v[36:39], v232 offset:256\n"
      "v_mfma_f32_16x16x32_bf16 v[136:139], v[164:167], v[40:43], v[136:139]\n"
      "ds_read_b128 v[40:43], v233 offset:256\n"
      "v_mfma_f32_16x16x32_bf16 v[136:139], v[180:183], v[44:47], v[136:139]\n"
      "ds_read_b128 v[44:47], v234 offset:256\n"
      "v_mfma_f32_16x16x32_bf16 v[136:139], v[184:187], v[60:63], v[136:139]\n"
      "ds_read_b128 v[60:63], v235 offset:256\n"
      "v_cvt_pk_bf16_f32 v208, v132, v133\n"
      "v_mfma_f32_16x16x32_bf16 v[136:139], v[188:191], v[64:67], v[136:139]\n"
      "ds_read_b128 v[64:67], v232 offset:8448\n"
      "v_cvt_pk_bf16_f32 v209, v134, v135\n"
      "v_mfma_f32_16x16x32_bf16 v[136:139], v[204:207], v[68:71], v[136:139]\n"
      "ds_read_b128 v[68:71], v233 offset:8448\n"
      "v_mfma_f32_16x16x32_bf16 v[136:139], a[24:27], v[84:87], v[136:139]\n"
      "ds_read_b128 v[84:87], v234 offset:8448\n"
      "v_mfma_f32_16x16x32_bf16 v[136:139], a[28:31], v[88:91], v[136:139]\n"
      "ds_read_b128 v[88:91], v235 offset:8448\n"
      "v_mfma_f32_16x16x32_bf16 v[136:139], a[32:35], v[92:95], v[136:139]\n"
      "ds_read_b128 v[92:95], v232 offset:16640\n"
      "v_mfma_f32_16x16x32_bf16 v[136:139], a[36:39], v[108:111], v[136:139]\n"
      "ds_read_b128 v[108:111], v233 offset:16640\n"
      "v_mfma_f32_16x16x32_bf16 v[136:139], a[40:43], v[112:115], v[136:139]\n"
      "ds_read_b128 v[112:115], v234 offset:16640\n"
      "v_mfma_f32_16x16x32_bf16 v[136:139], a[44:47], v[116:119], v[136:139]\n"
      "ds_read_b128 v[116:119], v235 offset:16640\n"
      "s_waitcnt lgkmcnt(8)\n"
      "v_mfma_f32_16x16x32_bf16 v[132:135], a[240:243], v[36:39], v[0:3]\n"
      "ds_write_b64 v244, v[208:209] offset:24704\n"
      "v_mfma_f32_16x16x32_bf16 v[132:135], a[244:247], v[40:43], v[132:135]\n"
      "v_mfma_f32_16x16x32_bf16 v[132:135], a[248:251], v[44:47], v[132:135]\n"
      "v_mfma_f32_16x16x32_bf16 v[132:135], a[252:255], v[60:63], v[132:135]\n"
      "v_cvt_pk_bf16_f32 v210, v136, v137\n"
      "s_waitcnt lgkmcnt(5)\n"
      "v_mfma_f32_16x16x32_bf16 v[132:135], a[48:51], v[64:67], v[132:135]\n"
      "v_cvt_pk_bf16_f32 v211, v138, v139\n"
      "v_mfma_f32_16x16x32_bf16 v[132:135], a[52:55], v[68:71], v[132:135]\n"
      "v_mfma_f32_16x16x32_bf16 v[132:135], a[56:59], v[84:87], v[132:135]\n"
      "ds_read_b128 v[96:99], v246 offset:12288\n"
      "v_mfma_f32_16x16x32_bf16 v[132:135], a[60:63], v[88:91], v[132:135]\n"
      "ds_read_b128 v[100:103], v246 offset:13312\n"
      "s_waitcnt lgkmcnt(3)\n"
      "v_mfma_f32_16x16x32_bf16 v[132:135], a[64:67], v[92:95], v[132:135]\n"
      "ds_read_b128 v[104:107], v246 offset:14336\n"
      "v_mfma_f32_16x16x32_bf16 v[132:135], a[68:71], v[108:111], v[132:135]\n"
      "ds_write_b64 v244, v[210:211] offset:24712\n"
      "v_mfma_f32_16x16x32_bf16 v[132:135], a[72:75], v[112:115], v[132:135]\n"
      "s_cbranch_vccnz LT54\n"
      "LS54:\n"
      "v_mfma_f32_16x16x32_bf16 v[132:135], a[76:79], v[116:119], v[132:135]\n"
      "v_mfma_f32_16x16x32_bf16 v[136:139], a[80:83], v[36:39], v[4:7]\n"
      "v_mfma_f32_16x16x32_bf16 v[136:139], a[84:87], v[40:43], v[136:139]\n"
      "v_mfma_f32_16x16x32_bf16 v[136:139], a[88:91], v[44:47], v[136:139]\n"
      "v_mfma_f32_16x16x32_bf16 v[136:139], a[92:95], v[60:63], v[136:139]\n"
      "v_cvt_pk_bf16_f32 v208, v132, v133\n"
      "v_mfma_f32_16x16x32_bf16 v[136:139], a[96:99], v[64:67], v[136:139]\n"
      "v_cvt_pk_bf16_f32 v209, v134, v135\n"
      "v_mfma_f32_16x16x32_bf16 v[136:139], a[100:103], v[68:71], v[136:139]\n"
      "v_mfma_f32_16x16x32_bf16 v[136:139], a[104:107], v[84:87], v[136:139]\n"
      "v_mfma_f32_16x16x32_bf16 v[136:139], a[108:111], v[88:91], v[136:139]\n"
      "s_add_u32 m0, s17, 0x7000\n"
      "v_mfma_f32_16x16x32_bf16 v[136:139], a[112:115], v[92:95], v[136:139]\n"
      "global_load_lds_dwordx4 v237, s[52:53] offset:0 nt\n"
      "v_mfma_f32_16x16x32_bf16 v[136:139], a[116:119], v[108:111], v[136:139]\n"
      "ds_write_b64 v244, v[208:209] offset:0\n"
      "v_mfma_f32_16x16x32_bf16 v[136:139], a[120:123], v[112:115], v[136:139]\n"
      "v_mfma_f32_16x16x32_bf16 v[136:139], a[124:127], v[116:119], v[136:139]\n"
      "v_mfma_f32_16x16x32_bf16 v[132:135], a[128:131], v[36:39], v[8:11]\n"
      "v_mfma_f32_16x16x32_bf16 v[132:135], a[132:135], v[40:43], v[132:135]\n"
      "v_mfma_f32_16x16x32_bf16 v[132:135], a[136:139], v[44:47], v[132:135]\n"
      "v_mfma_f32_16x16x32_bf16 v[132:135], a[140:143], v[60:63], v[132:135]\n"
      "v_cvt_pk_bf16_f32 v210, v136, v137\n"
      "v_mfma_f32_16x16x32_bf16 v[132:135], a[144:147], v[64:67], v[132:135]\n"
      "v_cvt_pk_bf16_f32 v211, v138, v139\n"
      "v_mfma_f32_16x16x32_bf16 v[132:135], a[148:151], v[68:71], v[132:135]\n"
      "v_mfma_f32_16x16x32_bf16 v[132:135], a[152:155], v[84:87], v[132:135]\n"
      "s_add_u32 m0, s17, 0x8f00\n"
      "v_mfma_f32_16x16x32_bf16 v[132:135], a[156:159], v[88:91], v[132:135]\n"
      "global_load_lds_dwordx4 v237, s[52:53] offset:256 nt\n"
      "v_mfma_f32_16x16x32_bf16 v[132:135], a[160:163], v[92:95], v[132:135]\n"
      "v_mfma_f32_16x16x32_bf16 v[132:135], a[164:167], v[108:111], v[132:135]\n"
      "ds_write_b64 v244, v[210:211] offset:8\n"
      "v_mfma_f32_16x16x32_bf16 v[132:135], a[168:171], v[112:115], v[132:135]\n"
      "v_mfma_f32_16x16x32_bf16 v[132:135], a[172:175], v[116:119], v[132:135]\n"
      "v_mfma_f32_16x16x32_bf16 v[136:139], a[176:179], v[36:39], v[12:15]\n"
      "v_mfma_f32_16x16x32_bf16 v[136:139], a[180:183], v[40:43], v[136:139]\n"
      "v_mfma_f32_16x16x32_bf16 v[136:139], a[184:187], v[44:47], v[136:139]\n"
      "v_mfma_f32_16x16x32_bf16 v[136:139], a[188:191], v[60:63], v[136:139]\n"
      "v_cvt_pk_bf16_f32 v212, v132, v133\n"
      "v_mfma_f32_16x16x32_bf16 v[136:139], a[192:195], v[64:67], v[136:139]\n"
      "v_cvt_pk_bf16_f32 v213, v134, v135\n"
      "v_mfma_f32_16x16x32_bf16 v[136:139], a[196:199], v[68:71], v[136:139]\n"
      "s_add_u32 m0, s17, 0xae00\n"
      "v_mfma_f32_16x16x32_bf16 v[136:139], a[200:203], v[84:87], v[136:139]\n"
      "global_load_lds_dwordx4 v237, s[52:53] offset:512 nt\n"
      "v_mfma_f32_16x16x32_bf16 v[136:139], a[204:207], v[88:91], v[136:139]\n"
      "v_mfma_f32_16x16x32_bf16 v[136:139], a[208:211], v[92:95], v[136:139]\n"
      "s_cbranch_vccnz LT55\n"
      "LS55:\n"
      "v_mfma_f32_16x16x32_bf16 v[136:139], a[212:215], v[108:111], v[136:139]\n"
      "ds_write_b64 v245, v[212:213] offset:0\n"
      "v_mfma_f32_16x16x32_bf16 v[136:139], a[216:219], v[112:115], v[136:139]\n"
      "v_mfma_f32_16x16x32_bf16 v[136:139], a[220:223], v[116:119], v[136:139]\n"
      "v_mfma_f32_16x16x32_bf16 v[132:135], a[224:227], v[36:39], v[16:19]\n"
      "v_mfma_f32_16x16x32_bf16 v[132:135], a[228:231], v[40:43], v[132:135]\n"
      "v_mfma_f32_16x16x32_bf16 v[132:135], a[232:235], v[44:47], v[132:135]\n"
      "v_mfma_f32_16x16x32_bf16 v[132:135], a[236:239], v[60:63], v[132:135]\n"
      "s_memrealtime s[84:85]\n"
      "v_mfma_f32_16x16x32_bf16 v[132:135], a[0:3], v[64:67], v[132:135]\n"
      "v_cvt_pk_bf16_f32 v214, v136, v137\n"
      "v_mfma_f32_16x16x32_bf16 v[132:135], a[4:7], v[68:71], v[132:135]\n"
      "v_cvt_pk_bf16_f32 v215, v138, v139\n"
      "v_mfma_f32_16x16x32_bf16 v[132:135], a[8:11], v[84:87], v[132:135]\n"
      "v_mfma_f32_16x16x32_bf16 v[132:135], a[12:15], v[88:91], v[132:135]\n"
      "s_add_u32 s83, s83, s86\n"
      "v_mfma_f32_16x16x32_bf16 v[132:135], a[16:19], v[92:95], v[132:135]\n"
      "v_mfma_f32_16x16x32_bf16 v[132:135], a[20:23], v[108:111], v[132:135]\n"
      "v_mfma_f32_16x16x32_bf16 v[132:135], v[140:143], v[112:115], v[132:135]\n"
      "s_waitcnt lgkmcnt(0)\n"
      "s_mov_b32 s66, s84\n"
      "s_sub_u32 s84, s84, s83\n"
      "v_mfma_f32_16x16x32_bf16 v[132:135], v[156:159], v[116:119], v[132:135]\n"
      "s_cmp_lt_i32 s84, 0\n"
      "s_cselect_b64 vcc, -1, 0\n"
      "s_waitcnt vmcnt(21)\n"
      "s_barrier\n"
      "v_mfma_f32_16x16x32_bf16 v[136:139], v[160:163], v[36:39], v[20:23]\n"
      "ds_read_b128 v[36:39], v232 offset:24576\n"
      "ds_write_b64 v245, v[214:215] offset:8\n"
      "v_mfma_f32_16x16x32_bf16 v[136:139], v[164:167], v[40:43], v[136:139]\n"
      "ds_read_b128 v[40:43], v233 offset:24576\n"
      "v_mfma_f32_16x16x32_bf16 v[136:139], v[180:183], v[44:47], v[136:139]\n"
      "ds_read_b128 v[44:47], v234 offset:24576\n"
      "v_mfma_f32_16x16x32_bf16 v[136:139], v[184:187], v[60:63], v[136:139]\n"
      "ds_read_b128 v[60:63], v235 offset:24576\n"
      "v_cvt_pk_bf16_f32 v208, v132, v133\n"
      "v_mfma_f32_16x16x32_bf16 v[136:139], v[188:191], v[64:67], v[136:139]\n"
      "ds_read_b128 v[64:67], v232 offset:32768\n"
      "v_cvt_pk_bf16_f32 v209, v134, v135\n"
      "v_mfma_f32_16x16x32_bf16 v[136:139], v[204:207], v[68:71], v[136:139]\n"
      "ds_read_b128 v[68:71], v233 offset:32768\n"
      "v_mfma_f32_16x16x32_bf16 v[136:139], a[24:27], v[84:87], v[136:139]\n"
      "ds_read_b128 v[84:87], v234 offset:32768\n"
      "v_mfma_f32_16x16x32_bf16 v[136:139], a[28:31], v[88:91], v[136:139]\n"
      "ds_read_b128 v[88:91], v235 offset:32768\n"
      "v_mfma_f32_16x16x32_bf16 v[136:139], a[32:35], v[92:95], v[136:139]\n"
      "ds_read_b128 v[92:95], v232 offset:40960\n"
      "v_mfma_f32_16x16x32_bf16 v[136:139], a[36:39], v[108:111], v[136:139]\n"
      "ds_read_b128 v[108:111], v233 offset:40960\n"
      "ds_write_b64 v244, v[208:209] offset:128\n"
      "v_mfma_f32_16x16x32_bf16 v[136:139], a[40:43], v[112:115], v[136:139]\n"
      "ds_read_b128 v[112:115], v234 offset:40960\n"
      "v_mfma_f32_16x16x32_bf16 v[136:139], a[44:47], v[116:119], v[136:139]\n"
      "ds_read_b128 v[116:119], v235 offset:40960\n"
      "s_waitcnt lgkmcnt(9)\n"
      "v_mfma_f32_16x16x32_bf16 v[132:135], a[240:243], v[36:39], v[0:3]\n"
      "s_sub_u32 s69, s66, s68\n"
      "v_mfma_f32_16x16x32_bf16 v[132:135], a[244:247], v[40:43], v[132:135]\n"
      "s_mov_b32 s68, s67\n"
      "v_mfma_f32_16x16x32_bf16 v[132:135], a[248:251], v[44:47], v[132:135]\n"
      "s_mov_b32 s67, s66\n"
      "v_mfma_f32_16x16x32_bf16 v[132:135], a[252:255], v[60:63], v[132:135]\n"
      "s_cmp_gt_i32 s69, 460\n"
      "s_cselect_b64 s[70:71], -1, s[70:71]\n"
      "s_waitcnt lgkmcnt(5)\n"
      "v_mfma_f32_16x16x32_bf16 v[132:135], a[48:51], v[64:67], v[132:135]\n"
      "s_or_b64 vcc, vcc, s[70:71]\n"
      "v_mfma_f32_16x16x32_bf16 v[132:135], a[52:55], v[68:71], v[132:135]\n"
      "v_cvt_pk_bf16_f32 v210, v136, v137\n"
      "v_mfma_f32_16x16x32_bf16 v[132:135], a[56:59], v[84:87], v[132:135]\n"
      "v_cvt_pk_bf16_f32 v211, v138, v139\n"
      "v_mfma_f32_16x16x32_bf16 v[132:135], a[60:63], v[88:91], v[132:135]\n"
      "ds_read_b128 v[120:123], v246 offset:24576\n"
      "s_waitcnt lgkmcnt(1)\n"
      "v_mfma_f32_16x16x32_bf16 v[132:135], a[64:67], v[92:95], v[132:135]\n"
      "ds_read_b128 v[124:127], v246 offset:25600\n"
      "v_mfma_f32_16x16x32_bf16 v[132:135], a[68:71], v[108:111], v[132:135]\n"
      "ds_read_b128 v[128:131], v246 offset:26624\n"
      "v_mfma_f32_16x16x32_bf16 v[132:135], a[72:75], v[112:115], v[132:135]\n"
      "LPD15:\n"
      "s_cbranch_execz LRET\n"
      "s_cbranch_vccnz LT56\n"
      "LS56:\n"
      "v_mfma_f32_16x16x32_bf16 v[132:135], a[76:79], v[116:119], v[132:135]\n"
      "ds_write_b64 v244, v[210:211] offset:136\n"
      "v_mfma_f32_16x16x32_bf16 v[136:139], a[80:83], v[36:39], v[4:7]\n"
      "v_mfma_f32_16x16x32_bf16 v[136:139], a[84:87], v[40:43], v[136:139]\n"
      "v_mfma_f32_16x16x32_bf16 v[136:139], a[88:91], v[44:47], v[136:139]\n"
      "v_mfma_f32_16x16x32_bf16 v[136:139], a[92:95], v[60:63], v[136:139]\n"
      "v_cvt_pk_bf16_f32 v208, v132, v133\n"
      "v_mfma_f32_16x16x32_bf16 v[136:139], a[96:99], v[64:67], v[136:139]\n"
      "v_cvt_pk_bf16_f32 v209, v134, v135\n"
      "v_mfma_f32_16x16x32_bf16 v[136:139], a[100:103], v[68:71], v[136:139]\n"
      "v_mfma_f32_16x16x32_bf16 v[136:139], a[104:107], v[84:87], v[136:139]\n"
      "v_mfma_f32_16x16x32_bf16 v[136:139], a[108:111], v[88:91], v[136:139]\n"
      "v_mfma_f32_16x16x32_bf16 v[136:139], a[112:115], v[92:95], v[136:139]\n"
      "v_mfma_f32_16x16x32_bf16 v[136:139], a[116:119], v[108:111], v[136:139]\n"
      "ds_write_b64 v244, v[208:209] offset:12288\n"
      "v_mfma_f32_16x16x32_bf16 v[136:139], a[120:123], v[112:115], v[136:139]\n"
      "v_mfma_f32_16x16x32_bf16 v[136:139], a[124:127], v[116:119], v[136:139]\n"
      "v_mfma_f32_16x16x32_bf16 v[132:135], a[128:131], v[36:39], v[8:11]\n"
      "v_mfma_f32_16x16x32_bf16 v[132:135], a[132:135], v[40:43], v[132:135]\n"
      "v_mfma_f32_16x16x32_bf16 v[132:135], a[136:139], v[44:47], v[132:135]\n"
      "v_mfma_f32_16x16x32_bf16 v[132:135], a[140:143], v[60:63], v[132:135]\n"
      "v_cvt_pk_bf16_f32 v210, v136, v137\n"
      "v_mfma_f32_16x16x32_bf16 v[132:135], a[144:147], v[64:67], v[132:135]\n"
      "v_cvt_pk_bf16_f32 v211, v138, v139\n"
      "v_mfma_f32_16x16x32_bf16 v[132:135], a[148:151], v[68:71], v[132:135]\n"
      "v_mfma_f32_16x16x32_bf16 v[132:135], a[152:155], v[84:87], v[132:135]\n"
      "v_mfma_f32_16x16x32_bf16 v[132:135], a[156:159], v[88:91], v[132:135]\n"
      "v_mfma_f32_16x16x32_bf16 v[132:135], a[160:163], v[92:95], v[132:135]\n"
      "v_mfma_f32_16x16x32_bf16 v[132:135], a[164:167], v[108:111], v[132:135]\n"
      "ds_write_b64 v244, v[210:211] offset:12296\n"
      "v_mfma_f32_16x16x32_bf16 v[132:135], a[168:171], v[112:115], v[132:135]\n"
      "v_mfma_f32_16x16x32_bf16 v[132:135], a[172:175], v[116:119], v[132:135]\n"
      "v_mfma_f32_16x16x32_bf16 v[136:139], a[176:179], v[36:39], v[12:15]\n"
      "v_mfma_f32_16x16x32_bf16 v[136:139], a[180:183], v[40:43], v[136:139]\n"
      "v_mfma_f32_16x16x32_bf16 v[136:139], a[184:187], v[44:47], v[136:139]\n"
      "v_mfma_f32_16x16x32_bf16 v[136:139], a[188:191], v[60:63], v[136:139]\n"
      "v_cvt_pk_bf16_f32 v212, v132, v133\n"
      "v_mfma_f32_16x16x32_bf16 v[136:139], a[192:195], v[64:67], v[136:139]\n"
      "v_cvt_pk_bf16_f32 v213, v134, v135\n"
      "v_mfma_f32_16x16x32_bf16 v[136:139], a[196:199], v[68:71], v[136:139]\n"
      "v_mfma_f32_16x16x32_bf16 v[136:139], a[200:203], v[84:87], v[136:139]\n"
      "v_mfma_f32_16x16x32_bf16 v[136:139], a[204:207], v[88:91], v[136:139]\n"
      "v_mfma_f32_16x16x32_bf16 v[136:139], a[208:211], v[92:95], v[136:139]\n"
      "s_cbranch_vccnz LT57\n"
      "LS57:\n"
      "v_mfma_f32_16x16x32_bf16 v[136:139], a[212:215], v[108:111], v[136:139]\n"
      "ds_write_b64 v245, v[212:213] offset:12288\n"
      "v_mfma_f32_16x16x32_bf16 v[136:139], a[216:219], v[112:115], v[136:139]\n"
      "v_mfma_f32_16x16x32_bf16 v[136:139], a[220:223], v[116:119], v[136:139]\n"
      "v_mfma_f32_16x16x32_bf16 v[132:135], a[224:227], v[36:39], v[16:19]\n"
      "v_mfma_f32_16x16x32_bf16 v[132:135], a[228:231], v[40:43], v[132:135]\n"
      "v_mfma_f32_16x16x32_bf16 v[132:135], a[232:235], v[44:47], v[132:135]\n"
      "v_mfma_f32_16x16x32_bf16 v[132:135], a[236:239], v[60:63], v[132:135]\n"
      "v_cvt_pk_bf16_f32 v214, v136, v137\n"
      "v_mfma_f32_16x16x32_bf16 v[132:135], a[0:3], v[64:67], v[132:135]\n"
      "v_cvt_pk_bf16_f32 v215, v138, v139\n"
      "v_mfma_f32_16x16x32_bf16 v[132:135], a[4:7], v[68:71], v[132:135]\n"
      "v_mfma_f32_16x16x32_bf16 v[132:135], a[8:11], v[84:87], v[132:135]\n"
      "v_mfma_f32_16x16x32_bf16 v[132:135], a[12:15], v[88:91], v[132:135]\n"
      "v_mfma_f32_16x16x32_bf16 v[132:135], a[16:19], v[92:95], v[132:135]\n"
      "v_mfma_f32_16x16x32_bf16 v[132:135], a[20:23], v[108:111], v[132:135]\n"
      "ds_write_b64 v245, v[214:215] offset:12296\n"
      "v_mfma_f32_16x16x32_bf16 v[132:135], v[140:143], v[112:115], v[132:135]\n"
      "v_mfma_f32_16x16x32_bf16 v[132:135], v[156:159], v[116:119], v[132:135]\n"
      "s_waitcnt lgkmcnt(4)\n"
      "s_barrier\n"
      "v_mfma_f32_16x16x32_bf16 v[136:139], v[160:163], v[36:39], v[20:23]\n"
      "ds_read_b128 v[36:39], v232 offset:24832\n"
      "v_mfma_f32_16x16x32_bf16 v[136:139], v[164:167], v[40:43], v[136:139]\n"
      "ds_read_b128 v[40:43], v233 offset:24832\n"
      "v_mfma_f32_16x16x32_bf16 v[136:139], v[180:183], v[44:47], v[136:139]\n"
      "ds_read_b128 v[44:47], v234 offset:24832\n"
      "v_mfma_f32_16x16x32_bf16 v[136:139], v[184:187], v[60:63], v[136:139]\n"
      "ds_read_b128 v[60:63], v235 offset:24832\n"
      "v_cvt_pk_bf16_f32 v208, v132, v133\n"
      "v_mfma_f32_16x16x32_bf16 v[136:139], v[188:191], v[64:67], v[136:139]\n"
      "ds_read_b128 v[64:67], v232 offset:33024\n"
      "v_cvt_pk_bf16_f32 v209, v134, v135\n"
      "v_mfma_f32_16x16x32_bf16 v[136:139], v[204:207], v[68:71], v[136:139]\n"
      "ds_read_b128 v[68:71], v233 offset:33024\n"
      "v_mfma_f32_16x16x32_bf16 v[136:139], a[24:27], v[84:87], v[136:139]\n"
      "ds_read_b128 v[84:87], v234 offset:33024\n"
      "v_mfma_f32_16x16x32_bf16 v[136:139], a[28:31], v[88:91], v[136:139]\n"
      "ds_read_b128 v[88:91], v235 offset:33024\n"
      "v_mfma_f32_16x16x32_bf16 v[136:139], a[32:35], v[92:95], v[136:139]\n"
      "ds_read_b128 v[92:95], v232 offset:41216\n"
      "v_mfma_f32_16x16x32_bf16 v[136:139], a[36:39], v[108:111], v[136:139]\n"
      "ds_read_b128 v[108:111], v233 offset:41216\n"
      "ds_write_b64 v244, v[208:209] offset:12416\n"
      "v_mfma_f32_16x16x32_bf16 v[136:139], a[40:43], v[112:115], v[136:139]\n"
      "ds_read_b128 v[112:115], v234 offset:41216\n"
      "v_mfma_f32_16x16x32_bf16 v[136:139], a[44:47], v[116:119], v[136:139]\n"
      "ds_read_b128 v[116:119], v235 offset:41216\n"
      "s_waitcnt lgkmcnt(9)\n"
      "v_mfma_f32_16x16x32_bf16 v[132:135], a[240:243], v[36:39], v[0:3]\n"
      "v_add_u32 v232, 0xffff4000, v232\n"
      "v_mfma_f32_16x16x32_bf16 v[132:135], a[244:247], v[40:43], v[132:135]\n"
      "v_add_u32 v233, 0xffff4000, v233\n"
      "v_mfma_f32_16x16x32_bf16 v[132:135], a[248:251], v[44:47], v[132:135]\n"
      "v_add_u32 v234, 0xffff4000, v234\n"
      "v_mfma_f32_16x16x32_bf16 v[132:135], a[252:255], v[60:63], v[132:135]\n"
      "v_add_u32 v235, 0xffff4000, v235\n"
      "s_waitcnt lgkmcnt(5)\n"
      "v_mfma_f32_16x16x32_bf16 v[132:135], a[48:51], v[64:67], v[132:135]\n"
      "v_cvt_pk_bf16_f32 v210, v136, v137\n"
      "v_mfma_f32_16x16x32_bf16 v[132:135], a[52:55], v[68:71], v[132:135]\n"
      "v_cvt_pk_bf16_f32 v211, v138, v139\n"
      "v_mfma_f32_16x16x32_bf16 v[132:135], a[56:59], v[84:87], v[132:135]\n"
      "ds_read_b128 v[144:147], v246 offset:0\n"
      "v_mfma_f32_16x16x32_bf16 v[132:135], a[60:63], v[88:91], v[132:135]\n"
      "ds_read_b128 v[148:151], v246 offset:1024\n"
      "s_waitcnt lgkmcnt(2)\n"
      "v_mfma_f32_16x16x32_bf16 v[132:135], a[64:67], v[92:95], v[132:135]\n"
      "ds_read_b128 v[152:155], v246 offset:2048\n"
      "s_cbranch_vccnz LT58\n"
      "LS58:\n"
      "v_mfma_f32_16x16x32_bf16 v[132:135], a[68:71], v[108:111], v[132:135]\n"
      "v_lshlrev_b32 v0, 16, v192\n"
      "v_mfma_f32_16x16x32_bf16 v[132:135], a[72:75], v[112:115], v[132:135]\n"
      "ds_write_b64 v244, v[210:211] offset:12424\n"
      "v_mfma_f32_16x16x32_bf16 v[132:135], a[76:79], v[116:119], v[132:135]\n"
      "v_and_b32 v1, 0xffff0000, v192\n"
      "v_mfma_f32_16x16x32_bf16 v[136:139], a[80:83], v[36:39], v[4:7]\n"
      "v_lshlrev_b32 v2, 16, v193\n"
      "v_mfma_f32_16x16x32_bf16 v[136:139], a[84:87], v[40:43], v[136:139]\n"
      "v_and_b32 v3, 0xffff0000, v193\n"
      "v_mfma_f32_16x16x32_bf16 v[136:139], a[88:91], v[44:47], v[136:139]\n"
      "v_mfma_f32_16x16x32_bf16 v[136:139], a[92:95], v[60:63], v[136:139]\n"
      "v_cvt_pk_bf16_f32 v208, v132, v133\n"
      "v_mfma_f32_16x16x32_bf16 v[136:139], a[96:99], v[64:67], v[136:139]\n"
      "v_cvt_pk_bf16_f32 v209, v134, v135\n"
      "v_mfma_f32_16x16x32_bf16 v[136:139], a[100:103], v[68:71], v[136:139]\n"
      "v_lshlrev_b32 v4, 16, v194\n"
      "v_mfma_f32_16x16x32_bf16 v[136:139], a[104:107], v[84:87], v[136:139]\n"
      "v_and_b32 v5, 0xffff0000, v194\n"
      "v_mfma_f32_16x16x32_bf16 v[136:139], a[108:111], v[88:91], v[136:139]\n"
      "v_lshlrev_b32 v6, 16, v195\n"
      "v_mfma_f32_16x16x32_bf16 v[136:139], a[112:115], v[92:95], v[136:139]\n"
      "v_and_b32 v7, 0xffff0000, v195\n"
      "v_mfma_f32_16x16x32_bf16 v[136:139], a[116:119], v[108:111], v[136:139]\n"
      "ds_write_b64 v244, v[208:209] offset:24576\n"
      "v_mfma_f32_16x16x32_bf16 v[136:139], a[120:123], v[112:115], v[136:139]\n"
      "v_mfma_f32_16x16x32_bf16 v[136:139], a[124:127], v[116:119], v[136:139]\n"
      "v_mfma_f32_16x16x32_bf16 v[132:135], a[128:131], v[36:39], v[8:11]\n"
      "v_mfma_f32_16x16x32_bf16 v[132:135], a[132:135], v[40:43], v[132:135]\n"
      "v_mfma_f32_16x16x32_bf16 v[132:135], a[136:139], v[44:47], v[132:135]\n"
      "v_mfma_f32_16x16x32_bf16 v[132:135], a[140:143], v[60:63], v[132:135]\n"
      "v_cvt_pk_bf16_f32 v210, v136, v137\n"
      "v_mfma_f32_16x16x32_bf16 v[132:135], a[144:147], v[64:67], v[132:135]\n"
      "v_cvt_pk_bf16_f32 v211, v138, v139\n"
      "v_mfma_f32_16x16x32_bf16 v[132:135], a[148:151], v[68:71], v[132:135]\n"
      "v_lshlrev_b32 v8, 16, v196\n"
      "v_mfma_f32_16x16x32_bf16 v[132:135], a[152:155], v[84:87], v[132:135]\n"
      "v_and_b32 v9, 0xffff0000, v196\n"
      "v_mfma_f32_16x16x32_bf16 v[132:135], a[156:159], v[88:91], v[132:135]\n"
      "v_lshlrev_b32 v10, 16, v197\n"
      "v_mfma_f32_16x16x32_bf16 v[132:135], a[160:163], v[92:95], v[132:135]\n"
      "v_and_b32 v11, 0xffff0000, v197\n"
      "v_mfma_f32_16x16x32_bf16 v[132:135], a[164:167], v[108:111], v[132:135]\n"
      "ds_write_b64 v244, v[210:211] offset:24584\n"
      "v_mfma_f32_16x16x32_bf16 v[132:135], a[168:171], v[112:115], v[132:135]\n"
      "v_mfma_f32_16x16x32_bf16 v[132:135], a[172:175], v[116:119], v[132:135]\n"
      "v_mfma_f32_16x16x32_bf16 v[136:139], a[176:179], v[36:39], v[12:15]\n"
      "v_mfma_f32_16x16x32_bf16 v[136:139], a[180:183], v[40:43], v[136:139]\n"
      "v_mfma_f32_16x16x32_bf16 v[136:139], a[184:187], v[44:47], v[136:139]\n"
      "v_mfma_f32_16x16x32_bf16 v[136:139], a[188:191], v[60:63], v[136:139]\n"
      "v_cvt_pk_bf16_f32 v212, v132, v133\n"
      "v_mfma_f32_16x16x32_bf16 v[136:139], a[192:195], v[64:67], v[136:139]\n"
      "v_cvt_pk_bf16_f32 v213, v134, v135\n"
      "v_mfma_f32_16x16x32_bf16 v[136:139], a[196:199], v[68:71], v[136:139]\n"
      "v_lshlrev_b32 v12, 16, v198\n"
      "v_mfma_f32_16x16x32_bf16 v[136:139], a[200:203], v[84:87], v[136:139]\n"
      "v_and_b32 v13, 0xffff0000, v198\n"
      "v_mfma_f32_16x16x32_bf16 v[136:139], a[204:207], v[88:91], v[136:139]\n"
      "v_lshlrev_b32 v14, 16, v199\n"
      "v_mfma_f32_16x16x32_bf16 v[136:139], a[208:211], v[92:95], v[136:139]\n"
      "v_and_b32 v15, 0xffff0000, v199\n"
      "v_mfma_f32_16x16x32_bf16 v[136:139], a[212:215], v[108:111], v[136:139]\n"
      "ds_write_b64 v245, v[212:213] offset:24576\n"
      "v_mfma_f32_16x16x32_bf16 v[136:139], a[216:219], v[112:115], v[136:139]\n"
      "s_cbranch_vccnz LT59\n"
      "LS59:\n"
      "v_mfma_f32_16x16x32_bf16 v[136:139], a[220:223], v[116:119], v[136:139]\n"
      "v_mfma_f32_16x16x32_bf16 v[132:135], a[224:227], v[36:39], v[16:19]\n"
      "v_mfma_f32_16x16x32_bf16 v[132:135], a[228:231], v[40:43], v[132:135]\n"
      "v_mfma_f32_16x16x32_bf16 v[132:135], a[232:235], v[44:47], v[132:135]\n"
      "v_mfma_f32_16x16x32_bf16 v[132:135], a[236:239], v[60:63], v[132:135]\n"
      "s_memrealtime s[84:85]\n"
      "v_mfma_f32_16x16x32_bf16 v[132:135], a[0:3], v[64:67], v[132:135]\n"
      "v_cvt_pk_bf16_f32 v214, v136, v137\n"
      "v_mfma_f32_16x16x32_bf16 v[132:135], a[4:7], v[68:71], v[132:135]\n"
      "v_cvt_pk_bf16_f32 v215, v138, v139\n"
      "v_mfma_f32_16x16x32_bf16 v[132:135], a[8:11], v[84:87], v[132:135]\n"
      "v_lshlrev_b32 v16, 16, v200\n"
      "v_mfma_f32_16x16x32_bf16 v[132:135], a[12:15], v[88:91], v[132:135]\n"
      "v_and_b32 v17, 0xffff0000, v200\n"
      "v_mfma_f32_16x16x32_bf16 v[132:135], a[16:19], v[92:95], v[132:135]\n"
      "v_lshlrev_b32 v18, 16, v201\n"
      "v_mfma_f32_16x16x32_bf16 v[132:135], a[20:23], v[108:111], v[132:135]\n"
      "v_and_b32 v19, 0xffff0000, v201\n"
      "v_mfma_f32_16x16x32_bf16 v[132:135], v[140:143], v[112:115], v[132:135]\n"
      "s_waitcnt lgkmcnt(0)\n"
      "s_mov_b32 s66, s84\n"
      "v_mfma_f32_16x16x32_bf16 v[132:135], v[156:159], v[116:119], v[132:135]\n"
      "s_waitcnt vmcnt(9)\n"
      "s_barrier\n"
      "ds_write_b64 v245, v[214:215] offset:24584\n"
      "v_mfma_f32_16x16x32_bf16 v[136:139], v[160:163], v[36:39], v[20:23]\n"
      "ds_read_b128 v[36:39], v232 offset:0\n"
      "v_mfma_f32_16x16x32_bf16 v[136:139], v[164:167], v[40:43], v[136:139]\n"
      "ds_read_b128 v[40:43], v233 offset:0\n"
      "v_mfma_f32_16x16x32_bf16 v[136:139], v[180:183], v[44:47], v[136:139]\n"
      "ds_read_b128 v[44:47], v234 offset:0\n"
      "v_mfma_f32_16x16x32_bf16 v[136:139], v[184:187], v[60:63], v[136:139]\n"
      "ds_read_b128 v[60:63], v235 offset:0\n"
      "v_cvt_pk_bf16_f32 v208, v132, v133\n"
      "v_mfma_f32_16x16x32_bf16 v[136:139], v[188:191], v[64:67], v[136:139]\n"
      "ds_read_b128 v[64:67], v232 offset:8192\n"
      "v_cvt_pk_bf16_f32 v209, v134, v135\n"
      "v_mfma_f32_16x16x32_bf16 v[136:139], v[204:207], v[68:71], v[136:139]\n"
      "ds_read_b128 v[68:71], v233 offset:8192\n"
      "v_mfma_f32_16x16x32_bf16 v[136:139], a[24:27], v[84:87], v[136:139]\n"
      "ds_read_b128 v[84:87], v234 offset:8192\n"
      "v_mfma_f32_16x16x32_bf16 v[136:139], a[28:31], v[88:91], v[136:139]\n"
      "ds_read_b128 v[88:91], v235 offset:8192\n"
      "v_mfma_f32_16x16x32_bf16 v[136:139], a[32:35], v[92:95], v[136:139]\n"
      "ds_read_b128 v[92:95], v232 offset:16384\n"
      "v_mfma_f32_16x16x32_bf16 v[136:139], a[36:39], v[108:111], v[136:139]\n"
      "ds_read_b128 v[108:111], v233 offset:16384\n"
      "ds_write_b64 v244, v[208:209] offset:24704\n"
      "v_mfma_f32_16x16x32_bf16 v[136:139], a[40:43], v[112:115], v[136:139]\n"
      "ds_read_b128 v[112:115], v234 offset:16384\n"
      "v_mfma_f32_16x16x32_bf16 v[136:139], a[44:47], v[116:119], v[136:139]\n"
      "ds_read_b128 v[116:119], v235 offset:16384\n"
      "s_waitcnt lgkmcnt(9)\n"
      "v_mfma_f32_16x16x32_bf16 v[132:135], a[240:243], v[36:39], v[0:3]\n"
      "s_sub_u32 s69, s66, s68\n"
      "v_mfma_f32_16x16x32_bf16 v[132:135], a[244:247], v[40:43], v[132:135]\n"
      "s_mov_b32 s68, s67\n"
      "v_mfma_f32_16x16x32_bf16 v[132:135], a[248:251], v[44:47], v[132:135]\n"
      "s_mov_b32 s67, s66\n"
      "v_mfma_f32_16x16x32_bf16 v[132:135], a[252:255], v[60:63], v[132:135]\n"
      "s_cmp_gt_i32 s69, 460\n"
      "s_cselect_b64 s[70:71], -1, s[70:71]\n"
      "global_store_dwordx4 v247, v[96:99], s[54:55] offset:768 sc1\n"
      "s_waitcnt lgkmcnt(5)\n"
      "v_mfma_f32_16x16x32_bf16 v[132:135], a[48:51], v[64:67], v[132:135]\n"
      "s_mov_b64 vcc, s[70:71]\n"
      "v_cvt_pk_bf16_f32 v211, v138, v139\n"
      "v_mfma_f32_16x16x32_bf16 v[132:135], a[52:55], v[68:71], v[132:135]\n"
      "v_lshlrev_b32 v20, 16, v202\n"
      "v_mfma_f32_16x16x32_bf16 v[132:135], a[56:59], v[84:87], v[132:135]\n"
      "v_and_b32 v21, 0xffff0000, v202\n"
      "v_mfma_f32_16x16x32_bf16 v[132:135], a[60:63], v[88:91], v[132:135]\n"
      "v_lshlrev_b32 v22, 16, v203\n"
      "s_waitcnt lgkmcnt(0)\n"
      "v_mfma_f32_16x16x32_bf16 v[132:135], a[64:67], v[92:95], v[132:135]\n"
      "v_and_b32 v23, 0xffff0000, v203\n"
      "s_cbranch_vccnz LT60\n"
      "LS60:\n"
      "v_mfma_f32_16x16x32_bf16 v[132:135], a[68:71], v[108:111], v[132:135]\n"
      "v_cvt_pk_bf16_f32 v210, v136, v137\n"
      "v_mfma_f32_16x16x32_bf16 v[132:135], a[72:75], v[112:115], v[132:135]\n"
      "ds_read_b128 v[24:27], v246 offset:12288\n"
      "v_mfma_f32_16x16x32_bf16 v[132:135], a[76:79], v[116:119], v[132:135]\n"
      "ds_read_b128 v[28:31], v246 offset:13312\n"
      "v_mfma_f32_16x16x32_bf16 v[136:139], a[80:83], v[36:39], v[4:7]\n"
      "ds_read_b128 v[32:35], v246 offset:14336\n"
      "v_mfma_f32_16x16x32_bf16 v[136:139], a[84:87], v[40:43], v[136:139]\n"
      "v_mfma_f32_16x16x32_bf16 v[136:139], a[88:91], v[44:47], v[136:139]\n"
      "ds_write_b64 v244, v[210:211] offset:24712\n"
      "v_mfma_f32_16x16x32_bf16 v[136:139], a[92:95], v[60:63], v[136:139]\n"
      "v_cvt_pk_bf16_f32 v208, v132, v133\n"
      "global_store_dwordx4 v252, v[100:103], s[54:55] offset:768 sc1\n"
      "v_mfma_f32_16x16x32_bf16 v[136:139], a[96:99], v[64:67], v[136:139]\n"
      "v_cvt_pk_bf16_f32 v209, v134, v135\n"
      "v_mfma_f32_16x16x32_bf16 v[136:139], a[100:103], v[68:71], v[136:139]\n"
      "v_mfma_f32_16x16x32_bf16 v[136:139], a[104:107], v[84:87], v[136:139]\n"
      "v_mfma_f32_16x16x32_bf16 v[136:139], a[108:111], v[88:91], v[136:139]\n"
      "v_mfma_f32_16x16x32_bf16 v[136:139], a[112:115], v[92:95], v[136:139]\n"
      "v_mfma_f32_16x16x32_bf16 v[136:139], a[116:119], v[108:111], v[136:139]\n"
      "ds_write_b64 v244, v[208:209] offset:0\n"
      "v_mfma_f32_16x16x32_bf16 v[136:139], a[120:123], v[112:115], v[136:139]\n"
      "v_mfma_f32_16x16x32_bf16 v[136:139], a[124:127], v[116:119], v[136:139]\n"
      "v_mfma_f32_16x16x32_bf16 v[132:135], a[128:131], v[36:39], v[8:11]\n"
      "v_mfma_f32_16x16x32_bf16 v[132:135], a[132:135], v[40:43], v[132:135]\n"
      "v_mfma_f32_16x16x32_bf16 v[132:135], a[136:139], v[44:47], v[132:135]\n"
      "v_mfma_f32_16x16x32_bf16 v[132:135], a[140:143], v[60:63], v[132:135]\n"
      "v_cvt_pk_bf16_f32 v210, v136, v137\n"
      "v_mfma_f32_16x16x32_bf16 v[132:135], a[144:147], v[64:67], v[132:135]\n"
      "v_cvt_pk_bf16_f32 v211, v138, v139\n"
      "v_mfma_f32_16x16x32_bf16 v[132:135], a[148:151], v[68:71], v[132:135]\n"
      "global_store_dwordx4 v253, v[104:107], s[54:55] offset:768 sc1\n"
      "v_mfma_f32_16x16x32_bf16 v[132:135], a[152:155], v[84:87], v[132:135]\n"
      "v_mfma_f32_16x16x32_bf16 v[132:135], a[156:159], v[88:91], v[132:135]\n"
      "s_lshl_b32 s79, s96, 1\n"
      "v_mfma_f32_16x16x32_bf16 v[132:135], a[160:163], v[92:95], v[132:135]\n"
      "v_mad_i32_i24 v252, v251, s79, v252\n"
      "v_mfma_f32_16x16x32_bf16 v[132:135], a[164:167], v[108:111], v[132:135]\n"
      "ds_write_b64 v244, v[210:211] offset:8\n"
      "v_mfma_f32_16x16x32_bf16 v[132:135], a[168:171], v[112:115], v[132:135]\n"
      "s_mul_i32 s79, s79, s100\n"
      "v_mfma_f32_16x16x32_bf16 v[132:135], a[172:175], v[116:119], v[132:135]\n"
      "v_add_u32 v247, s79, v247\n"
      "v_mfma_f32_16x16x32_bf16 v[136:139], a[176:179], v[36:39], v[12:15]\n"
      "s_mul_i32 s79, s79, s101\n"
      "v_mfma_f32_16x16x32_bf16 v[136:139], a[180:183], v[40:43], v[136:139]\n"
      "v_add_u32 v253, s79, v253\n"
      "v_mfma_f32_16x16x32_bf16 v[136:139], a[184:187], v[44:47], v[136:139]\n"
      "s_add_u32 s54, s30, s39\n"
      "s_addc_u32 s55, s31, 0\n"
      "v_mfma_f32_16x16x32_bf16 v[136:139], a[188:191], v[60:63], v[136:139]\n"
      "v_cvt_pk_bf16_f32 v212, v132, v133\n"
      "v_mfma_f32_16x16x32_bf16 v[136:139], a[192:195], v[64:67], v[136:139]\n"
      "v_cvt_pk_bf16_f32 v213, v134, v135\n"
      "v_mfma_f32_16x16x32_bf16 v[136:139], a[196:199], v[68:71], v[136:139]\n"
      "global_store_dwordx4 v247, v[120:123], s[54:55] offset:0 sc1\n"
      "v_mfma_f32_16x16x32_bf16 v[136:139], a[200:203], v[84:87], v[136:139]\n"
      "v_mfma_f32_16x16x32_bf16 v[136:139], a[204:207], v[88:91], v[136:139]\n"
      "v_mfma_f32_16x16x32_bf16 v[136:139], a[208:211], v[92:95], v[136:139]\n"
      "s_cbranch_vccnz LT61\n"
      "LS61:\n"
      "v_mfma_f32_16x16x32_bf16 v[136:139], a[212:215], v[108:111], v[136:139]\n"
      "ds_write_b64 v245, v[212:213] offset:0\n"
      "v_mfma_f32_16x16x32_bf16 v[136:139], a[216:219], v[112:115], v[136:139]\n"
      "v_mfma_f32_16x16x32_bf16 v[136:139], a[220:223], v[116:119], v[136:139]\n"
      "v_mfma_f32_16x16x32_bf16 v[132:135], a[224:227], v[36:39], v[16:19]\n"
      "v_mfma_f32_16x16x32_bf16 v[132:135], a[228:231], v[40:43], v[132:135]\n"
      "v_mfma_f32_16x16x32_bf16 v[132:135], a[232:235], v[44:47], v[132:135]\n"
      "v_mfma_f32_16x16x32_bf16 v[132:135], a[236:239], v[60:63], v[132:135]\n"
      "v_cvt_pk_bf16_f32 v214, v136, v137\n"
      "v_mfma_f32_16x16x32_bf16 v[132:135], a[0:3], v[64:67], v[132:135]\n"
      "v_cvt_pk_bf16_f32 v215, v138, v139\n"
      "v_mfma_f32_16x16x32_bf16 v[132:135], a[4:7], v[68:71], v[132:135]\n"
      "v_mfma_f32_16x16x32_bf16 v[132:135], a[8:11], v[84:87], v[132:135]\n"
      "global_store_dwordx4 v252, v[124:127], s[54:55] offset:0 sc1\n"
      "v_mfma_f32_16x16x32_bf16 v[132:135], a[12:15], v[88:91], v[132:135]\n"
      "v_mfma_f32_16x16x32_bf16 v[132:135], a[16:19], v[92:95], v[132:135]\n"
      "v_mfma_f32_16x16x32_bf16 v[132:135], a[20:23], v[108:111], v[132:135]\n"
      "ds_write_b64 v245, v[214:215] offset:8\n"
      "v_mfma_f32_16x16x32_bf16 v[132:135], v[140:143], v[112:115], v[132:135]\n"
      "v_mfma_f32_16x16x32_bf16 v[132:135], v[156:159], v[116:119], v[132:135]\n"
      "s_waitcnt lgkmcnt(4)\n"
      "s_barrier\n"
      "v_mfma_f32_16x16x32_bf16 v[136:139], v[160:163], v[36:39], v[20:23]\n"
      "ds_read_b128 v[36:39], v232 offset:256\n"
      "v_mfma_f32_16x16x32_bf16 v[136:139], v[164:167], v[40:43], v[136:139]\n"
      "ds_read_b128 v[40:43], v233 offset:256\n"
      "v_mfma_f32_16x16x32_bf16 v[136:139], v[180:183], v[44:47], v[136:139]\n"
      "ds_read_b128 v[44:47], v234 offset:256\n"
      "v_mfma_f32_16x16x32_bf16 v[136:139], v[184:187], v[60:63], v[136:139]\n"
      "ds_read_b128 v[60:63], v235 offset:256\n"
      "v_cvt_pk_bf16_f32 v208, v132, v133\n"
      "v_mfma_f32_16x16x32_bf16 v[136:139], v[188:191], v[64:67], v[136:139]\n"
      "ds_read_b128 v[64:67], v232 offset:8448\n"
      "v_cvt_pk_bf16_f32 v209, v134, v135\n"
      "v_mfma_f32_16x16x32_bf16 v[136:139], v[204:207], v[68:71], v[136:139]\n"
      "ds_read_b128 v[68:71], v233 offset:8448\n"
      "v_mfma_f32_16x16x32_bf16 v[136:139], a[24:27], v[84:87], v[136:139]\n"
      "ds_read_b128 v[84:87], v234 offset:8448\n"
      "v_mfma_f32_16x16x32_bf16 v[136:139], a[28:31], v[88:91], v[136:139]\n"
      "ds_read_b128 v[88:91], v235 offset:8448\n"
      "v_mfma_f32_16x16x32_bf16 v[136:139], a[32:35], v[92:95], v[136:139]\n"
      "ds_read_b128 v[92:95], v232 offset:16640\n"
      "v_mfma_f32_16x16x32_bf16 v[136:139], a[36:39], v[108:111], v[136:139]\n"
      "ds_read_b128 v[108:111], v233 offset:16640\n"
      "v_mfma_f32_16x16x32_bf16 v[136:139], a[40:43], v[112:115], v[136:139]\n"
      "ds_read_b128 v[112:115], v234 offset:16640\n"
      "v_mfma_f32_16x16x32_bf16 v[136:139], a[44:47], v[116:119], v[136:139]\n"
      "ds_read_b128 v[116:119], v235 offset:16640\n"
      "s_waitcnt lgkmcnt(8)\n"
      "v_mfma_f32_16x16x32_bf16 v[132:135], a[240:243], v[36:39], v[0:3]\n"
      "ds_write_b64 v244, v[208:209] offset:128\n"
      "v_mfma_f32_16x16x32_bf16 v[132:135], a[244:247], v[40:43], v[132:135]\n"
      "v_mfma_f32_16x16x32_bf16 v[132:135], a[248:251], v[44:47], v[132:135]\n"
      "v_mfma_f32_16x16x32_bf16 v[132:135], a[252:255], v[60:63], v[132:135]\n"
      "v_cvt_pk_bf16_f32 v210, v136, v137\n"
      "global_store_dwordx4 v253, v[128:131], s[54:55] offset:0 sc1\n"
      "s_waitcnt lgkmcnt(5)\n"
      "v_mfma_f32_16x16x32_bf16 v[132:135], a[48:51], v[64:67], v[132:135]\n"
      "v_cvt_pk_bf16_f32 v211, v138, v139\n"
      "v_mfma_f32_16x16x32_bf16 v[132:135], a[52:55], v[68:71], v[132:135]\n"
      "v_mfma_f32_16x16x32_bf16 v[132:135], a[56:59], v[84:87], v[132:135]\n"
      "ds_read_b128 v[48:51], v246 offset:24576\n"
      "v_mfma_f32_16x16x32_bf16 v[132:135], a[60:63], v[88:91], v[132:135]\n"
      "ds_read_b128 v[52:55], v246 offset:25600\n"
      "s_waitcnt lgkmcnt(3)\n"
      "v_mfma_f32_16x16x32_bf16 v[132:135], a[64:67], v[92:95], v[132:135]\n"
      "ds_read_b128 v[56:59], v246 offset:26624\n"
      "v_mfma_f32_16x16x32_bf16 v[132:135], a[68:71], v[108:111], v[132:135]\n"
      "ds_write_b64 v244, v[210:211] offset:136\n"
      "v_mfma_f32_16x16x32_bf16 v[132:135], a[72:75], v[112:115], v[132:135]\n"
      "s_cbranch_vccnz LT62\n"
      "LS62:\n"
      "v_mfma_f32_16x16x32_bf16 v[132:135], a[76:79], v[116:119], v[132:135]\n"
      "v_mfma_f32_16x16x32_bf16 v[136:139], a[80:83], v[36:39], v[4:7]\n"
      "v_mfma_f32_16x16x32_bf16 v[136:139], a[84:87], v[40:43], v[136:139]\n"
      "v_mfma_f32_16x16x32_bf16 v[136:139], a[88:91], v[44:47], v[136:139]\n"
      "v_mfma_f32_16x16x32_bf16 v[136:139], a[92:95], v[60:63], v[136:139]\n"
      "v_cvt_pk_bf16_f32 v208, v132, v133\n"
      "global_store_dwordx4 v247, v[144:147], s[54:55] offset:768 sc1\n"
      "v_mfma_f32_16x16x32_bf16 v[136:139], a[96:99], v[64:67], v[136:139]\n"
      "v_cvt_pk_bf16_f32 v209, v134, v135\n"
      "v_mfma_f32_16x16x32_bf16 v[136:139], a[100:103], v[68:71], v[136:139]\n"
      "v_mfma_f32_16x16x32_bf16 v[136:139], a[104:107], v[84:87], v[136:139]\n"
      "v_mfma_f32_16x16x32_bf16 v[136:139], a[108:111], v[88:91], v[136:139]\n"
      "v_mfma_f32_16x16x32_bf16 v[136:139], a[112:115], v[92:95], v[136:139]\n"
      "v_mfma_f32_16x16x32_bf16 v[136:139], a[116:119], v[108:111], v[136:139]\n"
      "ds_write_b64 v244, v[208:209] offset:12288\n"
      "v_mfma_f32_16x16x32_bf16 v[136:139], a[120:123], v[112:115], v[136:139]\n"
      "v_mfma_f32_16x16x32_bf16 v[136:139], a[124:127], v[116:119], v[136:139]\n"
      "v_mfma_f32_16x16x32_bf16 v[132:135], a[128:131], v[36:39], v[8:11]\n"
      "v_mfma_f32_16x16x32_bf16 v[132:135], a[132:135], v[40:43], v[132:135]\n"
      "v_mfma_f32_16x16x32_bf16 v[132:135], a[136:139], v[44:47], v[132:135]\n"
      "v_mfma_f32_16x16x32_bf16 v[132:135], a[140:143], v[60:63], v[132:135]\n"
      "v_cvt_pk_bf16_f32 v210, v136, v137\n"
      "v_mfma_f32_16x16x32_bf16 v[132:135], a[144:147], v[64:67], v[132:135]\n"
      "v_cvt_pk_bf16_f32 v211, v138, v139\n"
      "v_mfma_f32_16x16x32_bf16 v[132:135], a[148:151], v[68:71], v[132:135]\n"
      "global_store_dwordx4 v252, v[148:151], s[54:55] offset:768 sc1\n"
      "v_mfma_f32_16x16x32_bf16 v[132:135], a[152:155], v[84:87], v[132:135]\n"
      "v_mfma_f32_16x16x32_bf16 v[132:135], a[156:159], v[88:91], v[132:135]\n"
      "v_mfma_f32_16x16x32_bf16 v[132:135], a[160:163], v[92:95], v[132:135]\n"
      "v_mfma_f32_16x16x32_bf16 v[132:135], a[164:167], v[108:111], v[132:135]\n"
      "ds_write_b64 v244, v[210:211] offset:12296\n"
      "v_mfma_f32_16x16x32_bf16 v[132:135], a[168:171], v[112:115], v[132:135]\n"
      "v_mfma_f32_16x16x32_bf16 v[132:135], a[172:175], v[116:119], v[132:135]\n"
      "v_mfma_f32_16x16x32_bf16 v[136:139], a[176:179], v[36:39], v[12:15]\n"
      "v_mfma_f32_16x16x32_bf16 v[136:139], a[180:183], v[40:43], v[136:139]\n"
      "v_mfma_f32_16x16x32_bf16 v[136:139], a[184:187], v[44:47], v[136:139]\n"
      "v_mfma_f32_16x16x32_bf16 v[136:139], a[188:191], v[60:63], v[136:139]\n"
      "v_cvt_pk_bf16_f32 v212, v132, v133\n"
      "v_mfma_f32_16x16x32_bf16 v[136:139], a[192:195], v[64:67], v[136:139]\n"
      "v_cvt_pk_bf16_f32 v213, v134, v135\n"
      "v_mfma_f32_16x16x32_bf16 v[136:139], a[196:199], v[68:71], v[136:139]\n"
      "v_mfma_f32_16x16x32_bf16 v[136:139], a[200:203], v[84:87], v[136:139]\n"
      "global_store_dwordx4 v253, v[152:155], s[54:55] offset:768 sc1\n"
      "v_mfma_f32_16x16x32_bf16 v[136:139], a[204:207], v[88:91], v[136:139]\n"
      "v_mfma_f32_16x16x32_bf16 v[136:139], a[208:211], v[92:95], v[136:139]\n"
      "s_cbranch_vccnz LT63\n"
      "LS63:\n"
      "v_mfma_f32_16x16x32_bf16 v[136:139], a[212:215], v[108:111], v[136:139]\n"
      "ds_write_b64 v245, v[212:213] offset:12288\n"
      "v_mfma_f32_16x16x32_bf16 v[136:139], a[216:219], v[112:115], v[136:139]\n"
      "v_mfma_f32_16x16x32_bf16 v[136:139], a[220:223], v[116:119], v[136:139]\n"
      "v_mfma_f32_16x16x32_bf16 v[132:135], a[224:227], v[36:39], v[16:19]\n"
      "v_mfma_f32_16x16x32_bf16 v[132:135], a[228:231], v[40:43], v[132:135]\n"
      "v_mfma_f32_16x16x32_bf16 v[132:135], a[232:235], v[44:47], v[132:135]\n"
      "v_mfma_f32_16x16x32_bf16 v[132:135], a[236:239], v[60:63], v[132:135]\n"
      "s_memrealtime s[84:85]\n"
      "s_add_u32 s54, s44, s39\n"
      "s_addc_u32 s55, s45, 0\n"
      "v_mfma_f32_16x16x32_bf16 v[132:135], a[0:3], v[64:67], v[132:135]\n"
      "v_cvt_pk_bf16_f32 v214, v136, v137\n"
      "v_mfma_f32_16x16x32_bf16 v[132:135], a[4:7], v[68:71], v[132:135]\n"
      "v_cvt_pk_bf16_f32 v215, v138, v139\n"
      "v_mfma_f32_16x16x32_bf16 v[132:135], a[8:11], v[84:87], v[132:135]\n"
      "global_store_dwordx4 v247, v[24:27], s[54:55] offset:0 sc1\n"
      "v_mfma_f32_16x16x32_bf16 v[132:135], a[12:15], v[88:91], v[132:135]\n"
      "v_mfma_f32_16x16x32_bf16 v[132:135], a[16:19], v[92:95], v[132:135]\n"
      "v_mfma_f32_16x16x32_bf16 v[132:135], a[20:23], v[108:111], v[132:135]\n"
      "v_mfma_f32_16x16x32_bf16 v[132:135], v[140:143], v[112:115], v[132:135]\n"
      "s_waitcnt lgkmcnt(0)\n"
      "s_mov_b32 s66, s84\n"
      "v_mfma_f32_16x16x32_bf16 v[132:135], v[156:159], v[116:119], v[132:135]\n"
      "s_waitcnt vmcnt(10)\n"
      "s_barrier\n"
      "ds_write_b64 v245, v[214:215] offset:12296\n"
      "v_mfma_f32_16x16x32_bf16 v[136:139], v[160:163], v[36:39], v[20:23]\n"
      "ds_read_b128 v[36:39], v232 offset:24576\n"
      "v_mfma_f32_16x16x32_bf16 v[136:139], v[164:167], v[40:43], v[136:139]\n"
      "ds_read_b128 v[40:43], v233 offset:24576\n"
      "v_mfma_f32_16x16x32_bf16 v[136:139], v[180:183], v[44:47], v[136:139]\n"
      "ds_read_b128 v[44:47], v234 offset:24576\n"
      "v_mfma_f32_16x16x32_bf16 v[136:139], v[184:187], v[60:63], v[136:139]\n"
      "ds_read_b128 v[60:63], v235 offset:24576\n"
      "v_cvt_pk_bf16_f32 v208, v132, v133\n"
      "v_mfma_f32_16x16x32_bf16 v[136:139], v[188:191], v[64:67], v[136:139]\n"
      "ds_read_b128 v[64:67], v232 offset:32768\n"
      "v_cvt_pk_bf16_f32 v209, v134, v135\n"
      "v_mfma_f32_16x16x32_bf16 v[136:139], v[204:207], v[68:71], v[136:139]\n"
      "ds_read_b128 v[68:71], v233 offset:32768\n"
      "v_mfma_f32_16x16x32_bf16 v[136:139], a[24:27], v[84:87], v[136:139]\n"
      "ds_read_b128 v[84:87], v234 offset:32768\n"
      "v_mfma_f32_16x16x32_bf16 v[136:139], a[28:31], v[88:91], v[136:139]\n"
      "ds_read_b128 v[88:91], v235 offset:32768\n"
      "v_mfma_f32_16x16x32_bf16 v[136:139], a[32:35], v[92:95], v[136:139]\n"
      "ds_read_b128 v[92:95], v232 offset:40960\n"
      "v_mfma_f32_16x16x32_bf16 v[136:139], a[36:39], v[108:111], v[136:139]\n"
      "ds_read_b128 v[108:111], v233 offset:40960\n"
      "ds_write_b64 v244, v[208:209] offset:12416\n"
      "v_mfma_f32_16x16x32_bf16 v[136:139], a[40:43], v[112:115], v[136:139]\n"
      "ds_read_b128 v[112:115], v234 offset:40960\n"
      "v_mfma_f32_16x16x32_bf16 v[136:139], a[44:47], v[116:119], v[136:139]\n"
      "ds_read_b128 v[116:119], v235 offset:40960\n"
      "s_waitcnt lgkmcnt(9)\n"
      "v_mfma_f32_16x16x32_bf16 v[132:135], a[240:243], v[36:39], v[0:3]\n"
      "s_sub_u32 s69, s66, s68\n"
      "v_mfma_f32_16x16x32_bf16 v[132:135], a[244:247], v[40:43], v[132:135]\n"
      "s_mov_b32 s68, s67\n"
      "v_mfma_f32_16x16x32_bf16 v[132:135], a[248:251], v[44:47], v[132:135]\n"
      "s_mov_b32 s67, s66\n"
      "v_mfma_f32_16x16x32_bf16 v[132:135], a[252:255], v[60:63], v[132:135]\n"
      "s_cmp_gt_i32 s69, 460\n"
      "s_cselect_b64 s[70:71], -1, s[70:71]\n"
      "s_waitcnt lgkmcnt(5)\n"
      "v_mfma_f32_16x16x32_bf16 v[132:135], a[48:51], v[64:67], v[132:135]\n"
      "s_mov_b64 vcc, s[70:71]\n"
      "global_store_dwordx4 v252, v[28:31], s[54:55] offset:0 sc1\n"
      "v_mfma_f32_16x16x32_bf16 v[132:135], a[52:55], v[68:71], v[132:135]\n"
      "v_cvt_pk_bf16_f32 v210, v136, v137\n"
      "v_mfma_f32_16x16x32_bf16 v[132:135], a[56:59], v[84:87], v[132:135]\n"
      "v_cvt_pk_bf16_f32 v211, v138, v139\n"
      "v_mfma_f32_16x16x32_bf16 v[132:135], a[60:63], v[88:91], v[132:135]\n"
      "ds_read_b128 v[72:75], v246 offset:0\n"
      "s_waitcnt lgkmcnt(1)\n"
      "v_mfma_f32_16x16x32_bf16 v[132:135], a[64:67], v[92:95], v[132:135]\n"
      "ds_read_b128 v[76:79], v246 offset:1024\n"
      "v_mfma_f32_16x16x32_bf16 v[132:135], a[68:71], v[108:111], v[132:135]\n"
      "ds_read_b128 v[80:83], v246 offset:2048\n"
      "v_mfma_f32_16x16x32_bf16 v[132:135], a[72:75], v[112:115], v[132:135]\n"
      "s_cbranch_vccnz LT64\n"
      "LS64:\n"
      "v_mfma_f32_16x16x32_bf16 v[132:135], a[76:79], v[116:119], v[132:135]\n"
      "ds_write_b64 v244, v[210:211] offset:12424\n"
      "v_mfma_f32_16x16x32_bf16 v[136:139], a[80:83], v[36:39], v[4:7]\n"
      "v_mfma_f32_16x16x32_bf16 v[136:139], a[84:87], v[40:43], v[136:139]\n"
      "v_mfma_f32_16x16x32_bf16 v[136:139], a[88:91], v[44:47], v[136:139]\n"
      "v_mfma_f32_16x16x32_bf16 v[136:139], a[92:95], v[60:63], v[136:139]\n"
      "v_cvt_pk_bf16_f32 v208, v132, v133\n"
      "global_store_dwordx4 v253, v[32:35], s[54:55] offset:0 sc1\n"
      "v_mfma_f32_16x16x32_bf16 v[136:139], a[96:99], v[64:67], v[136:139]\n"
      "v_cvt_pk_bf16_f32 v209, v134, v135\n"
      "v_mfma_f32_16x16x32_bf16 v[136:139], a[100:103], v[68:71], v[136:139]\n"
      "v_mfma_f32_16x16x32_bf16 v[136:139], a[104:107], v[84:87], v[136:139]\n"
      "v_mfma_f32_16x16x32_bf16 v[136:139], a[108:111], v[88:91], v[136:139]\n"
      "v_mfma_f32_16x16x32_bf16 v[136:139], a[112:115], v[92:95], v[136:139]\n"
      "v_mfma_f32_16x16x32_bf16 v[136:139], a[116:119], v[108:111], v[136:139]\n"
      "ds_write_b64 v244, v[208:209] offset:24576\n"
      "v_mfma_f32_16x16x32_bf16 v[136:139], a[120:123], v[112:115], v[136:139]\n"
      "v_mfma_f32_16x16x32_bf16 v[136:139], a[124:127], v[116:119], v[136:139]\n"
      "v_mfma_f32_16x16x32_bf16 v[132:135], a[128:131], v[36:39], v[8:11]\n"
      "v_mfma_f32_16x16x32_bf16 v[132:135], a[132:135], v[40:43], v[132:135]\n"
      "v_mfma_f32_16x16x32_bf16 v[132:135], a[136:139], v[44:47], v[132:135]\n"
      "v_mfma_f32_16x16x32_bf16 v[132:135], a[140:143], v[60:63], v[132:135]\n"
      "v_cvt_pk_bf16_f32 v210, v136, v137\n"
      "v_mfma_f32_16x16x32_bf16 v[132:135], a[144:147], v[64:67], v[132:135]\n"
      "v_cvt_pk_bf16_f32 v211, v138, v139\n"
      "v_mfma_f32_16x16x32_bf16 v[132:135], a[148:151], v[68:71], v[132:135]\n"
      "global_store_dwordx4 v247, v[48:51], s[54:55] offset:768 sc1\n"
      "LPD16:\n"
      "s_cbranch_execz LRET\n"
      "v_mfma_f32_16x16x32_bf16 v[132:135], a[152:155], v[84:87], v[132:135]\n"
      "v_mfma_f32_16x16x32_bf16 v[132:135], a[156:159], v[88:91], v[132:135]\n"
      "v_mfma_f32_16x16x32_bf16 v[132:135], a[160:163], v[92:95], v[132:135]\n"
      "v_mfma_f32_16x16x32_bf16 v[132:135], a[164:167], v[108:111], v[132:135]\n"
      "ds_write_b64 v244, v[210:211] offset:24584\n"
      "v_mfma_f32_16x16x32_bf16 v[132:135], a[168:171], v[112:115], v[132:135]\n"
      "v_mfma_f32_16x16x32_bf16 v[132:135], a[172:175], v[116:119], v[132:135]\n"
      "v_mfma_f32_16x16x32_bf16 v[136:139], a[176:179], v[36:39], v[12:15]\n"
      "v_mfma_f32_16x16x32_bf16 v[136:139], a[180:183], v[40:43], v[136:139]\n"
      "v_mfma_f32_16x16x32_bf16 v[136:139], a[184:187], v[44:47], v[136:139]\n"
      "v_mfma_f32_16x16x32_bf16 v[136:139], a[188:191], v[60:63], v[136:139]\n"
      "v_cvt_pk_bf16_f32 v212, v132, v133\n"
      "v_mfma_f32_16x16x32_bf16 v[136:139], a[192:195], v[64:67], v[136:139]\n"
      "v_cvt_pk_bf16_f32 v213, v134, v135\n"
      "v_mfma_f32_16x16x32_bf16 v[136:139], a[196:199], v[68:71], v[136:139]\n"
      "v_mfma_f32_16x16x32_bf16 v[136:139], a[200:203], v[84:87], v[136:139]\n"
      "global_store_dwordx4 v252, v[52:55], s[54:55] offset:768 sc1\n"
      "v_mfma_f32_16x16x32_bf16 v[136:139], a[204:207], v[88:91], v[136:139]\n"
      "v_mfma_f32_16x16x32_bf16 v[136:139], a[208:211], v[92:95], v[136:139]\n"
      "s_cbranch_vccnz LT65\n"
      "LS65:\n"
      "v_mfma_f32_16x16x32_bf16 v[136:139], a[212:215], v[108:111], v[136:139]\n"
      "ds_write_b64 v245, v[212:213] offset:24576\n"
      "v_mfma_f32_16x16x32_bf16 v[136:139], a[216:219], v[112:115], v[136:139]\n"
      "v_mfma_f32_16x16x32_bf16 v[136:139], a[220:223], v[116:119], v[136:139]\n"
      "v_mfma_f32_16x16x32_bf16 v[132:135], a[224:227], v[36:39], v[16:19]\n"
      "v_mfma_f32_16x16x32_bf16 v[132:135], a[228:231], v[40:43], v[132:135]\n"
      "v_mfma_f32_16x16x32_bf16 v[132:135], a[232:235], v[44:47], v[132:135]\n"
      "v_mfma_f32_16x16x32_bf16 v[132:135], a[236:239], v[60:63], v[132:135]\n"
      "v_cvt_pk_bf16_f32 v214, v136, v137\n"
      "v_mfma_f32_16x16x32_bf16 v[132:135], a[0:3], v[64:67], v[132:135]\n"
      "v_cvt_pk_bf16_f32 v215, v138, v139\n"
      "v_mfma_f32_16x16x32_bf16 v[132:135], a[4:7], v[68:71], v[132:135]\n"
      "v_mfma_f32_16x16x32_bf16 v[132:135], a[8:11], v[84:87], v[132:135]\n"
      "global_store_dwordx4 v253, v[56:59], s[54:55] offset:768 sc1\n"
      "v_mfma_f32_16x16x32_bf16 v[132:135], a[12:15], v[88:91], v[132:135]\n"
      "v_mfma_f32_16x16x32_bf16 v[132:135], a[16:19], v[92:95], v[132:135]\n"
      "v_mfma_f32_16x16x32_bf16 v[132:135], a[20:23], v[108:111], v[132:135]\n"
      "ds_write_b64 v245, v[214:215] offset:24584\n"
      "v_mfma_f32_16x16x32_bf16 v[132:135], v[140:143], v[112:115], v[132:135]\n"
      "v_mfma_f32_16x16x32_bf16 v[132:135], v[156:159], v[116:119], v[132:135]\n"
      "s_waitcnt lgkmcnt(4)\n"
      "s_barrier\n"
      "v_mfma_f32_16x16x32_bf16 v[136:139], v[160:163], v[36:39], v[20:23]\n"
      "ds_read_b128 v[36:39], v232 offset:24832\n"
      "s_lshl_b32 s79, s97, 1\n"
      "v_mfma_f32_16x16x32_bf16 v[136:139], v[164:167], v[40:43], v[136:139]\n"
      "ds_read_b128 v[40:43], v233 offset:24832\n"
      "v_mad_i32_i24 v252, v251, s79, v252\n"
      "v_mfma_f32_16x16x32_bf16 v[136:139], v[180:183], v[44:47], v[136:139]\n"
      "ds_read_b128 v[44:47], v234 offset:24832\n"
      "s_mul_i32 s79, s79, s100\n"
      "v_mfma_f32_16x16x32_bf16 v[136:139], v[184:187], v[60:63], v[136:139]\n"
      "ds_read_b128 v[60:63], v235 offset:24832\n"
      "v_cvt_pk_bf16_f32 v208, v132, v133\n"
      "v_add_u32 v247, s79, v247\n"
      "v_mfma_f32_16x16x32_bf16 v[136:139], v[188:191], v[64:67], v[136:139]\n"
      "ds_read_b128 v[64:67], v232 offset:33024\n"
      "v_cvt_pk_bf16_f32 v209, v134, v135\n"
      "v_mfma_f32_16x16x32_bf16 v[136:139], v[204:207], v[68:71], v[136:139]\n"
      "ds_read_b128 v[68:71], v233 offset:33024\n"
      "s_mul_i32 s79, s79, s101\n"
      "v_mfma_f32_16x16x32_bf16 v[136:139], a[24:27], v[84:87], v[136:139]\n"
      "ds_read_b128 v[84:87], v234 offset:33024\n"
      "v_add_u32 v253, s79, v253\n"
      "v_mfma_f32_16x16x32_bf16 v[136:139], a[28:31], v[88:91], v[136:139]\n"
      "ds_read_b128 v[88:91], v235 offset:33024\n"
      "s_add_u32 s54, s30, s40\n"
      "s_addc_u32 s55, s31, 0\n"
      "v_mfma_f32_16x16x32_bf16 v[136:139], a[32:35], v[92:95], v[136:139]\n"
      "ds_read_b128 v[92:95], v232 offset:41216\n"
      "v_mfma_f32_16x16x32_bf16 v[136:139], a[36:39], v[108:111], v[136:139]\n"
      "ds_read_b128 v[108:111], v233 offset:41216\n"
      "global_store_dwordx4 v247, v[72:75], s[54:55] offset:0 sc1\n"
      "v_mfma_f32_16x16x32_bf16 v[136:139], a[40:43], v[112:115], v[136:139]\n"
      "ds_read_b128 v[112:115], v234 offset:41216\n"
      "v_mfma_f32_16x16x32_bf16 v[136:139], a[44:47], v[116:119], v[136:139]\n"
      "ds_read_b128 v[116:119], v235 offset:41216\n"
      "s_waitcnt lgkmcnt(8)\n"
      "v_mfma_f32_16x16x32_bf16 v[132:135], a[240:243], v[36:39], v[0:3]\n"
      "ds_write_b64 v244, v[208:209] offset:24704\n"
      "v_mfma_f32_16x16x32_bf16 v[132:135], a[244:247], v[40:43], v[132:135]\n"
      "v_mfma_f32_16x16x32_bf16 v[132:135], a[248:251], v[44:47], v[132:135]\n"
      "v_mfma_f32_16x16x32_bf16 v[132:135], a[252:255], v[60:63], v[132:135]\n"
      "v_cvt_pk_bf16_f32 v210, v136, v137\n"
      "s_waitcnt lgkmcnt(5)\n"
      "v_mfma_f32_16x16x32_bf16 v[132:135], a[48:51], v[64:67], v[132:135]\n"
      "v_cvt_pk_bf16_f32 v211, v138, v139\n"
      "v_mfma_f32_16x16x32_bf16 v[132:135], a[52:55], v[68:71], v[132:135]\n"
      "v_mfma_f32_16x16x32_bf16 v[132:135], a[56:59], v[84:87], v[132:135]\n"
      "ds_read_b128 v[96:99], v246 offset:12288\n"
      "v_mfma_f32_16x16x32_bf16 v[132:135], a[60:63], v[88:91], v[132:135]\n"
      "ds_read_b128 v[100:103], v246 offset:13312\n"
      "s_waitcnt lgkmcnt(3)\n"
      "v_mfma_f32_16x16x32_bf16 v[132:135], a[64:67], v[92:95], v[132:135]\n"
      "ds_read_b128 v[104:107], v246 offset:14336\n"
      "v_mfma_f32_16x16x32_bf16 v[132:135], a[68:71], v[108:111], v[132:135]\n"
      "ds_write_b64 v244, v[210:211] offset:24712\n"
      "v_mfma_f32_16x16x32_bf16 v[132:135], a[72:75], v[112:115], v[132:135]\n"
      "s_cbranch_vccnz LT66\n"
      "LS66:\n"
      "v_mfma_f32_16x16x32_bf16 v[132:135], a[76:79], v[116:119], v[132:135]\n"
      "v_mfma_f32_16x16x32_bf16 v[136:139], a[80:83], v[36:39], v[4:7]\n"
      "v_mfma_f32_16x16x32_bf16 v[136:139], a[84:87], v[40:43], v[136:139]\n"
      "global_store_dwordx4 v252, v[76:79], s[54:55] offset:0 sc1\n"
      "v_mfma_f32_16x16x32_bf16 v[136:139], a[88:91], v[44:47], v[136:139]\n"
      "v_mfma_f32_16x16x32_bf16 v[136:139], a[92:95], v[60:63], v[136:139]\n"
      "v_cvt_pk_bf16_f32 v208, v132, v133\n"
      "v_mfma_f32_16x16x32_bf16 v[136:139], a[96:99], v[64:67], v[136:139]\n"
      "v_cvt_pk_bf16_f32 v209, v134, v135\n"
      "v_mfma_f32_16x16x32_bf16 v[136:139], a[100:103], v[68:71], v[136:139]\n"
      "v_mfma_f32_16x16x32_bf16 v[136:139], a[104:107], v[84:87], v[136:139]\n"
      "v_mfma_f32_16x16x32_bf16 v[136:139], a[108:111], v[88:91], v[136:139]\n"
      "v_mfma_f32_16x16x32_bf16 v[136:139], a[112:115], v[92:95], v[136:139]\n"
      "v_mfma_f32_16x16x32_bf16 v[136:139], a[116:119], v[108:111], v[136:139]\n"
      "ds_write_b64 v244, v[208:209] offset:0\n"
      "v_mfma_f32_16x16x32_bf16 v[136:139], a[120:123], v[112:115], v[136:139]\n"
      "v_mfma_f32_16x16x32_bf16 v[136:139], a[124:127], v[116:119], v[136:139]\n"
      "v_mfma_f32_16x16x32_bf16 v[132:135], a[128:131], v[36:39], v[8:11]\n"
      "global_store_dwordx4 v253, v[80:83], s[54:55] offset:0 sc1\n"
      "v_mfma_f32_16x16x32_bf16 v[132:135], a[132:135], v[40:43], v[132:135]\n"
      "v_mfma_f32_16x16x32_bf16 v[132:135], a[136:139], v[44:47], v[132:135]\n"
      "v_mfma_f32_16x16x32_bf16 v[132:135], a[140:143], v[60:63], v[132:135]\n"
      "v_cvt_pk_bf16_f32 v210, v136, v137\n"
      "v_mfma_f32_16x16x32_bf16 v[132:135], a[144:147], v[64:67], v[132:135]\n"
      "v_cvt_pk_bf16_f32 v211, v138, v139\n"
      "v_mfma_f32_16x16x32_bf16 v[132:135], a[148:151], v[68:71], v[132:135]\n"
      "v_mfma_f32_16x16x32_bf16 v[132:135], a[152:155], v[84:87], v[132:135]\n"
      "v_mfma_f32_16x16x32_bf16 v[132:135], a[156:159], v[88:91], v[132:135]\n"
      "v_mfma_f32_16x16x32_bf16 v[132:135], a[160:163], v[92:95], v[132:135]\n"
      "v_mfma_f32_16x16x32_bf16 v[132:135], a[164:167], v[108:111], v[132:135]\n"
      "ds_write_b64 v244, v[210:211] offset:8\n"
      "v_mfma_f32_16x16x32_bf16 v[132:135], a[168:171], v[112:115], v[132:135]\n"
      "s_waitcnt lgkmcnt(5)\n"
      "v_mfma_f32_16x16x32_bf16 v[132:135], a[172:175], v[116:119], v[132:135]\n"
      "global_store_dwordx4 v247, v[96:99], s[54:55] offset:768 sc1\n"
      "v_mfma_f32_16x16x32_bf16 v[136:139], a[176:179], v[36:39], v[12:15]\n"
      "v_mfma_f32_16x16x32_bf16 v[136:139], a[180:183], v[40:43], v[136:139]\n"
      "v_mfma_f32_16x16x32_bf16 v[136:139], a[184:187], v[44:47], v[136:139]\n"
      "v_mfma_f32_16x16x32_bf16 v[136:139], a[188:191], v[60:63], v[136:139]\n"
      "v_cvt_pk_bf16_f32 v212, v132, v133\n"
      "v_mfma_f32_16x16x32_bf16 v[136:139], a[192:195], v[64:67], v[136:139]\n"
      "v_cvt_pk_bf16_f32 v213, v134, v135\n"
      "v_mfma_f32_16x16x32_bf16 v[136:139], a[196:199], v[68:71], v[136:139]\n"
      "v_mfma_f32_16x16x32_bf16 v[136:139], a[200:203], v[84:87], v[136:139]\n"
      "v_mfma_f32_16x16x32_bf16 v[136:139], a[204:207], v[88:91], v[136:139]\n"
      "v_mfma_f32_16x16x32_bf16 v[136:139], a[208:211], v[92:95], v[136:139]\n"
      "s_waitcnt lgkmcnt(4)\n"
      "s_cbranch_vccnz LT67\n"
      "LS67:\n"
      "v_mfma_f32_16x16x32_bf16 v[136:139], a[212:215], v[108:111], v[136:139]\n"
      "ds_write_b64 v245, v[212:213] offset:0\n"
      "v_mfma_f32_16x16x32_bf16 v[136:139], a[216:219], v[112:115], v[136:139]\n"
      "global_store_dwordx4 v252, v[100:103], s[54:55] offset:768 sc1\n"
      "v_mfma_f32_16x16x32_bf16 v[136:139], a[220:223], v[116:119], v[136:139]\n"
      "v_mfma_f32_16x16x32_bf16 v[132:135], a[224:227], v[36:39], v[16:19]\n"
      "v_mfma_f32_16x16x32_bf16 v[132:135], a[228:231], v[40:43], v[132:135]\n"
      "v_mfma_f32_16x16x32_bf16 v[132:135], a[232:235], v[44:47], v[132:135]\n"
      "v_mfma_f32_16x16x32_bf16 v[132:135], a[236:239], v[60:63], v[132:135]\n"
      "v_cvt_pk_bf16_f32 v214, v136, v137\n"
      "v_mfma_f32_16x16x32_bf16 v[132:135], a[0:3], v[64:67], v[132:135]\n"
      "v_cvt_pk_bf16_f32 v215, v138, v139\n"
      "v_mfma_f32_16x16x32_bf16 v[132:135], a[4:7], v[68:71], v[132:135]\n"
      "v_mfma_f32_16x16x32_bf16 v[132:135], a[8:11], v[84:87], v[132:135]\n"
      "v_mfma_f32_16x16x32_bf16 v[132:135], a[12:15], v[88:91], v[132:135]\n"
      "s_waitcnt lgkmcnt(4)\n"
      "v_mfma_f32_16x16x32_bf16 v[132:135], a[16:19], v[92:95], v[132:135]\n"
      "global_store_dwordx4 v253, v[104:107], s[54:55] offset:768 sc1\n"
      "v_mfma_f32_16x16x32_bf16 v[132:135], a[20:23], v[108:111], v[132:135]\n"
      "ds_write_b64 v245, v[214:215] offset:8\n"
      "v_mfma_f32_16x16x32_bf16 v[132:135], v[140:143], v[112:115], v[132:135]\n"
      "v_mfma_f32_16x16x32_bf16 v[132:135], v[156:159], v[116:119], v[132:135]\n"
      "s_waitcnt lgkmcnt(4)\n"
      "s_barrier\n"
      "v_mfma_f32_16x16x32_bf16 v[136:139], v[160:163], v[36:39], v[20:23]\n"
      "v_mfma_f32_16x16x32_bf16 v[136:139], v[164:167], v[40:43], v[136:139]\n"
      "v_mfma_f32_16x16x32_bf16 v[136:139], v[180:183], v[44:47], v[136:139]\n"
      "v_mfma_f32_16x16x32_bf16 v[136:139], v[184:187], v[60:63], v[136:139]\n"
      "v_cvt_pk_bf16_f32 v208, v132, v133\n"
      "v_mfma_f32_16x16x32_bf16 v[136:139], v[188:191], v[64:67], v[136:139]\n"
      "v_cvt_pk_bf16_f32 v209, v134, v135\n"
      "v_mfma_f32_16x16x32_bf16 v[136:139], v[204:207], v[68:71], v[136:139]\n"
      "v_mfma_f32_16x16x32_bf16 v[136:139], a[24:27], v[84:87], v[136:139]\n"
      "v_mfma_f32_16x16x32_bf16 v[136:139], a[28:31], v[88:91], v[136:139]\n"
      "v_mfma_f32_16x16x32_bf16 v[136:139], a[32:35], v[92:95], v[136:139]\n"
      "v_mfma_f32_16x16x32_bf16 v[136:139], a[36:39], v[108:111], v[136:139]\n"
      "ds_write_b64 v244, v[208:209] offset:128\n"
      "v_mfma_f32_16x16x32_bf16 v[136:139], a[40:43], v[112:115], v[136:139]\n"
      "v_mfma_f32_16x16x32_bf16 v[136:139], a[44:47], v[116:119], v[136:139]\n"
      "ds_read_b128 v[120:123], v246 offset:24576\n"
      "ds_read_b128 v[124:127], v246 offset:25600\n"
      "ds_read_b128 v[128:131], v246 offset:26624\n"
      "s_add_u32 s54, s44, s40\n"
      "s_addc_u32 s55, s45, 0\n"
      "s_nop 3\n"
      "v_cvt_pk_bf16_f32 v210, v136, v137\n"
      "v_cvt_pk_bf16_f32 v211, v138, v139\n"
      "ds_write_b128 v244, v[208:211] offset:128\n"
      "s_waitcnt lgkmcnt(3)\n"
      "global_store_dwordx4 v247, v[120:123], s[54:55] offset:0 sc1\n"
      "s_waitcnt lgkmcnt(2)\n"
      "global_store_dwordx4 v252, v[124:127], s[54:55] offset:0 sc1\n"
      "s_waitcnt lgkmcnt(1)\n"
      "global_store_dwordx4 v253, v[128:131], s[54:55] offset:0 sc1\n"
      "s_waitcnt lgkmcnt(0)\n"
      "s_barrier\n"
      "ds_read_b128 v[144:147], v246 offset:0\n"
      "ds_read_b128 v[148:151], v246 offset:1024\n"
      "ds_read_b128 v[152:155], v246 offset:2048\n"
      "s_waitcnt lgkmcnt(2)\n"
      "global_store_dwordx4 v247, v[144:147], s[54:55] offset:768 sc1\n"
      "s_waitcnt lgkmcnt(1)\n"
      "global_store_dwordx4 v252, v[148:151], s[54:55] offset:768 sc1\n"
      "s_waitcnt lgkmcnt(0)\n"
      "global_store_dwordx4 v253, v[152:155], s[54:55] offset:768 sc1\n"
      "s_cmp_lg_u32 s87, 0\n"
      "s_cbranch_scc1 LE1\n"
      "v_min_u32 v0, 0x370, v239\n"
      "v_add_u32 v0, 0x2480, v0\n"
      "s_mov_b32 s82, 0x40000\n"
      "LE0:\n"
      "global_load_dwordx4 v[4:7], v0, s[28:29] sc1\n"
      "s_sub_u32 s82, s82, 1\n"
      "s_cbranch_scc1 LE1\n"
      "s_waitcnt vmcnt(0)\n"
      "v_cmp_ne_u64 vcc, s[80:81], v[4:5]\n"
      "v_cmp_ne_u64 s[56:57], s[80:81], v[6:7]\n"
      "s_nop 3\n"
      "s_or_b64 vcc, vcc, s[56:57]\n"
      "s_cbranch_scc1 LE0\n"
      "v_mov_b32 v0, 0\n"
      "v_mov_b32 v2, s80\n"
      "v_mov_b32 v3, s81\n"
      "s_mov_b64 exec, 1\n"
      "global_store_dwordx2 v0, v[2:3], s[28:29] sc1\n"
      "s_mov_b64 exec, -1\n"
      "LE1:\n"
      "s_waitcnt vmcnt(0)\n"
      "s_branch LCEND\n"
      "LPS:\n"
      "s_mov_b64 exec, 1\n"
      "global_store_dword v229, v230, s[28:29] sc1\n"
      "s_mov_b64 exec, -1\n"
      "s_branch LPR\n"
      "LT0:\n"
      "s_nop 15\n"
      "s_nop 3\n"
      "s_branch LS0\n"
      "LT1:\n"
      "s_nop 15\n"
      "s_nop 3\n"
      "s_branch LS1\n"
      "LT2:\n"
      "s_nop 15\n"
      "s_nop 3\n"
      "s_branch LS2\n"
      "LT3:\n"
      "s_nop 15\n"
      "s_nop 3\n"
      "s_branch LS3\n"
      "LT4:\n"
      "s_nop 15\n"
      "s_nop 3\n"
      "s_branch LS4\n"
      "LT5:\n"
      "s_nop 15\n"
      "s_nop 3\n"
      "s_branch LS5\n"
      "LT6:\n"
      "s_nop 15\n"
      "s_nop 3\n"
      "s_branch LS6\n"
      "LT7:\n"
      "s_nop 15\n"
      "s_nop 3\n"
      "s_branch LS7\n"
      "LT8:\n"
      "s_nop 15\n"
      "s_nop 3\n"
      "s_branch LS8\n"
      "LT9:\n"
      "s_nop 15\n"
      "s_nop 3\n"
      "s_branch LS9\n"
      "LT10:\n"
      "s_nop 15\n"
      "s_nop 3\n"
      "s_branch LS10\n"
      "LT11:\n"
      "s_nop 15\n"
      "s_nop 3\n"
      "s_branch LS11\n"
      "LT12:\n"
      "s_nop 15\n"
      "s_nop 3\n"
      "s_branch LS12\n"
      "LT13:\n"
      "s_nop 15\n"
      "s_nop 3\n"
      "s_branch LS13\n"
      "LT14:\n"
      "s_nop 15\n"
      "s_nop 3\n"
      "s_branch LS14\n"
      "LT15:\n"
      "s_nop 15\n"
      "s_nop 3\n"
      "s_branch LS15\n"
      "LT16:\n"
      "s_nop 15\n"
      "s_nop 3\n"
      "s_branch LS16\n"
      "LT17:\n"
      "s_nop 15\n"
      "s_nop 3\n"
      "s_branch LS17\n"
      "LT18:\n"
      "s_nop 15\n"
      "s_nop 3\n"
      "s_branch LS18\n"
      "LT19:\n"
      "s_nop 15\n"
      "s_nop 3\n"
      "s_branch LS19\n"
      "LT20:\n"
      "s_nop 15\n"
      "s_nop 3\n"
      "s_branch LS20\n"
      "LT21:\n"
      "s_nop 15\n"
      "s_nop 3\n"
      "s_branch LS21\n"
      "LT22:\n"
      "s_nop 15\n"
      "s_nop 3\n"
      "s_branch LS22\n"
      "LT23:\n"
      "s_nop 15\n"
      "s_nop 3\n"
      "s_branch LS23\n"
      "LT24:\n"
      "s_nop 15\n"
      "s_nop 3\n"
      "s_branch LS24\n"
      "LT25:\n"
      "s_nop 15\n"
      "s_nop 3\n"
      "s_branch LS25\n"
      "LT26:\n"
      "s_nop 15\n"
      "s_nop 3\n"
      "s_branch LS26\n"
      "LT27:\n"
      "s_nop 15\n"
      "s_nop 3\n"
      "s_branch LS27\n"
      "LT28:\n"
      "s_nop 15\n"
      "s_nop 3\n"
      "s_branch LS28\n"
      "LT29:\n"
      "s_nop 15\n"
      "s_nop 3\n"
      "s_branch LS29\n"
      "LT30:\n"
      "s_nop 15\n"
      "s_nop 3\n"
      "s_branch LS30\n"
      "LT31:\n"
      "s_nop 15\n"
      "s_nop 3\n"
      "s_branch LS31\n"
      "LT32:\n"
      "s_nop 15\n"
      "s_nop 3\n"
      "s_branch LS32\n"
      "LT33:\n"
      "s_nop 15\n"
      "s_nop 3\n"
      "s_branch LS33\n"
      "LT34:\n"
      "s_nop 15\n"
      "s_nop 3\n"
      "s_branch LS34\n"
      "LT35:\n"
      "s_nop 15\n"
      "s_nop 3\n"
      "s_branch LS35\n"
      "LT36:\n"
      "s_nop 15\n"
      "s_nop 3\n"
      "s_branch LS36\n"
      "LT37:\n"
      "s_nop 15\n"
      "s_nop 3\n"
      "s_branch LS37\n"
      "LT38:\n"
      "s_nop 15\n"
      "s_nop 3\n"
      "s_branch LS38\n"
      "LT39:\n"
      "s_nop 15\n"
      "s_nop 3\n"
      "s_branch LS39\n"
      "LT40:\n"
      "s_nop 15\n"
      "s_nop 3\n"
      "s_branch LS40\n"
      "LT41:\n"
      "s_nop 15\n"
      "s_nop 3\n"
      "s_branch LS41\n"
      "LT42:\n"
      "s_nop 15\n"
      "s_nop 3\n"
      "s_branch LS42\n"
      "LT43:\n"
      "s_nop 15\n"
      "s_nop 3\n"
      "s_branch LS43\n"
      "LT44:\n"
      "s_nop 15\n"
      "s_nop 3\n"
      "s_branch LS44\n"
      "LT45:\n"
      "s_nop 15\n"
      "s_nop 3\n"
      "s_branch LS45\n"
      "LT46:\n"
      "s_nop 15\n"
      "s_nop 3\n"
      "s_branch LS46\n"
      "LT47:\n"
      "s_nop 15\n"
      "s_nop 3\n"
      "s_branch LS47\n"
      "LT48:\n"
      "s_nop 15\n"
      "s_nop 3\n"
      "s_branch LS48\n"
      "LT49:\n"
      "s_nop 15\n"
      "s_nop 3\n"
      "s_branch LS49\n"
      "LT50:\n"
      "s_nop 15\n"
      "s_nop 3\n"
      "s_branch LS50\n"
      "LT51:\n"
      "s_nop 15\n"
      "s_nop 3\n"
      "s_branch LS51\n"
      "LT52:\n"
      "s_nop 15\n"
      "s_nop 3\n"
      "s_branch LS52\n"
      "LT53:\n"
      "s_nop 15\n"
      "s_nop 3\n"
      "s_branch LS53\n"
      "LT54:\n"
      "s_nop 15\n"
      "s_nop 3\n"
      "s_branch LS54\n"
      "LT55:\n"
      "s_nop 15\n"
      "s_nop 3\n"
      "s_branch LS55\n"
      "LT56:\n"
      "s_nop 15\n"
      "s_nop 3\n"
      "s_branch LS56\n"
      "LT57:\n"
      "s_nop 15\n"
      "s_nop 3\n"
      "s_branch LS57\n"
      "LT58:\n"
      "s_nop 15\n"
      "s_nop 3\n"
      "s_branch LS58\n"
      "LT59:\n"
      "s_nop 15\n"
      "s_nop 3\n"
      "s_branch LS59\n"
      "LT60:\n"
      "s_nop 15\n"
      "s_nop 3\n"
      "s_branch LS60\n"
      "LT61:\n"
      "s_nop 15\n"
      "s_nop 3\n"
      "s_branch LS61\n"
      "LT62:\n"
      "s_nop 15\n"
      "s_nop 3\n"
      "s_branch LS62\n"
      "LT63:\n"
      "s_nop 15\n"
      "s_nop 3\n"
      "s_branch LS63\n"
      "LT64:\n"
      "s_nop 15\n"
      "s_nop 3\n"
      "s_branch LS64\n"
      "LT65:\n"
      "s_nop 15\n"
      "s_nop 3\n"
      "s_branch LS65\n"
      "LT66:\n"
      "s_nop 15\n"
      "s_nop 3\n"
      "s_branch LS66\n"
      "LT67:\n"
      "s_nop 15\n"
      "s_nop 3\n"
      "s_branch LS67\n"
      "LCEND:\n"

      :
      : "{v232}"(cB0), "{v233}"(cB0 ^ 64), "{v234}"(cB0 ^ 128), "{v235}"(cB0 ^ 192), "{v238}"(pvo),
        "{v239}"(lo16), "{v240}"(wro[0]), "{v241}"(wro[1]), "{v242}"(wro[2]), "{v243}"(tra), "{v248}"(fla), "{v249}"(c256),
        "{v250}"(pk), "{v105}"(two), "{v106}"(tcode), "{v[252:253]}"(ep),
        "{s[20:21]}"(cf), "{s[22:23]}"(pc), "{s[24:25]}"(wb), "{s[30:31]}"(out), "{s[28:29]}"(ws), "{s18}"(mP), "{s17}"(mX),
        "{s32}"(xo[0]), "{s33}"(xo[1]), "{s34}"(xo[2]), "{s35}"(xo[3]), "{s36}"(xo[4]), "{s37}"(xo[5]), "{s38}"(xo[6]),
        "{s39}"(xo[7]), "{s40}"(xo[8]), "{s[42:43]}"(cf + 294912), "{s[44:45]}"(out + 294912), "{s[46:47]}"(tab), "{s50}"(var), "{s51}"(b),
        "{s[64:65]}"(w2w), "{s74}"(mW), "{s76}"(wid), "{s77}"(jfl), "{v251}"(sgn),
        "{s89}"(dj[0]), "{s90}"(dj[1]), "{s91}"(dj[2]), "{s92}"(dj[3]), "{s93}"(dj[4]), "{s94}"(dj[5]), "{s95}"(dj[6]),
        "{s96}"(dj[7]), "{s97}"(dj[8]), "{s98}"(d0), "{s99}"(wid ? -1 : 1), "{s100}"(wid < 3 ? 1 : -1), "{s101}"(wid == 2 ? -1 : 1),
        "{v102}"(jt), "{v103}"(jrw), "{v104}"(jrr), "{v[110:111]}"(cd), "{v[112:113]}"(cr), "{v107}"(sg1), "{v108}"(scr), "{s[72:73]}"(jo),
        "{v[4:7]}"(ja0), "{v[8:11]}"(ja1), "{v[12:15]}"(ja2), "{v[16:19]}"(ja3), "{v[20:23]}"(ja4), "{v[24:27]}"(ja5),
        "{s83}"(t_start + (0) + var * ((0) - (0))), "{s86}"(158 + var * ((158) - (158)))
      : "memory", "scc", "vcc", "v0", "v1", "v2", "v3", "v28", "v29", "v30", "v31", "v32", "v33", "v34", "v35", "v36", "v37", "v38", "v39", "v40", "v41", "v42", "v43", "v44", "v45", "v46", "v47", "v48", "v49", "v50", "v51", "v52", "v53", "v54", "v55", "v56", "v57", "v58", "v59", "v60", "v61", "v62", "v63", "v64", "v65", "v66", "v67", "v68", "v69", "v70", "v71", "v72", "v73", "v74", "v75", "v76", "v77", "v78", "v79", "v80", "v81", "v82", "v83", "v84", "v85", "v86", "v87", "v88", "v89", "v90", "v91", "v92", "v93", "v94", "v95", "v96", "v97", "v98", "v99", "v100", "v101", "v109", "v114", "v115", "v116", "v117", "v118", "v119", "v120", "v121", "v122", "v123", "v124", "v125", "v126", "v127", "v128", "v129", "v130", "v131", "v132", "v133", "v134", "v135", "v136", "v137", "v138", "v139", "v140", "v141", "v142", "v143", "v144", "v145", "v146", "v147", "v148", "v149", "v150", "v151", "v152", "v153", "v154", "v155", "v156", "v157", "v158", "v159", "v160", "v161", "v162", "v163", "v164", "v165", "v166", "v167", "v168", "v169", "v170", "v171", "v172", "v173", "v174", "v175", "v176", "v177", "v178", "v179", "v180", "v181", "v182", "v183", "v184", "v185", "v186", "v187", "v188", "v189", "v190", "v191", "v192", "v193", "v194", "v195", "v196", "v197", "v198", "v199", "v200", "v201", "v202", "v203", "v204", "v205", "v206", "v207", "v208", "v209", "v210", "v211", "v212", "v213", "v214", "v215", "v216", "v217", "v218", "v219", "v220", "v221", "v222", "v223", "v224", "v225", "v226", "v227", "v228", "v229", "v230", "v231", "v236", "v237", "v244", "v245", "v246", "v247", "v254", "v255", "a0", "a1", "a2", "a3", "a4", "a5", "a6", "a7", "a8", "a9", "a10", "a11", "a12", "a13", "a14", "a15", "a16", "a17", "a18", "a19", "a20", "a21", "a22", "a23", "a24", "a25", "a26", "a27", "a28", "a29", "a30", "a31", "a32", "a33", "a34", "a35", "a36", "a37", "a38", "a39", "a40", "a41", "a42", "a43", "a44", "a45", "a46", "a47", "a48", "a49", "a50", "a51", "a52", "a53", "a54", "a55", "a56", "a57", "a58", "a59", "a60", "a61", "a62", "a63", "a64", "a65", "a66", "a67", "a68", "a69", "a70", "a71", "a72", "a73", "a74", "a75", "a76", "a77", "a78", "a79", "a80", "a81", "a82", "a83", "a84", "a85", "a86", "a87", "a88", "a89", "a90", "a91", "a92", "a93", "a94", "a95", "a96", "a97", "a98", "a99", "a100", "a101", "a102", "a103", "a104", "a105", "a106", "a107", "a108", "a109", "a110", "a111", "a112", "a113", "a114", "a115", "a116", "a117", "a118", "a119", "a120", "a121", "a122", "a123", "a124", "a125", "a126", "a127", "a128", "a129", "a130", "a131", "a132", "a133", "a134", "a135", "a136", "a137", "a138", "a139", "a140", "a141", "a142", "a143", "a144", "a145", "a146", "a147", "a148", "a149", "a150", "a151", "a152", "a153", "a154", "a155", "a156", "a157", "a158", "a159", "a160", "a161", "a162", "a163", "a164", "a165", "a166", "a167", "a168", "a169", "a170", "a171", "a172", "a173", "a174", "a175", "a176", "a177", "a178", "a179", "a180", "a181", "a182", "a183", "a184", "a185", "a186", "a187", "a188", "a189", "a190", "a191", "a192", "a193", "a194", "a195", "a196", "a197", "a198", "a199", "a200", "a201", "a202", "a203", "a204", "a205", "a206", "a207", "a208", "a209", "a210", "a211", "a212", "a213", "a214", "a215", "a216", "a217", "a218", "a219", "a220", "a221", "a222", "a223", "a224", "a225", "a226", "a227", "a228", "a229", "a230", "a231", "a232", "a233", "a234", "a235", "a236", "a237", "a238", "a239", "a240", "a241", "a242", "a243", "a244", "a245", "a246", "a247", "a248", "a249", "a250", "a251", "a252", "a253", "a254", "a255", "s48", "s49", "s66", "s67", "s68", "s69", "s70", "s71", "s52", "s53", "s54", "s55", "s56", "s57", "s58", "s59", "s60", "s61", "s62", "s63", "s78", "s79", "s80", "s81", "s82", "s84", "s85", "s87", "s19");
}

extern "C" void kernel_launch(void* const* d_in, const int* in_sizes, int n_in,
                              void* d_out, int out_size, void* d_ws, size_t ws_size,
                              hipStream_t stream) {
  (void)hipFuncSetAttribute((const void*)fpn_kernel, hipFuncAttributeMaxDynamicSharedMemorySize, 159808);
  fpn_kernel<<<256, 256, 159808, stream>>>((const char*)d_in[0], (const char*)d_in[1], (const char*)d_in[2],
                                                (const char*)d_in[3], (char*)d_ws, (char*)d_out);
}
```

```cpp
#include <hip/hip_runtime.h>
#include <hip/hip_bf16.h>

typedef __attribute__((ext_vector_type(4))) unsigned u32x4;
typedef __attribute__((ext_vector_type(2))) unsigned u32x2;
#define NSTAMPS 0

__device__ __forceinline__ unsigned col0(unsigned i) { return i + (i >= 5 ? 6 : 0) + (i >= 10 ? 5 : 0); }

static __device__ const unsigned __attribute__((aligned(128))) lane_tab[2048] = {
  98304, 98368, 98304, 0, 99088, 99152, 98320, 16, 99872, 99936, 98336, 32, 100656, 100720, 98352, 48,
  101440, 101376, 98368, 64, 102224, 102160, 98384, 80, 103008, 102944, 98400, 96, 103792, 103728, 98416, 112,
  104448, 104512, 98432, 128, 105232, 105296, 98448, 144, 106016, 106080, 98464, 160, 106800, 106864, 98480, 176,
  107584, 107520, 98496, 192, 108368, 108304, 98512, 208, 109152, 109088, 98528, 224, 109936, 109872, 98544, 240,
  98320, 98384, 98560, 256, 99072, 99136, 98576, 272, 99888, 99952, 98592, 288, 100640, 100704, 98608, 304,
  101456, 101392, 98624, 320, 102208, 102144, 98640, 336, 103024, 102960, 98656, 352, 103776, 103712, 98672, 368,
  104464, 104528, 98688, 384, 105216, 105280, 98704, 400, 106032, 106096, 98720, 416, 106784, 106848, 98736, 432,
  107600, 107536, 98752, 448, 108352, 108288, 98768, 464, 109168, 109104, 98784, 480, 109920, 109856, 98800, 496,
  98336, 98400, 98816, 512, 99120, 99184, 98832, 528, 99840, 99904, 98848, 544, 100624, 100688, 98864, 560,
  101472, 101408, 98880, 576, 102256, 102192, 98896, 592, 102976, 102912, 98912, 608, 103760, 103696, 98928, 624,
  104480, 104544, 98944, 640, 105264, 105328, 98960, 656, 105984, 106048, 98976, 672, 106768, 106832, 98992, 688,
  107616, 107552, 99008, 704, 108400, 108336, 99024, 720, 109120, 109056, 99040, 736, 109904, 109840, 99056, 752,
  98352, 98416, 99072, 1552, 99104, 99168, 99088, 1536, 99856, 99920, 99104, 1584, 100608, 100672, 99120, 1568,
  101488, 101424, 99136, 1616, 102240, 102176, 99152, 1600, 102992, 102928, 99168, 1648, 103744, 103680, 99184, 1632,
  104496, 104560, 99200, 1680, 105248, 105312, 99216, 1664, 106000, 106064, 99232, 1712, 106752, 106816, 99248, 1696,
  107632, 107568, 99264, 1744, 108384, 108320, 99280, 1728, 109136, 109072, 99296, 1776, 109888, 109824, 99312, 1760,
  98496, 98560, 101376, 6208, 99280, 99344, 101392, 6224, 100064, 100128, 101408, 6240, 100848, 100912, 101424, 6256,
  101504, 101696, 101440, 6144, 102288, 102480, 101456, 6160, 103072, 103264, 101472, 6176, 103856, 104048, 101488, 6192,
  104640, 104704, 101504, 6336, 105424, 105488, 101520, 6352, 106208, 106272, 101536, 6368, 106992, 107056, 101552, 6384,
  107648, 107840, 101568, 6272, 108432, 108624, 101584, 6288, 109216, 109408, 101600, 6304, 110000, 110192, 101616, 6320,
  98512, 98576, 101632, 6464, 99264, 99328, 101648, 6480, 100080, 100144, 101664, 6496, 100832, 100896, 101680, 6512,
  101520, 101712, 101696, 6400, 102272, 102464, 101712, 6416, 103088, 103280, 101728, 6432, 103840, 104032, 101744, 6448,
  104656, 104720, 101760, 6592, 105408, 105472, 101776, 6608, 106224, 106288, 101792, 6624, 106976, 107040, 101808, 6640,
  107664, 107856, 101824, 6528, 108416, 108608, 101840, 6544, 109232, 109424, 101856, 6560, 109984, 110176, 101872, 6576,
  98528, 98592, 101888, 6720, 99312, 99376, 101904, 6736, 100032, 100096, 101920, 6752, 100816, 100880, 101936, 6768,
  101536, 101728, 101952, 6656, 102320, 102512, 101968, 6672, 103040, 103232, 101984, 6688, 103824, 104016, 102000, 6704,
  104672, 104736, 102016, 6848, 105456, 105520, 102032, 6864, 106176, 106240, 102048, 6880, 106960, 107024, 102064, 6896,
  107680, 107872, 102080, 6784, 108464, 108656, 102096, 6800, 109184, 109376, 102112, 6816, 109968, 110160, 102128, 6832,
  98544, 98608, 102144, 16976, 99296, 99360, 102160, 16960, 100048, 100112, 102176, 17008, 100800, 100864, 102192, 16992,
  101552, 101744, 102208, 16912, 102304, 102496, 102224, 16896, 103056, 103248, 102240, 16944, 103808, 104000, 102256, 16928,
  104688, 104752, 102272, 17104, 105440, 105504, 102288, 17088, 106192, 106256, 102304, 17136, 106944, 107008, 102320, 17120,
  107696, 107888, 102336, 17040, 108448, 108640, 102352, 17024, 109200, 109392, 102368, 17072, 109952, 110144, 102384, 17056,
  1808, 3616, 0, 21632, 1792, 3632, 16, 21648, 1840, 3584, 32, 21664, 1824, 3600, 48, 21680,
  1872, 3680, 64, 21696, 1856, 3696, 80, 21712, 1904, 3648, 96, 21728, 1888, 3664, 112, 21744,
  1936, 3744, 128, 21504, 1920, 3760, 144, 21520, 1968, 3712, 160, 21536, 1952, 3728, 176, 21552,
  2000, 3808, 192, 21568, 1984, 3824, 208, 21584, 2032, 3776, 224, 21600, 2016, 3792, 240, 21616,
  2064, 4656, 768, 22400, 2048, 4640, 784, 22416, 2096, 4624, 800, 22432, 2080, 4608, 816, 22448,
  2128, 4720, 832, 22464, 2112, 4704, 848, 22480, 2160, 4688, 864, 22496, 2144, 4672, 880, 22512,
  2192, 4784, 896, 22272, 2176, 4768, 912, 22288, 2224, 4752, 928, 22304, 2208, 4736, 944, 22320,
  2256, 4848, 960, 22336, 2240, 4832, 976, 22352, 2288, 4816, 992, 22368, 2272, 4800, 1008, 22384,
  3104, 4912, 1552, 23184, 3120, 4896, 1536, 23168, 3072, 4880, 1584, 23216, 3088, 4864, 1568, 23200,
  3168, 4976, 1616, 23248, 3184, 4960, 1600, 23232, 3136, 4944, 1648, 23280, 3152, 4928, 1632, 23264,
  3232, 5040, 1680, 23056, 3248, 5024, 1664, 23040, 3200, 5008, 1712, 23088, 3216, 4992, 1696, 23072,
  3296, 5104, 1744, 23120, 3312, 5088, 1728, 23104, 3264, 5072, 1776, 23152, 3280, 5056, 1760, 23136,
  3360, 5168, 2320, 23952, 3376, 5152, 2304, 23936, 3328, 5136, 2352, 23984, 3344, 5120, 2336, 23968,
  3424, 5232, 2384, 24016, 3440, 5216, 2368, 24000, 3392, 5200, 2416, 24048, 3408, 5184, 2400, 24032,
  3488, 5296, 2448, 23824, 3504, 5280, 2432, 23808, 3456, 5264, 2480, 23856, 3472, 5248, 2464, 23840,
  3552, 5360, 2512, 23888, 3568, 5344, 2496, 23872, 3520, 5328, 2544, 23920, 3536, 5312, 2528, 23904,
  17232, 19040, 3104, 32416, 17216, 19056, 3120, 32432, 17264, 19008, 3072, 32384, 17248, 19024, 3088, 32400,
  17168, 18976, 3168, 32480, 17152, 18992, 3184, 32496, 17200, 18944, 3136, 32448, 17184, 18960, 3152, 32464,
  17360, 19168, 3232, 32288, 17344, 19184, 3248, 32304, 17392, 19136, 3200, 32256, 17376, 19152, 3216, 32272,
  17296, 19104, 3296, 32352, 17280, 19120, 3312, 32368, 17328, 19072, 3264, 32320, 17312, 19088, 3280, 32336,
  17488, 20080, 3872, 33184, 17472, 20064, 3888, 33200, 17520, 20048, 3840, 33152, 17504, 20032, 3856, 33168,
  17424, 20016, 3936, 33248, 17408, 20000, 3952, 33264, 17456, 19984, 3904, 33216, 17440, 19968, 3920, 33232,
  17616, 20208, 4000, 33056, 17600, 20192, 4016, 33072, 17648, 20176, 3968, 33024, 17632, 20160, 3984, 33040,
  17552, 20144, 4064, 33120, 17536, 20128, 4080, 33136, 17584, 20112, 4032, 33088, 17568, 20096, 4048, 33104,
  18528, 20336, 4656, 33968, 18544, 20320, 4640, 33952, 18496, 20304, 4624, 33936, 18512, 20288, 4608, 33920,
  18464, 20272, 4720, 34032, 18480, 20256, 4704, 34016, 18432, 20240, 4688, 34000, 18448, 20224, 4672, 33984,
  18656, 20464, 4784, 33840, 18672, 20448, 4768, 33824, 18624, 20432, 4752, 33808, 18640, 20416, 4736, 33792,
  18592, 20400, 4848, 33904, 18608, 20384, 4832, 33888, 18560, 20368, 4816, 33872, 18576, 20352, 4800, 33856,
  18784, 20592, 5424, 34736, 18800, 20576, 5408, 34720, 18752, 20560, 5392, 34704, 18768, 20544, 5376, 34688,
  18720, 20528, 5488, 34800, 18736, 20512, 5472, 34784, 18688, 20496, 5456, 34768, 18704, 20480, 5440, 34752,
  18912, 20720, 5552, 34608, 18928, 20704, 5536, 34592, 18880, 20688, 5520, 34576, 18896, 20672, 5504, 34560,
  18848, 20656, 5616, 34672, 18864, 20640, 5600, 34656, 18816, 20624, 5584, 34640, 18832, 20608, 5568, 34624,
  98688, 98752, 104448, 21504, 99472, 99536, 104464, 21520, 100256, 100320, 104480, 21536, 101040, 101104, 104496, 21552,
  101824, 101760, 104512, 21568, 102608, 102544, 104528, 21584, 103392, 103328, 104544, 21600, 104176, 104112, 104560, 21616,
  104832, 104896, 104576, 21632, 105616, 105680, 104592, 21648, 106400, 106464, 104608, 21664, 107184, 107248, 104624, 21680,
  107968, 107904, 104640, 21696, 108752, 108688, 104656, 21712, 109536, 109472, 104672, 21728, 110320, 110256, 104688, 21744,
  98704, 98768, 104704, 21760, 99456, 99520, 104720, 21776, 100272, 100336, 104736, 21792, 101024, 101088, 104752, 21808,
  101840, 101776, 104768, 21824, 102592, 102528, 104784, 21840, 103408, 103344, 104800, 21856, 104160, 104096, 104816, 21872,
  104848, 104912, 104832, 21888, 105600, 105664, 104848, 21904, 106416, 106480, 104864, 21920, 107168, 107232, 104880, 21936,
  107984, 107920, 104896, 21952, 108736, 108672, 104912, 21968, 109552, 109488, 104928, 21984, 110304, 110240, 104944, 22000,
  98720, 98784, 104960, 22016, 99504, 99568, 104976, 22032, 100224, 100288, 104992, 22048, 101008, 101072, 105008, 22064,
  101856, 101792, 105024, 22080, 102640, 102576, 105040, 22096, 103360, 103296, 105056, 22112, 104144, 104080, 105072, 22128,
  104864, 104928, 105088, 22144, 105648, 105712, 105104, 22160, 106368, 106432, 105120, 22176, 107152, 107216, 105136, 22192,
  108000, 107936, 105152, 22208, 108784, 108720, 105168, 22224, 109504, 109440, 105184, 22240, 110288, 110224, 105200, 22256,
  98736, 98800, 105216, 23056, 99488, 99552, 105232, 23040, 100240, 100304, 105248, 23088, 100992, 101056, 105264, 23072,
  101872, 101808, 105280, 23120, 102624, 102560, 105296, 23104, 103376, 103312, 105312, 23152, 104128, 104064, 105328, 23136,
  104880, 104944, 105344, 23184, 105632, 105696, 105360, 23168, 106384, 106448, 105376, 23216, 107136, 107200, 105392, 23200,
  108016, 107952, 105408, 23248, 108768, 108704, 105424, 23232, 109520, 109456, 105440, 23280, 110272, 110208, 105456, 23264,
  98880, 98944, 107520, 35392, 99664, 99728, 107536, 35408, 100448, 100512, 107552, 35424, 101232, 101296, 107568, 35440,
  101888, 102080, 107584, 35328, 102672, 102864, 107600, 35344, 103456, 103648, 107616, 35360, 104240, 104432, 107632, 35376,
  105024, 105088, 107648, 35520, 105808, 105872, 107664, 35536, 106592, 106656, 107680, 35552, 107376, 107440, 107696, 35568,
  108032, 108224, 107712, 35456, 108816, 109008, 107728, 35472, 109600, 109792, 107744, 35488, 110384, 110576, 107760, 35504,
  98896, 98960, 107776, 35648, 99648, 99712, 107792, 35664, 100464, 100528, 107808, 35680, 101216, 101280, 107824, 35696,
  101904, 102096, 107840, 35584, 102656, 102848, 107856, 35600, 103472, 103664, 107872, 35616, 104224, 104416, 107888, 35632,
  105040, 105104, 107904, 35776, 105792, 105856, 107920, 35792, 106608, 106672, 107936, 35808, 107360, 107424, 107952, 35824,
  108048, 108240, 107968, 35712, 108800, 108992, 107984, 35728, 109616, 109808, 108000, 35744, 110368, 110560, 108016, 35760,
  98912, 98976, 108032, 35904, 99696, 99760, 108048, 35920, 100416, 100480, 108064, 35936, 101200, 101264, 108080, 35952,
  101920, 102112, 108096, 35840, 102704, 102896, 108112, 35856, 103424, 103616, 108128, 35872, 104208, 104400, 108144, 35888,
  105056, 105120, 108160, 36032, 105840, 105904, 108176, 36048, 106560, 106624, 108192, 36064, 107344, 107408, 108208, 36080,
  108064, 108256, 108224, 35968, 108848, 109040, 108240, 35984, 109568, 109760, 108256, 36000, 110352, 110544, 108272, 36016,
  98928, 98992, 108288, 36944, 99680, 99744, 108304, 36928, 100432, 100496, 108320, 36976, 101184, 101248, 108336, 36960,
  101936, 102128, 108352, 36880, 102688, 102880, 108368, 36864, 103440, 103632, 108384, 36912, 104192, 104384, 108400, 36896,
  105072, 105136, 108416, 37072, 105824, 105888, 108432, 37056, 106576, 106640, 108448, 37104, 107328, 107392, 108464, 37088,
  108080, 108272, 108480, 37008, 108832, 109024, 108496, 36992, 109584, 109776, 108512, 37040, 110336, 110528, 108528, 37024,
  23312, 32800, 6208, 35520, 23296, 32816, 6224, 35536, 23344, 32768, 6240, 35552, 23328, 32784, 6256, 35568,
  23376, 32864, 6144, 35456, 23360, 32880, 6160, 35472, 23408, 32832, 6176, 35488, 23392, 32848, 6192, 35504,
  23440, 32928, 6336, 35392, 23424, 32944, 6352, 35408, 23472, 32896, 6368, 35424, 23456, 32912, 6384, 35440,
  23504, 32992, 6272, 35328, 23488, 33008, 6288, 35344, 23536, 32960, 6304, 35360, 23520, 32976, 6320, 35376,
  23568, 33840, 6976, 36288, 23552, 33824, 6992, 36304, 23600, 33808, 7008, 36320, 23584, 33792, 7024, 36336,
  23632, 33904, 6912, 36224, 23616, 33888, 6928, 36240, 23664, 33872, 6944, 36256, 23648, 33856, 6960, 36272,
  23696, 33968, 7104, 36160, 23680, 33952, 7120, 36176, 23728, 33936, 7136, 36192, 23712, 33920, 7152, 36208,
  23760, 34032, 7040, 36096, 23744, 34016, 7056, 36112, 23792, 34000, 7072, 36128, 23776, 33984, 7088, 36144,
  32288, 34096, 16976, 37072, 32304, 34080, 16960, 37056, 32256, 34064, 17008, 37104, 32272, 34048, 16992, 37088,
  32352, 34160, 16912, 37008, 32368, 34144, 16896, 36992, 32320, 34128, 16944, 37040, 32336, 34112, 16928, 37024,
  32416, 34224, 17104, 36944, 32432, 34208, 17088, 36928, 32384, 34192, 17136, 36976, 32400, 34176, 17120, 36960,
  32480, 34288, 17040, 36880, 32496, 34272, 17024, 36864, 32448, 34256, 17072, 36912, 32464, 34240, 17056, 36896,
  32544, 34352, 17744, 37840, 32560, 34336, 17728, 37824, 32512, 34320, 17776, 37872, 32528, 34304, 17760, 37856,
  32608, 34416, 17680, 37776, 32624, 34400, 17664, 37760, 32576, 34384, 17712, 37808, 32592, 34368, 17696, 37792,
  32672, 34480, 17872, 37712, 32688, 34464, 17856, 37696, 32640, 34448, 17904, 37744, 32656, 34432, 17888, 37728,
  32736, 34544, 17808, 37648, 32752, 34528, 17792, 37632, 32704, 34512, 17840, 37680, 32720, 34496, 17824, 37664,
  37200, 39008, 18528, 38624, 37184, 39024, 18544, 38640, 37232, 38976, 18496, 38592, 37216, 38992, 18512, 38608,
  37136, 38944, 18464, 38560, 37120, 38960, 18480, 38576, 37168, 38912, 18432, 38528, 37152, 38928, 18448, 38544,
  37328, 39136, 18656, 38496, 37312, 39152, 18672, 38512, 37360, 39104, 18624, 38464, 37344, 39120, 18640, 38480,
  37264, 39072, 18592, 38432, 37248, 39088, 18608, 38448, 37296, 39040, 18560, 38400, 37280, 39056, 18576, 38416,
  37456, 40048, 19296, 39392, 37440, 40032, 19312, 39408, 37488, 40016, 19264, 39360, 37472, 40000, 19280, 39376,
  37392, 39984, 19232, 39328, 37376, 39968, 19248, 39344, 37424, 39952, 19200, 39296, 37408, 39936, 19216, 39312,
  37584, 40176, 19424, 39264, 37568, 40160, 19440, 39280, 37616, 40144, 19392, 39232, 37600, 40128, 19408, 39248,
  37520, 40112, 19360, 39200, 37504, 40096, 19376, 39216, 37552, 40080, 19328, 39168, 37536, 40064, 19344, 39184,
  38496, 40304, 20080, 40176, 38512, 40288, 20064, 40160, 38464, 40272, 20048, 40144, 38480, 40256, 20032, 40128,
  38432, 40240, 20016, 40112, 38448, 40224, 20000, 40096, 38400, 40208, 19984, 40080, 38416, 40192, 19968, 40064,
  38624, 40432, 20208, 40048, 38640, 40416, 20192, 40032, 38592, 40400, 20176, 40016, 38608, 40384, 20160, 40000,
  38560, 40368, 20144, 39984, 38576, 40352, 20128, 39968, 38528, 40336, 20112, 39952, 38544, 40320, 20096, 39936,
  38752, 40560, 20848, 40944, 38768, 40544, 20832, 40928, 38720, 40528, 20816, 40912, 38736, 40512, 20800, 40896,
  38688, 40496, 20784, 40880, 38704, 40480, 20768, 40864, 38656, 40464, 20752, 40848, 38672, 40448, 20736, 40832,
  38880, 40688, 20976, 40816, 38896, 40672, 20960, 40800, 38848, 40656, 20944, 40784, 38864, 40640, 20928, 40768,
  38816, 40624, 20912, 40752, 38832, 40608, 20896, 40736, 38784, 40592, 20880, 40720, 38800, 40576, 20864, 40704};

__global__ __attribute__((aligned(4096))) __launch_bounds__(256, 1) void fpn_kernel(const char* __restrict__ cf, const char* __restrict__ pc,
                                                     const char* __restrict__ w1, const char* __restrict__ w2,
                                                     char* __restrict__ ws, char* __restrict__ out) {
  unsigned lane = threadIdx.x & 63;
  unsigned wid = __builtin_amdgcn_readfirstlane(threadIdx.x >> 6);
  unsigned b = blockIdx.x;
  unsigned long long t64;
  asm volatile("s_memrealtime %0" : "={s[88:89]}"(t64), "+s"(b), "+s"(wid));
  const unsigned t_start = (unsigned)t64;
  const unsigned pr = (b >> 3) * 4 + (b & 3);
  unsigned xo[9], pg[9];
#pragma unroll
  for (int j = 0; j < 9; ++j) {
    xo[j] = ((3 * pr) >> 1) * 589824u + (pr & 1) * (147456u + (j >= 3 && j < 6 ? 294912u : 0)) + j * 49152u + (j >= 6 ? 294912u : 0);
    pg[j] = xo[j] >> 21;
  }
  int dj[9];
#pragma unroll
  for (int j = 1; j < 9; ++j) dj[j] = (int)(pg[j] - pg[j - 1]) * (j <= 4 ? -12288 : 12288);
  const int d0 = (int)(pg[4] - pg[0]) * 12288;
  dj[0] = d0 - (int)(pg[8] - pg[4]) * 12288;
  const unsigned scr = 159744 + wid * 4;
  const int sgn = (4 * wid + (lane >> 4)) < 10 ? 1 : -1;
  const unsigned mP = 73728 + wid * 1024;
  unsigned pvo = __umul24(col0(4 * wid + (lane >> 4)), 768) + ((((lane & 15) ^ (lane >> 4) ^ (4 * wid)) & 15) << 4)
                 + pr * (9 * 24576);
  int sg1;
  u32x2 cd, cr, po;
  po[0] = xo[4] + lane * 1024 + (b & 4) * 128;
  po[1] = po[0] + 8192;
  const unsigned mJ = 110592 + wid * 6144;
  const unsigned var = (b + 112) >> 8;
  u32x4 ja0, ja1, ja2, ja3, ja4, ja5;
  u32x2 ep;
  unsigned two = min((((b >> 3) - 18) * 4 + wid) * 42 + min(lane, 41u), 2303u) * 128;
  unsigned tcode = ((((b >> 3) - 18) * 4 + wid) * 11 + min(lane, 10u)) * 128;
  const unsigned jb = b < 144 ? b : 0;
  const unsigned G32 = (jb * 171) >> 11, jcb = jb - 12 * G32;
  {
    const unsigned jw = __umul24(lane >> 2, 768) + (lane & 3) * 16 + 64 * G32 + wid * (96 * 768);
    const unsigned ja = __umul24(8 * ((lane & 15) >> 2) + (lane & 3), 768) + 16 * (lane >> 4) + jcb * (32 * 768) + wid * 192;
    const unsigned cla = 1048576 + pr * 128;
    asm volatile(
      "s_nop 4\n"
      "s_cmp_lg_u32 s76, 0\n"
      "s_cbranch_scc1 LQ0\n"
      "s_mul_i32 s62, s88, 0x1080\n"
      "s_and_b32 s62, s62, 0x4180\n"
      "s_add_u32 s56, s30, s62\n"
      "s_addc_u32 s57, s31, 0\n"
      "s_cmp_lg_u32 s76, 0\n"
      "s_mov_b64 exec, 0x1\n"
      "global_load_dword v116, v118, s[56:57] nt\n"
      "global_load_dword v117, v119, s[56:57] nt\n"
      "s_mov_b64 exec, -1\n"
      "LQ0:\n"
      "v_mov_b32 v0, 0\n"
      "s_barrier\n"
      "global_load_dwordx2 v[252:253], v0, s[28:29] sc1\n"
      "s_cbranch_scc1 LQ1\n"
      ".p2align 5\n"
      "s_waitcnt vmcnt(2)\n"
      "s_memtime s[60:61]\n"
      "s_waitcnt vmcnt(1)\n"
      "s_memtime s[62:63]\n"
      "s_waitcnt vmcnt(0) lgkmcnt(0)\n"
      "s_sub_u32 s62, s62, s60\n"
      "s_bfe_u32 s63, s51, 0x10002\n"
      "s_cmp_lt_u32 s62, 30\n"
      "s_cselect_b32 s62, 1, 0\n"
      "v_mov_b32 v116, s62\n"
      "ds_write_b32 v108, v116 offset:16\n"
      "s_xor_b32 s62, s62, s63\n"
      "v_add_co_u32 v110, vcc, 0x7f4a7c15, v252\n"
      "v_mov_b32 v111, 0x9e3779b9\n"
      "v_addc_co_u32 v111, vcc, v111, v253, vcc\n"
      "v_and_or_b32 v110, v110, -2, s62\n"
      "s_mov_b64 exec, 1\n"
      "global_atomic_swap_x2 v[112:113], v114, v[110:111], s[28:29] sc0 sc1\n"
      "s_mov_b64 exec, -1\n"
      "LQ1:\n"
      "s_cmp_lg_u32 s50, 0\n"
      "s_cbranch_scc0 LU1\n"
      "s_getpc_b64 s[56:57]\n"
      "LC0_1:\n"
      "v_min_u32 v106, LCEND-LC0_1-4, v106\n"
      "global_load_dword v106, v106, s[56:57]\n"
      "global_load_dword v105, v105, s[26:27]\n"
      "v_and_b32 v117, -16, v108\n"
      "s_waitcnt lgkmcnt(0)\n"
      "s_barrier\n"
      "ds_read_b32 v116, v117 offset:16\n"
      "s_waitcnt lgkmcnt(0)\n"
      "v_readfirstlane_b32 s62, v116\n"
      "s_nop 3\n"
      "s_mul_i32 s63, s62, 0x3000\n"
      "s_sub_u32 s82, s98, s63\n"
      "s_lshl_b32 s62, s62, 1\n"
      "s_sub_u32 s62, 1, s62\n"
      "v_mul_i32_i24 v107, s62, v251\n"
      "v_mad_i32_i24 v238, v107, s82, v238\n"
      "s_add_u32 s56, s22, 0x0\n"
      "s_addc_u32 s57, s23, 0\n"
      "s_add_u32 m0, s18, 0x0\n"
      "s_nop 0\n"
      "global_load_lds_dwordx4 v238, s[56:57] offset:0 \n"
      "v_mad_i32_i24 v238, v107, s90, v238\n"
      "s_add_u32 s56, s22, 0x6000\n"
      "s_addc_u32 s57, s23, 0\n"
      "s_add_u32 m0, s18, 0x1000\n"
      "s_nop 0\n"
      "global_load_lds_dwordx4 v238, s[56:57] offset:0 \n"
      "v_mad_i32_i24 v238, v107, s91, v238\n"
      "s_add_u32 s56, s22, 0xc000\n"
      "s_addc_u32 s57, s23, 0\n"
      "s_add_u32 m0, s18, 0x2000\n"
      "s_nop 0\n"
      "global_load_lds_dwordx4 v238, s[56:57] offset:0 \n"
      "v_mad_i32_i24 v238, v107, s92, v238\n"
      "s_add_u32 s56, s22, 0x12000\n"
      "s_addc_u32 s57, s23, 0\n"
      "s_add_u32 m0, s18, 0x3000\n"
      "s_nop 0\n"
      "global_load_lds_dwordx4 v238, s[56:57] offset:0 \n"
      "v_mad_i32_i24 v238, v107, s93, v238\n"
      "s_add_u32 s56, s22, 0x18000\n"
      "s_addc_u32 s57, s23, 0\n"
      "s_add_u32 m0, s18, 0x4000\n"
      "s_nop 0\n"
      "global_load_lds_dwordx4 v238, s[56:57] offset:0 \n"
      "v_mad_i32_i24 v238, v107, s94, v238\n"
      "s_add_u32 s56, s22, 0x1e000\n"
      "s_addc_u32 s57, s23, 0\n"
      "s_add_u32 m0, s18, 0x5000\n"
      "s_nop 0\n"
      "global_load_lds_dwordx4 v238, s[56:57] offset:0 \n"
      "v_mad_i32_i24 v238, v107, s95, v238\n"
      "s_add_u32 s56, s22, 0x24000\n"
      "s_addc_u32 s57, s23, 0\n"
      "s_add_u32 m0, s18, 0x6000\n"
      "s_nop 0\n"
      "global_load_lds_dwordx4 v238, s[56:57] offset:0 \n"
      "v_mad_i32_i24 v238, v107, s96, v238\n"
      "s_add_u32 s56, s22, 0x2a000\n"
      "s_addc_u32 s57, s23, 0\n"
      "s_add_u32 m0, s18, 0x7000\n"
      "s_nop 0\n"
      "global_load_lds_dwordx4 v238, s[56:57] offset:0 \n"
      "v_mad_i32_i24 v238, v107, s97, v238\n"
      "s_add_u32 s56, s22, 0x30000\n"
      "s_addc_u32 s57, s23, 0\n"
      "s_add_u32 m0, s18, 0x8000\n"
      "s_nop 0\n"
      "global_load_lds_dwordx4 v238, s[56:57] offset:0 \n"
      "s_branch LUE\n"
      "LU1:\n"
      "s_add_u32 s56, s26, 0x0\n"
      "s_addc_u32 s57, s27, 0\n"
      "s_add_u32 m0, s75, 0x0\n"
      "s_nop 0\n"
      "global_load_lds_dwordx4 v100, s[56:57]\n"
      "s_add_u32 s56, s26, 0x3000\n"
      "s_addc_u32 s57, s27, 0\n"
      "s_add_u32 m0, s75, 0x400\n"
      "s_nop 0\n"
      "global_load_lds_dwordx4 v100, s[56:57]\n"
      "s_add_u32 s56, s26, 0x6000\n"
      "s_addc_u32 s57, s27, 0\n"
      "s_add_u32 m0, s75, 0x800\n"
      "s_nop 0\n"
      "global_load_lds_dwordx4 v100, s[56:57]\n"
      "s_add_u32 s56, s26, 0x9000\n"
      "s_addc_u32 s57, s27, 0\n"
      "s_add_u32 m0, s75, 0xc00\n"
      "s_nop 0\n"
      "global_load_lds_dwordx4 v100, s[56:57]\n"
      "s_add_u32 s56, s26, 0xc000\n"
      "s_addc_u32 s57, s27, 0\n"
      "s_add_u32 m0, s75, 0x1000\n"
      "s_nop 0\n"
      "global_load_lds_dwordx4 v100, s[56:57]\n"
      "s_add_u32 s56, s26, 0xf000\n"
      "s_addc_u32 s57, s27, 0\n"
      "s_add_u32 m0, s75, 0x1400\n"
      "s_nop 0\n"
      "global_load_lds_dwordx4 v100, s[56:57]\n"
      "global_load_dwordx4 v[4:7], v101, s[24:25] offset:0\n"
      "global_load_dwordx4 v[8:11], v101, s[24:25] offset:3072\n"
      "global_load_dwordx4 v[12:15], v101, s[24:25] offset:64\n"
      "global_load_dwordx4 v[16:19], v101, s[24:25] offset:3136\n"
      "global_load_dwordx4 v[20:23], v101, s[24:25] offset:128\n"
      "global_load_dwordx4 v[24:27], v101, s[24:25] offset:3200\n"
      "LUE:\n"

      : "={v[4:7]}"(ja0), "={v[8:11]}"(ja1), "={v[12:15]}"(ja2), "={v[16:19]}"(ja3), "={v[20:23]}"(ja4), "={v[24:27]}"(ja5),
        "={v[252:253]}"(ep), "+{v105}"(two), "+{v106}"(tcode), "={v[110:111]}"(cd), "={v[112:113]}"(cr), "+{v[118:119]}"(po),
        "+{v238}"(pvo), "={v107}"(sg1)
      : "{v114}"(cla), "{s[88:89]}"(t64), "{v100}"(jw), "{v101}"(ja), "{s[20:21]}"(cf), "{s[24:25]}"(w1), "{s[26:27]}"(w2), "{s[28:29]}"(ws), "{s[30:31]}"(out),
        "{s75}"(mJ), "{s51}"(b), "{s50}"(var), "{s76}"(wid), "{s[22:23]}"(pc), "{s18}"(mP), "{v108}"(scr), "{v251}"(sgn),
        "{s90}"(dj[1]), "{s91}"(dj[2]), "{s92}"(dj[3]), "{s93}"(dj[4]), "{s94}"(dj[5]), "{s95}"(dj[6]), "{s96}"(dj[7]), "{s97}"(dj[8]), "{s98}"(d0)
      : "memory", "scc", "vcc", "v0", "v116", "v117", "s56", "s57", "s60", "s61", "s62", "s63", "s82");
  }
  asm volatile("" : "+v"(lane));
  asm volatile("" : "+s"(wid));
  unsigned wro[3];
#pragma unroll
  for (int i = 0; i < 3; ++i) { const unsigned x = lane + 64 * i, q = __umul24(x, 171) >> 11; wro[i] = __umul24(q, 576) + x * 16; }
  const unsigned mW = wid * 6144;
  const char* w2w = w2 + wid * 192;
  const unsigned lo16 = lane * 16;
  const char* wb = ws + (65536 + wid * 73728);
  const unsigned c = lane & 15, g = lane >> 4;
  const unsigned cB0 = 73728 + c * 256 + ((g ^ c) << 4);
  const unsigned r8 = 8 * g + (c >> 2), c3 = (c & 3) * 16;
  const unsigned tra = mW + __umul24(r8, 192) + c3;
  const unsigned mX = wid * 1024;
  const unsigned fla = 4096 + wid * 288 + min(lo16, 272u);
  const unsigned c256 = c * 256 - 73728;
  const unsigned jfl = (b < 144 ? 4096 : 8192) + 8 * b;
  char* jo = ws + (65536 + (24 * G32 + jcb) * 1024);
  const unsigned jt = mJ + r8 * 64 + c3;
  const unsigned jrr = 135168 + lo16;
  const unsigned jrw = jrr + wid * 4096;
  const unsigned pk = 151552 + mX + lo16;
  const char* tab = (const char*)lane_tab + ((wid >> 1) * 4096 + (wid & 1) * 1024);
  asm volatile(
      "s_nop 4\n"
      "LBODY:\n"
      "s_branch LB0\n"
      "LRET:\n"
      "s_setpc_b64 s[56:57]\n"
      "LB0:\n"
      "s_cmp_lg_u32 s50, 0\n"
      "s_cbranch_scc1 LV1\n"
      "s_getpc_b64 s[56:57]\n"
      "LTR_0:\n"
      "s_add_u32 s56, s56, LTE_0-LTR_0\n"
      "s_addc_u32 s57, s57, 0\n"
      "s_mov_b64 exec, 0\n"
      "s_cmp_eq_u32 s76, 3\n"
      "s_cbranch_scc1 LPD2\n"
      "s_cmp_eq_u32 s76, 2\n"
      "s_cbranch_scc1 LPD1\n"
      "s_cmp_eq_u32 s76, 1\n"
      "s_cbranch_scc1 LPD0\n"
      "LTE_0:\n"
      "s_mov_b64 exec, -1\n"
      "s_waitcnt vmcnt(12)\n"
      "v_readfirstlane_b32 s80, v252\n"
      "v_readfirstlane_b32 s81, v253\n"
      "s_nop 3\n"
      "s_add_u32 s80, s80, 0x7f4a7c15\n"
      "s_addc_u32 s81, s81, 0x9e3779b9\n"
      "v_mov_b32 v252, s80\n"
      "v_mov_b32 v253, s81\n"
      "v_xor_b32 v116, v112, v110\n"
      "v_xor_b32 v117, v113, v111\n"
      "v_and_or_b32 v116, v116, -2, v117\n"
      "v_cmp_eq_u32 vcc, 0, v116\n"
      "v_cndmask_b32 v116, v110, v112, vcc\n"
      "s_mov_b64 exec, 1\n"
      "ds_write_b32 v108, v116\n"
      "s_mov_b64 exec, -1\n"
      "v_and_b32 v108, -16, v108\n"
      "s_waitcnt lgkmcnt(0)\n"
      "s_barrier\n"
      "ds_read_b32 v116, v108\n"
      "s_waitcnt lgkmcnt(0)\n"
      "v_readfirstlane_b32 s62, v116\n"
      "s_lshr_b32 s63, s51, 2\n"
      "s_nop 2\n"
      "s_xor_b32 s62, s62, s63\n"
      "s_and_b32 s62, s62, 1\n"
      "s_mul_i32 s63, s62, 0x3000\n"
      "s_sub_u32 s98, s98, s63\n"
      "s_lshl_b32 s62, s62, 1\n"
      "s_sub_u32 s62, 1, s62\n"
      "v_mul_i32_i24 v251, s62, v251\n"
      "s_lshl_b32 s19, s62, 1\n"
      "s_mul_i32 s100, s100, s62\n"
      "v_mad_i32_i24 v238, v251, s98, v238\n"
      "s_add_u32 s56, s22, 0x0\n"
      "s_addc_u32 s57, s23, 0\n"
      "s_add_u32 m0, s18, 0x0\n"
      "s_nop 0\n"
      "global_load_lds_dwordx4 v238, s[56:57] offset:0 \n"
      "v_mad_i32_i24 v238, v251, s90, v238\n"
      "s_add_u32 s56, s22, 0x6000\n"
      "s_addc_u32 s57, s23, 0\n"
      "s_add_u32 m0, s18, 0x1000\n"
      "s_nop 0\n"
      "global_load_lds_dwordx4 v238, s[56:57] offset:0 \n"
      "v_mad_i32_i24 v238, v251, s91, v238\n"
      "s_add_u32 s56, s22, 0xc000\n"
      "s_addc_u32 s57, s23, 0\n"
      "s_add_u32 m0, s18, 0x2000\n"
      "s_nop 0\n"
      "global_load_lds_dwordx4 v238, s[56:57] offset:0 \n"
      "v_mad_i32_i24 v238, v251, s92, v238\n"
      "s_add_u32 s56, s22, 0x12000\n"
      "s_addc_u32 s57, s23, 0\n"
      "s_add_u32 m0, s18, 0x3000\n"
      "s_nop 0\n"
      "global_load_lds_dwordx4 v238, s[56:57] offset:0 \n"
      "v_mad_i32_i24 v238, v251, s93, v238\n"
      "s_add_u32 s56, s22, 0x18000\n"
      "s_addc_u32 s57, s23, 0\n"
      "s_add_u32 m0, s18, 0x4000\n"
      "s_nop 0\n"
      "global_load_lds_dwordx4 v238, s[56:57] offset:0 \n"
      "v_mad_i32_i24 v238, v251, s94, v238\n"
      "s_add_u32 s56, s22, 0x1e000\n"
      "s_addc_u32 s57, s23, 0\n"
      "s_add_u32 m0, s18, 0x5000\n"
      "s_nop 0\n"
      "global_load_lds_dwordx4 v238, s[56:57] offset:0 \n"
      "v_mad_i32_i24 v238, v251, s95, v238\n"
      "s_add_u32 s56, s22, 0x24000\n"
      "s_addc_u32 s57, s23, 0\n"
      "s_add_u32 m0, s18, 0x6000\n"
      "s_nop 0\n"
      "global_load_lds_dwordx4 v238, s[56:57] offset:0 \n"
      "v_mad_i32_i24 v238, v251, s96, v238\n"
      "s_add_u32 s56, s22, 0x2a000\n"
      "s_addc_u32 s57, s23, 0\n"
      "s_add_u32 m0, s18, 0x7000\n"
      "s_nop 0\n"
      "global_load_lds_dwordx4 v238, s[56:57] offset:0 \n"
      "v_mad_i32_i24 v238, v251, s97, v238\n"
      "s_add_u32 s56, s22, 0x30000\n"
      "s_addc_u32 s57, s23, 0\n"
      "s_add_u32 m0, s18, 0x8000\n"
      "s_nop 0\n"
      "global_load_lds_dwordx4 v238, s[56:57] offset:0 \n"
      "s_waitcnt vmcnt(9)\n"
      "ds_read_b64_tr_b16 v[28:29], v102 offset:0\n"
      "ds_read_b64_tr_b16 v[30:31], v102 offset:256\n"
      "ds_read_b64_tr_b16 v[32:33], v102 offset:8\n"
      "ds_read_b64_tr_b16 v[34:35], v102 offset:264\n"
      "ds_read_b64_tr_b16 v[36:37], v102 offset:2048\n"
      "ds_read_b64_tr_b16 v[38:39], v102 offset:2304\n"
      "ds_read_b64_tr_b16 v[40:41], v102 offset:2056\n"
      "ds_read_b64_tr_b16 v[42:43], v102 offset:2312\n"
      "ds_read_b64_tr_b16 v[44:45], v102 offset:4096\n"
      "ds_read_b64_tr_b16 v[46:47], v102 offset:4352\n"
      "ds_read_b64_tr_b16 v[48:49], v102 offset:4104\n"
      "ds_read_b64_tr_b16 v[50:51], v102 offset:4360\n"
      "s_waitcnt lgkmcnt(0)\n"
      "v_mfma_f32_16x16x32_bf16 v[52:55], v[4:7], v[28:31], 0\n"
      "v_mfma_f32_16x16x32_bf16 v[56:59], v[8:11], v[28:31], 0\n"
      "v_mfma_f32_16x16x32_bf16 v[60:63], v[4:7], v[32:35], 0\n"
      "v_mfma_f32_16x16x32_bf16 v[64:67], v[8:11], v[32:35], 0\n"
      "v_mfma_f32_16x16x32_bf16 v[52:55], v[12:15], v[36:39], v[52:55]\n"
      "v_mfma_f32_16x16x32_bf16 v[56:59], v[16:19], v[36:39], v[56:59]\n"
      "v_mfma_f32_16x16x32_bf16 v[60:63], v[12:15], v[40:43], v[60:63]\n"
      "v_mfma_f32_16x16x32_bf16 v[64:67], v[16:19], v[40:43], v[64:67]\n"
      "v_mfma_f32_16x16x32_bf16 v[52:55], v[20:23], v[44:47], v[52:55]\n"
      "v_mfma_f32_16x16x32_bf16 v[56:59], v[24:27], v[44:47], v[56:59]\n"
      "v_mfma_f32_16x16x32_bf16 v[60:63], v[20:23], v[48:51], v[60:63]\n"
      "v_mfma_f32_16x16x32_bf16 v[64:67], v[24:27], v[48:51], v[64:67]\n"
      "s_nop 7\n"
      "ds_write_b128 v103, v[52:55] offset:0\n"
      "ds_write_b128 v103, v[56:59] offset:1024\n"
      "ds_write_b128 v103, v[60:63] offset:2048\n"
      "ds_write_b128 v103, v[64:67] offset:3072\n"
      "s_waitcnt lgkmcnt(0)\n"
      "s_barrier\n"
      "s_cmp_lg_u32 s76, 1\n"
      "s_cbranch_scc1 LJ1\n"
      "ds_read_b128 v[4:7], v104 offset:0\n"
      "ds_read_b128 v[8:11], v104 offset:1024\n"
      "ds_read_b128 v[12:15], v104 offset:2048\n"
      "ds_read_b128 v[16:19], v104 offset:3072\n"
      "ds_read_b128 v[20:23], v104 offset:4096\n"
      "ds_read_b128 v[24:27], v104 offset:5120\n"
      "ds_read_b128 v[28:31], v104 offset:6144\n"
      "ds_read_b128 v[32:35], v104 offset:7168\n"
      "ds_read_b128 v[36:39], v104 offset:8192\n"
      "ds_read_b128 v[40:43], v104 offset:9216\n"
      "ds_read_b128 v[44:47], v104 offset:10240\n"
      "ds_read_b128 v[48:51], v104 offset:11264\n"
      "ds_read_b128 v[52:55], v104 offset:12288\n"
      "ds_read_b128 v[56:59], v104 offset:13312\n"
      "ds_read_b128 v[60:63], v104 offset:14336\n"
      "ds_read_b128 v[64:67], v104 offset:15360\n"
      "s_waitcnt lgkmcnt(8)\n"
      "v_add_f32 v4, v4, v20\n"
      "v_add_f32 v5, v5, v21\n"
      "v_add_f32 v6, v6, v22\n"
      "v_add_f32 v7, v7, v23\n"
      "v_add_f32 v8, v8, v24\n"
      "v_add_f32 v9, v9, v25\n"
      "v_add_f32 v10, v10, v26\n"
      "v_add_f32 v11, v11, v27\n"
      "v_add_f32 v12, v12, v28\n"
      "v_add_f32 v13, v13, v29\n"
      "v_add_f32 v14, v14, v30\n"
      "v_add_f32 v15, v15, v31\n"
      "v_add_f32 v16, v16, v32\n"
      "v_add_f32 v17, v17, v33\n"
      "v_add_f32 v18, v18, v34\n"
      "v_add_f32 v19, v19, v35\n"
      "s_waitcnt lgkmcnt(4)\n"
      "v_add_f32 v4, v4, v36\n"
      "v_add_f32 v5, v5, v37\n"
      "v_add_f32 v6, v6, v38\n"
      "v_add_f32 v7, v7, v39\n"
      "v_add_f32 v8, v8, v40\n"
      "v_add_f32 v9, v9, v41\n"
      "v_add_f32 v10, v10, v42\n"
      "v_add_f32 v11, v11, v43\n"
      "v_add_f32 v12, v12, v44\n"
      "v_add_f32 v13, v13, v45\n"
      "v_add_f32 v14, v14, v46\n"
      "v_add_f32 v15, v15, v47\n"
      "v_add_f32 v16, v16, v48\n"
      "v_add_f32 v17, v17, v49\n"
      "v_add_f32 v18, v18, v50\n"
      "v_add_f32 v19, v19, v51\n"
      "s_waitcnt lgkmcnt(0)\n"
      "v_add_f32 v4, v4, v52\n"
      "v_add_f32 v5, v5, v53\n"
      "v_add_f32 v6, v6, v54\n"
      "v_add_f32 v7, v7, v55\n"
      "v_add_f32 v8, v8, v56\n"
      "v_add_f32 v9, v9, v57\n"
      "v_add_f32 v10, v10, v58\n"
      "v_add_f32 v11, v11, v59\n"
      "v_add_f32 v12, v12, v60\n"
      "v_add_f32 v13, v13, v61\n"
      "v_add_f32 v14, v14, v62\n"
      "v_add_f32 v15, v15, v63\n"
      "v_add_f32 v16, v16, v64\n"
      "v_add_f32 v17, v17, v65\n"
      "v_add_f32 v18, v18, v66\n"
      "v_add_f32 v19, v19, v67\n"
      "v_cvt_pk_bf16_f32 v68, v4, v5\n"
      "v_cvt_pk_bf16_f32 v69, v6, v7\n"
      "v_cvt_pk_bf16_f32 v70, v8, v9\n"
      "v_cvt_pk_bf16_f32 v71, v10, v11\n"
      "v_cvt_pk_bf16_f32 v72, v12, v13\n"
      "v_cvt_pk_bf16_f32 v73, v14, v15\n"
      "v_cvt_pk_bf16_f32 v74, v16, v17\n"
      "v_cvt_pk_bf16_f32 v75, v18, v19\n"
      "global_store_dwordx4 v239, v[68:71], s[72:73] sc1\n"
      "s_add_u32 s72, s72, 0x3000\n"
      "s_addc_u32 s73, s73, 0\n"
      "global_store_dwordx4 v239, v[72:75], s[72:73] sc1\n"
      "LJ1:\n"
      "s_add_u32 s78, s64, 0x0\n"
      "s_addc_u32 s79, s65, 0\n"
      "s_add_u32 m0, s74, 0x0\n"
      "s_nop 0\n"
      "global_load_lds_dwordx4 v240, s[78:79]\n"
      "s_add_u32 m0, s74, 0x400\n"
      "s_nop 0\n"
      "global_load_lds_dwordx4 v241, s[78:79]\n"
      "s_add_u32 m0, s74, 0x800\n"
      "s_nop 0\n"
      "global_load_lds_dwordx4 v242, s[78:79]\n"
      "s_add_u32 s78, s64, 0x3000\n"
      "s_addc_u32 s79, s65, 0\n"
      "s_add_u32 m0, s74, 0xc00\n"
      "s_nop 0\n"
      "global_load_lds_dwordx4 v240, s[78:79]\n"
      "s_add_u32 m0, s74, 0x1000\n"
      "s_nop 0\n"
      "global_load_lds_dwordx4 v241, s[78:79]\n"
      "s_add_u32 m0, s74, 0x1400\n"
      "s_nop 0\n"
      "global_load_lds_dwordx4 v242, s[78:79]\n"
      "s_add_u32 s78, s64, 0x6000\n"
      "s_addc_u32 s79, s65, 0\n"
      "s_add_u32 m0, s74, 0x6000\n"
      "s_nop 0\n"
      "global_load_lds_dwordx4 v240, s[78:79]\n"
      "s_add_u32 m0, s74, 0x6400\n"
      "s_nop 0\n"
      "global_load_lds_dwordx4 v241, s[78:79]\n"
      "s_add_u32 m0, s74, 0x6800\n"
      "s_nop 0\n"
      "global_load_lds_dwordx4 v242, s[78:79]\n"
      "s_add_u32 s78, s64, 0x9000\n"
      "s_addc_u32 s79, s65, 0\n"
      "s_add_u32 m0, s74, 0x6c00\n"
      "s_nop 0\n"
      "global_load_lds_dwordx4 v240, s[78:79]\n"
      "s_add_u32 m0, s74, 0x7000\n"
      "s_nop 0\n"
      "global_load_lds_dwordx4 v241, s[78:79]\n"
      "s_add_u32 m0, s74, 0x7400\n"
      "s_nop 0\n"
      "global_load_lds_dwordx4 v242, s[78:79]\n"
      "s_add_u32 s78, s64, 0xc000\n"
      "s_addc_u32 s79, s65, 0\n"
      "s_add_u32 m0, s74, 0xc000\n"
      "s_nop 0\n"
      "global_load_lds_dwordx4 v240, s[78:79]\n"
      "s_add_u32 m0, s74, 0xc400\n"
      "s_nop 0\n"
      "global_load_lds_dwordx4 v241, s[78:79]\n"
      "s_add_u32 m0, s74, 0xc800\n"
      "s_nop 0\n"
      "global_load_lds_dwordx4 v242, s[78:79]\n"
      "s_add_u32 s78, s64, 0xf000\n"
      "s_addc_u32 s79, s65, 0\n"
      "s_add_u32 m0, s74, 0xcc00\n"
      "s_nop 0\n"
      "global_load_lds_dwordx4 v240, s[78:79]\n"
      "s_add_u32 m0, s74, 0xd000\n"
      "s_nop 0\n"
      "global_load_lds_dwordx4 v241, s[78:79]\n"
      "s_add_u32 m0, s74, 0xd400\n"
      "s_nop 0\n"
      "global_load_lds_dwordx4 v242, s[78:79]\n"
      "s_add_u32 m0, s17, 0x25000\n"
      "s_nop 0\n"
      "global_load_lds_dwordx4 v239, s[46:47] offset:0\n"
      "s_add_u32 m0, s17, 0x25800\n"
      "s_nop 0\n"
      "global_load_lds_dwordx4 v239, s[46:47] offset:2048\n"
      "s_waitcnt vmcnt(14)\n"
      "ds_read_b64_tr_b16 a[0:1], v243 offset:0\n"
      "ds_read_b64_tr_b16 a[2:3], v243 offset:768\n"
      "ds_read_b64_tr_b16 a[4:5], v243 offset:8\n"
      "ds_read_b64_tr_b16 a[6:7], v243 offset:776\n"
      "ds_read_b64_tr_b16 a[8:9], v243 offset:64\n"
      "ds_read_b64_tr_b16 a[10:11], v243 offset:832\n"
      "ds_read_b64_tr_b16 a[12:13], v243 offset:72\n"
      "ds_read_b64_tr_b16 a[14:15], v243 offset:840\n"
      "ds_read_b64_tr_b16 a[16:17], v243 offset:128\n"
      "ds_read_b64_tr_b16 a[18:19], v243 offset:896\n"
      "ds_read_b64_tr_b16 a[20:21], v243 offset:136\n"
      "ds_read_b64_tr_b16 a[22:23], v243 offset:904\n"
      "s_barrier\n"
      "s_cmp_lg_u32 s76, 1\n"
      "s_cbranch_scc1 LA_0\n"
      "v_mov_b32 v244, s77\n"
      "s_mov_b64 exec, 1\n"
      "global_store_dwordx2 v244, v[252:253], s[28:29] sc1\n"
      "s_mov_b64 exec, -1\n"
      "s_nop 4\n"
      "LA_0:\n"
      "ds_read_b128 v[216:219], v232 offset:0\n"
      "ds_read_b128 v[220:223], v232 offset:4096\n"
      "ds_read_b128 v[224:227], v232 offset:8192\n"
      "s_waitcnt lgkmcnt(2)\n"
      "v_mfma_f32_16x16x32_bf16 v[0:3], a[0:3], v[216:219], 0\n"
      "s_add_u32 s78, s64, 0x12000\n"
      "s_addc_u32 s79, s65, 0\n"
      "s_add_u32 m0, s74, 0x0\n"
      "v_mfma_f32_16x16x32_bf16 v[4:7], a[4:7], v[216:219], 0\n"
      "global_load_lds_dwordx4 v240, s[78:79]\n"
      "v_mfma_f32_16x16x32_bf16 v[8:11], a[8:11], v[216:219], 0\n"
      "ds_read_b128 v[228:231], v232 offset:12288\n"
      "v_mfma_f32_16x16x32_bf16 v[12:15], a[12:15], v[216:219], 0\n"
      "v_mfma_f32_16x16x32_bf16 v[16:19], a[16:19], v[216:219], 0\n"
      "s_add_u32 m0, s74, 0x400\n"
      "v_mfma_f32_16x16x32_bf16 v[20:23], a[20:23], v[216:219], 0\n"
      "global_load_lds_dwordx4 v241, s[78:79]\n"
      "s_waitcnt lgkmcnt(2)\n"
      "v_mfma_f32_16x16x32_bf16 v[24:27], a[0:3], v[220:223], 0\n"
      "s_waitcnt vmcnt(10)\n"
      "v_mfma_f32_16x16x32_bf16 v[28:31], a[4:7], v[220:223], 0\n"
      "ds_read_b64_tr_b16 a[24:25], v243 offset:24576\n"
      "v_mfma_f32_16x16x32_bf16 v[32:35], a[8:11], v[220:223], 0\n"
      "ds_read_b128 v[216:219], v232 offset:16384\n"
      "s_add_u32 m0, s74, 0x800\n"
      "v_mfma_f32_16x16x32_bf16 v[36:39], a[12:15], v[220:223], 0\n"
      "ds_read_b64_tr_b16 a[26:27], v243 offset:25344\n"
      "global_load_lds_dwordx4 v242, s[78:79]\n"
      "v_mfma_f32_16x16x32_bf16 v[40:43], a[16:19], v[220:223], 0\n"
      "LPD0:\n"
      "s_cbranch_execz LPD3\n"
      "ds_read_b64_tr_b16 a[28:29], v243 offset:24584\n"
      "v_mfma_f32_16x16x32_bf16 v[44:47], a[20:23], v[220:223], 0\n"
      "s_waitcnt lgkmcnt(5)\n"
      "v_mfma_f32_16x16x32_bf16 v[48:51], a[0:3], v[224:227], 0\n"
      "ds_read_b64_tr_b16 a[30:31], v243 offset:25352\n"
      "s_add_u32 s78, s64, 0x15000\n"
      "s_addc_u32 s79, s65, 0\n"
      "s_add_u32 m0, s74, 0xc00\n"
      "v_mfma_f32_16x16x32_bf16 v[52:55], a[4:7], v[224:227], 0\n"
      "ds_read_b64_tr_b16 a[32:33], v243 offset:24640\n"
      "global_load_lds_dwordx4 v240, s[78:79]\n"
      "v_mfma_f32_16x16x32_bf16 v[56:59], a[8:11], v[224:227], 0\n"
      "ds_read_b128 v[220:223], v232 offset:20480\n"
      "v_mfma_f32_16x16x32_bf16 v[60:63], a[12:15], v[224:227], 0\n"
      "ds_read_b64_tr_b16 a[34:35], v243 offset:25408\n"
      "v_mfma_f32_16x16x32_bf16 v[64:67], a[16:19], v[224:227], 0\n"
      "ds_read_b64_tr_b16 a[36:37], v243 offset:24648\n"
      "s_add_u32 m0, s74, 0x1000\n"
      "v_mfma_f32_16x16x32_bf16 v[68:71], a[20:23], v[224:227], 0\n"
      "global_load_lds_dwordx4 v241, s[78:79]\n"
      "s_waitcnt lgkmcnt(9)\n"
      "v_mfma_f32_16x16x32_bf16 v[72:75], a[0:3], v[228:231], 0\n"
      "ds_read_b64_tr_b16 a[38:39], v243 offset:25416\n"
      "v_mfma_f32_16x16x32_bf16 v[76:79], a[4:7], v[228:231], 0\n"
      "ds_read_b64_tr_b16 a[40:41], v243 offset:24704\n"
      "v_mfma_f32_16x16x32_bf16 v[80:83], a[8:11], v[228:231], 0\n"
      "ds_read_b128 v[224:227], v232 offset:24576\n"
      "s_add_u32 m0, s74, 0x1400\n"
      "v_mfma_f32_16x16x32_bf16 v[84:87], a[12:15], v[228:231], 0\n"
      "ds_read_b64_tr_b16 a[42:43], v243 offset:25472\n"
      "global_load_lds_dwordx4 v242, s[78:79]\n"
      "v_mfma_f32_16x16x32_bf16 v[88:91], a[16:19], v[228:231], 0\n"
      "ds_read_b64_tr_b16 a[44:45], v243 offset:24712\n"
      "v_mfma_f32_16x16x32_bf16 v[92:95], a[20:23], v[228:231], 0\n"
      "s_waitcnt lgkmcnt(12)\n"
      "v_mfma_f32_16x16x32_bf16 v[96:99], a[0:3], v[216:219], 0\n"
      "ds_read_b64_tr_b16 a[46:47], v243 offset:25480\n"
      "v_mfma_f32_16x16x32_bf16 v[100:103], a[4:7], v[216:219], 0\n"
      "s_waitcnt vmcnt(8)\n"
      "v_mfma_f32_16x16x32_bf16 v[104:107], a[8:11], v[216:219], 0\n"
      "ds_read_b128 v[228:231], v232 offset:28672\n"
      "v_mfma_f32_16x16x32_bf16 v[108:111], a[12:15], v[216:219], 0\n"
      "ds_read_b64_tr_b16 a[48:49], v243 offset:49152\n"
      "v_mfma_f32_16x16x32_bf16 v[112:115], a[16:19], v[216:219], 0\n"
      "ds_read_b64_tr_b16 a[50:51], v243 offset:49920\n"
      "v_mfma_f32_16x16x32_bf16 v[116:119], a[20:23], v[216:219], 0\n"
      "s_waitcnt lgkmcnt(11)\n"
      "v_mfma_f32_16x16x32_bf16 v[120:123], a[0:3], v[220:223], 0\n"
      "ds_read_b64_tr_b16 a[52:53], v243 offset:49160\n"
      "v_mfma_f32_16x16x32_bf16 v[124:127], a[4:7], v[220:223], 0\n"
      "ds_read_b64_tr_b16 a[54:55], v243 offset:49928\n"
      "v_mfma_f32_16x16x32_bf16 v[128:131], a[8:11], v[220:223], 0\n"
      "ds_read_b128 v[216:219], v232 offset:32768\n"
      "v_add_u32 v232, 0x9000, v232\n"
      "v_mfma_f32_16x16x32_bf16 v[132:135], a[12:15], v[220:223], 0\n"
      "ds_read_b64_tr_b16 a[56:57], v243 offset:49216\n"
      "v_mfma_f32_16x16x32_bf16 v[136:139], a[16:19], v[220:223], 0\n"
      "ds_read_b64_tr_b16 a[58:59], v243 offset:49984\n"
      "s_waitcnt lgkmcnt(8)\n"
      "s_add_u32 s78, s64, 0x18000\n"
      "s_addc_u32 s79, s65, 0\n"
      "s_add_u32 m0, s74, 0x6000\n"
      "v_mfma_f32_16x16x32_bf16 v[140:143], a[20:23], v[220:223], 0\n"
      "global_load_lds_dwordx4 v240, s[78:79]\n"
      "v_mfma_f32_16x16x32_bf16 v[144:147], a[0:3], v[224:227], 0\n"
      "ds_read_b64_tr_b16 a[60:61], v243 offset:49224\n"
      "v_mfma_f32_16x16x32_bf16 v[148:151], a[4:7], v[224:227], 0\n"
      "ds_read_b64_tr_b16 a[62:63], v243 offset:49992\n"
      "v_mfma_f32_16x16x32_bf16 v[152:155], a[8:11], v[224:227], 0\n"
      "ds_read_b128 v[220:223], v233 offset:0\n"
      "s_add_u32 m0, s74, 0x6400\n"
      "v_mfma_f32_16x16x32_bf16 v[156:159], a[12:15], v[224:227], 0\n"
      "ds_read_b64_tr_b16 a[64:65], v243 offset:49280\n"
      "global_load_lds_dwordx4 v241, s[78:79]\n"
      "v_mfma_f32_16x16x32_bf16 v[160:163], a[16:19], v[224:227], 0\n"
      "ds_read_b64_tr_b16 a[66:67], v243 offset:50048\n"
      "v_mfma_f32_16x16x32_bf16 v[164:167], a[20:23], v[224:227], 0\n"
      "s_waitcnt lgkmcnt(12)\n"
      "v_mfma_f32_16x16x32_bf16 v[168:171], a[0:3], v[228:231], 0\n"
      "ds_read_b64_tr_b16 a[68:69], v243 offset:49288\n"
      "s_add_u32 m0, s74, 0x6800\n"
      "v_mfma_f32_16x16x32_bf16 v[172:175], a[4:7], v[228:231], 0\n"
      "ds_read_b64_tr_b16 a[70:71], v243 offset:50056\n"
      "global_load_lds_dwordx4 v242, s[78:79]\n"
      "v_mfma_f32_16x16x32_bf16 v[176:179], a[8:11], v[228:231], 0\n"
      "ds_read_b128 v[224:227], v233 offset:4096\n"
      "v_mfma_f32_16x16x32_bf16 v[180:183], a[12:15], v[228:231], 0\n"
      "v_mfma_f32_16x16x32_bf16 v[184:187], a[16:19], v[228:231], 0\n"
      "s_add_u32 s78, s64, 0x1b000\n"
      "s_addc_u32 s79, s65, 0\n"
      "s_add_u32 m0, s74, 0x6c00\n"
      "v_mfma_f32_16x16x32_bf16 v[188:191], a[20:23], v[228:231], 0\n"
      "global_load_lds_dwordx4 v240, s[78:79]\n"
      "s_waitcnt lgkmcnt(10)\n"
      "v_mfma_f32_16x16x32_bf16 v[192:195], a[0:3], v[216:219], 0\n"
      "v_mfma_f32_16x16x32_bf16 v[196:199], a[4:7], v[216:219], 0\n"
      "v_mfma_f32_16x16x32_bf16 v[200:203], a[8:11], v[216:219], 0\n"
      "ds_read_b128 v[228:231], v233 offset:8192\n"
      "s_add_u32 m0, s74, 0x7000\n"
      "v_mfma_f32_16x16x32_bf16 v[204:207], a[12:15], v[216:219], 0\n"
      "global_load_lds_dwordx4 v241, s[78:79]\n"
      "v_mfma_f32_16x16x32_bf16 v[208:211], a[16:19], v[216:219], 0\n"
      "v_mfma_f32_16x16x32_bf16 v[212:215], a[20:23], v[216:219], 0\n"
      "s_waitcnt lgkmcnt(6)\n"
      "v_mfma_f32_16x16x32_bf16 v[0:3], a[24:27], v[220:223], v[0:3]\n"
      "s_add_u32 m0, s74, 0x7400\n"
      "v_mfma_f32_16x16x32_bf16 v[4:7], a[28:31], v[220:223], v[4:7]\n"
      "global_load_lds_dwordx4 v242, s[78:79]\n"
      "v_mfma_f32_16x16x32_bf16 v[8:11], a[32:35], v[220:223], v[8:11]\n"
      "ds_read_b128 v[216:219], v233 offset:12288\n"
      "s_waitcnt vmcnt(6)\n"
      "v_mfma_f32_16x16x32_bf16 v[12:15], a[36:39], v[220:223], v[12:15]\n"
      "ds_read_b64_tr_b16 a[72:73], v243 offset:0\n"
      "v_mfma_f32_16x16x32_bf16 v[16:19], a[40:43], v[220:223], v[16:19]\n"
      "ds_read_b64_tr_b16 a[74:75], v243 offset:768\n"
      "v_mad_i32_i24 v238, v251, s89, v238\n"
      "s_add_u32 s56, s22, 0x0\n"
      "s_addc_u32 s57, s23, 0\n"
      "s_add_u32 m0, s18, 0x8f00\n"
      "v_mfma_f32_16x16x32_bf16 v[20:23], a[44:47], v[220:223], v[20:23]\n"
      "global_load_lds_dwordx4 v238, s[56:57] offset:256 \n"
      "s_waitcnt lgkmcnt(4)\n"
      "v_mfma_f32_16x16x32_bf16 v[24:27], a[24:27], v[224:227], v[24:27]\n"
      "ds_read_b64_tr_b16 a[76:77], v243 offset:8\n"
      "v_mfma_f32_16x16x32_bf16 v[28:31], a[28:31], v[224:227], v[28:31]\n"
      "ds_read_b64_tr_b16 a[78:79], v243 offset:776\n"
      "v_mad_i32_i24 v238, v251, s90, v238\n"
      "s_add_u32 s56, s22, 0x6000\n"
      "s_addc_u32 s57, s23, 0\n"
      "s_add_u32 m0, s18, 0x9f00\n"
      "v_mfma_f32_16x16x32_bf16 v[32:35], a[32:35], v[224:227], v[32:35]\n"
      "ds_read_b128 v[220:223], v233 offset:16384\n"
      "global_load_lds_dwordx4 v238, s[56:57] offset:256 \n"
      "v_mfma_f32_16x16x32_bf16 v[36:39], a[36:39], v[224:227], v[36:39]\n"
      "ds_read_b64_tr_b16 a[80:81], v243 offset:64\n"
      "v_mfma_f32_16x16x32_bf16 v[40:43], a[40:43], v[224:227], v[40:43]\n"
      "ds_read_b64_tr_b16 a[82:83], v243 offset:832\n"
      "v_mad_i32_i24 v238, v251, s91, v238\n"
      "s_add_u32 s56, s22, 0xc000\n"
      "s_addc_u32 s57, s23, 0\n"
      "s_add_u32 m0, s18, 0xaf00\n"
      "v_mfma_f32_16x16x32_bf16 v[44:47], a[44:47], v[224:227], v[44:47]\n"
      "global_load_lds_dwordx4 v238, s[56:57] offset:256 \n"
      "s_waitcnt lgkmcnt(8)\n"
      "v_mfma_f32_16x16x32_bf16 v[48:51], a[24:27], v[228:231], v[48:51]\n"
      "ds_read_b64_tr_b16 a[84:85], v243 offset:72\n"
      "v_mfma_f32_16x16x32_bf16 v[52:55], a[28:31], v[228:231], v[52:55]\n"
      "ds_read_b64_tr_b16 a[86:87], v243 offset:840\n"
      "v_mad_i32_i24 v238, v251, s92, v238\n"
      "s_add_u32 s56, s22, 0x12000\n"
      "s_addc_u32 s57, s23, 0\n"
      "s_add_u32 m0, s18, 0xbf00\n"
      "v_mfma_f32_16x16x32_bf16 v[56:59], a[32:35], v[228:231], v[56:59]\n"
      "ds_read_b128 v[224:227], v233 offset:20480\n"
      "global_load_lds_dwordx4 v238, s[56:57] offset:256 \n"
      "v_mfma_f32_16x16x32_bf16 v[60:63], a[36:39], v[228:231], v[60:63]\n"
      "ds_read_b64_tr_b16 a[88:89], v243 offset:128\n"
      "v_mfma_f32_16x16x32_bf16 v[64:67], a[40:43], v[228:231], v[64:67]\n"
      "ds_read_b64_tr_b16 a[90:91], v243 offset:896\n"
      "v_mad_i32_i24 v238, v251, s93, v238\n"
      "s_add_u32 s56, s22, 0x18000\n"
      "s_addc_u32 s57, s23, 0\n"
      "s_add_u32 m0, s18, 0xcf00\n"
      "v_mfma_f32_16x16x32_bf16 v[68:71], a[44:47], v[228:231], v[68:71]\n"
      "global_load_lds_dwordx4 v238, s[56:57] offset:256 \n"
      "s_waitcnt lgkmcnt(12)\n"
      "v_mfma_f32_16x16x32_bf16 v[72:75], a[24:27], v[216:219], v[72:75]\n"
      "ds_read_b64_tr_b16 a[92:93], v243 offset:136\n"
      "v_mfma_f32_16x16x32_bf16 v[76:79], a[28:31], v[216:219], v[76:79]\n"
      "ds_read_b64_tr_b16 a[94:95], v243 offset:904\n"
      "v_mad_i32_i24 v238, v251, s94, v238\n"
      "s_add_u32 s56, s22, 0x1e000\n"
      "s_addc_u32 s57, s23, 0\n"
      "s_add_u32 m0, s18, 0xdf00\n"
      "v_mfma_f32_16x16x32_bf16 v[80:83], a[32:35], v[216:219], v[80:83]\n"
      "ds_read_b128 v[228:231], v233 offset:24576\n"
      "global_load_lds_dwordx4 v238, s[56:57] offset:256 \n"
      "v_mfma_f32_16x16x32_bf16 v[84:87], a[36:39], v[216:219], v[84:87]\n"
      "v_mfma_f32_16x16x32_bf16 v[88:91], a[40:43], v[216:219], v[88:91]\n"
      "v_mad_i32_i24 v238, v251, s95, v238\n"
      "s_add_u32 s56, s22, 0x24000\n"
      "s_addc_u32 s57, s23, 0\n"
      "s_add_u32 m0, s18, 0xef00\n"
      "v_mfma_f32_16x16x32_bf16 v[92:95], a[44:47], v[216:219], v[92:95]\n"
      "global_load_lds_dwordx4 v238, s[56:57] offset:256 \n"
      "s_waitcnt lgkmcnt(10)\n"
      "v_mfma_f32_16x16x32_bf16 v[96:99], a[24:27], v[220:223], v[96:99]\n"
      "v_mfma_f32_16x16x32_bf16 v[100:103], a[28:31], v[220:223], v[100:103]\n"
      "v_mad_i32_i24 v238, v251, s96, v238\n"
      "s_add_u32 s56, s22, 0x2a000\n"
      "s_addc_u32 s57, s23, 0\n"
      "s_add_u32 m0, s18, 0xff00\n"
      "v_mfma_f32_16x16x32_bf16 v[104:107], a[32:35], v[220:223], v[104:107]\n"
      "ds_read_b128 v[216:219], v233 offset:28672\n"
      "global_load_lds_dwordx4 v238, s[56:57] offset:256 \n"
      "v_mfma_f32_16x16x32_bf16 v[108:111], a[36:39], v[220:223], v[108:111]\n"
      "v_mfma_f32_16x16x32_bf16 v[112:115], a[40:43], v[220:223], v[112:115]\n"
      "v_mad_i32_i24 v238, v251, s97, v238\n"
      "s_add_u32 s56, s22, 0x30000\n"
      "s_addc_u32 s57, s23, 0\n"
      "s_add_u32 m0, s18, 0x10f00\n"
      "v_mfma_f32_16x16x32_bf16 v[116:119], a[44:47], v[220:223], v[116:119]\n"
      "global_load_lds_dwordx4 v238, s[56:57] offset:256 \n"
      "s_waitcnt lgkmcnt(6)\n"
      "v_mfma_f32_16x16x32_bf16 v[120:123], a[24:27], v[224:227], v[120:123]\n"
      "v_mfma_f32_16x16x32_bf16 v[124:127], a[28:31], v[224:227], v[124:127]\n"
      "v_mfma_f32_16x16x32_bf16 v[128:131], a[32:35], v[224:227], v[128:131]\n"
      "ds_read_b128 v[220:223], v233 offset:32768\n"
      "v_add_u32 v233, 0x9000, v233\n"
      "s_add_u32 s78, s64, 0x1e000\n"
      "s_addc_u32 s79, s65, 0\n"
      "s_add_u32 m0, s74, 0xc000\n"
      "v_mfma_f32_16x16x32_bf16 v[132:135], a[36:39], v[224:227], v[132:135]\n"
      "global_load_lds_dwordx4 v240, s[78:79]\n"
      "v_mfma_f32_16x16x32_bf16 v[136:139], a[40:43], v[224:227], v[136:139]\n"
      "v_mfma_f32_16x16x32_bf16 v[140:143], a[44:47], v[224:227], v[140:143]\n"
      "s_waitcnt lgkmcnt(2)\n"
      "v_mfma_f32_16x16x32_bf16 v[144:147], a[24:27], v[228:231], v[144:147]\n"
      "s_waitcnt vmcnt(10)\n"
      "s_add_u32 m0, s74, 0xc400\n"
      "v_mfma_f32_16x16x32_bf16 v[148:151], a[28:31], v[228:231], v[148:151]\n"
      "ds_read_b64_tr_b16 a[96:97], v243 offset:24576\n"
      "global_load_lds_dwordx4 v241, s[78:79]\n"
      "v_mfma_f32_16x16x32_bf16 v[152:155], a[32:35], v[228:231], v[152:155]\n"
      "ds_read_b128 v[224:227], v234 offset:0\n"
      "v_mfma_f32_16x16x32_bf16 v[156:159], a[36:39], v[228:231], v[156:159]\n"
      "ds_read_b64_tr_b16 a[98:99], v243 offset:25344\n"
      "v_mfma_f32_16x16x32_bf16 v[160:163], a[40:43], v[228:231], v[160:163]\n"
      "ds_read_b64_tr_b16 a[100:101], v243 offset:24584\n"
      "s_add_u32 m0, s74, 0xc800\n"
      "v_mfma_f32_16x16x32_bf16 v[164:167], a[44:47], v[228:231], v[164:167]\n"
      "global_load_lds_dwordx4 v242, s[78:79]\n"
      "s_waitcnt lgkmcnt(5)\n"
      "v_mfma_f32_16x16x32_bf16 v[168:171], a[24:27], v[216:219], v[168:171]\n"
      "ds_read_b64_tr_b16 a[102:103], v243 offset:25352\n"
      "v_mfma_f32_16x16x32_bf16 v[172:175], a[28:31], v[216:219], v[172:175]\n"
      "ds_read_b64_tr_b16 a[104:105], v243 offset:24640\n"
      "v_mfma_f32_16x16x32_bf16 v[176:179], a[32:35], v[216:219], v[176:179]\n"
      "ds_read_b128 v[228:231], v234 offset:4096\n"
      "s_add_u32 s78, s64, 0x21000\n"
      "s_addc_u32 s79, s65, 0\n"
      "s_add_u32 m0, s74, 0xcc00\n"
      "v_mfma_f32_16x16x32_bf16 v[180:183], a[36:39], v[216:219], v[180:183]\n"
      "ds_read_b64_tr_b16 a[106:107], v243 offset:25408\n"
      "global_load_lds_dwordx4 v240, s[78:79]\n"
      "v_mfma_f32_16x16x32_bf16 v[184:187], a[40:43], v[216:219], v[184:187]\n"
      "ds_read_b64_tr_b16 a[108:109], v243 offset:24648\n"
      "v_mfma_f32_16x16x32_bf16 v[188:191], a[44:47], v[216:219], v[188:191]\n"
      "s_waitcnt lgkmcnt(9)\n"
      "v_mfma_f32_16x16x32_bf16 v[192:195], a[24:27], v[220:223], v[192:195]\n"
      "ds_read_b64_tr_b16 a[110:111], v243 offset:25416\n"
      "s_add_u32 m0, s74, 0xd000\n"
      "v_mfma_f32_16x16x32_bf16 v[196:199], a[28:31], v[220:223], v[196:199]\n"
      "ds_read_b64_tr_b16 a[112:113], v243 offset:24704\n"
      "global_load_lds_dwordx4 v241, s[78:79]\n"
      "v_mfma_f32_16x16x32_bf16 v[200:203], a[32:35], v[220:223], v[200:203]\n"
      "ds_read_b128 v[216:219], v234 offset:8192\n"
      "v_mfma_f32_16x16x32_bf16 v[204:207], a[36:39], v[220:223], v[204:207]\n"
      "ds_read_b64_tr_b16 a[114:115], v243 offset:25472\n"
      "v_mfma_f32_16x16x32_bf16 v[208:211], a[40:43], v[220:223], v[208:211]\n"
      "ds_read_b64_tr_b16 a[116:117], v243 offset:24712\n"
      "s_add_u32 m0, s74, 0xd400\n"
      "v_mfma_f32_16x16x32_bf16 v[212:215], a[44:47], v[220:223], v[212:215]\n"
      "global_load_lds_dwordx4 v242, s[78:79]\n"
      "s_waitcnt lgkmcnt(12)\n"
      "v_mfma_f32_16x16x32_bf16 v[0:3], a[48:51], v[224:227], v[0:3]\n"
      "ds_read_b64_tr_b16 a[118:119], v243 offset:25480\n"
      "v_mfma_f32_16x16x32_bf16 v[4:7], a[52:55], v[224:227], v[4:7]\n"
      "v_mfma_f32_16x16x32_bf16 v[8:11], a[56:59], v[224:227], v[8:11]\n"
      "ds_read_b128 v[220:223], v234 offset:12288\n"
      "s_add_u32 s78, s64, 0x24000\n"
      "s_addc_u32 s79, s65, 0\n"
      "s_add_u32 m0, s74, 0x0\n"
      "v_mfma_f32_16x16x32_bf16 v[12:15], a[60:63], v[224:227], v[12:15]\n"
      "global_load_lds_dwordx4 v240, s[78:79]\n"
      "v_mfma_f32_16x16x32_bf16 v[16:19], a[64:67], v[224:227], v[16:19]\n"
      "v_mfma_f32_16x16x32_bf16 v[20:23], a[68:71], v[224:227], v[20:23]\n"
      "s_waitcnt lgkmcnt(9)\n"
      "v_mfma_f32_16x16x32_bf16 v[24:27], a[48:51], v[228:231], v[24:27]\n"
      "s_add_u32 m0, s74, 0x400\n"
      "v_mfma_f32_16x16x32_bf16 v[28:31], a[52:55], v[228:231], v[28:31]\n"
      "global_load_lds_dwordx4 v241, s[78:79]\n"
      "v_mfma_f32_16x16x32_bf16 v[32:35], a[56:59], v[228:231], v[32:35]\n"
      "ds_read_b128 v[224:227], v234 offset:16384\n"
      "v_mfma_f32_16x16x32_bf16 v[36:39], a[60:63], v[228:231], v[36:39]\n"
      "v_mfma_f32_16x16x32_bf16 v[40:43], a[64:67], v[228:231], v[40:43]\n"
      "s_add_u32 m0, s74, 0x800\n"
      "v_mfma_f32_16x16x32_bf16 v[44:47], a[68:71], v[228:231], v[44:47]\n"
      "global_load_lds_dwordx4 v242, s[78:79]\n"
      "s_waitcnt lgkmcnt(5)\n"
      "v_mfma_f32_16x16x32_bf16 v[48:51], a[48:51], v[216:219], v[48:51]\n"
      "v_mfma_f32_16x16x32_bf16 v[52:55], a[52:55], v[216:219], v[52:55]\n"
      "v_mfma_f32_16x16x32_bf16 v[56:59], a[56:59], v[216:219], v[56:59]\n"
      "ds_read_b128 v[228:231], v234 offset:20480\n"
      "s_add_u32 s78, s64, 0x27000\n"
      "s_addc_u32 s79, s65, 0\n"
      "s_add_u32 m0, s74, 0xc00\n"
      "v_mfma_f32_16x16x32_bf16 v[60:63], a[60:63], v[216:219], v[60:63]\n"
      "global_load_lds_dwordx4 v240, s[78:79]\n"
      "v_mfma_f32_16x16x32_bf16 v[64:67], a[64:67], v[216:219], v[64:67]\n"
      "v_mfma_f32_16x16x32_bf16 v[68:71], a[68:71], v[216:219], v[68:71]\n"
      "s_waitcnt lgkmcnt(2)\n"
      "v_mfma_f32_16x16x32_bf16 v[72:75], a[48:51], v[220:223], v[72:75]\n"
      "s_add_u32 m0, s74, 0x1000\n"
      "v_mfma_f32_16x16x32_bf16 v[76:79], a[52:55], v[220:223], v[76:79]\n"
      "global_load_lds_dwordx4 v241, s[78:79]\n"
      "v_mfma_f32_16x16x32_bf16 v[80:83], a[56:59], v[220:223], v[80:83]\n"
      "ds_read_b128 v[216:219], v234 offset:24576\n"
      "v_mfma_f32_16x16x32_bf16 v[84:87], a[60:63], v[220:223], v[84:87]\n"
      "v_mfma_f32_16x16x32_bf16 v[88:91], a[64:67], v[220:223], v[88:91]\n"
      "s_add_u32 m0, s74, 0x1400\n"
      "v_mfma_f32_16x16x32_bf16 v[92:95], a[68:71], v[220:223], v[92:95]\n"
      "global_load_lds_dwordx4 v242, s[78:79]\n"
      "s_waitcnt lgkmcnt(2)\n"
      "v_mfma_f32_16x16x32_bf16 v[96:99], a[48:51], v[224:227], v[96:99]\n"
      "v_mfma_f32_16x16x32_bf16 v[100:103], a[52:55], v[224:227], v[100:103]\n"
      "v_mfma_f32_16x16x32_bf16 v[104:107], a[56:59], v[224:227], v[104:107]\n"
      "ds_read_b128 v[220:223], v234 offset:28672\n"
      "s_add_u32 s78, s64, 0x2a000\n"
      "s_addc_u32 s79, s65, 0\n"
      "s_add_u32 m0, s74, 0x6000\n"
      "v_mfma_f32_16x16x32_bf16 v[108:111], a[60:63], v[224:227], v[108:111]\n"
      "global_load_lds_dwordx4 v240, s[78:79]\n"
      "v_mfma_f32_16x16x32_bf16 v[112:115], a[64:67], v[224:227], v[112:115]\n"
      "v_mfma_f32_16x16x32_bf16 v[116:119], a[68:71], v[224:227], v[116:119]\n"
      "s_waitcnt lgkmcnt(2)\n"
      "v_mfma_f32_16x16x32_bf16 v[120:123], a[48:51], v[228:231], v[120:123]\n"
      "s_add_u32 m0, s74, 0x6400\n"
      "v_mfma_f32_16x16x32_bf16 v[124:127], a[52:55], v[228:231], v[124:127]\n"
      "global_load_lds_dwordx4 v241, s[78:79]\n"
      "v_mfma_f32_16x16x32_bf16 v[128:131], a[56:59], v[228:231], v[128:131]\n"
      "ds_read_b128 v[224:227], v234 offset:32768\n"
      "v_add_u32 v234, 0x9000, v234\n"
      "v_mfma_f32_16x16x32_bf16 v[132:135], a[60:63], v[228:231], v[132:135]\n"
      "v_mfma_f32_16x16x32_bf16 v[136:139], a[64:67], v[228:231], v[136:139]\n"
      "s_waitcnt vmcnt(8)\n"
      "s_add_u32 m0, s74, 0x6800\n"
      "v_mfma_f32_16x16x32_bf16 v[140:143], a[68:71], v[228:231], v[140:143]\n"
      "global_load_lds_dwordx4 v242, s[78:79]\n"
      "s_waitcnt lgkmcnt(2)\n"
      "v_mfma_f32_16x16x32_bf16 v[144:147], a[48:51], v[216:219], v[144:147]\n"
      "ds_read_b64_tr_b16 a[120:121], v243 offset:49152\n"
      "v_mfma_f32_16x16x32_bf16 v[148:151], a[52:55], v[216:219], v[148:151]\n"
      "ds_read_b64_tr_b16 a[122:123], v243 offset:49920\n"
      "v_mfma_f32_16x16x32_bf16 v[152:155], a[56:59], v[216:219], v[152:155]\n"
      "ds_read_b128 v[228:231], v235 offset:0\n"
      "s_add_u32 s78, s64, 0x2d000\n"
      "s_addc_u32 s79, s65, 0\n"
      "s_add_u32 m0, s74, 0x6c00\n"
      "v_mfma_f32_16x16x32_bf16 v[156:159], a[60:63], v[216:219], v[156:159]\n"
      "ds_read_b64_tr_b16 a[124:125], v243 offset:49160\n"
      "global_load_lds_dwordx4 v240, s[78:79]\n"
      "v_mfma_f32_16x16x32_bf16 v[160:163], a[64:67], v[216:219], v[160:163]\n"
      "ds_read_b64_tr_b16 a[126:127], v243 offset:49928\n"
      "v_mfma_f32_16x16x32_bf16 v[164:167], a[68:71], v[216:219], v[164:167]\n"
      "s_waitcnt lgkmcnt(6)\n"
      "v_mfma_f32_16x16x32_bf16 v[168:171], a[48:51], v[220:223], v[168:171]\n"
      "ds_read_b64_tr_b16 a[128:129], v243 offset:49216\n"
      "s_add_u32 m0, s74, 0x7000\n"
      "v_mfma_f32_16x16x32_bf16 v[172:175], a[52:55], v[220:223], v[172:175]\n"
      "ds_read_b64_tr_b16 a[130:131], v243 offset:49984\n"
      "global_load_lds_dwordx4 v241, s[78:79]\n"
      "v_mfma_f32_16x16x32_bf16 v[176:179], a[56:59], v[220:223], v[176:179]\n"
      "ds_read_b128 v[216:219], v235 offset:4096\n"
      "v_mfma_f32_16x16x32_bf16 v[180:183], a[60:63], v[220:223], v[180:183]\n"
      "ds_read_b64_tr_b16 a[132:133], v243 offset:49224\n"
      "v_mfma_f32_16x16x32_bf16 v[184:187], a[64:67], v[220:223], v[184:187]\n"
      "ds_read_b64_tr_b16 a[134:135], v243 offset:49992\n"
      "s_add_u32 m0, s74, 0x7400\n"
      "v_mfma_f32_16x16x32_bf16 v[188:191], a[68:71], v[220:223], v[188:191]\n"
      "global_load_lds_dwordx4 v242, s[78:79]\n"
      "s_waitcnt lgkmcnt(10)\n"
      "v_mfma_f32_16x16x32_bf16 v[192:195], a[48:51], v[224:227], v[192:195]\n"
      "ds_read_b64_tr_b16 a[136:137], v243 offset:49280\n"
      "v_mfma_f32_16x16x32_bf16 v[196:199], a[52:55], v[224:227], v[196:199]\n"
      "ds_read_b64_tr_b16 a[138:139], v243 offset:50048\n"
      "v_mfma_f32_16x16x32_bf16 v[200:203], a[56:59], v[224:227], v[200:203]\n"
      "ds_read_b128 v[220:223], v235 offset:8192\n"
      "global_load_dwordx4 v[244:247], v248, s[28:29] sc1\n"
      "v_mfma_f32_16x16x32_bf16 v[204:207], a[60:63], v[224:227], v[204:207]\n"
      "ds_read_b64_tr_b16 a[140:141], v243 offset:49288\n"
      "v_mfma_f32_16x16x32_bf16 v[208:211], a[64:67], v[224:227], v[208:211]\n"
      "ds_read_b64_tr_b16 a[142:143], v243 offset:50056\n"
      "v_mfma_f32_16x16x32_bf16 v[212:215], a[68:71], v[224:227], v[212:215]\n"
      "s_waitcnt lgkmcnt(12)\n"
      "v_mfma_f32_16x16x32_bf16 v[0:3], a[72:75], v[228:231], v[0:3]\n"
      "v_mfma_f32_16x16x32_bf16 v[4:7], a[76:79], v[228:231], v[4:7]\n"
      "v_mfma_f32_16x16x32_bf16 v[8:11], a[80:83], v[228:231], v[8:11]\n"
      "ds_read_b128 v[224:227], v235 offset:12288\n"
      "v_mfma_f32_16x16x32_bf16 v[12:15], a[84:87], v[228:231], v[12:15]\n"
      "v_mfma_f32_16x16x32_bf16 v[16:19], a[88:91], v[228:231], v[16:19]\n"
      "s_waitcnt vmcnt(7)\n"
      "v_mfma_f32_16x16x32_bf16 v[20:23], a[92:95], v[228:231], v[20:23]\n"
      "s_waitcnt lgkmcnt(8)\n"
      "v_mfma_f32_16x16x32_bf16 v[24:27], a[72:75], v[216:219], v[24:27]\n"
      "ds_read_b64_tr_b16 a[144:145], v243 offset:0\n"
      "v_mfma_f32_16x16x32_bf16 v[28:31], a[76:79], v[216:219], v[28:31]\n"
      "ds_read_b64_tr_b16 a[146:147], v243 offset:768\n"
      "v_mfma_f32_16x16x32_bf16 v[32:35], a[80:83], v[216:219], v[32:35]\n"
      "ds_read_b128 v[228:231], v235 offset:16384\n"
      "s_waitcnt lgkmcnt(4)\n"
      "s_add_u32 s78, s64, 0x30000\n"
      "s_addc_u32 s79, s65, 0\n"
      "s_add_u32 m0, s74, 0xc000\n"
      "v_mfma_f32_16x16x32_bf16 v[36:39], a[84:87], v[216:219], v[36:39]\n"
      "ds_read_b64_tr_b16 a[148:149], v243 offset:8\n"
      "global_load_lds_dwordx4 v240, s[78:79]\n"
      "v_mfma_f32_16x16x32_bf16 v[40:43], a[88:91], v[216:219], v[40:43]\n"
      "ds_read_b64_tr_b16 a[150:151], v243 offset:776\n"
      "v_mfma_f32_16x16x32_bf16 v[44:47], a[92:95], v[216:219], v[44:47]\n"
      "v_mfma_f32_16x16x32_bf16 v[48:51], a[72:75], v[220:223], v[48:51]\n"
      "ds_read_b64_tr_b16 a[152:153], v243 offset:64\n"
      "s_add_u32 m0, s74, 0xc400\n"
      "v_mfma_f32_16x16x32_bf16 v[52:55], a[76:79], v[220:223], v[52:55]\n"
      "ds_read_b64_tr_b16 a[154:155], v243 offset:832\n"
      "global_load_lds_dwordx4 v241, s[78:79]\n"
      "v_mfma_f32_16x16x32_bf16 v[56:59], a[80:83], v[220:223], v[56:59]\n"
      "ds_read_b128 v[216:219], v235 offset:20480\n"
      "v_mfma_f32_16x16x32_bf16 v[60:63], a[84:87], v[220:223], v[60:63]\n"
      "ds_read_b64_tr_b16 a[156:157], v243 offset:72\n"
      "v_mfma_f32_16x16x32_bf16 v[64:67], a[88:91], v[220:223], v[64:67]\n"
      "ds_read_b64_tr_b16 a[158:159], v243 offset:840\n"
      "s_add_u32 m0, s74, 0xc800\n"
      "v_mfma_f32_16x16x32_bf16 v[68:71], a[92:95], v[220:223], v[68:71]\n"
      "global_load_lds_dwordx4 v242, s[78:79]\n"
      "s_waitcnt lgkmcnt(10)\n"
      "v_mfma_f32_16x16x32_bf16 v[72:75], a[72:75], v[224:227], v[72:75]\n"
      "ds_read_b64_tr_b16 a[160:161], v243 offset:128\n"
      "v_mfma_f32_16x16x32_bf16 v[76:79], a[76:79], v[224:227], v[76:79]\n"
      "ds_read_b64_tr_b16 a[162:163], v243 offset:896\n"
      "v_mfma_f32_16x16x32_bf16 v[80:83], a[80:83], v[224:227], v[80:83]\n"
      "ds_read_b128 v[220:223], v235 offset:24576\n"
      "s_add_u32 s78, s64, 0x33000\n"
      "s_addc_u32 s79, s65, 0\n"
      "s_add_u32 m0, s74, 0xcc00\n"
      "v_mfma_f32_16x16x32_bf16 v[84:87], a[84:87], v[224:227], v[84:87]\n"
      "ds_read_b64_tr_b16 a[164:165], v243 offset:136\n"
      "global_load_lds_dwordx4 v240, s[78:79]\n"
      "v_mfma_f32_16x16x32_bf16 v[88:91], a[88:91], v[224:227], v[88:91]\n"
      "ds_read_b64_tr_b16 a[166:167], v243 offset:904\n"
      "v_mfma_f32_16x16x32_bf16 v[92:95], a[92:95], v[224:227], v[92:95]\n"
      "s_waitcnt lgkmcnt(12)\n"
      "v_mfma_f32_16x16x32_bf16 v[96:99], a[72:75], v[228:231], v[96:99]\n"
      "s_add_u32 m0, s74, 0xd000\n"
      "v_mfma_f32_16x16x32_bf16 v[100:103], a[76:79], v[228:231], v[100:103]\n"
      "global_load_lds_dwordx4 v241, s[78:79]\n"
      "v_mfma_f32_16x16x32_bf16 v[104:107], a[80:83], v[228:231], v[104:107]\n"
      "ds_read_b128 v[224:227], v235 offset:28672\n"
      "v_mfma_f32_16x16x32_bf16 v[108:111], a[84:87], v[228:231], v[108:111]\n"
      "v_mfma_f32_16x16x32_bf16 v[112:115], a[88:91], v[228:231], v[112:115]\n"
      "s_waitcnt vmcnt(6)\n"
      "s_add_u32 m0, s74, 0xd400\n"
      "v_mfma_f32_16x16x32_bf16 v[116:119], a[92:95], v[228:231], v[116:119]\n"
      "global_load_lds_dwordx4 v242, s[78:79]\n"
      "s_waitcnt lgkmcnt(8)\n"
      "v_mfma_f32_16x16x32_bf16 v[120:123], a[72:75], v[216:219], v[120:123]\n"
      "ds_read_b64_tr_b16 a[168:169], v243 offset:24576\n"
      "v_mfma_f32_16x16x32_bf16 v[124:127], a[76:79], v[216:219], v[124:127]\n"
      "ds_read_b64_tr_b16 a[170:171], v243 offset:25344\n"
      "v_mfma_f32_16x16x32_bf16 v[128:131], a[80:83], v[216:219], v[128:131]\n"
      "ds_read_b128 v[228:231], v235 offset:32768\n"
      "v_add_u32 v235, 0x9000, v235\n"
      "s_waitcnt lgkmcnt(4)\n"
      "s_add_u32 s78, s64, 0x36000\n"
      "s_addc_u32 s79, s65, 0\n"
      "s_add_u32 m0, s74, 0x0\n"
      "v_mfma_f32_16x16x32_bf16 v[132:135], a[84:87], v[216:219], v[132:135]\n"
      "ds_read_b64_tr_b16 a[172:173], v243 offset:24584\n"
      "global_load_lds_dwordx4 v240, s[78:79]\n"
      "v_mfma_f32_16x16x32_bf16 v[136:139], a[88:91], v[216:219], v[136:139]\n"
      "ds_read_b64_tr_b16 a[174:175], v243 offset:25352\n"
      "v_mfma_f32_16x16x32_bf16 v[140:143], a[92:95], v[216:219], v[140:143]\n"
      "v_mfma_f32_16x16x32_bf16 v[144:147], a[72:75], v[220:223], v[144:147]\n"
      "ds_read_b64_tr_b16 a[176:177], v243 offset:24640\n"
      "s_waitcnt vmcnt(7)\n"
      "v_cmp_ne_u64 vcc, s[80:81], v[244:245]\n"
      "v_cmp_ne_u64 s[56:57], s[80:81], v[246:247]\n"
      "s_nop 3\n"
      "s_or_b64 vcc, vcc, s[56:57]\n"
      "s_cbranch_scc0 LP1_0\n"
      "s_mov_b32 s82, 0x40000\n"
      "LP0_0:\n"
      "s_sleep 2\n"
      "global_load_dwordx4 v[244:247], v248, s[28:29] sc1\n"
      "s_sub_u32 s82, s82, 1\n"
      "s_cbranch_scc1 LP2_0\n"
      "s_waitcnt vmcnt(0)\n"
      "v_cmp_ne_u64 vcc, s[80:81], v[244:245]\n"
      "v_cmp_ne_u64 s[56:57], s[80:81], v[246:247]\n"
      "s_nop 3\n"
      "s_or_b64 vcc, vcc, s[56:57]\n"
      "s_cbranch_scc1 LP0_0\n"
      "LP2_0:\n"
      "s_waitcnt vmcnt(0)\n"
      "LP1_0:\n"
      "s_nop 0\n"
      "v_mfma_f32_16x16x32_bf16 v[148:151], a[76:79], v[220:223], v[148:151]\n"
      "s_barrier\n"
      "s_add_u32 m0, s74, 0x400\n"
      "v_mfma_f32_16x16x32_bf16 v[152:155], a[80:83], v[220:223], v[152:155]\n"
      "ds_read_b128 v[216:219], v232 offset:0\n"
      "global_load_lds_dwordx4 v241, s[78:79]\n"
      "v_mfma_f32_16x16x32_bf16 v[156:159], a[84:87], v[220:223], v[156:159]\n"
      "ds_read_b64_tr_b16 a[178:179], v243 offset:25408\n"
      "v_mfma_f32_16x16x32_bf16 v[160:163], a[88:91], v[220:223], v[160:163]\n"
      "ds_read_b64_tr_b16 a[180:181], v243 offset:24648\n"
      "v_mfma_f32_16x16x32_bf16 v[164:167], a[92:95], v[220:223], v[164:167]\n"
      "s_add_u32 m0, s74, 0x800\n"
      "s_waitcnt lgkmcnt(9)\n"
      "v_mfma_f32_16x16x32_bf16 v[168:171], a[72:75], v[224:227], v[168:171]\n"
      "ds_read_b64_tr_b16 a[182:183], v243 offset:25416\n"
      "global_load_lds_dwordx4 v242, s[78:79]\n"
      "v_mfma_f32_16x16x32_bf16 v[172:175], a[76:79], v[224:227], v[172:175]\n"
      "ds_read_b64_tr_b16 a[184:185], v243 offset:24704\n"
      "v_mfma_f32_16x16x32_bf16 v[176:179], a[80:83], v[224:227], v[176:179]\n"
      "ds_read_b128 v[220:223], v232 offset:4096\n"
      "v_mfma_f32_16x16x32_bf16 v[180:183], a[84:87], v[224:227], v[180:183]\n"
      "ds_read_b64_tr_b16 a[186:187], v243 offset:25472\n"
      "s_add_u32 s78, s64, 0x39000\n"
      "s_addc_u32 s79, s65, 0\n"
      "s_add_u32 m0, s74, 0xc00\n"
      "v_mfma_f32_16x16x32_bf16 v[184:187], a[88:91], v[224:227], v[184:187]\n"
      "ds_read_b64_tr_b16 a[188:189], v243 offset:24712\n"
      "global_load_lds_dwordx4 v240, s[78:79]\n"
      "v_mfma_f32_16x16x32_bf16 v[188:191], a[92:95], v[224:227], v[188:191]\n"
      "s_waitcnt lgkmcnt(11)\n"
      "v_mfma_f32_16x16x32_bf16 v[192:195], a[72:75], v[228:231], v[192:195]\n"
      "ds_read_b64_tr_b16 a[190:191], v243 offset:25480\n"
      "v_mfma_f32_16x16x32_bf16 v[196:199], a[76:79], v[228:231], v[196:199]\n"
      "s_add_u32 m0, s74, 0x1000\n"
      "v_mfma_f32_16x16x32_bf16 v[200:203], a[80:83], v[228:231], v[200:203]\n"
      "ds_read_b128 v[224:227], v232 offset:8192\n"
      "global_load_lds_dwordx4 v241, s[78:79]\n"
      "v_mfma_f32_16x16x32_bf16 v[204:207], a[84:87], v[228:231], v[204:207]\n"
      "v_mfma_f32_16x16x32_bf16 v[208:211], a[88:91], v[228:231], v[208:211]\n"
      "v_mfma_f32_16x16x32_bf16 v[212:215], a[92:95], v[228:231], v[212:215]\n"
      "s_add_u32 m0, s74, 0x1400\n"
      "s_waitcnt lgkmcnt(9)\n"
      "v_mfma_f32_16x16x32_bf16 v[0:3], a[96:99], v[216:219], v[0:3]\n"
      "global_load_lds_dwordx4 v242, s[78:79]\n"
      "v_mfma_f32_16x16x32_bf16 v[4:7], a[100:103], v[216:219], v[4:7]\n"
      "s_barrier\n"
      "v_mfma_f32_16x16x32_bf16 v[8:11], a[104:107], v[216:219], v[8:11]\n"
      "ds_read_b128 v[228:231], v232 offset:12288\n"
      "v_mfma_f32_16x16x32_bf16 v[12:15], a[108:111], v[216:219], v[12:15]\n"
      "s_add_u32 s58, s24, 0x0\n"
      "s_addc_u32 s59, s25, 0\n"
      "global_load_dwordx4 a[240:243], v239, s[58:59] offset:0 sc1\n"
      "v_mfma_f32_16x16x32_bf16 v[16:19], a[112:115], v[216:219], v[16:19]\n"
      "v_mfma_f32_16x16x32_bf16 v[20:23], a[116:119], v[216:219], v[20:23]\n"
      "s_waitcnt lgkmcnt(5)\n"
      "v_mfma_f32_16x16x32_bf16 v[24:27], a[96:99], v[220:223], v[24:27]\n"
      "v_mfma_f32_16x16x32_bf16 v[28:31], a[100:103], v[220:223], v[28:31]\n"
      "s_waitcnt lgkmcnt(2)\n"
      "s_add_u32 s78, s64, 0x3c000\n"
      "s_addc_u32 s79, s65, 0\n"
      "s_add_u32 m0, s74, 0x6000\n"
      "v_mfma_f32_16x16x32_bf16 v[32:35], a[104:107], v[220:223], v[32:35]\n"
      "ds_read_b128 v[216:219], v232 offset:16384\n"
      "LPD1:\n"
      "s_cbranch_execz LPD8\n"
      "global_load_lds_dwordx4 v240, s[78:79]\n"
      "v_mfma_f32_16x16x32_bf16 v[36:39], a[108:111], v[220:223], v[36:39]\n"
      "v_mfma_f32_16x16x32_bf16 v[40:43], a[112:115], v[220:223], v[40:43]\n"
      "s_waitcnt vmcnt(8)\n"
      "v_mfma_f32_16x16x32_bf16 v[44:47], a[116:119], v[220:223], v[44:47]\n"
      "s_add_u32 m0, s74, 0x6400\n"
      "s_waitcnt lgkmcnt(2)\n"
      "v_mfma_f32_16x16x32_bf16 v[48:51], a[96:99], v[224:227], v[48:51]\n"
      "ds_read_b64_tr_b16 a[192:193], v243 offset:49152\n"
      "global_load_lds_dwordx4 v241, s[78:79]\n"
      "v_mfma_f32_16x16x32_bf16 v[52:55], a[100:103], v[224:227], v[52:55]\n"
      "ds_read_b64_tr_b16 a[194:195], v243 offset:49920\n"
      "v_mfma_f32_16x16x32_bf16 v[56:59], a[104:107], v[224:227], v[56:59]\n"
      "ds_read_b128 v[220:223], v232 offset:20480\n"
      "v_mfma_f32_16x16x32_bf16 v[60:63], a[108:111], v[224:227], v[60:63]\n"
      "ds_read_b64_tr_b16 a[196:197], v243 offset:49160\n"
      "s_add_u32 m0, s74, 0x6800\n"
      "v_mfma_f32_16x16x32_bf16 v[64:67], a[112:115], v[224:227], v[64:67]\n"
      "ds_read_b64_tr_b16 a[198:199], v243 offset:49928\n"
      "global_load_lds_dwordx4 v242, s[78:79]\n"
      "v_mfma_f32_16x16x32_bf16 v[68:71], a[116:119], v[224:227], v[68:71]\n"
      "s_waitcnt lgkmcnt(6)\n"
      "v_mfma_f32_16x16x32_bf16 v[72:75], a[96:99], v[228:231], v[72:75]\n"
      "ds_read_b64_tr_b16 a[200:201], v243 offset:49216\n"
      "v_mfma_f32_16x16x32_bf16 v[76:79], a[100:103], v[228:231], v[76:79]\n"
      "ds_read_b64_tr_b16 a[202:203], v243 offset:49984\n"
      "s_add_u32 s78, s64, 0x3f000\n"
      "s_addc_u32 s79, s65, 0\n"
      "s_add_u32 m0, s74, 0x6c00\n"
      "v_mfma_f32_16x16x32_bf16 v[80:83], a[104:107], v[228:231], v[80:83]\n"
      "ds_read_b128 v[224:227], v232 offset:24576\n"
      "global_load_lds_dwordx4 v240, s[78:79]\n"
      "v_mfma_f32_16x16x32_bf16 v[84:87], a[108:111], v[228:231], v[84:87]\n"
      "ds_read_b64_tr_b16 a[204:205], v243 offset:49224\n"
      "v_mfma_f32_16x16x32_bf16 v[88:91], a[112:115], v[228:231], v[88:91]\n"
      "ds_read_b64_tr_b16 a[206:207], v243 offset:49992\n"
      "v_mfma_f32_16x16x32_bf16 v[92:95], a[116:119], v[228:231], v[92:95]\n"
      "s_add_u32 m0, s74, 0x7000\n"
      "s_waitcnt lgkmcnt(10)\n"
      "v_mfma_f32_16x16x32_bf16 v[96:99], a[96:99], v[216:219], v[96:99]\n"
      "ds_read_b64_tr_b16 a[208:209], v243 offset:49280\n"
      "global_load_lds_dwordx4 v241, s[78:79]\n"
      "v_mfma_f32_16x16x32_bf16 v[100:103], a[100:103], v[216:219], v[100:103]\n"
      "ds_read_b64_tr_b16 a[210:211], v243 offset:50048\n"
      "v_mfma_f32_16x16x32_bf16 v[104:107], a[104:107], v[216:219], v[104:107]\n"
      "ds_read_b128 v[228:231], v232 offset:28672\n"
      "v_mfma_f32_16x16x32_bf16 v[108:111], a[108:111], v[216:219], v[108:111]\n"
      "ds_read_b64_tr_b16 a[212:213], v243 offset:49288\n"
      "s_add_u32 m0, s74, 0x7400\n"
      "v_mfma_f32_16x16x32_bf16 v[112:115], a[112:115], v[216:219], v[112:115]\n"
      "ds_read_b64_tr_b16 a[214:215], v243 offset:50056\n"
      "global_load_lds_dwordx4 v242, s[78:79]\n"
      "v_mfma_f32_16x16x32_bf16 v[116:119], a[116:119], v[216:219], v[116:119]\n"
      "s_waitcnt lgkmcnt(12)\n"
      "v_mfma_f32_16x16x32_bf16 v[120:123], a[96:99], v[220:223], v[120:123]\n"
      "v_mfma_f32_16x16x32_bf16 v[124:127], a[100:103], v[220:223], v[124:127]\n"
      "global_load_dwordx4 a[244:247], v239, s[58:59] offset:1024 sc1\n"
      "v_mfma_f32_16x16x32_bf16 v[128:131], a[104:107], v[220:223], v[128:131]\n"
      "ds_read_b128 v[216:219], v232 offset:32768\n"
      "v_add_u32 v232, 0xffff7000, v232\n"
      "v_mfma_f32_16x16x32_bf16 v[132:135], a[108:111], v[220:223], v[132:135]\n"
      "v_mfma_f32_16x16x32_bf16 v[136:139], a[112:115], v[220:223], v[136:139]\n"
      "v_mfma_f32_16x16x32_bf16 v[140:143], a[116:119], v[220:223], v[140:143]\n"
      "global_load_dwordx4 a[248:251], v239, s[58:59] offset:2048 sc1\n"
      "s_waitcnt lgkmcnt(8)\n"
      "v_mfma_f32_16x16x32_bf16 v[144:147], a[96:99], v[224:227], v[144:147]\n"
      "s_waitcnt vmcnt(9)\n"
      "v_mfma_f32_16x16x32_bf16 v[148:151], a[100:103], v[224:227], v[148:151]\n"
      "ds_read_b64_tr_b16 a[216:217], v243 offset:0\n"
      "v_mfma_f32_16x16x32_bf16 v[152:155], a[104:107], v[224:227], v[152:155]\n"
      "ds_read_b128 v[220:223], v233 offset:0\n"
      "v_mfma_f32_16x16x32_bf16 v[156:159], a[108:111], v[224:227], v[156:159]\n"
      "ds_read_b64_tr_b16 a[218:219], v243 offset:768\n"
      "s_waitcnt lgkmcnt(4)\n"
      "s_add_u32 s78, s64, 0x42000\n"
      "s_addc_u32 s79, s65, 0\n"
      "s_add_u32 m0, s74, 0xc000\n"
      "v_mfma_f32_16x16x32_bf16 v[160:163], a[112:115], v[224:227], v[160:163]\n"
      "ds_read_b64_tr_b16 a[220:221], v243 offset:8\n"
      "global_load_lds_dwordx4 v240, s[78:79]\n"
      "v_mfma_f32_16x16x32_bf16 v[164:167], a[116:119], v[224:227], v[164:167]\n"
      "v_mfma_f32_16x16x32_bf16 v[168:171], a[96:99], v[228:231], v[168:171]\n"
      "ds_read_b64_tr_b16 a[222:223], v243 offset:776\n"
      "v_mfma_f32_16x16x32_bf16 v[172:175], a[100:103], v[228:231], v[172:175]\n"
      "ds_read_b64_tr_b16 a[224:225], v243 offset:64\n"
      "s_add_u32 m0, s74, 0xc400\n"
      "v_mfma_f32_16x16x32_bf16 v[176:179], a[104:107], v[228:231], v[176:179]\n"
      "ds_read_b128 v[224:227], v233 offset:4096\n"
      "global_load_lds_dwordx4 v241, s[78:79]\n"
      "v_mfma_f32_16x16x32_bf16 v[180:183], a[108:111], v[228:231], v[180:183]\n"
      "ds_read_b64_tr_b16 a[226:227], v243 offset:832\n"
      "v_mfma_f32_16x16x32_bf16 v[184:187], a[112:115], v[228:231], v[184:187]\n"
      "ds_read_b64_tr_b16 a[228:229], v243 offset:72\n"
      "v_mfma_f32_16x16x32_bf16 v[188:191], a[116:119], v[228:231], v[188:191]\n"
      "s_add_u32 m0, s74, 0xc800\n"
      "s_waitcnt lgkmcnt(9)\n"
      "v_mfma_f32_16x16x32_bf16 v[192:195], a[96:99], v[216:219], v[192:195]\n"
      "ds_read_b64_tr_b16 a[230:231], v243 offset:840\n"
      "global_load_lds_dwordx4 v242, s[78:79]\n"
      "v_mfma_f32_16x16x32_bf16 v[196:199], a[100:103], v[216:219], v[196:199]\n"
      "ds_read_b64_tr_b16 a[232:233], v243 offset:128\n"
      "v_mfma_f32_16x16x32_bf16 v[200:203], a[104:107], v[216:219], v[200:203]\n"
      "ds_read_b128 v[228:231], v233 offset:8192\n"
      "v_mfma_f32_16x16x32_bf16 v[204:207], a[108:111], v[216:219], v[204:207]\n"
      "ds_read_b64_tr_b16 a[234:235], v243 offset:896\n"
      "s_add_u32 s78, s64, 0x45000\n"
      "s_addc_u32 s79, s65, 0\n"
      "s_add_u32 m0, s74, 0xcc00\n"
      "v_mfma_f32_16x16x32_bf16 v[208:211], a[112:115], v[216:219], v[208:211]\n"
      "ds_read_b64_tr_b16 a[236:237], v243 offset:136\n"
      "global_load_lds_dwordx4 v240, s[78:79]\n"
      "v_mfma_f32_16x16x32_bf16 v[212:215], a[116:119], v[216:219], v[212:215]\n"
      "s_waitcnt lgkmcnt(12)\n"
      "v_mfma_f32_16x16x32_bf16 v[0:3], a[120:123], v[220:223], v[0:3]\n"
      "ds_read_b64_tr_b16 a[238:239], v243 offset:904\n"
      "v_mfma_f32_16x16x32_bf16 v[4:7], a[124:127], v[220:223], v[4:7]\n"
      "s_add_u32 m0, s74, 0xd000\n"
      "v_mfma_f32_16x16x32_bf16 v[8:11], a[128:131], v[220:223], v[8:11]\n"
      "ds_read_b128 v[216:219], v233 offset:12288\n"
      "global_load_lds_dwordx4 v241, s[78:79]\n"
      "v_mfma_f32_16x16x32_bf16 v[12:15], a[132:135], v[220:223], v[12:15]\n"
      "v_mfma_f32_16x16x32_bf16 v[16:19], a[136:139], v[220:223], v[16:19]\n"
      "v_mfma_f32_16x16x32_bf16 v[20:23], a[140:143], v[220:223], v[20:23]\n"
      "s_add_u32 m0, s74, 0xd400\n"
      "s_waitcnt lgkmcnt(9)\n"
      "v_mfma_f32_16x16x32_bf16 v[24:27], a[120:123], v[224:227], v[24:27]\n"
      "global_load_lds_dwordx4 v242, s[78:79]\n"
      "v_mfma_f32_16x16x32_bf16 v[28:31], a[124:127], v[224:227], v[28:31]\n"
      "v_mfma_f32_16x16x32_bf16 v[32:35], a[128:131], v[224:227], v[32:35]\n"
      "ds_read_b128 v[220:223], v233 offset:16384\n"
      "v_mfma_f32_16x16x32_bf16 v[36:39], a[132:135], v[224:227], v[36:39]\n"
      "s_waitcnt vmcnt(8)\n"
      "v_mad_i32_i24 v238, v251, s89, v238\n"
      "s_add_u32 s56, s22, 0x0\n"
      "s_addc_u32 s57, s23, 0\n"
      "s_add_u32 m0, s18, 0xfffffe00\n"
      "v_mfma_f32_16x16x32_bf16 v[40:43], a[136:139], v[224:227], v[40:43]\n"
      "ds_read_b64_tr_b16 a[0:1], v243 offset:24576\n"
      "global_load_lds_dwordx4 v238, s[56:57] offset:512 \n"
      "v_mfma_f32_16x16x32_bf16 v[44:47], a[140:143], v[224:227], v[44:47]\n"
      "s_waitcnt lgkmcnt(6)\n"
      "v_mfma_f32_16x16x32_bf16 v[48:51], a[120:123], v[228:231], v[48:51]\n"
      "ds_read_b64_tr_b16 a[2:3], v243 offset:25344\n"
      "v_mad_i32_i24 v238, v251, s90, v238\n"
      "s_add_u32 s56, s22, 0x6000\n"
      "s_addc_u32 s57, s23, 0\n"
      "s_add_u32 m0, s18, 0xe00\n"
      "v_mfma_f32_16x16x32_bf16 v[52:55], a[124:127], v[228:231], v[52:55]\n"
      "ds_read_b64_tr_b16 a[4:5], v243 offset:24584\n"
      "global_load_lds_dwordx4 v238, s[56:57] offset:512 \n"
      "v_mfma_f32_16x16x32_bf16 v[56:59], a[128:131], v[228:231], v[56:59]\n"
      "ds_read_b128 v[224:227], v233 offset:20480\n"
      "v_mfma_f32_16x16x32_bf16 v[60:63], a[132:135], v[228:231], v[60:63]\n"
      "ds_read_b64_tr_b16 a[6:7], v243 offset:25352\n"
      "v_mad_i32_i24 v238, v251, s91, v238\n"
      "s_add_u32 s56, s22, 0xc000\n"
      "s_addc_u32 s57, s23, 0\n"
      "s_add_u32 m0, s18, 0x1e00\n"
      "v_mfma_f32_16x16x32_bf16 v[64:67], a[136:139], v[228:231], v[64:67]\n"
      "ds_read_b64_tr_b16 a[8:9], v243 offset:24640\n"
      "global_load_lds_dwordx4 v238, s[56:57] offset:512 \n"
      "v_mfma_f32_16x16x32_bf16 v[68:71], a[140:143], v[228:231], v[68:71]\n"
      "s_waitcnt lgkmcnt(7)\n"
      "v_mfma_f32_16x16x32_bf16 v[72:75], a[120:123], v[216:219], v[72:75]\n"
      "ds_read_b64_tr_b16 a[10:11], v243 offset:25408\n"
      "v_mad_i32_i24 v238, v251, s92, v238\n"
      "s_add_u32 s56, s22, 0x12000\n"
      "s_addc_u32 s57, s23, 0\n"
      "s_add_u32 m0, s18, 0x2e00\n"
      "v_mfma_f32_16x16x32_bf16 v[76:79], a[124:127], v[216:219], v[76:79]\n"
      "ds_read_b64_tr_b16 a[12:13], v243 offset:24648\n"
      "global_load_lds_dwordx4 v238, s[56:57] offset:512 \n"
      "v_mfma_f32_16x16x32_bf16 v[80:83], a[128:131], v[216:219], v[80:83]\n"
      "ds_read_b128 v[228:231], v233 offset:24576\n"
      "v_mfma_f32_16x16x32_bf16 v[84:87], a[132:135], v[216:219], v[84:87]\n"
      "ds_read_b64_tr_b16 a[14:15], v243 offset:25416\n"
      "v_mad_i32_i24 v238, v251, s93, v238\n"
      "s_add_u32 s56, s22, 0x18000\n"
      "s_addc_u32 s57, s23, 0\n"
      "s_add_u32 m0, s18, 0x3e00\n"
      "v_mfma_f32_16x16x32_bf16 v[88:91], a[136:139], v[216:219], v[88:91]\n"
      "ds_read_b64_tr_b16 a[16:17], v243 offset:24704\n"
      "global_load_lds_dwordx4 v238, s[56:57] offset:512 \n"
      "v_mfma_f32_16x16x32_bf16 v[92:95], a[140:143], v[216:219], v[92:95]\n"
      "s_waitcnt lgkmcnt(11)\n"
      "v_mfma_f32_16x16x32_bf16 v[96:99], a[120:123], v[220:223], v[96:99]\n"
      "ds_read_b64_tr_b16 a[18:19], v243 offset:25472\n"
      "v_mad_i32_i24 v238, v251, s94, v238\n"
      "s_add_u32 s56, s22, 0x1e000\n"
      "s_addc_u32 s57, s23, 0\n"
      "s_add_u32 m0, s18, 0x4e00\n"
      "v_mfma_f32_16x16x32_bf16 v[100:103], a[124:127], v[220:223], v[100:103]\n"
      "ds_read_b64_tr_b16 a[20:21], v243 offset:24712\n"
      "global_load_lds_dwordx4 v238, s[56:57] offset:512 \n"
      "v_mfma_f32_16x16x32_bf16 v[104:107], a[128:131], v[220:223], v[104:107]\n"
      "ds_read_b128 v[216:219], v233 offset:28672\n"
      "v_mfma_f32_16x16x32_bf16 v[108:111], a[132:135], v[220:223], v[108:111]\n"
      "ds_read_b64_tr_b16 a[22:23], v243 offset:25480\n"
      "v_mad_i32_i24 v238, v251, s95, v238\n"
      "s_add_u32 s56, s22, 0x24000\n"
      "s_addc_u32 s57, s23, 0\n"
      "s_add_u32 m0, s18, 0x5e00\n"
      "v_mfma_f32_16x16x32_bf16 v[112:115], a[136:139], v[220:223], v[112:115]\n"
      "global_load_lds_dwordx4 v238, s[56:57] offset:512 \n"
      "v_mfma_f32_16x16x32_bf16 v[116:119], a[140:143], v[220:223], v[116:119]\n"
      "s_waitcnt lgkmcnt(11)\n"
      "v_mfma_f32_16x16x32_bf16 v[120:123], a[120:123], v[224:227], v[120:123]\n"
      "v_mad_i32_i24 v238, v251, s96, v238\n"
      "s_add_u32 s56, s22, 0x2a000\n"
      "s_addc_u32 s57, s23, 0\n"
      "s_add_u32 m0, s18, 0x6e00\n"
      "v_mfma_f32_16x16x32_bf16 v[124:127], a[124:127], v[224:227], v[124:127]\n"
      "global_load_lds_dwordx4 v238, s[56:57] offset:512 \n"
      "v_mfma_f32_16x16x32_bf16 v[128:131], a[128:131], v[224:227], v[128:131]\n"
      "ds_read_b128 v[220:223], v233 offset:32768\n"
      "v_add_u32 v233, 0xffff7000, v233\n"
      "v_mfma_f32_16x16x32_bf16 v[132:135], a[132:135], v[224:227], v[132:135]\n"
      "v_mad_i32_i24 v238, v251, s97, v238\n"
      "s_add_u32 s56, s22, 0x30000\n"
      "s_addc_u32 s57, s23, 0\n"
      "s_add_u32 m0, s18, 0x7e00\n"
      "v_mfma_f32_16x16x32_bf16 v[136:139], a[136:139], v[224:227], v[136:139]\n"
      "global_load_lds_dwordx4 v238, s[56:57] offset:512 \n"
      "v_mfma_f32_16x16x32_bf16 v[140:143], a[140:143], v[224:227], v[140:143]\n"
      "s_waitcnt lgkmcnt(7)\n"
      "v_mfma_f32_16x16x32_bf16 v[144:147], a[120:123], v[228:231], v[144:147]\n"
      "v_mfma_f32_16x16x32_bf16 v[148:151], a[124:127], v[228:231], v[148:151]\n"
      "global_load_dwordx4 a[252:255], v239, s[58:59] offset:3072 sc1\n"
      "v_mfma_f32_16x16x32_bf16 v[152:155], a[128:131], v[228:231], v[152:155]\n"
      "ds_read_b128 v[224:227], v234 offset:0\n"
      "v_mfma_f32_16x16x32_bf16 v[156:159], a[132:135], v[228:231], v[156:159]\n"
      "v_mfma_f32_16x16x32_bf16 v[160:163], a[136:139], v[228:231], v[160:163]\n"
      "v_mfma_f32_16x16x32_bf16 v[164:167], a[140:143], v[228:231], v[164:167]\n"
      "s_add_u32 s58, s24, 0x1000\n"
      "s_addc_u32 s59, s25, 0\n"
      "global_load_dwordx4 a[48:51], v239, s[58:59] offset:0 sc1\n"
      "s_waitcnt lgkmcnt(3)\n"
      "v_mfma_f32_16x16x32_bf16 v[168:171], a[120:123], v[216:219], v[168:171]\n"
      "s_waitcnt vmcnt(11)\n"
      "v_mfma_f32_16x16x32_bf16 v[172:175], a[124:127], v[216:219], v[172:175]\n"
      "ds_read_b64_tr_b16 a[24:25], v243 offset:49152\n"
      "v_mfma_f32_16x16x32_bf16 v[176:179], a[128:131], v[216:219], v[176:179]\n"
      "ds_read_b128 v[228:231], v234 offset:4096\n"
      "v_mfma_f32_16x16x32_bf16 v[180:183], a[132:135], v[216:219], v[180:183]\n"
      "ds_read_b64_tr_b16 a[26:27], v243 offset:49920\n"
      "global_load_dwordx4 a[52:55], v239, s[58:59] offset:1024 sc1\n"
      "v_mfma_f32_16x16x32_bf16 v[184:187], a[136:139], v[216:219], v[184:187]\n"
      "ds_read_b64_tr_b16 a[28:29], v243 offset:49160\n"
      "v_mfma_f32_16x16x32_bf16 v[188:191], a[140:143], v[216:219], v[188:191]\n"
      "s_waitcnt lgkmcnt(5)\n"
      "v_mfma_f32_16x16x32_bf16 v[192:195], a[120:123], v[220:223], v[192:195]\n"
      "ds_read_b64_tr_b16 a[30:31], v243 offset:49928\n"
      "v_mfma_f32_16x16x32_bf16 v[196:199], a[124:127], v[220:223], v[196:199]\n"
      "ds_read_b64_tr_b16 a[32:33], v243 offset:49216\n"
      "global_load_dwordx4 a[56:59], v239, s[58:59] offset:2048 sc1\n"
      "v_mfma_f32_16x16x32_bf16 v[200:203], a[128:131], v[220:223], v[200:203]\n"
      "ds_read_b128 v[216:219], v234 offset:8192\n"
      "v_mfma_f32_16x16x32_bf16 v[204:207], a[132:135], v[220:223], v[204:207]\n"
      "ds_read_b64_tr_b16 a[34:35], v243 offset:49984\n"
      "v_mfma_f32_16x16x32_bf16 v[208:211], a[136:139], v[220:223], v[208:211]\n"
      "ds_read_b64_tr_b16 a[36:37], v243 offset:49224\n"
      "v_mfma_f32_16x16x32_bf16 v[212:215], a[140:143], v[220:223], v[212:215]\n"
      "global_load_dwordx4 a[60:63], v239, s[58:59] offset:3072 sc1\n"
      "s_waitcnt lgkmcnt(9)\n"
      "v_mfma_f32_16x16x32_bf16 v[0:3], a[144:147], v[224:227], v[0:3]\n"
      "ds_read_b64_tr_b16 a[38:39], v243 offset:49992\n"
      "v_mfma_f32_16x16x32_bf16 v[4:7], a[148:151], v[224:227], v[4:7]\n"
      "ds_read_b64_tr_b16 a[40:41], v243 offset:49280\n"
      "v_mfma_f32_16x16x32_bf16 v[8:11], a[152:155], v[224:227], v[8:11]\n"
      "ds_read_b128 v[220:223], v234 offset:12288\n"
      "v_mfma_f32_16x16x32_bf16 v[12:15], a[156:159], v[224:227], v[12:15]\n"
      "ds_read_b64_tr_b16 a[42:43], v243 offset:50048\n"
      "s_add_u32 s58, s24, 0x2000\n"
      "s_addc_u32 s59, s25, 0\n"
      "global_load_dwordx4 a[64:67], v239, s[58:59] offset:0 sc1\n"
      "v_mfma_f32_16x16x32_bf16 v[16:19], a[160:163], v[224:227], v[16:19]\n"
      "ds_read_b64_tr_b16 a[44:45], v243 offset:49288\n"
      "v_mfma_f32_16x16x32_bf16 v[20:23], a[164:167], v[224:227], v[20:23]\n"
      "s_waitcnt lgkmcnt(12)\n"
      "v_mfma_f32_16x16x32_bf16 v[24:27], a[144:147], v[228:231], v[24:27]\n"
      "ds_read_b64_tr_b16 a[46:47], v243 offset:50056\n"
      "v_mfma_f32_16x16x32_bf16 v[28:31], a[148:151], v[228:231], v[28:31]\n"
      "global_load_dwordx4 a[68:71], v239, s[58:59] offset:1024 sc1\n"
      "v_mfma_f32_16x16x32_bf16 v[32:35], a[152:155], v[228:231], v[32:35]\n"
      "ds_read_b128 v[224:227], v234 offset:16384\n"
      "v_mfma_f32_16x16x32_bf16 v[36:39], a[156:159], v[228:231], v[36:39]\n"
      "v_mfma_f32_16x16x32_bf16 v[40:43], a[160:163], v[228:231], v[40:43]\n"
      "v_mfma_f32_16x16x32_bf16 v[44:47], a[164:167], v[228:231], v[44:47]\n"
      "global_load_dwordx4 a[72:75], v239, s[58:59] offset:2048 sc1\n"
      "s_waitcnt lgkmcnt(9)\n"
      "v_mfma_f32_16x16x32_bf16 v[48:51], a[144:147], v[216:219], v[48:51]\n"
      "v_mfma_f32_16x16x32_bf16 v[52:55], a[148:151], v[216:219], v[52:55]\n"
      "v_mfma_f32_16x16x32_bf16 v[56:59], a[152:155], v[216:219], v[56:59]\n"
      "ds_read_b128 v[228:231], v234 offset:20480\n"
      "v_mfma_f32_16x16x32_bf16 v[60:63], a[156:159], v[216:219], v[60:63]\n"
      "global_load_dwordx4 a[76:79], v239, s[58:59] offset:3072 sc1\n"
      "v_mfma_f32_16x16x32_bf16 v[64:67], a[160:163], v[216:219], v[64:67]\n"
      "v_mfma_f32_16x16x32_bf16 v[68:71], a[164:167], v[216:219], v[68:71]\n"
      "s_waitcnt lgkmcnt(5)\n"
      "v_mfma_f32_16x16x32_bf16 v[72:75], a[144:147], v[220:223], v[72:75]\n"
      "v_mfma_f32_16x16x32_bf16 v[76:79], a[148:151], v[220:223], v[76:79]\n"
      "s_add_u32 s58, s24, 0x3000\n"
      "s_addc_u32 s59, s25, 0\n"
      "global_load_dwordx4 a[80:83], v239, s[58:59] offset:0 sc1\n"
      "v_mfma_f32_16x16x32_bf16 v[80:83], a[152:155], v[220:223], v[80:83]\n"
      "ds_read_b128 v[216:219], v234 offset:24576\n"
      "v_mfma_f32_16x16x32_bf16 v[84:87], a[156:159], v[220:223], v[84:87]\n"
      "v_mfma_f32_16x16x32_bf16 v[88:91], a[160:163], v[220:223], v[88:91]\n"
      "v_mfma_f32_16x16x32_bf16 v[92:95], a[164:167], v[220:223], v[92:95]\n"
      "global_load_dwordx4 a[84:87], v239, s[58:59] offset:1024 sc1\n"
      "s_waitcnt lgkmcnt(2)\n"
      "v_mfma_f32_16x16x32_bf16 v[96:99], a[144:147], v[224:227], v[96:99]\n"
      "v_mfma_f32_16x16x32_bf16 v[100:103], a[148:151], v[224:227], v[100:103]\n"
      "v_mfma_f32_16x16x32_bf16 v[104:107], a[152:155], v[224:227], v[104:107]\n"
      "ds_read_b128 v[220:223], v234 offset:28672\n"
      "v_mfma_f32_16x16x32_bf16 v[108:111], a[156:159], v[224:227], v[108:111]\n"
      "global_load_dwordx4 a[88:91], v239, s[58:59] offset:2048 sc1\n"
      "v_mfma_f32_16x16x32_bf16 v[112:115], a[160:163], v[224:227], v[112:115]\n"
      "v_mfma_f32_16x16x32_bf16 v[116:119], a[164:167], v[224:227], v[116:119]\n"
      "s_waitcnt lgkmcnt(2)\n"
      "v_mfma_f32_16x16x32_bf16 v[120:123], a[144:147], v[228:231], v[120:123]\n"
      "v_mfma_f32_16x16x32_bf16 v[124:127], a[148:151], v[228:231], v[124:127]\n"
      "global_load_dwordx4 a[92:95], v239, s[58:59] offset:3072 sc1\n"
      "v_mfma_f32_16x16x32_bf16 v[128:131], a[152:155], v[228:231], v[128:131]\n"
      "ds_read_b128 v[224:227], v234 offset:32768\n"
      "v_add_u32 v234, 0xffff7000, v234\n"
      "v_mfma_f32_16x16x32_bf16 v[132:135], a[156:159], v[228:231], v[132:135]\n"
      "v_mfma_f32_16x16x32_bf16 v[136:139], a[160:163], v[228:231], v[136:139]\n"
      "v_mfma_f32_16x16x32_bf16 v[140:143], a[164:167], v[228:231], v[140:143]\n"
      "s_add_u32 s58, s24, 0x4000\n"
      "s_addc_u32 s59, s25, 0\n"
      "global_load_dwordx4 a[96:99], v239, s[58:59] offset:0 sc1\n"
      "s_waitcnt lgkmcnt(2)\n"
      "v_mfma_f32_16x16x32_bf16 v[144:147], a[144:147], v[216:219], v[144:147]\n"
      "ds_read_b64 v[236:237], v250 offset:4104\n"
      "v_mfma_f32_16x16x32_bf16 v[148:151], a[148:151], v[216:219], v[148:151]\n"
      "v_mfma_f32_16x16x32_bf16 v[152:155], a[152:155], v[216:219], v[152:155]\n"
      "ds_read_b128 v[228:231], v235 offset:0\n"
      "v_mfma_f32_16x16x32_bf16 v[156:159], a[156:159], v[216:219], v[156:159]\n"
      "global_load_dwordx4 a[100:103], v239, s[58:59] offset:1024 sc1\n"
      "v_mfma_f32_16x16x32_bf16 v[160:163], a[160:163], v[216:219], v[160:163]\n"
      "v_mfma_f32_16x16x32_bf16 v[164:167], a[164:167], v[216:219], v[164:167]\n"
      "s_waitcnt lgkmcnt(3)\n"
      "v_mfma_f32_16x16x32_bf16 v[168:171], a[144:147], v[220:223], v[168:171]\n"
      "v_mfma_f32_16x16x32_bf16 v[172:175], a[148:151], v[220:223], v[172:175]\n"
      "global_load_dwordx4 a[104:107], v239, s[58:59] offset:2048 sc1\n"
      "v_mfma_f32_16x16x32_bf16 v[176:179], a[152:155], v[220:223], v[176:179]\n"
      "ds_read_b128 v[216:219], v235 offset:4096\n"
      "v_mfma_f32_16x16x32_bf16 v[180:183], a[156:159], v[220:223], v[180:183]\n"
      "v_mfma_f32_16x16x32_bf16 v[184:187], a[160:163], v[220:223], v[184:187]\n"
      "v_mfma_f32_16x16x32_bf16 v[188:191], a[164:167], v[220:223], v[188:191]\n"
      "global_load_dwordx4 a[108:111], v239, s[58:59] offset:3072 sc1\n"
      "s_waitcnt lgkmcnt(3)\n"
      "v_mfma_f32_16x16x32_bf16 v[192:195], a[144:147], v[224:227], v[192:195]\n"
      "v_mfma_f32_16x16x32_bf16 v[196:199], a[148:151], v[224:227], v[196:199]\n"
      "v_mfma_f32_16x16x32_bf16 v[200:203], a[152:155], v[224:227], v[200:203]\n"
      "ds_read_b128 v[220:223], v235 offset:8192\n"
      "v_mfma_f32_16x16x32_bf16 v[204:207], a[156:159], v[224:227], v[204:207]\n"
      "s_add_u32 s58, s24, 0x5000\n"
      "s_addc_u32 s59, s25, 0\n"
      "global_load_dwordx4 a[112:115], v239, s[58:59] offset:0 sc1\n"
      "v_mfma_f32_16x16x32_bf16 v[208:211], a[160:163], v[224:227], v[208:211]\n"
      "v_mfma_f32_16x16x32_bf16 v[212:215], a[164:167], v[224:227], v[212:215]\n"
      "s_waitcnt lgkmcnt(2)\n"
      "v_mfma_f32_16x16x32_bf16 v[0:3], a[168:171], v[228:231], v[0:3]\n"
      "v_mfma_f32_16x16x32_bf16 v[4:7], a[172:175], v[228:231], v[4:7]\n"
      "global_load_dwordx4 a[116:119], v239, s[58:59] offset:1024 sc1\n"
      "v_mfma_f32_16x16x32_bf16 v[8:11], a[176:179], v[228:231], v[8:11]\n"
      "ds_read_b128 v[224:227], v235 offset:12288\n"
      "v_mfma_f32_16x16x32_bf16 v[12:15], a[180:183], v[228:231], v[12:15]\n"
      "v_mfma_f32_16x16x32_bf16 v[16:19], a[184:187], v[228:231], v[16:19]\n"
      "v_mfma_f32_16x16x32_bf16 v[20:23], a[188:191], v[228:231], v[20:23]\n"
      "global_load_dwordx4 a[120:123], v239, s[58:59] offset:2048 sc1\n"
      "s_waitcnt lgkmcnt(2)\n"
      "v_mfma_f32_16x16x32_bf16 v[24:27], a[168:171], v[216:219], v[24:27]\n"
      "v_mfma_f32_16x16x32_bf16 v[28:31], a[172:175], v[216:219], v[28:31]\n"
      "v_mfma_f32_16x16x32_bf16 v[32:35], a[176:179], v[216:219], v[32:35]\n"
      "ds_read_b128 v[228:231], v235 offset:16384\n"
      "v_mfma_f32_16x16x32_bf16 v[36:39], a[180:183], v[216:219], v[36:39]\n"
      "global_load_dwordx4 a[124:127], v239, s[58:59] offset:3072 sc1\n"
      "v_mfma_f32_16x16x32_bf16 v[40:43], a[184:187], v[216:219], v[40:43]\n"
      "v_mfma_f32_16x16x32_bf16 v[44:47], a[188:191], v[216:219], v[44:47]\n"
      "s_waitcnt lgkmcnt(2)\n"
      "v_mfma_f32_16x16x32_bf16 v[48:51], a[168:171], v[220:223], v[48:51]\n"
      "v_mfma_f32_16x16x32_bf16 v[52:55], a[172:175], v[220:223], v[52:55]\n"
      "s_add_u32 s58, s24, 0x6000\n"
      "s_addc_u32 s59, s25, 0\n"
      "global_load_dwordx4 a[128:131], v239, s[58:59] offset:0 sc1\n"
      "v_mfma_f32_16x16x32_bf16 v[56:59], a[176:179], v[220:223], v[56:59]\n"
      "ds_read_b128 v[216:219], v235 offset:20480\n"
      "v_mfma_f32_16x16x32_bf16 v[60:63], a[180:183], v[220:223], v[60:63]\n"
      "v_mfma_f32_16x16x32_bf16 v[64:67], a[184:187], v[220:223], v[64:67]\n"
      "v_mfma_f32_16x16x32_bf16 v[68:71], a[188:191], v[220:223], v[68:71]\n"
      "global_load_dwordx4 a[132:135], v239, s[58:59] offset:1024 sc1\n"
      "s_waitcnt lgkmcnt(2)\n"
      "v_mfma_f32_16x16x32_bf16 v[72:75], a[168:171], v[224:227], v[72:75]\n"
      "v_mfma_f32_16x16x32_bf16 v[76:79], a[172:175], v[224:227], v[76:79]\n"
      "v_mfma_f32_16x16x32_bf16 v[80:83], a[176:179], v[224:227], v[80:83]\n"
      "ds_read_b128 v[220:223], v235 offset:24576\n"
      "v_mfma_f32_16x16x32_bf16 v[84:87], a[180:183], v[224:227], v[84:87]\n"
      "global_load_dwordx4 a[136:139], v239, s[58:59] offset:2048 sc1\n"
      "v_mfma_f32_16x16x32_bf16 v[88:91], a[184:187], v[224:227], v[88:91]\n"
      "v_mfma_f32_16x16x32_bf16 v[92:95], a[188:191], v[224:227], v[92:95]\n"
      "s_waitcnt lgkmcnt(2)\n"
      "v_mfma_f32_16x16x32_bf16 v[96:99], a[168:171], v[228:231], v[96:99]\n"
      "v_mfma_f32_16x16x32_bf16 v[100:103], a[172:175], v[228:231], v[100:103]\n"
      "global_load_dwordx4 a[140:143], v239, s[58:59] offset:3072 sc1\n"
      "v_mfma_f32_16x16x32_bf16 v[104:107], a[176:179], v[228:231], v[104:107]\n"
      "ds_read_b128 v[224:227], v235 offset:28672\n"
      "v_mfma_f32_16x16x32_bf16 v[108:111], a[180:183], v[228:231], v[108:111]\n"
      "v_mfma_f32_16x16x32_bf16 v[112:115], a[184:187], v[228:231], v[112:115]\n"
      "v_mfma_f32_16x16x32_bf16 v[116:119], a[188:191], v[228:231], v[116:119]\n"
      "s_add_u32 s58, s24, 0x7000\n"
      "s_addc_u32 s59, s25, 0\n"
      "global_load_dwordx4 a[144:147], v239, s[58:59] offset:0 sc1\n"
      "s_waitcnt lgkmcnt(2)\n"
      "v_mfma_f32_16x16x32_bf16 v[120:123], a[168:171], v[216:219], v[120:123]\n"
      "v_mfma_f32_16x16x32_bf16 v[124:127], a[172:175], v[216:219], v[124:127]\n"
      "v_mfma_f32_16x16x32_bf16 v[128:131], a[176:179], v[216:219], v[128:131]\n"
      "ds_read_b128 v[228:231], v235 offset:32768\n"
      "v_add_u32 v235, 0xffff7000, v235\n"
      "v_mfma_f32_16x16x32_bf16 v[132:135], a[180:183], v[216:219], v[132:135]\n"
      "global_load_dwordx4 a[148:151], v239, s[58:59] offset:1024 sc1\n"
      "v_mfma_f32_16x16x32_bf16 v[136:139], a[184:187], v[216:219], v[136:139]\n"
      "v_mfma_f32_16x16x32_bf16 v[140:143], a[188:191], v[216:219], v[140:143]\n"
      "s_waitcnt lgkmcnt(2)\n"
      "v_mfma_f32_16x16x32_bf16 v[144:147], a[168:171], v[220:223], v[144:147]\n"
      "v_mfma_f32_16x16x32_bf16 v[148:151], a[172:175], v[220:223], v[148:151]\n"
      "s_waitcnt vmcnt(27)\n"
      "s_barrier\n"
      "global_load_dwordx4 a[152:155], v239, s[58:59] offset:2048 sc1\n"
      "v_mfma_f32_16x16x32_bf16 v[152:155], a[176:179], v[220:223], v[152:155]\n"
      "ds_read_b128 v[216:219], v232 offset:0\n"
      "v_mfma_f32_16x16x32_bf16 v[156:159], a[180:183], v[220:223], v[156:159]\n"
      "v_mfma_f32_16x16x32_bf16 v[160:163], a[184:187], v[220:223], v[160:163]\n"
      "v_mfma_f32_16x16x32_bf16 v[164:167], a[188:191], v[220:223], v[164:167]\n"
      "global_load_dwordx4 a[156:159], v239, s[58:59] offset:3072 sc1\n"
      "s_waitcnt lgkmcnt(2)\n"
      "v_mfma_f32_16x16x32_bf16 v[168:171], a[168:171], v[224:227], v[168:171]\n"
      "v_mfma_f32_16x16x32_bf16 v[172:175], a[172:175], v[224:227], v[172:175]\n"
      "v_mfma_f32_16x16x32_bf16 v[176:179], a[176:179], v[224:227], v[176:179]\n"
      "ds_read_b128 v[220:223], v232 offset:4096\n"
      "v_mfma_f32_16x16x32_bf16 v[180:183], a[180:183], v[224:227], v[180:183]\n"
      "s_add_u32 s58, s24, 0x8000\n"
      "s_addc_u32 s59, s25, 0\n"
      "global_load_dwordx4 a[160:163], v239, s[58:59] offset:0 sc1\n"
      "v_mfma_f32_16x16x32_bf16 v[184:187], a[184:187], v[224:227], v[184:187]\n"
      "v_mfma_f32_16x16x32_bf16 v[188:191], a[188:191], v[224:227], v[188:191]\n"
      "s_waitcnt lgkmcnt(2)\n"
      "v_mfma_f32_16x16x32_bf16 v[192:195], a[168:171], v[228:231], v[192:195]\n"
      "v_mfma_f32_16x16x32_bf16 v[196:199], a[172:175], v[228:231], v[196:199]\n"
      "global_load_dwordx4 a[164:167], v239, s[58:59] offset:1024 sc1\n"
      "v_mfma_f32_16x16x32_bf16 v[200:203], a[176:179], v[228:231], v[200:203]\n"
      "ds_read_b128 v[224:227], v232 offset:8192\n"
      "v_mfma_f32_16x16x32_bf16 v[204:207], a[180:183], v[228:231], v[204:207]\n"
      "v_mfma_f32_16x16x32_bf16 v[208:211], a[184:187], v[228:231], v[208:211]\n"
      "v_mfma_f32_16x16x32_bf16 v[212:215], a[188:191], v[228:231], v[212:215]\n"
      "s_waitcnt lgkmcnt(2)\n"
      "v_mfma_f32_16x16x32_bf16 v[0:3], a[192:195], v[216:219], v[0:3]\n"
      "global_load_dwordx4 a[168:171], v239, s[58:59] offset:2048 sc1\n"
      "v_mfma_f32_16x16x32_bf16 v[4:7], a[196:199], v[216:219], v[4:7]\n"
      "v_mfma_f32_16x16x32_bf16 v[8:11], a[200:203], v[216:219], v[8:11]\n"
      "ds_read_b128 v[228:231], v232 offset:12288\n"
      "v_mfma_f32_16x16x32_bf16 v[12:15], a[204:207], v[216:219], v[12:15]\n"
      "v_mfma_f32_16x16x32_bf16 v[16:19], a[208:211], v[216:219], v[16:19]\n"
      "global_load_dwordx4 a[172:175], v239, s[58:59] offset:3072 sc1\n"
      "v_mfma_f32_16x16x32_bf16 v[20:23], a[212:215], v[216:219], v[20:23]\n"
      "s_waitcnt lgkmcnt(2)\n"
      "v_mfma_f32_16x16x32_bf16 v[24:27], a[192:195], v[220:223], v[24:27]\n"
      "v_mfma_f32_16x16x32_bf16 v[28:31], a[196:199], v[220:223], v[28:31]\n"
      "v_mfma_f32_16x16x32_bf16 v[32:35], a[200:203], v[220:223], v[32:35]\n"
      "ds_read_b128 v[216:219], v232 offset:16384\n"
      "s_add_u32 s58, s24, 0x9000\n"
      "s_addc_u32 s59, s25, 0\n"
      "global_load_dwordx4 a[176:179], v239, s[58:59] offset:0 sc1\n"
      "v_mfma_f32_16x16x32_bf16 v[36:39], a[204:207], v[220:223], v[36:39]\n"
      "v_mfma_f32_16x16x32_bf16 v[40:43], a[208:211], v[220:223], v[40:43]\n"
      "v_mfma_f32_16x16x32_bf16 v[44:47], a[212:215], v[220:223], v[44:47]\n"
      "s_waitcnt lgkmcnt(2)\n"
      "v_mfma_f32_16x16x32_bf16 v[48:51], a[192:195], v[224:227], v[48:51]\n"
      "global_load_dwordx4 a[180:183], v239, s[58:59] offset:1024 sc1\n"
      "v_mfma_f32_16x16x32_bf16 v[52:55], a[196:199], v[224:227], v[52:55]\n"
      "v_mfma_f32_16x16x32_bf16 v[56:59], a[200:203], v[224:227], v[56:59]\n"
      "ds_read_b128 v[220:223], v232 offset:20480\n"
      "v_mfma_f32_16x16x32_bf16 v[60:63], a[204:207], v[224:227], v[60:63]\n"
      "v_mfma_f32_16x16x32_bf16 v[64:67], a[208:211], v[224:227], v[64:67]\n"
      "global_load_dwordx4 a[184:187], v239, s[58:59] offset:2048 sc1\n"
      "v_mfma_f32_16x16x32_bf16 v[68:71], a[212:215], v[224:227], v[68:71]\n"
      "s_waitcnt lgkmcnt(2)\n"
      "v_mfma_f32_16x16x32_bf16 v[72:75], a[192:195], v[228:231], v[72:75]\n"
      "v_mfma_f32_16x16x32_bf16 v[76:79], a[196:199], v[228:231], v[76:79]\n"
      "v_mfma_f32_16x16x32_bf16 v[80:83], a[200:203], v[228:231], v[80:83]\n"
      "ds_read_b128 v[224:227], v232 offset:24576\n"
      "global_load_dwordx4 a[188:191], v239, s[58:59] offset:3072 sc1\n"
      "v_mfma_f32_16x16x32_bf16 v[84:87], a[204:207], v[228:231], v[84:87]\n"
      "v_mfma_f32_16x16x32_bf16 v[88:91], a[208:211], v[228:231], v[88:91]\n"
      "v_mfma_f32_16x16x32_bf16 v[92:95], a[212:215], v[228:231], v[92:95]\n"
      "s_waitcnt lgkmcnt(2)\n"
      "v_mfma_f32_16x16x32_bf16 v[96:99], a[192:195], v[216:219], v[96:99]\n"
      "s_mul_i32 s78, s98, s19\n"
      "v_mfma_f32_16x16x32_bf16 v[100:103], a[196:199], v[216:219], v[100:103]\n"
      "v_add_u32 v236, s78, v236\n"
      "v_mfma_f32_16x16x32_bf16 v[104:107], a[200:203], v[216:219], v[104:107]\n"
      "ds_read_b128 v[228:231], v232 offset:28672\n"
      "s_mul_i32 s78, s78, s99\n"
      "v_mfma_f32_16x16x32_bf16 v[108:111], a[204:207], v[216:219], v[108:111]\n"
      "v_add_u32 v237, s78, v237\n"
      "v_mfma_f32_16x16x32_bf16 v[112:115], a[208:211], v[216:219], v[112:115]\n"
      "s_add_u32 s52, s20, s32\n"
      "s_addc_u32 s53, s21, 0\n"
      "s_add_u32 m0, s17, 0x0\n"
      "v_mfma_f32_16x16x32_bf16 v[116:119], a[212:215], v[216:219], v[116:119]\n"
      "global_load_lds_dwordx4 v236, s[52:53] offset:0 \n"
      "s_waitcnt lgkmcnt(2)\n"
      "v_mfma_f32_16x16x32_bf16 v[120:123], a[192:195], v[220:223], v[120:123]\n"
      "v_mfma_f32_16x16x32_bf16 v[124:127], a[196:199], v[220:223], v[124:127]\n"
      "s_add_u32 m0, s17, 0x1f00\n"
      "v_mfma_f32_16x16x32_bf16 v[128:131], a[200:203], v[220:223], v[128:131]\n"
      "ds_read_b128 v[216:219], v232 offset:32768\n"
      "v_add_u32 v232, v249, v232\n"
      "global_load_lds_dwordx4 v236, s[52:53] offset:256 \n"
      "v_mfma_f32_16x16x32_bf16 v[132:135], a[204:207], v[220:223], v[132:135]\n"
      "v_mfma_f32_16x16x32_bf16 v[136:139], a[208:211], v[220:223], v[136:139]\n"
      "s_add_u32 m0, s17, 0x3e00\n"
      "v_mfma_f32_16x16x32_bf16 v[140:143], a[212:215], v[220:223], v[140:143]\n"
      "global_load_lds_dwordx4 v236, s[52:53] offset:512 \n"
      "s_waitcnt lgkmcnt(2)\n"
      "v_mfma_f32_16x16x32_bf16 v[144:147], a[192:195], v[224:227], v[144:147]\n"
      "v_mfma_f32_16x16x32_bf16 v[148:151], a[196:199], v[224:227], v[148:151]\n"
      "s_add_u32 m0, s17, 0x1000\n"
      "v_mfma_f32_16x16x32_bf16 v[152:155], a[200:203], v[224:227], v[152:155]\n"
      "ds_read_b128 v[220:223], v233 offset:0\n"
      "global_load_lds_dwordx4 v237, s[52:53] offset:0 \n"
      "v_mfma_f32_16x16x32_bf16 v[156:159], a[204:207], v[224:227], v[156:159]\n"
      "v_mfma_f32_16x16x32_bf16 v[160:163], a[208:211], v[224:227], v[160:163]\n"
      "s_add_u32 m0, s17, 0x2f00\n"
      "v_mfma_f32_16x16x32_bf16 v[164:167], a[212:215], v[224:227], v[164:167]\n"
      "global_load_lds_dwordx4 v237, s[52:53] offset:256 \n"
      "s_waitcnt lgkmcnt(2)\n"
      "v_mfma_f32_16x16x32_bf16 v[168:171], a[192:195], v[228:231], v[168:171]\n"
      "v_mfma_f32_16x16x32_bf16 v[172:175], a[196:199], v[228:231], v[172:175]\n"
      "s_add_u32 m0, s17, 0x4e00\n"
      "v_mfma_f32_16x16x32_bf16 v[176:179], a[200:203], v[228:231], v[176:179]\n"
      "ds_read_b128 v[224:227], v233 offset:4096\n"
      "global_load_lds_dwordx4 v237, s[52:53] offset:512 \n"
      "v_mfma_f32_16x16x32_bf16 v[180:183], a[204:207], v[228:231], v[180:183]\n"
      "v_mfma_f32_16x16x32_bf16 v[184:187], a[208:211], v[228:231], v[184:187]\n"
      "v_mfma_f32_16x16x32_bf16 v[188:191], a[212:215], v[228:231], v[188:191]\n"
      "s_waitcnt lgkmcnt(2)\n"
      "v_mfma_f32_16x16x32_bf16 v[192:195], a[192:195], v[216:219], v[192:195]\n"
      "v_mfma_f32_16x16x32_bf16 v[196:199], a[196:199], v[216:219], v[196:199]\n"
      "LPD2:\n"
      "s_cbranch_execz LPD7\n"
      "v_mfma_f32_16x16x32_bf16 v[200:203], a[200:203], v[216:219], v[200:203]\n"
      "ds_read_b128 v[228:231], v233 offset:8192\n"
      "v_mfma_f32_16x16x32_bf16 v[204:207], a[204:207], v[216:219], v[204:207]\n"
      "v_mfma_f32_16x16x32_bf16 v[208:211], a[208:211], v[216:219], v[208:211]\n"
      "v_mfma_f32_16x16x32_bf16 v[212:215], a[212:215], v[216:219], v[212:215]\n"
      "s_waitcnt lgkmcnt(2)\n"
      "v_mfma_f32_16x16x32_bf16 v[0:3], a[216:219], v[220:223], v[0:3]\n"
      "s_add_u32 s58, s24, 0xa000\n"
      "s_addc_u32 s59, s25, 0\n"
      "global_load_dwordx4 a[192:195], v239, s[58:59] offset:0 sc1\n"
      "v_mfma_f32_16x16x32_bf16 v[4:7], a[220:223], v[220:223], v[4:7]\n"
      "v_mfma_f32_16x16x32_bf16 v[8:11], a[224:227], v[220:223], v[8:11]\n"
      "ds_read_b128 v[216:219], v233 offset:12288\n"
      "v_mfma_f32_16x16x32_bf16 v[12:15], a[228:231], v[220:223], v[12:15]\n"
      "v_mfma_f32_16x16x32_bf16 v[16:19], a[232:235], v[220:223], v[16:19]\n"
      "global_load_dwordx4 a[196:199], v239, s[58:59] offset:1024 sc1\n"
      "v_mfma_f32_16x16x32_bf16 v[20:23], a[236:239], v[220:223], v[20:23]\n"
      "s_waitcnt lgkmcnt(2)\n"
      "v_mfma_f32_16x16x32_bf16 v[24:27], a[216:219], v[224:227], v[24:27]\n"
      "v_mfma_f32_16x16x32_bf16 v[28:31], a[220:223], v[224:227], v[28:31]\n"
      "v_mfma_f32_16x16x32_bf16 v[32:35], a[224:227], v[224:227], v[32:35]\n"
      "ds_read_b128 v[220:223], v233 offset:16384\n"
      "global_load_dwordx4 a[200:203], v239, s[58:59] offset:2048 sc1\n"
      "v_mfma_f32_16x16x32_bf16 v[36:39], a[228:231], v[224:227], v[36:39]\n"
      "v_mfma_f32_16x16x32_bf16 v[40:43], a[232:235], v[224:227], v[40:43]\n"
      "v_mfma_f32_16x16x32_bf16 v[44:47], a[236:239], v[224:227], v[44:47]\n"
      "s_waitcnt lgkmcnt(2)\n"
      "v_mfma_f32_16x16x32_bf16 v[48:51], a[216:219], v[228:231], v[48:51]\n"
      "global_load_dwordx4 a[204:207], v239, s[58:59] offset:3072 sc1\n"
      "v_mfma_f32_16x16x32_bf16 v[52:55], a[220:223], v[228:231], v[52:55]\n"
      "v_mfma_f32_16x16x32_bf16 v[56:59], a[224:227], v[228:231], v[56:59]\n"
      "ds_read_b128 v[224:227], v233 offset:20480\n"
      "v_mfma_f32_16x16x32_bf16 v[60:63], a[228:231], v[228:231], v[60:63]\n"
      "v_mfma_f32_16x16x32_bf16 v[64:67], a[232:235], v[228:231], v[64:67]\n"
      "s_add_u32 s58, s24, 0xb000\n"
      "s_addc_u32 s59, s25, 0\n"
      "global_load_dwordx4 a[208:211], v239, s[58:59] offset:0 sc1\n"
      "v_mfma_f32_16x16x32_bf16 v[68:71], a[236:239], v[228:231], v[68:71]\n"
      "s_waitcnt lgkmcnt(2)\n"
      "v_mfma_f32_16x16x32_bf16 v[72:75], a[216:219], v[216:219], v[72:75]\n"
      "v_mfma_f32_16x16x32_bf16 v[76:79], a[220:223], v[216:219], v[76:79]\n"
      "v_mfma_f32_16x16x32_bf16 v[80:83], a[224:227], v[216:219], v[80:83]\n"
      "ds_read_b128 v[228:231], v233 offset:24576\n"
      "global_load_dwordx4 a[212:215], v239, s[58:59] offset:1024 sc1\n"
      "v_mfma_f32_16x16x32_bf16 v[84:87], a[228:231], v[216:219], v[84:87]\n"
      "v_mfma_f32_16x16x32_bf16 v[88:91], a[232:235], v[216:219], v[88:91]\n"
      "v_mfma_f32_16x16x32_bf16 v[92:95], a[236:239], v[216:219], v[92:95]\n"
      "s_waitcnt lgkmcnt(2)\n"
      "v_mfma_f32_16x16x32_bf16 v[96:99], a[216:219], v[220:223], v[96:99]\n"
      "v_mfma_f32_16x16x32_bf16 v[100:103], a[220:223], v[220:223], v[100:103]\n"
      "v_mfma_f32_16x16x32_bf16 v[104:107], a[224:227], v[220:223], v[104:107]\n"
      "ds_read_b128 v[216:219], v233 offset:28672\n"
      "v_mfma_f32_16x16x32_bf16 v[108:111], a[228:231], v[220:223], v[108:111]\n"
      "v_mfma_f32_16x16x32_bf16 v[112:115], a[232:235], v[220:223], v[112:115]\n"
      "v_mfma_f32_16x16x32_bf16 v[116:119], a[236:239], v[220:223], v[116:119]\n"
      "s_waitcnt lgkmcnt(2)\n"
      "v_mfma_f32_16x16x32_bf16 v[120:123], a[216:219], v[224:227], v[120:123]\n"
      "v_mfma_f32_16x16x32_bf16 v[124:127], a[220:223], v[224:227], v[124:127]\n"
      "v_mfma_f32_16x16x32_bf16 v[128:131], a[224:227], v[224:227], v[128:131]\n"
      "ds_read_b128 v[220:223], v233 offset:32768\n"
      "v_add_u32 v233, v249, v233\n"
      "v_mfma_f32_16x16x32_bf16 v[132:135], a[228:231], v[224:227], v[132:135]\n"
      "v_mfma_f32_16x16x32_bf16 v[136:139], a[232:235], v[224:227], v[136:139]\n"
      "v_mfma_f32_16x16x32_bf16 v[140:143], a[236:239], v[224:227], v[140:143]\n"
      "s_waitcnt lgkmcnt(2)\n"
      "v_mfma_f32_16x16x32_bf16 v[144:147], a[216:219], v[228:231], v[144:147]\n"
      "v_mfma_f32_16x16x32_bf16 v[148:151], a[220:223], v[228:231], v[148:151]\n"
      "v_mfma_f32_16x16x32_bf16 v[152:155], a[224:227], v[228:231], v[152:155]\n"
      "ds_read_b128 v[224:227], v234 offset:0\n"
      "v_mfma_f32_16x16x32_bf16 v[156:159], a[228:231], v[228:231], v[156:159]\n"
      "v_mfma_f32_16x16x32_bf16 v[160:163], a[232:235], v[228:231], v[160:163]\n"
      "v_mfma_f32_16x16x32_bf16 v[164:167], a[236:239], v[228:231], v[164:167]\n"
      "s_waitcnt lgkmcnt(2)\n"
      "v_mfma_f32_16x16x32_bf16 v[168:171], a[216:219], v[216:219], v[168:171]\n"
      "v_mfma_f32_16x16x32_bf16 v[172:175], a[220:223], v[216:219], v[172:175]\n"
      "v_mfma_f32_16x16x32_bf16 v[176:179], a[224:227], v[216:219], v[176:179]\n"
      "ds_read_b128 v[228:231], v234 offset:4096\n"
      "v_mfma_f32_16x16x32_bf16 v[180:183], a[228:231], v[216:219], v[180:183]\n"
      "v_mfma_f32_16x16x32_bf16 v[184:187], a[232:235], v[216:219], v[184:187]\n"
      "s_add_u32 s52, s42, s32\n"
      "s_addc_u32 s53, s43, 0\n"
      "s_add_u32 m0, s17, 0x6000\n"
      "v_mfma_f32_16x16x32_bf16 v[188:191], a[236:239], v[216:219], v[188:191]\n"
      "global_load_lds_dwordx4 v236, s[52:53] offset:0 \n"
      "s_waitcnt lgkmcnt(2)\n"
      "v_mfma_f32_16x16x32_bf16 v[192:195], a[216:219], v[220:223], v[192:195]\n"
      "v_mfma_f32_16x16x32_bf16 v[196:199], a[220:223], v[220:223], v[196:199]\n"
      "s_add_u32 m0, s17, 0x7f00\n"
      "v_mfma_f32_16x16x32_bf16 v[200:203], a[224:227], v[220:223], v[200:203]\n"
      "ds_read_b128 v[216:219], v234 offset:8192\n"
      "global_load_lds_dwordx4 v236, s[52:53] offset:256 \n"
      "v_mfma_f32_16x16x32_bf16 v[204:207], a[228:231], v[220:223], v[204:207]\n"
      "v_mfma_f32_16x16x32_bf16 v[208:211], a[232:235], v[220:223], v[208:211]\n"
      "s_add_u32 m0, s17, 0x9e00\n"
      "v_mfma_f32_16x16x32_bf16 v[212:215], a[236:239], v[220:223], v[212:215]\n"
      "global_load_lds_dwordx4 v236, s[52:53] offset:512 \n"
      "s_waitcnt lgkmcnt(2)\n"
      "v_mfma_f32_16x16x32_bf16 v[0:3], a[0:3], v[224:227], v[0:3]\n"
      "ds_read_b128 v[244:247], v250\n"
      "v_mfma_f32_16x16x32_bf16 v[4:7], a[4:7], v[224:227], v[4:7]\n"
      "s_add_u32 m0, s17, 0x7000\n"
      "ds_read_b64 v[252:253], v250 offset:4096\n"
      "v_mfma_f32_16x16x32_bf16 v[8:11], a[8:11], v[224:227], v[8:11]\n"
      "ds_read_b128 v[220:223], v234 offset:12288\n"
      "global_load_lds_dwordx4 v237, s[52:53] offset:0 \n"
      "v_mfma_f32_16x16x32_bf16 v[12:15], a[12:15], v[224:227], v[12:15]\n"
      "v_mfma_f32_16x16x32_bf16 v[16:19], a[16:19], v[224:227], v[16:19]\n"
      "s_add_u32 m0, s17, 0x8f00\n"
      "v_mfma_f32_16x16x32_bf16 v[20:23], a[20:23], v[224:227], v[20:23]\n"
      "global_load_lds_dwordx4 v237, s[52:53] offset:256 \n"
      "s_waitcnt lgkmcnt(4)\n"
      "v_mfma_f32_16x16x32_bf16 v[24:27], a[0:3], v[228:231], v[24:27]\n"
      "v_mfma_f32_16x16x32_bf16 v[28:31], a[4:7], v[228:231], v[28:31]\n"
      "s_add_u32 m0, s17, 0xae00\n"
      "v_mfma_f32_16x16x32_bf16 v[32:35], a[8:11], v[228:231], v[32:35]\n"
      "ds_read_b128 v[224:227], v234 offset:16384\n"
      "global_load_lds_dwordx4 v237, s[52:53] offset:512 \n"
      "v_mfma_f32_16x16x32_bf16 v[36:39], a[12:15], v[228:231], v[36:39]\n"
      "v_mfma_f32_16x16x32_bf16 v[40:43], a[16:19], v[228:231], v[40:43]\n"
      "v_mfma_f32_16x16x32_bf16 v[44:47], a[20:23], v[228:231], v[44:47]\n"
      "global_load_dwordx4 a[216:219], v239, s[58:59] offset:2048 sc1\n"
      "s_waitcnt lgkmcnt(4)\n"
      "v_mfma_f32_16x16x32_bf16 v[48:51], a[0:3], v[216:219], v[48:51]\n"
      "v_mfma_f32_16x16x32_bf16 v[52:55], a[4:7], v[216:219], v[52:55]\n"
      "v_mfma_f32_16x16x32_bf16 v[56:59], a[8:11], v[216:219], v[56:59]\n"
      "ds_read_b128 v[228:231], v234 offset:20480\n"
      "v_mfma_f32_16x16x32_bf16 v[60:63], a[12:15], v[216:219], v[60:63]\n"
      "global_load_dwordx4 a[220:223], v239, s[58:59] offset:3072 sc1\n"
      "v_mfma_f32_16x16x32_bf16 v[64:67], a[16:19], v[216:219], v[64:67]\n"
      "v_mfma_f32_16x16x32_bf16 v[68:71], a[20:23], v[216:219], v[68:71]\n"
      "s_waitcnt lgkmcnt(2)\n"
      "v_mfma_f32_16x16x32_bf16 v[72:75], a[0:3], v[220:223], v[72:75]\n"
      "v_mfma_f32_16x16x32_bf16 v[76:79], a[4:7], v[220:223], v[76:79]\n"
      "s_add_u32 s58, s24, 0xc000\n"
      "s_addc_u32 s59, s25, 0\n"
      "global_load_dwordx4 a[224:227], v239, s[58:59] offset:0 sc1\n"
      "v_mfma_f32_16x16x32_bf16 v[80:83], a[8:11], v[220:223], v[80:83]\n"
      "ds_read_b128 v[216:219], v234 offset:24576\n"
      "v_mfma_f32_16x16x32_bf16 v[84:87], a[12:15], v[220:223], v[84:87]\n"
      "v_mfma_f32_16x16x32_bf16 v[88:91], a[16:19], v[220:223], v[88:91]\n"
      "v_mfma_f32_16x16x32_bf16 v[92:95], a[20:23], v[220:223], v[92:95]\n"
      "global_load_dwordx4 a[228:231], v239, s[58:59] offset:1024 sc1\n"
      "s_waitcnt lgkmcnt(2)\n"
      "v_mfma_f32_16x16x32_bf16 v[96:99], a[0:3], v[224:227], v[96:99]\n"
      "v_mfma_f32_16x16x32_bf16 v[100:103], a[4:7], v[224:227], v[100:103]\n"
      "v_mfma_f32_16x16x32_bf16 v[104:107], a[8:11], v[224:227], v[104:107]\n"
      "ds_read_b128 v[220:223], v234 offset:28672\n"
      "v_mfma_f32_16x16x32_bf16 v[108:111], a[12:15], v[224:227], v[108:111]\n"
      "global_load_dwordx4 a[232:235], v239, s[58:59] offset:2048 sc1\n"
      "v_mfma_f32_16x16x32_bf16 v[112:115], a[16:19], v[224:227], v[112:115]\n"
      "v_mfma_f32_16x16x32_bf16 v[116:119], a[20:23], v[224:227], v[116:119]\n"
      "s_waitcnt lgkmcnt(2)\n"
      "v_mfma_f32_16x16x32_bf16 v[120:123], a[0:3], v[228:231], v[120:123]\n"
      "v_mfma_f32_16x16x32_bf16 v[124:127], a[4:7], v[228:231], v[124:127]\n"
      "global_load_dwordx4 a[236:239], v239, s[58:59] offset:3072 sc1\n"
      "v_mfma_f32_16x16x32_bf16 v[128:131], a[8:11], v[228:231], v[128:131]\n"
      "ds_read_b128 v[224:227], v234 offset:32768\n"
      "v_add_u32 v234, v249, v234\n"
      "v_mfma_f32_16x16x32_bf16 v[132:135], a[12:15], v[228:231], v[132:135]\n"
      "v_mfma_f32_16x16x32_bf16 v[136:139], a[16:19], v[228:231], v[136:139]\n"
      "v_mfma_f32_16x16x32_bf16 v[140:143], a[20:23], v[228:231], v[140:143]\n"
      "s_waitcnt lgkmcnt(2)\n"
      "v_mfma_f32_16x16x32_bf16 v[144:147], a[0:3], v[216:219], v[144:147]\n"
      "v_mfma_f32_16x16x32_bf16 v[148:151], a[4:7], v[216:219], v[148:151]\n"
      "v_mfma_f32_16x16x32_bf16 v[152:155], a[8:11], v[216:219], v[152:155]\n"
      "ds_read_b128 v[228:231], v235 offset:4096\n"
      "v_mfma_f32_16x16x32_bf16 v[156:159], a[12:15], v[216:219], v[156:159]\n"
      "v_mfma_f32_16x16x32_bf16 v[160:163], a[16:19], v[216:219], v[160:163]\n"
      "v_mfma_f32_16x16x32_bf16 v[164:167], a[20:23], v[216:219], v[164:167]\n"
      "s_waitcnt lgkmcnt(2)\n"
      "v_mfma_f32_16x16x32_bf16 v[168:171], a[0:3], v[220:223], v[168:171]\n"
      "v_mfma_f32_16x16x32_bf16 v[172:175], a[4:7], v[220:223], v[172:175]\n"
      "v_mfma_f32_16x16x32_bf16 v[176:179], a[8:11], v[220:223], v[176:179]\n"
      "ds_read_b128 v[216:219], v235 offset:8192\n"
      "v_mfma_f32_16x16x32_bf16 v[180:183], a[12:15], v[220:223], v[180:183]\n"
      "v_mfma_f32_16x16x32_bf16 v[184:187], a[16:19], v[220:223], v[184:187]\n"
      "v_mfma_f32_16x16x32_bf16 v[188:191], a[20:23], v[220:223], v[188:191]\n"
      "s_waitcnt lgkmcnt(2)\n"
      "v_mfma_f32_16x16x32_bf16 v[192:195], a[0:3], v[224:227], v[192:195]\n"
      "v_mfma_f32_16x16x32_bf16 v[196:199], a[4:7], v[224:227], v[196:199]\n"
      "v_mfma_f32_16x16x32_bf16 v[200:203], a[8:11], v[224:227], v[200:203]\n"
      "ds_read_b128 v[220:223], v235 offset:12288\n"
      "v_mfma_f32_16x16x32_bf16 v[204:207], a[12:15], v[224:227], v[204:207]\n"
      "v_mfma_f32_16x16x32_bf16 v[208:211], a[16:19], v[224:227], v[208:211]\n"
      "v_mfma_f32_16x16x32_bf16 v[212:215], a[20:23], v[224:227], v[212:215]\n"
      "s_waitcnt lgkmcnt(2)\n"
      "v_mfma_f32_16x16x32_bf16 v[24:27], a[24:27], v[228:231], v[24:27]\n"
      "s_add_u32 s58, s24, 0xd000\n"
      "s_addc_u32 s59, s25, 0\n"
      "global_load_dwordx4 a[0:3], v239, s[58:59] offset:0 sc1\n"
      "v_mfma_f32_16x16x32_bf16 v[28:31], a[28:31], v[228:231], v[28:31]\n"
      "v_mfma_f32_16x16x32_bf16 v[32:35], a[32:35], v[228:231], v[32:35]\n"
      "ds_read_b128 v[224:227], v235 offset:16384\n"
      "v_mfma_f32_16x16x32_bf16 v[36:39], a[36:39], v[228:231], v[36:39]\n"
      "v_mfma_f32_16x16x32_bf16 v[40:43], a[40:43], v[228:231], v[40:43]\n"
      "global_load_dwordx4 a[4:7], v239, s[58:59] offset:1024 sc1\n"
      "v_mfma_f32_16x16x32_bf16 v[44:47], a[44:47], v[228:231], v[44:47]\n"
      "s_waitcnt lgkmcnt(2)\n"
      "v_mfma_f32_16x16x32_bf16 v[48:51], a[24:27], v[216:219], v[48:51]\n"
      "v_cvt_pk_bf16_f32 v24, v24, v25\n"
      "v_cvt_pk_bf16_f32 v25, v26, v27\n"
      "v_mfma_f32_16x16x32_bf16 v[52:55], a[28:31], v[216:219], v[52:55]\n"
      "v_cvt_pk_bf16_f32 v26, v28, v29\n"
      "v_cvt_pk_bf16_f32 v27, v30, v31\n"
      "v_mfma_f32_16x16x32_bf16 v[56:59], a[32:35], v[216:219], v[56:59]\n"
      "ds_read_b128 v[228:231], v235 offset:20480\n"
      "v_cvt_pk_bf16_f32 v28, v32, v33\n"
      "v_cvt_pk_bf16_f32 v29, v34, v35\n"
      "global_load_dwordx4 a[8:11], v239, s[58:59] offset:2048 sc1\n"
      "v_mfma_f32_16x16x32_bf16 v[60:63], a[36:39], v[216:219], v[60:63]\n"
      "v_cvt_pk_bf16_f32 v30, v36, v37\n"
      "v_cvt_pk_bf16_f32 v31, v38, v39\n"
      "v_mfma_f32_16x16x32_bf16 v[64:67], a[40:43], v[216:219], v[64:67]\n"
      "v_cvt_pk_bf16_f32 v32, v40, v41\n"
      "v_cvt_pk_bf16_f32 v33, v42, v43\n"
      "v_mfma_f32_16x16x32_bf16 v[68:71], a[44:47], v[216:219], v[68:71]\n"
      "v_cvt_pk_bf16_f32 v34, v44, v45\n"
      "v_cvt_pk_bf16_f32 v35, v46, v47\n"
      "s_waitcnt lgkmcnt(2)\n"
      "v_mfma_f32_16x16x32_bf16 v[72:75], a[24:27], v[220:223], v[72:75]\n"
      "v_cvt_pk_bf16_f32 v48, v48, v49\n"
      "v_cvt_pk_bf16_f32 v49, v50, v51\n"
      "global_load_dwordx4 a[12:15], v239, s[58:59] offset:3072 sc1\n"
      "v_mfma_f32_16x16x32_bf16 v[76:79], a[28:31], v[220:223], v[76:79]\n"
      "v_cvt_pk_bf16_f32 v50, v52, v53\n"
      "v_cvt_pk_bf16_f32 v51, v54, v55\n"
      "v_mfma_f32_16x16x32_bf16 v[80:83], a[32:35], v[220:223], v[80:83]\n"
      "ds_read_b128 v[216:219], v235 offset:24576\n"
      "v_cvt_pk_bf16_f32 v52, v56, v57\n"
      "v_cvt_pk_bf16_f32 v53, v58, v59\n"
      "v_mfma_f32_16x16x32_bf16 v[84:87], a[36:39], v[220:223], v[84:87]\n"
      "v_cvt_pk_bf16_f32 v54, v60, v61\n"
      "v_cvt_pk_bf16_f32 v55, v62, v63\n"
      "v_mfma_f32_16x16x32_bf16 v[88:91], a[40:43], v[220:223], v[88:91]\n"
      "v_cvt_pk_bf16_f32 v56, v64, v65\n"
      "v_cvt_pk_bf16_f32 v57, v66, v67\n"
      "s_add_u32 s58, s24, 0xe000\n"
      "s_addc_u32 s59, s25, 0\n"
      "global_load_dwordx4 a[16:19], v239, s[58:59] offset:0 sc1\n"
      "v_mfma_f32_16x16x32_bf16 v[92:95], a[44:47], v[220:223], v[92:95]\n"
      "v_cvt_pk_bf16_f32 v58, v68, v69\n"
      "v_cvt_pk_bf16_f32 v59, v70, v71\n"
      "s_waitcnt lgkmcnt(2)\n"
      "v_mfma_f32_16x16x32_bf16 v[96:99], a[24:27], v[224:227], v[96:99]\n"
      "v_cvt_pk_bf16_f32 v72, v72, v73\n"
      "v_cvt_pk_bf16_f32 v73, v74, v75\n"
      "v_mfma_f32_16x16x32_bf16 v[100:103], a[28:31], v[224:227], v[100:103]\n"
      "v_cvt_pk_bf16_f32 v74, v76, v77\n"
      "v_cvt_pk_bf16_f32 v75, v78, v79\n"
      "v_mfma_f32_16x16x32_bf16 v[104:107], a[32:35], v[224:227], v[104:107]\n"
      "ds_read_b128 v[220:223], v235 offset:28672\n"
      "v_cvt_pk_bf16_f32 v76, v80, v81\n"
      "v_cvt_pk_bf16_f32 v77, v82, v83\n"
      "global_load_dwordx4 a[20:23], v239, s[58:59] offset:1024 sc1\n"
      "v_mfma_f32_16x16x32_bf16 v[108:111], a[36:39], v[224:227], v[108:111]\n"
      "v_cvt_pk_bf16_f32 v78, v84, v85\n"
      "v_cvt_pk_bf16_f32 v79, v86, v87\n"
      "v_mfma_f32_16x16x32_bf16 v[112:115], a[40:43], v[224:227], v[112:115]\n"
      "v_cvt_pk_bf16_f32 v80, v88, v89\n"
      "v_cvt_pk_bf16_f32 v81, v90, v91\n"
      "v_mfma_f32_16x16x32_bf16 v[116:119], a[44:47], v[224:227], v[116:119]\n"
      "v_cvt_pk_bf16_f32 v82, v92, v93\n"
      "v_cvt_pk_bf16_f32 v83, v94, v95\n"
      "s_waitcnt lgkmcnt(2)\n"
      "v_mfma_f32_16x16x32_bf16 v[120:123], a[24:27], v[228:231], v[120:123]\n"
      "v_cvt_pk_bf16_f32 v96, v96, v97\n"
      "v_cvt_pk_bf16_f32 v97, v98, v99\n"
      "v_mfma_f32_16x16x32_bf16 v[124:127], a[28:31], v[228:231], v[124:127]\n"
      "v_cvt_pk_bf16_f32 v98, v100, v101\n"
      "v_cvt_pk_bf16_f32 v99, v102, v103\n"
      "v_mfma_f32_16x16x32_bf16 v[128:131], a[32:35], v[228:231], v[128:131]\n"
      "ds_read_b128 v[224:227], v235 offset:32768\n"
      "v_cvt_pk_bf16_f32 v100, v104, v105\n"
      "v_cvt_pk_bf16_f32 v101, v106, v107\n"
      "v_mfma_f32_16x16x32_bf16 v[132:135], a[36:39], v[228:231], v[132:135]\n"
      "v_cvt_pk_bf16_f32 v102, v108, v109\n"
      "v_cvt_pk_bf16_f32 v103, v110, v111\n"
      "v_mfma_f32_16x16x32_bf16 v[136:139], a[40:43], v[228:231], v[136:139]\n"
      "v_cvt_pk_bf16_f32 v104, v112, v113\n"
      "v_cvt_pk_bf16_f32 v105, v114, v115\n"
      "v_mfma_f32_16x16x32_bf16 v[140:143], a[44:47], v[228:231], v[140:143]\n"
      "v_cvt_pk_bf16_f32 v106, v116, v117\n"
      "v_cvt_pk_bf16_f32 v107, v118, v119\n"
      "s_waitcnt lgkmcnt(2)\n"
      "v_mfma_f32_16x16x32_bf16 v[144:147], a[24:27], v[216:219], v[144:147]\n"
      "v_cvt_pk_bf16_f32 v120, v120, v121\n"
      "v_cvt_pk_bf16_f32 v121, v122, v123\n"
      "v_mfma_f32_16x16x32_bf16 v[148:151], a[28:31], v[216:219], v[148:151]\n"
      "v_cvt_pk_bf16_f32 v122, v124, v125\n"
      "v_cvt_pk_bf16_f32 v123, v126, v127\n"
      "v_mfma_f32_16x16x32_bf16 v[152:155], a[32:35], v[216:219], v[152:155]\n"
      "ds_read_b128 v[228:231], v235 offset:0\n"
      "v_add_u32 v235, v249, v235\n"
      "s_waitcnt vmcnt(24)\n"
      "s_barrier\n"
      "v_cvt_pk_bf16_f32 v124, v128, v129\n"
      "v_cvt_pk_bf16_f32 v125, v130, v131\n"
      "v_mfma_f32_16x16x32_bf16 v[156:159], a[36:39], v[216:219], v[156:159]\n"
      "ds_read_b128 v[36:39], v232 offset:0\n"
      "v_cvt_pk_bf16_f32 v126, v132, v133\n"
      "v_cvt_pk_bf16_f32 v127, v134, v135\n"
      "v_mfma_f32_16x16x32_bf16 v[160:163], a[40:43], v[216:219], v[160:163]\n"
      "v_cvt_pk_bf16_f32 v128, v136, v137\n"
      "v_cvt_pk_bf16_f32 v129, v138, v139\n"
      "v_mfma_f32_16x16x32_bf16 v[164:167], a[44:47], v[216:219], v[164:167]\n"
      "ds_read_b128 v[40:43], v233 offset:0\n"
      "v_cvt_pk_bf16_f32 v130, v140, v141\n"
      "v_cvt_pk_bf16_f32 v131, v142, v143\n"
      "s_waitcnt lgkmcnt(4)\n"
      "v_mfma_f32_16x16x32_bf16 v[168:171], a[24:27], v[220:223], v[168:171]\n"
      "v_cvt_pk_bf16_f32 v144, v144, v145\n"
      "v_cvt_pk_bf16_f32 v145, v146, v147\n"
      "global_load_dwordx4 v[140:143], v239, s[58:59] offset:2048 sc1\n"
      "v_mfma_f32_16x16x32_bf16 v[172:175], a[28:31], v[220:223], v[172:175]\n"
      "ds_read_b128 v[44:47], v234 offset:0\n"
      "v_cvt_pk_bf16_f32 v146, v148, v149\n"
      "v_cvt_pk_bf16_f32 v147, v150, v151\n"
      "v_mfma_f32_16x16x32_bf16 v[176:179], a[32:35], v[220:223], v[176:179]\n"
      "v_cvt_pk_bf16_f32 v148, v152, v153\n"
      "v_cvt_pk_bf16_f32 v149, v154, v155\n"
      "v_mfma_f32_16x16x32_bf16 v[180:183], a[36:39], v[220:223], v[180:183]\n"
      "ds_read_b128 v[60:63], v235 offset:0\n"
      "v_cvt_pk_bf16_f32 v150, v156, v157\n"
      "v_cvt_pk_bf16_f32 v151, v158, v159\n"
      "v_mfma_f32_16x16x32_bf16 v[184:187], a[40:43], v[220:223], v[184:187]\n"
      "v_cvt_pk_bf16_f32 v152, v160, v161\n"
      "v_cvt_pk_bf16_f32 v153, v162, v163\n"
      "v_mfma_f32_16x16x32_bf16 v[188:191], a[44:47], v[220:223], v[188:191]\n"
      "ds_read_b128 v[64:67], v232 offset:8192\n"
      "v_cvt_pk_bf16_f32 v154, v164, v165\n"
      "v_cvt_pk_bf16_f32 v155, v166, v167\n"
      "s_waitcnt lgkmcnt(6)\n"
      "v_mfma_f32_16x16x32_bf16 v[192:195], a[24:27], v[224:227], v[192:195]\n"
      "v_cvt_pk_bf16_f32 v168, v168, v169\n"
      "v_cvt_pk_bf16_f32 v169, v170, v171\n"
      "global_load_dwordx4 v[156:159], v239, s[58:59] offset:3072 sc1\n"
      "v_mfma_f32_16x16x32_bf16 v[196:199], a[28:31], v[224:227], v[196:199]\n"
      "ds_read_b128 v[68:71], v233 offset:8192\n"
      "v_cvt_pk_bf16_f32 v170, v172, v173\n"
      "v_cvt_pk_bf16_f32 v171, v174, v175\n"
      "v_mfma_f32_16x16x32_bf16 v[200:203], a[32:35], v[224:227], v[200:203]\n"
      "v_cvt_pk_bf16_f32 v172, v176, v177\n"
      "v_cvt_pk_bf16_f32 v173, v178, v179\n"
      "v_mfma_f32_16x16x32_bf16 v[204:207], a[36:39], v[224:227], v[204:207]\n"
      "ds_read_b128 v[84:87], v234 offset:8192\n"
      "v_cvt_pk_bf16_f32 v174, v180, v181\n"
      "v_cvt_pk_bf16_f32 v175, v182, v183\n"
      "v_mfma_f32_16x16x32_bf16 v[208:211], a[40:43], v[224:227], v[208:211]\n"
      "v_cvt_pk_bf16_f32 v176, v184, v185\n"
      "v_cvt_pk_bf16_f32 v177, v186, v187\n"
      "s_add_u32 s58, s24, 0xf000\n"
      "s_addc_u32 s59, s25, 0\n"
      "global_load_dwordx4 v[160:163], v239, s[58:59] offset:0 sc1\n"
      "v_mfma_f32_16x16x32_bf16 v[212:215], a[44:47], v[224:227], v[212:215]\n"
      "ds_read_b128 v[88:91], v235 offset:8192\n"
      "v_cvt_pk_bf16_f32 v178, v188, v189\n"
      "v_cvt_pk_bf16_f32 v179, v190, v191\n"
      "s_waitcnt lgkmcnt(8)\n"
      "v_mfma_f32_16x16x32_bf16 v[0:3], a[24:27], v[228:231], v[0:3]\n"
      "v_cvt_pk_bf16_f32 v192, v192, v193\n"
      "v_cvt_pk_bf16_f32 v193, v194, v195\n"
      "v_mfma_f32_16x16x32_bf16 v[4:7], a[28:31], v[228:231], v[4:7]\n"
      "ds_read_b128 v[92:95], v232 offset:16384\n"
      "v_cvt_pk_bf16_f32 v194, v196, v197\n"
      "v_cvt_pk_bf16_f32 v195, v198, v199\n"
      "v_mfma_f32_16x16x32_bf16 v[8:11], a[32:35], v[228:231], v[8:11]\n"
      "v_cvt_pk_bf16_f32 v196, v200, v201\n"
      "v_cvt_pk_bf16_f32 v197, v202, v203\n"
      "global_load_dwordx4 v[164:167], v239, s[58:59] offset:1024 sc1\n"
      "v_mfma_f32_16x16x32_bf16 v[12:15], a[36:39], v[228:231], v[12:15]\n"
      "ds_read_b128 v[108:111], v233 offset:16384\n"
      "v_cvt_pk_bf16_f32 v198, v204, v205\n"
      "v_cvt_pk_bf16_f32 v199, v206, v207\n"
      "v_mfma_f32_16x16x32_bf16 v[16:19], a[40:43], v[228:231], v[16:19]\n"
      "ds_read_b128 v[112:115], v234 offset:16384\n"
      "v_cvt_pk_bf16_f32 v200, v208, v209\n"
      "v_cvt_pk_bf16_f32 v201, v210, v211\n"
      "v_mfma_f32_16x16x32_bf16 v[20:23], a[44:47], v[228:231], v[20:23]\n"
      "ds_read_b128 v[116:119], v235 offset:16384\n"
      "v_cvt_pk_bf16_f32 v202, v212, v213\n"
      "v_cvt_pk_bf16_f32 v203, v214, v215\n"
      "s_waitcnt lgkmcnt(8)\n"
      "v_mfma_f32_16x16x32_bf16 v[132:135], a[240:243], v[36:39], v[0:3]\n"
      "global_load_dwordx4 v[180:183], v239, s[58:59] offset:2048 sc1\n"
      "s_mov_b64 vcc, 0\n"
      "v_mfma_f32_16x16x32_bf16 v[132:135], a[244:247], v[40:43], v[132:135]\n"
      "v_mfma_f32_16x16x32_bf16 v[132:135], a[248:251], v[44:47], v[132:135]\n"
      "v_mfma_f32_16x16x32_bf16 v[132:135], a[252:255], v[60:63], v[132:135]\n"
      "global_load_dwordx4 v[184:187], v239, s[58:59] offset:3072 sc1\n"
      "s_waitcnt lgkmcnt(4)\n"
      "v_mfma_f32_16x16x32_bf16 v[132:135], a[48:51], v[64:67], v[132:135]\n"
      "v_mfma_f32_16x16x32_bf16 v[132:135], a[52:55], v[68:71], v[132:135]\n"
      "v_mfma_f32_16x16x32_bf16 v[132:135], a[56:59], v[84:87], v[132:135]\n"
      "s_add_u32 s58, s24, 0x10000\n"
      "s_addc_u32 s59, s25, 0\n"
      "global_load_dwordx4 v[188:191], v239, s[58:59] offset:0 sc1\n"
      "v_mfma_f32_16x16x32_bf16 v[132:135], a[60:63], v[88:91], v[132:135]\n"
      "s_waitcnt lgkmcnt(0)\n"
      "v_mfma_f32_16x16x32_bf16 v[132:135], a[64:67], v[92:95], v[132:135]\n"
      "v_mfma_f32_16x16x32_bf16 v[132:135], a[68:71], v[108:111], v[132:135]\n"
      "global_load_dwordx4 v[204:207], v239, s[58:59] offset:1024 sc1\n"
      "v_mfma_f32_16x16x32_bf16 v[132:135], a[72:75], v[112:115], v[132:135]\n"
      "v_mfma_f32_16x16x32_bf16 v[132:135], a[76:79], v[116:119], v[132:135]\n"
      "v_mfma_f32_16x16x32_bf16 v[136:139], a[80:83], v[36:39], v[4:7]\n"
      "global_load_dwordx4 a[24:27], v239, s[58:59] offset:2048 sc1\n"
      "v_mfma_f32_16x16x32_bf16 v[136:139], a[84:87], v[40:43], v[136:139]\n"
      "v_mfma_f32_16x16x32_bf16 v[136:139], a[88:91], v[44:47], v[136:139]\n"
      "v_mfma_f32_16x16x32_bf16 v[136:139], a[92:95], v[60:63], v[136:139]\n"
      "global_load_dwordx4 a[28:31], v239, s[58:59] offset:3072 sc1\n"
      "v_mfma_f32_16x16x32_bf16 v[136:139], a[96:99], v[64:67], v[136:139]\n"
      "v_cvt_pk_bf16_f32 v208, v132, v133\n"
      "v_mfma_f32_16x16x32_bf16 v[136:139], a[100:103], v[68:71], v[136:139]\n"
      "v_cvt_pk_bf16_f32 v209, v134, v135\n"
      "v_mfma_f32_16x16x32_bf16 v[136:139], a[104:107], v[84:87], v[136:139]\n"
      "s_add_u32 s58, s24, 0x11000\n"
      "s_addc_u32 s59, s25, 0\n"
      "global_load_dwordx4 a[32:35], v239, s[58:59] offset:0 sc1\n"
      "v_mfma_f32_16x16x32_bf16 v[136:139], a[108:111], v[88:91], v[136:139]\n"
      "v_mfma_f32_16x16x32_bf16 v[136:139], a[112:115], v[92:95], v[136:139]\n"
      "v_mfma_f32_16x16x32_bf16 v[136:139], a[116:119], v[108:111], v[136:139]\n"
      "global_load_dwordx4 a[36:39], v239, s[58:59] offset:1024 sc1\n"
      "v_mfma_f32_16x16x32_bf16 v[136:139], a[120:123], v[112:115], v[136:139]\n"
      "ds_write_b64 v244, v[208:209] offset:12288\n"
      "v_mfma_f32_16x16x32_bf16 v[136:139], a[124:127], v[116:119], v[136:139]\n"
      "v_mfma_f32_16x16x32_bf16 v[132:135], a[128:131], v[36:39], v[8:11]\n"
      "global_load_dwordx4 a[40:43], v239, s[58:59] offset:2048 sc1\n"
      "v_mfma_f32_16x16x32_bf16 v[132:135], a[132:135], v[40:43], v[132:135]\n"
      "v_mfma_f32_16x16x32_bf16 v[132:135], a[136:139], v[44:47], v[132:135]\n"
      "v_mfma_f32_16x16x32_bf16 v[132:135], a[140:143], v[60:63], v[132:135]\n"
      "global_load_dwordx4 a[44:47], v239, s[58:59] offset:3072 sc1\n"
      "v_mfma_f32_16x16x32_bf16 v[132:135], a[144:147], v[64:67], v[132:135]\n"
      "v_cvt_pk_bf16_f32 v210, v136, v137\n"
      "v_mfma_f32_16x16x32_bf16 v[132:135], a[148:151], v[68:71], v[132:135]\n"
      "v_cvt_pk_bf16_f32 v211, v138, v139\n"
      "v_mfma_f32_16x16x32_bf16 v[132:135], a[152:155], v[84:87], v[132:135]\n"
      "s_mul_i32 s78, s90, s19\n"
      "v_mfma_f32_16x16x32_bf16 v[132:135], a[156:159], v[88:91], v[132:135]\n"
      "v_add_u32 v236, s78, v236\n"
      "v_mfma_f32_16x16x32_bf16 v[132:135], a[160:163], v[92:95], v[132:135]\n"
      "s_mul_i32 s78, s78, s99\n"
      "v_mfma_f32_16x16x32_bf16 v[132:135], a[164:167], v[108:111], v[132:135]\n"
      "v_add_u32 v237, s78, v237\n"
      "v_mfma_f32_16x16x32_bf16 v[132:135], a[168:171], v[112:115], v[132:135]\n"
      "s_add_u32 s52, s20, s33\n"
      "s_addc_u32 s53, s21, 0\n"
      "s_add_u32 m0, s17, 0xc000\n"
      "v_mfma_f32_16x16x32_bf16 v[132:135], a[172:175], v[116:119], v[132:135]\n"
      "global_load_lds_dwordx4 v236, s[52:53] offset:0 \n"
      "v_mfma_f32_16x16x32_bf16 v[136:139], a[176:179], v[36:39], v[12:15]\n"
      "ds_write_b64 v244, v[210:211] offset:12296\n"
      "v_mfma_f32_16x16x32_bf16 v[136:139], a[180:183], v[40:43], v[136:139]\n"
      "v_mfma_f32_16x16x32_bf16 v[136:139], a[184:187], v[44:47], v[136:139]\n"
      "v_mfma_f32_16x16x32_bf16 v[136:139], a[188:191], v[60:63], v[136:139]\n"
      "v_cvt_pk_bf16_f32 v212, v132, v133\n"
      "s_waitcnt vmcnt(35)\n"
      "v_mfma_f32_16x16x32_bf16 v[136:139], a[192:195], v[64:67], v[136:139]\n"
      "s_add_u32 m0, s17, 0xdf00\n"
      "v_mfma_f32_16x16x32_bf16 v[136:139], a[196:199], v[68:71], v[136:139]\n"
      "global_load_lds_dwordx4 v236, s[52:53] offset:256 \n"
      "v_mfma_f32_16x16x32_bf16 v[136:139], a[200:203], v[84:87], v[136:139]\n"
      "v_cvt_pk_bf16_f32 v213, v134, v135\n"
      "v_mfma_f32_16x16x32_bf16 v[136:139], a[204:207], v[88:91], v[136:139]\n"
      "s_waitcnt vmcnt(26)\n"
      "v_mfma_f32_16x16x32_bf16 v[136:139], a[208:211], v[92:95], v[136:139]\n"
      "v_mfma_f32_16x16x32_bf16 v[136:139], a[212:215], v[108:111], v[136:139]\n"
      "v_mfma_f32_16x16x32_bf16 v[136:139], a[216:219], v[112:115], v[136:139]\n"
      "s_add_u32 m0, s17, 0xfe00\n"
      "v_mfma_f32_16x16x32_bf16 v[136:139], a[220:223], v[116:119], v[136:139]\n"
      "global_load_lds_dwordx4 v236, s[52:53] offset:512 \n"
      "s_waitcnt vmcnt(23)\n"
      "v_mfma_f32_16x16x32_bf16 v[132:135], a[224:227], v[36:39], v[16:19]\n"
      "ds_write_b64 v245, v[212:213] offset:12288\n"
      "v_mfma_f32_16x16x32_bf16 v[132:135], a[228:231], v[40:43], v[132:135]\n"
      "v_mfma_f32_16x16x32_bf16 v[132:135], a[232:235], v[44:47], v[132:135]\n"
      "v_mfma_f32_16x16x32_bf16 v[132:135], a[236:239], v[60:63], v[132:135]\n"
      "v_cvt_pk_bf16_f32 v214, v136, v137\n"
      "s_waitcnt vmcnt(19)\n"
      "v_mfma_f32_16x16x32_bf16 v[132:135], a[0:3], v[64:67], v[132:135]\n"
      "s_add_u32 m0, s17, 0xd000\n"
      "v_mfma_f32_16x16x32_bf16 v[132:135], a[4:7], v[68:71], v[132:135]\n"
      "global_load_lds_dwordx4 v237, s[52:53] offset:0 \n"
      "v_mfma_f32_16x16x32_bf16 v[132:135], a[8:11], v[84:87], v[132:135]\n"
      "v_cvt_pk_bf16_f32 v215, v138, v139\n"
      "v_mfma_f32_16x16x32_bf16 v[132:135], a[12:15], v[88:91], v[132:135]\n"
      "s_waitcnt vmcnt(16)\n"
      "v_mfma_f32_16x16x32_bf16 v[132:135], a[16:19], v[92:95], v[132:135]\n"
      "v_mfma_f32_16x16x32_bf16 v[132:135], a[20:23], v[108:111], v[132:135]\n"
      "v_mfma_f32_16x16x32_bf16 v[132:135], v[140:143], v[112:115], v[132:135]\n"
      "s_add_u32 m0, s17, 0xef00\n"
      "v_mfma_f32_16x16x32_bf16 v[132:135], v[156:159], v[116:119], v[132:135]\n"
      "s_barrier\n"
      "global_load_lds_dwordx4 v237, s[52:53] offset:256 \n"
      "s_waitcnt vmcnt(13)\n"
      "v_mfma_f32_16x16x32_bf16 v[136:139], v[160:163], v[36:39], v[20:23]\n"
      "ds_read_b128 v[36:39], v232 offset:256\n"
      "ds_write_b64 v245, v[214:215] offset:12296\n"
      "v_mfma_f32_16x16x32_bf16 v[136:139], v[164:167], v[40:43], v[136:139]\n"
      "ds_read_b128 v[40:43], v233 offset:256\n"
      "v_mfma_f32_16x16x32_bf16 v[136:139], v[180:183], v[44:47], v[136:139]\n"
      "ds_read_b128 v[44:47], v234 offset:256\n"
      "v_mfma_f32_16x16x32_bf16 v[136:139], v[184:187], v[60:63], v[136:139]\n"
      "ds_read_b128 v[60:63], v235 offset:256\n"
      "v_cvt_pk_bf16_f32 v208, v132, v133\n"
      "s_waitcnt vmcnt(9)\n"
      "v_mfma_f32_16x16x32_bf16 v[136:139], v[188:191], v[64:67], v[136:139]\n"
      "ds_read_b128 v[64:67], v232 offset:8448\n"
      "s_add_u32 m0, s17, 0x10e00\n"
      "v_mfma_f32_16x16x32_bf16 v[136:139], v[204:207], v[68:71], v[136:139]\n"
      "ds_read_b128 v[68:71], v233 offset:8448\n"
      "global_load_lds_dwordx4 v237, s[52:53] offset:512 \n"
      "v_mfma_f32_16x16x32_bf16 v[136:139], a[24:27], v[84:87], v[136:139]\n"
      "ds_read_b128 v[84:87], v234 offset:8448\n"
      "v_cvt_pk_bf16_f32 v209, v134, v135\n"
      "v_mfma_f32_16x16x32_bf16 v[136:139], a[28:31], v[88:91], v[136:139]\n"
      "ds_read_b128 v[88:91], v235 offset:8448\n"
      "s_waitcnt vmcnt(6)\n"
      "v_mfma_f32_16x16x32_bf16 v[136:139], a[32:35], v[92:95], v[136:139]\n"
      "ds_read_b128 v[92:95], v232 offset:16640\n"
      "v_mfma_f32_16x16x32_bf16 v[136:139], a[36:39], v[108:111], v[136:139]\n"
      "ds_read_b128 v[108:111], v233 offset:16640\n"
      "v_mfma_f32_16x16x32_bf16 v[136:139], a[40:43], v[112:115], v[136:139]\n"
      "ds_read_b128 v[112:115], v234 offset:16640\n"
      "v_mfma_f32_16x16x32_bf16 v[136:139], a[44:47], v[116:119], v[136:139]\n"
      "ds_read_b128 v[116:119], v235 offset:16640\n"
      "s_waitcnt lgkmcnt(8)\n"
      "v_mfma_f32_16x16x32_bf16 v[132:135], a[240:243], v[36:39], v[0:3]\n"
      "s_add_u32 s52, s42, s33\n"
      "s_addc_u32 s53, s43, 0\n"
      "s_add_u32 m0, s17, 0x12000\n"
      "v_mfma_f32_16x16x32_bf16 v[132:135], a[244:247], v[40:43], v[132:135]\n"
      "global_load_lds_dwordx4 v236, s[52:53] offset:0 \n"
      "v_mfma_f32_16x16x32_bf16 v[132:135], a[248:251], v[44:47], v[132:135]\n"
      "ds_write_b64 v244, v[208:209] offset:12416\n"
      "v_mfma_f32_16x16x32_bf16 v[132:135], a[252:255], v[60:63], v[132:135]\n"
      "v_cvt_pk_bf16_f32 v210, v136, v137\n"
      "s_waitcnt lgkmcnt(5)\n"
      "v_mfma_f32_16x16x32_bf16 v[132:135], a[48:51], v[64:67], v[132:135]\n"
      "v_cvt_pk_bf16_f32 v211, v138, v139\n"
      "v_mfma_f32_16x16x32_bf16 v[132:135], a[52:55], v[68:71], v[132:135]\n"
      "v_mfma_f32_16x16x32_bf16 v[132:135], a[56:59], v[84:87], v[132:135]\n"
      "s_add_u32 m0, s17, 0x13f00\n"
      "v_mfma_f32_16x16x32_bf16 v[132:135], a[60:63], v[88:91], v[132:135]\n"
      "global_load_lds_dwordx4 v236, s[52:53] offset:256 \n"
      "s_waitcnt lgkmcnt(1)\n"
      "LPD3:\n"
      "s_cbranch_execz LPD9\n"
      "v_mfma_f32_16x16x32_bf16 v[132:135], a[64:67], v[92:95], v[132:135]\n"
      "v_mfma_f32_16x16x32_bf16 v[132:135], a[68:71], v[108:111], v[132:135]\n"
      "ds_write_b64 v244, v[210:211] offset:12424\n"
      "v_mfma_f32_16x16x32_bf16 v[132:135], a[72:75], v[112:115], v[132:135]\n"
      "v_mfma_f32_16x16x32_bf16 v[132:135], a[76:79], v[116:119], v[132:135]\n"
      "v_mfma_f32_16x16x32_bf16 v[136:139], a[80:83], v[36:39], v[4:7]\n"
      "s_add_u32 m0, s17, 0x15e00\n"
      "v_mfma_f32_16x16x32_bf16 v[136:139], a[84:87], v[40:43], v[136:139]\n"
      "global_load_lds_dwordx4 v236, s[52:53] offset:512 \n"
      "v_mfma_f32_16x16x32_bf16 v[136:139], a[88:91], v[44:47], v[136:139]\n"
      "v_mfma_f32_16x16x32_bf16 v[136:139], a[92:95], v[60:63], v[136:139]\n"
      "v_cvt_pk_bf16_f32 v208, v132, v133\n"
      "v_mfma_f32_16x16x32_bf16 v[136:139], a[96:99], v[64:67], v[136:139]\n"
      "v_cvt_pk_bf16_f32 v209, v134, v135\n"
      "v_mfma_f32_16x16x32_bf16 v[136:139], a[100:103], v[68:71], v[136:139]\n"
      "v_mfma_f32_16x16x32_bf16 v[136:139], a[104:107], v[84:87], v[136:139]\n"
      "s_add_u32 m0, s17, 0x13000\n"
      "v_mfma_f32_16x16x32_bf16 v[136:139], a[108:111], v[88:91], v[136:139]\n"
      "global_load_lds_dwordx4 v237, s[52:53] offset:0 \n"
      "v_mfma_f32_16x16x32_bf16 v[136:139], a[112:115], v[92:95], v[136:139]\n"
      "v_mfma_f32_16x16x32_bf16 v[136:139], a[116:119], v[108:111], v[136:139]\n"
      "ds_write_b64 v244, v[208:209] offset:24576\n"
      "v_mfma_f32_16x16x32_bf16 v[136:139], a[120:123], v[112:115], v[136:139]\n"
      "v_mfma_f32_16x16x32_bf16 v[136:139], a[124:127], v[116:119], v[136:139]\n"
      "v_mfma_f32_16x16x32_bf16 v[132:135], a[128:131], v[36:39], v[8:11]\n"
      "s_add_u32 m0, s17, 0x14f00\n"
      "v_mfma_f32_16x16x32_bf16 v[132:135], a[132:135], v[40:43], v[132:135]\n"
      "global_load_lds_dwordx4 v237, s[52:53] offset:256 \n"
      "v_mfma_f32_16x16x32_bf16 v[132:135], a[136:139], v[44:47], v[132:135]\n"
      "v_mfma_f32_16x16x32_bf16 v[132:135], a[140:143], v[60:63], v[132:135]\n"
      "v_cvt_pk_bf16_f32 v210, v136, v137\n"
      "v_mfma_f32_16x16x32_bf16 v[132:135], a[144:147], v[64:67], v[132:135]\n"
      "v_cvt_pk_bf16_f32 v211, v138, v139\n"
      "v_mfma_f32_16x16x32_bf16 v[132:135], a[148:151], v[68:71], v[132:135]\n"
      "v_mfma_f32_16x16x32_bf16 v[132:135], a[152:155], v[84:87], v[132:135]\n"
      "s_add_u32 m0, s17, 0x16e00\n"
      "v_mfma_f32_16x16x32_bf16 v[132:135], a[156:159], v[88:91], v[132:135]\n"
      "global_load_lds_dwordx4 v237, s[52:53] offset:512 \n"
      "v_mfma_f32_16x16x32_bf16 v[132:135], a[160:163], v[92:95], v[132:135]\n"
      "v_mfma_f32_16x16x32_bf16 v[132:135], a[164:167], v[108:111], v[132:135]\n"
      "ds_write_b64 v244, v[210:211] offset:24584\n"
      "v_mfma_f32_16x16x32_bf16 v[132:135], a[168:171], v[112:115], v[132:135]\n"
      "v_mfma_f32_16x16x32_bf16 v[132:135], a[172:175], v[116:119], v[132:135]\n"
      "v_mfma_f32_16x16x32_bf16 v[136:139], a[176:179], v[36:39], v[12:15]\n"
      "v_mfma_f32_16x16x32_bf16 v[136:139], a[180:183], v[40:43], v[136:139]\n"
      "v_mfma_f32_16x16x32_bf16 v[136:139], a[184:187], v[44:47], v[136:139]\n"
      "v_mfma_f32_16x16x32_bf16 v[136:139], a[188:191], v[60:63], v[136:139]\n"
      "v_cvt_pk_bf16_f32 v212, v132, v133\n"
      "v_mfma_f32_16x16x32_bf16 v[136:139], a[192:195], v[64:67], v[136:139]\n"
      "v_cvt_pk_bf16_f32 v213, v134, v135\n"
      "v_mfma_f32_16x16x32_bf16 v[136:139], a[196:199], v[68:71], v[136:139]\n"
      "v_mfma_f32_16x16x32_bf16 v[136:139], a[200:203], v[84:87], v[136:139]\n"
      "v_mfma_f32_16x16x32_bf16 v[136:139], a[204:207], v[88:91], v[136:139]\n"
      "v_mfma_f32_16x16x32_bf16 v[136:139], a[208:211], v[92:95], v[136:139]\n"
      "v_mfma_f32_16x16x32_bf16 v[136:139], a[212:215], v[108:111], v[136:139]\n"
      "ds_write_b64 v245, v[212:213] offset:24576\n"
      "v_mfma_f32_16x16x32_bf16 v[136:139], a[216:219], v[112:115], v[136:139]\n"
      "v_mfma_f32_16x16x32_bf16 v[136:139], a[220:223], v[116:119], v[136:139]\n"
      "v_mfma_f32_16x16x32_bf16 v[132:135], a[224:227], v[36:39], v[16:19]\n"
      "v_mfma_f32_16x16x32_bf16 v[132:135], a[228:231], v[40:43], v[132:135]\n"
      "v_mfma_f32_16x16x32_bf16 v[132:135], a[232:235], v[44:47], v[132:135]\n"
      "v_mfma_f32_16x16x32_bf16 v[132:135], a[236:239], v[60:63], v[132:135]\n"
      "v_cvt_pk_bf16_f32 v214, v136, v137\n"
      "v_mfma_f32_16x16x32_bf16 v[132:135], a[0:3], v[64:67], v[132:135]\n"
      "v_cvt_pk_bf16_f32 v215, v138, v139\n"
      "v_mfma_f32_16x16x32_bf16 v[132:135], a[4:7], v[68:71], v[132:135]\n"
      "v_mfma_f32_16x16x32_bf16 v[132:135], a[8:11], v[84:87], v[132:135]\n"
      "v_mfma_f32_16x16x32_bf16 v[132:135], a[12:15], v[88:91], v[132:135]\n"
      "v_mfma_f32_16x16x32_bf16 v[132:135], a[16:19], v[92:95], v[132:135]\n"
      "v_mfma_f32_16x16x32_bf16 v[132:135], a[20:23], v[108:111], v[132:135]\n"
      "ds_write_b64 v245, v[214:215] offset:24584\n"
      "v_mfma_f32_16x16x32_bf16 v[132:135], v[140:143], v[112:115], v[132:135]\n"
      "s_lshl_b32 s79, s98, 1\n"
      "v_mfma_f32_16x16x32_bf16 v[132:135], v[156:159], v[116:119], v[132:135]\n"
      "s_waitcnt lgkmcnt(4)\n"
      "s_barrier\n"
      "v_mad_i32_i24 v252, v251, s79, v252\n"
      "v_mfma_f32_16x16x32_bf16 v[136:139], v[160:163], v[36:39], v[20:23]\n"
      "ds_read_b128 v[36:39], v232 offset:24576\n"
      "s_mul_i32 s79, s79, s100\n"
      "v_mfma_f32_16x16x32_bf16 v[136:139], v[164:167], v[40:43], v[136:139]\n"
      "ds_read_b128 v[40:43], v233 offset:24576\n"
      "v_add_u32 v247, s79, v247\n"
      "v_mfma_f32_16x16x32_bf16 v[136:139], v[180:183], v[44:47], v[136:139]\n"
      "ds_read_b128 v[44:47], v234 offset:24576\n"
      "s_mul_i32 s79, s79, s101\n"
      "v_mfma_f32_16x16x32_bf16 v[136:139], v[184:187], v[60:63], v[136:139]\n"
      "ds_read_b128 v[60:63], v235 offset:24576\n"
      "v_cvt_pk_bf16_f32 v208, v132, v133\n"
      "v_mfma_f32_16x16x32_bf16 v[136:139], v[188:191], v[64:67], v[136:139]\n"
      "ds_read_b128 v[64:67], v232 offset:32768\n"
      "v_cvt_pk_bf16_f32 v209, v134, v135\n"
      "v_mfma_f32_16x16x32_bf16 v[136:139], v[204:207], v[68:71], v[136:139]\n"
      "ds_read_b128 v[68:71], v233 offset:32768\n"
      "v_add_u32 v253, s79, v253\n"
      "v_mfma_f32_16x16x32_bf16 v[136:139], a[24:27], v[84:87], v[136:139]\n"
      "ds_read_b128 v[84:87], v234 offset:32768\n"
      "v_mfma_f32_16x16x32_bf16 v[136:139], a[28:31], v[88:91], v[136:139]\n"
      "ds_read_b128 v[88:91], v235 offset:32768\n"
      "v_mfma_f32_16x16x32_bf16 v[136:139], a[32:35], v[92:95], v[136:139]\n"
      "ds_read_b128 v[92:95], v232 offset:40960\n"
      "v_mfma_f32_16x16x32_bf16 v[136:139], a[36:39], v[108:111], v[136:139]\n"
      "ds_read_b128 v[108:111], v233 offset:40960\n"
      "v_mfma_f32_16x16x32_bf16 v[136:139], a[40:43], v[112:115], v[136:139]\n"
      "ds_read_b128 v[112:115], v234 offset:40960\n"
      "v_mfma_f32_16x16x32_bf16 v[136:139], a[44:47], v[116:119], v[136:139]\n"
      "ds_read_b128 v[116:119], v235 offset:40960\n"
      "s_branch LJOIN\n"
      "LF_1:\n"
      "s_mul_i32 s62, s62, 0x3000\n"
      "s_sub_u32 s62, s98, s62\n"
      "s_sub_u32 s62, s62, s89\n"
      "v_mad_i32_i24 v238, v251, s62, v238\n"
      "v_mad_i32_i24 v238, v251, s98, v238\n"
      "s_add_u32 s56, s22, 0x0\n"
      "s_addc_u32 s57, s23, 0\n"
      "s_add_u32 m0, s18, 0x0\n"
      "s_nop 0\n"
      "global_load_lds_dwordx4 v238, s[56:57] offset:0 \n"
      "v_mad_i32_i24 v238, v251, s90, v238\n"
      "s_add_u32 s56, s22, 0x6000\n"
      "s_addc_u32 s57, s23, 0\n"
      "s_add_u32 m0, s18, 0x1000\n"
      "s_nop 0\n"
      "global_load_lds_dwordx4 v238, s[56:57] offset:0 \n"
      "v_mad_i32_i24 v238, v251, s91, v238\n"
      "s_add_u32 s56, s22, 0xc000\n"
      "s_addc_u32 s57, s23, 0\n"
      "s_add_u32 m0, s18, 0x2000\n"
      "s_nop 0\n"
      "global_load_lds_dwordx4 v238, s[56:57] offset:0 \n"
      "v_mad_i32_i24 v238, v251, s92, v238\n"
      "s_add_u32 s56, s22, 0x12000\n"
      "s_addc_u32 s57, s23, 0\n"
      "s_add_u32 m0, s18, 0x3000\n"
      "s_nop 0\n"
      "global_load_lds_dwordx4 v238, s[56:57] offset:0 \n"
      "v_mad_i32_i24 v238, v251, s93, v238\n"
      "s_add_u32 s56, s22, 0x18000\n"
      "s_addc_u32 s57, s23, 0\n"
      "s_add_u32 m0, s18, 0x4000\n"
      "s_nop 0\n"
      "global_load_lds_dwordx4 v238, s[56:57] offset:0 \n"
      "v_mad_i32_i24 v238, v251, s94, v238\n"
      "s_add_u32 s56, s22, 0x1e000\n"
      "s_addc_u32 s57, s23, 0\n"
      "s_add_u32 m0, s18, 0x5000\n"
      "s_nop 0\n"
      "global_load_lds_dwordx4 v238, s[56:57] offset:0 \n"
      "v_mad_i32_i24 v238, v251, s95, v238\n"
      "s_add_u32 s56, s22, 0x24000\n"
      "s_addc_u32 s57, s23, 0\n"
      "s_add_u32 m0, s18, 0x6000\n"
      "s_nop 0\n"
      "global_load_lds_dwordx4 v238, s[56:57] offset:0 \n"
      "v_mad_i32_i24 v238, v251, s96, v238\n"
      "s_add_u32 s56, s22, 0x2a000\n"
      "s_addc_u32 s57, s23, 0\n"
      "s_add_u32 m0, s18, 0x7000\n"
      "s_nop 0\n"
      "global_load_lds_dwordx4 v238, s[56:57] offset:0 \n"
      "v_mad_i32_i24 v238, v251, s97, v238\n"
      "s_add_u32 s56, s22, 0x30000\n"
      "s_addc_u32 s57, s23, 0\n"
      "s_add_u32 m0, s18, 0x8000\n"
      "s_nop 0\n"
      "global_load_lds_dwordx4 v238, s[56:57] offset:0 \n"
      "s_branch LR_1\n"
      "LV1:\n"
      "s_add_u32 s78, s64, 0x0\n"
      "s_addc_u32 s79, s65, 0\n"
      "s_add_u32 m0, s74, 0x0\n"
      "s_nop 0\n"
      "global_load_lds_dwordx4 v240, s[78:79]\n"
      "s_add_u32 m0, s74, 0x400\n"
      "s_nop 0\n"
      "global_load_lds_dwordx4 v241, s[78:79]\n"
      "s_add_u32 m0, s74, 0x800\n"
      "s_nop 0\n"
      "global_load_lds_dwordx4 v242, s[78:79]\n"
      "s_add_u32 s78, s64, 0x3000\n"
      "s_addc_u32 s79, s65, 0\n"
      "s_add_u32 m0, s74, 0xc00\n"
      "s_nop 0\n"
      "global_load_lds_dwordx4 v240, s[78:79]\n"
      "s_add_u32 m0, s74, 0x1000\n"
      "s_nop 0\n"
      "global_load_lds_dwordx4 v241, s[78:79]\n"
      "s_add_u32 m0, s74, 0x1400\n"
      "s_nop 0\n"
      "global_load_lds_dwordx4 v242, s[78:79]\n"
      "s_getpc_b64 s[56:57]\n"
      "LTR_1:\n"
      "s_add_u32 s56, s56, LTE_1-LTR_1\n"
      "s_addc_u32 s57, s57, 0\n"
      "s_mov_b64 exec, 0\n"
      "s_cmp_eq_u32 s76, 3\n"
      "s_cbranch_scc1 LPD6\n"
      "s_cmp_eq_u32 s76, 2\n"
      "s_cbranch_scc1 LPD5\n"
      "s_cmp_eq_u32 s76, 1\n"
      "s_cbranch_scc1 LPD4\n"
      "LTE_1:\n"
      "s_mov_b64 exec, -1\n"
      "s_waitcnt vmcnt(17)\n"
      "v_readfirstlane_b32 s80, v252\n"
      "v_readfirstlane_b32 s81, v253\n"
      "s_nop 3\n"
      "s_add_u32 s80, s80, 0x7f4a7c15\n"
      "s_addc_u32 s81, s81, 0x9e3779b9\n"
      "v_mov_b32 v252, s80\n"
      "v_mov_b32 v253, s81\n"
      "v_xor_b32 v116, v112, v110\n"
      "v_xor_b32 v117, v113, v111\n"
      "v_and_or_b32 v116, v116, -2, v117\n"
      "v_cmp_eq_u32 vcc, 0, v116\n"
      "v_cndmask_b32 v116, v110, v112, vcc\n"
      "s_mov_b64 exec, 1\n"
      "ds_write_b32 v108, v116\n"
      "s_mov_b64 exec, -1\n"
      "v_and_b32 v108, -16, v108\n"
      "s_waitcnt lgkmcnt(0)\n"
      "s_barrier\n"
      "ds_read_b32 v116, v108\n"
      "s_waitcnt lgkmcnt(0)\n"
      "v_readfirstlane_b32 s62, v116\n"
      "s_lshr_b32 s63, s51, 2\n"
      "s_nop 2\n"
      "s_xor_b32 s62, s62, s63\n"
      "s_and_b32 s62, s62, 1\n"
      "s_mul_i32 s63, s62, 0x3000\n"
      "s_sub_u32 s98, s98, s63\n"
      "s_lshl_b32 s62, s62, 1\n"
      "s_sub_u32 s62, 1, s62\n"
      "v_mul_i32_i24 v251, s62, v251\n"
      "s_lshl_b32 s19, s62, 1\n"
      "s_mul_i32 s100, s100, s62\n"
      "v_cmp_ne_u32 vcc, v107, v251\n"
      "s_nop 3\n"
      "s_cbranch_vccnz LF_1\n"
      "LR_1:\n"
      "s_nop 0\n"
      "s_add_u32 s78, s64, 0x6000\n"
      "s_addc_u32 s79, s65, 0\n"
      "s_add_u32 m0, s74, 0x6000\n"
      "s_nop 0\n"
      "global_load_lds_dwordx4 v240, s[78:79]\n"
      "s_add_u32 m0, s74, 0x6400\n"
      "s_nop 0\n"
      "global_load_lds_dwordx4 v241, s[78:79]\n"
      "s_add_u32 m0, s74, 0x6800\n"
      "s_nop 0\n"
      "global_load_lds_dwordx4 v242, s[78:79]\n"
      "s_add_u32 s78, s64, 0x9000\n"
      "s_addc_u32 s79, s65, 0\n"
      "s_add_u32 m0, s74, 0x6c00\n"
      "s_nop 0\n"
      "global_load_lds_dwordx4 v240, s[78:79]\n"
      "s_add_u32 m0, s74, 0x7000\n"
      "s_nop 0\n"
      "global_load_lds_dwordx4 v241, s[78:79]\n"
      "s_add_u32 m0, s74, 0x7400\n"
      "s_nop 0\n"
      "global_load_lds_dwordx4 v242, s[78:79]\n"
      "s_add_u32 s78, s64, 0xc000\n"
      "s_addc_u32 s79, s65, 0\n"
      "s_add_u32 m0, s74, 0xc000\n"
      "s_nop 0\n"
      "global_load_lds_dwordx4 v240, s[78:79]\n"
      "s_add_u32 m0, s74, 0xc400\n"
      "s_nop 0\n"
      "global_load_lds_dwordx4 v241, s[78:79]\n"
      "s_add_u32 m0, s74, 0xc800\n"
      "s_nop 0\n"
      "global_load_lds_dwordx4 v242, s[78:79]\n"
      "s_add_u32 s78, s64, 0xf000\n"
      "s_addc_u32 s79, s65, 0\n"
      "s_add_u32 m0, s74, 0xcc00\n"
      "s_nop 0\n"
      "global_load_lds_dwordx4 v240, s[78:79]\n"
      "s_add_u32 m0, s74, 0xd000\n"
      "s_nop 0\n"
      "global_load_lds_dwordx4 v241, s[78:79]\n"
      "s_add_u32 m0, s74, 0xd400\n"
      "s_nop 0\n"
      "global_load_lds_dwordx4 v242, s[78:79]\n"
      "s_add_u32 m0, s17, 0x25000\n"
      "s_nop 0\n"
      "global_load_lds_dwordx4 v239, s[46:47] offset:0\n"
      "s_add_u32 m0, s17, 0x25800\n"
      "s_nop 0\n"
      "global_load_lds_dwordx4 v239, s[46:47] offset:2048\n"
      "s_waitcnt vmcnt(14)\n"
      "ds_read_b64_tr_b16 a[0:1], v243 offset:0\n"
      "ds_read_b64_tr_b16 a[2:3], v243 offset:768\n"
      "ds_read_b64_tr_b16 a[4:5], v243 offset:8\n"
      "ds_read_b64_tr_b16 a[6:7], v243 offset:776\n"
      "ds_read_b64_tr_b16 a[8:9], v243 offset:64\n"
      "ds_read_b64_tr_b16 a[10:11], v243 offset:832\n"
      "ds_read_b64_tr_b16 a[12:13], v243 offset:72\n"
      "ds_read_b64_tr_b16 a[14:15], v243 offset:840\n"
      "ds_read_b64_tr_b16 a[16:17], v243 offset:128\n"
      "ds_read_b64_tr_b16 a[18:19], v243 offset:896\n"
      "ds_read_b64_tr_b16 a[20:21], v243 offset:136\n"
      "ds_read_b64_tr_b16 a[22:23], v243 offset:904\n"
      "s_waitcnt vmcnt(13)\n"
      "s_barrier\n"
      "ds_read_b128 v[216:219], v232 offset:0\n"
      "ds_read_b128 v[220:223], v232 offset:4096\n"
      "ds_read_b128 v[224:227], v232 offset:8192\n"
      "s_waitcnt lgkmcnt(2)\n"
      "v_mfma_f32_16x16x32_bf16 v[0:3], a[0:3], v[216:219], 0\n"
      "s_add_u32 s78, s64, 0x12000\n"
      "s_addc_u32 s79, s65, 0\n"
      "s_add_u32 m0, s74, 0x0\n"
      "v_mfma_f32_16x16x32_bf16 v[4:7], a[4:7], v[216:219], 0\n"
      "global_load_lds_dwordx4 v240, s[78:79]\n"
      "v_mfma_f32_16x16x32_bf16 v[8:11], a[8:11], v[216:219], 0\n"
      "ds_read_b128 v[228:231], v232 offset:12288\n"
      "v_mfma_f32_16x16x32_bf16 v[12:15], a[12:15], v[216:219], 0\n"
      "v_mfma_f32_16x16x32_bf16 v[16:19], a[16:19], v[216:219], 0\n"
      "s_add_u32 m0, s74, 0x400\n"
      "v_mfma_f32_16x16x32_bf16 v[20:23], a[20:23], v[216:219], 0\n"
      "global_load_lds_dwordx4 v241, s[78:79]\n"
      "s_waitcnt lgkmcnt(2)\n"
      "v_mfma_f32_16x16x32_bf16 v[24:27], a[0:3], v[220:223], 0\n"
      "s_waitcnt vmcnt(10)\n"
      "s_cmp_lg_u32 s76, 1\n"
      "s_cbranch_scc1 LA_1\n"
      "v_mov_b32 v244, s77\n"
      "s_mov_b64 exec, 1\n"
      "global_store_dwordx2 v244, v[252:253], s[28:29] sc1\n"
      "s_mov_b64 exec, -1\n"
      "s_nop 4\n"
      "LA_1:\n"
      "v_mfma_f32_16x16x32_bf16 v[28:31], a[4:7], v[220:223], 0\n"
      "ds_read_b64_tr_b16 a[24:25], v243 offset:24576\n"
      "v_mfma_f32_16x16x32_bf16 v[32:35], a[8:11], v[220:223], 0\n"
      "ds_read_b128 v[216:219], v232 offset:16384\n"
      "s_add_u32 m0, s74, 0x800\n"
      "v_mfma_f32_16x16x32_bf16 v[36:39], a[12:15], v[220:223], 0\n"
      "ds_read_b64_tr_b16 a[26:27], v243 offset:25344\n"
      "global_load_lds_dwordx4 v242, s[78:79]\n"
      "v_mfma_f32_16x16x32_bf16 v[40:43], a[16:19], v[220:223], 0\n"
      "ds_read_b64_tr_b16 a[28:29], v243 offset:24584\n"
      "v_mfma_f32_16x16x32_bf16 v[44:47], a[20:23], v[220:223], 0\n"
      "s_waitcnt lgkmcnt(5)\n"
      "v_mfma_f32_16x16x32_bf16 v[48:51], a[0:3], v[224:227], 0\n"
      "ds_read_b64_tr_b16 a[30:31], v243 offset:25352\n"
      "s_add_u32 s78, s64, 0x15000\n"
      "s_addc_u32 s79, s65, 0\n"
      "s_add_u32 m0, s74, 0xc00\n"
      "v_mfma_f32_16x16x32_bf16 v[52:55], a[4:7], v[224:227], 0\n"
      "ds_read_b64_tr_b16 a[32:33], v243 offset:24640\n"
      "global_load_lds_dwordx4 v240, s[78:79]\n"
      "v_mfma_f32_16x16x32_bf16 v[56:59], a[8:11], v[224:227], 0\n"
      "ds_read_b128 v[220:223], v232 offset:20480\n"
      "v_mfma_f32_16x16x32_bf16 v[60:63], a[12:15], v[224:227], 0\n"
      "ds_read_b64_tr_b16 a[34:35], v243 offset:25408\n"
      "v_mfma_f32_16x16x32_bf16 v[64:67], a[16:19], v[224:227], 0\n"
      "ds_read_b64_tr_b16 a[36:37], v243 offset:24648\n"
      "s_add_u32 m0, s74, 0x1000\n"
      "v_mfma_f32_16x16x32_bf16 v[68:71], a[20:23], v[224:227], 0\n"
      "global_load_lds_dwordx4 v241, s[78:79]\n"
      "s_waitcnt lgkmcnt(9)\n"
      "v_mfma_f32_16x16x32_bf16 v[72:75], a[0:3], v[228:231], 0\n"
      "ds_read_b64_tr_b16 a[38:39], v243 offset:25416\n"
      "v_mfma_f32_16x16x32_bf16 v[76:79], a[4:7], v[228:231], 0\n"
      "ds_read_b64_tr_b16 a[40:41], v243 offset:24704\n"
      "v_mfma_f32_16x16x32_bf16 v[80:83], a[8:11], v[228:231], 0\n"
      "ds_read_b128 v[224:227], v232 offset:24576\n"
      "s_add_u32 m0, s74, 0x1400\n"
      "v_mfma_f32_16x16x32_bf16 v[84:87], a[12:15], v[228:231], 0\n"
      "ds_read_b64_tr_b16 a[42:43], v243 offset:25472\n"
      "global_load_lds_dwordx4 v242, s[78:79]\n"
      "v_mfma_f32_16x16x32_bf16 v[88:91], a[16:19], v[228:231], 0\n"
      "ds_read_b64_tr_b16 a[44:45], v243 offset:24712\n"
      "v_mfma_f32_16x16x32_bf16 v[92:95], a[20:23], v[228:231], 0\n"
      "s_waitcnt lgkmcnt(12)\n"
      "v_mfma_f32_16x16x32_bf16 v[96:99], a[0:3], v[216:219], 0\n"
      "ds_read_b64_tr_b16 a[46:47], v243 offset:25480\n"
      "v_mfma_f32_16x16x32_bf16 v[100:103], a[4:7], v[216:219], 0\n"
      "s_waitcnt vmcnt(8)\n"
      "v_mfma_f32_16x16x32_bf16 v[104:107], a[8:11], v[216:219], 0\n"
      "ds_read_b128 v[228:231], v232 offset:28672\n"
      "v_mfma_f32_16x16x32_bf16 v[108:111], a[12:15], v[216:219], 0\n"
      "ds_read_b64_tr_b16 a[48:49], v243 offset:49152\n"
      "v_mfma_f32_16x16x32_bf16 v[112:115], a[16:19], v[216:219], 0\n"
      "ds_read_b64_tr_b16 a[50:51], v243 offset:49920\n"
      "v_mfma_f32_16x16x32_bf16 v[116:119], a[20:23], v[216:219], 0\n"
      "s_waitcnt lgkmcnt(11)\n"
      "v_mfma_f32_16x16x32_bf16 v[120:123], a[0:3], v[220:223], 0\n"
      "ds_read_b64_tr_b16 a[52:53], v243 offset:49160\n"
      "v_mfma_f32_16x16x32_bf16 v[124:127], a[4:7], v[220:223], 0\n"
      "ds_read_b64_tr_b16 a[54:55], v243 offset:49928\n"
      "v_mfma_f32_16x16x32_bf16 v[128:131], a[8:11], v[220:223], 0\n"
      "ds_read_b128 v[216:219], v232 offset:32768\n"
      "v_add_u32 v232, 0x9000, v232\n"
      "v_mfma_f32_16x16x32_bf16 v[132:135], a[12:15], v[220:223], 0\n"
      "ds_read_b64_tr_b16 a[56:57], v243 offset:49216\n"
      "v_mfma_f32_16x16x32_bf16 v[136:139], a[16:19], v[220:223], 0\n"
      "ds_read_b64_tr_b16 a[58:59], v243 offset:49984\n"
      "s_waitcnt lgkmcnt(8)\n"
      "s_add_u32 s78, s64, 0x18000\n"
      "s_addc_u32 s79, s65, 0\n"
      "s_add_u32 m0, s74, 0x6000\n"
      "v_mfma_f32_16x16x32_bf16 v[140:143], a[20:23], v[220:223], 0\n"
      "global_load_lds_dwordx4 v240, s[78:79]\n"
      "v_mfma_f32_16x16x32_bf16 v[144:147], a[0:3], v[224:227], 0\n"
      "ds_read_b64_tr_b16 a[60:61], v243 offset:49224\n"
      "v_mfma_f32_16x16x32_bf16 v[148:151], a[4:7], v[224:227], 0\n"
      "ds_read_b64_tr_b16 a[62:63], v243 offset:49992\n"
      "v_mfma_f32_16x16x32_bf16 v[152:155], a[8:11], v[224:227], 0\n"
      "ds_read_b128 v[220:223], v233 offset:0\n"
      "s_add_u32 m0, s74, 0x6400\n"
      "v_mfma_f32_16x16x32_bf16 v[156:159], a[12:15], v[224:227], 0\n"
      "ds_read_b64_tr_b16 a[64:65], v243 offset:49280\n"
      "global_load_lds_dwordx4 v241, s[78:79]\n"
      "v_mfma_f32_16x16x32_bf16 v[160:163], a[16:19], v[224:227], 0\n"
      "ds_read_b64_tr_b16 a[66:67], v243 offset:50048\n"
      "v_mfma_f32_16x16x32_bf16 v[164:167], a[20:23], v[224:227], 0\n"
      "s_waitcnt lgkmcnt(12)\n"
      "v_mfma_f32_16x16x32_bf16 v[168:171], a[0:3], v[228:231], 0\n"
      "ds_read_b64_tr_b16 a[68:69], v243 offset:49288\n"
      "s_add_u32 m0, s74, 0x6800\n"
      "v_mfma_f32_16x16x32_bf16 v[172:175], a[4:7], v[228:231], 0\n"
      "ds_read_b64_tr_b16 a[70:71], v243 offset:50056\n"
      "global_load_lds_dwordx4 v242, s[78:79]\n"
      "v_mfma_f32_16x16x32_bf16 v[176:179], a[8:11], v[228:231], 0\n"
      "ds_read_b128 v[224:227], v233 offset:4096\n"
      "v_mfma_f32_16x16x32_bf16 v[180:183], a[12:15], v[228:231], 0\n"
      "v_mfma_f32_16x16x32_bf16 v[184:187], a[16:19], v[228:231], 0\n"
      "s_add_u32 s78, s64, 0x1b000\n"
      "s_addc_u32 s79, s65, 0\n"
      "s_add_u32 m0, s74, 0x6c00\n"
      "v_mfma_f32_16x16x32_bf16 v[188:191], a[20:23], v[228:231], 0\n"
      "global_load_lds_dwordx4 v240, s[78:79]\n"
      "s_waitcnt lgkmcnt(10)\n"
      "v_mfma_f32_16x16x32_bf16 v[192:195], a[0:3], v[216:219], 0\n"
      "v_mfma_f32_16x16x32_bf16 v[196:199], a[4:7], v[216:219], 0\n"
      "v_mfma_f32_16x16x32_bf16 v[200:203], a[8:11], v[216:219], 0\n"
      "ds_read_b128 v[228:231], v233 offset:8192\n"
      "s_add_u32 m0, s74, 0x7000\n"
      "v_mfma_f32_16x16x32_bf16 v[204:207], a[12:15], v[216:219], 0\n"
      "global_load_lds_dwordx4 v241, s[78:79]\n"
      "v_mfma_f32_16x16x32_bf16 v[208:211], a[16:19], v[216:219], 0\n"
      "v_mfma_f32_16x16x32_bf16 v[212:215], a[20:23], v[216:219], 0\n"
      "s_waitcnt lgkmcnt(6)\n"
      "v_mfma_f32_16x16x32_bf16 v[0:3], a[24:27], v[220:223], v[0:3]\n"
      "s_add_u32 m0, s74, 0x7400\n"
      "v_mfma_f32_16x16x32_bf16 v[4:7], a[28:31], v[220:223], v[4:7]\n"
      "global_load_lds_dwordx4 v242, s[78:79]\n"
      "v_mfma_f32_16x16x32_bf16 v[8:11], a[32:35], v[220:223], v[8:11]\n"
      "ds_read_b128 v[216:219], v233 offset:12288\n"
      "s_waitcnt vmcnt(6)\n"
      "v_mfma_f32_16x16x32_bf16 v[12:15], a[36:39], v[220:223], v[12:15]\n"
      "ds_read_b64_tr_b16 a[72:73], v243 offset:0\n"
      "v_mfma_f32_16x16x32_bf16 v[16:19], a[40:43], v[220:223], v[16:19]\n"
      "ds_read_b64_tr_b16 a[74:75], v243 offset:768\n"
      "v_mad_i32_i24 v238, v251, s89, v238\n"
      "s_add_u32 s56, s22, 0x0\n"
      "s_addc_u32 s57, s23, 0\n"
      "s_add_u32 m0, s18, 0x8f00\n"
      "v_mfma_f32_16x16x32_bf16 v[20:23], a[44:47], v[220:223], v[20:23]\n"
      "global_load_lds_dwordx4 v238, s[56:57] offset:256 \n"
      "s_waitcnt lgkmcnt(4)\n"
      "v_mfma_f32_16x16x32_bf16 v[24:27], a[24:27], v[224:227], v[24:27]\n"
      "ds_read_b64_tr_b16 a[76:77], v243 offset:8\n"
      "v_mfma_f32_16x16x32_bf16 v[28:31], a[28:31], v[224:227], v[28:31]\n"
      "ds_read_b64_tr_b16 a[78:79], v243 offset:776\n"
      "v_mad_i32_i24 v238, v251, s90, v238\n"
      "s_add_u32 s56, s22, 0x6000\n"
      "s_addc_u32 s57, s23, 0\n"
      "s_add_u32 m0, s18, 0x9f00\n"
      "v_mfma_f32_16x16x32_bf16 v[32:35], a[32:35], v[224:227], v[32:35]\n"
      "ds_read_b128 v[220:223], v233 offset:16384\n"
      "global_load_lds_dwordx4 v238, s[56:57] offset:256 \n"
      "v_mfma_f32_16x16x32_bf16 v[36:39], a[36:39], v[224:227], v[36:39]\n"
      "ds_read_b64_tr_b16 a[80:81], v243 offset:64\n"
      "v_mfma_f32_16x16x32_bf16 v[40:43], a[40:43], v[224:227], v[40:43]\n"
      "ds_read_b64_tr_b16 a[82:83], v243 offset:832\n"
      "v_mad_i32_i24 v238, v251, s91, v238\n"
      "s_add_u32 s56, s22, 0xc000\n"
      "s_addc_u32 s57, s23, 0\n"
      "s_add_u32 m0, s18, 0xaf00\n"
      "v_mfma_f32_16x16x32_bf16 v[44:47], a[44:47], v[224:227], v[44:47]\n"
      "global_load_lds_dwordx4 v238, s[56:57] offset:256 \n"
      "s_waitcnt lgkmcnt(8)\n"
      "v_mfma_f32_16x16x32_bf16 v[48:51], a[24:27], v[228:231], v[48:51]\n"
      "ds_read_b64_tr_b16 a[84:85], v243 offset:72\n"
      "v_mfma_f32_16x16x32_bf16 v[52:55], a[28:31], v[228:231], v[52:55]\n"
      "ds_read_b64_tr_b16 a[86:87], v243 offset:840\n"
      "v_mad_i32_i24 v238, v251, s92, v238\n"
      "s_add_u32 s56, s22, 0x12000\n"
      "s_addc_u32 s57, s23, 0\n"
      "s_add_u32 m0, s18, 0xbf00\n"
      "v_mfma_f32_16x16x32_bf16 v[56:59], a[32:35], v[228:231], v[56:59]\n"
      "ds_read_b128 v[224:227], v233 offset:20480\n"
      "global_load_lds_dwordx4 v238, s[56:57] offset:256 \n"
      "v_mfma_f32_16x16x32_bf16 v[60:63], a[36:39], v[228:231], v[60:63]\n"
      "ds_read_b64_tr_b16 a[88:89], v243 offset:128\n"
      "v_mfma_f32_16x16x32_bf16 v[64:67], a[40:43], v[228:231], v[64:67]\n"
      "ds_read_b64_tr_b16 a[90:91], v243 offset:896\n"
      "v_mad_i32_i24 v238, v251, s93, v238\n"
      "s_add_u32 s56, s22, 0x18000\n"
      "s_addc_u32 s57, s23, 0\n"
      "s_add_u32 m0, s18, 0xcf00\n"
      "v_mfma_f32_16x16x32_bf16 v[68:71], a[44:47], v[228:231], v[68:71]\n"
      "global_load_lds_dwordx4 v238, s[56:57] offset:256 \n"
      "s_waitcnt lgkmcnt(12)\n"
      "v_mfma_f32_16x16x32_bf16 v[72:75], a[24:27], v[216:219], v[72:75]\n"
      "ds_read_b64_tr_b16 a[92:93], v243 offset:136\n"
      "v_mfma_f32_16x16x32_bf16 v[76:79], a[28:31], v[216:219], v[76:79]\n"
      "ds_read_b64_tr_b16 a[94:95], v243 offset:904\n"
      "v_mad_i32_i24 v238, v251, s94, v238\n"
      "s_add_u32 s56, s22, 0x1e000\n"
      "s_addc_u32 s57, s23, 0\n"
      "s_add_u32 m0, s18, 0xdf00\n"
      "v_mfma_f32_16x16x32_bf16 v[80:83], a[32:35], v[216:219], v[80:83]\n"
      "ds_read_b128 v[228:231], v233 offset:24576\n"
      "global_load_lds_dwordx4 v238, s[56:57] offset:256 \n"
      "v_mfma_f32_16x16x32_bf16 v[84:87], a[36:39], v[216:219], v[84:87]\n"
      "v_mfma_f32_16x16x32_bf16 v[88:91], a[40:43], v[216:219], v[88:91]\n"
      "v_mad_i32_i24 v238, v251, s95, v238\n"
      "s_add_u32 s56, s22, 0x24000\n"
      "s_addc_u32 s57, s23, 0\n"
      "s_add_u32 m0, s18, 0xef00\n"
      "v_mfma_f32_16x16x32_bf16 v[92:95], a[44:47], v[216:219], v[92:95]\n"
      "global_load_lds_dwordx4 v238, s[56:57] offset:256 \n"
      "s_waitcnt lgkmcnt(10)\n"
      "v_mfma_f32_16x16x32_bf16 v[96:99], a[24:27], v[220:223], v[96:99]\n"
      "v_mfma_f32_16x16x32_bf16 v[100:103], a[28:31], v[220:223], v[100:103]\n"
      "v_mad_i32_i24 v238, v251, s96, v238\n"
      "s_add_u32 s56, s22, 0x2a000\n"
      "s_addc_u32 s57, s23, 0\n"
      "s_add_u32 m0, s18, 0xff00\n"
      "v_mfma_f32_16x16x32_bf16 v[104:107], a[32:35], v[220:223], v[104:107]\n"
      "ds_read_b128 v[216:219], v233 offset:28672\n"
      "global_load_lds_dwordx4 v238, s[56:57] offset:256 \n"
      "v_mfma_f32_16x16x32_bf16 v[108:111], a[36:39], v[220:223], v[108:111]\n"
      "v_mfma_f32_16x16x32_bf16 v[112:115], a[40:43], v[220:223], v[112:115]\n"
      "v_mad_i32_i24 v238, v251, s97, v238\n"
      "s_add_u32 s56, s22, 0x30000\n"
      "s_addc_u32 s57, s23, 0\n"
      "s_add_u32 m0, s18, 0x10f00\n"
      "v_mfma_f32_16x16x32_bf16 v[116:119], a[44:47], v[220:223], v[116:119]\n"
      "global_load_lds_dwordx4 v238, s[56:57] offset:256 \n"
      "s_waitcnt lgkmcnt(6)\n"
      "v_mfma_f32_16x16x32_bf16 v[120:123], a[24:27], v[224:227], v[120:123]\n"
      "v_mfma_f32_16x16x32_bf16 v[124:127], a[28:31], v[224:227], v[124:127]\n"
      "v_mfma_f32_16x16x32_bf16 v[128:131], a[32:35], v[224:227], v[128:131]\n"
      "ds_read_b128 v[220:223], v233 offset:32768\n"
      "v_add_u32 v233, 0x9000, v233\n"
      "s_add_u32 s78, s64, 0x1e000\n"
      "s_addc_u32 s79, s65, 0\n"
      "s_add_u32 m0, s74, 0xc000\n"
      "v_mfma_f32_16x16x32_bf16 v[132:135], a[36:39], v[224:227], v[132:135]\n"
      "global_load_lds_dwordx4 v240, s[78:79]\n"
      "v_mfma_f32_16x16x32_bf16 v[136:139], a[40:43], v[224:227], v[136:139]\n"
      "v_mfma_f32_16x16x32_bf16 v[140:143], a[44:47], v[224:227], v[140:143]\n"
      "s_waitcnt lgkmcnt(2)\n"
      "v_mfma_f32_16x16x32_bf16 v[144:147], a[24:27], v[228:231], v[144:147]\n"
      "s_waitcnt vmcnt(10)\n"
      "s_add_u32 m0, s74, 0xc400\n"
      "v_mfma_f32_16x16x32_bf16 v[148:151], a[28:31], v[228:231], v[148:151]\n"
      "ds_read_b64_tr_b16 a[96:97], v243 offset:24576\n"
      "global_load_lds_dwordx4 v241, s[78:79]\n"
      "v_mfma_f32_16x16x32_bf16 v[152:155], a[32:35], v[228:231], v[152:155]\n"
      "ds_read_b128 v[224:227], v234 offset:0\n"
      "v_mfma_f32_16x16x32_bf16 v[156:159], a[36:39], v[228:231], v[156:159]\n"
      "ds_read_b64_tr_b16 a[98:99], v243 offset:25344\n"
      "v_mfma_f32_16x16x32_bf16 v[160:163], a[40:43], v[228:231], v[160:163]\n"
      "ds_read_b64_tr_b16 a[100:101], v243 offset:24584\n"
      "s_add_u32 m0, s74, 0xc800\n"
      "v_mfma_f32_16x16x32_bf16 v[164:167], a[44:47], v[228:231], v[164:167]\n"
      "global_load_lds_dwordx4 v242, s[78:79]\n"
      "s_waitcnt lgkmcnt(5)\n"
      "v_mfma_f32_16x16x32_bf16 v[168:171], a[24:27], v[216:219], v[168:171]\n"
      "ds_read_b64_tr_b16 a[102:103], v243 offset:25352\n"
      "v_mfma_f32_16x16x32_bf16 v[172:175], a[28:31], v[216:219], v[172:175]\n"
      "ds_read_b64_tr_b16 a[104:105], v243 offset:24640\n"
      "v_mfma_f32_16x16x32_bf16 v[176:179], a[32:35], v[216:219], v[176:179]\n"
      "ds_read_b128 v[228:231], v234 offset:4096\n"
      "s_add_u32 s78, s64, 0x21000\n"
      "s_addc_u32 s79, s65, 0\n"
      "s_add_u32 m0, s74, 0xcc00\n"
      "v_mfma_f32_16x16x32_bf16 v[180:183], a[36:39], v[216:219], v[180:183]\n"
      "ds_read_b64_tr_b16 a[106:107], v243 offset:25408\n"
      "global_load_lds_dwordx4 v240, s[78:79]\n"
      "v_mfma_f32_16x16x32_bf16 v[184:187], a[40:43], v[216:219], v[184:187]\n"
      "ds_read_b64_tr_b16 a[108:109], v243 offset:24648\n"
      "LPD4:\n"
      "s_cbranch_execz LPD9\n"
      "v_mfma_f32_16x16x32_bf16 v[188:191], a[44:47], v[216:219], v[188:191]\n"
      "s_waitcnt lgkmcnt(9)\n"
      "v_mfma_f32_16x16x32_bf16 v[192:195], a[24:27], v[220:223], v[192:195]\n"
      "ds_read_b64_tr_b16 a[110:111], v243 offset:25416\n"
      "s_add_u32 m0, s74, 0xd000\n"
      "v_mfma_f32_16x16x32_bf16 v[196:199], a[28:31], v[220:223], v[196:199]\n"
      "ds_read_b64_tr_b16 a[112:113], v243 offset:24704\n"
      "global_load_lds_dwordx4 v241, s[78:79]\n"
      "v_mfma_f32_16x16x32_bf16 v[200:203], a[32:35], v[220:223], v[200:203]\n"
      "ds_read_b128 v[216:219], v234 offset:8192\n"
      "v_mfma_f32_16x16x32_bf16 v[204:207], a[36:39], v[220:223], v[204:207]\n"
      "ds_read_b64_tr_b16 a[114:115], v243 offset:25472\n"
      "v_mfma_f32_16x16x32_bf16 v[208:211], a[40:43], v[220:223], v[208:211]\n"
      "ds_read_b64_tr_b16 a[116:117], v243 offset:24712\n"
      "s_add_u32 m0, s74, 0xd400\n"
      "v_mfma_f32_16x16x32_bf16 v[212:215], a[44:47], v[220:223], v[212:215]\n"
      "global_load_lds_dwordx4 v242, s[78:79]\n"
      "s_waitcnt lgkmcnt(12)\n"
      "v_mfma_f32_16x16x32_bf16 v[0:3], a[48:51], v[224:227], v[0:3]\n"
      "ds_read_b64_tr_b16 a[118:119], v243 offset:25480\n"
      "v_mfma_f32_16x16x32_bf16 v[4:7], a[52:55], v[224:227], v[4:7]\n"
      "v_mfma_f32_16x16x32_bf16 v[8:11], a[56:59], v[224:227], v[8:11]\n"
      "ds_read_b128 v[220:223], v234 offset:12288\n"
      "s_add_u32 s78, s64, 0x24000\n"
      "s_addc_u32 s79, s65, 0\n"
      "s_add_u32 m0, s74, 0x0\n"
      "v_mfma_f32_16x16x32_bf16 v[12:15], a[60:63], v[224:227], v[12:15]\n"
      "global_load_lds_dwordx4 v240, s[78:79]\n"
      "v_mfma_f32_16x16x32_bf16 v[16:19], a[64:67], v[224:227], v[16:19]\n"
      "v_mfma_f32_16x16x32_bf16 v[20:23], a[68:71], v[224:227], v[20:23]\n"
      "s_waitcnt lgkmcnt(9)\n"
      "v_mfma_f32_16x16x32_bf16 v[24:27], a[48:51], v[228:231], v[24:27]\n"
      "s_add_u32 m0, s74, 0x400\n"
      "v_mfma_f32_16x16x32_bf16 v[28:31], a[52:55], v[228:231], v[28:31]\n"
      "global_load_lds_dwordx4 v241, s[78:79]\n"
      "v_mfma_f32_16x16x32_bf16 v[32:35], a[56:59], v[228:231], v[32:35]\n"
      "ds_read_b128 v[224:227], v234 offset:16384\n"
      "v_mfma_f32_16x16x32_bf16 v[36:39], a[60:63], v[228:231], v[36:39]\n"
      "v_mfma_f32_16x16x32_bf16 v[40:43], a[64:67], v[228:231], v[40:43]\n"
      "s_add_u32 m0, s74, 0x800\n"
      "v_mfma_f32_16x16x32_bf16 v[44:47], a[68:71], v[228:231], v[44:47]\n"
      "global_load_lds_dwordx4 v242, s[78:79]\n"
      "s_waitcnt lgkmcnt(5)\n"
      "v_mfma_f32_16x16x32_bf16 v[48:51], a[48:51], v[216:219], v[48:51]\n"
      "v_mfma_f32_16x16x32_bf16 v[52:55], a[52:55], v[216:219], v[52:55]\n"
      "v_mfma_f32_16x16x32_bf16 v[56:59], a[56:59], v[216:219], v[56:59]\n"
      "ds_read_b128 v[228:231], v234 offset:20480\n"
      "s_add_u32 s78, s64, 0x27000\n"
      "s_addc_u32 s79, s65, 0\n"
      "s_add_u32 m0, s74, 0xc00\n"
      "v_mfma_f32_16x16x32_bf16 v[60:63], a[60:63], v[216:219], v[60:63]\n"
      "global_load_lds_dwordx4 v240, s[78:79]\n"
      "v_mfma_f32_16x16x32_bf16 v[64:67], a[64:67], v[216:219], v[64:67]\n"
      "v_mfma_f32_16x16x32_bf16 v[68:71], a[68:71], v[216:219], v[68:71]\n"
      "s_waitcnt lgkmcnt(2)\n"
      "v_mfma_f32_16x16x32_bf16 v[72:75], a[48:51], v[220:223], v[72:75]\n"
      "s_add_u32 m0, s74, 0x1000\n"
      "v_mfma_f32_16x16x32_bf16 v[76:79], a[52:55], v[220:223], v[76:79]\n"
      "global_load_lds_dwordx4 v241, s[78:79]\n"
      "v_mfma_f32_16x16x32_bf16 v[80:83], a[56:59], v[220:223], v[80:83]\n"
      "ds_read_b128 v[216:219], v234 offset:24576\n"
      "v_mfma_f32_16x16x32_bf16 v[84:87], a[60:63], v[220:223], v[84:87]\n"
      "v_mfma_f32_16x16x32_bf16 v[88:91], a[64:67], v[220:223], v[88:91]\n"
      "s_add_u32 m0, s74, 0x1400\n"
      "v_mfma_f32_16x16x32_bf16 v[92:95], a[68:71], v[220:223], v[92:95]\n"
      "global_load_lds_dwordx4 v242, s[78:79]\n"
      "s_waitcnt lgkmcnt(2)\n"
      "v_mfma_f32_16x16x32_bf16 v[96:99], a[48:51], v[224:227], v[96:99]\n"
      "v_mfma_f32_16x16x32_bf16 v[100:103], a[52:55], v[224:227], v[100:103]\n"
      "v_mfma_f32_16x16x32_bf16 v[104:107], a[56:59], v[224:227], v[104:107]\n"
      "ds_read_b128 v[220:223], v234 offset:28672\n"
      "s_add_u32 s78, s64, 0x2a000\n"
      "s_addc_u32 s79, s65, 0\n"
      "s_add_u32 m0, s74, 0x6000\n"
      "v_mfma_f32_16x16x32_bf16 v[108:111], a[60:63], v[224:227], v[108:111]\n"
      "global_load_lds_dwordx4 v240, s[78:79]\n"
      "v_mfma_f32_16x16x32_bf16 v[112:115], a[64:67], v[224:227], v[112:115]\n"
      "v_mfma_f32_16x16x32_bf16 v[116:119], a[68:71], v[224:227], v[116:119]\n"
      "s_waitcnt lgkmcnt(2)\n"
      "v_mfma_f32_16x16x32_bf16 v[120:123], a[48:51], v[228:231], v[120:123]\n"
      "s_add_u32 m0, s74, 0x6400\n"
      "v_mfma_f32_16x16x32_bf16 v[124:127], a[52:55], v[228:231], v[124:127]\n"
      "global_load_lds_dwordx4 v241, s[78:79]\n"
      "v_mfma_f32_16x16x32_bf16 v[128:131], a[56:59], v[228:231], v[128:131]\n"
      "ds_read_b128 v[224:227], v234 offset:32768\n"
      "v_add_u32 v234, 0x9000, v234\n"
      "v_mfma_f32_16x16x32_bf16 v[132:135], a[60:63], v[228:231], v[132:135]\n"
      "v_mfma_f32_16x16x32_bf16 v[136:139], a[64:67], v[228:231], v[136:139]\n"
      "s_waitcnt vmcnt(8)\n"
      "s_add_u32 m0, s74, 0x6800\n"
      "v_mfma_f32_16x16x32_bf16 v[140:143], a[68:71], v[228:231], v[140:143]\n"
      "global_load_lds_dwordx4 v242, s[78:79]\n"
      "s_waitcnt lgkmcnt(2)\n"
      "v_mfma_f32_16x16x32_bf16 v[144:147], a[48:51], v[216:219], v[144:147]\n"
      "ds_read_b64_tr_b16 a[120:121], v243 offset:49152\n"
      "v_mfma_f32_16x16x32_bf16 v[148:151], a[52:55], v[216:219], v[148:151]\n"
      "ds_read_b64_tr_b16 a[122:123], v243 offset:49920\n"
      "v_mfma_f32_16x16x32_bf16 v[152:155], a[56:59], v[216:219], v[152:155]\n"
      "ds_read_b128 v[228:231], v235 offset:0\n"
      "s_add_u32 s78, s64, 0x2d000\n"
      "s_addc_u32 s79, s65, 0\n"
      "s_add_u32 m0, s74, 0x6c00\n"
      "v_mfma_f32_16x16x32_bf16 v[156:159], a[60:63], v[216:219], v[156:159]\n"
      "ds_read_b64_tr_b16 a[124:125], v243 offset:49160\n"
      "global_load_lds_dwordx4 v240, s[78:79]\n"
      "v_mfma_f32_16x16x32_bf16 v[160:163], a[64:67], v[216:219], v[160:163]\n"
      "ds_read_b64_tr_b16 a[126:127], v243 offset:49928\n"
      "v_mfma_f32_16x16x32_bf16 v[164:167], a[68:71], v[216:219], v[164:167]\n"
      "s_waitcnt lgkmcnt(6)\n"
      "v_mfma_f32_16x16x32_bf16 v[168:171], a[48:51], v[220:223], v[168:171]\n"
      "ds_read_b64_tr_b16 a[128:129], v243 offset:49216\n"
      "s_add_u32 m0, s74, 0x7000\n"
      "v_mfma_f32_16x16x32_bf16 v[172:175], a[52:55], v[220:223], v[172:175]\n"
      "ds_read_b64_tr_b16 a[130:131], v243 offset:49984\n"
      "global_load_lds_dwordx4 v241, s[78:79]\n"
      "v_mfma_f32_16x16x32_bf16 v[176:179], a[56:59], v[220:223], v[176:179]\n"
      "ds_read_b128 v[216:219], v235 offset:4096\n"
      "v_mfma_f32_16x16x32_bf16 v[180:183], a[60:63], v[220:223], v[180:183]\n"
      "ds_read_b64_tr_b16 a[132:133], v243 offset:49224\n"
      "v_mfma_f32_16x16x32_bf16 v[184:187], a[64:67], v[220:223], v[184:187]\n"
      "ds_read_b64_tr_b16 a[134:135], v243 offset:49992\n"
      "s_add_u32 m0, s74, 0x7400\n"
      "v_mfma_f32_16x16x32_bf16 v[188:191], a[68:71], v[220:223], v[188:191]\n"
      "global_load_lds_dwordx4 v242, s[78:79]\n"
      "s_waitcnt lgkmcnt(10)\n"
      "v_mfma_f32_16x16x32_bf16 v[192:195], a[48:51], v[224:227], v[192:195]\n"
      "ds_read_b64_tr_b16 a[136:137], v243 offset:49280\n"
      "v_mfma_f32_16x16x32_bf16 v[196:199], a[52:55], v[224:227], v[196:199]\n"
      "ds_read_b64_tr_b16 a[138:139], v243 offset:50048\n"
      "v_mfma_f32_16x16x32_bf16 v[200:203], a[56:59], v[224:227], v[200:203]\n"
      "ds_read_b128 v[220:223], v235 offset:8192\n"
      "global_load_dwordx4 v[244:247], v248, s[28:29] sc1\n"
      "v_mfma_f32_16x16x32_bf16 v[204:207], a[60:63], v[224:227], v[204:207]\n"
      "ds_read_b64_tr_b16 a[140:141], v243 offset:49288\n"
      "v_mfma_f32_16x16x32_bf16 v[208:211], a[64:67], v[224:227], v[208:211]\n"
      "ds_read_b64_tr_b16 a[142:143], v243 offset:50056\n"
      "v_mfma_f32_16x16x32_bf16 v[212:215], a[68:71], v[224:227], v[212:215]\n"
      "s_waitcnt lgkmcnt(12)\n"
      "v_mfma_f32_16x16x32_bf16 v[0:3], a[72:75], v[228:231], v[0:3]\n"
      "v_mfma_f32_16x16x32_bf16 v[4:7], a[76:79], v[228:231], v[4:7]\n"
      "v_mfma_f32_16x16x32_bf16 v[8:11], a[80:83], v[228:231], v[8:11]\n"
      "ds_read_b128 v[224:227], v235 offset:12288\n"
      "v_mfma_f32_16x16x32_bf16 v[12:15], a[84:87], v[228:231], v[12:15]\n"
      "v_mfma_f32_16x16x32_bf16 v[16:19], a[88:91], v[228:231], v[16:19]\n"
      "s_waitcnt vmcnt(7)\n"
      "v_mfma_f32_16x16x32_bf16 v[20:23], a[92:95], v[228:231], v[20:23]\n"
      "s_waitcnt lgkmcnt(8)\n"
      "v_mfma_f32_16x16x32_bf16 v[24:27], a[72:75], v[216:219], v[24:27]\n"
      "ds_read_b64_tr_b16 a[144:145], v243 offset:0\n"
      "v_mfma_f32_16x16x32_bf16 v[28:31], a[76:79], v[216:219], v[28:31]\n"
      "ds_read_b64_tr_b16 a[146:147], v243 offset:768\n"
      "v_mfma_f32_16x16x32_bf16 v[32:35], a[80:83], v[216:219], v[32:35]\n"
      "ds_read_b128 v[228:231], v235 offset:16384\n"
      "s_waitcnt lgkmcnt(4)\n"
      "s_add_u32 s78, s64, 0x30000\n"
      "s_addc_u32 s79, s65, 0\n"
      "s_add_u32 m0, s74, 0xc000\n"
      "v_mfma_f32_16x16x32_bf16 v[36:39], a[84:87], v[216:219], v[36:39]\n"
      "ds_read_b64_tr_b16 a[148:149], v243 offset:8\n"
      "global_load_lds_dwordx4 v240, s[78:79]\n"
      "v_mfma_f32_16x16x32_bf16 v[40:43], a[88:91], v[216:219], v[40:43]\n"
      "ds_read_b64_tr_b16 a[150:151], v243 offset:776\n"
      "v_mfma_f32_16x16x32_bf16 v[44:47], a[92:95], v[216:219], v[44:47]\n"
      "v_mfma_f32_16x16x32_bf16 v[48:51], a[72:75], v[220:223], v[48:51]\n"
      "ds_read_b64_tr_b16 a[152:153], v243 offset:64\n"
      "s_add_u32 m0, s74, 0xc400\n"
      "v_mfma_f32_16x16x32_bf16 v[52:55], a[76:79], v[220:223], v[52:55]\n"
      "ds_read_b64_tr_b16 a[154:155], v243 offset:832\n"
      "global_load_lds_dwordx4 v241, s[78:79]\n"
      "v_mfma_f32_16x16x32_bf16 v[56:59], a[80:83], v[220:223], v[56:59]\n"
      "ds_read_b128 v[216:219], v235 offset:20480\n"
      "v_mfma_f32_16x16x32_bf16 v[60:63], a[84:87], v[220:223], v[60:63]\n"
      "ds_read_b64_tr_b16 a[156:157], v243 offset:72\n"
      "v_mfma_f32_16x16x32_bf16 v[64:67], a[88:91], v[220:223], v[64:67]\n"
      "ds_read_b64_tr_b16 a[158:159], v243 offset:840\n"
      "s_add_u32 m0, s74, 0xc800\n"
      "v_mfma_f32_16x16x32_bf16 v[68:71], a[92:95], v[220:223], v[68:71]\n"
      "global_load_lds_dwordx4 v242, s[78:79]\n"
      "s_waitcnt lgkmcnt(10)\n"
      "v_mfma_f32_16x16x32_bf16 v[72:75], a[72:75], v[224:227], v[72:75]\n"
      "ds_read_b64_tr_b16 a[160:161], v243 offset:128\n"
      "v_mfma_f32_16x16x32_bf16 v[76:79], a[76:79], v[224:227], v[76:79]\n"
      "ds_read_b64_tr_b16 a[162:163], v243 offset:896\n"
      "v_mfma_f32_16x16x32_bf16 v[80:83], a[80:83], v[224:227], v[80:83]\n"
      "ds_read_b128 v[220:223], v235 offset:24576\n"
      "s_add_u32 s78, s64, 0x33000\n"
      "s_addc_u32 s79, s65, 0\n"
      "s_add_u32 m0, s74, 0xcc00\n"
      "v_mfma_f32_16x16x32_bf16 v[84:87], a[84:87], v[224:227], v[84:87]\n"
      "ds_read_b64_tr_b16 a[164:165], v243 offset:136\n"
      "global_load_lds_dwordx4 v240, s[78:79]\n"
      "v_mfma_f32_16x16x32_bf16 v[88:91], a[88:91], v[224:227], v[88:91]\n"
      "ds_read_b64_tr_b16 a[166:167], v243 offset:904\n"
      "v_mfma_f32_16x16x32_bf16 v[92:95], a[92:95], v[224:227], v[92:95]\n"
      "s_waitcnt lgkmcnt(12)\n"
      "v_mfma_f32_16x16x32_bf16 v[96:99], a[72:75], v[228:231], v[96:99]\n"
      "s_add_u32 m0, s74, 0xd000\n"
      "v_mfma_f32_16x16x32_bf16 v[100:103], a[76:79], v[228:231], v[100:103]\n"
      "global_load_lds_dwordx4 v241, s[78:79]\n"
      "v_mfma_f32_16x16x32_bf16 v[104:107], a[80:83], v[228:231], v[104:107]\n"
      "ds_read_b128 v[224:227], v235 offset:28672\n"
      "v_mfma_f32_16x16x32_bf16 v[108:111], a[84:87], v[228:231], v[108:111]\n"
      "v_mfma_f32_16x16x32_bf16 v[112:115], a[88:91], v[228:231], v[112:115]\n"
      "s_waitcnt vmcnt(6)\n"
      "s_add_u32 m0, s74, 0xd400\n"
      "v_mfma_f32_16x16x32_bf16 v[116:119], a[92:95], v[228:231], v[116:119]\n"
      "global_load_lds_dwordx4 v242, s[78:79]\n"
      "s_waitcnt lgkmcnt(8)\n"
      "v_mfma_f32_16x16x32_bf16 v[120:123], a[72:75], v[216:219], v[120:123]\n"
      "ds_read_b64_tr_b16 a[168:169], v243 offset:24576\n"
      "v_mfma_f32_16x16x32_bf16 v[124:127], a[76:79], v[216:219], v[124:127]\n"
      "ds_read_b64_tr_b16 a[170:171], v243 offset:25344\n"
      "v_mfma_f32_16x16x32_bf16 v[128:131], a[80:83], v[216:219], v[128:131]\n"
      "ds_read_b128 v[228:231], v235 offset:32768\n"
      "v_add_u32 v235, 0x9000, v235\n"
      "s_waitcnt lgkmcnt(4)\n"
      "s_add_u32 s78, s64, 0x36000\n"
      "s_addc_u32 s79, s65, 0\n"
      "s_add_u32 m0, s74, 0x0\n"
      "v_mfma_f32_16x16x32_bf16 v[132:135], a[84:87], v[216:219], v[132:135]\n"
      "ds_read_b64_tr_b16 a[172:173], v243 offset:24584\n"
      "global_load_lds_dwordx4 v240, s[78:79]\n"
      "v_mfma_f32_16x16x32_bf16 v[136:139], a[88:91], v[216:219], v[136:139]\n"
      "ds_read_b64_tr_b16 a[174:175], v243 offset:25352\n"
      "v_mfma_f32_16x16x32_bf16 v[140:143], a[92:95], v[216:219], v[140:143]\n"
      "v_mfma_f32_16x16x32_bf16 v[144:147], a[72:75], v[220:223], v[144:147]\n"
      "ds_read_b64_tr_b16 a[176:177], v243 offset:24640\n"
      "s_waitcnt vmcnt(7)\n"
      "v_cmp_ne_u64 vcc, s[80:81], v[244:245]\n"
      "v_cmp_ne_u64 s[56:57], s[80:81], v[246:247]\n"
      "s_nop 3\n"
      "s_or_b64 vcc, vcc, s[56:57]\n"
      "s_cbranch_scc0 LP1_1\n"
      "s_mov_b32 s82, 0x40000\n"
      "LP0_1:\n"
      "s_sleep 2\n"
      "global_load_dwordx4 v[244:247], v248, s[28:29] sc1\n"
      "s_sub_u32 s82, s82, 1\n"
      "s_cbranch_scc1 LP2_1\n"
      "s_waitcnt vmcnt(0)\n"
      "v_cmp_ne_u64 vcc, s[80:81], v[244:245]\n"
      "v_cmp_ne_u64 s[56:57], s[80:81], v[246:247]\n"
      "s_nop 3\n"
      "s_or_b64 vcc, vcc, s[56:57]\n"
      "s_cbranch_scc1 LP0_1\n"
      "LP2_1:\n"
      "s_waitcnt vmcnt(0)\n"
      "LP1_1:\n"
      "s_nop 0\n"
      "v_mfma_f32_16x16x32_bf16 v[148:151], a[76:79], v[220:223], v[148:151]\n"
      "s_barrier\n"
      "s_add_u32 m0, s74, 0x400\n"
      "v_mfma_f32_16x16x32_bf16 v[152:155], a[80:83], v[220:223], v[152:155]\n"
      "ds_read_b128 v[216:219], v232 offset:0\n"
      "global_load_lds_dwordx4 v241, s[78:79]\n"
      "v_mfma_f32_16x16x32_bf16 v[156:159], a[84:87], v[220:223], v[156:159]\n"
      "ds_read_b64_tr_b16 a[178:179], v243 offset:25408\n"
      "v_mfma_f32_16x16x32_bf16 v[160:163], a[88:91], v[220:223], v[160:163]\n"
      "ds_read_b64_tr_b16 a[180:181], v243 offset:24648\n"
      "v_mfma_f32_16x16x32_bf16 v[164:167], a[92:95], v[220:223], v[164:167]\n"
      "s_add_u32 m0, s74, 0x800\n"
      "s_waitcnt lgkmcnt(9)\n"
      "v_mfma_f32_16x16x32_bf16 v[168:171], a[72:75], v[224:227], v[168:171]\n"
      "ds_read_b64_tr_b16 a[182:183], v243 offset:25416\n"
      "global_load_lds_dwordx4 v242, s[78:79]\n"
      "v_mfma_f32_16x16x32_bf16 v[172:175], a[76:79], v[224:227], v[172:175]\n"
      "ds_read_b64_tr_b16 a[184:185], v243 offset:24704\n"
      "v_mfma_f32_16x16x32_bf16 v[176:179], a[80:83], v[224:227], v[176:179]\n"
      "ds_read_b128 v[220:223], v232 offset:4096\n"
      "v_mfma_f32_16x16x32_bf16 v[180:183], a[84:87], v[224:227], v[180:183]\n"
      "ds_read_b64_tr_b16 a[186:187], v243 offset:25472\n"
      "s_add_u32 s78, s64, 0x39000\n"
      "s_addc_u32 s79, s65, 0\n"
      "s_add_u32 m0, s74, 0xc00\n"
      "v_mfma_f32_16x16x32_bf16 v[184:187], a[88:91], v[224:227], v[184:187]\n"
      "ds_read_b64_tr_b16 a[188:189], v243 offset:24712\n"
      "global_load_lds_dwordx4 v240, s[78:79]\n"
      "v_mfma_f32_16x16x32_bf16 v[188:191], a[92:95], v[224:227], v[188:191]\n"
      "s_waitcnt lgkmcnt(11)\n"
      "v_mfma_f32_16x16x32_bf16 v[192:195], a[72:75], v[228:231], v[192:195]\n"
      "ds_read_b64_tr_b16 a[190:191], v243 offset:25480\n"
      "v_mfma_f32_16x16x32_bf16 v[196:199], a[76:79], v[228:231], v[196:199]\n"
      "s_add_u32 m0, s74, 0x1000\n"
      "v_mfma_f32_16x16x32_bf16 v[200:203], a[80:83], v[228:231], v[200:203]\n"
      "ds_read_b128 v[224:227], v232 offset:8192\n"
      "global_load_lds_dwordx4 v241, s[78:79]\n"
      "v_mfma_f32_16x16x32_bf16 v[204:207], a[84:87], v[228:231], v[204:207]\n"
      "v_mfma_f32_16x16x32_bf16 v[208:211], a[88:91], v[228:231], v[208:211]\n"
      "v_mfma_f32_16x16x32_bf16 v[212:215], a[92:95], v[228:231], v[212:215]\n"
      "s_add_u32 m0, s74, 0x1400\n"
      "s_waitcnt lgkmcnt(9)\n"
      "v_mfma_f32_16x16x32_bf16 v[0:3], a[96:99], v[216:219], v[0:3]\n"
      "global_load_lds_dwordx4 v242, s[78:79]\n"
      "v_mfma_f32_16x16x32_bf16 v[4:7], a[100:103], v[216:219], v[4:7]\n"
      "s_barrier\n"
      "v_mfma_f32_16x16x32_bf16 v[8:11], a[104:107], v[216:219], v[8:11]\n"
      "ds_read_b128 v[228:231], v232 offset:12288\n"
      "v_mfma_f32_16x16x32_bf16 v[12:15], a[108:111], v[216:219], v[12:15]\n"
      "s_add_u32 s58, s24, 0x0\n"
      "s_addc_u32 s59, s25, 0\n"
      "global_load_dwordx4 a[240:243], v239, s[58:59] offset:0 sc1\n"
      "v_mfma_f32_16x16x32_bf16 v[16:19], a[112:115], v[216:219], v[16:19]\n"
      "v_mfma_f32_16x16x32_bf16 v[20:23], a[116:119], v[216:219], v[20:23]\n"
      "s_waitcnt lgkmcnt(5)\n"
      "v_mfma_f32_16x16x32_bf16 v[24:27], a[96:99], v[220:223], v[24:27]\n"
      "v_mfma_f32_16x16x32_bf16 v[28:31], a[100:103], v[220:223], v[28:31]\n"
      "s_waitcnt lgkmcnt(2)\n"
      "s_add_u32 s78, s64, 0x3c000\n"
      "s_addc_u32 s79, s65, 0\n"
      "s_add_u32 m0, s74, 0x6000\n"
      "v_mfma_f32_16x16x32_bf16 v[32:35], a[104:107], v[220:223], v[32:35]\n"
      "ds_read_b128 v[216:219], v232 offset:16384\n"
      "global_load_lds_dwordx4 v240, s[78:79]\n"
      "v_mfma_f32_16x16x32_bf16 v[36:39], a[108:111], v[220:223], v[36:39]\n"
      "v_mfma_f32_16x16x32_bf16 v[40:43], a[112:115], v[220:223], v[40:43]\n"
      "s_waitcnt vmcnt(8)\n"
      "v_mfma_f32_16x16x32_bf16 v[44:47], a[116:119], v[220:223], v[44:47]\n"
      "s_add_u32 m0, s74, 0x6400\n"
      "s_waitcnt lgkmcnt(2)\n"
      "v_mfma_f32_16x16x32_bf16 v[48:51], a[96:99], v[224:227], v[48:51]\n"
      "ds_read_b64_tr_b16 a[192:193], v243 offset:49152\n"
      "global_load_lds_dwordx4 v241, s[78:79]\n"
      "v_mfma_f32_16x16x32_bf16 v[52:55], a[100:103], v[224:227], v[52:55]\n"
      "ds_read_b64_tr_b16 a[194:195], v243 offset:49920\n"
      "v_mfma_f32_16x16x32_bf16 v[56:59], a[104:107], v[224:227], v[56:59]\n"
      "ds_read_b128 v[220:223], v232 offset:20480\n"
      "v_mfma_f32_16x16x32_bf16 v[60:63], a[108:111], v[224:227], v[60:63]\n"
      "ds_read_b64_tr_b16 a[196:197], v243 offset:49160\n"
      "s_add_u32 m0, s74, 0x6800\n"
      "v_mfma_f32_16x16x32_bf16 v[64:67], a[112:115], v[224:227], v[64:67]\n"
      "ds_read_b64_tr_b16 a[198:199], v243 offset:49928\n"
      "global_load_lds_dwordx4 v242, s[78:79]\n"
      "v_mfma_f32_16x16x32_bf16 v[68:71], a[116:119], v[224:227], v[68:71]\n"
      "s_waitcnt lgkmcnt(6)\n"
      "v_mfma_f32_16x16x32_bf16 v[72:75], a[96:99], v[228:231], v[72:75]\n"
      "ds_read_b64_tr_b16 a[200:201], v243 offset:49216\n"
      "v_mfma_f32_16x16x32_bf16 v[76:79], a[100:103], v[228:231], v[76:79]\n"
      "ds_read_b64_tr_b16 a[202:203], v243 offset:49984\n"
      "s_add_u32 s78, s64, 0x3f000\n"
      "s_addc_u32 s79, s65, 0\n"
      "s_add_u32 m0, s74, 0x6c00\n"
      "v_mfma_f32_16x16x32_bf16 v[80:83], a[104:107], v[228:231], v[80:83]\n"
      "ds_read_b128 v[224:227], v232 offset:24576\n"
      "global_load_lds_dwordx4 v240, s[78:79]\n"
      "v_mfma_f32_16x16x32_bf16 v[84:87], a[108:111], v[228:231], v[84:87]\n"
      "ds_read_b64_tr_b16 a[204:205], v243 offset:49224\n"
      "v_mfma_f32_16x16x32_bf16 v[88:91], a[112:115], v[228:231], v[88:91]\n"
      "ds_read_b64_tr_b16 a[206:207], v243 offset:49992\n"
      "v_mfma_f32_16x16x32_bf16 v[92:95], a[116:119], v[228:231], v[92:95]\n"
      "s_add_u32 m0, s74, 0x7000\n"
      "s_waitcnt lgkmcnt(10)\n"
      "v_mfma_f32_16x16x32_bf16 v[96:99], a[96:99], v[216:219], v[96:99]\n"
      "ds_read_b64_tr_b16 a[208:209], v243 offset:49280\n"
      "global_load_lds_dwordx4 v241, s[78:79]\n"
      "v_mfma_f32_16x16x32_bf16 v[100:103], a[100:103], v[216:219], v[100:103]\n"
      "ds_read_b64_tr_b16 a[210:211], v243 offset:50048\n"
      "v_mfma_f32_16x16x32_bf16 v[104:107], a[104:107], v[216:219], v[104:107]\n"
      "ds_read_b128 v[228:231], v232 offset:28672\n"
      "v_mfma_f32_16x16x32_bf16 v[108:111], a[108:111], v[216:219], v[108:111]\n"
      "ds_read_b64_tr_b16 a[212:213], v243 offset:49288\n"
      "s_add_u32 m0, s74, 0x7400\n"
      "v_mfma_f32_16x16x32_bf16 v[112:115], a[112:115], v[216:219], v[112:115]\n"
      "ds_read_b64_tr_b16 a[214:215], v243 offset:50056\n"
      "global_load_lds_dwordx4 v242, s[78:79]\n"
      "v_mfma_f32_16x16x32_bf16 v[116:119], a[116:119], v[216:219], v[116:119]\n"
      "s_waitcnt lgkmcnt(12)\n"
      "v_mfma_f32_16x16x32_bf16 v[120:123], a[96:99], v[220:223], v[120:123]\n"
      "v_mfma_f32_16x16x32_bf16 v[124:127], a[100:103], v[220:223], v[124:127]\n"
      "global_load_dwordx4 a[244:247], v239, s[58:59] offset:1024 sc1\n"
      "v_mfma_f32_16x16x32_bf16 v[128:131], a[104:107], v[220:223], v[128:131]\n"
      "ds_read_b128 v[216:219], v232 offset:32768\n"
      "v_add_u32 v232, 0xffff7000, v232\n"
      "v_mfma_f32_16x16x32_bf16 v[132:135], a[108:111], v[220:223], v[132:135]\n"
      "v_mfma_f32_16x16x32_bf16 v[136:139], a[112:115], v[220:223], v[136:139]\n"
      "v_mfma_f32_16x16x32_bf16 v[140:143], a[116:119], v[220:223], v[140:143]\n"
      "global_load_dwordx4 a[248:251], v239, s[58:59] offset:2048 sc1\n"
      "s_waitcnt lgkmcnt(8)\n"
      "v_mfma_f32_16x16x32_bf16 v[144:147], a[96:99], v[224:227], v[144:147]\n"
      "s_waitcnt vmcnt(9)\n"
      "v_mfma_f32_16x16x32_bf16 v[148:151], a[100:103], v[224:227], v[148:151]\n"
      "ds_read_b64_tr_b16 a[216:217], v243 offset:0\n"
      "v_mfma_f32_16x16x32_bf16 v[152:155], a[104:107], v[224:227], v[152:155]\n"
      "ds_read_b128 v[220:223], v233 offset:0\n"
      "v_mfma_f32_16x16x32_bf16 v[156:159], a[108:111], v[224:227], v[156:159]\n"
      "ds_read_b64_tr_b16 a[218:219], v243 offset:768\n"
      "s_waitcnt lgkmcnt(4)\n"
      "s_add_u32 s78, s64, 0x42000\n"
      "s_addc_u32 s79, s65, 0\n"
      "s_add_u32 m0, s74, 0xc000\n"
      "v_mfma_f32_16x16x32_bf16 v[160:163], a[112:115], v[224:227], v[160:163]\n"
      "ds_read_b64_tr_b16 a[220:221], v243 offset:8\n"
      "global_load_lds_dwordx4 v240, s[78:79]\n"
      "v_mfma_f32_16x16x32_bf16 v[164:167], a[116:119], v[224:227], v[164:167]\n"
      "v_mfma_f32_16x16x32_bf16 v[168:171], a[96:99], v[228:231], v[168:171]\n"
      "ds_read_b64_tr_b16 a[222:223], v243 offset:776\n"
      "v_mfma_f32_16x16x32_bf16 v[172:175], a[100:103], v[228:231], v[172:175]\n"
      "ds_read_b64_tr_b16 a[224:225], v243 offset:64\n"
      "s_add_u32 m0, s74, 0xc400\n"
      "v_mfma_f32_16x16x32_bf16 v[176:179], a[104:107], v[228:231], v[176:179]\n"
      "ds_read_b128 v[224:227], v233 offset:4096\n"
      "global_load_lds_dwordx4 v241, s[78:79]\n"
      "v_mfma_f32_16x16x32_bf16 v[180:183], a[108:111], v[228:231], v[180:183]\n"
      "ds_read_b64_tr_b16 a[226:227], v243 offset:832\n"
      "v_mfma_f32_16x16x32_bf16 v[184:187], a[112:115], v[228:231], v[184:187]\n"
      "ds_read_b64_tr_b16 a[228:229], v243 offset:72\n"
      "v_mfma_f32_16x16x32_bf16 v[188:191], a[116:119], v[228:231], v[188:191]\n"
      "s_add_u32 m0, s74, 0xc800\n"
      "s_waitcnt lgkmcnt(9)\n"
      "v_mfma_f32_16x16x32_bf16 v[192:195], a[96:99], v[216:219], v[192:195]\n"
      "ds_read_b64_tr_b16 a[230:231], v243 offset:840\n"
      "global_load_lds_dwordx4 v242, s[78:79]\n"
      "v_mfma_f32_16x16x32_bf16 v[196:199], a[100:103], v[216:219], v[196:199]\n"
      "ds_read_b64_tr_b16 a[232:233], v243 offset:128\n"
      "v_mfma_f32_16x16x32_bf16 v[200:203], a[104:107], v[216:219], v[200:203]\n"
      "ds_read_b128 v[228:231], v233 offset:8192\n"
      "v_mfma_f32_16x16x32_bf16 v[204:207], a[108:111], v[216:219], v[204:207]\n"
      "ds_read_b64_tr_b16 a[234:235], v243 offset:896\n"
      "s_add_u32 s78, s64, 0x45000\n"
      "s_addc_u32 s79, s65, 0\n"
      "s_add_u32 m0, s74, 0xcc00\n"
      "v_mfma_f32_16x16x32_bf16 v[208:211], a[112:115], v[216:219], v[208:211]\n"
      "ds_read_b64_tr_b16 a[236:237], v243 offset:136\n"
      "global_load_lds_dwordx4 v240, s[78:79]\n"
      "v_mfma_f32_16x16x32_bf16 v[212:215], a[116:119], v[216:219], v[212:215]\n"
      "s_waitcnt lgkmcnt(12)\n"
      "v_mfma_f32_16x16x32_bf16 v[0:3], a[120:123], v[220:223], v[0:3]\n"
      "ds_read_b64_tr_b16 a[238:239], v243 offset:904\n"
      "v_mfma_f32_16x16x32_bf16 v[4:7], a[124:127], v[220:223], v[4:7]\n"
      "s_add_u32 m0, s74, 0xd000\n"
      "v_mfma_f32_16x16x32_bf16 v[8:11], a[128:131], v[220:223], v[8:11]\n"
      "ds_read_b128 v[216:219], v233 offset:12288\n"
      "global_load_lds_dwordx4 v241, s[78:79]\n"
      "v_mfma_f32_16x16x32_bf16 v[12:15], a[132:135], v[220:223], v[12:15]\n"
      "v_mfma_f32_16x16x32_bf16 v[16:19], a[136:139], v[220:223], v[16:19]\n"
      "v_mfma_f32_16x16x32_bf16 v[20:23], a[140:143], v[220:223], v[20:23]\n"
      "s_add_u32 m0, s74, 0xd400\n"
      "s_waitcnt lgkmcnt(9)\n"
      "v_mfma_f32_16x16x32_bf16 v[24:27], a[120:123], v[224:227], v[24:27]\n"
      "global_load_lds_dwordx4 v242, s[78:79]\n"
      "v_mfma_f32_16x16x32_bf16 v[28:31], a[124:127], v[224:227], v[28:31]\n"
      "v_mfma_f32_16x16x32_bf16 v[32:35], a[128:131], v[224:227], v[32:35]\n"
      "ds_read_b128 v[220:223], v233 offset:16384\n"
      "v_mfma_f32_16x16x32_bf16 v[36:39], a[132:135], v[224:227], v[36:39]\n"
      "s_waitcnt vmcnt(8)\n"
      "v_mad_i32_i24 v238, v251, s89, v238\n"
      "s_add_u32 s56, s22, 0x0\n"
      "s_addc_u32 s57, s23, 0\n"
      "s_add_u32 m0, s18, 0xfffffe00\n"
      "v_mfma_f32_16x16x32_bf16 v[40:43], a[136:139], v[224:227], v[40:43]\n"
      "ds_read_b64_tr_b16 a[0:1], v243 offset:24576\n"
      "global_load_lds_dwordx4 v238, s[56:57] offset:512 \n"
      "v_mfma_f32_16x16x32_bf16 v[44:47], a[140:143], v[224:227], v[44:47]\n"
      "s_waitcnt lgkmcnt(6)\n"
      "v_mfma_f32_16x16x32_bf16 v[48:51], a[120:123], v[228:231], v[48:51]\n"
      "ds_read_b64_tr_b16 a[2:3], v243 offset:25344\n"
      "v_mad_i32_i24 v238, v251, s90, v238\n"
      "s_add_u32 s56, s22, 0x6000\n"
      "s_addc_u32 s57, s23, 0\n"
      "s_add_u32 m0, s18, 0xe00\n"
      "v_mfma_f32_16x16x32_bf16 v[52:55], a[124:127], v[228:231], v[52:55]\n"
      "ds_read_b64_tr_b16 a[4:5], v243 offset:24584\n"
      "global_load_lds_dwordx4 v238, s[56:57] offset:512 \n"
      "v_mfma_f32_16x16x32_bf16 v[56:59], a[128:131], v[228:231], v[56:59]\n"
      "ds_read_b128 v[224:227], v233 offset:20480\n"
      "v_mfma_f32_16x16x32_bf16 v[60:63], a[132:135], v[228:231], v[60:63]\n"
      "ds_read_b64_tr_b16 a[6:7], v243 offset:25352\n"
      "v_mad_i32_i24 v238, v251, s91, v238\n"
      "s_add_u32 s56, s22, 0xc000\n"
      "s_addc_u32 s57, s23, 0\n"
      "s_add_u32 m0, s18, 0x1e00\n"
      "v_mfma_f32_16x16x32_bf16 v[64:67], a[136:139], v[228:231], v[64:67]\n"
      "ds_read_b64_tr_b16 a[8:9], v243 offset:24640\n"
      "global_load_lds_dwordx4 v238, s[56:57] offset:512 \n"
      "v_mfma_f32_16x16x32_bf16 v[68:71], a[140:143], v[228:231], v[68:71]\n"
      "s_waitcnt lgkmcnt(7)\n"
      "v_mfma_f32_16x16x32_bf16 v[72:75], a[120:123], v[216:219], v[72:75]\n"
      "ds_read_b64_tr_b16 a[10:11], v243 offset:25408\n"
      "v_mad_i32_i24 v238, v251, s92, v238\n"
      "s_add_u32 s56, s22, 0x12000\n"
      "s_addc_u32 s57, s23, 0\n"
      "s_add_u32 m0, s18, 0x2e00\n"
      "v_mfma_f32_16x16x32_bf16 v[76:79], a[124:127], v[216:219], v[76:79]\n"
      "ds_read_b64_tr_b16 a[12:13], v243 offset:24648\n"
      "global_load_lds_dwordx4 v238, s[56:57] offset:512 \n"
      "v_mfma_f32_16x16x32_bf16 v[80:83], a[128:131], v[216:219], v[80:83]\n"
      "ds_read_b128 v[228:231], v233 offset:24576\n"
      "v_mfma_f32_16x16x32_bf16 v[84:87], a[132:135], v[216:219], v[84:87]\n"
      "ds_read_b64_tr_b16 a[14:15], v243 offset:25416\n"
      "v_mad_i32_i24 v238, v251, s93, v238\n"
      "s_add_u32 s56, s22, 0x18000\n"
      "s_addc_u32 s57, s23, 0\n"
      "s_add_u32 m0, s18, 0x3e00\n"
      "v_mfma_f32_16x16x32_bf16 v[88:91], a[136:139], v[216:219], v[88:91]\n"
      "ds_read_b64_tr_b16 a[16:17], v243 offset:24704\n"
      "global_load_lds_dwordx4 v238, s[56:57] offset:512 \n"
      "v_mfma_f32_16x16x32_bf16 v[92:95], a[140:143], v[216:219], v[92:95]\n"
      "s_waitcnt lgkmcnt(11)\n"
      "v_mfma_f32_16x16x32_bf16 v[96:99], a[120:123], v[220:223], v[96:99]\n"
      "ds_read_b64_tr_b16 a[18:19], v243 offset:25472\n"
      "v_mad_i32_i24 v238, v251, s94, v238\n"
      "s_add_u32 s56, s22, 0x1e000\n"
      "s_addc_u32 s57, s23, 0\n"
      "s_add_u32 m0, s18, 0x4e00\n"
      "v_mfma_f32_16x16x32_bf16 v[100:103], a[124:127], v[220:223], v[100:103]\n"
      "ds_read_b64_tr_b16 a[20:21], v243 offset:24712\n"
      "global_load_lds_dwordx4 v238, s[56:57] offset:512 \n"
      "v_mfma_f32_16x16x32_bf16 v[104:107], a[128:131], v[220:223], v[104:107]\n"
      "ds_read_b128 v[216:219], v233 offset:28672\n"
      "v_mfma_f32_16x16x32_bf16 v[108:111], a[132:135], v[220:223], v[108:111]\n"
      "ds_read_b64_tr_b16 a[22:23], v243 offset:25480\n"
      "v_mad_i32_i24 v238, v251, s95, v238\n"
      "s_add_u32 s56, s22, 0x24000\n"
      "s_addc_u32 s57, s23, 0\n"
      "s_add_u32 m0, s18, 0x5e00\n"
      "v_mfma_f32_16x16x32_bf16 v[112:115], a[136:139], v[220:223], v[112:115]\n"
      "global_load_lds_dwordx4 v238, s[56:57] offset:512 \n"
      "v_mfma_f32_16x16x32_bf16 v[116:119], a[140:143], v[220:223], v[116:119]\n"
      "s_waitcnt lgkmcnt(11)\n"
      "v_mfma_f32_16x16x32_bf16 v[120:123], a[120:123], v[224:227], v[120:123]\n"
      "v_mad_i32_i24 v238, v251, s96, v238\n"
      "s_add_u32 s56, s22, 0x2a000\n"
      "s_addc_u32 s57, s23, 0\n"
      "s_add_u32 m0, s18, 0x6e00\n"
      "v_mfma_f32_16x16x32_bf16 v[124:127], a[124:127], v[224:227], v[124:127]\n"
      "global_load_lds_dwordx4 v238, s[56:57] offset:512 \n"
      "v_mfma_f32_16x16x32_bf16 v[128:131], a[128:131], v[224:227], v[128:131]\n"
      "ds_read_b128 v[220:223], v233 offset:32768\n"
      "v_add_u32 v233, 0xffff7000, v233\n"
      "v_mfma_f32_16x16x32_bf16 v[132:135], a[132:135], v[224:227], v[132:135]\n"
      "v_mad_i32_i24 v238, v251, s97, v238\n"
      "s_add_u32 s56, s22, 0x30000\n"
      "s_addc_u32 s57, s23, 0\n"
      "s_add_u32 m0, s18, 0x7e00\n"
      "v_mfma_f32_16x16x32_bf16 v[136:139], a[136:139], v[224:227], v[136:139]\n"
      "global_load_lds_dwordx4 v238, s[56:57] offset:512 \n"
      "v_mfma_f32_16x16x32_bf16 v[140:143], a[140:143], v[224:227], v[140:143]\n"
      "s_waitcnt lgkmcnt(7)\n"
      "v_mfma_f32_16x16x32_bf16 v[144:147], a[120:123], v[228:231], v[144:147]\n"
      "v_mfma_f32_16x16x32_bf16 v[148:151], a[124:127], v[228:231], v[148:151]\n"
      "global_load_dwordx4 a[252:255], v239, s[58:59] offset:3072 sc1\n"
      "v_mfma_f32_16x16x32_bf16 v[152:155], a[128:131], v[228:231], v[152:155]\n"
      "ds_read_b128 v[224:227], v234 offset:0\n"
      "v_mfma_f32_16x16x32_bf16 v[156:159], a[132:135], v[228:231], v[156:159]\n"
      "v_mfma_f32_16x16x32_bf16 v[160:163], a[136:139], v[228:231], v[160:163]\n"
      "v_mfma_f32_16x16x32_bf16 v[164:167], a[140:143], v[228:231], v[164:167]\n"
      "s_add_u32 s58, s24, 0x1000\n"
      "s_addc_u32 s59, s25, 0\n"
      "global_load_dwordx4 a[48:51], v239, s[58:59] offset:0 sc1\n"
      "s_waitcnt lgkmcnt(3)\n"
      "v_mfma_f32_16x16x32_bf16 v[168:171], a[120:123], v[216:219], v[168:171]\n"
      "s_waitcnt vmcnt(11)\n"
      "v_mfma_f32_16x16x32_bf16 v[172:175], a[124:127], v[216:219], v[172:175]\n"
      "ds_read_b64_tr_b16 a[24:25], v243 offset:49152\n"
      "v_mfma_f32_16x16x32_bf16 v[176:179], a[128:131], v[216:219], v[176:179]\n"
      "ds_read_b128 v[228:231], v234 offset:4096\n"
      "v_mfma_f32_16x16x32_bf16 v[180:183], a[132:135], v[216:219], v[180:183]\n"
      "ds_read_b64_tr_b16 a[26:27], v243 offset:49920\n"
      "global_load_dwordx4 a[52:55], v239, s[58:59] offset:1024 sc1\n"
      "v_mfma_f32_16x16x32_bf16 v[184:187], a[136:139], v[216:219], v[184:187]\n"
      "LPD5:\n"
      "s_cbranch_execz LPD8\n"
      "ds_read_b64_tr_b16 a[28:29], v243 offset:49160\n"
      "v_mfma_f32_16x16x32_bf16 v[188:191], a[140:143], v[216:219], v[188:191]\n"
      "s_waitcnt lgkmcnt(5)\n"
      "v_mfma_f32_16x16x32_bf16 v[192:195], a[120:123], v[220:223], v[192:195]\n"
      "ds_read_b64_tr_b16 a[30:31], v243 offset:49928\n"
      "v_mfma_f32_16x16x32_bf16 v[196:199], a[124:127], v[220:223], v[196:199]\n"
      "ds_read_b64_tr_b16 a[32:33], v243 offset:49216\n"
      "global_load_dwordx4 a[56:59], v239, s[58:59] offset:2048 sc1\n"
      "v_mfma_f32_16x16x32_bf16 v[200:203], a[128:131], v[220:223], v[200:203]\n"
      "ds_read_b128 v[216:219], v234 offset:8192\n"
      "v_mfma_f32_16x16x32_bf16 v[204:207], a[132:135], v[220:223], v[204:207]\n"
      "ds_read_b64_tr_b16 a[34:35], v243 offset:49984\n"
      "v_mfma_f32_16x16x32_bf16 v[208:211], a[136:139], v[220:223], v[208:211]\n"
      "ds_read_b64_tr_b16 a[36:37], v243 offset:49224\n"
      "v_mfma_f32_16x16x32_bf16 v[212:215], a[140:143], v[220:223], v[212:215]\n"
      "global_load_dwordx4 a[60:63], v239, s[58:59] offset:3072 sc1\n"
      "s_waitcnt lgkmcnt(9)\n"
      "v_mfma_f32_16x16x32_bf16 v[0:3], a[144:147], v[224:227], v[0:3]\n"
      "ds_read_b64_tr_b16 a[38:39], v243 offset:49992\n"
      "v_mfma_f32_16x16x32_bf16 v[4:7], a[148:151], v[224:227], v[4:7]\n"
      "ds_read_b64_tr_b16 a[40:41], v243 offset:49280\n"
      "v_mfma_f32_16x16x32_bf16 v[8:11], a[152:155], v[224:227], v[8:11]\n"
      "ds_read_b128 v[220:223], v234 offset:12288\n"
      "v_mfma_f32_16x16x32_bf16 v[12:15], a[156:159], v[224:227], v[12:15]\n"
      "ds_read_b64_tr_b16 a[42:43], v243 offset:50048\n"
      "s_add_u32 s58, s24, 0x2000\n"
      "s_addc_u32 s59, s25, 0\n"
      "global_load_dwordx4 a[64:67], v239, s[58:59] offset:0 sc1\n"
      "v_mfma_f32_16x16x32_bf16 v[16:19], a[160:163], v[224:227], v[16:19]\n"
      "ds_read_b64_tr_b16 a[44:45], v243 offset:49288\n"
      "v_mfma_f32_16x16x32_bf16 v[20:23], a[164:167], v[224:227], v[20:23]\n"
      "s_waitcnt lgkmcnt(12)\n"
      "v_mfma_f32_16x16x32_bf16 v[24:27], a[144:147], v[228:231], v[24:27]\n"
      "ds_read_b64_tr_b16 a[46:47], v243 offset:50056\n"
      "v_mfma_f32_16x16x32_bf16 v[28:31], a[148:151], v[228:231], v[28:31]\n"
      "global_load_dwordx4 a[68:71], v239, s[58:59] offset:1024 sc1\n"
      "v_mfma_f32_16x16x32_bf16 v[32:35], a[152:155], v[228:231], v[32:35]\n"
      "ds_read_b128 v[224:227], v234 offset:16384\n"
      "v_mfma_f32_16x16x32_bf16 v[36:39], a[156:159], v[228:231], v[36:39]\n"
      "v_mfma_f32_16x16x32_bf16 v[40:43], a[160:163], v[228:231], v[40:43]\n"
      "v_mfma_f32_16x16x32_bf16 v[44:47], a[164:167], v[228:231], v[44:47]\n"
      "global_load_dwordx4 a[72:75], v239, s[58:59] offset:2048 sc1\n"
      "s_waitcnt lgkmcnt(9)\n"
      "v_mfma_f32_16x16x32_bf16 v[48:51], a[144:147], v[216:219], v[48:51]\n"
      "v_mfma_f32_16x16x32_bf16 v[52:55], a[148:151], v[216:219], v[52:55]\n"
      "v_mfma_f32_16x16x32_bf16 v[56:59], a[152:155], v[216:219], v[56:59]\n"
      "ds_read_b128 v[228:231], v234 offset:20480\n"
      "v_mfma_f32_16x16x32_bf16 v[60:63], a[156:159], v[216:219], v[60:63]\n"
      "global_load_dwordx4 a[76:79], v239, s[58:59] offset:3072 sc1\n"
      "v_mfma_f32_16x16x32_bf16 v[64:67], a[160:163], v[216:219], v[64:67]\n"
      "v_mfma_f32_16x16x32_bf16 v[68:71], a[164:167], v[216:219], v[68:71]\n"
      "s_waitcnt lgkmcnt(5)\n"
      "v_mfma_f32_16x16x32_bf16 v[72:75], a[144:147], v[220:223], v[72:75]\n"
      "v_mfma_f32_16x16x32_bf16 v[76:79], a[148:151], v[220:223], v[76:79]\n"
      "s_add_u32 s58, s24, 0x3000\n"
      "s_addc_u32 s59, s25, 0\n"
      "global_load_dwordx4 a[80:83], v239, s[58:59] offset:0 sc1\n"
      "v_mfma_f32_16x16x32_bf16 v[80:83], a[152:155], v[220:223], v[80:83]\n"
      "ds_read_b128 v[216:219], v234 offset:24576\n"
      "v_mfma_f32_16x16x32_bf16 v[84:87], a[156:159], v[220:223], v[84:87]\n"
      "v_mfma_f32_16x16x32_bf16 v[88:91], a[160:163], v[220:223], v[88:91]\n"
      "v_mfma_f32_16x16x32_bf16 v[92:95], a[164:167], v[220:223], v[92:95]\n"
      "global_load_dwordx4 a[84:87], v239, s[58:59] offset:1024 sc1\n"
      "s_waitcnt lgkmcnt(2)\n"
      "v_mfma_f32_16x16x32_bf16 v[96:99], a[144:147], v[224:227], v[96:99]\n"
      "v_mfma_f32_16x16x32_bf16 v[100:103], a[148:151], v[224:227], v[100:103]\n"
      "v_mfma_f32_16x16x32_bf16 v[104:107], a[152:155], v[224:227], v[104:107]\n"
      "ds_read_b128 v[220:223], v234 offset:28672\n"
      "v_mfma_f32_16x16x32_bf16 v[108:111], a[156:159], v[224:227], v[108:111]\n"
      "global_load_dwordx4 a[88:91], v239, s[58:59] offset:2048 sc1\n"
      "v_mfma_f32_16x16x32_bf16 v[112:115], a[160:163], v[224:227], v[112:115]\n"
      "v_mfma_f32_16x16x32_bf16 v[116:119], a[164:167], v[224:227], v[116:119]\n"
      "s_waitcnt lgkmcnt(2)\n"
      "v_mfma_f32_16x16x32_bf16 v[120:123], a[144:147], v[228:231], v[120:123]\n"
      "v_mfma_f32_16x16x32_bf16 v[124:127], a[148:151], v[228:231], v[124:127]\n"
      "global_load_dwordx4 a[92:95], v239, s[58:59] offset:3072 sc1\n"
      "v_mfma_f32_16x16x32_bf16 v[128:131], a[152:155], v[228:231], v[128:131]\n"
      "ds_read_b128 v[224:227], v234 offset:32768\n"
      "v_add_u32 v234, 0xffff7000, v234\n"
      "v_mfma_f32_16x16x32_bf16 v[132:135], a[156:159], v[228:231], v[132:135]\n"
      "v_mfma_f32_16x16x32_bf16 v[136:139], a[160:163], v[228:231], v[136:139]\n"
      "v_mfma_f32_16x16x32_bf16 v[140:143], a[164:167], v[228:231], v[140:143]\n"
      "s_add_u32 s58, s24, 0x4000\n"
      "s_addc_u32 s59, s25, 0\n"
      "global_load_dwordx4 a[96:99], v239, s[58:59] offset:0 sc1\n"
      "s_waitcnt lgkmcnt(2)\n"
      "v_mfma_f32_16x16x32_bf16 v[144:147], a[144:147], v[216:219], v[144:147]\n"
      "ds_read_b64 v[236:237], v250 offset:4104\n"
      "v_mfma_f32_16x16x32_bf16 v[148:151], a[148:151], v[216:219], v[148:151]\n"
      "v_mfma_f32_16x16x32_bf16 v[152:155], a[152:155], v[216:219], v[152:155]\n"
      "ds_read_b128 v[228:231], v235 offset:0\n"
      "v_mfma_f32_16x16x32_bf16 v[156:159], a[156:159], v[216:219], v[156:159]\n"
      "global_load_dwordx4 a[100:103], v239, s[58:59] offset:1024 sc1\n"
      "v_mfma_f32_16x16x32_bf16 v[160:163], a[160:163], v[216:219], v[160:163]\n"
      "v_mfma_f32_16x16x32_bf16 v[164:167], a[164:167], v[216:219], v[164:167]\n"
      "s_waitcnt lgkmcnt(3)\n"
      "v_mfma_f32_16x16x32_bf16 v[168:171], a[144:147], v[220:223], v[168:171]\n"
      "v_mfma_f32_16x16x32_bf16 v[172:175], a[148:151], v[220:223], v[172:175]\n"
      "global_load_dwordx4 a[104:107], v239, s[58:59] offset:2048 sc1\n"
      "v_mfma_f32_16x16x32_bf16 v[176:179], a[152:155], v[220:223], v[176:179]\n"
      "ds_read_b128 v[216:219], v235 offset:4096\n"
      "v_mfma_f32_16x16x32_bf16 v[180:183], a[156:159], v[220:223], v[180:183]\n"
      "v_mfma_f32_16x16x32_bf16 v[184:187], a[160:163], v[220:223], v[184:187]\n"
      "v_mfma_f32_16x16x32_bf16 v[188:191], a[164:167], v[220:223], v[188:191]\n"
      "global_load_dwordx4 a[108:111], v239, s[58:59] offset:3072 sc1\n"
      "s_waitcnt lgkmcnt(3)\n"
      "v_mfma_f32_16x16x32_bf16 v[192:195], a[144:147], v[224:227], v[192:195]\n"
      "v_mfma_f32_16x16x32_bf16 v[196:199], a[148:151], v[224:227], v[196:199]\n"
      "v_mfma_f32_16x16x32_bf16 v[200:203], a[152:155], v[224:227], v[200:203]\n"
      "ds_read_b128 v[220:223], v235 offset:8192\n"
      "v_mfma_f32_16x16x32_bf16 v[204:207], a[156:159], v[224:227], v[204:207]\n"
      "s_add_u32 s58, s24, 0x5000\n"
      "s_addc_u32 s59, s25, 0\n"
      "global_load_dwordx4 a[112:115], v239, s[58:59] offset:0 sc1\n"
      "v_mfma_f32_16x16x32_bf16 v[208:211], a[160:163], v[224:227], v[208:211]\n"
      "v_mfma_f32_16x16x32_bf16 v[212:215], a[164:167], v[224:227], v[212:215]\n"
      "s_waitcnt lgkmcnt(2)\n"
      "v_mfma_f32_16x16x32_bf16 v[0:3], a[168:171], v[228:231], v[0:3]\n"
      "v_mfma_f32_16x16x32_bf16 v[4:7], a[172:175], v[228:231], v[4:7]\n"
      "global_load_dwordx4 a[116:119], v239, s[58:59] offset:1024 sc1\n"
      "v_mfma_f32_16x16x32_bf16 v[8:11], a[176:179], v[228:231], v[8:11]\n"
      "ds_read_b128 v[224:227], v235 offset:12288\n"
      "v_mfma_f32_16x16x32_bf16 v[12:15], a[180:183], v[228:231], v[12:15]\n"
      "v_mfma_f32_16x16x32_bf16 v[16:19], a[184:187], v[228:231], v[16:19]\n"
      "v_mfma_f32_16x16x32_bf16 v[20:23], a[188:191], v[228:231], v[20:23]\n"
      "global_load_dwordx4 a[120:123], v239, s[58:59] offset:2048 sc1\n"
      "s_waitcnt lgkmcnt(2)\n"
      "v_mfma_f32_16x16x32_bf16 v[24:27], a[168:171], v[216:219], v[24:27]\n"
      "v_mfma_f32_16x16x32_bf16 v[28:31], a[172:175], v[216:219], v[28:31]\n"
      "v_mfma_f32_16x16x32_bf16 v[32:35], a[176:179], v[216:219], v[32:35]\n"
      "ds_read_b128 v[228:231], v235 offset:16384\n"
      "v_mfma_f32_16x16x32_bf16 v[36:39], a[180:183], v[216:219], v[36:39]\n"
      "global_load_dwordx4 a[124:127], v239, s[58:59] offset:3072 sc1\n"
      "v_mfma_f32_16x16x32_bf16 v[40:43], a[184:187], v[216:219], v[40:43]\n"
      "v_mfma_f32_16x16x32_bf16 v[44:47], a[188:191], v[216:219], v[44:47]\n"
      "s_waitcnt lgkmcnt(2)\n"
      "v_mfma_f32_16x16x32_bf16 v[48:51], a[168:171], v[220:223], v[48:51]\n"
      "v_mfma_f32_16x16x32_bf16 v[52:55], a[172:175], v[220:223], v[52:55]\n"
      "s_add_u32 s58, s24, 0x6000\n"
      "s_addc_u32 s59, s25, 0\n"
      "global_load_dwordx4 a[128:131], v239, s[58:59] offset:0 sc1\n"
      "v_mfma_f32_16x16x32_bf16 v[56:59], a[176:179], v[220:223], v[56:59]\n"
      "ds_read_b128 v[216:219], v235 offset:20480\n"
      "v_mfma_f32_16x16x32_bf16 v[60:63], a[180:183], v[220:223], v[60:63]\n"
      "v_mfma_f32_16x16x32_bf16 v[64:67], a[184:187], v[220:223], v[64:67]\n"
      "v_mfma_f32_16x16x32_bf16 v[68:71], a[188:191], v[220:223], v[68:71]\n"
      "global_load_dwordx4 a[132:135], v239, s[58:59] offset:1024 sc1\n"
      "s_waitcnt lgkmcnt(2)\n"
      "v_mfma_f32_16x16x32_bf16 v[72:75], a[168:171], v[224:227], v[72:75]\n"
      "v_mfma_f32_16x16x32_bf16 v[76:79], a[172:175], v[224:227], v[76:79]\n"
      "v_mfma_f32_16x16x32_bf16 v[80:83], a[176:179], v[224:227], v[80:83]\n"
      "ds_read_b128 v[220:223], v235 offset:24576\n"
      "v_mfma_f32_16x16x32_bf16 v[84:87], a[180:183], v[224:227], v[84:87]\n"
      "global_load_dwordx4 a[136:139], v239, s[58:59] offset:2048 sc1\n"
      "v_mfma_f32_16x16x32_bf16 v[88:91], a[184:187], v[224:227], v[88:91]\n"
      "v_mfma_f32_16x16x32_bf16 v[92:95], a[188:191], v[224:227], v[92:95]\n"
      "s_waitcnt lgkmcnt(2)\n"
      "v_mfma_f32_16x16x32_bf16 v[96:99], a[168:171], v[228:231], v[96:99]\n"
      "v_mfma_f32_16x16x32_bf16 v[100:103], a[172:175], v[228:231], v[100:103]\n"
      "global_load_dwordx4 a[140:143], v239, s[58:59] offset:3072 sc1\n"
      "v_mfma_f32_16x16x32_bf16 v[104:107], a[176:179], v[228:231], v[104:107]\n"
      "ds_read_b128 v[224:227], v235 offset:28672\n"
      "v_mfma_f32_16x16x32_bf16 v[108:111], a[180:183], v[228:231], v[108:111]\n"
      "v_mfma_f32_16x16x32_bf16 v[112:115], a[184:187], v[228:231], v[112:115]\n"
      "v_mfma_f32_16x16x32_bf16 v[116:119], a[188:191], v[228:231], v[116:119]\n"
      "s_add_u32 s58, s24, 0x7000\n"
      "s_addc_u32 s59, s25, 0\n"
      "global_load_dwordx4 a[144:147], v239, s[58:59] offset:0 sc1\n"
      "s_waitcnt lgkmcnt(2)\n"
      "v_mfma_f32_16x16x32_bf16 v[120:123], a[168:171], v[216:219], v[120:123]\n"
      "v_mfma_f32_16x16x32_bf16 v[124:127], a[172:175], v[216:219], v[124:127]\n"
      "v_mfma_f32_16x16x32_bf16 v[128:131], a[176:179], v[216:219], v[128:131]\n"
      "ds_read_b128 v[228:231], v235 offset:32768\n"
      "v_add_u32 v235, 0xffff7000, v235\n"
      "v_mfma_f32_16x16x32_bf16 v[132:135], a[180:183], v[216:219], v[132:135]\n"
      "global_load_dwordx4 a[148:151], v239, s[58:59] offset:1024 sc1\n"
      "v_mfma_f32_16x16x32_bf16 v[136:139], a[184:187], v[216:219], v[136:139]\n"
      "v_mfma_f32_16x16x32_bf16 v[140:143], a[188:191], v[216:219], v[140:143]\n"
      "s_waitcnt lgkmcnt(2)\n"
      "v_mfma_f32_16x16x32_bf16 v[144:147], a[168:171], v[220:223], v[144:147]\n"
      "v_mfma_f32_16x16x32_bf16 v[148:151], a[172:175], v[220:223], v[148:151]\n"
      "s_waitcnt vmcnt(27)\n"
      "s_barrier\n"
      "global_load_dwordx4 a[152:155], v239, s[58:59] offset:2048 sc1\n"
      "v_mfma_f32_16x16x32_bf16 v[152:155], a[176:179], v[220:223], v[152:155]\n"
      "ds_read_b128 v[216:219], v232 offset:0\n"
      "v_mfma_f32_16x16x32_bf16 v[156:159], a[180:183], v[220:223], v[156:159]\n"
      "v_mfma_f32_16x16x32_bf16 v[160:163], a[184:187], v[220:223], v[160:163]\n"
      "v_mfma_f32_16x16x32_bf16 v[164:167], a[188:191], v[220:223], v[164:167]\n"
      "global_load_dwordx4 a[156:159], v239, s[58:59] offset:3072 sc1\n"
      "s_waitcnt lgkmcnt(2)\n"
      "v_mfma_f32_16x16x32_bf16 v[168:171], a[168:171], v[224:227], v[168:171]\n"
      "v_mfma_f32_16x16x32_bf16 v[172:175], a[172:175], v[224:227], v[172:175]\n"
      "v_mfma_f32_16x16x32_bf16 v[176:179], a[176:179], v[224:227], v[176:179]\n"
      "ds_read_b128 v[220:223], v232 offset:4096\n"
      "v_mfma_f32_16x16x32_bf16 v[180:183], a[180:183], v[224:227], v[180:183]\n"
      "s_add_u32 s58, s24, 0x8000\n"
      "s_addc_u32 s59, s25, 0\n"
      "global_load_dwordx4 a[160:163], v239, s[58:59] offset:0 sc1\n"
      "v_mfma_f32_16x16x32_bf16 v[184:187], a[184:187], v[224:227], v[184:187]\n"
      "v_mfma_f32_16x16x32_bf16 v[188:191], a[188:191], v[224:227], v[188:191]\n"
      "s_waitcnt lgkmcnt(2)\n"
      "v_mfma_f32_16x16x32_bf16 v[192:195], a[168:171], v[228:231], v[192:195]\n"
      "v_mfma_f32_16x16x32_bf16 v[196:199], a[172:175], v[228:231], v[196:199]\n"
      "global_load_dwordx4 a[164:167], v239, s[58:59] offset:1024 sc1\n"
      "v_mfma_f32_16x16x32_bf16 v[200:203], a[176:179], v[228:231], v[200:203]\n"
      "ds_read_b128 v[224:227], v232 offset:8192\n"
      "v_mfma_f32_16x16x32_bf16 v[204:207], a[180:183], v[228:231], v[204:207]\n"
      "v_mfma_f32_16x16x32_bf16 v[208:211], a[184:187], v[228:231], v[208:211]\n"
      "v_mfma_f32_16x16x32_bf16 v[212:215], a[188:191], v[228:231], v[212:215]\n"
      "s_waitcnt lgkmcnt(2)\n"
      "v_mfma_f32_16x16x32_bf16 v[0:3], a[192:195], v[216:219], v[0:3]\n"
      "global_load_dwordx4 a[168:171], v239, s[58:59] offset:2048 sc1\n"
      "v_mfma_f32_16x16x32_bf16 v[4:7], a[196:199], v[216:219], v[4:7]\n"
      "v_mfma_f32_16x16x32_bf16 v[8:11], a[200:203], v[216:219], v[8:11]\n"
      "ds_read_b128 v[228:231], v232 offset:12288\n"
      "v_mfma_f32_16x16x32_bf16 v[12:15], a[204:207], v[216:219], v[12:15]\n"
      "v_mfma_f32_16x16x32_bf16 v[16:19], a[208:211], v[216:219], v[16:19]\n"
      "global_load_dwordx4 a[172:175], v239, s[58:59] offset:3072 sc1\n"
      "v_mfma_f32_16x16x32_bf16 v[20:23], a[212:215], v[216:219], v[20:23]\n"
      "s_waitcnt lgkmcnt(2)\n"
      "v_mfma_f32_16x16x32_bf16 v[24:27], a[192:195], v[220:223], v[24:27]\n"
      "v_mfma_f32_16x16x32_bf16 v[28:31], a[196:199], v[220:223], v[28:31]\n"
      "v_mfma_f32_16x16x32_bf16 v[32:35], a[200:203], v[220:223], v[32:35]\n"
      "ds_read_b128 v[216:219], v232 offset:16384\n"
      "s_add_u32 s58, s24, 0x9000\n"
      "s_addc_u32 s59, s25, 0\n"
      "global_load_dwordx4 a[176:179], v239, s[58:59] offset:0 sc1\n"
      "v_mfma_f32_16x16x32_bf16 v[36:39], a[204:207], v[220:223], v[36:39]\n"
      "v_mfma_f32_16x16x32_bf16 v[40:43], a[208:211], v[220:223], v[40:43]\n"
      "v_mfma_f32_16x16x32_bf16 v[44:47], a[212:215], v[220:223], v[44:47]\n"
      "s_waitcnt lgkmcnt(2)\n"
      "v_mfma_f32_16x16x32_bf16 v[48:51], a[192:195], v[224:227], v[48:51]\n"
      "global_load_dwordx4 a[180:183], v239, s[58:59] offset:1024 sc1\n"
      "v_mfma_f32_16x16x32_bf16 v[52:55], a[196:199], v[224:227], v[52:55]\n"
      "v_mfma_f32_16x16x32_bf16 v[56:59], a[200:203], v[224:227], v[56:59]\n"
      "ds_read_b128 v[220:223], v232 offset:20480\n"
      "v_mfma_f32_16x16x32_bf16 v[60:63], a[204:207], v[224:227], v[60:63]\n"
      "v_mfma_f32_16x16x32_bf16 v[64:67], a[208:211], v[224:227], v[64:67]\n"
      "global_load_dwordx4 a[184:187], v239, s[58:59] offset:2048 sc1\n"
      "v_mfma_f32_16x16x32_bf16 v[68:71], a[212:215], v[224:227], v[68:71]\n"
      "s_waitcnt lgkmcnt(2)\n"
      "v_mfma_f32_16x16x32_bf16 v[72:75], a[192:195], v[228:231], v[72:75]\n"
      "v_mfma_f32_16x16x32_bf16 v[76:79], a[196:199], v[228:231], v[76:79]\n"
      "v_mfma_f32_16x16x32_bf16 v[80:83], a[200:203], v[228:231], v[80:83]\n"
      "ds_read_b128 v[224:227], v232 offset:24576\n"
      "global_load_dwordx4 a[188:191], v239, s[58:59] offset:3072 sc1\n"
      "v_mfma_f32_16x16x32_bf16 v[84:87], a[204:207], v[228:231], v[84:87]\n"
      "v_mfma_f32_16x16x32_bf16 v[88:91], a[208:211], v[228:231], v[88:91]\n"
      "v_mfma_f32_16x16x32_bf16 v[92:95], a[212:215], v[228:231], v[92:95]\n"
      "s_waitcnt lgkmcnt(2)\n"
      "v_mfma_f32_16x16x32_bf16 v[96:99], a[192:195], v[216:219], v[96:99]\n"
      "s_mul_i32 s78, s98, s19\n"
      "v_mfma_f32_16x16x32_bf16 v[100:103], a[196:199], v[216:219], v[100:103]\n"
      "v_add_u32 v236, s78, v236\n"
      "v_mfma_f32_16x16x32_bf16 v[104:107], a[200:203], v[216:219], v[104:107]\n"
      "ds_read_b128 v[228:231], v232 offset:28672\n"
      "s_mul_i32 s78, s78, s99\n"
      "v_mfma_f32_16x16x32_bf16 v[108:111], a[204:207], v[216:219], v[108:111]\n"
      "v_add_u32 v237, s78, v237\n"
      "v_mfma_f32_16x16x32_bf16 v[112:115], a[208:211], v[216:219], v[112:115]\n"
      "s_add_u32 s52, s20, s32\n"
      "s_addc_u32 s53, s21, 0\n"
      "s_add_u32 m0, s17, 0x0\n"
      "v_mfma_f32_16x16x32_bf16 v[116:119], a[212:215], v[216:219], v[116:119]\n"
      "global_load_lds_dwordx4 v236, s[52:53] offset:0 \n"
      "s_waitcnt lgkmcnt(2)\n"
      "v_mfma_f32_16x16x32_bf16 v[120:123], a[192:195], v[220:223], v[120:123]\n"
      "v_mfma_f32_16x16x32_bf16 v[124:127], a[196:199], v[220:223], v[124:127]\n"
      "s_add_u32 m0, s17, 0x1f00\n"
      "v_mfma_f32_16x16x32_bf16 v[128:131], a[200:203], v[220:223], v[128:131]\n"
      "ds_read_b128 v[216:219], v232 offset:32768\n"
      "v_add_u32 v232, v249, v232\n"
      "global_load_lds_dwordx4 v236, s[52:53] offset:256 \n"
      "v_mfma_f32_16x16x32_bf16 v[132:135], a[204:207], v[220:223], v[132:135]\n"
      "v_mfma_f32_16x16x32_bf16 v[136:139], a[208:211], v[220:223], v[136:139]\n"
      "s_add_u32 m0, s17, 0x3e00\n"
      "v_mfma_f32_16x16x32_bf16 v[140:143], a[212:215], v[220:223], v[140:143]\n"
      "global_load_lds_dwordx4 v236, s[52:53] offset:512 \n"
      "s_waitcnt lgkmcnt(2)\n"
      "v_mfma_f32_16x16x32_bf16 v[144:147], a[192:195], v[224:227], v[144:147]\n"
      "v_mfma_f32_16x16x32_bf16 v[148:151], a[196:199], v[224:227], v[148:151]\n"
      "s_add_u32 m0, s17, 0x1000\n"
      "v_mfma_f32_16x16x32_bf16 v[152:155], a[200:203], v[224:227], v[152:155]\n"
      "ds_read_b128 v[220:223], v233 offset:0\n"
      "global_load_lds_dwordx4 v237, s[52:53] offset:0 \n"
      "v_mfma_f32_16x16x32_bf16 v[156:159], a[204:207], v[224:227], v[156:159]\n"
      "v_mfma_f32_16x16x32_bf16 v[160:163], a[208:211], v[224:227], v[160:163]\n"
      "s_add_u32 m0, s17, 0x2f00\n"
      "v_mfma_f32_16x16x32_bf16 v[164:167], a[212:215], v[224:227], v[164:167]\n"
      "global_load_lds_dwordx4 v237, s[52:53] offset:256 \n"
      "s_waitcnt lgkmcnt(2)\n"
      "v_mfma_f32_16x16x32_bf16 v[168:171], a[192:195], v[228:231], v[168:171]\n"
      "v_mfma_f32_16x16x32_bf16 v[172:175], a[196:199], v[228:231], v[172:175]\n"
      "s_add_u32 m0, s17, 0x4e00\n"
      "v_mfma_f32_16x16x32_bf16 v[176:179], a[200:203], v[228:231], v[176:179]\n"
      "ds_read_b128 v[224:227], v233 offset:4096\n"
      "global_load_lds_dwordx4 v237, s[52:53] offset:512 \n"
      "v_mfma_f32_16x16x32_bf16 v[180:183], a[204:207], v[228:231], v[180:183]\n"
      "v_mfma_f32_16x16x32_bf16 v[184:187], a[208:211], v[228:231], v[184:187]\n"
      "v_mfma_f32_16x16x32_bf16 v[188:191], a[212:215], v[228:231], v[188:191]\n"
      "s_waitcnt lgkmcnt(2)\n"
      "v_mfma_f32_16x16x32_bf16 v[192:195], a[192:195], v[216:219], v[192:195]\n"
      "v_mfma_f32_16x16x32_bf16 v[196:199], a[196:199], v[216:219], v[196:199]\n"
      "v_mfma_f32_16x16x32_bf16 v[200:203], a[200:203], v[216:219], v[200:203]\n"
      "ds_read_b128 v[228:231], v233 offset:8192\n"
      "v_mfma_f32_16x16x32_bf16 v[204:207], a[204:207], v[216:219], v[204:207]\n"
      "v_mfma_f32_16x16x32_bf16 v[208:211], a[208:211], v[216:219], v[208:211]\n"
      "v_mfma_f32_16x16x32_bf16 v[212:215], a[212:215], v[216:219], v[212:215]\n"
      "s_waitcnt lgkmcnt(2)\n"
      "v_mfma_f32_16x16x32_bf16 v[0:3], a[216:219], v[220:223], v[0:3]\n"
      "s_add_u32 s58, s24, 0xa000\n"
      "s_addc_u32 s59, s25, 0\n"
      "global_load_dwordx4 a[192:195], v239, s[58:59] offset:0 sc1\n"
      "v_mfma_f32_16x16x32_bf16 v[4:7], a[220:223], v[220:223], v[4:7]\n"
      "v_mfma_f32_16x16x32_bf16 v[8:11], a[224:227], v[220:223], v[8:11]\n"
      "ds_read_b128 v[216:219], v233 offset:12288\n"
      "v_mfma_f32_16x16x32_bf16 v[12:15], a[228:231], v[220:223], v[12:15]\n"
      "v_mfma_f32_16x16x32_bf16 v[16:19], a[232:235], v[220:223], v[16:19]\n"
      "global_load_dwordx4 a[196:199], v239, s[58:59] offset:1024 sc1\n"
      "v_mfma_f32_16x16x32_bf16 v[20:23], a[236:239], v[220:223], v[20:23]\n"
      "s_waitcnt lgkmcnt(2)\n"
      "v_mfma_f32_16x16x32_bf16 v[24:27], a[216:219], v[224:227], v[24:27]\n"
      "v_mfma_f32_16x16x32_bf16 v[28:31], a[220:223], v[224:227], v[28:31]\n"
      "v_mfma_f32_16x16x32_bf16 v[32:35], a[224:227], v[224:227], v[32:35]\n"
      "ds_read_b128 v[220:223], v233 offset:16384\n"
      "global_load_dwordx4 a[200:203], v239, s[58:59] offset:2048 sc1\n"
      "v_mfma_f32_16x16x32_bf16 v[36:39], a[228:231], v[224:227], v[36:39]\n"
      "v_mfma_f32_16x16x32_bf16 v[40:43], a[232:235], v[224:227], v[40:43]\n"
      "v_mfma_f32_16x16x32_bf16 v[44:47], a[236:239], v[224:227], v[44:47]\n"
      "s_waitcnt lgkmcnt(2)\n"
      "v_mfma_f32_16x16x32_bf16 v[48:51], a[216:219], v[228:231], v[48:51]\n"
      "global_load_dwordx4 a[204:207], v239, s[58:59] offset:3072 sc1\n"
      "v_mfma_f32_16x16x32_bf16 v[52:55], a[220:223], v[228:231], v[52:55]\n"
      "v_mfma_f32_16x16x32_bf16 v[56:59], a[224:227], v[228:231], v[56:59]\n"
      "ds_read_b128 v[224:227], v233 offset:20480\n"
      "v_mfma_f32_16x16x32_bf16 v[60:63], a[228:231], v[228:231], v[60:63]\n"
      "v_mfma_f32_16x16x32_bf16 v[64:67], a[232:235], v[228:231], v[64:67]\n"
      "s_add_u32 s58, s24, 0xb000\n"
      "s_addc_u32 s59, s25, 0\n"
      "global_load_dwordx4 a[208:211], v239, s[58:59] offset:0 sc1\n"
      "v_mfma_f32_16x16x32_bf16 v[68:71], a[236:239], v[228:231], v[68:71]\n"
      "s_waitcnt lgkmcnt(2)\n"
      "v_mfma_f32_16x16x32_bf16 v[72:75], a[216:219], v[216:219], v[72:75]\n"
      "v_mfma_f32_16x16x32_bf16 v[76:79], a[220:223], v[216:219], v[76:79]\n"
      "v_mfma_f32_16x16x32_bf16 v[80:83], a[224:227], v[216:219], v[80:83]\n"
      "ds_read_b128 v[228:231], v233 offset:24576\n"
      "global_load_dwordx4 a[212:215], v239, s[58:59] offset:1024 sc1\n"
      "v_mfma_f32_16x16x32_bf16 v[84:87], a[228:231], v[216:219], v[84:87]\n"
      "v_mfma_f32_16x16x32_bf16 v[88:91], a[232:235], v[216:219], v[88:91]\n"
      "v_mfma_f32_16x16x32_bf16 v[92:95], a[236:239], v[216:219], v[92:95]\n"
      "s_waitcnt lgkmcnt(2)\n"
      "v_mfma_f32_16x16x32_bf16 v[96:99], a[216:219], v[220:223], v[96:99]\n"
      "v_mfma_f32_16x16x32_bf16 v[100:103], a[220:223], v[220:223], v[100:103]\n"
      "v_mfma_f32_16x16x32_bf16 v[104:107], a[224:227], v[220:223], v[104:107]\n"
      "ds_read_b128 v[216:219], v233 offset:28672\n"
      "v_mfma_f32_16x16x32_bf16 v[108:111], a[228:231], v[220:223], v[108:111]\n"
      "v_mfma_f32_16x16x32_bf16 v[112:115], a[232:235], v[220:223], v[112:115]\n"
      "v_mfma_f32_16x16x32_bf16 v[116:119], a[236:239], v[220:223], v[116:119]\n"
      "s_waitcnt lgkmcnt(2)\n"
      "v_mfma_f32_16x16x32_bf16 v[120:123], a[216:219], v[224:227], v[120:123]\n"
      "v_mfma_f32_16x16x32_bf16 v[124:127], a[220:223], v[224:227], v[124:127]\n"
      "v_mfma_f32_16x16x32_bf16 v[128:131], a[224:227], v[224:227], v[128:131]\n"
      "ds_read_b128 v[220:223], v233 offset:32768\n"
      "v_add_u32 v233, v249, v233\n"
      "v_mfma_f32_16x16x32_bf16 v[132:135], a[228:231], v[224:227], v[132:135]\n"
      "v_mfma_f32_16x16x32_bf16 v[136:139], a[232:235], v[224:227], v[136:139]\n"
      "v_mfma_f32_16x16x32_bf16 v[140:143], a[236:239], v[224:227], v[140:143]\n"
      "s_waitcnt lgkmcnt(2)\n"
      "v_mfma_f32_16x16x32_bf16 v[144:147], a[216:219], v[228:231], v[144:147]\n"
      "v_mfma_f32_16x16x32_bf16 v[148:151], a[220:223], v[228:231], v[148:151]\n"
      "v_mfma_f32_16x16x32_bf16 v[152:155], a[224:227], v[228:231], v[152:155]\n"
      "ds_read_b128 v[224:227], v234 offset:0\n"
      "v_mfma_f32_16x16x32_bf16 v[156:159], a[228:231], v[228:231], v[156:159]\n"
      "v_mfma_f32_16x16x32_bf16 v[160:163], a[232:235], v[228:231], v[160:163]\n"
      "v_mfma_f32_16x16x32_bf16 v[164:167], a[236:239], v[228:231], v[164:167]\n"
      "s_waitcnt lgkmcnt(2)\n"
      "v_mfma_f32_16x16x32_bf16 v[168:171], a[216:219], v[216:219], v[168:171]\n"
      "v_mfma_f32_16x16x32_bf16 v[172:175], a[220:223], v[216:219], v[172:175]\n"
      "v_mfma_f32_16x16x32_bf16 v[176:179], a[224:227], v[216:219], v[176:179]\n"
      "ds_read_b128 v[228:231], v234 offset:4096\n"
      "v_mfma_f32_16x16x32_bf16 v[180:183], a[228:231], v[216:219], v[180:183]\n"
      "v_mfma_f32_16x16x32_bf16 v[184:187], a[232:235], v[216:219], v[184:187]\n"
      "s_add_u32 s52, s42, s32\n"
      "s_addc_u32 s53, s43, 0\n"
      "s_add_u32 m0, s17, 0x6000\n"
      "v_mfma_f32_16x16x32_bf16 v[188:191], a[236:239], v[216:219], v[188:191]\n"
      "global_load_lds_dwordx4 v236, s[52:53] offset:0 \n"
      "s_waitcnt lgkmcnt(2)\n"
      "v_mfma_f32_16x16x32_bf16 v[192:195], a[216:219], v[220:223], v[192:195]\n"
      "v_mfma_f32_16x16x32_bf16 v[196:199], a[220:223], v[220:223], v[196:199]\n"
      "s_add_u32 m0, s17, 0x7f00\n"
      "v_mfma_f32_16x16x32_bf16 v[200:203], a[224:227], v[220:223], v[200:203]\n"
      "ds_read_b128 v[216:219], v234 offset:8192\n"
      "global_load_lds_dwordx4 v236, s[52:53] offset:256 \n"
      "v_mfma_f32_16x16x32_bf16 v[204:207], a[228:231], v[220:223], v[204:207]\n"
      "v_mfma_f32_16x16x32_bf16 v[208:211], a[232:235], v[220:223], v[208:211]\n"
      "s_add_u32 m0, s17, 0x9e00\n"
      "v_mfma_f32_16x16x32_bf16 v[212:215], a[236:239], v[220:223], v[212:215]\n"
      "global_load_lds_dwordx4 v236, s[52:53] offset:512 \n"
      "s_waitcnt lgkmcnt(2)\n"
      "v_mfma_f32_16x16x32_bf16 v[0:3], a[0:3], v[224:227], v[0:3]\n"
      "ds_read_b128 v[244:247], v250\n"
      "v_mfma_f32_16x16x32_bf16 v[4:7], a[4:7], v[224:227], v[4:7]\n"
      "s_add_u32 m0, s17, 0x7000\n"
      "ds_read_b64 v[252:253], v250 offset:4096\n"
      "v_mfma_f32_16x16x32_bf16 v[8:11], a[8:11], v[224:227], v[8:11]\n"
      "ds_read_b128 v[220:223], v234 offset:12288\n"
      "global_load_lds_dwordx4 v237, s[52:53] offset:0 \n"
      "v_mfma_f32_16x16x32_bf16 v[12:15], a[12:15], v[224:227], v[12:15]\n"
      "v_mfma_f32_16x16x32_bf16 v[16:19], a[16:19], v[224:227], v[16:19]\n"
      "s_add_u32 m0, s17, 0x8f00\n"
      "v_mfma_f32_16x16x32_bf16 v[20:23], a[20:23], v[224:227], v[20:23]\n"
      "global_load_lds_dwordx4 v237, s[52:53] offset:256 \n"
      "s_waitcnt lgkmcnt(4)\n"
      "v_mfma_f32_16x16x32_bf16 v[24:27], a[0:3], v[228:231], v[24:27]\n"
      "v_mfma_f32_16x16x32_bf16 v[28:31], a[4:7], v[228:231], v[28:31]\n"
      "s_add_u32 m0, s17, 0xae00\n"
      "v_mfma_f32_16x16x32_bf16 v[32:35], a[8:11], v[228:231], v[32:35]\n"
      "ds_read_b128 v[224:227], v234 offset:16384\n"
      "global_load_lds_dwordx4 v237, s[52:53] offset:512 \n"
      "v_mfma_f32_16x16x32_bf16 v[36:39], a[12:15], v[228:231], v[36:39]\n"
      "v_mfma_f32_16x16x32_bf16 v[40:43], a[16:19], v[228:231], v[40:43]\n"
      "v_mfma_f32_16x16x32_bf16 v[44:47], a[20:23], v[228:231], v[44:47]\n"
      "global_load_dwordx4 a[216:219], v239, s[58:59] offset:2048 sc1\n"
      "s_waitcnt lgkmcnt(4)\n"
      "v_mfma_f32_16x16x32_bf16 v[48:51], a[0:3], v[216:219], v[48:51]\n"
      "v_mfma_f32_16x16x32_bf16 v[52:55], a[4:7], v[216:219], v[52:55]\n"
      "v_mfma_f32_16x16x32_bf16 v[56:59], a[8:11], v[216:219], v[56:59]\n"
      "ds_read_b128 v[228:231], v234 offset:20480\n"
      "v_mfma_f32_16x16x32_bf16 v[60:63], a[12:15], v[216:219], v[60:63]\n"
      "global_load_dwordx4 a[220:223], v239, s[58:59] offset:3072 sc1\n"
      "v_mfma_f32_16x16x32_bf16 v[64:67], a[16:19], v[216:219], v[64:67]\n"
      "v_mfma_f32_16x16x32_bf16 v[68:71], a[20:23], v[216:219], v[68:71]\n"
      "s_waitcnt lgkmcnt(2)\n"
      "v_mfma_f32_16x16x32_bf16 v[72:75], a[0:3], v[220:223], v[72:75]\n"
      "v_mfma_f32_16x16x32_bf16 v[76:79], a[4:7], v[220:223], v[76:79]\n"
      "s_add_u32 s58, s24, 0xc000\n"
      "s_addc_u32 s59, s25, 0\n"
      "global_load_dwordx4 a[224:227], v239, s[58:59] offset:0 sc1\n"
      "v_mfma_f32_16x16x32_bf16 v[80:83], a[8:11], v[220:223], v[80:83]\n"
      "ds_read_b128 v[216:219], v234 offset:24576\n"
      "v_mfma_f32_16x16x32_bf16 v[84:87], a[12:15], v[220:223], v[84:87]\n"
      "v_mfma_f32_16x16x32_bf16 v[88:91], a[16:19], v[220:223], v[88:91]\n"
      "v_mfma_f32_16x16x32_bf16 v[92:95], a[20:23], v[220:223], v[92:95]\n"
      "global_load_dwordx4 a[228:231], v239, s[58:59] offset:1024 sc1\n"
      "s_waitcnt lgkmcnt(2)\n"
      "v_mfma_f32_16x16x32_bf16 v[96:99], a[0:3], v[224:227], v[96:99]\n"
      "v_mfma_f32_16x16x32_bf16 v[100:103], a[4:7], v[224:227], v[100:103]\n"
      "v_mfma_f32_16x16x32_bf16 v[104:107], a[8:11], v[224:227], v[104:107]\n"
      "ds_read_b128 v[220:223], v234 offset:28672\n"
      "v_mfma_f32_16x16x32_bf16 v[108:111], a[12:15], v[224:227], v[108:111]\n"
      "global_load_dwordx4 a[232:235], v239, s[58:59] offset:2048 sc1\n"
      "v_mfma_f32_16x16x32_bf16 v[112:115], a[16:19], v[224:227], v[112:115]\n"
      "v_mfma_f32_16x16x32_bf16 v[116:119], a[20:23], v[224:227], v[116:119]\n"
      "s_waitcnt lgkmcnt(2)\n"
      "v_mfma_f32_16x16x32_bf16 v[120:123], a[0:3], v[228:231], v[120:123]\n"
      "v_mfma_f32_16x16x32_bf16 v[124:127], a[4:7], v[228:231], v[124:127]\n"
      "global_load_dwordx4 a[236:239], v239, s[58:59] offset:3072 sc1\n"
      "v_mfma_f32_16x16x32_bf16 v[128:131], a[8:11], v[228:231], v[128:131]\n"
      "ds_read_b128 v[224:227], v234 offset:32768\n"
      "v_add_u32 v234, v249, v234\n"
      "v_mfma_f32_16x16x32_bf16 v[132:135], a[12:15], v[228:231], v[132:135]\n"
      "v_mfma_f32_16x16x32_bf16 v[136:139], a[16:19], v[228:231], v[136:139]\n"
      "v_mfma_f32_16x16x32_bf16 v[140:143], a[20:23], v[228:231], v[140:143]\n"
      "s_waitcnt lgkmcnt(2)\n"
      "v_mfma_f32_16x16x32_bf16 v[144:147], a[0:3], v[216:219], v[144:147]\n"
      "v_mfma_f32_16x16x32_bf16 v[148:151], a[4:7], v[216:219], v[148:151]\n"
      "v_mfma_f32_16x16x32_bf16 v[152:155], a[8:11], v[216:219], v[152:155]\n"
      "ds_read_b128 v[228:231], v235 offset:4096\n"
      "v_mfma_f32_16x16x32_bf16 v[156:159], a[12:15], v[216:219], v[156:159]\n"
      "v_mfma_f32_16x16x32_bf16 v[160:163], a[16:19], v[216:219], v[160:163]\n"
      "v_mfma_f32_16x16x32_bf16 v[164:167], a[20:23], v[216:219], v[164:167]\n"
      "s_waitcnt lgkmcnt(2)\n"
      "v_mfma_f32_16x16x32_bf16 v[168:171], a[0:3], v[220:223], v[168:171]\n"
      "v_mfma_f32_16x16x32_bf16 v[172:175], a[4:7], v[220:223], v[172:175]\n"
      "v_mfma_f32_16x16x32_bf16 v[176:179], a[8:11], v[220:223], v[176:179]\n"
      "ds_read_b128 v[216:219], v235 offset:8192\n"
      "v_mfma_f32_16x16x32_bf16 v[180:183], a[12:15], v[220:223], v[180:183]\n"
      "v_mfma_f32_16x16x32_bf16 v[184:187], a[16:19], v[220:223], v[184:187]\n"
      "v_mfma_f32_16x16x32_bf16 v[188:191], a[20:23], v[220:223], v[188:191]\n"
      "s_waitcnt lgkmcnt(2)\n"
      "v_mfma_f32_16x16x32_bf16 v[192:195], a[0:3], v[224:227], v[192:195]\n"
      "v_mfma_f32_16x16x32_bf16 v[196:199], a[4:7], v[224:227], v[196:199]\n"
      "v_mfma_f32_16x16x32_bf16 v[200:203], a[8:11], v[224:227], v[200:203]\n"
      "ds_read_b128 v[220:223], v235 offset:12288\n"
      "v_mfma_f32_16x16x32_bf16 v[204:207], a[12:15], v[224:227], v[204:207]\n"
      "v_mfma_f32_16x16x32_bf16 v[208:211], a[16:19], v[224:227], v[208:211]\n"
      "v_mfma_f32_16x16x32_bf16 v[212:215], a[20:23], v[224:227], v[212:215]\n"
      "s_waitcnt lgkmcnt(2)\n"
      "v_mfma_f32_16x16x32_bf16 v[24:27], a[24:27], v[228:231], v[24:27]\n"
      "s_add_u32 s58, s24, 0xd000\n"
      "s_addc_u32 s59, s25, 0\n"
      "global_load_dwordx4 a[0:3], v239, s[58:59] offset:0 sc1\n"
      "v_mfma_f32_16x16x32_bf16 v[28:31], a[28:31], v[228:231], v[28:31]\n"
      "v_mfma_f32_16x16x32_bf16 v[32:35], a[32:35], v[228:231], v[32:35]\n"
      "ds_read_b128 v[224:227], v235 offset:16384\n"
      "v_mfma_f32_16x16x32_bf16 v[36:39], a[36:39], v[228:231], v[36:39]\n"
      "v_mfma_f32_16x16x32_bf16 v[40:43], a[40:43], v[228:231], v[40:43]\n"
      "global_load_dwordx4 a[4:7], v239, s[58:59] offset:1024 sc1\n"
      "v_mfma_f32_16x16x32_bf16 v[44:47], a[44:47], v[228:231], v[44:47]\n"
      "s_waitcnt lgkmcnt(2)\n"
      "v_mfma_f32_16x16x32_bf16 v[48:51], a[24:27], v[216:219], v[48:51]\n"
      "v_cvt_pk_bf16_f32 v24, v24, v25\n"
      "v_cvt_pk_bf16_f32 v25, v26, v27\n"
      "v_mfma_f32_16x16x32_bf16 v[52:55], a[28:31], v[216:219], v[52:55]\n"
      "v_cvt_pk_bf16_f32 v26, v28, v29\n"
      "v_cvt_pk_bf16_f32 v27, v30, v31\n"
      "v_mfma_f32_16x16x32_bf16 v[56:59], a[32:35], v[216:219], v[56:59]\n"
      "ds_read_b128 v[228:231], v235 offset:20480\n"
      "v_cvt_pk_bf16_f32 v28, v32, v33\n"
      "v_cvt_pk_bf16_f32 v29, v34, v35\n"
      "global_load_dwordx4 a[8:11], v239, s[58:59] offset:2048 sc1\n"
      "v_mfma_f32_16x16x32_bf16 v[60:63], a[36:39], v[216:219], v[60:63]\n"
      "v_cvt_pk_bf16_f32 v30, v36, v37\n"
      "v_cvt_pk_bf16_f32 v31, v38, v39\n"
      "v_mfma_f32_16x16x32_bf16 v[64:67], a[40:43], v[216:219], v[64:67]\n"
      "v_cvt_pk_bf16_f32 v32, v40, v41\n"
      "v_cvt_pk_bf16_f32 v33, v42, v43\n"
      "v_mfma_f32_16x16x32_bf16 v[68:71], a[44:47], v[216:219], v[68:71]\n"
      "v_cvt_pk_bf16_f32 v34, v44, v45\n"
      "v_cvt_pk_bf16_f32 v35, v46, v47\n"
      "s_waitcnt lgkmcnt(2)\n"
      "v_mfma_f32_16x16x32_bf16 v[72:75], a[24:27], v[220:223], v[72:75]\n"
      "v_cvt_pk_bf16_f32 v48, v48, v49\n"
      "v_cvt_pk_bf16_f32 v49, v50, v51\n"
      "global_load_dwordx4 a[12:15], v239, s[58:59] offset:3072 sc1\n"
      "v_mfma_f32_16x16x32_bf16 v[76:79], a[28:31], v[220:223], v[76:79]\n"
      "v_cvt_pk_bf16_f32 v50, v52, v53\n"
      "v_cvt_pk_bf16_f32 v51, v54, v55\n"
      "v_mfma_f32_16x16x32_bf16 v[80:83], a[32:35], v[220:223], v[80:83]\n"
      "ds_read_b128 v[216:219], v235 offset:24576\n"
      "v_cvt_pk_bf16_f32 v52, v56, v57\n"
      "v_cvt_pk_bf16_f32 v53, v58, v59\n"
      "v_mfma_f32_16x16x32_bf16 v[84:87], a[36:39], v[220:223], v[84:87]\n"
      "v_cvt_pk_bf16_f32 v54, v60, v61\n"
      "v_cvt_pk_bf16_f32 v55, v62, v63\n"
      "v_mfma_f32_16x16x32_bf16 v[88:91], a[40:43], v[220:223], v[88:91]\n"
      "v_cvt_pk_bf16_f32 v56, v64, v65\n"
      "v_cvt_pk_bf16_f32 v57, v66, v67\n"
      "s_add_u32 s58, s24, 0xe000\n"
      "s_addc_u32 s59, s25, 0\n"
      "global_load_dwordx4 a[16:19], v239, s[58:59] offset:0 sc1\n"
      "v_mfma_f32_16x16x32_bf16 v[92:95], a[44:47], v[220:223], v[92:95]\n"
      "v_cvt_pk_bf16_f32 v58, v68, v69\n"
      "v_cvt_pk_bf16_f32 v59, v70, v71\n"
      "s_waitcnt lgkmcnt(2)\n"
      "v_mfma_f32_16x16x32_bf16 v[96:99], a[24:27], v[224:227], v[96:99]\n"
      "v_cvt_pk_bf16_f32 v72, v72, v73\n"
      "v_cvt_pk_bf16_f32 v73, v74, v75\n"
      "LPD6:\n"
      "s_cbranch_execz LPD7\n"
      "v_mfma_f32_16x16x32_bf16 v[100:103], a[28:31], v[224:227], v[100:103]\n"
      "v_cvt_pk_bf16_f32 v74, v76, v77\n"
      "v_cvt_pk_bf16_f32 v75, v78, v79\n"
      "v_mfma_f32_16x16x32_bf16 v[104:107], a[32:35], v[224:227], v[104:107]\n"
      "ds_read_b128 v[220:223], v235 offset:28672\n"
      "v_cvt_pk_bf16_f32 v76, v80, v81\n"
      "v_cvt_pk_bf16_f32 v77, v82, v83\n"
      "global_load_dwordx4 a[20:23], v239, s[58:59] offset:1024 sc1\n"
      "v_mfma_f32_16x16x32_bf16 v[108:111], a[36:39], v[224:227], v[108:111]\n"
      "v_cvt_pk_bf16_f32 v78, v84, v85\n"
      "v_cvt_pk_bf16_f32 v79, v86, v87\n"
      "v_mfma_f32_16x16x32_bf16 v[112:115], a[40:43], v[224:227], v[112:115]\n"
      "v_cvt_pk_bf16_f32 v80, v88, v89\n"
      "v_cvt_pk_bf16_f32 v81, v90, v91\n"
      "v_mfma_f32_16x16x32_bf16 v[116:119], a[44:47], v[224:227], v[116:119]\n"
      "v_cvt_pk_bf16_f32 v82, v92, v93\n"
      "v_cvt_pk_bf16_f32 v83, v94, v95\n"
      "s_waitcnt lgkmcnt(2)\n"
      "v_mfma_f32_16x16x32_bf16 v[120:123], a[24:27], v[228:231], v[120:123]\n"
      "v_cvt_pk_bf16_f32 v96, v96, v97\n"
      "v_cvt_pk_bf16_f32 v97, v98, v99\n"
      "v_mfma_f32_16x16x32_bf16 v[124:127], a[28:31], v[228:231], v[124:127]\n"
      "v_cvt_pk_bf16_f32 v98, v100, v101\n"
      "v_cvt_pk_bf16_f32 v99, v102, v103\n"
      "v_mfma_f32_16x16x32_bf16 v[128:131], a[32:35], v[228:231], v[128:131]\n"
      "ds_read_b128 v[224:227], v235 offset:32768\n"
      "v_cvt_pk_bf16_f32 v100, v104, v105\n"
      "v_cvt_pk_bf16_f32 v101, v106, v107\n"
      "v_mfma_f32_16x16x32_bf16 v[132:135], a[36:39], v[228:231], v[132:135]\n"
      "v_cvt_pk_bf16_f32 v102, v108, v109\n"
      "v_cvt_pk_bf16_f32 v103, v110, v111\n"
      "v_mfma_f32_16x16x32_bf16 v[136:139], a[40:43], v[228:231], v[136:139]\n"
      "v_cvt_pk_bf16_f32 v104, v112, v113\n"
      "v_cvt_pk_bf16_f32 v105, v114, v115\n"
      "v_mfma_f32_16x16x32_bf16 v[140:143], a[44:47], v[228:231], v[140:143]\n"
      "v_cvt_pk_bf16_f32 v106, v116, v117\n"
      "v_cvt_pk_bf16_f32 v107, v118, v119\n"
      "s_waitcnt lgkmcnt(2)\n"
      "v_mfma_f32_16x16x32_bf16 v[144:147], a[24:27], v[216:219], v[144:147]\n"
      "v_cvt_pk_bf16_f32 v120, v120, v121\n"
      "v_cvt_pk_bf16_f32 v121, v122, v123\n"
      "v_mfma_f32_16x16x32_bf16 v[148:151], a[28:31], v[216:219], v[148:151]\n"
      "v_cvt_pk_bf16_f32 v122, v124, v125\n"
      "v_cvt_pk_bf16_f32 v123, v126, v127\n"
      "v_mfma_f32_16x16x32_bf16 v[152:155], a[32:35], v[216:219], v[152:155]\n"
      "ds_read_b128 v[228:231], v235 offset:0\n"
      "v_add_u32 v235, v249, v235\n"
      "s_waitcnt vmcnt(24)\n"
      "s_barrier\n"
      "v_cvt_pk_bf16_f32 v124, v128, v129\n"
      "v_cvt_pk_bf16_f32 v125, v130, v131\n"
      "v_mfma_f32_16x16x32_bf16 v[156:159], a[36:39], v[216:219], v[156:159]\n"
      "ds_read_b128 v[36:39], v232 offset:0\n"
      "v_cvt_pk_bf16_f32 v126, v132, v133\n"
      "v_cvt_pk_bf16_f32 v127, v134, v135\n"
      "v_mfma_f32_16x16x32_bf16 v[160:163], a[40:43], v[216:219], v[160:163]\n"
      "v_cvt_pk_bf16_f32 v128, v136, v137\n"
      "v_cvt_pk_bf16_f32 v129, v138, v139\n"
      "v_mfma_f32_16x16x32_bf16 v[164:167], a[44:47], v[216:219], v[164:167]\n"
      "ds_read_b128 v[40:43], v233 offset:0\n"
      "v_cvt_pk_bf16_f32 v130, v140, v141\n"
      "v_cvt_pk_bf16_f32 v131, v142, v143\n"
      "s_waitcnt lgkmcnt(4)\n"
      "v_mfma_f32_16x16x32_bf16 v[168:171], a[24:27], v[220:223], v[168:171]\n"
      "v_cvt_pk_bf16_f32 v144, v144, v145\n"
      "v_cvt_pk_bf16_f32 v145, v146, v147\n"
      "global_load_dwordx4 v[140:143], v239, s[58:59] offset:2048 sc1\n"
      "v_mfma_f32_16x16x32_bf16 v[172:175], a[28:31], v[220:223], v[172:175]\n"
      "ds_read_b128 v[44:47], v234 offset:0\n"
      "v_cvt_pk_bf16_f32 v146, v148, v149\n"
      "v_cvt_pk_bf16_f32 v147, v150, v151\n"
      "v_mfma_f32_16x16x32_bf16 v[176:179], a[32:35], v[220:223], v[176:179]\n"
      "v_cvt_pk_bf16_f32 v148, v152, v153\n"
      "v_cvt_pk_bf16_f32 v149, v154, v155\n"
      "v_mfma_f32_16x16x32_bf16 v[180:183], a[36:39], v[220:223], v[180:183]\n"
      "ds_read_b128 v[60:63], v235 offset:0\n"
      "v_cvt_pk_bf16_f32 v150, v156, v157\n"
      "v_cvt_pk_bf16_f32 v151, v158, v159\n"
      "v_mfma_f32_16x16x32_bf16 v[184:187], a[40:43], v[220:223], v[184:187]\n"
      "v_cvt_pk_bf16_f32 v152, v160, v161\n"
      "v_cvt_pk_bf16_f32 v153, v162, v163\n"
      "v_mfma_f32_16x16x32_bf16 v[188:191], a[44:47], v[220:223], v[188:191]\n"
      "ds_read_b128 v[64:67], v232 offset:8192\n"
      "v_cvt_pk_bf16_f32 v154, v164, v165\n"
      "v_cvt_pk_bf16_f32 v155, v166, v167\n"
      "s_waitcnt lgkmcnt(6)\n"
      "v_mfma_f32_16x16x32_bf16 v[192:195], a[24:27], v[224:227], v[192:195]\n"
      "v_cvt_pk_bf16_f32 v168, v168, v169\n"
      "v_cvt_pk_bf16_f32 v169, v170, v171\n"
      "global_load_dwordx4 v[156:159], v239, s[58:59] offset:3072 sc1\n"
      "v_mfma_f32_16x16x32_bf16 v[196:199], a[28:31], v[224:227], v[196:199]\n"
      "ds_read_b128 v[68:71], v233 offset:8192\n"
      "v_cvt_pk_bf16_f32 v170, v172, v173\n"
      "v_cvt_pk_bf16_f32 v171, v174, v175\n"
      "v_mfma_f32_16x16x32_bf16 v[200:203], a[32:35], v[224:227], v[200:203]\n"
      "v_cvt_pk_bf16_f32 v172, v176, v177\n"
      "v_cvt_pk_bf16_f32 v173, v178, v179\n"
      "v_mfma_f32_16x16x32_bf16 v[204:207], a[36:39], v[224:227], v[204:207]\n"
      "ds_read_b128 v[84:87], v234 offset:8192\n"
      "v_cvt_pk_bf16_f32 v174, v180, v181\n"
      "v_cvt_pk_bf16_f32 v175, v182, v183\n"
      "v_mfma_f32_16x16x32_bf16 v[208:211], a[40:43], v[224:227], v[208:211]\n"
      "v_cvt_pk_bf16_f32 v176, v184, v185\n"
      "v_cvt_pk_bf16_f32 v177, v186, v187\n"
      "s_add_u32 s58, s24, 0xf000\n"
      "s_addc_u32 s59, s25, 0\n"
      "global_load_dwordx4 v[160:163], v239, s[58:59] offset:0 sc1\n"
      "v_mfma_f32_16x16x32_bf16 v[212:215], a[44:47], v[224:227], v[212:215]\n"
      "ds_read_b128 v[88:91], v235 offset:8192\n"
      "v_cvt_pk_bf16_f32 v178, v188, v189\n"
      "v_cvt_pk_bf16_f32 v179, v190, v191\n"
      "s_waitcnt lgkmcnt(8)\n"
      "v_mfma_f32_16x16x32_bf16 v[0:3], a[24:27], v[228:231], v[0:3]\n"
      "v_cvt_pk_bf16_f32 v192, v192, v193\n"
      "v_cvt_pk_bf16_f32 v193, v194, v195\n"
      "v_mfma_f32_16x16x32_bf16 v[4:7], a[28:31], v[228:231], v[4:7]\n"
      "ds_read_b128 v[92:95], v232 offset:16384\n"
      "v_cvt_pk_bf16_f32 v194, v196, v197\n"
      "v_cvt_pk_bf16_f32 v195, v198, v199\n"
      "v_mfma_f32_16x16x32_bf16 v[8:11], a[32:35], v[228:231], v[8:11]\n"
      "v_cvt_pk_bf16_f32 v196, v200, v201\n"
      "v_cvt_pk_bf16_f32 v197, v202, v203\n"
      "global_load_dwordx4 v[164:167], v239, s[58:59] offset:1024 sc1\n"
      "v_mfma_f32_16x16x32_bf16 v[12:15], a[36:39], v[228:231], v[12:15]\n"
      "ds_read_b128 v[108:111], v233 offset:16384\n"
      "v_cvt_pk_bf16_f32 v198, v204, v205\n"
      "v_cvt_pk_bf16_f32 v199, v206, v207\n"
      "v_mfma_f32_16x16x32_bf16 v[16:19], a[40:43], v[228:231], v[16:19]\n"
      "ds_read_b128 v[112:115], v234 offset:16384\n"
      "v_cvt_pk_bf16_f32 v200, v208, v209\n"
      "v_cvt_pk_bf16_f32 v201, v210, v211\n"
      "v_mfma_f32_16x16x32_bf16 v[20:23], a[44:47], v[228:231], v[20:23]\n"
      "ds_read_b128 v[116:119], v235 offset:16384\n"
      "v_cvt_pk_bf16_f32 v202, v212, v213\n"
      "v_cvt_pk_bf16_f32 v203, v214, v215\n"
      "s_waitcnt lgkmcnt(8)\n"
      "v_mfma_f32_16x16x32_bf16 v[132:135], a[240:243], v[36:39], v[0:3]\n"
      "global_load_dwordx4 v[180:183], v239, s[58:59] offset:2048 sc1\n"
      "s_mov_b64 vcc, 0\n"
      "v_mfma_f32_16x16x32_bf16 v[132:135], a[244:247], v[40:43], v[132:135]\n"
      "v_mfma_f32_16x16x32_bf16 v[132:135], a[248:251], v[44:47], v[132:135]\n"
      "v_mfma_f32_16x16x32_bf16 v[132:135], a[252:255], v[60:63], v[132:135]\n"
      "global_load_dwordx4 v[184:187], v239, s[58:59] offset:3072 sc1\n"
      "s_waitcnt lgkmcnt(4)\n"
      "v_mfma_f32_16x16x32_bf16 v[132:135], a[48:51], v[64:67], v[132:135]\n"
      "v_mfma_f32_16x16x32_bf16 v[132:135], a[52:55], v[68:71], v[132:135]\n"
      "v_mfma_f32_16x16x32_bf16 v[132:135], a[56:59], v[84:87], v[132:135]\n"
      "s_add_u32 s58, s24, 0x10000\n"
      "s_addc_u32 s59, s25, 0\n"
      "global_load_dwordx4 v[188:191], v239, s[58:59] offset:0 sc1\n"
      "v_mfma_f32_16x16x32_bf16 v[132:135], a[60:63], v[88:91], v[132:135]\n"
      "s_waitcnt lgkmcnt(0)\n"
      "v_mfma_f32_16x16x32_bf16 v[132:135], a[64:67], v[92:95], v[132:135]\n"
      "v_mfma_f32_16x16x32_bf16 v[132:135], a[68:71], v[108:111], v[132:135]\n"
      "global_load_dwordx4 v[204:207], v239, s[58:59] offset:1024 sc1\n"
      "v_mfma_f32_16x16x32_bf16 v[132:135], a[72:75], v[112:115], v[132:135]\n"
      "v_mfma_f32_16x16x32_bf16 v[132:135], a[76:79], v[116:119], v[132:135]\n"
      "v_mfma_f32_16x16x32_bf16 v[136:139], a[80:83], v[36:39], v[4:7]\n"
      "global_load_dwordx4 a[24:27], v239, s[58:59] offset:2048 sc1\n"
      "v_mfma_f32_16x16x32_bf16 v[136:139], a[84:87], v[40:43], v[136:139]\n"
      "v_mfma_f32_16x16x32_bf16 v[136:139], a[88:91], v[44:47], v[136:139]\n"
      "v_mfma_f32_16x16x32_bf16 v[136:139], a[92:95], v[60:63], v[136:139]\n"
      "global_load_dwordx4 a[28:31], v239, s[58:59] offset:3072 sc1\n"
      "v_mfma_f32_16x16x32_bf16 v[136:139], a[96:99], v[64:67], v[136:139]\n"
      "v_cvt_pk_bf16_f32 v208, v132, v133\n"
      "v_mfma_f32_16x16x32_bf16 v[136:139], a[100:103], v[68:71], v[136:139]\n"
      "v_cvt_pk_bf16_f32 v209, v134, v135\n"
      "v_mfma_f32_16x16x32_bf16 v[136:139], a[104:107], v[84:87], v[136:139]\n"
      "s_add_u32 s58, s24, 0x11000\n"
      "s_addc_u32 s59, s25, 0\n"
      "global_load_dwordx4 a[32:35], v239, s[58:59] offset:0 sc1\n"
      "v_mfma_f32_16x16x32_bf16 v[136:139], a[108:111], v[88:91], v[136:139]\n"
      "v_mfma_f32_16x16x32_bf16 v[136:139], a[112:115], v[92:95], v[136:139]\n"
      "v_mfma_f32_16x16x32_bf16 v[136:139], a[116:119], v[108:111], v[136:139]\n"
      "global_load_dwordx4 a[36:39], v239, s[58:59] offset:1024 sc1\n"
      "v_mfma_f32_16x16x32_bf16 v[136:139], a[120:123], v[112:115], v[136:139]\n"
      "ds_write_b64 v244, v[208:209] offset:12288\n"
      "v_mfma_f32_16x16x32_bf16 v[136:139], a[124:127], v[116:119], v[136:139]\n"
      "v_mfma_f32_16x16x32_bf16 v[132:135], a[128:131], v[36:39], v[8:11]\n"
      "global_load_dwordx4 a[40:43], v239, s[58:59] offset:2048 sc1\n"
      "v_mfma_f32_16x16x32_bf16 v[132:135], a[132:135], v[40:43], v[132:135]\n"
      "v_mfma_f32_16x16x32_bf16 v[132:135], a[136:139], v[44:47], v[132:135]\n"
      "v_mfma_f32_16x16x32_bf16 v[132:135], a[140:143], v[60:63], v[132:135]\n"
      "global_load_dwordx4 a[44:47], v239, s[58:59] offset:3072 sc1\n"
      "v_mfma_f32_16x16x32_bf16 v[132:135], a[144:147], v[64:67], v[132:135]\n"
      "v_cvt_pk_bf16_f32 v210, v136, v137\n"
      "v_mfma_f32_16x16x32_bf16 v[132:135], a[148:151], v[68:71], v[132:135]\n"
      "v_cvt_pk_bf16_f32 v211, v138, v139\n"
      "v_mfma_f32_16x16x32_bf16 v[132:135], a[152:155], v[84:87], v[132:135]\n"
      "s_mul_i32 s78, s90, s19\n"
      "v_mfma_f32_16x16x32_bf16 v[132:135], a[156:159], v[88:91], v[132:135]\n"
      "v_add_u32 v236, s78, v236\n"
      "v_mfma_f32_16x16x32_bf16 v[132:135], a[160:163], v[92:95], v[132:135]\n"
      "s_mul_i32 s78, s78, s99\n"
      "v_mfma_f32_16x16x32_bf16 v[132:135], a[164:167], v[108:111], v[132:135]\n"
      "v_add_u32 v237, s78, v237\n"
      "v_mfma_f32_16x16x32_bf16 v[132:135], a[168:171], v[112:115], v[132:135]\n"
      "s_add_u32 s52, s20, s33\n"
      "s_addc_u32 s53, s21, 0\n"
      "s_add_u32 m0, s17, 0xc000\n"
      "v_mfma_f32_16x16x32_bf16 v[132:135], a[172:175], v[116:119], v[132:135]\n"
      "global_load_lds_dwordx4 v236, s[52:53] offset:0 \n"
      "v_mfma_f32_16x16x32_bf16 v[136:139], a[176:179], v[36:39], v[12:15]\n"
      "ds_write_b64 v244, v[210:211] offset:12296\n"
      "v_mfma_f32_16x16x32_bf16 v[136:139], a[180:183], v[40:43], v[136:139]\n"
      "v_mfma_f32_16x16x32_bf16 v[136:139], a[184:187], v[44:47], v[136:139]\n"
      "v_mfma_f32_16x16x32_bf16 v[136:139], a[188:191], v[60:63], v[136:139]\n"
      "v_cvt_pk_bf16_f32 v212, v132, v133\n"
      "s_waitcnt vmcnt(35)\n"
      "v_mfma_f32_16x16x32_bf16 v[136:139], a[192:195], v[64:67], v[136:139]\n"
      "s_add_u32 m0, s17, 0xdf00\n"
      "v_mfma_f32_16x16x32_bf16 v[136:139], a[196:199], v[68:71], v[136:139]\n"
      "global_load_lds_dwordx4 v236, s[52:53] offset:256 \n"
      "v_mfma_f32_16x16x32_bf16 v[136:139], a[200:203], v[84:87], v[136:139]\n"
      "v_cvt_pk_bf16_f32 v213, v134, v135\n"
      "v_mfma_f32_16x16x32_bf16 v[136:139], a[204:207], v[88:91], v[136:139]\n"
      "s_waitcnt vmcnt(26)\n"
      "v_mfma_f32_16x16x32_bf16 v[136:139], a[208:211], v[92:95], v[136:139]\n"
      "v_mfma_f32_16x16x32_bf16 v[136:139], a[212:215], v[108:111], v[136:139]\n"
      "v_mfma_f32_16x16x32_bf16 v[136:139], a[216:219], v[112:115], v[136:139]\n"
      "s_add_u32 m0, s17, 0xfe00\n"
      "v_mfma_f32_16x16x32_bf16 v[136:139], a[220:223], v[116:119], v[136:139]\n"
      "global_load_lds_dwordx4 v236, s[52:53] offset:512 \n"
      "s_waitcnt vmcnt(23)\n"
      "v_mfma_f32_16x16x32_bf16 v[132:135], a[224:227], v[36:39], v[16:19]\n"
      "ds_write_b64 v245, v[212:213] offset:12288\n"
      "v_mfma_f32_16x16x32_bf16 v[132:135], a[228:231], v[40:43], v[132:135]\n"
      "v_mfma_f32_16x16x32_bf16 v[132:135], a[232:235], v[44:47], v[132:135]\n"
      "v_mfma_f32_16x16x32_bf16 v[132:135], a[236:239], v[60:63], v[132:135]\n"
      "v_cvt_pk_bf16_f32 v214, v136, v137\n"
      "s_waitcnt vmcnt(19)\n"
      "v_mfma_f32_16x16x32_bf16 v[132:135], a[0:3], v[64:67], v[132:135]\n"
      "s_add_u32 m0, s17, 0xd000\n"
      "v_mfma_f32_16x16x32_bf16 v[132:135], a[4:7], v[68:71], v[132:135]\n"
      "global_load_lds_dwordx4 v237, s[52:53] offset:0 \n"
      "v_mfma_f32_16x16x32_bf16 v[132:135], a[8:11], v[84:87], v[132:135]\n"
      "v_cvt_pk_bf16_f32 v215, v138, v139\n"
      "v_mfma_f32_16x16x32_bf16 v[132:135], a[12:15], v[88:91], v[132:135]\n"
      "s_waitcnt vmcnt(16)\n"
      "v_mfma_f32_16x16x32_bf16 v[132:135], a[16:19], v[92:95], v[132:135]\n"
      "v_mfma_f32_16x16x32_bf16 v[132:135], a[20:23], v[108:111], v[132:135]\n"
      "v_mfma_f32_16x16x32_bf16 v[132:135], v[140:143], v[112:115], v[132:135]\n"
      "s_add_u32 m0, s17, 0xef00\n"
      "v_mfma_f32_16x16x32_bf16 v[132:135], v[156:159], v[116:119], v[132:135]\n"
      "s_barrier\n"
      "global_load_lds_dwordx4 v237, s[52:53] offset:256 \n"
      "s_waitcnt vmcnt(13)\n"
      "v_mfma_f32_16x16x32_bf16 v[136:139], v[160:163], v[36:39], v[20:23]\n"
      "ds_read_b128 v[36:39], v232 offset:256\n"
      "ds_write_b64 v245, v[214:215] offset:12296\n"
      "v_mfma_f32_16x16x32_bf16 v[136:139], v[164:167], v[40:43], v[136:139]\n"
      "ds_read_b128 v[40:43], v233 offset:256\n"
      "v_mfma_f32_16x16x32_bf16 v[136:139], v[180:183], v[44:47], v[136:139]\n"
      "ds_read_b128 v[44:47], v234 offset:256\n"
      "v_mfma_f32_16x16x32_bf16 v[136:139], v[184:187], v[60:63], v[136:139]\n"
      "ds_read_b128 v[60:63], v235 offset:256\n"
      "v_cvt_pk_bf16_f32 v208, v132, v133\n"
      "s_waitcnt vmcnt(9)\n"
      "v_mfma_f32_16x16x32_bf16 v[136:139], v[188:191], v[64:67], v[136:139]\n"
      "ds_read_b128 v[64:67], v232 offset:8448\n"
      "s_add_u32 m0, s17, 0x10e00\n"
      "v_mfma_f32_16x16x32_bf16 v[136:139], v[204:207], v[68:71], v[136:139]\n"
      "ds_read_b128 v[68:71], v233 offset:8448\n"
      "global_load_lds_dwordx4 v237, s[52:53] offset:512 \n"
      "v_mfma_f32_16x16x32_bf16 v[136:139], a[24:27], v[84:87], v[136:139]\n"
      "ds_read_b128 v[84:87], v234 offset:8448\n"
      "v_cvt_pk_bf16_f32 v209, v134, v135\n"
      "v_mfma_f32_16x16x32_bf16 v[136:139], a[28:31], v[88:91], v[136:139]\n"
      "ds_read_b128 v[88:91], v235 offset:8448\n"
      "s_waitcnt vmcnt(6)\n"
      "v_mfma_f32_16x16x32_bf16 v[136:139], a[32:35], v[92:95], v[136:139]\n"
      "ds_read_b128 v[92:95], v232 offset:16640\n"
      "v_mfma_f32_16x16x32_bf16 v[136:139], a[36:39], v[108:111], v[136:139]\n"
      "ds_read_b128 v[108:111], v233 offset:16640\n"
      "v_mfma_f32_16x16x32_bf16 v[136:139], a[40:43], v[112:115], v[136:139]\n"
      "ds_read_b128 v[112:115], v234 offset:16640\n"
      "v_mfma_f32_16x16x32_bf16 v[136:139], a[44:47], v[116:119], v[136:139]\n"
      "ds_read_b128 v[116:119], v235 offset:16640\n"
      "s_waitcnt lgkmcnt(8)\n"
      "v_mfma_f32_16x16x32_bf16 v[132:135], a[240:243], v[36:39], v[0:3]\n"
      "s_add_u32 s52, s42, s33\n"
      "s_addc_u32 s53, s43, 0\n"
      "s_add_u32 m0, s17, 0x12000\n"
      "v_mfma_f32_16x16x32_bf16 v[132:135], a[244:247], v[40:43], v[132:135]\n"
      "global_load_lds_dwordx4 v236, s[52:53] offset:0 \n"
      "v_mfma_f32_16x16x32_bf16 v[132:135], a[248:251], v[44:47], v[132:135]\n"
      "ds_write_b64 v244, v[208:209] offset:12416\n"
      "v_mfma_f32_16x16x32_bf16 v[132:135], a[252:255], v[60:63], v[132:135]\n"
      "v_cvt_pk_bf16_f32 v210, v136, v137\n"
      "s_waitcnt lgkmcnt(5)\n"
      "v_mfma_f32_16x16x32_bf16 v[132:135], a[48:51], v[64:67], v[132:135]\n"
      "v_cvt_pk_bf16_f32 v211, v138, v139\n"
      "v_mfma_f32_16x16x32_bf16 v[132:135], a[52:55], v[68:71], v[132:135]\n"
      "v_mfma_f32_16x16x32_bf16 v[132:135], a[56:59], v[84:87], v[132:135]\n"
      "s_add_u32 m0, s17, 0x13f00\n"
      "v_mfma_f32_16x16x32_bf16 v[132:135], a[60:63], v[88:91], v[132:135]\n"
      "global_load_lds_dwordx4 v236, s[52:53] offset:256 \n"
      "s_waitcnt lgkmcnt(1)\n"
      "v_mfma_f32_16x16x32_bf16 v[132:135], a[64:67], v[92:95], v[132:135]\n"
      "v_mfma_f32_16x16x32_bf16 v[132:135], a[68:71], v[108:111], v[132:135]\n"
      "ds_write_b64 v244, v[210:211] offset:12424\n"
      "v_mfma_f32_16x16x32_bf16 v[132:135], a[72:75], v[112:115], v[132:135]\n"
      "v_mfma_f32_16x16x32_bf16 v[132:135], a[76:79], v[116:119], v[132:135]\n"
      "v_mfma_f32_16x16x32_bf16 v[136:139], a[80:83], v[36:39], v[4:7]\n"
      "s_add_u32 m0, s17, 0x15e00\n"
      "v_mfma_f32_16x16x32_bf16 v[136:139], a[84:87], v[40:43], v[136:139]\n"
      "global_load_lds_dwordx4 v236, s[52:53] offset:512 \n"
      "v_mfma_f32_16x16x32_bf16 v[136:139], a[88:91], v[44:47], v[136:139]\n"
      "v_mfma_f32_16x16x32_bf16 v[136:139], a[92:95], v[60:63], v[136:139]\n"
      "v_cvt_pk_bf16_f32 v208, v132, v133\n"
      "v_mfma_f32_16x16x32_bf16 v[136:139], a[96:99], v[64:67], v[136:139]\n"
      "v_cvt_pk_bf16_f32 v209, v134, v135\n"
      "v_mfma_f32_16x16x32_bf16 v[136:139], a[100:103], v[68:71], v[136:139]\n"
      "v_mfma_f32_16x16x32_bf16 v[136:139], a[104:107], v[84:87], v[136:139]\n"
      "s_add_u32 m0, s17, 0x13000\n"
      "v_mfma_f32_16x16x32_bf16 v[136:139], a[108:111], v[88:91], v[136:139]\n"
      "global_load_lds_dwordx4 v237, s[52:53] offset:0 \n"
      "v_mfma_f32_16x16x32_bf16 v[136:139], a[112:115], v[92:95], v[136:139]\n"
      "v_mfma_f32_16x16x32_bf16 v[136:139], a[116:119], v[108:111], v[136:139]\n"
      "ds_write_b64 v244, v[208:209] offset:24576\n"
      "v_mfma_f32_16x16x32_bf16 v[136:139], a[120:123], v[112:115], v[136:139]\n"
      "v_mfma_f32_16x16x32_bf16 v[136:139], a[124:127], v[116:119], v[136:139]\n"
      "v_mfma_f32_16x16x32_bf16 v[132:135], a[128:131], v[36:39], v[8:11]\n"
      "s_add_u32 m0, s17, 0x14f00\n"
      "v_mfma_f32_16x16x32_bf16 v[132:135], a[132:135], v[40:43], v[132:135]\n"
      "global_load_lds_dwordx4 v237, s[52:53] offset:256 \n"
      "v_mfma_f32_16x16x32_bf16 v[132:135], a[136:139], v[44:47], v[132:135]\n"
      "v_mfma_f32_16x16x32_bf16 v[132:135], a[140:143], v[60:63], v[132:135]\n"
      "v_cvt_pk_bf16_f32 v210, v136, v137\n"
      "v_mfma_f32_16x16x32_bf16 v[132:135], a[144:147], v[64:67], v[132:135]\n"
      "v_cvt_pk_bf16_f32 v211, v138, v139\n"
      "v_mfma_f32_16x16x32_bf16 v[132:135], a[148:151], v[68:71], v[132:135]\n"
      "v_mfma_f32_16x16x32_bf16 v[132:135], a[152:155], v[84:87], v[132:135]\n"
      "s_add_u32 m0, s17, 0x16e00\n"
      "v_mfma_f32_16x16x32_bf16 v[132:135], a[156:159], v[88:91], v[132:135]\n"
      "global_load_lds_dwordx4 v237, s[52:53] offset:512 \n"
      "v_mfma_f32_16x16x32_bf16 v[132:135], a[160:163], v[92:95], v[132:135]\n"
      "v_mfma_f32_16x16x32_bf16 v[132:135], a[164:167], v[108:111], v[132:135]\n"
      "ds_write_b64 v244, v[210:211] offset:24584\n"
      "v_mfma_f32_16x16x32_bf16 v[132:135], a[168:171], v[112:115], v[132:135]\n"
      "v_mfma_f32_16x16x32_bf16 v[132:135], a[172:175], v[116:119], v[132:135]\n"
      "v_mfma_f32_16x16x32_bf16 v[136:139], a[176:179], v[36:39], v[12:15]\n"
      "v_mfma_f32_16x16x32_bf16 v[136:139], a[180:183], v[40:43], v[136:139]\n"
      "v_mfma_f32_16x16x32_bf16 v[136:139], a[184:187], v[44:47], v[136:139]\n"
      "v_mfma_f32_16x16x32_bf16 v[136:139], a[188:191], v[60:63], v[136:139]\n"
      "v_cvt_pk_bf16_f32 v212, v132, v133\n"
      "v_mfma_f32_16x16x32_bf16 v[136:139], a[192:195], v[64:67], v[136:139]\n"
      "v_cvt_pk_bf16_f32 v213, v134, v135\n"
      "v_mfma_f32_16x16x32_bf16 v[136:139], a[196:199], v[68:71], v[136:139]\n"
      "v_mfma_f32_16x16x32_bf16 v[136:139], a[200:203], v[84:87], v[136:139]\n"
      "v_mfma_f32_16x16x32_bf16 v[136:139], a[204:207], v[88:91], v[136:139]\n"
      "v_mfma_f32_16x16x32_bf16 v[136:139], a[208:211], v[92:95], v[136:139]\n"
      "v_mfma_f32_16x16x32_bf16 v[136:139], a[212:215], v[108:111], v[136:139]\n"
      "ds_write_b64 v245, v[212:213] offset:24576\n"
      "v_mfma_f32_16x16x32_bf16 v[136:139], a[216:219], v[112:115], v[136:139]\n"
      "v_mfma_f32_16x16x32_bf16 v[136:139], a[220:223], v[116:119], v[136:139]\n"
      "v_mfma_f32_16x16x32_bf16 v[132:135], a[224:227], v[36:39], v[16:19]\n"
      "v_mfma_f32_16x16x32_bf16 v[132:135], a[228:231], v[40:43], v[132:135]\n"
      "v_mfma_f32_16x16x32_bf16 v[132:135], a[232:235], v[44:47], v[132:135]\n"
      "v_mfma_f32_16x16x32_bf16 v[132:135], a[236:239], v[60:63], v[132:135]\n"
      "v_cvt_pk_bf16_f32 v214, v136, v137\n"
      "v_mfma_f32_16x16x32_bf16 v[132:135], a[0:3], v[64:67], v[132:135]\n"
      "v_cvt_pk_bf16_f32 v215, v138, v139\n"
      "v_mfma_f32_16x16x32_bf16 v[132:135], a[4:7], v[68:71], v[132:135]\n"
      "v_mfma_f32_16x16x32_bf16 v[132:135], a[8:11], v[84:87], v[132:135]\n"
      "v_mfma_f32_16x16x32_bf16 v[132:135], a[12:15], v[88:91], v[132:135]\n"
      "v_mfma_f32_16x16x32_bf16 v[132:135], a[16:19], v[92:95], v[132:135]\n"
      "v_mfma_f32_16x16x32_bf16 v[132:135], a[20:23], v[108:111], v[132:135]\n"
      "ds_write_b64 v245, v[214:215] offset:24584\n"
      "v_mfma_f32_16x16x32_bf16 v[132:135], v[140:143], v[112:115], v[132:135]\n"
      "s_lshl_b32 s79, s98, 1\n"
      "v_mfma_f32_16x16x32_bf16 v[132:135], v[156:159], v[116:119], v[132:135]\n"
      "s_waitcnt lgkmcnt(4)\n"
      "s_barrier\n"
      "v_mad_i32_i24 v252, v251, s79, v252\n"
      "v_mfma_f32_16x16x32_bf16 v[136:139], v[160:163], v[36:39], v[20:23]\n"
      "ds_read_b128 v[36:39], v232 offset:24576\n"
      "s_mul_i32 s79, s79, s100\n"
      "v_mfma_f32_16x16x32_bf16 v[136:139], v[164:167], v[40:43], v[136:139]\n"
      "ds_read_b128 v[40:43], v233 offset:24576\n"
      "v_add_u32 v247, s79, v247\n"
      "v_mfma_f32_16x16x32_bf16 v[136:139], v[180:183], v[44:47], v[136:139]\n"
      "ds_read_b128 v[44:47], v234 offset:24576\n"
      "s_mul_i32 s79, s79, s101\n"
      "v_mfma_f32_16x16x32_bf16 v[136:139], v[184:187], v[60:63], v[136:139]\n"
      "ds_read_b128 v[60:63], v235 offset:24576\n"
      "v_cvt_pk_bf16_f32 v208, v132, v133\n"
      "v_mfma_f32_16x16x32_bf16 v[136:139], v[188:191], v[64:67], v[136:139]\n"
      "ds_read_b128 v[64:67], v232 offset:32768\n"
      "v_cvt_pk_bf16_f32 v209, v134, v135\n"
      "v_mfma_f32_16x16x32_bf16 v[136:139], v[204:207], v[68:71], v[136:139]\n"
      "ds_read_b128 v[68:71], v233 offset:32768\n"
      "v_add_u32 v253, s79, v253\n"
      "v_mfma_f32_16x16x32_bf16 v[136:139], a[24:27], v[84:87], v[136:139]\n"
      "ds_read_b128 v[84:87], v234 offset:32768\n"
      "v_mfma_f32_16x16x32_bf16 v[136:139], a[28:31], v[88:91], v[136:139]\n"
      "ds_read_b128 v[88:91], v235 offset:32768\n"
      "v_mfma_f32_16x16x32_bf16 v[136:139], a[32:35], v[92:95], v[136:139]\n"
      "ds_read_b128 v[92:95], v232 offset:40960\n"
      "v_mfma_f32_16x16x32_bf16 v[136:139], a[36:39], v[108:111], v[136:139]\n"
      "ds_read_b128 v[108:111], v233 offset:40960\n"
      "v_mfma_f32_16x16x32_bf16 v[136:139], a[40:43], v[112:115], v[136:139]\n"
      "ds_read_b128 v[112:115], v234 offset:40960\n"
      "v_mfma_f32_16x16x32_bf16 v[136:139], a[44:47], v[116:119], v[136:139]\n"
      "ds_read_b128 v[116:119], v235 offset:40960\n"
      "LJOIN:\n"
      "LPD7:\n"
      "s_cbranch_execz LPD10\n"
      "s_waitcnt lgkmcnt(8)\n"
      "v_mfma_f32_16x16x32_bf16 v[132:135], a[240:243], v[36:39], v[0:3]\n"
      "ds_write_b64 v244, v[208:209] offset:24704\n"
      "v_mfma_f32_16x16x32_bf16 v[132:135], a[244:247], v[40:43], v[132:135]\n"
      "s_add_u32 s54, s30, s32\n"
      "s_addc_u32 s55, s31, 0\n"
      "v_mfma_f32_16x16x32_bf16 v[132:135], a[248:251], v[44:47], v[132:135]\n"
      "v_mfma_f32_16x16x32_bf16 v[132:135], a[252:255], v[60:63], v[132:135]\n"
      "v_cvt_pk_bf16_f32 v210, v136, v137\n"
      "s_waitcnt lgkmcnt(5)\n"
      "v_mfma_f32_16x16x32_bf16 v[132:135], a[48:51], v[64:67], v[132:135]\n"
      "v_cvt_pk_bf16_f32 v211, v138, v139\n"
      "v_mfma_f32_16x16x32_bf16 v[132:135], a[52:55], v[68:71], v[132:135]\n"
      "s_mul_i32 s78, s91, s19\n"
      "v_mfma_f32_16x16x32_bf16 v[132:135], a[56:59], v[84:87], v[132:135]\n"
      "ds_read_b128 v[216:219], v246 offset:12288\n"
      "v_add_u32 v236, s78, v236\n"
      "v_mfma_f32_16x16x32_bf16 v[132:135], a[60:63], v[88:91], v[132:135]\n"
      "ds_read_b128 v[220:223], v246 offset:13312\n"
      "s_waitcnt lgkmcnt(3)\n"
      "v_mfma_f32_16x16x32_bf16 v[132:135], a[64:67], v[92:95], v[132:135]\n"
      "ds_read_b128 v[224:227], v246 offset:14336\n"
      "v_mfma_f32_16x16x32_bf16 v[132:135], a[68:71], v[108:111], v[132:135]\n"
      "ds_write_b64 v244, v[210:211] offset:24712\n"
      "v_mfma_f32_16x16x32_bf16 v[132:135], a[72:75], v[112:115], v[132:135]\n"
      "s_mul_i32 s78, s78, s99\n"
      "v_mfma_f32_16x16x32_bf16 v[132:135], a[76:79], v[116:119], v[132:135]\n"
      "v_add_u32 v237, s78, v237\n"
      "v_mfma_f32_16x16x32_bf16 v[136:139], a[80:83], v[36:39], v[4:7]\n"
      "v_mfma_f32_16x16x32_bf16 v[136:139], a[84:87], v[40:43], v[136:139]\n"
      "v_mfma_f32_16x16x32_bf16 v[136:139], a[88:91], v[44:47], v[136:139]\n"
      "v_mfma_f32_16x16x32_bf16 v[136:139], a[92:95], v[60:63], v[136:139]\n"
      "v_cvt_pk_bf16_f32 v208, v132, v133\n"
      "v_mfma_f32_16x16x32_bf16 v[136:139], a[96:99], v[64:67], v[136:139]\n"
      "v_cvt_pk_bf16_f32 v209, v134, v135\n"
      "v_mfma_f32_16x16x32_bf16 v[136:139], a[100:103], v[68:71], v[136:139]\n"
      "s_waitcnt lgkmcnt(3)\n"
      "v_mfma_f32_16x16x32_bf16 v[136:139], a[104:107], v[84:87], v[136:139]\n"
      "global_store_dwordx4 v247, v[216:219], s[54:55] offset:0 sc1\n"
      "v_mfma_f32_16x16x32_bf16 v[136:139], a[108:111], v[88:91], v[136:139]\n"
      "s_add_u32 s52, s20, s34\n"
      "s_addc_u32 s53, s21, 0\n"
      "v_mfma_f32_16x16x32_bf16 v[136:139], a[112:115], v[92:95], v[136:139]\n"
      "s_add_u32 m0, s17, 0x0\n"
      "v_mfma_f32_16x16x32_bf16 v[136:139], a[116:119], v[108:111], v[136:139]\n"
      "ds_write_b64 v244, v[208:209] offset:0\n"
      "v_mfma_f32_16x16x32_bf16 v[136:139], a[120:123], v[112:115], v[136:139]\n"
      "global_load_lds_dwordx4 v236, s[52:53] offset:0 nt\n"
      "v_mfma_f32_16x16x32_bf16 v[136:139], a[124:127], v[116:119], v[136:139]\n"
      "v_mfma_f32_16x16x32_bf16 v[132:135], a[128:131], v[36:39], v[8:11]\n"
      "v_mfma_f32_16x16x32_bf16 v[132:135], a[132:135], v[40:43], v[132:135]\n"
      "v_mfma_f32_16x16x32_bf16 v[132:135], a[136:139], v[44:47], v[132:135]\n"
      "v_mfma_f32_16x16x32_bf16 v[132:135], a[140:143], v[60:63], v[132:135]\n"
      "v_cvt_pk_bf16_f32 v210, v136, v137\n"
      "v_mfma_f32_16x16x32_bf16 v[132:135], a[144:147], v[64:67], v[132:135]\n"
      "v_cvt_pk_bf16_f32 v211, v138, v139\n"
      "v_mfma_f32_16x16x32_bf16 v[132:135], a[148:151], v[68:71], v[132:135]\n"
      "v_mfma_f32_16x16x32_bf16 v[132:135], a[152:155], v[84:87], v[132:135]\n"
      "s_waitcnt lgkmcnt(3)\n"
      "v_mfma_f32_16x16x32_bf16 v[132:135], a[156:159], v[88:91], v[132:135]\n"
      "global_store_dwordx4 v252, v[220:223], s[54:55] offset:0 sc1\n"
      "v_mfma_f32_16x16x32_bf16 v[132:135], a[160:163], v[92:95], v[132:135]\n"
      "s_add_u32 m0, s17, 0x1f00\n"
      "v_mfma_f32_16x16x32_bf16 v[132:135], a[164:167], v[108:111], v[132:135]\n"
      "ds_write_b64 v244, v[210:211] offset:8\n"
      "v_mfma_f32_16x16x32_bf16 v[132:135], a[168:171], v[112:115], v[132:135]\n"
      "global_load_lds_dwordx4 v236, s[52:53] offset:256 nt\n"
      "v_mfma_f32_16x16x32_bf16 v[132:135], a[172:175], v[116:119], v[132:135]\n"
      "v_mfma_f32_16x16x32_bf16 v[136:139], a[176:179], v[36:39], v[12:15]\n"
      "v_mfma_f32_16x16x32_bf16 v[136:139], a[180:183], v[40:43], v[136:139]\n"
      "v_mfma_f32_16x16x32_bf16 v[136:139], a[184:187], v[44:47], v[136:139]\n"
      "v_mfma_f32_16x16x32_bf16 v[136:139], a[188:191], v[60:63], v[136:139]\n"
      "v_cvt_pk_bf16_f32 v212, v132, v133\n"
      "v_mfma_f32_16x16x32_bf16 v[136:139], a[192:195], v[64:67], v[136:139]\n"
      "v_cvt_pk_bf16_f32 v213, v134, v135\n"
      "v_mfma_f32_16x16x32_bf16 v[136:139], a[196:199], v[68:71], v[136:139]\n"
      "v_mfma_f32_16x16x32_bf16 v[136:139], a[200:203], v[84:87], v[136:139]\n"
      "v_mfma_f32_16x16x32_bf16 v[136:139], a[204:207], v[88:91], v[136:139]\n"
      "s_waitcnt lgkmcnt(3)\n"
      "v_mfma_f32_16x16x32_bf16 v[136:139], a[208:211], v[92:95], v[136:139]\n"
      "global_store_dwordx4 v253, v[224:227], s[54:55] offset:0 sc1\n"
      "v_mfma_f32_16x16x32_bf16 v[136:139], a[212:215], v[108:111], v[136:139]\n"
      "ds_write_b64 v245, v[212:213] offset:0\n"
      "v_mfma_f32_16x16x32_bf16 v[136:139], a[216:219], v[112:115], v[136:139]\n"
      "s_add_u32 m0, s17, 0x3e00\n"
      "v_mfma_f32_16x16x32_bf16 v[136:139], a[220:223], v[116:119], v[136:139]\n"
      "global_load_lds_dwordx4 v236, s[52:53] offset:512 nt\n"
      "v_mfma_f32_16x16x32_bf16 v[132:135], a[224:227], v[36:39], v[16:19]\n"
      "v_mfma_f32_16x16x32_bf16 v[132:135], a[228:231], v[40:43], v[132:135]\n"
      "v_mfma_f32_16x16x32_bf16 v[132:135], a[232:235], v[44:47], v[132:135]\n"
      "v_mfma_f32_16x16x32_bf16 v[132:135], a[236:239], v[60:63], v[132:135]\n"
      "v_cvt_pk_bf16_f32 v214, v136, v137\n"
      "v_mfma_f32_16x16x32_bf16 v[132:135], a[0:3], v[64:67], v[132:135]\n"
      "v_cvt_pk_bf16_f32 v215, v138, v139\n"
      "v_mfma_f32_16x16x32_bf16 v[132:135], a[4:7], v[68:71], v[132:135]\n"
      "v_mfma_f32_16x16x32_bf16 v[132:135], a[8:11], v[84:87], v[132:135]\n"
      "v_mfma_f32_16x16x32_bf16 v[132:135], a[12:15], v[88:91], v[132:135]\n"
      "v_mfma_f32_16x16x32_bf16 v[132:135], a[16:19], v[92:95], v[132:135]\n"
      "v_mfma_f32_16x16x32_bf16 v[132:135], a[20:23], v[108:111], v[132:135]\n"
      "ds_write_b64 v245, v[214:215] offset:8\n"
      "v_mfma_f32_16x16x32_bf16 v[132:135], v[140:143], v[112:115], v[132:135]\n"
      "v_mfma_f32_16x16x32_bf16 v[132:135], v[156:159], v[116:119], v[132:135]\n"
      "s_waitcnt lgkmcnt(4)\n"
      "s_barrier\n"
      "v_mfma_f32_16x16x32_bf16 v[136:139], v[160:163], v[36:39], v[20:23]\n"
      "ds_read_b128 v[36:39], v232 offset:24832\n"
      "v_mfma_f32_16x16x32_bf16 v[136:139], v[164:167], v[40:43], v[136:139]\n"
      "ds_read_b128 v[40:43], v233 offset:24832\n"
      "v_mfma_f32_16x16x32_bf16 v[136:139], v[180:183], v[44:47], v[136:139]\n"
      "ds_read_b128 v[44:47], v234 offset:24832\n"
      "v_mfma_f32_16x16x32_bf16 v[136:139], v[184:187], v[60:63], v[136:139]\n"
      "ds_read_b128 v[60:63], v235 offset:24832\n"
      "v_cvt_pk_bf16_f32 v208, v132, v133\n"
      "v_mfma_f32_16x16x32_bf16 v[136:139], v[188:191], v[64:67], v[136:139]\n"
      "ds_read_b128 v[64:67], v232 offset:33024\n"
      "v_cvt_pk_bf16_f32 v209, v134, v135\n"
      "v_mfma_f32_16x16x32_bf16 v[136:139], v[204:207], v[68:71], v[136:139]\n"
      "ds_read_b128 v[68:71], v233 offset:33024\n"
      "v_mfma_f32_16x16x32_bf16 v[136:139], a[24:27], v[84:87], v[136:139]\n"
      "ds_read_b128 v[84:87], v234 offset:33024\n"
      "v_mfma_f32_16x16x32_bf16 v[136:139], a[28:31], v[88:91], v[136:139]\n"
      "ds_read_b128 v[88:91], v235 offset:33024\n"
      "v_mfma_f32_16x16x32_bf16 v[136:139], a[32:35], v[92:95], v[136:139]\n"
      "ds_read_b128 v[92:95], v232 offset:41216\n"
      "v_mfma_f32_16x16x32_bf16 v[136:139], a[36:39], v[108:111], v[136:139]\n"
      "ds_read_b128 v[108:111], v233 offset:41216\n"
      "ds_write_b64 v244, v[208:209] offset:128\n"
      "v_mfma_f32_16x16x32_bf16 v[136:139], a[40:43], v[112:115], v[136:139]\n"
      "ds_read_b128 v[112:115], v234 offset:41216\n"
      "v_mfma_f32_16x16x32_bf16 v[136:139], a[44:47], v[116:119], v[136:139]\n"
      "ds_read_b128 v[116:119], v235 offset:41216\n"
      "s_waitcnt lgkmcnt(9)\n"
      "v_mfma_f32_16x16x32_bf16 v[132:135], a[240:243], v[36:39], v[0:3]\n"
      "v_add_u32 v232, 0xc000, v232\n"
      "v_mfma_f32_16x16x32_bf16 v[132:135], a[244:247], v[40:43], v[132:135]\n"
      "v_add_u32 v233, 0xc000, v233\n"
      "LPD8:\n"
      "s_cbranch_execz LPD11\n"
      "v_mfma_f32_16x16x32_bf16 v[132:135], a[248:251], v[44:47], v[132:135]\n"
      "v_add_u32 v234, 0xc000, v234\n"
      "v_mfma_f32_16x16x32_bf16 v[132:135], a[252:255], v[60:63], v[132:135]\n"
      "v_add_u32 v235, 0xc000, v235\n"
      "s_waitcnt lgkmcnt(5)\n"
      "v_mfma_f32_16x16x32_bf16 v[132:135], a[48:51], v[64:67], v[132:135]\n"
      "v_cvt_pk_bf16_f32 v210, v136, v137\n"
      "v_mfma_f32_16x16x32_bf16 v[132:135], a[52:55], v[68:71], v[132:135]\n"
      "v_cvt_pk_bf16_f32 v211, v138, v139\n"
      "v_mfma_f32_16x16x32_bf16 v[132:135], a[56:59], v[84:87], v[132:135]\n"
      "ds_read_b128 v[216:219], v246 offset:24576\n"
      "v_mfma_f32_16x16x32_bf16 v[132:135], a[60:63], v[88:91], v[132:135]\n"
      "ds_read_b128 v[220:223], v246 offset:25600\n"
      "s_waitcnt lgkmcnt(2)\n"
      "v_mfma_f32_16x16x32_bf16 v[132:135], a[64:67], v[92:95], v[132:135]\n"
      "ds_read_b128 v[224:227], v246 offset:26624\n"
      "v_mfma_f32_16x16x32_bf16 v[132:135], a[68:71], v[108:111], v[132:135]\n"
      "v_lshlrev_b32 v0, 16, v24\n"
      "v_mfma_f32_16x16x32_bf16 v[132:135], a[72:75], v[112:115], v[132:135]\n"
      "ds_write_b64 v244, v[210:211] offset:136\n"
      "v_mfma_f32_16x16x32_bf16 v[132:135], a[76:79], v[116:119], v[132:135]\n"
      "v_and_b32 v1, 0xffff0000, v24\n"
      "v_mfma_f32_16x16x32_bf16 v[136:139], a[80:83], v[36:39], v[4:7]\n"
      "v_lshlrev_b32 v2, 16, v25\n"
      "v_mfma_f32_16x16x32_bf16 v[136:139], a[84:87], v[40:43], v[136:139]\n"
      "v_and_b32 v3, 0xffff0000, v25\n"
      "v_mfma_f32_16x16x32_bf16 v[136:139], a[88:91], v[44:47], v[136:139]\n"
      "v_mfma_f32_16x16x32_bf16 v[136:139], a[92:95], v[60:63], v[136:139]\n"
      "v_cvt_pk_bf16_f32 v208, v132, v133\n"
      "v_mfma_f32_16x16x32_bf16 v[136:139], a[96:99], v[64:67], v[136:139]\n"
      "v_cvt_pk_bf16_f32 v209, v134, v135\n"
      "v_mfma_f32_16x16x32_bf16 v[136:139], a[100:103], v[68:71], v[136:139]\n"
      "s_waitcnt lgkmcnt(3)\n"
      "v_mfma_f32_16x16x32_bf16 v[136:139], a[104:107], v[84:87], v[136:139]\n"
      "global_store_dwordx4 v247, v[216:219], s[54:55] offset:768 sc1\n"
      "v_mfma_f32_16x16x32_bf16 v[136:139], a[108:111], v[88:91], v[136:139]\n"
      "s_add_u32 m0, s17, 0x1000\n"
      "v_mfma_f32_16x16x32_bf16 v[136:139], a[112:115], v[92:95], v[136:139]\n"
      "global_load_lds_dwordx4 v237, s[52:53] offset:0 nt\n"
      "v_mfma_f32_16x16x32_bf16 v[136:139], a[116:119], v[108:111], v[136:139]\n"
      "ds_write_b64 v244, v[208:209] offset:12288\n"
      "v_mfma_f32_16x16x32_bf16 v[136:139], a[120:123], v[112:115], v[136:139]\n"
      "v_lshlrev_b32 v4, 16, v26\n"
      "v_mfma_f32_16x16x32_bf16 v[136:139], a[124:127], v[116:119], v[136:139]\n"
      "v_and_b32 v5, 0xffff0000, v26\n"
      "v_mfma_f32_16x16x32_bf16 v[132:135], a[128:131], v[36:39], v[8:11]\n"
      "v_lshlrev_b32 v6, 16, v27\n"
      "v_mfma_f32_16x16x32_bf16 v[132:135], a[132:135], v[40:43], v[132:135]\n"
      "v_and_b32 v7, 0xffff0000, v27\n"
      "v_mfma_f32_16x16x32_bf16 v[132:135], a[136:139], v[44:47], v[132:135]\n"
      "v_mfma_f32_16x16x32_bf16 v[132:135], a[140:143], v[60:63], v[132:135]\n"
      "v_cvt_pk_bf16_f32 v210, v136, v137\n"
      "v_mfma_f32_16x16x32_bf16 v[132:135], a[144:147], v[64:67], v[132:135]\n"
      "v_cvt_pk_bf16_f32 v211, v138, v139\n"
      "v_mfma_f32_16x16x32_bf16 v[132:135], a[148:151], v[68:71], v[132:135]\n"
      "v_lshlrev_b32 v8, 16, v28\n"
      "v_mfma_f32_16x16x32_bf16 v[132:135], a[152:155], v[84:87], v[132:135]\n"
      "s_waitcnt lgkmcnt(3)\n"
      "v_mfma_f32_16x16x32_bf16 v[132:135], a[156:159], v[88:91], v[132:135]\n"
      "global_store_dwordx4 v252, v[220:223], s[54:55] offset:768 sc1\n"
      "v_mfma_f32_16x16x32_bf16 v[132:135], a[160:163], v[92:95], v[132:135]\n"
      "s_add_u32 m0, s17, 0x2f00\n"
      "v_mfma_f32_16x16x32_bf16 v[132:135], a[164:167], v[108:111], v[132:135]\n"
      "ds_write_b64 v244, v[210:211] offset:12296\n"
      "v_mfma_f32_16x16x32_bf16 v[132:135], a[168:171], v[112:115], v[132:135]\n"
      "global_load_lds_dwordx4 v237, s[52:53] offset:256 nt\n"
      "v_mfma_f32_16x16x32_bf16 v[132:135], a[172:175], v[116:119], v[132:135]\n"
      "v_and_b32 v9, 0xffff0000, v28\n"
      "v_mfma_f32_16x16x32_bf16 v[136:139], a[176:179], v[36:39], v[12:15]\n"
      "v_lshlrev_b32 v10, 16, v29\n"
      "v_mfma_f32_16x16x32_bf16 v[136:139], a[180:183], v[40:43], v[136:139]\n"
      "v_and_b32 v11, 0xffff0000, v29\n"
      "v_mfma_f32_16x16x32_bf16 v[136:139], a[184:187], v[44:47], v[136:139]\n"
      "v_mfma_f32_16x16x32_bf16 v[136:139], a[188:191], v[60:63], v[136:139]\n"
      "v_cvt_pk_bf16_f32 v212, v132, v133\n"
      "v_mfma_f32_16x16x32_bf16 v[136:139], a[192:195], v[64:67], v[136:139]\n"
      "v_cvt_pk_bf16_f32 v213, v134, v135\n"
      "v_mfma_f32_16x16x32_bf16 v[136:139], a[196:199], v[68:71], v[136:139]\n"
      "v_lshlrev_b32 v12, 16, v30\n"
      "v_mfma_f32_16x16x32_bf16 v[136:139], a[200:203], v[84:87], v[136:139]\n"
      "v_and_b32 v13, 0xffff0000, v30\n"
      "v_mfma_f32_16x16x32_bf16 v[136:139], a[204:207], v[88:91], v[136:139]\n"
      "s_waitcnt lgkmcnt(3)\n"
      "v_mfma_f32_16x16x32_bf16 v[136:139], a[208:211], v[92:95], v[136:139]\n"
      "global_store_dwordx4 v253, v[224:227], s[54:55] offset:768 sc1\n"
      "v_mfma_f32_16x16x32_bf16 v[136:139], a[212:215], v[108:111], v[136:139]\n"
      "ds_write_b64 v245, v[212:213] offset:12288\n"
      "v_mfma_f32_16x16x32_bf16 v[136:139], a[216:219], v[112:115], v[136:139]\n"
      "s_add_u32 m0, s17, 0x4e00\n"
      "v_mfma_f32_16x16x32_bf16 v[136:139], a[220:223], v[116:119], v[136:139]\n"
      "global_load_lds_dwordx4 v237, s[52:53] offset:512 nt\n"
      "v_mfma_f32_16x16x32_bf16 v[132:135], a[224:227], v[36:39], v[16:19]\n"
      "v_lshlrev_b32 v14, 16, v31\n"
      "v_mfma_f32_16x16x32_bf16 v[132:135], a[228:231], v[40:43], v[132:135]\n"
      "v_and_b32 v15, 0xffff0000, v31\n"
      "v_mfma_f32_16x16x32_bf16 v[132:135], a[232:235], v[44:47], v[132:135]\n"
      "v_mfma_f32_16x16x32_bf16 v[132:135], a[236:239], v[60:63], v[132:135]\n"
      "s_memrealtime s[84:85]\n"
      "v_mfma_f32_16x16x32_bf16 v[132:135], a[0:3], v[64:67], v[132:135]\n"
      "v_cvt_pk_bf16_f32 v214, v136, v137\n"
      "v_mfma_f32_16x16x32_bf16 v[132:135], a[4:7], v[68:71], v[132:135]\n"
      "v_cvt_pk_bf16_f32 v215, v138, v139\n"
      "v_mfma_f32_16x16x32_bf16 v[132:135], a[8:11], v[84:87], v[132:135]\n"
      "v_lshlrev_b32 v16, 16, v32\n"
      "v_mfma_f32_16x16x32_bf16 v[132:135], a[12:15], v[88:91], v[132:135]\n"
      "v_and_b32 v17, 0xffff0000, v32\n"
      "v_mfma_f32_16x16x32_bf16 v[132:135], a[16:19], v[92:95], v[132:135]\n"
      "v_lshlrev_b32 v18, 16, v33\n"
      "v_mfma_f32_16x16x32_bf16 v[132:135], a[20:23], v[108:111], v[132:135]\n"
      "v_and_b32 v19, 0xffff0000, v33\n"
      "v_mfma_f32_16x16x32_bf16 v[132:135], v[140:143], v[112:115], v[132:135]\n"
      "s_waitcnt lgkmcnt(0)\n"
      "s_mov_b32 s48, s84\n"
      "v_mfma_f32_16x16x32_bf16 v[132:135], v[156:159], v[116:119], v[132:135]\n"
      "s_waitcnt vmcnt(18)\n"
      "s_barrier\n"
      "ds_write_b64 v245, v[214:215] offset:12296\n"
      "v_mfma_f32_16x16x32_bf16 v[136:139], v[160:163], v[36:39], v[20:23]\n"
      "ds_read_b128 v[36:39], v232 offset:0\n"
      "v_mfma_f32_16x16x32_bf16 v[136:139], v[164:167], v[40:43], v[136:139]\n"
      "ds_read_b128 v[40:43], v233 offset:0\n"
      "v_mfma_f32_16x16x32_bf16 v[136:139], v[180:183], v[44:47], v[136:139]\n"
      "ds_read_b128 v[44:47], v234 offset:0\n"
      "v_mfma_f32_16x16x32_bf16 v[136:139], v[184:187], v[60:63], v[136:139]\n"
      "ds_read_b128 v[60:63], v235 offset:0\n"
      "v_cvt_pk_bf16_f32 v208, v132, v133\n"
      "v_mfma_f32_16x16x32_bf16 v[136:139], v[188:191], v[64:67], v[136:139]\n"
      "ds_read_b128 v[64:67], v232 offset:8192\n"
      "v_cvt_pk_bf16_f32 v209, v134, v135\n"
      "v_mfma_f32_16x16x32_bf16 v[136:139], v[204:207], v[68:71], v[136:139]\n"
      "ds_read_b128 v[68:71], v233 offset:8192\n"
      "v_mfma_f32_16x16x32_bf16 v[136:139], a[24:27], v[84:87], v[136:139]\n"
      "ds_read_b128 v[84:87], v234 offset:8192\n"
      "v_mfma_f32_16x16x32_bf16 v[136:139], a[28:31], v[88:91], v[136:139]\n"
      "ds_read_b128 v[88:91], v235 offset:8192\n"
      "v_mfma_f32_16x16x32_bf16 v[136:139], a[32:35], v[92:95], v[136:139]\n"
      "ds_read_b128 v[92:95], v232 offset:16384\n"
      "v_mfma_f32_16x16x32_bf16 v[136:139], a[36:39], v[108:111], v[136:139]\n"
      "ds_read_b128 v[108:111], v233 offset:16384\n"
      "v_mfma_f32_16x16x32_bf16 v[136:139], a[40:43], v[112:115], v[136:139]\n"
      "ds_read_b128 v[112:115], v234 offset:16384\n"
      "v_mfma_f32_16x16x32_bf16 v[136:139], a[44:47], v[116:119], v[136:139]\n"
      "ds_read_b128 v[116:119], v235 offset:16384\n"
      "s_waitcnt lgkmcnt(8)\n"
      "v_mfma_f32_16x16x32_bf16 v[132:135], a[240:243], v[36:39], v[0:3]\n"
      "ds_write_b64 v244, v[208:209] offset:12416\n"
      "v_mfma_f32_16x16x32_bf16 v[132:135], a[244:247], v[40:43], v[132:135]\n"
      "v_lshlrev_b32 v20, 16, v34\n"
      "v_mfma_f32_16x16x32_bf16 v[132:135], a[248:251], v[44:47], v[132:135]\n"
      "v_and_b32 v21, 0xffff0000, v34\n"
      "v_mfma_f32_16x16x32_bf16 v[132:135], a[252:255], v[60:63], v[132:135]\n"
      "v_lshlrev_b32 v22, 16, v35\n"
      "s_waitcnt lgkmcnt(5)\n"
      "v_mfma_f32_16x16x32_bf16 v[132:135], a[48:51], v[64:67], v[132:135]\n"
      "v_and_b32 v23, 0xffff0000, v35\n"
      "v_mfma_f32_16x16x32_bf16 v[132:135], a[52:55], v[68:71], v[132:135]\n"
      "v_cvt_pk_bf16_f32 v210, v136, v137\n"
      "v_mfma_f32_16x16x32_bf16 v[132:135], a[56:59], v[84:87], v[132:135]\n"
      "v_cvt_pk_bf16_f32 v211, v138, v139\n"
      "v_mfma_f32_16x16x32_bf16 v[132:135], a[60:63], v[88:91], v[132:135]\n"
      "s_add_u32 s54, s44, s32\n"
      "s_addc_u32 s55, s45, 0\n"
      "s_waitcnt lgkmcnt(1)\n"
      "v_mfma_f32_16x16x32_bf16 v[132:135], a[64:67], v[92:95], v[132:135]\n"
      "ds_read_b128 v[216:219], v246 offset:0\n"
      "v_mfma_f32_16x16x32_bf16 v[132:135], a[68:71], v[108:111], v[132:135]\n"
      "ds_read_b128 v[220:223], v246 offset:1024\n"
      "v_mfma_f32_16x16x32_bf16 v[132:135], a[72:75], v[112:115], v[132:135]\n"
      "ds_read_b128 v[224:227], v246 offset:2048\n"
      "v_mfma_f32_16x16x32_bf16 v[132:135], a[76:79], v[116:119], v[132:135]\n"
      "ds_write_b64 v244, v[210:211] offset:12424\n"
      "v_mfma_f32_16x16x32_bf16 v[136:139], a[80:83], v[36:39], v[4:7]\n"
      "v_mfma_f32_16x16x32_bf16 v[136:139], a[84:87], v[40:43], v[136:139]\n"
      "v_mfma_f32_16x16x32_bf16 v[136:139], a[88:91], v[44:47], v[136:139]\n"
      "v_mfma_f32_16x16x32_bf16 v[136:139], a[92:95], v[60:63], v[136:139]\n"
      "v_cvt_pk_bf16_f32 v208, v132, v133\n"
      "v_mfma_f32_16x16x32_bf16 v[136:139], a[96:99], v[64:67], v[136:139]\n"
      "v_cvt_pk_bf16_f32 v209, v134, v135\n"
      "v_mfma_f32_16x16x32_bf16 v[136:139], a[100:103], v[68:71], v[136:139]\n"
      "s_waitcnt lgkmcnt(3)\n"
      "v_mfma_f32_16x16x32_bf16 v[136:139], a[104:107], v[84:87], v[136:139]\n"
      "global_store_dwordx4 v247, v[216:219], s[54:55] offset:0 sc1\n"
      "v_mfma_f32_16x16x32_bf16 v[136:139], a[108:111], v[88:91], v[136:139]\n"
      "s_add_u32 s52, s42, s34\n"
      "s_addc_u32 s53, s43, 0\n"
      "v_mfma_f32_16x16x32_bf16 v[136:139], a[112:115], v[92:95], v[136:139]\n"
      "s_add_u32 m0, s17, 0x6000\n"
      "v_mfma_f32_16x16x32_bf16 v[136:139], a[116:119], v[108:111], v[136:139]\n"
      "ds_write_b64 v244, v[208:209] offset:24576\n"
      "v_mfma_f32_16x16x32_bf16 v[136:139], a[120:123], v[112:115], v[136:139]\n"
      "global_load_lds_dwordx4 v236, s[52:53] offset:0 nt\n"
      "v_mfma_f32_16x16x32_bf16 v[136:139], a[124:127], v[116:119], v[136:139]\n"
      "v_mfma_f32_16x16x32_bf16 v[132:135], a[128:131], v[36:39], v[8:11]\n"
      "v_mfma_f32_16x16x32_bf16 v[132:135], a[132:135], v[40:43], v[132:135]\n"
      "v_mfma_f32_16x16x32_bf16 v[132:135], a[136:139], v[44:47], v[132:135]\n"
      "v_mfma_f32_16x16x32_bf16 v[132:135], a[140:143], v[60:63], v[132:135]\n"
      "v_cvt_pk_bf16_f32 v210, v136, v137\n"
      "v_mfma_f32_16x16x32_bf16 v[132:135], a[144:147], v[64:67], v[132:135]\n"
      "v_cvt_pk_bf16_f32 v211, v138, v139\n"
      "v_mfma_f32_16x16x32_bf16 v[132:135], a[148:151], v[68:71], v[132:135]\n"
      "v_mfma_f32_16x16x32_bf16 v[132:135], a[152:155], v[84:87], v[132:135]\n"
      "s_waitcnt lgkmcnt(3)\n"
      "v_mfma_f32_16x16x32_bf16 v[132:135], a[156:159], v[88:91], v[132:135]\n"
      "global_store_dwordx4 v252, v[220:223], s[54:55] offset:0 sc1\n"
      "v_mfma_f32_16x16x32_bf16 v[132:135], a[160:163], v[92:95], v[132:135]\n"
      "s_add_u32 m0, s17, 0x7f00\n"
      "v_mfma_f32_16x16x32_bf16 v[132:135], a[164:167], v[108:111], v[132:135]\n"
      "ds_write_b64 v244, v[210:211] offset:24584\n"
      "v_mfma_f32_16x16x32_bf16 v[132:135], a[168:171], v[112:115], v[132:135]\n"
      "global_load_lds_dwordx4 v236, s[52:53] offset:256 nt\n"
      "v_mfma_f32_16x16x32_bf16 v[132:135], a[172:175], v[116:119], v[132:135]\n"
      "v_mfma_f32_16x16x32_bf16 v[136:139], a[176:179], v[36:39], v[12:15]\n"
      "v_mfma_f32_16x16x32_bf16 v[136:139], a[180:183], v[40:43], v[136:139]\n"
      "v_mfma_f32_16x16x32_bf16 v[136:139], a[184:187], v[44:47], v[136:139]\n"
      "v_mfma_f32_16x16x32_bf16 v[136:139], a[188:191], v[60:63], v[136:139]\n"
      "v_cvt_pk_bf16_f32 v212, v132, v133\n"
      "v_mfma_f32_16x16x32_bf16 v[136:139], a[192:195], v[64:67], v[136:139]\n"
      "v_cvt_pk_bf16_f32 v213, v134, v135\n"
      "v_mfma_f32_16x16x32_bf16 v[136:139], a[196:199], v[68:71], v[136:139]\n"
      "v_mfma_f32_16x16x32_bf16 v[136:139], a[200:203], v[84:87], v[136:139]\n"
      "v_mfma_f32_16x16x32_bf16 v[136:139], a[204:207], v[88:91], v[136:139]\n"
      "s_waitcnt lgkmcnt(3)\n"
      "v_mfma_f32_16x16x32_bf16 v[136:139], a[208:211], v[92:95], v[136:139]\n"
      "global_store_dwordx4 v253, v[224:227], s[54:55] offset:0 sc1\n"
      "v_mfma_f32_16x16x32_bf16 v[136:139], a[212:215], v[108:111], v[136:139]\n"
      "ds_write_b64 v245, v[212:213] offset:24576\n"
      "v_mfma_f32_16x16x32_bf16 v[136:139], a[216:219], v[112:115], v[136:139]\n"
      "s_add_u32 m0, s17, 0x9e00\n"
      "v_mfma_f32_16x16x32_bf16 v[136:139], a[220:223], v[116:119], v[136:139]\n"
      "global_load_lds_dwordx4 v236, s[52:53] offset:512 nt\n"
      "v_mfma_f32_16x16x32_bf16 v[132:135], a[224:227], v[36:39], v[16:19]\n"
      "v_mfma_f32_16x16x32_bf16 v[132:135], a[228:231], v[40:43], v[132:135]\n"
      "v_mfma_f32_16x16x32_bf16 v[132:135], a[232:235], v[44:47], v[132:135]\n"
      "v_mfma_f32_16x16x32_bf16 v[132:135], a[236:239], v[60:63], v[132:135]\n"
      "v_cvt_pk_bf16_f32 v214, v136, v137\n"
      "v_mfma_f32_16x16x32_bf16 v[132:135], a[0:3], v[64:67], v[132:135]\n"
      "v_cvt_pk_bf16_f32 v215, v138, v139\n"
      "v_mfma_f32_16x16x32_bf16 v[132:135], a[4:7], v[68:71], v[132:135]\n"
      "v_mfma_f32_16x16x32_bf16 v[132:135], a[8:11], v[84:87], v[132:135]\n"
      "v_mfma_f32_16x16x32_bf16 v[132:135], a[12:15], v[88:91], v[132:135]\n"
      "v_mfma_f32_16x16x32_bf16 v[132:135], a[16:19], v[92:95], v[132:135]\n"
      "v_mfma_f32_16x16x32_bf16 v[132:135], a[20:23], v[108:111], v[132:135]\n"
      "ds_write_b64 v245, v[214:215] offset:24584\n"
      "v_mfma_f32_16x16x32_bf16 v[132:135], v[140:143], v[112:115], v[132:135]\n"
      "v_mfma_f32_16x16x32_bf16 v[132:135], v[156:159], v[116:119], v[132:135]\n"
      "s_waitcnt lgkmcnt(4)\n"
      "s_barrier\n"
      "v_mfma_f32_16x16x32_bf16 v[136:139], v[160:163], v[36:39], v[20:23]\n"
      "ds_read_b128 v[36:39], v232 offset:256\n"
      "v_mfma_f32_16x16x32_bf16 v[136:139], v[164:167], v[40:43], v[136:139]\n"
      "ds_read_b128 v[40:43], v233 offset:256\n"
      "v_mfma_f32_16x16x32_bf16 v[136:139], v[180:183], v[44:47], v[136:139]\n"
      "ds_read_b128 v[44:47], v234 offset:256\n"
      "v_mfma_f32_16x16x32_bf16 v[136:139], v[184:187], v[60:63], v[136:139]\n"
      "ds_read_b128 v[60:63], v235 offset:256\n"
      "v_cvt_pk_bf16_f32 v208, v132, v133\n"
      "v_mfma_f32_16x16x32_bf16 v[136:139], v[188:191], v[64:67], v[136:139]\n"
      "ds_read_b128 v[64:67], v232 offset:8448\n"
      "v_cvt_pk_bf16_f32 v209, v134, v135\n"
      "v_mfma_f32_16x16x32_bf16 v[136:139], v[204:207], v[68:71], v[136:139]\n"
      "ds_read_b128 v[68:71], v233 offset:8448\n"
      "v_mfma_f32_16x16x32_bf16 v[136:139], a[24:27], v[84:87], v[136:139]\n"
      "ds_read_b128 v[84:87], v234 offset:8448\n"
      "v_mfma_f32_16x16x32_bf16 v[136:139], a[28:31], v[88:91], v[136:139]\n"
      "ds_read_b128 v[88:91], v235 offset:8448\n"
      "v_mfma_f32_16x16x32_bf16 v[136:139], a[32:35], v[92:95], v[136:139]\n"
      "ds_read_b128 v[92:95], v232 offset:16640\n"
      "v_mfma_f32_16x16x32_bf16 v[136:139], a[36:39], v[108:111], v[136:139]\n"
      "ds_read_b128 v[108:111], v233 offset:16640\n"
      "v_mfma_f32_16x16x32_bf16 v[136:139], a[40:43], v[112:115], v[136:139]\n"
      "ds_read_b128 v[112:115], v234 offset:16640\n"
      "v_mfma_f32_16x16x32_bf16 v[136:139], a[44:47], v[116:119], v[136:139]\n"
      "ds_read_b128 v[116:119], v235 offset:16640\n"
      "s_waitcnt lgkmcnt(8)\n"
      "v_mfma_f32_16x16x32_bf16 v[132:135], a[240:243], v[36:39], v[0:3]\n"
      "ds_write_b64 v244, v[208:209] offset:24704\n"
      "v_mfma_f32_16x16x32_bf16 v[132:135], a[244:247], v[40:43], v[132:135]\n"
      "v_mfma_f32_16x16x32_bf16 v[132:135], a[248:251], v[44:47], v[132:135]\n"
      "v_mfma_f32_16x16x32_bf16 v[132:135], a[252:255], v[60:63], v[132:135]\n"
      "v_cvt_pk_bf16_f32 v210, v136, v137\n"
      "s_waitcnt lgkmcnt(5)\n"
      "v_mfma_f32_16x16x32_bf16 v[132:135], a[48:51], v[64:67], v[132:135]\n"
      "v_cvt_pk_bf16_f32 v211, v138, v139\n"
      "v_mfma_f32_16x16x32_bf16 v[132:135], a[52:55], v[68:71], v[132:135]\n"
      "v_mfma_f32_16x16x32_bf16 v[132:135], a[56:59], v[84:87], v[132:135]\n"
      "ds_read_b128 v[216:219], v246 offset:12288\n"
      "v_mfma_f32_16x16x32_bf16 v[132:135], a[60:63], v[88:91], v[132:135]\n"
      "ds_read_b128 v[220:223], v246 offset:13312\n"
      "s_waitcnt lgkmcnt(3)\n"
      "v_mfma_f32_16x16x32_bf16 v[132:135], a[64:67], v[92:95], v[132:135]\n"
      "ds_read_b128 v[224:227], v246 offset:14336\n"
      "v_mfma_f32_16x16x32_bf16 v[132:135], a[68:71], v[108:111], v[132:135]\n"
      "ds_write_b64 v244, v[210:211] offset:24712\n"
      "v_mfma_f32_16x16x32_bf16 v[132:135], a[72:75], v[112:115], v[132:135]\n"
      "v_mfma_f32_16x16x32_bf16 v[132:135], a[76:79], v[116:119], v[132:135]\n"
      "v_mfma_f32_16x16x32_bf16 v[136:139], a[80:83], v[36:39], v[4:7]\n"
      "v_mfma_f32_16x16x32_bf16 v[136:139], a[84:87], v[40:43], v[136:139]\n"
      "v_mfma_f32_16x16x32_bf16 v[136:139], a[88:91], v[44:47], v[136:139]\n"
      "v_mfma_f32_16x16x32_bf16 v[136:139], a[92:95], v[60:63], v[136:139]\n"
      "v_cvt_pk_bf16_f32 v208, v132, v133\n"
      "v_mfma_f32_16x16x32_bf16 v[136:139], a[96:99], v[64:67], v[136:139]\n"
      "v_cvt_pk_bf16_f32 v209, v134, v135\n"
      "v_mfma_f32_16x16x32_bf16 v[136:139], a[100:103], v[68:71], v[136:139]\n"
      "s_waitcnt lgkmcnt(3)\n"
      "v_mfma_f32_16x16x32_bf16 v[136:139], a[104:107], v[84:87], v[136:139]\n"
      "global_store_dwordx4 v247, v[216:219], s[54:55] offset:768 sc1\n"
      "v_mfma_f32_16x16x32_bf16 v[136:139], a[108:111], v[88:91], v[136:139]\n"
      "s_add_u32 m0, s17, 0x7000\n"
      "v_mfma_f32_16x16x32_bf16 v[136:139], a[112:115], v[92:95], v[136:139]\n"
      "global_load_lds_dwordx4 v237, s[52:53] offset:0 nt\n"
      "v_mfma_f32_16x16x32_bf16 v[136:139], a[116:119], v[108:111], v[136:139]\n"
      "ds_write_b64 v244, v[208:209] offset:0\n"
      "v_mfma_f32_16x16x32_bf16 v[136:139], a[120:123], v[112:115], v[136:139]\n"
      "v_mfma_f32_16x16x32_bf16 v[136:139], a[124:127], v[116:119], v[136:139]\n"
      "v_mfma_f32_16x16x32_bf16 v[132:135], a[128:131], v[36:39], v[8:11]\n"
      "v_mfma_f32_16x16x32_bf16 v[132:135], a[132:135], v[40:43], v[132:135]\n"
      "v_mfma_f32_16x16x32_bf16 v[132:135], a[136:139], v[44:47], v[132:135]\n"
      "v_mfma_f32_16x16x32_bf16 v[132:135], a[140:143], v[60:63], v[132:135]\n"
      "v_cvt_pk_bf16_f32 v210, v136, v137\n"
      "v_mfma_f32_16x16x32_bf16 v[132:135], a[144:147], v[64:67], v[132:135]\n"
      "v_cvt_pk_bf16_f32 v211, v138, v139\n"
      "v_mfma_f32_16x16x32_bf16 v[132:135], a[148:151], v[68:71], v[132:135]\n"
      "v_mfma_f32_16x16x32_bf16 v[132:135], a[152:155], v[84:87], v[132:135]\n"
      "s_waitcnt lgkmcnt(3)\n"
      "v_mfma_f32_16x16x32_bf16 v[132:135], a[156:159], v[88:91], v[132:135]\n"
      "global_store_dwordx4 v252, v[220:223], s[54:55] offset:768 sc1\n"
      "v_mfma_f32_16x16x32_bf16 v[132:135], a[160:163], v[92:95], v[132:135]\n"
      "s_add_u32 m0, s17, 0x8f00\n"
      "v_mfma_f32_16x16x32_bf16 v[132:135], a[164:167], v[108:111], v[132:135]\n"
      "ds_write_b64 v244, v[210:211] offset:8\n"
      "v_mfma_f32_16x16x32_bf16 v[132:135], a[168:171], v[112:115], v[132:135]\n"
      "global_load_lds_dwordx4 v237, s[52:53] offset:256 nt\n"
      "v_mfma_f32_16x16x32_bf16 v[132:135], a[172:175], v[116:119], v[132:135]\n"
      "v_mfma_f32_16x16x32_bf16 v[136:139], a[176:179], v[36:39], v[12:15]\n"
      "v_mfma_f32_16x16x32_bf16 v[136:139], a[180:183], v[40:43], v[136:139]\n"
      "v_mfma_f32_16x16x32_bf16 v[136:139], a[184:187], v[44:47], v[136:139]\n"
      "v_mfma_f32_16x16x32_bf16 v[136:139], a[188:191], v[60:63], v[136:139]\n"
      "v_cvt_pk_bf16_f32 v212, v132, v133\n"
      "v_mfma_f32_16x16x32_bf16 v[136:139], a[192:195], v[64:67], v[136:139]\n"
      "v_cvt_pk_bf16_f32 v213, v134, v135\n"
      "v_mfma_f32_16x16x32_bf16 v[136:139], a[196:199], v[68:71], v[136:139]\n"
      "v_mfma_f32_16x16x32_bf16 v[136:139], a[200:203], v[84:87], v[136:139]\n"
      "v_mfma_f32_16x16x32_bf16 v[136:139], a[204:207], v[88:91], v[136:139]\n"
      "s_waitcnt lgkmcnt(3)\n"
      "v_mfma_f32_16x16x32_bf16 v[136:139], a[208:211], v[92:95], v[136:139]\n"
      "global_store_dwordx4 v253, v[224:227], s[54:55] offset:768 sc1\n"
      "v_mfma_f32_16x16x32_bf16 v[136:139], a[212:215], v[108:111], v[136:139]\n"
      "ds_write_b64 v245, v[212:213] offset:0\n"
      "v_mfma_f32_16x16x32_bf16 v[136:139], a[216:219], v[112:115], v[136:139]\n"
      "s_add_u32 m0, s17, 0xae00\n"
      "v_mfma_f32_16x16x32_bf16 v[136:139], a[220:223], v[116:119], v[136:139]\n"
      "global_load_lds_dwordx4 v237, s[52:53] offset:512 nt\n"
      "v_mfma_f32_16x16x32_bf16 v[132:135], a[224:227], v[36:39], v[16:19]\n"
      "v_mfma_f32_16x16x32_bf16 v[132:135], a[228:231], v[40:43], v[132:135]\n"
      "v_mfma_f32_16x16x32_bf16 v[132:135], a[232:235], v[44:47], v[132:135]\n"
      "v_mfma_f32_16x16x32_bf16 v[132:135], a[236:239], v[60:63], v[132:135]\n"
      "v_cvt_pk_bf16_f32 v214, v136, v137\n"
      "v_mfma_f32_16x16x32_bf16 v[132:135], a[0:3], v[64:67], v[132:135]\n"
      "v_cvt_pk_bf16_f32 v215, v138, v139\n"
      "v_mfma_f32_16x16x32_bf16 v[132:135], a[4:7], v[68:71], v[132:135]\n"
      "v_mfma_f32_16x16x32_bf16 v[132:135], a[8:11], v[84:87], v[132:135]\n"
      "v_mfma_f32_16x16x32_bf16 v[132:135], a[12:15], v[88:91], v[132:135]\n"
      "v_mfma_f32_16x16x32_bf16 v[132:135], a[16:19], v[92:95], v[132:135]\n"
      "v_mfma_f32_16x16x32_bf16 v[132:135], a[20:23], v[108:111], v[132:135]\n"
      "ds_write_b64 v245, v[214:215] offset:8\n"
      "v_mfma_f32_16x16x32_bf16 v[132:135], v[140:143], v[112:115], v[132:135]\n"
      "s_lshl_b32 s79, s90, 1\n"
      "v_mfma_f32_16x16x32_bf16 v[132:135], v[156:159], v[116:119], v[132:135]\n"
      "s_waitcnt vmcnt(24) lgkmcnt(4)\n"
      "s_barrier\n"
      "v_mad_i32_i24 v252, v251, s79, v252\n"
      "v_mfma_f32_16x16x32_bf16 v[136:139], v[160:163], v[36:39], v[20:23]\n"
      "ds_read_b128 v[36:39], v232 offset:24576\n"
      "s_mul_i32 s79, s79, s100\n"
      "v_mfma_f32_16x16x32_bf16 v[136:139], v[164:167], v[40:43], v[136:139]\n"
      "ds_read_b128 v[40:43], v233 offset:24576\n"
      "v_add_u32 v247, s79, v247\n"
      "v_mfma_f32_16x16x32_bf16 v[136:139], v[180:183], v[44:47], v[136:139]\n"
      "ds_read_b128 v[44:47], v234 offset:24576\n"
      "s_mul_i32 s79, s79, s101\n"
      "v_mfma_f32_16x16x32_bf16 v[136:139], v[184:187], v[60:63], v[136:139]\n"
      "ds_read_b128 v[60:63], v235 offset:24576\n"
      "v_cvt_pk_bf16_f32 v208, v132, v133\n"
      "v_mfma_f32_16x16x32_bf16 v[136:139], v[188:191], v[64:67], v[136:139]\n"
      "ds_read_b128 v[64:67], v232 offset:32768\n"
      "v_cvt_pk_bf16_f32 v209, v134, v135\n"
      "v_mfma_f32_16x16x32_bf16 v[136:139], v[204:207], v[68:71], v[136:139]\n"
      "ds_read_b128 v[68:71], v233 offset:32768\n"
      "v_add_u32 v253, s79, v253\n"
      "v_mfma_f32_16x16x32_bf16 v[136:139], a[24:27], v[84:87], v[136:139]\n"
      "ds_read_b128 v[84:87], v234 offset:32768\n"
      "v_mfma_f32_16x16x32_bf16 v[136:139], a[28:31], v[88:91], v[136:139]\n"
      "ds_read_b128 v[88:91], v235 offset:32768\n"
      "v_mfma_f32_16x16x32_bf16 v[136:139], a[32:35], v[92:95], v[136:139]\n"
      "ds_read_b128 v[92:95], v232 offset:40960\n"
      "v_mfma_f32_16x16x32_bf16 v[136:139], a[36:39], v[108:111], v[136:139]\n"
      "ds_read_b128 v[108:111], v233 offset:40960\n"
      "v_mfma_f32_16x16x32_bf16 v[136:139], a[40:43], v[112:115], v[136:139]\n"
      "ds_read_b128 v[112:115], v234 offset:40960\n"
      "v_mfma_f32_16x16x32_bf16 v[136:139], a[44:47], v[116:119], v[136:139]\n"
      "ds_read_b128 v[116:119], v235 offset:40960\n"
      "s_waitcnt lgkmcnt(8)\n"
      "v_mfma_f32_16x16x32_bf16 v[132:135], a[240:243], v[36:39], v[0:3]\n"
      "ds_write_b64 v244, v[208:209] offset:128\n"
      "v_mfma_f32_16x16x32_bf16 v[132:135], a[244:247], v[40:43], v[132:135]\n"
      "s_add_u32 s54, s30, s33\n"
      "s_addc_u32 s55, s31, 0\n"
      "v_mfma_f32_16x16x32_bf16 v[132:135], a[248:251], v[44:47], v[132:135]\n"
      "v_mfma_f32_16x16x32_bf16 v[132:135], a[252:255], v[60:63], v[132:135]\n"
      "v_cvt_pk_bf16_f32 v210, v136, v137\n"
      "s_waitcnt lgkmcnt(5)\n"
      "v_mfma_f32_16x16x32_bf16 v[132:135], a[48:51], v[64:67], v[132:135]\n"
      "v_cvt_pk_bf16_f32 v211, v138, v139\n"
      "v_mfma_f32_16x16x32_bf16 v[132:135], a[52:55], v[68:71], v[132:135]\n"
      "s_mul_i32 s78, s92, s19\n"
      "v_mfma_f32_16x16x32_bf16 v[132:135], a[56:59], v[84:87], v[132:135]\n"
      "ds_read_b128 v[216:219], v246 offset:24576\n"
      "v_add_u32 v236, s78, v236\n"
      "v_mfma_f32_16x16x32_bf16 v[132:135], a[60:63], v[88:91], v[132:135]\n"
      "ds_read_b128 v[220:223], v246 offset:25600\n"
      "s_waitcnt lgkmcnt(3)\n"
      "v_mfma_f32_16x16x32_bf16 v[132:135], a[64:67], v[92:95], v[132:135]\n"
      "ds_read_b128 v[224:227], v246 offset:26624\n"
      "v_mfma_f32_16x16x32_bf16 v[132:135], a[68:71], v[108:111], v[132:135]\n"
      "ds_write_b64 v244, v[210:211] offset:136\n"
      "v_mfma_f32_16x16x32_bf16 v[132:135], a[72:75], v[112:115], v[132:135]\n"
      "s_mul_i32 s78, s78, s99\n"
      "v_mfma_f32_16x16x32_bf16 v[132:135], a[76:79], v[116:119], v[132:135]\n"
      "v_add_u32 v237, s78, v237\n"
      "v_mfma_f32_16x16x32_bf16 v[136:139], a[80:83], v[36:39], v[4:7]\n"
      "v_mfma_f32_16x16x32_bf16 v[136:139], a[84:87], v[40:43], v[136:139]\n"
      "v_mfma_f32_16x16x32_bf16 v[136:139], a[88:91], v[44:47], v[136:139]\n"
      "v_mfma_f32_16x16x32_bf16 v[136:139], a[92:95], v[60:63], v[136:139]\n"
      "v_cvt_pk_bf16_f32 v208, v132, v133\n"
      "v_mfma_f32_16x16x32_bf16 v[136:139], a[96:99], v[64:67], v[136:139]\n"
      "v_cvt_pk_bf16_f32 v209, v134, v135\n"
      "v_mfma_f32_16x16x32_bf16 v[136:139], a[100:103], v[68:71], v[136:139]\n"
      "s_waitcnt lgkmcnt(3)\n"
      "v_mfma_f32_16x16x32_bf16 v[136:139], a[104:107], v[84:87], v[136:139]\n"
      "global_store_dwordx4 v247, v[216:219], s[54:55] offset:0 sc1\n"
      "v_mfma_f32_16x16x32_bf16 v[136:139], a[108:111], v[88:91], v[136:139]\n"
      "s_add_u32 s52, s20, s35\n"
      "s_addc_u32 s53, s21, 0\n"
      "v_mfma_f32_16x16x32_bf16 v[136:139], a[112:115], v[92:95], v[136:139]\n"
      "s_add_u32 m0, s17, 0xc000\n"
      "v_mfma_f32_16x16x32_bf16 v[136:139], a[116:119], v[108:111], v[136:139]\n"
      "ds_write_b64 v244, v[208:209] offset:12288\n"
      "v_mfma_f32_16x16x32_bf16 v[136:139], a[120:123], v[112:115], v[136:139]\n"
      "global_load_lds_dwordx4 v236, s[52:53] offset:0 nt\n"
      "v_mfma_f32_16x16x32_bf16 v[136:139], a[124:127], v[116:119], v[136:139]\n"
      "v_mfma_f32_16x16x32_bf16 v[132:135], a[128:131], v[36:39], v[8:11]\n"
      "v_min_u32 v28, 0x370, v239\n"
      "v_mfma_f32_16x16x32_bf16 v[132:135], a[132:135], v[40:43], v[132:135]\n"
      "v_mfma_f32_16x16x32_bf16 v[132:135], a[136:139], v[44:47], v[132:135]\n"
      "v_mfma_f32_16x16x32_bf16 v[132:135], a[140:143], v[60:63], v[132:135]\n"
      "v_cvt_pk_bf16_f32 v210, v136, v137\n"
      "v_mfma_f32_16x16x32_bf16 v[132:135], a[144:147], v[64:67], v[132:135]\n"
      "v_cvt_pk_bf16_f32 v211, v138, v139\n"
      "v_mfma_f32_16x16x32_bf16 v[132:135], a[148:151], v[68:71], v[132:135]\n"
      "v_add_u32 v28, 0x2480, v28\n"
      "v_mfma_f32_16x16x32_bf16 v[132:135], a[152:155], v[84:87], v[132:135]\n"
      "s_waitcnt lgkmcnt(3)\n"
      "global_load_dwordx4 v[24:27], v28, s[28:29] sc1\n"
      "v_mfma_f32_16x16x32_bf16 v[132:135], a[156:159], v[88:91], v[132:135]\n"
      "global_store_dwordx4 v252, v[220:223], s[54:55] offset:0 sc1\n"
      "v_mfma_f32_16x16x32_bf16 v[132:135], a[160:163], v[92:95], v[132:135]\n"
      "s_add_u32 m0, s17, 0xdf00\n"
      "v_mfma_f32_16x16x32_bf16 v[132:135], a[164:167], v[108:111], v[132:135]\n"
      "ds_write_b64 v244, v[210:211] offset:12296\n"
      "v_mfma_f32_16x16x32_bf16 v[132:135], a[168:171], v[112:115], v[132:135]\n"
      "global_load_lds_dwordx4 v236, s[52:53] offset:256 nt\n"
      "v_mfma_f32_16x16x32_bf16 v[132:135], a[172:175], v[116:119], v[132:135]\n"
      "v_mfma_f32_16x16x32_bf16 v[136:139], a[176:179], v[36:39], v[12:15]\n"
      "v_mfma_f32_16x16x32_bf16 v[136:139], a[180:183], v[40:43], v[136:139]\n"
      "v_mfma_f32_16x16x32_bf16 v[136:139], a[184:187], v[44:47], v[136:139]\n"
      "v_mfma_f32_16x16x32_bf16 v[136:139], a[188:191], v[60:63], v[136:139]\n"
      "v_cvt_pk_bf16_f32 v212, v132, v133\n"
      "v_mfma_f32_16x16x32_bf16 v[136:139], a[192:195], v[64:67], v[136:139]\n"
      "v_cvt_pk_bf16_f32 v213, v134, v135\n"
      "v_mfma_f32_16x16x32_bf16 v[136:139], a[196:199], v[68:71], v[136:139]\n"
      "v_mfma_f32_16x16x32_bf16 v[136:139], a[200:203], v[84:87], v[136:139]\n"
      "v_mfma_f32_16x16x32_bf16 v[136:139], a[204:207], v[88:91], v[136:139]\n"
      "s_waitcnt lgkmcnt(3)\n"
      "v_mfma_f32_16x16x32_bf16 v[136:139], a[208:211], v[92:95], v[136:139]\n"
      "global_store_dwordx4 v253, v[224:227], s[54:55] offset:0 sc1\n"
      "v_mfma_f32_16x16x32_bf16 v[136:139], a[212:215], v[108:111], v[136:139]\n"
      "ds_write_b64 v245, v[212:213] offset:12288\n"
      "v_mfma_f32_16x16x32_bf16 v[136:139], a[216:219], v[112:115], v[136:139]\n"
      "s_add_u32 m0, s17, 0xfe00\n"
      "v_mfma_f32_16x16x32_bf16 v[136:139], a[220:223], v[116:119], v[136:139]\n"
      "global_load_lds_dwordx4 v236, s[52:53] offset:512 nt\n"
      "v_mfma_f32_16x16x32_bf16 v[132:135], a[224:227], v[36:39], v[16:19]\n"
      "v_mfma_f32_16x16x32_bf16 v[132:135], a[228:231], v[40:43], v[132:135]\n"
      "v_mfma_f32_16x16x32_bf16 v[132:135], a[232:235], v[44:47], v[132:135]\n"
      "v_mfma_f32_16x16x32_bf16 v[132:135], a[236:239], v[60:63], v[132:135]\n"
      "v_cvt_pk_bf16_f32 v214, v136, v137\n"
      "v_mfma_f32_16x16x32_bf16 v[132:135], a[0:3], v[64:67], v[132:135]\n"
      "v_cvt_pk_bf16_f32 v215, v138, v139\n"
      "v_mfma_f32_16x16x32_bf16 v[132:135], a[4:7], v[68:71], v[132:135]\n"
      "v_mfma_f32_16x16x32_bf16 v[132:135], a[8:11], v[84:87], v[132:135]\n"
      "v_mfma_f32_16x16x32_bf16 v[132:135], a[12:15], v[88:91], v[132:135]\n"
      "v_mfma_f32_16x16x32_bf16 v[132:135], a[16:19], v[92:95], v[132:135]\n"
      "v_mfma_f32_16x16x32_bf16 v[132:135], a[20:23], v[108:111], v[132:135]\n"
      "ds_write_b64 v245, v[214:215] offset:12296\n"
      "v_mfma_f32_16x16x32_bf16 v[132:135], v[140:143], v[112:115], v[132:135]\n"
      "v_mfma_f32_16x16x32_bf16 v[132:135], v[156:159], v[116:119], v[132:135]\n"
      "s_waitcnt lgkmcnt(4)\n"
      "s_barrier\n"
      "v_mfma_f32_16x16x32_bf16 v[136:139], v[160:163], v[36:39], v[20:23]\n"
      "ds_read_b128 v[36:39], v232 offset:24832\n"
      "v_mfma_f32_16x16x32_bf16 v[136:139], v[164:167], v[40:43], v[136:139]\n"
      "ds_read_b128 v[40:43], v233 offset:24832\n"
      "v_mfma_f32_16x16x32_bf16 v[136:139], v[180:183], v[44:47], v[136:139]\n"
      "ds_read_b128 v[44:47], v234 offset:24832\n"
      "v_mfma_f32_16x16x32_bf16 v[136:139], v[184:187], v[60:63], v[136:139]\n"
      "ds_read_b128 v[60:63], v235 offset:24832\n"
      "v_cvt_pk_bf16_f32 v208, v132, v133\n"
      "v_mfma_f32_16x16x32_bf16 v[136:139], v[188:191], v[64:67], v[136:139]\n"
      "ds_read_b128 v[64:67], v232 offset:33024\n"
      "v_cvt_pk_bf16_f32 v209, v134, v135\n"
      "v_mfma_f32_16x16x32_bf16 v[136:139], v[204:207], v[68:71], v[136:139]\n"
      "ds_read_b128 v[68:71], v233 offset:33024\n"
      "v_mfma_f32_16x16x32_bf16 v[136:139], a[24:27], v[84:87], v[136:139]\n"
      "ds_read_b128 v[84:87], v234 offset:33024\n"
      "v_mfma_f32_16x16x32_bf16 v[136:139], a[28:31], v[88:91], v[136:139]\n"
      "ds_read_b128 v[88:91], v235 offset:33024\n"
      "v_mfma_f32_16x16x32_bf16 v[136:139], a[32:35], v[92:95], v[136:139]\n"
      "ds_read_b128 v[92:95], v232 offset:41216\n"
      "v_mfma_f32_16x16x32_bf16 v[136:139], a[36:39], v[108:111], v[136:139]\n"
      "ds_read_b128 v[108:111], v233 offset:41216\n"
      "ds_write_b64 v244, v[208:209] offset:12416\n"
      "v_mfma_f32_16x16x32_bf16 v[136:139], a[40:43], v[112:115], v[136:139]\n"
      "ds_read_b128 v[112:115], v234 offset:41216\n"
      "v_mfma_f32_16x16x32_bf16 v[136:139], a[44:47], v[116:119], v[136:139]\n"
      "ds_read_b128 v[116:119], v235 offset:41216\n"
      "s_waitcnt lgkmcnt(9)\n"
      "v_mfma_f32_16x16x32_bf16 v[132:135], a[240:243], v[36:39], v[0:3]\n"
      "v_add_u32 v232, 0xffff4000, v232\n"
      "v_mfma_f32_16x16x32_bf16 v[132:135], a[244:247], v[40:43], v[132:135]\n"
      "v_add_u32 v233, 0xffff4000, v233\n"
      "v_mfma_f32_16x16x32_bf16 v[132:135], a[248:251], v[44:47], v[132:135]\n"
      "v_add_u32 v234, 0xffff4000, v234\n"
      "v_mfma_f32_16x16x32_bf16 v[132:135], a[252:255], v[60:63], v[132:135]\n"
      "LPD9:\n"
      "s_cbranch_execz LPD12\n"
      "v_add_u32 v235, 0xffff4000, v235\n"
      "s_waitcnt lgkmcnt(5)\n"
      "v_mfma_f32_16x16x32_bf16 v[132:135], a[48:51], v[64:67], v[132:135]\n"
      "v_cvt_pk_bf16_f32 v210, v136, v137\n"
      "v_mfma_f32_16x16x32_bf16 v[132:135], a[52:55], v[68:71], v[132:135]\n"
      "v_cvt_pk_bf16_f32 v211, v138, v139\n"
      "v_mfma_f32_16x16x32_bf16 v[132:135], a[56:59], v[84:87], v[132:135]\n"
      "ds_read_b128 v[216:219], v246 offset:0\n"
      "v_mfma_f32_16x16x32_bf16 v[132:135], a[60:63], v[88:91], v[132:135]\n"
      "ds_read_b128 v[220:223], v246 offset:1024\n"
      "s_waitcnt lgkmcnt(2)\n"
      "v_mfma_f32_16x16x32_bf16 v[132:135], a[64:67], v[92:95], v[132:135]\n"
      "ds_read_b128 v[224:227], v246 offset:2048\n"
      "v_mfma_f32_16x16x32_bf16 v[132:135], a[68:71], v[108:111], v[132:135]\n"
      "v_lshlrev_b32 v0, 16, v48\n"
      "v_mfma_f32_16x16x32_bf16 v[132:135], a[72:75], v[112:115], v[132:135]\n"
      "ds_write_b64 v244, v[210:211] offset:12424\n"
      "v_mfma_f32_16x16x32_bf16 v[132:135], a[76:79], v[116:119], v[132:135]\n"
      "v_and_b32 v1, 0xffff0000, v48\n"
      "v_mfma_f32_16x16x32_bf16 v[136:139], a[80:83], v[36:39], v[4:7]\n"
      "v_lshlrev_b32 v2, 16, v49\n"
      "v_mfma_f32_16x16x32_bf16 v[136:139], a[84:87], v[40:43], v[136:139]\n"
      "v_and_b32 v3, 0xffff0000, v49\n"
      "v_mfma_f32_16x16x32_bf16 v[136:139], a[88:91], v[44:47], v[136:139]\n"
      "v_mfma_f32_16x16x32_bf16 v[136:139], a[92:95], v[60:63], v[136:139]\n"
      "v_cvt_pk_bf16_f32 v208, v132, v133\n"
      "v_mfma_f32_16x16x32_bf16 v[136:139], a[96:99], v[64:67], v[136:139]\n"
      "v_cvt_pk_bf16_f32 v209, v134, v135\n"
      "v_mfma_f32_16x16x32_bf16 v[136:139], a[100:103], v[68:71], v[136:139]\n"
      "s_waitcnt lgkmcnt(3)\n"
      "v_mfma_f32_16x16x32_bf16 v[136:139], a[104:107], v[84:87], v[136:139]\n"
      "global_store_dwordx4 v247, v[216:219], s[54:55] offset:768 sc1\n"
      "v_mfma_f32_16x16x32_bf16 v[136:139], a[108:111], v[88:91], v[136:139]\n"
      "s_add_u32 m0, s17, 0xd000\n"
      "v_mfma_f32_16x16x32_bf16 v[136:139], a[112:115], v[92:95], v[136:139]\n"
      "global_load_lds_dwordx4 v237, s[52:53] offset:0 nt\n"
      "v_mfma_f32_16x16x32_bf16 v[136:139], a[116:119], v[108:111], v[136:139]\n"
      "ds_write_b64 v244, v[208:209] offset:24576\n"
      "v_mfma_f32_16x16x32_bf16 v[136:139], a[120:123], v[112:115], v[136:139]\n"
      "v_lshlrev_b32 v4, 16, v50\n"
      "v_mfma_f32_16x16x32_bf16 v[136:139], a[124:127], v[116:119], v[136:139]\n"
      "v_and_b32 v5, 0xffff0000, v50\n"
      "v_mfma_f32_16x16x32_bf16 v[132:135], a[128:131], v[36:39], v[8:11]\n"
      "v_lshlrev_b32 v6, 16, v51\n"
      "v_mfma_f32_16x16x32_bf16 v[132:135], a[132:135], v[40:43], v[132:135]\n"
      "v_and_b32 v7, 0xffff0000, v51\n"
      "v_mfma_f32_16x16x32_bf16 v[132:135], a[136:139], v[44:47], v[132:135]\n"
      "v_mfma_f32_16x16x32_bf16 v[132:135], a[140:143], v[60:63], v[132:135]\n"
      "v_cvt_pk_bf16_f32 v210, v136, v137\n"
      "v_mfma_f32_16x16x32_bf16 v[132:135], a[144:147], v[64:67], v[132:135]\n"
      "v_cvt_pk_bf16_f32 v211, v138, v139\n"
      "v_mfma_f32_16x16x32_bf16 v[132:135], a[148:151], v[68:71], v[132:135]\n"
      "v_lshlrev_b32 v8, 16, v52\n"
      "v_mfma_f32_16x16x32_bf16 v[132:135], a[152:155], v[84:87], v[132:135]\n"
      "s_waitcnt lgkmcnt(3)\n"
      "v_mfma_f32_16x16x32_bf16 v[132:135], a[156:159], v[88:91], v[132:135]\n"
      "global_store_dwordx4 v252, v[220:223], s[54:55] offset:768 sc1\n"
      "v_mfma_f32_16x16x32_bf16 v[132:135], a[160:163], v[92:95], v[132:135]\n"
      "s_add_u32 m0, s17, 0xef00\n"
      "v_mfma_f32_16x16x32_bf16 v[132:135], a[164:167], v[108:111], v[132:135]\n"
      "ds_write_b64 v244, v[210:211] offset:24584\n"
      "v_mfma_f32_16x16x32_bf16 v[132:135], a[168:171], v[112:115], v[132:135]\n"
      "global_load_lds_dwordx4 v237, s[52:53] offset:256 nt\n"
      "v_mfma_f32_16x16x32_bf16 v[132:135], a[172:175], v[116:119], v[132:135]\n"
      "v_and_b32 v9, 0xffff0000, v52\n"
      "v_mfma_f32_16x16x32_bf16 v[136:139], a[176:179], v[36:39], v[12:15]\n"
      "v_lshlrev_b32 v10, 16, v53\n"
      "v_mfma_f32_16x16x32_bf16 v[136:139], a[180:183], v[40:43], v[136:139]\n"
      "v_and_b32 v11, 0xffff0000, v53\n"
      "v_mfma_f32_16x16x32_bf16 v[136:139], a[184:187], v[44:47], v[136:139]\n"
      "v_mfma_f32_16x16x32_bf16 v[136:139], a[188:191], v[60:63], v[136:139]\n"
      "v_cvt_pk_bf16_f32 v212, v132, v133\n"
      "v_mfma_f32_16x16x32_bf16 v[136:139], a[192:195], v[64:67], v[136:139]\n"
      "v_cvt_pk_bf16_f32 v213, v134, v135\n"
      "v_mfma_f32_16x16x32_bf16 v[136:139], a[196:199], v[68:71], v[136:139]\n"
      "v_lshlrev_b32 v12, 16, v54\n"
      "v_mfma_f32_16x16x32_bf16 v[136:139], a[200:203], v[84:87], v[136:139]\n"
      "v_and_b32 v13, 0xffff0000, v54\n"
      "v_mfma_f32_16x16x32_bf16 v[136:139], a[204:207], v[88:91], v[136:139]\n"
      "s_waitcnt lgkmcnt(3)\n"
      "v_mfma_f32_16x16x32_bf16 v[136:139], a[208:211], v[92:95], v[136:139]\n"
      "global_store_dwordx4 v253, v[224:227], s[54:55] offset:768 sc1\n"
      "v_mfma_f32_16x16x32_bf16 v[136:139], a[212:215], v[108:111], v[136:139]\n"
      "ds_write_b64 v245, v[212:213] offset:24576\n"
      "v_mfma_f32_16x16x32_bf16 v[136:139], a[216:219], v[112:115], v[136:139]\n"
      "s_add_u32 m0, s17, 0x10e00\n"
      "v_mfma_f32_16x16x32_bf16 v[136:139], a[220:223], v[116:119], v[136:139]\n"
      "global_load_lds_dwordx4 v237, s[52:53] offset:512 nt\n"
      "v_mfma_f32_16x16x32_bf16 v[132:135], a[224:227], v[36:39], v[16:19]\n"
      "v_lshlrev_b32 v14, 16, v55\n"
      "v_mfma_f32_16x16x32_bf16 v[132:135], a[228:231], v[40:43], v[132:135]\n"
      "v_and_b32 v15, 0xffff0000, v55\n"
      "v_mfma_f32_16x16x32_bf16 v[132:135], a[232:235], v[44:47], v[132:135]\n"
      "v_mfma_f32_16x16x32_bf16 v[132:135], a[236:239], v[60:63], v[132:135]\n"
      "v_cvt_pk_bf16_f32 v214, v136, v137\n"
      "v_mfma_f32_16x16x32_bf16 v[132:135], a[0:3], v[64:67], v[132:135]\n"
      "v_cvt_pk_bf16_f32 v215, v138, v139\n"
      "v_mfma_f32_16x16x32_bf16 v[132:135], a[4:7], v[68:71], v[132:135]\n"
      "v_lshlrev_b32 v16, 16, v56\n"
      "v_mfma_f32_16x16x32_bf16 v[132:135], a[8:11], v[84:87], v[132:135]\n"
      "v_and_b32 v17, 0xffff0000, v56\n"
      "v_mfma_f32_16x16x32_bf16 v[132:135], a[12:15], v[88:91], v[132:135]\n"
      "v_lshlrev_b32 v18, 16, v57\n"
      "v_mfma_f32_16x16x32_bf16 v[132:135], a[16:19], v[92:95], v[132:135]\n"
      "v_and_b32 v19, 0xffff0000, v57\n"
      "v_mfma_f32_16x16x32_bf16 v[132:135], a[20:23], v[108:111], v[132:135]\n"
      "ds_write_b64 v245, v[214:215] offset:24584\n"
      "v_mfma_f32_16x16x32_bf16 v[132:135], v[140:143], v[112:115], v[132:135]\n"
      "v_mfma_f32_16x16x32_bf16 v[132:135], v[156:159], v[116:119], v[132:135]\n"
      "s_waitcnt vmcnt(25) lgkmcnt(4)\n"
      "s_barrier\n"
      "v_mfma_f32_16x16x32_bf16 v[136:139], v[160:163], v[36:39], v[20:23]\n"
      "ds_read_b128 v[36:39], v232 offset:0\n"
      "v_mfma_f32_16x16x32_bf16 v[136:139], v[164:167], v[40:43], v[136:139]\n"
      "ds_read_b128 v[40:43], v233 offset:0\n"
      "v_mfma_f32_16x16x32_bf16 v[136:139], v[180:183], v[44:47], v[136:139]\n"
      "ds_read_b128 v[44:47], v234 offset:0\n"
      "v_mfma_f32_16x16x32_bf16 v[136:139], v[184:187], v[60:63], v[136:139]\n"
      "ds_read_b128 v[60:63], v235 offset:0\n"
      "v_cvt_pk_bf16_f32 v208, v132, v133\n"
      "v_mfma_f32_16x16x32_bf16 v[136:139], v[188:191], v[64:67], v[136:139]\n"
      "ds_read_b128 v[64:67], v232 offset:8192\n"
      "v_cvt_pk_bf16_f32 v209, v134, v135\n"
      "v_mfma_f32_16x16x32_bf16 v[136:139], v[204:207], v[68:71], v[136:139]\n"
      "ds_read_b128 v[68:71], v233 offset:8192\n"
      "v_mfma_f32_16x16x32_bf16 v[136:139], a[24:27], v[84:87], v[136:139]\n"
      "ds_read_b128 v[84:87], v234 offset:8192\n"
      "v_mfma_f32_16x16x32_bf16 v[136:139], a[28:31], v[88:91], v[136:139]\n"
      "ds_read_b128 v[88:91], v235 offset:8192\n"
      "v_mfma_f32_16x16x32_bf16 v[136:139], a[32:35], v[92:95], v[136:139]\n"
      "ds_read_b128 v[92:95], v232 offset:16384\n"
      "v_mfma_f32_16x16x32_bf16 v[136:139], a[36:39], v[108:111], v[136:139]\n"
      "ds_read_b128 v[108:111], v233 offset:16384\n"
      "v_mfma_f32_16x16x32_bf16 v[136:139], a[40:43], v[112:115], v[136:139]\n"
      "ds_read_b128 v[112:115], v234 offset:16384\n"
      "v_mfma_f32_16x16x32_bf16 v[136:139], a[44:47], v[116:119], v[136:139]\n"
      "ds_read_b128 v[116:119], v235 offset:16384\n"
      "s_waitcnt lgkmcnt(8)\n"
      "v_mfma_f32_16x16x32_bf16 v[132:135], a[240:243], v[36:39], v[0:3]\n"
      "ds_write_b64 v244, v[208:209] offset:24704\n"
      "v_mfma_f32_16x16x32_bf16 v[132:135], a[244:247], v[40:43], v[132:135]\n"
      "v_lshlrev_b32 v20, 16, v58\n"
      "v_mfma_f32_16x16x32_bf16 v[132:135], a[248:251], v[44:47], v[132:135]\n"
      "v_and_b32 v21, 0xffff0000, v58\n"
      "v_mfma_f32_16x16x32_bf16 v[132:135], a[252:255], v[60:63], v[132:135]\n"
      "v_lshlrev_b32 v22, 16, v59\n"
      "s_waitcnt lgkmcnt(5)\n"
      "v_mfma_f32_16x16x32_bf16 v[132:135], a[48:51], v[64:67], v[132:135]\n"
      "v_and_b32 v23, 0xffff0000, v59\n"
      "v_mfma_f32_16x16x32_bf16 v[132:135], a[52:55], v[68:71], v[132:135]\n"
      "v_cvt_pk_bf16_f32 v210, v136, v137\n"
      "v_mfma_f32_16x16x32_bf16 v[132:135], a[56:59], v[84:87], v[132:135]\n"
      "v_cvt_pk_bf16_f32 v211, v138, v139\n"
      "v_mfma_f32_16x16x32_bf16 v[132:135], a[60:63], v[88:91], v[132:135]\n"
      "s_add_u32 s54, s44, s33\n"
      "s_addc_u32 s55, s45, 0\n"
      "s_waitcnt lgkmcnt(1)\n"
      "v_mfma_f32_16x16x32_bf16 v[132:135], a[64:67], v[92:95], v[132:135]\n"
      "ds_read_b128 v[216:219], v246 offset:12288\n"
      "v_mfma_f32_16x16x32_bf16 v[132:135], a[68:71], v[108:111], v[132:135]\n"
      "ds_read_b128 v[220:223], v246 offset:13312\n"
      "v_mfma_f32_16x16x32_bf16 v[132:135], a[72:75], v[112:115], v[132:135]\n"
      "ds_read_b128 v[224:227], v246 offset:14336\n"
      "v_mfma_f32_16x16x32_bf16 v[132:135], a[76:79], v[116:119], v[132:135]\n"
      "ds_write_b64 v244, v[210:211] offset:24712\n"
      "v_mfma_f32_16x16x32_bf16 v[136:139], a[80:83], v[36:39], v[4:7]\n"
      "v_mfma_f32_16x16x32_bf16 v[136:139], a[84:87], v[40:43], v[136:139]\n"
      "v_mfma_f32_16x16x32_bf16 v[136:139], a[88:91], v[44:47], v[136:139]\n"
      "v_mfma_f32_16x16x32_bf16 v[136:139], a[92:95], v[60:63], v[136:139]\n"
      "v_cvt_pk_bf16_f32 v208, v132, v133\n"
      "v_mfma_f32_16x16x32_bf16 v[136:139], a[96:99], v[64:67], v[136:139]\n"
      "v_cvt_pk_bf16_f32 v209, v134, v135\n"
      "v_mfma_f32_16x16x32_bf16 v[136:139], a[100:103], v[68:71], v[136:139]\n"
      "s_waitcnt lgkmcnt(3)\n"
      "v_mfma_f32_16x16x32_bf16 v[136:139], a[104:107], v[84:87], v[136:139]\n"
      "global_store_dwordx4 v247, v[216:219], s[54:55] offset:0 sc1\n"
      "v_mfma_f32_16x16x32_bf16 v[136:139], a[108:111], v[88:91], v[136:139]\n"
      "s_add_u32 s52, s42, s35\n"
      "s_addc_u32 s53, s43, 0\n"
      "v_mfma_f32_16x16x32_bf16 v[136:139], a[112:115], v[92:95], v[136:139]\n"
      "s_add_u32 m0, s17, 0x12000\n"
      "v_mfma_f32_16x16x32_bf16 v[136:139], a[116:119], v[108:111], v[136:139]\n"
      "ds_write_b64 v244, v[208:209] offset:0\n"
      "v_mfma_f32_16x16x32_bf16 v[136:139], a[120:123], v[112:115], v[136:139]\n"
      "global_load_lds_dwordx4 v236, s[52:53] offset:0 nt\n"
      "v_mfma_f32_16x16x32_bf16 v[136:139], a[124:127], v[116:119], v[136:139]\n"
      "v_mfma_f32_16x16x32_bf16 v[132:135], a[128:131], v[36:39], v[8:11]\n"
      "v_mfma_f32_16x16x32_bf16 v[132:135], a[132:135], v[40:43], v[132:135]\n"
      "v_mfma_f32_16x16x32_bf16 v[132:135], a[136:139], v[44:47], v[132:135]\n"
      "v_mfma_f32_16x16x32_bf16 v[132:135], a[140:143], v[60:63], v[132:135]\n"
      "v_cvt_pk_bf16_f32 v210, v136, v137\n"
      "v_mfma_f32_16x16x32_bf16 v[132:135], a[144:147], v[64:67], v[132:135]\n"
      "v_cvt_pk_bf16_f32 v211, v138, v139\n"
      "v_mfma_f32_16x16x32_bf16 v[132:135], a[148:151], v[68:71], v[132:135]\n"
      "v_mfma_f32_16x16x32_bf16 v[132:135], a[152:155], v[84:87], v[132:135]\n"
      "s_waitcnt lgkmcnt(3)\n"
      "v_mfma_f32_16x16x32_bf16 v[132:135], a[156:159], v[88:91], v[132:135]\n"
      "global_store_dwordx4 v252, v[220:223], s[54:55] offset:0 sc1\n"
      "v_mfma_f32_16x16x32_bf16 v[132:135], a[160:163], v[92:95], v[132:135]\n"
      "s_add_u32 m0, s17, 0x13f00\n"
      "v_mfma_f32_16x16x32_bf16 v[132:135], a[164:167], v[108:111], v[132:135]\n"
      "ds_write_b64 v244, v[210:211] offset:8\n"
      "v_mfma_f32_16x16x32_bf16 v[132:135], a[168:171], v[112:115], v[132:135]\n"
      "global_load_lds_dwordx4 v236, s[52:53] offset:256 nt\n"
      "v_mfma_f32_16x16x32_bf16 v[132:135], a[172:175], v[116:119], v[132:135]\n"
      "v_mfma_f32_16x16x32_bf16 v[136:139], a[176:179], v[36:39], v[12:15]\n"
      "v_mfma_f32_16x16x32_bf16 v[136:139], a[180:183], v[40:43], v[136:139]\n"
      "v_mfma_f32_16x16x32_bf16 v[136:139], a[184:187], v[44:47], v[136:139]\n"
      "v_mfma_f32_16x16x32_bf16 v[136:139], a[188:191], v[60:63], v[136:139]\n"
      "v_cvt_pk_bf16_f32 v212, v132, v133\n"
      "v_mfma_f32_16x16x32_bf16 v[136:139], a[192:195], v[64:67], v[136:139]\n"
      "v_cvt_pk_bf16_f32 v213, v134, v135\n"
      "v_mfma_f32_16x16x32_bf16 v[136:139], a[196:199], v[68:71], v[136:139]\n"
      "v_mfma_f32_16x16x32_bf16 v[136:139], a[200:203], v[84:87], v[136:139]\n"
      "v_mfma_f32_16x16x32_bf16 v[136:139], a[204:207], v[88:91], v[136:139]\n"
      "s_waitcnt lgkmcnt(3)\n"
      "v_mfma_f32_16x16x32_bf16 v[136:139], a[208:211], v[92:95], v[136:139]\n"
      "global_store_dwordx4 v253, v[224:227], s[54:55] offset:0 sc1\n"
      "v_mfma_f32_16x16x32_bf16 v[136:139], a[212:215], v[108:111], v[136:139]\n"
      "ds_write_b64 v245, v[212:213] offset:0\n"
      "v_mfma_f32_16x16x32_bf16 v[136:139], a[216:219], v[112:115], v[136:139]\n"
      "s_add_u32 m0, s17, 0x15e00\n"
      "v_mfma_f32_16x16x32_bf16 v[136:139], a[220:223], v[116:119], v[136:139]\n"
      "global_load_lds_dwordx4 v236, s[52:53] offset:512 nt\n"
      "v_mfma_f32_16x16x32_bf16 v[132:135], a[224:227], v[36:39], v[16:19]\n"
      "v_mfma_f32_16x16x32_bf16 v[132:135], a[228:231], v[40:43], v[132:135]\n"
      "v_mfma_f32_16x16x32_bf16 v[132:135], a[232:235], v[44:47], v[132:135]\n"
      "v_mfma_f32_16x16x32_bf16 v[132:135], a[236:239], v[60:63], v[132:135]\n"
      "v_cvt_pk_bf16_f32 v214, v136, v137\n"
      "v_mfma_f32_16x16x32_bf16 v[132:135], a[0:3], v[64:67], v[132:135]\n"
      "v_cvt_pk_bf16_f32 v215, v138, v139\n"
      "v_mfma_f32_16x16x32_bf16 v[132:135], a[4:7], v[68:71], v[132:135]\n"
      "v_mfma_f32_16x16x32_bf16 v[132:135], a[8:11], v[84:87], v[132:135]\n"
      "v_mfma_f32_16x16x32_bf16 v[132:135], a[12:15], v[88:91], v[132:135]\n"
      "v_mfma_f32_16x16x32_bf16 v[132:135], a[16:19], v[92:95], v[132:135]\n"
      "v_mfma_f32_16x16x32_bf16 v[132:135], a[20:23], v[108:111], v[132:135]\n"
      "ds_write_b64 v245, v[214:215] offset:8\n"
      "v_mfma_f32_16x16x32_bf16 v[132:135], v[140:143], v[112:115], v[132:135]\n"
      "v_mfma_f32_16x16x32_bf16 v[132:135], v[156:159], v[116:119], v[132:135]\n"
      "s_waitcnt lgkmcnt(4)\n"
      "s_barrier\n"
      "v_mfma_f32_16x16x32_bf16 v[136:139], v[160:163], v[36:39], v[20:23]\n"
      "ds_read_b128 v[36:39], v232 offset:256\n"
      "v_mfma_f32_16x16x32_bf16 v[136:139], v[164:167], v[40:43], v[136:139]\n"
      "ds_read_b128 v[40:43], v233 offset:256\n"
      "v_mfma_f32_16x16x32_bf16 v[136:139], v[180:183], v[44:47], v[136:139]\n"
      "ds_read_b128 v[44:47], v234 offset:256\n"
      "v_mfma_f32_16x16x32_bf16 v[136:139], v[184:187], v[60:63], v[136:139]\n"
      "ds_read_b128 v[60:63], v235 offset:256\n"
      "v_cvt_pk_bf16_f32 v208, v132, v133\n"
      "v_mfma_f32_16x16x32_bf16 v[136:139], v[188:191], v[64:67], v[136:139]\n"
      "ds_read_b128 v[64:67], v232 offset:8448\n"
      "v_cvt_pk_bf16_f32 v209, v134, v135\n"
      "v_mfma_f32_16x16x32_bf16 v[136:139], v[204:207], v[68:71], v[136:139]\n"
      "ds_read_b128 v[68:71], v233 offset:8448\n"
      "v_mfma_f32_16x16x32_bf16 v[136:139], a[24:27], v[84:87], v[136:139]\n"
      "ds_read_b128 v[84:87], v234 offset:8448\n"
      "v_mfma_f32_16x16x32_bf16 v[136:139], a[28:31], v[88:91], v[136:139]\n"
      "ds_read_b128 v[88:91], v235 offset:8448\n"
      "v_mfma_f32_16x16x32_bf16 v[136:139], a[32:35], v[92:95], v[136:139]\n"
      "ds_read_b128 v[92:95], v232 offset:16640\n"
      "v_mfma_f32_16x16x32_bf16 v[136:139], a[36:39], v[108:111], v[136:139]\n"
      "ds_read_b128 v[108:111], v233 offset:16640\n"
      "v_mfma_f32_16x16x32_bf16 v[136:139], a[40:43], v[112:115], v[136:139]\n"
      "ds_read_b128 v[112:115], v234 offset:16640\n"
      "v_mfma_f32_16x16x32_bf16 v[136:139], a[44:47], v[116:119], v[136:139]\n"
      "ds_read_b128 v[116:119], v235 offset:16640\n"
      "s_waitcnt lgkmcnt(8)\n"
      "v_mfma_f32_16x16x32_bf16 v[132:135], a[240:243], v[36:39], v[0:3]\n"
      "ds_write_b64 v244, v[208:209] offset:128\n"
      "v_mfma_f32_16x16x32_bf16 v[132:135], a[244:247], v[40:43], v[132:135]\n"
      "v_mfma_f32_16x16x32_bf16 v[132:135], a[248:251], v[44:47], v[132:135]\n"
      "v_mfma_f32_16x16x32_bf16 v[132:135], a[252:255], v[60:63], v[132:135]\n"
      "v_cvt_pk_bf16_f32 v210, v136, v137\n"
      "s_waitcnt lgkmcnt(5)\n"
      "v_mfma_f32_16x16x32_bf16 v[132:135], a[48:51], v[64:67], v[132:135]\n"
      "v_cvt_pk_bf16_f32 v211, v138, v139\n"
      "v_mfma_f32_16x16x32_bf16 v[132:135], a[52:55], v[68:71], v[132:135]\n"
      "v_mfma_f32_16x16x32_bf16 v[132:135], a[56:59], v[84:87], v[132:135]\n"
      "ds_read_b128 v[216:219], v246 offset:24576\n"
      "v_mfma_f32_16x16x32_bf16 v[132:135], a[60:63], v[88:91], v[132:135]\n"
      "ds_read_b128 v[220:223], v246 offset:25600\n"
      "s_waitcnt lgkmcnt(3)\n"
      "v_mfma_f32_16x16x32_bf16 v[132:135], a[64:67], v[92:95], v[132:135]\n"
      "ds_read_b128 v[224:227], v246 offset:26624\n"
      "v_mfma_f32_16x16x32_bf16 v[132:135], a[68:71], v[108:111], v[132:135]\n"
      "ds_write_b64 v244, v[210:211] offset:136\n"
      "v_mfma_f32_16x16x32_bf16 v[132:135], a[72:75], v[112:115], v[132:135]\n"
      "v_mfma_f32_16x16x32_bf16 v[132:135], a[76:79], v[116:119], v[132:135]\n"
      "v_mfma_f32_16x16x32_bf16 v[136:139], a[80:83], v[36:39], v[4:7]\n"
      "v_mfma_f32_16x16x32_bf16 v[136:139], a[84:87], v[40:43], v[136:139]\n"
      "v_mfma_f32_16x16x32_bf16 v[136:139], a[88:91], v[44:47], v[136:139]\n"
      "v_mfma_f32_16x16x32_bf16 v[136:139], a[92:95], v[60:63], v[136:139]\n"
      "v_cvt_pk_bf16_f32 v208, v132, v133\n"
      "v_mfma_f32_16x16x32_bf16 v[136:139], a[96:99], v[64:67], v[136:139]\n"
      "v_cvt_pk_bf16_f32 v209, v134, v135\n"
      "v_mfma_f32_16x16x32_bf16 v[136:139], a[100:103], v[68:71], v[136:139]\n"
      "s_waitcnt lgkmcnt(3)\n"
      "v_mfma_f32_16x16x32_bf16 v[136:139], a[104:107], v[84:87], v[136:139]\n"
      "global_store_dwordx4 v247, v[216:219], s[54:55] offset:768 sc1\n"
      "v_mfma_f32_16x16x32_bf16 v[136:139], a[108:111], v[88:91], v[136:139]\n"
      "s_add_u32 m0, s17, 0x13000\n"
      "v_mfma_f32_16x16x32_bf16 v[136:139], a[112:115], v[92:95], v[136:139]\n"
      "global_load_lds_dwordx4 v237, s[52:53] offset:0 nt\n"
      "v_mfma_f32_16x16x32_bf16 v[136:139], a[116:119], v[108:111], v[136:139]\n"
      "ds_write_b64 v244, v[208:209] offset:12288\n"
      "v_mfma_f32_16x16x32_bf16 v[136:139], a[120:123], v[112:115], v[136:139]\n"
      "v_mfma_f32_16x16x32_bf16 v[136:139], a[124:127], v[116:119], v[136:139]\n"
      "v_mfma_f32_16x16x32_bf16 v[132:135], a[128:131], v[36:39], v[8:11]\n"
      "v_mfma_f32_16x16x32_bf16 v[132:135], a[132:135], v[40:43], v[132:135]\n"
      "v_mfma_f32_16x16x32_bf16 v[132:135], a[136:139], v[44:47], v[132:135]\n"
      "v_mfma_f32_16x16x32_bf16 v[132:135], a[140:143], v[60:63], v[132:135]\n"
      "v_cvt_pk_bf16_f32 v210, v136, v137\n"
      "v_mfma_f32_16x16x32_bf16 v[132:135], a[144:147], v[64:67], v[132:135]\n"
      "v_cvt_pk_bf16_f32 v211, v138, v139\n"
      "v_mfma_f32_16x16x32_bf16 v[132:135], a[148:151], v[68:71], v[132:135]\n"
      "v_mfma_f32_16x16x32_bf16 v[132:135], a[152:155], v[84:87], v[132:135]\n"
      "s_waitcnt lgkmcnt(3)\n"
      "s_waitcnt vmcnt(18)\n"
      "s_mov_b32 s87, 1\n"
      "s_or_b32 s56, s51, s76\n"
      "s_cmp_lg_u32 s56, 0\n"
      "s_cbranch_scc1 LG1\n"
      "v_cmp_ne_u64 s[58:59], s[80:81], v[24:25]\n"
      "v_cmp_ne_u64 s[56:57], s[80:81], v[26:27]\n"
      "s_mov_b32 s87, 0\n"
      "s_nop 3\n"
      "s_or_b64 s[56:57], s[56:57], s[58:59]\n"
      "s_cbranch_scc1 LG1\n"
      "v_mov_b32 v28, 0\n"
      "v_mov_b32 v30, s80\n"
      "v_mov_b32 v31, s81\n"
      "s_mov_b32 s87, 1\n"
      "s_mov_b64 exec, 1\n"
      "global_store_dwordx2 v28, v[30:31], s[28:29] sc1\n"
      "s_mov_b64 exec, -1\n"
      "s_nop 4\n"
      "LG1:\n"
      "v_mfma_f32_16x16x32_bf16 v[132:135], a[156:159], v[88:91], v[132:135]\n"
      "global_store_dwordx4 v252, v[220:223], s[54:55] offset:768 sc1\n"
      "v_mfma_f32_16x16x32_bf16 v[132:135], a[160:163], v[92:95], v[132:135]\n"
      "s_add_u32 m0, s17, 0x14f00\n"
      "v_mfma_f32_16x16x32_bf16 v[132:135], a[164:167], v[108:111], v[132:135]\n"
      "ds_write_b64 v244, v[210:211] offset:12296\n"
      "v_mfma_f32_16x16x32_bf16 v[132:135], a[168:171], v[112:115], v[132:135]\n"
      "global_load_lds_dwordx4 v237, s[52:53] offset:256 nt\n"
      "v_mfma_f32_16x16x32_bf16 v[132:135], a[172:175], v[116:119], v[132:135]\n"
      "v_mfma_f32_16x16x32_bf16 v[136:139], a[176:179], v[36:39], v[12:15]\n"
      "v_mfma_f32_16x16x32_bf16 v[136:139], a[180:183], v[40:43], v[136:139]\n"
      "v_mfma_f32_16x16x32_bf16 v[136:139], a[184:187], v[44:47], v[136:139]\n"
      "v_mfma_f32_16x16x32_bf16 v[136:139], a[188:191], v[60:63], v[136:139]\n"
      "v_cvt_pk_bf16_f32 v212, v132, v133\n"
      "v_mfma_f32_16x16x32_bf16 v[136:139], a[192:195], v[64:67], v[136:139]\n"
      "v_cvt_pk_bf16_f32 v213, v134, v135\n"
      "v_mfma_f32_16x16x32_bf16 v[136:139], a[196:199], v[68:71], v[136:139]\n"
      "v_mfma_f32_16x16x32_bf16 v[136:139], a[200:203], v[84:87], v[136:139]\n"
      "v_mfma_f32_16x16x32_bf16 v[136:139], a[204:207], v[88:91], v[136:139]\n"
      "s_waitcnt lgkmcnt(3)\n"
      "v_mfma_f32_16x16x32_bf16 v[136:139], a[208:211], v[92:95], v[136:139]\n"
      "global_store_dwordx4 v253, v[224:227], s[54:55] offset:768 sc1\n"
      "v_mfma_f32_16x16x32_bf16 v[136:139], a[212:215], v[108:111], v[136:139]\n"
      "ds_write_b64 v245, v[212:213] offset:12288\n"
      "v_mfma_f32_16x16x32_bf16 v[136:139], a[216:219], v[112:115], v[136:139]\n"
      "s_add_u32 m0, s17, 0x16e00\n"
      "v_mfma_f32_16x16x32_bf16 v[136:139], a[220:223], v[116:119], v[136:139]\n"
      "global_load_lds_dwordx4 v237, s[52:53] offset:512 nt\n"
      "v_mfma_f32_16x16x32_bf16 v[132:135], a[224:227], v[36:39], v[16:19]\n"
      "v_mfma_f32_16x16x32_bf16 v[132:135], a[228:231], v[40:43], v[132:135]\n"
      "v_mfma_f32_16x16x32_bf16 v[132:135], a[232:235], v[44:47], v[132:135]\n"
      "v_mfma_f32_16x16x32_bf16 v[132:135], a[236:239], v[60:63], v[132:135]\n"
      "s_memrealtime s[84:85]\n"
      "v_mfma_f32_16x16x32_bf16 v[132:135], a[0:3], v[64:67], v[132:135]\n"
      "v_cvt_pk_bf16_f32 v214, v136, v137\n"
      "v_mfma_f32_16x16x32_bf16 v[132:135], a[4:7], v[68:71], v[132:135]\n"
      "v_cvt_pk_bf16_f32 v215, v138, v139\n"
      "v_mfma_f32_16x16x32_bf16 v[132:135], a[8:11], v[84:87], v[132:135]\n"
      "v_mfma_f32_16x16x32_bf16 v[132:135], a[12:15], v[88:91], v[132:135]\n"
      "v_mfma_f32_16x16x32_bf16 v[132:135], a[16:19], v[92:95], v[132:135]\n"
      "v_mfma_f32_16x16x32_bf16 v[132:135], a[20:23], v[108:111], v[132:135]\n"
      "v_mfma_f32_16x16x32_bf16 v[132:135], v[140:143], v[112:115], v[132:135]\n"
      "s_waitcnt lgkmcnt(0)\n"
      "s_sub_u32 s48, s84, s48\n"
      "v_mfma_f32_16x16x32_bf16 v[132:135], v[156:159], v[116:119], v[132:135]\n"
      "s_barrier\n"
      "ds_write_b64 v245, v[214:215] offset:12296\n"
      "v_mfma_f32_16x16x32_bf16 v[136:139], v[160:163], v[36:39], v[20:23]\n"
      "ds_read_b128 v[36:39], v232 offset:24576\n"
      "s_lshl_b32 s79, s91, 1\n"
      "v_mfma_f32_16x16x32_bf16 v[136:139], v[164:167], v[40:43], v[136:139]\n"
      "ds_read_b128 v[40:43], v233 offset:24576\n"
      "v_mad_i32_i24 v252, v251, s79, v252\n"
      "v_mfma_f32_16x16x32_bf16 v[136:139], v[180:183], v[44:47], v[136:139]\n"
      "ds_read_b128 v[44:47], v234 offset:24576\n"
      "s_mul_i32 s79, s79, s100\n"
      "v_mfma_f32_16x16x32_bf16 v[136:139], v[184:187], v[60:63], v[136:139]\n"
      "ds_read_b128 v[60:63], v235 offset:24576\n"
      "v_cvt_pk_bf16_f32 v208, v132, v133\n"
      "v_mfma_f32_16x16x32_bf16 v[136:139], v[188:191], v[64:67], v[136:139]\n"
      "ds_read_b128 v[64:67], v232 offset:32768\n"
      "v_cvt_pk_bf16_f32 v209, v134, v135\n"
      "v_mfma_f32_16x16x32_bf16 v[136:139], v[204:207], v[68:71], v[136:139]\n"
      "ds_read_b128 v[68:71], v233 offset:32768\n"
      "v_add_u32 v247, s79, v247\n"
      "v_mfma_f32_16x16x32_bf16 v[136:139], a[24:27], v[84:87], v[136:139]\n"
      "ds_read_b128 v[84:87], v234 offset:32768\n"
      "s_mul_i32 s79, s79, s101\n"
      "v_mfma_f32_16x16x32_bf16 v[136:139], a[28:31], v[88:91], v[136:139]\n"
      "ds_read_b128 v[88:91], v235 offset:32768\n"
      "v_add_u32 v253, s79, v253\n"
      "v_mfma_f32_16x16x32_bf16 v[136:139], a[32:35], v[92:95], v[136:139]\n"
      "ds_read_b128 v[92:95], v232 offset:40960\n"
      "v_mfma_f32_16x16x32_bf16 v[136:139], a[36:39], v[108:111], v[136:139]\n"
      "ds_read_b128 v[108:111], v233 offset:40960\n"
      "v_mfma_f32_16x16x32_bf16 v[136:139], a[40:43], v[112:115], v[136:139]\n"
      "ds_read_b128 v[112:115], v234 offset:40960\n"
      "v_mfma_f32_16x16x32_bf16 v[136:139], a[44:47], v[116:119], v[136:139]\n"
      "ds_read_b128 v[116:119], v235 offset:40960\n"
      "s_waitcnt lgkmcnt(8)\n"
      "v_mfma_f32_16x16x32_bf16 v[132:135], a[240:243], v[36:39], v[0:3]\n"
      "ds_write_b64 v244, v[208:209] offset:12416\n"
      "v_mfma_f32_16x16x32_bf16 v[132:135], a[244:247], v[40:43], v[132:135]\n"
      "s_add_u32 s54, s30, s34\n"
      "s_addc_u32 s55, s31, 0\n"
      "v_mfma_f32_16x16x32_bf16 v[132:135], a[248:251], v[44:47], v[132:135]\n"
      "s_mul_i32 s48, s48, 853\n"
      "v_mfma_f32_16x16x32_bf16 v[132:135], a[252:255], v[60:63], v[132:135]\n"
      "s_lshr_b32 s48, s48, 8\n"
      "v_cvt_pk_bf16_f32 v210, v136, v137\n"
      "s_waitcnt lgkmcnt(5)\n"
      "v_mfma_f32_16x16x32_bf16 v[132:135], a[48:51], v[64:67], v[132:135]\n"
      "s_add_u32 s48, s48, s84\n"
      "v_cvt_pk_bf16_f32 v211, v138, v139\n"
      "v_mfma_f32_16x16x32_bf16 v[132:135], a[52:55], v[68:71], v[132:135]\n"
      "s_xor_b32 s49, s48, s80\n"
      "s_mul_i32 s78, s93, s19\n"
      "v_mfma_f32_16x16x32_bf16 v[132:135], a[56:59], v[84:87], v[132:135]\n"
      "v_mov_b32 v230, s49\n"
      "v_add_u32 v236, s78, v236\n"
      "v_mfma_f32_16x16x32_bf16 v[132:135], a[60:63], v[88:91], v[132:135]\n"
      "s_mul_i32 s49, s51, 0x1100\n"
      "s_mul_i32 s78, s78, s99\n"
      "s_waitcnt lgkmcnt(1)\n"
      "v_mfma_f32_16x16x32_bf16 v[132:135], a[64:67], v[92:95], v[132:135]\n"
      "s_and_b32 s49, s49, 0xf000\n"
      "v_add_u32 v237, s78, v237\n"
      "v_mfma_f32_16x16x32_bf16 v[132:135], a[68:71], v[108:111], v[132:135]\n"
      "s_lshr_b32 s49, s49, 0\n"
      "ds_write_b64 v244, v[210:211] offset:12424\n"
      "v_mfma_f32_16x16x32_bf16 v[132:135], a[72:75], v[112:115], v[132:135]\n"
      "s_add_u32 s49, s49, 1114112\n"
      "v_mfma_f32_16x16x32_bf16 v[132:135], a[76:79], v[116:119], v[132:135]\n"
      "v_mov_b32 v229, s49\n"
      "v_mfma_f32_16x16x32_bf16 v[136:139], a[80:83], v[36:39], v[4:7]\n"
      "s_and_b32 s49, s51, 0x78\n"
      "v_mfma_f32_16x16x32_bf16 v[136:139], a[84:87], v[40:43], v[136:139]\n"
      "s_xor_b32 s49, s49, 0x40\n"
      "v_mfma_f32_16x16x32_bf16 v[136:139], a[88:91], v[44:47], v[136:139]\n"
      "s_or_b32 s49, s49, s76\n"
      "v_mfma_f32_16x16x32_bf16 v[136:139], a[92:95], v[60:63], v[136:139]\n"
      "v_cvt_pk_bf16_f32 v208, v132, v133\n"
      "v_mfma_f32_16x16x32_bf16 v[136:139], a[96:99], v[64:67], v[136:139]\n"
      "s_cmp_lg_u32 s49, 0\n"
      "s_cbranch_scc0 LPS\n"
      "LPR:\n"
      "v_mfma_f32_16x16x32_bf16 v[136:139], a[100:103], v[68:71], v[136:139]\n"
      "v_cvt_pk_bf16_f32 v209, v134, v135\n"
      "v_mfma_f32_16x16x32_bf16 v[136:139], a[104:107], v[84:87], v[136:139]\n"
      "ds_read_b128 v[216:219], v246 offset:0\n"
      "v_mfma_f32_16x16x32_bf16 v[136:139], a[108:111], v[88:91], v[136:139]\n"
      "ds_read_b128 v[220:223], v246 offset:1024\n"
      "v_mfma_f32_16x16x32_bf16 v[136:139], a[112:115], v[92:95], v[136:139]\n"
      "ds_read_b128 v[224:227], v246 offset:2048\n"
      "v_mfma_f32_16x16x32_bf16 v[136:139], a[116:119], v[108:111], v[136:139]\n"
      "s_waitcnt lgkmcnt(2)\n"
      "v_mfma_f32_16x16x32_bf16 v[136:139], a[120:123], v[112:115], v[136:139]\n"
      "ds_write_b64 v244, v[208:209] offset:24576\n"
      "v_mfma_f32_16x16x32_bf16 v[136:139], a[124:127], v[116:119], v[136:139]\n"
      "global_store_dwordx4 v247, v[216:219], s[54:55] offset:0 sc1\n"
      "v_mfma_f32_16x16x32_bf16 v[132:135], a[128:131], v[36:39], v[8:11]\n"
      "s_add_u32 s52, s20, s36\n"
      "s_addc_u32 s53, s21, 0\n"
      "v_mfma_f32_16x16x32_bf16 v[132:135], a[132:135], v[40:43], v[132:135]\n"
      "s_add_u32 m0, s17, 0x0\n"
      "v_mfma_f32_16x16x32_bf16 v[132:135], a[136:139], v[44:47], v[132:135]\n"
      "global_load_lds_dwordx4 v236, s[52:53] offset:0 nt\n"
      "v_mfma_f32_16x16x32_bf16 v[132:135], a[140:143], v[60:63], v[132:135]\n"
      "v_cvt_pk_bf16_f32 v210, v136, v137\n"
      "v_mfma_f32_16x16x32_bf16 v[132:135], a[144:147], v[64:67], v[132:135]\n"
      "v_cvt_pk_bf16_f32 v211, v138, v139\n"
      "v_mfma_f32_16x16x32_bf16 v[132:135], a[148:151], v[68:71], v[132:135]\n"
      "v_mfma_f32_16x16x32_bf16 v[132:135], a[152:155], v[84:87], v[132:135]\n"
      "s_waitcnt lgkmcnt(2)\n"
      "v_mfma_f32_16x16x32_bf16 v[132:135], a[156:159], v[88:91], v[132:135]\n"
      "global_store_dwordx4 v252, v[220:223], s[54:55] offset:0 sc1\n"
      "v_mfma_f32_16x16x32_bf16 v[132:135], a[160:163], v[92:95], v[132:135]\n"
      "v_mfma_f32_16x16x32_bf16 v[132:135], a[164:167], v[108:111], v[132:135]\n"
      "ds_write_b64 v244, v[210:211] offset:24584\n"
      "v_mfma_f32_16x16x32_bf16 v[132:135], a[168:171], v[112:115], v[132:135]\n"
      "v_mfma_f32_16x16x32_bf16 v[132:135], a[172:175], v[116:119], v[132:135]\n"
      "v_mfma_f32_16x16x32_bf16 v[136:139], a[176:179], v[36:39], v[12:15]\n"
      "s_add_u32 m0, s17, 0x1f00\n"
      "v_mfma_f32_16x16x32_bf16 v[136:139], a[180:183], v[40:43], v[136:139]\n"
      "global_load_lds_dwordx4 v236, s[52:53] offset:256 nt\n"
      "v_mfma_f32_16x16x32_bf16 v[136:139], a[184:187], v[44:47], v[136:139]\n"
      "v_mfma_f32_16x16x32_bf16 v[136:139], a[188:191], v[60:63], v[136:139]\n"
      "v_cvt_pk_bf16_f32 v212, v132, v133\n"
      "v_mfma_f32_16x16x32_bf16 v[136:139], a[192:195], v[64:67], v[136:139]\n"
      "v_cvt_pk_bf16_f32 v213, v134, v135\n"
      "v_mfma_f32_16x16x32_bf16 v[136:139], a[196:199], v[68:71], v[136:139]\n"
      "v_mfma_f32_16x16x32_bf16 v[136:139], a[200:203], v[84:87], v[136:139]\n"
      "v_mfma_f32_16x16x32_bf16 v[136:139], a[204:207], v[88:91], v[136:139]\n"
      "s_waitcnt lgkmcnt(2)\n"
      "v_mfma_f32_16x16x32_bf16 v[136:139], a[208:211], v[92:95], v[136:139]\n"
      "global_store_dwordx4 v253, v[224:227], s[54:55] offset:0 sc1\n"
      "v_mfma_f32_16x16x32_bf16 v[136:139], a[212:215], v[108:111], v[136:139]\n"
      "ds_write_b64 v245, v[212:213] offset:24576\n"
      "v_mfma_f32_16x16x32_bf16 v[136:139], a[216:219], v[112:115], v[136:139]\n"
      "v_mfma_f32_16x16x32_bf16 v[136:139], a[220:223], v[116:119], v[136:139]\n"
      "s_add_u32 m0, s17, 0x3e00\n"
      "v_mfma_f32_16x16x32_bf16 v[132:135], a[224:227], v[36:39], v[16:19]\n"
      "global_load_lds_dwordx4 v236, s[52:53] offset:512 nt\n"
      "v_mfma_f32_16x16x32_bf16 v[132:135], a[228:231], v[40:43], v[132:135]\n"
      "v_mfma_f32_16x16x32_bf16 v[132:135], a[232:235], v[44:47], v[132:135]\n"
      "v_mfma_f32_16x16x32_bf16 v[132:135], a[236:239], v[60:63], v[132:135]\n"
      "v_cvt_pk_bf16_f32 v214, v136, v137\n"
      "v_mfma_f32_16x16x32_bf16 v[132:135], a[0:3], v[64:67], v[132:135]\n"
      "v_cvt_pk_bf16_f32 v215, v138, v139\n"
      "v_mfma_f32_16x16x32_bf16 v[132:135], a[4:7], v[68:71], v[132:135]\n"
      "v_mfma_f32_16x16x32_bf16 v[132:135], a[8:11], v[84:87], v[132:135]\n"
      "v_mfma_f32_16x16x32_bf16 v[132:135], a[12:15], v[88:91], v[132:135]\n"
      "v_mfma_f32_16x16x32_bf16 v[132:135], a[16:19], v[92:95], v[132:135]\n"
      "v_mfma_f32_16x16x32_bf16 v[132:135], a[20:23], v[108:111], v[132:135]\n"
      "ds_write_b64 v245, v[214:215] offset:24584\n"
      "v_mfma_f32_16x16x32_bf16 v[132:135], v[140:143], v[112:115], v[132:135]\n"
      "v_mfma_f32_16x16x32_bf16 v[132:135], v[156:159], v[116:119], v[132:135]\n"
      "s_barrier\n"
      "v_mfma_f32_16x16x32_bf16 v[136:139], v[160:163], v[36:39], v[20:23]\n"
      "ds_read_b128 v[36:39], v232 offset:24832\n"
      "v_mfma_f32_16x16x32_bf16 v[136:139], v[164:167], v[40:43], v[136:139]\n"
      "ds_read_b128 v[40:43], v233 offset:24832\n"
      "v_mfma_f32_16x16x32_bf16 v[136:139], v[180:183], v[44:47], v[136:139]\n"
      "ds_read_b128 v[44:47], v234 offset:24832\n"
      "v_mfma_f32_16x16x32_bf16 v[136:139], v[184:187], v[60:63], v[136:139]\n"
      "ds_read_b128 v[60:63], v235 offset:24832\n"
      "v_cvt_pk_bf16_f32 v208, v132, v133\n"
      "v_mfma_f32_16x16x32_bf16 v[136:139], v[188:191], v[64:67], v[136:139]\n"
      "ds_read_b128 v[64:67], v232 offset:33024\n"
      "v_cvt_pk_bf16_f32 v209, v134, v135\n"
      "v_mfma_f32_16x16x32_bf16 v[136:139], v[204:207], v[68:71], v[136:139]\n"
      "ds_read_b128 v[68:71], v233 offset:33024\n"
      "v_mfma_f32_16x16x32_bf16 v[136:139], a[24:27], v[84:87], v[136:139]\n"
      "ds_read_b128 v[84:87], v234 offset:33024\n"
      "v_mfma_f32_16x16x32_bf16 v[136:139], a[28:31], v[88:91], v[136:139]\n"
      "ds_read_b128 v[88:91], v235 offset:33024\n"
      "v_mfma_f32_16x16x32_bf16 v[136:139], a[32:35], v[92:95], v[136:139]\n"
      "ds_read_b128 v[92:95], v232 offset:41216\n"
      "v_mfma_f32_16x16x32_bf16 v[136:139], a[36:39], v[108:111], v[136:139]\n"
      "LPD10:\n"
      "s_cbranch_execz LPD13\n"
      "ds_read_b128 v[108:111], v233 offset:41216\n"
      "ds_write_b64 v244, v[208:209] offset:24704\n"
      "v_mfma_f32_16x16x32_bf16 v[136:139], a[40:43], v[112:115], v[136:139]\n"
      "ds_read_b128 v[112:115], v234 offset:41216\n"
      "v_mfma_f32_16x16x32_bf16 v[136:139], a[44:47], v[116:119], v[136:139]\n"
      "ds_read_b128 v[116:119], v235 offset:41216\n"
      "s_waitcnt lgkmcnt(9)\n"
      "v_mfma_f32_16x16x32_bf16 v[132:135], a[240:243], v[36:39], v[0:3]\n"
      "v_add_u32 v232, 0xc000, v232\n"
      "v_mfma_f32_16x16x32_bf16 v[132:135], a[244:247], v[40:43], v[132:135]\n"
      "v_add_u32 v233, 0xc000, v233\n"
      "v_mfma_f32_16x16x32_bf16 v[132:135], a[248:251], v[44:47], v[132:135]\n"
      "v_add_u32 v234, 0xc000, v234\n"
      "v_mfma_f32_16x16x32_bf16 v[132:135], a[252:255], v[60:63], v[132:135]\n"
      "v_add_u32 v235, 0xc000, v235\n"
      "s_waitcnt lgkmcnt(5)\n"
      "v_mfma_f32_16x16x32_bf16 v[132:135], a[48:51], v[64:67], v[132:135]\n"
      "v_cvt_pk_bf16_f32 v210, v136, v137\n"
      "v_mfma_f32_16x16x32_bf16 v[132:135], a[52:55], v[68:71], v[132:135]\n"
      "v_cvt_pk_bf16_f32 v211, v138, v139\n"
      "v_mfma_f32_16x16x32_bf16 v[132:135], a[56:59], v[84:87], v[132:135]\n"
      "ds_read_b128 v[216:219], v246 offset:12288\n"
      "v_mfma_f32_16x16x32_bf16 v[132:135], a[60:63], v[88:91], v[132:135]\n"
      "ds_read_b128 v[220:223], v246 offset:13312\n"
      "s_waitcnt lgkmcnt(2)\n"
      "v_mfma_f32_16x16x32_bf16 v[132:135], a[64:67], v[92:95], v[132:135]\n"
      "ds_read_b128 v[224:227], v246 offset:14336\n"
      "v_mfma_f32_16x16x32_bf16 v[132:135], a[68:71], v[108:111], v[132:135]\n"
      "v_lshlrev_b32 v0, 16, v72\n"
      "v_mfma_f32_16x16x32_bf16 v[132:135], a[72:75], v[112:115], v[132:135]\n"
      "ds_write_b64 v244, v[210:211] offset:24712\n"
      "v_mfma_f32_16x16x32_bf16 v[132:135], a[76:79], v[116:119], v[132:135]\n"
      "v_and_b32 v1, 0xffff0000, v72\n"
      "v_mfma_f32_16x16x32_bf16 v[136:139], a[80:83], v[36:39], v[4:7]\n"
      "v_lshlrev_b32 v2, 16, v73\n"
      "v_mfma_f32_16x16x32_bf16 v[136:139], a[84:87], v[40:43], v[136:139]\n"
      "v_and_b32 v3, 0xffff0000, v73\n"
      "v_mfma_f32_16x16x32_bf16 v[136:139], a[88:91], v[44:47], v[136:139]\n"
      "v_mfma_f32_16x16x32_bf16 v[136:139], a[92:95], v[60:63], v[136:139]\n"
      "v_cvt_pk_bf16_f32 v208, v132, v133\n"
      "v_mfma_f32_16x16x32_bf16 v[136:139], a[96:99], v[64:67], v[136:139]\n"
      "v_cvt_pk_bf16_f32 v209, v134, v135\n"
      "v_mfma_f32_16x16x32_bf16 v[136:139], a[100:103], v[68:71], v[136:139]\n"
      "s_waitcnt lgkmcnt(3)\n"
      "v_mfma_f32_16x16x32_bf16 v[136:139], a[104:107], v[84:87], v[136:139]\n"
      "global_store_dwordx4 v247, v[216:219], s[54:55] offset:768 sc1\n"
      "v_mfma_f32_16x16x32_bf16 v[136:139], a[108:111], v[88:91], v[136:139]\n"
      "s_add_u32 m0, s17, 0x1000\n"
      "v_mfma_f32_16x16x32_bf16 v[136:139], a[112:115], v[92:95], v[136:139]\n"
      "global_load_lds_dwordx4 v237, s[52:53] offset:0 nt\n"
      "v_mfma_f32_16x16x32_bf16 v[136:139], a[116:119], v[108:111], v[136:139]\n"
      "ds_write_b64 v244, v[208:209] offset:0\n"
      "v_mfma_f32_16x16x32_bf16 v[136:139], a[120:123], v[112:115], v[136:139]\n"
      "v_lshlrev_b32 v4, 16, v74\n"
      "v_mfma_f32_16x16x32_bf16 v[136:139], a[124:127], v[116:119], v[136:139]\n"
      "v_and_b32 v5, 0xffff0000, v74\n"
      "v_mfma_f32_16x16x32_bf16 v[132:135], a[128:131], v[36:39], v[8:11]\n"
      "v_lshlrev_b32 v6, 16, v75\n"
      "v_mfma_f32_16x16x32_bf16 v[132:135], a[132:135], v[40:43], v[132:135]\n"
      "v_and_b32 v7, 0xffff0000, v75\n"
      "v_mfma_f32_16x16x32_bf16 v[132:135], a[136:139], v[44:47], v[132:135]\n"
      "v_mfma_f32_16x16x32_bf16 v[132:135], a[140:143], v[60:63], v[132:135]\n"
      "v_cvt_pk_bf16_f32 v210, v136, v137\n"
      "v_mfma_f32_16x16x32_bf16 v[132:135], a[144:147], v[64:67], v[132:135]\n"
      "v_cvt_pk_bf16_f32 v211, v138, v139\n"
      "v_mfma_f32_16x16x32_bf16 v[132:135], a[148:151], v[68:71], v[132:135]\n"
      "v_lshlrev_b32 v8, 16, v76\n"
      "v_mfma_f32_16x16x32_bf16 v[132:135], a[152:155], v[84:87], v[132:135]\n"
      "s_waitcnt lgkmcnt(3)\n"
      "v_mfma_f32_16x16x32_bf16 v[132:135], a[156:159], v[88:91], v[132:135]\n"
      "global_store_dwordx4 v252, v[220:223], s[54:55] offset:768 sc1\n"
      "v_mfma_f32_16x16x32_bf16 v[132:135], a[160:163], v[92:95], v[132:135]\n"
      "s_add_u32 m0, s17, 0x2f00\n"
      "v_mfma_f32_16x16x32_bf16 v[132:135], a[164:167], v[108:111], v[132:135]\n"
      "ds_write_b64 v244, v[210:211] offset:8\n"
      "v_mfma_f32_16x16x32_bf16 v[132:135], a[168:171], v[112:115], v[132:135]\n"
      "global_load_lds_dwordx4 v237, s[52:53] offset:256 nt\n"
      "v_mfma_f32_16x16x32_bf16 v[132:135], a[172:175], v[116:119], v[132:135]\n"
      "v_and_b32 v9, 0xffff0000, v76\n"
      "v_mfma_f32_16x16x32_bf16 v[136:139], a[176:179], v[36:39], v[12:15]\n"
      "v_lshlrev_b32 v10, 16, v77\n"
      "v_mfma_f32_16x16x32_bf16 v[136:139], a[180:183], v[40:43], v[136:139]\n"
      "v_and_b32 v11, 0xffff0000, v77\n"
      "v_mfma_f32_16x16x32_bf16 v[136:139], a[184:187], v[44:47], v[136:139]\n"
      "v_mfma_f32_16x16x32_bf16 v[136:139], a[188:191], v[60:63], v[136:139]\n"
      "v_cvt_pk_bf16_f32 v212, v132, v133\n"
      "v_mfma_f32_16x16x32_bf16 v[136:139], a[192:195], v[64:67], v[136:139]\n"
      "v_cvt_pk_bf16_f32 v213, v134, v135\n"
      "v_mfma_f32_16x16x32_bf16 v[136:139], a[196:199], v[68:71], v[136:139]\n"
      "v_lshlrev_b32 v12, 16, v78\n"
      "v_mfma_f32_16x16x32_bf16 v[136:139], a[200:203], v[84:87], v[136:139]\n"
      "v_and_b32 v13, 0xffff0000, v78\n"
      "v_mfma_f32_16x16x32_bf16 v[136:139], a[204:207], v[88:91], v[136:139]\n"
      "s_waitcnt lgkmcnt(3)\n"
      "v_mfma_f32_16x16x32_bf16 v[136:139], a[208:211], v[92:95], v[136:139]\n"
      "global_store_dwordx4 v253, v[224:227], s[54:55] offset:768 sc1\n"
      "v_mfma_f32_16x16x32_bf16 v[136:139], a[212:215], v[108:111], v[136:139]\n"
      "ds_write_b64 v245, v[212:213] offset:0\n"
      "v_mfma_f32_16x16x32_bf16 v[136:139], a[216:219], v[112:115], v[136:139]\n"
      "s_add_u32 m0, s17, 0x4e00\n"
      "v_mfma_f32_16x16x32_bf16 v[136:139], a[220:223], v[116:119], v[136:139]\n"
      "global_load_lds_dwordx4 v237, s[52:53] offset:512 nt\n"
      "v_mfma_f32_16x16x32_bf16 v[132:135], a[224:227], v[36:39], v[16:19]\n"
      "v_lshlrev_b32 v14, 16, v79\n"
      "v_mfma_f32_16x16x32_bf16 v[132:135], a[228:231], v[40:43], v[132:135]\n"
      "v_and_b32 v15, 0xffff0000, v79\n"
      "v_mfma_f32_16x16x32_bf16 v[132:135], a[232:235], v[44:47], v[132:135]\n"
      "v_mfma_f32_16x16x32_bf16 v[132:135], a[236:239], v[60:63], v[132:135]\n"
      "v_cvt_pk_bf16_f32 v214, v136, v137\n"
      "v_mfma_f32_16x16x32_bf16 v[132:135], a[0:3], v[64:67], v[132:135]\n"
      "v_cvt_pk_bf16_f32 v215, v138, v139\n"
      "v_mfma_f32_16x16x32_bf16 v[132:135], a[4:7], v[68:71], v[132:135]\n"
      "v_lshlrev_b32 v16, 16, v80\n"
      "v_mfma_f32_16x16x32_bf16 v[132:135], a[8:11], v[84:87], v[132:135]\n"
      "v_and_b32 v17, 0xffff0000, v80\n"
      "v_mfma_f32_16x16x32_bf16 v[132:135], a[12:15], v[88:91], v[132:135]\n"
      "v_lshlrev_b32 v18, 16, v81\n"
      "v_mfma_f32_16x16x32_bf16 v[132:135], a[16:19], v[92:95], v[132:135]\n"
      "v_and_b32 v19, 0xffff0000, v81\n"
      "v_mfma_f32_16x16x32_bf16 v[132:135], a[20:23], v[108:111], v[132:135]\n"
      "ds_write_b64 v245, v[214:215] offset:8\n"
      "v_mfma_f32_16x16x32_bf16 v[132:135], v[140:143], v[112:115], v[132:135]\n"
      "v_mfma_f32_16x16x32_bf16 v[132:135], v[156:159], v[116:119], v[132:135]\n"
      "s_waitcnt vmcnt(24) lgkmcnt(4)\n"
      "s_barrier\n"
      "v_mfma_f32_16x16x32_bf16 v[136:139], v[160:163], v[36:39], v[20:23]\n"
      "ds_read_b128 v[36:39], v232 offset:0\n"
      "v_mfma_f32_16x16x32_bf16 v[136:139], v[164:167], v[40:43], v[136:139]\n"
      "ds_read_b128 v[40:43], v233 offset:0\n"
      "v_mfma_f32_16x16x32_bf16 v[136:139], v[180:183], v[44:47], v[136:139]\n"
      "ds_read_b128 v[44:47], v234 offset:0\n"
      "v_mfma_f32_16x16x32_bf16 v[136:139], v[184:187], v[60:63], v[136:139]\n"
      "ds_read_b128 v[60:63], v235 offset:0\n"
      "v_cvt_pk_bf16_f32 v208, v132, v133\n"
      "v_mfma_f32_16x16x32_bf16 v[136:139], v[188:191], v[64:67], v[136:139]\n"
      "ds_read_b128 v[64:67], v232 offset:8192\n"
      "v_cvt_pk_bf16_f32 v209, v134, v135\n"
      "v_mfma_f32_16x16x32_bf16 v[136:139], v[204:207], v[68:71], v[136:139]\n"
      "ds_read_b128 v[68:71], v233 offset:8192\n"
      "v_mfma_f32_16x16x32_bf16 v[136:139], a[24:27], v[84:87], v[136:139]\n"
      "ds_read_b128 v[84:87], v234 offset:8192\n"
      "v_mfma_f32_16x16x32_bf16 v[136:139], a[28:31], v[88:91], v[136:139]\n"
      "ds_read_b128 v[88:91], v235 offset:8192\n"
      "v_mfma_f32_16x16x32_bf16 v[136:139], a[32:35], v[92:95], v[136:139]\n"
      "ds_read_b128 v[92:95], v232 offset:16384\n"
      "v_mfma_f32_16x16x32_bf16 v[136:139], a[36:39], v[108:111], v[136:139]\n"
      "ds_read_b128 v[108:111], v233 offset:16384\n"
      "v_mfma_f32_16x16x32_bf16 v[136:139], a[40:43], v[112:115], v[136:139]\n"
      "ds_read_b128 v[112:115], v234 offset:16384\n"
      "v_mfma_f32_16x16x32_bf16 v[136:139], a[44:47], v[116:119], v[136:139]\n"
      "ds_read_b128 v[116:119], v235 offset:16384\n"
      "s_waitcnt lgkmcnt(8)\n"
      "v_mfma_f32_16x16x32_bf16 v[132:135], a[240:243], v[36:39], v[0:3]\n"
      "ds_write_b64 v244, v[208:209] offset:128\n"
      "v_mfma_f32_16x16x32_bf16 v[132:135], a[244:247], v[40:43], v[132:135]\n"
      "v_lshlrev_b32 v20, 16, v82\n"
      "v_mfma_f32_16x16x32_bf16 v[132:135], a[248:251], v[44:47], v[132:135]\n"
      "v_and_b32 v21, 0xffff0000, v82\n"
      "v_mfma_f32_16x16x32_bf16 v[132:135], a[252:255], v[60:63], v[132:135]\n"
      "v_lshlrev_b32 v22, 16, v83\n"
      "s_waitcnt lgkmcnt(5)\n"
      "v_mfma_f32_16x16x32_bf16 v[132:135], a[48:51], v[64:67], v[132:135]\n"
      "v_and_b32 v23, 0xffff0000, v83\n"
      "v_mfma_f32_16x16x32_bf16 v[132:135], a[52:55], v[68:71], v[132:135]\n"
      "v_cvt_pk_bf16_f32 v210, v136, v137\n"
      "v_mfma_f32_16x16x32_bf16 v[132:135], a[56:59], v[84:87], v[132:135]\n"
      "v_cvt_pk_bf16_f32 v211, v138, v139\n"
      "v_mfma_f32_16x16x32_bf16 v[132:135], a[60:63], v[88:91], v[132:135]\n"
      "s_add_u32 s54, s44, s34\n"
      "s_addc_u32 s55, s45, 0\n"
      "s_waitcnt lgkmcnt(1)\n"
      "v_mfma_f32_16x16x32_bf16 v[132:135], a[64:67], v[92:95], v[132:135]\n"
      "ds_read_b128 v[216:219], v246 offset:24576\n"
      "v_mfma_f32_16x16x32_bf16 v[132:135], a[68:71], v[108:111], v[132:135]\n"
      "ds_read_b128 v[220:223], v246 offset:25600\n"
      "v_mfma_f32_16x16x32_bf16 v[132:135], a[72:75], v[112:115], v[132:135]\n"
      "ds_read_b128 v[224:227], v246 offset:26624\n"
      "v_mfma_f32_16x16x32_bf16 v[132:135], a[76:79], v[116:119], v[132:135]\n"
      "ds_write_b64 v244, v[210:211] offset:136\n"
      "v_mfma_f32_16x16x32_bf16 v[136:139], a[80:83], v[36:39], v[4:7]\n"
      "v_mfma_f32_16x16x32_bf16 v[136:139], a[84:87], v[40:43], v[136:139]\n"
      "v_mfma_f32_16x16x32_bf16 v[136:139], a[88:91], v[44:47], v[136:139]\n"
      "v_mfma_f32_16x16x32_bf16 v[136:139], a[92:95], v[60:63], v[136:139]\n"
      "v_cvt_pk_bf16_f32 v208, v132, v133\n"
      "v_mfma_f32_16x16x32_bf16 v[136:139], a[96:99], v[64:67], v[136:139]\n"
      "v_cvt_pk_bf16_f32 v209, v134, v135\n"
      "v_mfma_f32_16x16x32_bf16 v[136:139], a[100:103], v[68:71], v[136:139]\n"
      "s_waitcnt lgkmcnt(3)\n"
      "v_mfma_f32_16x16x32_bf16 v[136:139], a[104:107], v[84:87], v[136:139]\n"
      "global_store_dwordx4 v247, v[216:219], s[54:55] offset:0 sc1\n"
      "v_mfma_f32_16x16x32_bf16 v[136:139], a[108:111], v[88:91], v[136:139]\n"
      "s_add_u32 s52, s42, s36\n"
      "s_addc_u32 s53, s43, 0\n"
      "v_mfma_f32_16x16x32_bf16 v[136:139], a[112:115], v[92:95], v[136:139]\n"
      "s_add_u32 m0, s17, 0x6000\n"
      "v_mfma_f32_16x16x32_bf16 v[136:139], a[116:119], v[108:111], v[136:139]\n"
      "ds_write_b64 v244, v[208:209] offset:12288\n"
      "v_mfma_f32_16x16x32_bf16 v[136:139], a[120:123], v[112:115], v[136:139]\n"
      "global_load_lds_dwordx4 v236, s[52:53] offset:0 nt\n"
      "v_mfma_f32_16x16x32_bf16 v[136:139], a[124:127], v[116:119], v[136:139]\n"
      "v_mfma_f32_16x16x32_bf16 v[132:135], a[128:131], v[36:39], v[8:11]\n"
      "v_mfma_f32_16x16x32_bf16 v[132:135], a[132:135], v[40:43], v[132:135]\n"
      "v_mfma_f32_16x16x32_bf16 v[132:135], a[136:139], v[44:47], v[132:135]\n"
      "v_mfma_f32_16x16x32_bf16 v[132:135], a[140:143], v[60:63], v[132:135]\n"
      "v_cvt_pk_bf16_f32 v210, v136, v137\n"
      "v_mfma_f32_16x16x32_bf16 v[132:135], a[144:147], v[64:67], v[132:135]\n"
      "v_cvt_pk_bf16_f32 v211, v138, v139\n"
      "v_mfma_f32_16x16x32_bf16 v[132:135], a[148:151], v[68:71], v[132:135]\n"
      "v_mfma_f32_16x16x32_bf16 v[132:135], a[152:155], v[84:87], v[132:135]\n"
      "s_waitcnt lgkmcnt(3)\n"
      "v_mfma_f32_16x16x32_bf16 v[132:135], a[156:159], v[88:91], v[132:135]\n"
      "global_store_dwordx4 v252, v[220:223], s[54:55] offset:0 sc1\n"
      "v_mfma_f32_16x16x32_bf16 v[132:135], a[160:163], v[92:95], v[132:135]\n"
      "s_add_u32 m0, s17, 0x7f00\n"
      "v_mfma_f32_16x16x32_bf16 v[132:135], a[164:167], v[108:111], v[132:135]\n"
      "ds_write_b64 v244, v[210:211] offset:12296\n"
      "v_mfma_f32_16x16x32_bf16 v[132:135], a[168:171], v[112:115], v[132:135]\n"
      "global_load_lds_dwordx4 v236, s[52:53] offset:256 nt\n"
      "v_mfma_f32_16x16x32_bf16 v[132:135], a[172:175], v[116:119], v[132:135]\n"
      "v_mfma_f32_16x16x32_bf16 v[136:139], a[176:179], v[36:39], v[12:15]\n"
      "v_mfma_f32_16x16x32_bf16 v[136:139], a[180:183], v[40:43], v[136:139]\n"
      "v_mfma_f32_16x16x32_bf16 v[136:139], a[184:187], v[44:47], v[136:139]\n"
      "v_mfma_f32_16x16x32_bf16 v[136:139], a[188:191], v[60:63], v[136:139]\n"
      "v_cvt_pk_bf16_f32 v212, v132, v133\n"
      "v_mfma_f32_16x16x32_bf16 v[136:139], a[192:195], v[64:67], v[136:139]\n"
      "v_cvt_pk_bf16_f32 v213, v134, v135\n"
      "v_mfma_f32_16x16x32_bf16 v[136:139], a[196:199], v[68:71], v[136:139]\n"
      "v_mfma_f32_16x16x32_bf16 v[136:139], a[200:203], v[84:87], v[136:139]\n"
      "v_mfma_f32_16x16x32_bf16 v[136:139], a[204:207], v[88:91], v[136:139]\n"
      "s_waitcnt lgkmcnt(3)\n"
      "v_mfma_f32_16x16x32_bf16 v[136:139], a[208:211], v[92:95], v[136:139]\n"
      "global_store_dwordx4 v253, v[224:227], s[54:55] offset:0 sc1\n"
      "v_mfma_f32_16x16x32_bf16 v[136:139], a[212:215], v[108:111], v[136:139]\n"
      "ds_write_b64 v245, v[212:213] offset:12288\n"
      "v_mfma_f32_16x16x32_bf16 v[136:139], a[216:219], v[112:115], v[136:139]\n"
      "s_add_u32 m0, s17, 0x9e00\n"
      "v_mfma_f32_16x16x32_bf16 v[136:139], a[220:223], v[116:119], v[136:139]\n"
      "global_load_lds_dwordx4 v236, s[52:53] offset:512 nt\n"
      "v_mfma_f32_16x16x32_bf16 v[132:135], a[224:227], v[36:39], v[16:19]\n"
      "v_mfma_f32_16x16x32_bf16 v[132:135], a[228:231], v[40:43], v[132:135]\n"
      "v_mfma_f32_16x16x32_bf16 v[132:135], a[232:235], v[44:47], v[132:135]\n"
      "v_mfma_f32_16x16x32_bf16 v[132:135], a[236:239], v[60:63], v[132:135]\n"
      "v_cvt_pk_bf16_f32 v214, v136, v137\n"
      "v_mfma_f32_16x16x32_bf16 v[132:135], a[0:3], v[64:67], v[132:135]\n"
      "v_cvt_pk_bf16_f32 v215, v138, v139\n"
      "v_mfma_f32_16x16x32_bf16 v[132:135], a[4:7], v[68:71], v[132:135]\n"
      "v_mfma_f32_16x16x32_bf16 v[132:135], a[8:11], v[84:87], v[132:135]\n"
      "v_mfma_f32_16x16x32_bf16 v[132:135], a[12:15], v[88:91], v[132:135]\n"
      "v_mfma_f32_16x16x32_bf16 v[132:135], a[16:19], v[92:95], v[132:135]\n"
      "v_mfma_f32_16x16x32_bf16 v[132:135], a[20:23], v[108:111], v[132:135]\n"
      "ds_write_b64 v245, v[214:215] offset:12296\n"
      "v_mfma_f32_16x16x32_bf16 v[132:135], v[140:143], v[112:115], v[132:135]\n"
      "v_mfma_f32_16x16x32_bf16 v[132:135], v[156:159], v[116:119], v[132:135]\n"
      "s_waitcnt lgkmcnt(4)\n"
      "s_barrier\n"
      "v_mfma_f32_16x16x32_bf16 v[136:139], v[160:163], v[36:39], v[20:23]\n"
      "ds_read_b128 v[36:39], v232 offset:256\n"
      "v_mfma_f32_16x16x32_bf16 v[136:139], v[164:167], v[40:43], v[136:139]\n"
      "ds_read_b128 v[40:43], v233 offset:256\n"
      "v_mfma_f32_16x16x32_bf16 v[136:139], v[180:183], v[44:47], v[136:139]\n"
      "ds_read_b128 v[44:47], v234 offset:256\n"
      "v_mfma_f32_16x16x32_bf16 v[136:139], v[184:187], v[60:63], v[136:139]\n"
      "ds_read_b128 v[60:63], v235 offset:256\n"
      "v_cvt_pk_bf16_f32 v208, v132, v133\n"
      "v_mfma_f32_16x16x32_bf16 v[136:139], v[188:191], v[64:67], v[136:139]\n"
      "ds_read_b128 v[64:67], v232 offset:8448\n"
      "v_cvt_pk_bf16_f32 v209, v134, v135\n"
      "v_mfma_f32_16x16x32_bf16 v[136:139], v[204:207], v[68:71], v[136:139]\n"
      "ds_read_b128 v[68:71], v233 offset:8448\n"
      "v_mfma_f32_16x16x32_bf16 v[136:139], a[24:27], v[84:87], v[136:139]\n"
      "ds_read_b128 v[84:87], v234 offset:8448\n"
      "v_mfma_f32_16x16x32_bf16 v[136:139], a[28:31], v[88:91], v[136:139]\n"
      "ds_read_b128 v[88:91], v235 offset:8448\n"
      "v_mfma_f32_16x16x32_bf16 v[136:139], a[32:35], v[92:95], v[136:139]\n"
      "ds_read_b128 v[92:95], v232 offset:16640\n"
      "v_mfma_f32_16x16x32_bf16 v[136:139], a[36:39], v[108:111], v[136:139]\n"
      "ds_read_b128 v[108:111], v233 offset:16640\n"
      "v_mfma_f32_16x16x32_bf16 v[136:139], a[40:43], v[112:115], v[136:139]\n"
      "ds_read_b128 v[112:115], v234 offset:16640\n"
      "v_mfma_f32_16x16x32_bf16 v[136:139], a[44:47], v[116:119], v[136:139]\n"
      "ds_read_b128 v[116:119], v235 offset:16640\n"
      "s_waitcnt lgkmcnt(8)\n"
      "v_mfma_f32_16x16x32_bf16 v[132:135], a[240:243], v[36:39], v[0:3]\n"
      "ds_write_b64 v244, v[208:209] offset:12416\n"
      "v_mfma_f32_16x16x32_bf16 v[132:135], a[244:247], v[40:43], v[132:135]\n"
      "v_mfma_f32_16x16x32_bf16 v[132:135], a[248:251], v[44:47], v[132:135]\n"
      "v_mfma_f32_16x16x32_bf16 v[132:135], a[252:255], v[60:63], v[132:135]\n"
      "v_cvt_pk_bf16_f32 v210, v136, v137\n"
      "s_waitcnt lgkmcnt(5)\n"
      "v_mfma_f32_16x16x32_bf16 v[132:135], a[48:51], v[64:67], v[132:135]\n"
      "v_lshlrev_b32 v229, 8, v239\n"
      "v_mfma_f32_16x16x32_bf16 v[132:135], a[52:55], v[68:71], v[132:135]\n"
      "v_add_u32 v229, 1114112, v229\n"
      "v_mfma_f32_16x16x32_bf16 v[132:135], a[56:59], v[84:87], v[132:135]\n"
      "v_cvt_pk_bf16_f32 v211, v138, v139\n"
      "v_mfma_f32_16x16x32_bf16 v[132:135], a[60:63], v[88:91], v[132:135]\n"
      "s_mov_b64 exec, 0xffff\n"
      "global_load_dword v228, v229, s[28:29] sc1\n"
      "s_mov_b64 exec, -1\n"
      "s_nop 1\n"
      "s_waitcnt lgkmcnt(1)\n"
      "v_mfma_f32_16x16x32_bf16 v[132:135], a[64:67], v[92:95], v[132:135]\n"
      "ds_read_b128 v[216:219], v246 offset:0\n"
      "v_mfma_f32_16x16x32_bf16 v[132:135], a[68:71], v[108:111], v[132:135]\n"
      "ds_read_b128 v[220:223], v246 offset:1024\n"
      "v_mfma_f32_16x16x32_bf16 v[132:135], a[72:75], v[112:115], v[132:135]\n"
      "ds_read_b128 v[224:227], v246 offset:2048\n"
      "v_mfma_f32_16x16x32_bf16 v[132:135], a[76:79], v[116:119], v[132:135]\n"
      "ds_write_b64 v244, v[210:211] offset:12424\n"
      "v_mfma_f32_16x16x32_bf16 v[136:139], a[80:83], v[36:39], v[4:7]\n"
      "v_mfma_f32_16x16x32_bf16 v[136:139], a[84:87], v[40:43], v[136:139]\n"
      "v_mfma_f32_16x16x32_bf16 v[136:139], a[88:91], v[44:47], v[136:139]\n"
      "v_mfma_f32_16x16x32_bf16 v[136:139], a[92:95], v[60:63], v[136:139]\n"
      "v_cvt_pk_bf16_f32 v208, v132, v133\n"
      "v_mfma_f32_16x16x32_bf16 v[136:139], a[96:99], v[64:67], v[136:139]\n"
      "v_cvt_pk_bf16_f32 v209, v134, v135\n"
      "v_mfma_f32_16x16x32_bf16 v[136:139], a[100:103], v[68:71], v[136:139]\n"
      "s_waitcnt lgkmcnt(3)\n"
      "v_mfma_f32_16x16x32_bf16 v[136:139], a[104:107], v[84:87], v[136:139]\n"
      "global_store_dwordx4 v247, v[216:219], s[54:55] offset:768 sc1\n"
      "v_mfma_f32_16x16x32_bf16 v[136:139], a[108:111], v[88:91], v[136:139]\n"
      "s_add_u32 m0, s17, 0x7000\n"
      "v_mfma_f32_16x16x32_bf16 v[136:139], a[112:115], v[92:95], v[136:139]\n"
      "global_load_lds_dwordx4 v237, s[52:53] offset:0 nt\n"
      "v_mfma_f32_16x16x32_bf16 v[136:139], a[116:119], v[108:111], v[136:139]\n"
      "ds_write_b64 v244, v[208:209] offset:24576\n"
      "v_mfma_f32_16x16x32_bf16 v[136:139], a[120:123], v[112:115], v[136:139]\n"
      "v_mfma_f32_16x16x32_bf16 v[136:139], a[124:127], v[116:119], v[136:139]\n"
      "v_mfma_f32_16x16x32_bf16 v[132:135], a[128:131], v[36:39], v[8:11]\n"
      "v_mfma_f32_16x16x32_bf16 v[132:135], a[132:135], v[40:43], v[132:135]\n"
      "v_mfma_f32_16x16x32_bf16 v[132:135], a[136:139], v[44:47], v[132:135]\n"
      "v_mfma_f32_16x16x32_bf16 v[132:135], a[140:143], v[60:63], v[132:135]\n"
      "v_cvt_pk_bf16_f32 v210, v136, v137\n"
      "v_mfma_f32_16x16x32_bf16 v[132:135], a[144:147], v[64:67], v[132:135]\n"
      "v_cvt_pk_bf16_f32 v211, v138, v139\n"
      "v_mfma_f32_16x16x32_bf16 v[132:135], a[148:151], v[68:71], v[132:135]\n"
      "v_mfma_f32_16x16x32_bf16 v[132:135], a[152:155], v[84:87], v[132:135]\n"
      "s_waitcnt lgkmcnt(3)\n"
      "v_mfma_f32_16x16x32_bf16 v[132:135], a[156:159], v[88:91], v[132:135]\n"
      "global_store_dwordx4 v252, v[220:223], s[54:55] offset:768 sc1\n"
      "v_mfma_f32_16x16x32_bf16 v[132:135], a[160:163], v[92:95], v[132:135]\n"
      "s_add_u32 m0, s17, 0x8f00\n"
      "v_mfma_f32_16x16x32_bf16 v[132:135], a[164:167], v[108:111], v[132:135]\n"
      "ds_write_b64 v244, v[210:211] offset:24584\n"
      "v_mfma_f32_16x16x32_bf16 v[132:135], a[168:171], v[112:115], v[132:135]\n"
      "global_load_lds_dwordx4 v237, s[52:53] offset:256 nt\n"
      "v_mfma_f32_16x16x32_bf16 v[132:135], a[172:175], v[116:119], v[132:135]\n"
      "v_mfma_f32_16x16x32_bf16 v[136:139], a[176:179], v[36:39], v[12:15]\n"
      "v_mfma_f32_16x16x32_bf16 v[136:139], a[180:183], v[40:43], v[136:139]\n"
      "v_mfma_f32_16x16x32_bf16 v[136:139], a[184:187], v[44:47], v[136:139]\n"
      "v_mfma_f32_16x16x32_bf16 v[136:139], a[188:191], v[60:63], v[136:139]\n"
      "v_cvt_pk_bf16_f32 v212, v132, v133\n"
      "v_mfma_f32_16x16x32_bf16 v[136:139], a[192:195], v[64:67], v[136:139]\n"
      "v_cvt_pk_bf16_f32 v213, v134, v135\n"
      "v_mfma_f32_16x16x32_bf16 v[136:139], a[196:199], v[68:71], v[136:139]\n"
      "v_mfma_f32_16x16x32_bf16 v[136:139], a[200:203], v[84:87], v[136:139]\n"
      "v_mfma_f32_16x16x32_bf16 v[136:139], a[204:207], v[88:91], v[136:139]\n"
      "s_waitcnt lgkmcnt(3)\n"
      "v_mfma_f32_16x16x32_bf16 v[136:139], a[208:211], v[92:95], v[136:139]\n"
      "global_store_dwordx4 v253, v[224:227], s[54:55] offset:768 sc1\n"
      "v_mfma_f32_16x16x32_bf16 v[136:139], a[212:215], v[108:111], v[136:139]\n"
      "ds_write_b64 v245, v[212:213] offset:24576\n"
      "v_mfma_f32_16x16x32_bf16 v[136:139], a[216:219], v[112:115], v[136:139]\n"
      "s_add_u32 m0, s17, 0xae00\n"
      "v_mfma_f32_16x16x32_bf16 v[136:139], a[220:223], v[116:119], v[136:139]\n"
      "global_load_lds_dwordx4 v237, s[52:53] offset:512 nt\n"
      "v_mfma_f32_16x16x32_bf16 v[132:135], a[224:227], v[36:39], v[16:19]\n"
      "v_mfma_f32_16x16x32_bf16 v[132:135], a[228:231], v[40:43], v[132:135]\n"
      "v_mfma_f32_16x16x32_bf16 v[132:135], a[232:235], v[44:47], v[132:135]\n"
      "v_mfma_f32_16x16x32_bf16 v[132:135], a[236:239], v[60:63], v[132:135]\n"
      "v_cvt_pk_bf16_f32 v214, v136, v137\n"
      "v_mfma_f32_16x16x32_bf16 v[132:135], a[0:3], v[64:67], v[132:135]\n"
      "v_cvt_pk_bf16_f32 v215, v138, v139\n"
      "v_mfma_f32_16x16x32_bf16 v[132:135], a[4:7], v[68:71], v[132:135]\n"
      "v_mfma_f32_16x16x32_bf16 v[132:135], a[8:11], v[84:87], v[132:135]\n"
      "v_mfma_f32_16x16x32_bf16 v[132:135], a[12:15], v[88:91], v[132:135]\n"
      "v_mfma_f32_16x16x32_bf16 v[132:135], a[16:19], v[92:95], v[132:135]\n"
      "v_mfma_f32_16x16x32_bf16 v[132:135], a[20:23], v[108:111], v[132:135]\n"
      "ds_write_b64 v245, v[214:215] offset:24584\n"
      "v_mfma_f32_16x16x32_bf16 v[132:135], v[140:143], v[112:115], v[132:135]\n"
      "s_lshl_b32 s79, s92, 1\n"
      "v_mfma_f32_16x16x32_bf16 v[132:135], v[156:159], v[116:119], v[132:135]\n"
      "s_waitcnt vmcnt(25) lgkmcnt(4)\n"
      "s_barrier\n"
      "v_mad_i32_i24 v252, v251, s79, v252\n"
      "v_mfma_f32_16x16x32_bf16 v[136:139], v[160:163], v[36:39], v[20:23]\n"
      "ds_read_b128 v[36:39], v232 offset:24576\n"
      "s_mul_i32 s79, s79, s100\n"
      "v_mfma_f32_16x16x32_bf16 v[136:139], v[164:167], v[40:43], v[136:139]\n"
      "ds_read_b128 v[40:43], v233 offset:24576\n"
      "v_add_u32 v247, s79, v247\n"
      "v_mfma_f32_16x16x32_bf16 v[136:139], v[180:183], v[44:47], v[136:139]\n"
      "ds_read_b128 v[44:47], v234 offset:24576\n"
      "s_mul_i32 s79, s79, s101\n"
      "v_mfma_f32_16x16x32_bf16 v[136:139], v[184:187], v[60:63], v[136:139]\n"
      "ds_read_b128 v[60:63], v235 offset:24576\n"
      "v_cvt_pk_bf16_f32 v208, v132, v133\n"
      "v_mfma_f32_16x16x32_bf16 v[136:139], v[188:191], v[64:67], v[136:139]\n"
      "ds_read_b128 v[64:67], v232 offset:32768\n"
      "v_cvt_pk_bf16_f32 v209, v134, v135\n"
      "v_mfma_f32_16x16x32_bf16 v[136:139], v[204:207], v[68:71], v[136:139]\n"
      "ds_read_b128 v[68:71], v233 offset:32768\n"
      "v_add_u32 v253, s79, v253\n"
      "v_mfma_f32_16x16x32_bf16 v[136:139], a[24:27], v[84:87], v[136:139]\n"
      "ds_read_b128 v[84:87], v234 offset:32768\n"
      "v_mfma_f32_16x16x32_bf16 v[136:139], a[28:31], v[88:91], v[136:139]\n"
      "ds_read_b128 v[88:91], v235 offset:32768\n"
      "v_mfma_f32_16x16x32_bf16 v[136:139], a[32:35], v[92:95], v[136:139]\n"
      "ds_read_b128 v[92:95], v232 offset:40960\n"
      "v_mfma_f32_16x16x32_bf16 v[136:139], a[36:39], v[108:111], v[136:139]\n"
      "ds_read_b128 v[108:111], v233 offset:40960\n"
      "v_mfma_f32_16x16x32_bf16 v[136:139], a[40:43], v[112:115], v[136:139]\n"
      "ds_read_b128 v[112:115], v234 offset:40960\n"
      "v_mfma_f32_16x16x32_bf16 v[136:139], a[44:47], v[116:119], v[136:139]\n"
      "ds_read_b128 v[116:119], v235 offset:40960\n"
      "s_waitcnt lgkmcnt(8)\n"
      "v_mfma_f32_16x16x32_bf16 v[132:135], a[240:243], v[36:39], v[0:3]\n"
      "ds_write_b64 v244, v[208:209] offset:24704\n"
      "v_mfma_f32_16x16x32_bf16 v[132:135], a[244:247], v[40:43], v[132:135]\n"
      "s_add_u32 s54, s30, s35\n"
      "s_addc_u32 s55, s31, 0\n"
      "v_mfma_f32_16x16x32_bf16 v[132:135], a[248:251], v[44:47], v[132:135]\n"
      "v_mfma_f32_16x16x32_bf16 v[132:135], a[252:255], v[60:63], v[132:135]\n"
      "v_cvt_pk_bf16_f32 v210, v136, v137\n"
      "s_waitcnt lgkmcnt(5)\n"
      "v_mfma_f32_16x16x32_bf16 v[132:135], a[48:51], v[64:67], v[132:135]\n"
      "v_cvt_pk_bf16_f32 v211, v138, v139\n"
      "v_mfma_f32_16x16x32_bf16 v[132:135], a[52:55], v[68:71], v[132:135]\n"
      "s_mul_i32 s78, s94, s19\n"
      "v_mfma_f32_16x16x32_bf16 v[132:135], a[56:59], v[84:87], v[132:135]\n"
      "ds_read_b128 v[216:219], v246 offset:12288\n"
      "v_add_u32 v236, s78, v236\n"
      "v_mfma_f32_16x16x32_bf16 v[132:135], a[60:63], v[88:91], v[132:135]\n"
      "ds_read_b128 v[220:223], v246 offset:13312\n"
      "s_waitcnt lgkmcnt(3)\n"
      "v_mfma_f32_16x16x32_bf16 v[132:135], a[64:67], v[92:95], v[132:135]\n"
      "ds_read_b128 v[224:227], v246 offset:14336\n"
      "v_mfma_f32_16x16x32_bf16 v[132:135], a[68:71], v[108:111], v[132:135]\n"
      "ds_write_b64 v244, v[210:211] offset:24712\n"
      "v_mfma_f32_16x16x32_bf16 v[132:135], a[72:75], v[112:115], v[132:135]\n"
      "s_mul_i32 s78, s78, s99\n"
      "v_mfma_f32_16x16x32_bf16 v[132:135], a[76:79], v[116:119], v[132:135]\n"
      "v_add_u32 v237, s78, v237\n"
      "v_mfma_f32_16x16x32_bf16 v[136:139], a[80:83], v[36:39], v[4:7]\n"
      "v_mfma_f32_16x16x32_bf16 v[136:139], a[84:87], v[40:43], v[136:139]\n"
      "v_mfma_f32_16x16x32_bf16 v[136:139], a[88:91], v[44:47], v[136:139]\n"
      "v_mfma_f32_16x16x32_bf16 v[136:139], a[92:95], v[60:63], v[136:139]\n"
      "v_cvt_pk_bf16_f32 v208, v132, v133\n"
      "v_mfma_f32_16x16x32_bf16 v[136:139], a[96:99], v[64:67], v[136:139]\n"
      "v_cvt_pk_bf16_f32 v209, v134, v135\n"
      "v_mfma_f32_16x16x32_bf16 v[136:139], a[100:103], v[68:71], v[136:139]\n"
      "s_waitcnt lgkmcnt(3)\n"
      "v_mfma_f32_16x16x32_bf16 v[136:139], a[104:107], v[84:87], v[136:139]\n"
      "global_store_dwordx4 v247, v[216:219], s[54:55] offset:0 sc1\n"
      "v_mfma_f32_16x16x32_bf16 v[136:139], a[108:111], v[88:91], v[136:139]\n"
      "s_add_u32 s52, s20, s37\n"
      "s_addc_u32 s53, s21, 0\n"
      "v_mfma_f32_16x16x32_bf16 v[136:139], a[112:115], v[92:95], v[136:139]\n"
      "s_add_u32 m0, s17, 0xc000\n"
      "v_mfma_f32_16x16x32_bf16 v[136:139], a[116:119], v[108:111], v[136:139]\n"
      "ds_write_b64 v244, v[208:209] offset:0\n"
      "v_mfma_f32_16x16x32_bf16 v[136:139], a[120:123], v[112:115], v[136:139]\n"
      "global_load_lds_dwordx4 v236, s[52:53] offset:0 nt\n"
      "v_mfma_f32_16x16x32_bf16 v[136:139], a[124:127], v[116:119], v[136:139]\n"
      "v_mfma_f32_16x16x32_bf16 v[132:135], a[128:131], v[36:39], v[8:11]\n"
      "v_mfma_f32_16x16x32_bf16 v[132:135], a[132:135], v[40:43], v[132:135]\n"
      "v_mfma_f32_16x16x32_bf16 v[132:135], a[136:139], v[44:47], v[132:135]\n"
      "v_mfma_f32_16x16x32_bf16 v[132:135], a[140:143], v[60:63], v[132:135]\n"
      "v_cvt_pk_bf16_f32 v210, v136, v137\n"
      "v_mfma_f32_16x16x32_bf16 v[132:135], a[144:147], v[64:67], v[132:135]\n"
      "v_cvt_pk_bf16_f32 v211, v138, v139\n"
      "v_mfma_f32_16x16x32_bf16 v[132:135], a[148:151], v[68:71], v[132:135]\n"
      "v_mfma_f32_16x16x32_bf16 v[132:135], a[152:155], v[84:87], v[132:135]\n"
      "s_waitcnt lgkmcnt(3)\n"
      "v_mfma_f32_16x16x32_bf16 v[132:135], a[156:159], v[88:91], v[132:135]\n"
      "global_store_dwordx4 v252, v[220:223], s[54:55] offset:0 sc1\n"
      "v_mfma_f32_16x16x32_bf16 v[132:135], a[160:163], v[92:95], v[132:135]\n"
      "s_add_u32 m0, s17, 0xdf00\n"
      "v_mfma_f32_16x16x32_bf16 v[132:135], a[164:167], v[108:111], v[132:135]\n"
      "ds_write_b64 v244, v[210:211] offset:8\n"
      "v_mfma_f32_16x16x32_bf16 v[132:135], a[168:171], v[112:115], v[132:135]\n"
      "global_load_lds_dwordx4 v236, s[52:53] offset:256 nt\n"
      "v_mfma_f32_16x16x32_bf16 v[132:135], a[172:175], v[116:119], v[132:135]\n"
      "v_mfma_f32_16x16x32_bf16 v[136:139], a[176:179], v[36:39], v[12:15]\n"
      "v_mfma_f32_16x16x32_bf16 v[136:139], a[180:183], v[40:43], v[136:139]\n"
      "v_mfma_f32_16x16x32_bf16 v[136:139], a[184:187], v[44:47], v[136:139]\n"
      "v_mfma_f32_16x16x32_bf16 v[136:139], a[188:191], v[60:63], v[136:139]\n"
      "v_cvt_pk_bf16_f32 v212, v132, v133\n"
      "v_mfma_f32_16x16x32_bf16 v[136:139], a[192:195], v[64:67], v[136:139]\n"
      "v_cvt_pk_bf16_f32 v213, v134, v135\n"
      "v_mfma_f32_16x16x32_bf16 v[136:139], a[196:199], v[68:71], v[136:139]\n"
      "v_mfma_f32_16x16x32_bf16 v[136:139], a[200:203], v[84:87], v[136:139]\n"
      "v_mfma_f32_16x16x32_bf16 v[136:139], a[204:207], v[88:91], v[136:139]\n"
      "s_waitcnt lgkmcnt(3)\n"
      "v_mfma_f32_16x16x32_bf16 v[136:139], a[208:211], v[92:95], v[136:139]\n"
      "global_store_dwordx4 v253, v[224:227], s[54:55] offset:0 sc1\n"
      "v_mfma_f32_16x16x32_bf16 v[136:139], a[212:215], v[108:111], v[136:139]\n"
      "ds_write_b64 v245, v[212:213] offset:0\n"
      "v_mfma_f32_16x16x32_bf16 v[136:139], a[216:219], v[112:115], v[136:139]\n"
      "s_add_u32 m0, s17, 0xfe00\n"
      "v_mfma_f32_16x16x32_bf16 v[136:139], a[220:223], v[116:119], v[136:139]\n"
      "global_load_lds_dwordx4 v236, s[52:53] offset:512 nt\n"
      "v_mfma_f32_16x16x32_bf16 v[132:135], a[224:227], v[36:39], v[16:19]\n"
      "v_mfma_f32_16x16x32_bf16 v[132:135], a[228:231], v[40:43], v[132:135]\n"
      "v_mfma_f32_16x16x32_bf16 v[132:135], a[232:235], v[44:47], v[132:135]\n"
      "v_mfma_f32_16x16x32_bf16 v[132:135], a[236:239], v[60:63], v[132:135]\n"
      "v_cvt_pk_bf16_f32 v214, v136, v137\n"
      "v_mfma_f32_16x16x32_bf16 v[132:135], a[0:3], v[64:67], v[132:135]\n"
      "v_cvt_pk_bf16_f32 v215, v138, v139\n"
      "v_mfma_f32_16x16x32_bf16 v[132:135], a[4:7], v[68:71], v[132:135]\n"
      "v_mfma_f32_16x16x32_bf16 v[132:135], a[8:11], v[84:87], v[132:135]\n"
      "v_mfma_f32_16x16x32_bf16 v[132:135], a[12:15], v[88:91], v[132:135]\n"
      "v_mfma_f32_16x16x32_bf16 v[132:135], a[16:19], v[92:95], v[132:135]\n"
      "v_mfma_f32_16x16x32_bf16 v[132:135], a[20:23], v[108:111], v[132:135]\n"
      "ds_write_b64 v245, v[214:215] offset:8\n"
      "v_mfma_f32_16x16x32_bf16 v[132:135], v[140:143], v[112:115], v[132:135]\n"
      "v_mfma_f32_16x16x32_bf16 v[132:135], v[156:159], v[116:119], v[132:135]\n"
      "s_waitcnt lgkmcnt(4)\n"
      "s_barrier\n"
      "v_mfma_f32_16x16x32_bf16 v[136:139], v[160:163], v[36:39], v[20:23]\n"
      "ds_read_b128 v[36:39], v232 offset:24832\n"
      "v_mfma_f32_16x16x32_bf16 v[136:139], v[164:167], v[40:43], v[136:139]\n"
      "ds_read_b128 v[40:43], v233 offset:24832\n"
      "v_mfma_f32_16x16x32_bf16 v[136:139], v[180:183], v[44:47], v[136:139]\n"
      "ds_read_b128 v[44:47], v234 offset:24832\n"
      "v_mfma_f32_16x16x32_bf16 v[136:139], v[184:187], v[60:63], v[136:139]\n"
      "ds_read_b128 v[60:63], v235 offset:24832\n"
      "v_cvt_pk_bf16_f32 v208, v132, v133\n"
      "v_mfma_f32_16x16x32_bf16 v[136:139], v[188:191], v[64:67], v[136:139]\n"
      "ds_read_b128 v[64:67], v232 offset:33024\n"
      "v_cvt_pk_bf16_f32 v209, v134, v135\n"
      "v_mfma_f32_16x16x32_bf16 v[136:139], v[204:207], v[68:71], v[136:139]\n"
      "ds_read_b128 v[68:71], v233 offset:33024\n"
      "v_mfma_f32_16x16x32_bf16 v[136:139], a[24:27], v[84:87], v[136:139]\n"
      "ds_read_b128 v[84:87], v234 offset:33024\n"
      "v_mfma_f32_16x16x32_bf16 v[136:139], a[28:31], v[88:91], v[136:139]\n"
      "ds_read_b128 v[88:91], v235 offset:33024\n"
      "v_mfma_f32_16x16x32_bf16 v[136:139], a[32:35], v[92:95], v[136:139]\n"
      "ds_read_b128 v[92:95], v232 offset:41216\n"
      "v_mfma_f32_16x16x32_bf16 v[136:139], a[36:39], v[108:111], v[136:139]\n"
      "ds_read_b128 v[108:111], v233 offset:41216\n"
      "ds_write_b64 v244, v[208:209] offset:128\n"
      "v_mfma_f32_16x16x32_bf16 v[136:139], a[40:43], v[112:115], v[136:139]\n"
      "LPD11:\n"
      "s_cbranch_execz LPD14\n"
      "ds_read_b128 v[112:115], v234 offset:41216\n"
      "v_mfma_f32_16x16x32_bf16 v[136:139], a[44:47], v[116:119], v[136:139]\n"
      "ds_read_b128 v[116:119], v235 offset:41216\n"
      "s_waitcnt lgkmcnt(9)\n"
      "v_mfma_f32_16x16x32_bf16 v[132:135], a[240:243], v[36:39], v[0:3]\n"
      "v_add_u32 v232, 0xffff4000, v232\n"
      "v_mfma_f32_16x16x32_bf16 v[132:135], a[244:247], v[40:43], v[132:135]\n"
      "v_add_u32 v233, 0xffff4000, v233\n"
      "v_mfma_f32_16x16x32_bf16 v[132:135], a[248:251], v[44:47], v[132:135]\n"
      "v_add_u32 v234, 0xffff4000, v234\n"
      "v_mfma_f32_16x16x32_bf16 v[132:135], a[252:255], v[60:63], v[132:135]\n"
      "v_add_u32 v235, 0xffff4000, v235\n"
      "s_waitcnt lgkmcnt(5)\n"
      "v_mfma_f32_16x16x32_bf16 v[132:135], a[48:51], v[64:67], v[132:135]\n"
      "v_cvt_pk_bf16_f32 v210, v136, v137\n"
      "v_mfma_f32_16x16x32_bf16 v[132:135], a[52:55], v[68:71], v[132:135]\n"
      "v_cvt_pk_bf16_f32 v211, v138, v139\n"
      "v_mfma_f32_16x16x32_bf16 v[132:135], a[56:59], v[84:87], v[132:135]\n"
      "ds_read_b128 v[216:219], v246 offset:24576\n"
      "v_mfma_f32_16x16x32_bf16 v[132:135], a[60:63], v[88:91], v[132:135]\n"
      "ds_read_b128 v[220:223], v246 offset:25600\n"
      "s_waitcnt lgkmcnt(2)\n"
      "v_mfma_f32_16x16x32_bf16 v[132:135], a[64:67], v[92:95], v[132:135]\n"
      "ds_read_b128 v[224:227], v246 offset:26624\n"
      "v_mfma_f32_16x16x32_bf16 v[132:135], a[68:71], v[108:111], v[132:135]\n"
      "v_lshlrev_b32 v0, 16, v96\n"
      "v_mfma_f32_16x16x32_bf16 v[132:135], a[72:75], v[112:115], v[132:135]\n"
      "ds_write_b64 v244, v[210:211] offset:136\n"
      "v_mfma_f32_16x16x32_bf16 v[132:135], a[76:79], v[116:119], v[132:135]\n"
      "v_and_b32 v1, 0xffff0000, v96\n"
      "v_mfma_f32_16x16x32_bf16 v[136:139], a[80:83], v[36:39], v[4:7]\n"
      "v_lshlrev_b32 v2, 16, v97\n"
      "v_mfma_f32_16x16x32_bf16 v[136:139], a[84:87], v[40:43], v[136:139]\n"
      "v_and_b32 v3, 0xffff0000, v97\n"
      "v_mfma_f32_16x16x32_bf16 v[136:139], a[88:91], v[44:47], v[136:139]\n"
      "s_waitcnt vmcnt(12)\n"
      "s_sub_u32 s49, s48, 1000\n"
      "v_mfma_f32_16x16x32_bf16 v[136:139], a[92:95], v[60:63], v[136:139]\n"
      "v_mov_b32 v230, 1100\n"
      "v_cvt_pk_bf16_f32 v209, v134, v135\n"
      "v_mfma_f32_16x16x32_bf16 v[136:139], a[96:99], v[64:67], v[136:139]\n"
      "v_xor_b32 v228, s80, v228\n"
      "v_mfma_f32_16x16x32_bf16 v[136:139], a[100:103], v[68:71], v[136:139]\n"
      "v_subrev_u32 v228, s49, v228\n"
      "v_mfma_f32_16x16x32_bf16 v[136:139], a[104:107], v[84:87], v[136:139]\n"
      "v_mov_b32 v231, 2000\n"
      "v_mfma_f32_16x16x32_bf16 v[136:139], a[108:111], v[88:91], v[136:139]\n"
      "v_cmp_gt_u32 s[58:59], v231, v228\n"
      "v_mfma_f32_16x16x32_bf16 v[136:139], a[112:115], v[92:95], v[136:139]\n"
      "v_cndmask_b32 v228, v230, v228, s[58:59]\n"
      "v_mfma_f32_16x16x32_bf16 v[136:139], a[116:119], v[108:111], v[136:139]\n"
      "v_cvt_pk_bf16_f32 v208, v132, v133\n"
      "v_mfma_f32_16x16x32_bf16 v[136:139], a[120:123], v[112:115], v[136:139]\n"
      "v_add_u32_dpp v228, v228, v228 row_shr:1 row_mask:0xf bank_mask:0xf bound_ctrl:0\n"
      "v_mfma_f32_16x16x32_bf16 v[136:139], a[124:127], v[116:119], v[136:139]\n"
      "s_waitcnt lgkmcnt(3)\n"
      "v_mfma_f32_16x16x32_bf16 v[132:135], a[128:131], v[36:39], v[8:11]\n"
      "v_add_u32_dpp v228, v228, v228 row_shr:2 row_mask:0xf bank_mask:0xf bound_ctrl:0\n"
      "v_mfma_f32_16x16x32_bf16 v[132:135], a[132:135], v[40:43], v[132:135]\n"
      "global_store_dwordx4 v247, v[216:219], s[54:55] offset:768 sc1\n"
      "v_mfma_f32_16x16x32_bf16 v[132:135], a[136:139], v[44:47], v[132:135]\n"
      "v_add_u32_dpp v228, v228, v228 row_shr:4 row_mask:0xf bank_mask:0xf bound_ctrl:0\n"
      "v_mfma_f32_16x16x32_bf16 v[132:135], a[140:143], v[60:63], v[132:135]\n"
      "ds_write_b64 v244, v[208:209] offset:12288\n"
      "v_mfma_f32_16x16x32_bf16 v[132:135], a[144:147], v[64:67], v[132:135]\n"
      "v_add_u32_dpp v228, v228, v228 row_shr:8 row_mask:0xf bank_mask:0xf bound_ctrl:0\n"
      "v_mfma_f32_16x16x32_bf16 v[132:135], a[148:151], v[68:71], v[132:135]\n"
      "v_cvt_pk_bf16_f32 v210, v136, v137\n"
      "v_mfma_f32_16x16x32_bf16 v[132:135], a[152:155], v[84:87], v[132:135]\n"
      "v_readlane_b32 s62, v228, 15\n"
      "v_mfma_f32_16x16x32_bf16 v[132:135], a[156:159], v[88:91], v[132:135]\n"
      "v_cvt_pk_bf16_f32 v211, v138, v139\n"
      "v_mfma_f32_16x16x32_bf16 v[132:135], a[160:163], v[92:95], v[132:135]\n"
      "s_waitcnt lgkmcnt(3)\n"
      "v_mfma_f32_16x16x32_bf16 v[132:135], a[164:167], v[108:111], v[132:135]\n"
      "s_lshr_b32 s62, s62, 4\n"
      "v_mfma_f32_16x16x32_bf16 v[132:135], a[168:171], v[112:115], v[132:135]\n"
      "s_add_u32 s62, s62, 20\n"
      "v_mfma_f32_16x16x32_bf16 v[132:135], a[172:175], v[116:119], v[132:135]\n"
      "s_add_u32 s49, s49, s62\n"
      "v_mfma_f32_16x16x32_bf16 v[136:139], a[176:179], v[36:39], v[12:15]\n"
      "s_add_u32 s85, s83, 3490\n"
      "ds_write_b64 v244, v[210:211] offset:12296\n"
      "v_mfma_f32_16x16x32_bf16 v[136:139], a[180:183], v[40:43], v[136:139]\n"
      "s_sub_u32 s85, s85, s49\n"
      "v_mfma_f32_16x16x32_bf16 v[136:139], a[184:187], v[44:47], v[136:139]\n"
      "s_max_i32 s85, s85, 0\n"
      "v_mfma_f32_16x16x32_bf16 v[136:139], a[188:191], v[60:63], v[136:139]\n"
      "s_add_u32 s49, s49, s85\n"
      "v_mfma_f32_16x16x32_bf16 v[136:139], a[192:195], v[64:67], v[136:139]\n"
      "s_add_u32 s62, s62, s85\n"
      "v_mfma_f32_16x16x32_bf16 v[136:139], a[196:199], v[68:71], v[136:139]\n"
      "v_cvt_pk_bf16_f32 v212, v132, v133\n"
      "v_mfma_f32_16x16x32_bf16 v[136:139], a[200:203], v[84:87], v[136:139]\n"
      "v_cvt_pk_bf16_f32 v213, v134, v135\n"
      "v_mfma_f32_16x16x32_bf16 v[136:139], a[204:207], v[88:91], v[136:139]\n"
      "global_store_dwordx4 v252, v[220:223], s[54:55] offset:768 sc1\n"
      "v_mfma_f32_16x16x32_bf16 v[136:139], a[208:211], v[92:95], v[136:139]\n"
      "s_waitcnt lgkmcnt(3)\n"
      "v_mfma_f32_16x16x32_bf16 v[136:139], a[212:215], v[108:111], v[136:139]\n"
      "global_store_dwordx4 v253, v[224:227], s[54:55] offset:768 sc1\n"
      "v_mfma_f32_16x16x32_bf16 v[136:139], a[216:219], v[112:115], v[136:139]\n"
      "s_add_u32 m0, s17, 0xd000\n"
      "v_mfma_f32_16x16x32_bf16 v[136:139], a[220:223], v[116:119], v[136:139]\n"
      "ds_write_b64 v245, v[212:213] offset:12288\n"
      "v_mfma_f32_16x16x32_bf16 v[132:135], a[224:227], v[36:39], v[16:19]\n"
      "global_load_lds_dwordx4 v237, s[52:53] offset:0 nt\n"
      "v_mfma_f32_16x16x32_bf16 v[132:135], a[228:231], v[40:43], v[132:135]\n"
      "v_lshlrev_b32 v4, 16, v98\n"
      "v_mfma_f32_16x16x32_bf16 v[132:135], a[232:235], v[44:47], v[132:135]\n"
      "v_and_b32 v5, 0xffff0000, v98\n"
      "v_mfma_f32_16x16x32_bf16 v[132:135], a[236:239], v[60:63], v[132:135]\n"
      "s_memrealtime s[84:85]\n"
      "v_mfma_f32_16x16x32_bf16 v[132:135], a[0:3], v[64:67], v[132:135]\n"
      "v_cvt_pk_bf16_f32 v214, v136, v137\n"
      "v_mfma_f32_16x16x32_bf16 v[132:135], a[4:7], v[68:71], v[132:135]\n"
      "v_cvt_pk_bf16_f32 v215, v138, v139\n"
      "v_mfma_f32_16x16x32_bf16 v[132:135], a[8:11], v[84:87], v[132:135]\n"
      "v_lshlrev_b32 v6, 16, v99\n"
      "v_mfma_f32_16x16x32_bf16 v[132:135], a[12:15], v[88:91], v[132:135]\n"
      "v_and_b32 v7, 0xffff0000, v99\n"
      "v_mfma_f32_16x16x32_bf16 v[132:135], a[16:19], v[92:95], v[132:135]\n"
      "v_lshlrev_b32 v8, 16, v100\n"
      "v_mfma_f32_16x16x32_bf16 v[132:135], a[20:23], v[108:111], v[132:135]\n"
      "v_and_b32 v9, 0xffff0000, v100\n"
      "v_mfma_f32_16x16x32_bf16 v[132:135], v[140:143], v[112:115], v[132:135]\n"
      "s_waitcnt lgkmcnt(0)\n"
      "s_sub_u32 s86, s49, s84\n"
      "s_mov_b32 s83, s84\n"
      "v_mfma_f32_16x16x32_bf16 v[132:135], v[156:159], v[116:119], v[132:135]\n"
      "s_cmp_gt_u32 s62, 1080\n"
      "s_cselect_b64 vcc, -1, 0\n"
      "s_barrier\n"
      "v_mfma_f32_16x16x32_bf16 v[136:139], v[160:163], v[36:39], v[20:23]\n"
      "ds_read_b128 v[36:39], v232 offset:0\n"
      "ds_write_b64 v245, v[214:215] offset:12296\n"
      "v_mfma_f32_16x16x32_bf16 v[136:139], v[164:167], v[40:43], v[136:139]\n"
      "ds_read_b128 v[40:43], v233 offset:0\n"
      "v_mfma_f32_16x16x32_bf16 v[136:139], v[180:183], v[44:47], v[136:139]\n"
      "ds_read_b128 v[44:47], v234 offset:0\n"
      "v_mfma_f32_16x16x32_bf16 v[136:139], v[184:187], v[60:63], v[136:139]\n"
      "ds_read_b128 v[60:63], v235 offset:0\n"
      "v_cvt_pk_bf16_f32 v208, v132, v133\n"
      "v_mfma_f32_16x16x32_bf16 v[136:139], v[188:191], v[64:67], v[136:139]\n"
      "ds_read_b128 v[64:67], v232 offset:8192\n"
      "v_cvt_pk_bf16_f32 v209, v134, v135\n"
      "v_mfma_f32_16x16x32_bf16 v[136:139], v[204:207], v[68:71], v[136:139]\n"
      "ds_read_b128 v[68:71], v233 offset:8192\n"
      "v_mfma_f32_16x16x32_bf16 v[136:139], a[24:27], v[84:87], v[136:139]\n"
      "ds_read_b128 v[84:87], v234 offset:8192\n"
      "v_mfma_f32_16x16x32_bf16 v[136:139], a[28:31], v[88:91], v[136:139]\n"
      "ds_read_b128 v[88:91], v235 offset:8192\n"
      "v_mfma_f32_16x16x32_bf16 v[136:139], a[32:35], v[92:95], v[136:139]\n"
      "ds_read_b128 v[92:95], v232 offset:16384\n"
      "v_mfma_f32_16x16x32_bf16 v[136:139], a[36:39], v[108:111], v[136:139]\n"
      "ds_read_b128 v[108:111], v233 offset:16384\n"
      "v_mfma_f32_16x16x32_bf16 v[136:139], a[40:43], v[112:115], v[136:139]\n"
      "ds_read_b128 v[112:115], v234 offset:16384\n"
      "v_mfma_f32_16x16x32_bf16 v[136:139], a[44:47], v[116:119], v[136:139]\n"
      "ds_read_b128 v[116:119], v235 offset:16384\n"
      "s_waitcnt lgkmcnt(8)\n"
      "v_mfma_f32_16x16x32_bf16 v[132:135], a[240:243], v[36:39], v[0:3]\n"
      "ds_write_b64 v244, v[208:209] offset:12416\n"
      "s_cbranch_vccnz LT0\n"
      "LS0:\n"
      "v_mfma_f32_16x16x32_bf16 v[132:135], a[244:247], v[40:43], v[132:135]\n"
      "s_add_u32 m0, s17, 0xef00\n"
      "v_mfma_f32_16x16x32_bf16 v[132:135], a[248:251], v[44:47], v[132:135]\n"
      "s_mul_i32 s86, s86, 146\n"
      "v_mfma_f32_16x16x32_bf16 v[132:135], a[252:255], v[60:63], v[132:135]\n"
      "global_load_lds_dwordx4 v237, s[52:53] offset:256 nt\n"
      "v_cvt_pk_bf16_f32 v210, v136, v137\n"
      "s_waitcnt lgkmcnt(5)\n"
      "v_mfma_f32_16x16x32_bf16 v[132:135], a[48:51], v[64:67], v[132:135]\n"
      "s_ashr_i32 s86, s86, 10\n"
      "v_cvt_pk_bf16_f32 v211, v138, v139\n"
      "v_mfma_f32_16x16x32_bf16 v[132:135], a[52:55], v[68:71], v[132:135]\n"
      "v_lshlrev_b32 v10, 16, v101\n"
      "v_mfma_f32_16x16x32_bf16 v[132:135], a[56:59], v[84:87], v[132:135]\n"
      "v_and_b32 v11, 0xffff0000, v101\n"
      "v_mfma_f32_16x16x32_bf16 v[132:135], a[60:63], v[88:91], v[132:135]\n"
      "v_lshlrev_b32 v12, 16, v102\n"
      "s_waitcnt lgkmcnt(1)\n"
      "v_mfma_f32_16x16x32_bf16 v[132:135], a[64:67], v[92:95], v[132:135]\n"
      "v_and_b32 v13, 0xffff0000, v102\n"
      "v_mfma_f32_16x16x32_bf16 v[132:135], a[68:71], v[108:111], v[132:135]\n"
      "v_lshlrev_b32 v14, 16, v103\n"
      "ds_write_b64 v244, v[210:211] offset:12424\n"
      "v_mfma_f32_16x16x32_bf16 v[132:135], a[72:75], v[112:115], v[132:135]\n"
      "v_and_b32 v15, 0xffff0000, v103\n"
      "v_mfma_f32_16x16x32_bf16 v[132:135], a[76:79], v[116:119], v[132:135]\n"
      "v_lshlrev_b32 v16, 16, v104\n"
      "v_mfma_f32_16x16x32_bf16 v[136:139], a[80:83], v[36:39], v[4:7]\n"
      "v_and_b32 v17, 0xffff0000, v104\n"
      "v_mfma_f32_16x16x32_bf16 v[136:139], a[84:87], v[40:43], v[136:139]\n"
      "s_add_u32 m0, s17, 0x10e00\n"
      "v_mfma_f32_16x16x32_bf16 v[136:139], a[88:91], v[44:47], v[136:139]\n"
      "global_load_lds_dwordx4 v237, s[52:53] offset:512 nt\n"
      "v_mfma_f32_16x16x32_bf16 v[136:139], a[92:95], v[60:63], v[136:139]\n"
      "v_lshlrev_b32 v18, 16, v105\n"
      "v_cvt_pk_bf16_f32 v209, v134, v135\n"
      "v_mfma_f32_16x16x32_bf16 v[136:139], a[96:99], v[64:67], v[136:139]\n"
      "v_and_b32 v19, 0xffff0000, v105\n"
      "v_mfma_f32_16x16x32_bf16 v[136:139], a[100:103], v[68:71], v[136:139]\n"
      "v_lshlrev_b32 v20, 16, v106\n"
      "v_mfma_f32_16x16x32_bf16 v[136:139], a[104:107], v[84:87], v[136:139]\n"
      "v_and_b32 v21, 0xffff0000, v106\n"
      "v_mfma_f32_16x16x32_bf16 v[136:139], a[108:111], v[88:91], v[136:139]\n"
      "v_lshlrev_b32 v22, 16, v107\n"
      "v_mfma_f32_16x16x32_bf16 v[136:139], a[112:115], v[92:95], v[136:139]\n"
      "v_and_b32 v23, 0xffff0000, v107\n"
      "v_mfma_f32_16x16x32_bf16 v[136:139], a[116:119], v[108:111], v[136:139]\n"
      "v_cvt_pk_bf16_f32 v208, v132, v133\n"
      "v_mfma_f32_16x16x32_bf16 v[136:139], a[120:123], v[112:115], v[136:139]\n"
      "s_add_u32 s54, s44, s35\n"
      "s_addc_u32 s55, s45, 0\n"
      "v_mfma_f32_16x16x32_bf16 v[136:139], a[124:127], v[116:119], v[136:139]\n"
      "ds_read_b128 v[216:219], v246 offset:0\n"
      "v_mfma_f32_16x16x32_bf16 v[132:135], a[128:131], v[36:39], v[8:11]\n"
      "ds_read_b128 v[220:223], v246 offset:1024\n"
      "s_cbranch_vccnz LT1\n"
      "LS1:\n"
      "v_mfma_f32_16x16x32_bf16 v[132:135], a[132:135], v[40:43], v[132:135]\n"
      "ds_read_b128 v[224:227], v246 offset:2048\n"
      "v_mfma_f32_16x16x32_bf16 v[132:135], a[136:139], v[44:47], v[132:135]\n"
      "ds_write_b64 v244, v[208:209] offset:24576\n"
      "v_mfma_f32_16x16x32_bf16 v[132:135], a[140:143], v[60:63], v[132:135]\n"
      "v_cvt_pk_bf16_f32 v210, v136, v137\n"
      "v_mfma_f32_16x16x32_bf16 v[132:135], a[144:147], v[64:67], v[132:135]\n"
      "v_cvt_pk_bf16_f32 v211, v138, v139\n"
      "v_mfma_f32_16x16x32_bf16 v[132:135], a[148:151], v[68:71], v[132:135]\n"
      "s_waitcnt lgkmcnt(3)\n"
      "v_mfma_f32_16x16x32_bf16 v[132:135], a[152:155], v[84:87], v[132:135]\n"
      "global_store_dwordx4 v247, v[216:219], s[54:55] offset:0 sc1\n"
      "v_mfma_f32_16x16x32_bf16 v[132:135], a[156:159], v[88:91], v[132:135]\n"
      "s_waitcnt lgkmcnt(2)\n"
      "v_mfma_f32_16x16x32_bf16 v[132:135], a[160:163], v[92:95], v[132:135]\n"
      "global_store_dwordx4 v252, v[220:223], s[54:55] offset:0 sc1\n"
      "v_mfma_f32_16x16x32_bf16 v[132:135], a[164:167], v[108:111], v[132:135]\n"
      "ds_write_b64 v244, v[210:211] offset:24584\n"
      "v_mfma_f32_16x16x32_bf16 v[132:135], a[168:171], v[112:115], v[132:135]\n"
      "s_add_u32 s52, s42, s37\n"
      "s_addc_u32 s53, s43, 0\n"
      "v_mfma_f32_16x16x32_bf16 v[132:135], a[172:175], v[116:119], v[132:135]\n"
      "s_add_u32 m0, s17, 0x12000\n"
      "v_mfma_f32_16x16x32_bf16 v[136:139], a[176:179], v[36:39], v[12:15]\n"
      "global_load_lds_dwordx4 v236, s[52:53] offset:0 nt\n"
      "v_mfma_f32_16x16x32_bf16 v[136:139], a[180:183], v[40:43], v[136:139]\n"
      "v_mfma_f32_16x16x32_bf16 v[136:139], a[184:187], v[44:47], v[136:139]\n"
      "v_mfma_f32_16x16x32_bf16 v[136:139], a[188:191], v[60:63], v[136:139]\n"
      "v_cvt_pk_bf16_f32 v212, v132, v133\n"
      "v_mfma_f32_16x16x32_bf16 v[136:139], a[192:195], v[64:67], v[136:139]\n"
      "v_cvt_pk_bf16_f32 v213, v134, v135\n"
      "v_mfma_f32_16x16x32_bf16 v[136:139], a[196:199], v[68:71], v[136:139]\n"
      "v_mfma_f32_16x16x32_bf16 v[136:139], a[200:203], v[84:87], v[136:139]\n"
      "v_mfma_f32_16x16x32_bf16 v[136:139], a[204:207], v[88:91], v[136:139]\n"
      "s_waitcnt lgkmcnt(2)\n"
      "v_mfma_f32_16x16x32_bf16 v[136:139], a[208:211], v[92:95], v[136:139]\n"
      "global_store_dwordx4 v253, v[224:227], s[54:55] offset:0 sc1\n"
      "v_mfma_f32_16x16x32_bf16 v[136:139], a[212:215], v[108:111], v[136:139]\n"
      "ds_write_b64 v245, v[212:213] offset:24576\n"
      "v_mfma_f32_16x16x32_bf16 v[136:139], a[216:219], v[112:115], v[136:139]\n"
      "s_add_u32 m0, s17, 0x13f00\n"
      "v_mfma_f32_16x16x32_bf16 v[136:139], a[220:223], v[116:119], v[136:139]\n"
      "global_load_lds_dwordx4 v236, s[52:53] offset:256 nt\n"
      "v_mfma_f32_16x16x32_bf16 v[132:135], a[224:227], v[36:39], v[16:19]\n"
      "s_cbranch_vccnz LT2\n"
      "LS2:\n"
      "v_mfma_f32_16x16x32_bf16 v[132:135], a[228:231], v[40:43], v[132:135]\n"
      "v_mfma_f32_16x16x32_bf16 v[132:135], a[232:235], v[44:47], v[132:135]\n"
      "v_mfma_f32_16x16x32_bf16 v[132:135], a[236:239], v[60:63], v[132:135]\n"
      "v_cvt_pk_bf16_f32 v214, v136, v137\n"
      "v_mfma_f32_16x16x32_bf16 v[132:135], a[0:3], v[64:67], v[132:135]\n"
      "v_cvt_pk_bf16_f32 v215, v138, v139\n"
      "v_mfma_f32_16x16x32_bf16 v[132:135], a[4:7], v[68:71], v[132:135]\n"
      "v_mfma_f32_16x16x32_bf16 v[132:135], a[8:11], v[84:87], v[132:135]\n"
      "v_mfma_f32_16x16x32_bf16 v[132:135], a[12:15], v[88:91], v[132:135]\n"
      "v_mfma_f32_16x16x32_bf16 v[132:135], a[16:19], v[92:95], v[132:135]\n"
      "v_mfma_f32_16x16x32_bf16 v[132:135], a[20:23], v[108:111], v[132:135]\n"
      "ds_write_b64 v245, v[214:215] offset:24584\n"
      "v_mfma_f32_16x16x32_bf16 v[132:135], v[140:143], v[112:115], v[132:135]\n"
      "s_add_u32 m0, s17, 0x15e00\n"
      "v_mfma_f32_16x16x32_bf16 v[132:135], v[156:159], v[116:119], v[132:135]\n"
      "s_barrier\n"
      "v_mfma_f32_16x16x32_bf16 v[136:139], v[160:163], v[36:39], v[20:23]\n"
      "ds_read_b128 v[36:39], v232 offset:256\n"
      "v_mfma_f32_16x16x32_bf16 v[136:139], v[164:167], v[40:43], v[136:139]\n"
      "ds_read_b128 v[40:43], v233 offset:256\n"
      "v_mfma_f32_16x16x32_bf16 v[136:139], v[180:183], v[44:47], v[136:139]\n"
      "ds_read_b128 v[44:47], v234 offset:256\n"
      "v_mfma_f32_16x16x32_bf16 v[136:139], v[184:187], v[60:63], v[136:139]\n"
      "ds_read_b128 v[60:63], v235 offset:256\n"
      "v_cvt_pk_bf16_f32 v208, v132, v133\n"
      "v_mfma_f32_16x16x32_bf16 v[136:139], v[188:191], v[64:67], v[136:139]\n"
      "ds_read_b128 v[64:67], v232 offset:8448\n"
      "v_cvt_pk_bf16_f32 v209, v134, v135\n"
      "v_mfma_f32_16x16x32_bf16 v[136:139], v[204:207], v[68:71], v[136:139]\n"
      "ds_read_b128 v[68:71], v233 offset:8448\n"
      "v_mfma_f32_16x16x32_bf16 v[136:139], a[24:27], v[84:87], v[136:139]\n"
      "ds_read_b128 v[84:87], v234 offset:8448\n"
      "v_mfma_f32_16x16x32_bf16 v[136:139], a[28:31], v[88:91], v[136:139]\n"
      "ds_read_b128 v[88:91], v235 offset:8448\n"
      "v_mfma_f32_16x16x32_bf16 v[136:139], a[32:35], v[92:95], v[136:139]\n"
      "ds_read_b128 v[92:95], v232 offset:16640\n"
      "v_mfma_f32_16x16x32_bf16 v[136:139], a[36:39], v[108:111], v[136:139]\n"
      "ds_read_b128 v[108:111], v233 offset:16640\n"
      "v_mfma_f32_16x16x32_bf16 v[136:139], a[40:43], v[112:115], v[136:139]\n"
      "ds_read_b128 v[112:115], v234 offset:16640\n"
      "v_mfma_f32_16x16x32_bf16 v[136:139], a[44:47], v[116:119], v[136:139]\n"
      "ds_read_b128 v[116:119], v235 offset:16640\n"
      "s_waitcnt lgkmcnt(8)\n"
      "v_mfma_f32_16x16x32_bf16 v[132:135], a[240:243], v[36:39], v[0:3]\n"
      "ds_write_b64 v244, v[208:209] offset:24704\n"
      "v_mfma_f32_16x16x32_bf16 v[132:135], a[244:247], v[40:43], v[132:135]\n"
      "global_load_lds_dwordx4 v236, s[52:53] offset:512 nt\n"
      "v_mfma_f32_16x16x32_bf16 v[132:135], a[248:251], v[44:47], v[132:135]\n"
      "s_cbranch_vccnz LT3\n"
      "LS3:\n"
      "v_mfma_f32_16x16x32_bf16 v[132:135], a[252:255], v[60:63], v[132:135]\n"
      "v_cvt_pk_bf16_f32 v210, v136, v137\n"
      "s_waitcnt lgkmcnt(5)\n"
      "v_mfma_f32_16x16x32_bf16 v[132:135], a[48:51], v[64:67], v[132:135]\n"
      "v_cvt_pk_bf16_f32 v211, v138, v139\n"
      "v_mfma_f32_16x16x32_bf16 v[132:135], a[52:55], v[68:71], v[132:135]\n"
      "v_mfma_f32_16x16x32_bf16 v[132:135], a[56:59], v[84:87], v[132:135]\n"
      "ds_read_b128 v[216:219], v246 offset:12288\n"
      "v_mfma_f32_16x16x32_bf16 v[132:135], a[60:63], v[88:91], v[132:135]\n"
      "ds_read_b128 v[220:223], v246 offset:13312\n"
      "s_waitcnt lgkmcnt(3)\n"
      "v_mfma_f32_16x16x32_bf16 v[132:135], a[64:67], v[92:95], v[132:135]\n"
      "ds_read_b128 v[224:227], v246 offset:14336\n"
      "v_mfma_f32_16x16x32_bf16 v[132:135], a[68:71], v[108:111], v[132:135]\n"
      "ds_write_b64 v244, v[210:211] offset:24712\n"
      "v_mfma_f32_16x16x32_bf16 v[132:135], a[72:75], v[112:115], v[132:135]\n"
      "v_mfma_f32_16x16x32_bf16 v[132:135], a[76:79], v[116:119], v[132:135]\n"
      "v_mfma_f32_16x16x32_bf16 v[136:139], a[80:83], v[36:39], v[4:7]\n"
      "v_mfma_f32_16x16x32_bf16 v[136:139], a[84:87], v[40:43], v[136:139]\n"
      "v_mfma_f32_16x16x32_bf16 v[136:139], a[88:91], v[44:47], v[136:139]\n"
      "v_mfma_f32_16x16x32_bf16 v[136:139], a[92:95], v[60:63], v[136:139]\n"
      "v_cvt_pk_bf16_f32 v208, v132, v133\n"
      "v_mfma_f32_16x16x32_bf16 v[136:139], a[96:99], v[64:67], v[136:139]\n"
      "v_cvt_pk_bf16_f32 v209, v134, v135\n"
      "v_mfma_f32_16x16x32_bf16 v[136:139], a[100:103], v[68:71], v[136:139]\n"
      "s_waitcnt lgkmcnt(3)\n"
      "v_mfma_f32_16x16x32_bf16 v[136:139], a[104:107], v[84:87], v[136:139]\n"
      "global_store_dwordx4 v247, v[216:219], s[54:55] offset:768 sc1\n"
      "v_mfma_f32_16x16x32_bf16 v[136:139], a[108:111], v[88:91], v[136:139]\n"
      "s_add_u32 m0, s17, 0x13000\n"
      "v_mfma_f32_16x16x32_bf16 v[136:139], a[112:115], v[92:95], v[136:139]\n"
      "global_load_lds_dwordx4 v237, s[52:53] offset:0 nt\n"
      "v_mfma_f32_16x16x32_bf16 v[136:139], a[116:119], v[108:111], v[136:139]\n"
      "ds_write_b64 v244, v[208:209] offset:0\n"
      "v_mfma_f32_16x16x32_bf16 v[136:139], a[120:123], v[112:115], v[136:139]\n"
      "v_mfma_f32_16x16x32_bf16 v[136:139], a[124:127], v[116:119], v[136:139]\n"
      "v_mfma_f32_16x16x32_bf16 v[132:135], a[128:131], v[36:39], v[8:11]\n"
      "s_cbranch_vccnz LT4\n"
      "LS4:\n"
      "v_mfma_f32_16x16x32_bf16 v[132:135], a[132:135], v[40:43], v[132:135]\n"
      "v_mfma_f32_16x16x32_bf16 v[132:135], a[136:139], v[44:47], v[132:135]\n"
      "v_mfma_f32_16x16x32_bf16 v[132:135], a[140:143], v[60:63], v[132:135]\n"
      "v_cvt_pk_bf16_f32 v210, v136, v137\n"
      "v_mfma_f32_16x16x32_bf16 v[132:135], a[144:147], v[64:67], v[132:135]\n"
      "v_cvt_pk_bf16_f32 v211, v138, v139\n"
      "v_mfma_f32_16x16x32_bf16 v[132:135], a[148:151], v[68:71], v[132:135]\n"
      "v_mfma_f32_16x16x32_bf16 v[132:135], a[152:155], v[84:87], v[132:135]\n"
      "s_waitcnt lgkmcnt(3)\n"
      "v_mfma_f32_16x16x32_bf16 v[132:135], a[156:159], v[88:91], v[132:135]\n"
      "global_store_dwordx4 v252, v[220:223], s[54:55] offset:768 sc1\n"
      "v_mfma_f32_16x16x32_bf16 v[132:135], a[160:163], v[92:95], v[132:135]\n"
      "s_add_u32 m0, s17, 0x14f00\n"
      "v_mfma_f32_16x16x32_bf16 v[132:135], a[164:167], v[108:111], v[132:135]\n"
      "ds_write_b64 v244, v[210:211] offset:8\n"
      "v_mfma_f32_16x16x32_bf16 v[132:135], a[168:171], v[112:115], v[132:135]\n"
      "global_load_lds_dwordx4 v237, s[52:53] offset:256 nt\n"
      "v_mfma_f32_16x16x32_bf16 v[132:135], a[172:175], v[116:119], v[132:135]\n"
      "v_mfma_f32_16x16x32_bf16 v[136:139], a[176:179], v[36:39], v[12:15]\n"
      "v_mfma_f32_16x16x32_bf16 v[136:139], a[180:183], v[40:43], v[136:139]\n"
      "v_mfma_f32_16x16x32_bf16 v[136:139], a[184:187], v[44:47], v[136:139]\n"
      "v_mfma_f32_16x16x32_bf16 v[136:139], a[188:191], v[60:63], v[136:139]\n"
      "v_cvt_pk_bf16_f32 v212, v132, v133\n"
      "v_mfma_f32_16x16x32_bf16 v[136:139], a[192:195], v[64:67], v[136:139]\n"
      "v_cvt_pk_bf16_f32 v213, v134, v135\n"
      "v_mfma_f32_16x16x32_bf16 v[136:139], a[196:199], v[68:71], v[136:139]\n"
      "v_mfma_f32_16x16x32_bf16 v[136:139], a[200:203], v[84:87], v[136:139]\n"
      "v_mfma_f32_16x16x32_bf16 v[136:139], a[204:207], v[88:91], v[136:139]\n"
      "s_waitcnt lgkmcnt(3)\n"
      "v_mfma_f32_16x16x32_bf16 v[136:139], a[208:211], v[92:95], v[136:139]\n"
      "global_store_dwordx4 v253, v[224:227], s[54:55] offset:768 sc1\n"
      "v_mfma_f32_16x16x32_bf16 v[136:139], a[212:215], v[108:111], v[136:139]\n"
      "ds_write_b64 v245, v[212:213] offset:0\n"
      "v_mfma_f32_16x16x32_bf16 v[136:139], a[216:219], v[112:115], v[136:139]\n"
      "s_add_u32 m0, s17, 0x16e00\n"
      "v_mfma_f32_16x16x32_bf16 v[136:139], a[220:223], v[116:119], v[136:139]\n"
      "global_load_lds_dwordx4 v237, s[52:53] offset:512 nt\n"
      "v_mfma_f32_16x16x32_bf16 v[132:135], a[224:227], v[36:39], v[16:19]\n"
      "s_cbranch_vccnz LT5\n"
      "LS5:\n"
      "v_mfma_f32_16x16x32_bf16 v[132:135], a[228:231], v[40:43], v[132:135]\n"
      "v_mfma_f32_16x16x32_bf16 v[132:135], a[232:235], v[44:47], v[132:135]\n"
      "v_mfma_f32_16x16x32_bf16 v[132:135], a[236:239], v[60:63], v[132:135]\n"
      "s_memrealtime s[84:85]\n"
      "v_mfma_f32_16x16x32_bf16 v[132:135], a[0:3], v[64:67], v[132:135]\n"
      "v_cvt_pk_bf16_f32 v214, v136, v137\n"
      "v_mfma_f32_16x16x32_bf16 v[132:135], a[4:7], v[68:71], v[132:135]\n"
      "v_cvt_pk_bf16_f32 v215, v138, v139\n"
      "v_mfma_f32_16x16x32_bf16 v[132:135], a[8:11], v[84:87], v[132:135]\n"
      "v_mfma_f32_16x16x32_bf16 v[132:135], a[12:15], v[88:91], v[132:135]\n"
      "s_add_u32 s83, s83, s86\n"
      "v_mfma_f32_16x16x32_bf16 v[132:135], a[16:19], v[92:95], v[132:135]\n"
      "v_mfma_f32_16x16x32_bf16 v[132:135], a[20:23], v[108:111], v[132:135]\n"
      "v_mfma_f32_16x16x32_bf16 v[132:135], v[140:143], v[112:115], v[132:135]\n"
      "s_waitcnt lgkmcnt(0)\n"
      "s_sub_u32 s84, s84, s83\n"
      "v_mfma_f32_16x16x32_bf16 v[132:135], v[156:159], v[116:119], v[132:135]\n"
      "s_cmp_lt_i32 s84, 0\n"
      "s_cselect_b64 vcc, -1, 0\n"
      "s_waitcnt vmcnt(24)\n"
      "s_barrier\n"
      "v_mfma_f32_16x16x32_bf16 v[136:139], v[160:163], v[36:39], v[20:23]\n"
      "ds_read_b128 v[36:39], v232 offset:24576\n"
      "ds_write_b64 v245, v[214:215] offset:8\n"
      "v_mfma_f32_16x16x32_bf16 v[136:139], v[164:167], v[40:43], v[136:139]\n"
      "ds_read_b128 v[40:43], v233 offset:24576\n"
      "s_lshl_b32 s79, s93, 1\n"
      "v_mfma_f32_16x16x32_bf16 v[136:139], v[180:183], v[44:47], v[136:139]\n"
      "ds_read_b128 v[44:47], v234 offset:24576\n"
      "v_mad_i32_i24 v252, v251, s79, v252\n"
      "v_mfma_f32_16x16x32_bf16 v[136:139], v[184:187], v[60:63], v[136:139]\n"
      "ds_read_b128 v[60:63], v235 offset:24576\n"
      "v_cvt_pk_bf16_f32 v208, v132, v133\n"
      "v_mfma_f32_16x16x32_bf16 v[136:139], v[188:191], v[64:67], v[136:139]\n"
      "ds_read_b128 v[64:67], v232 offset:32768\n"
      "v_cvt_pk_bf16_f32 v209, v134, v135\n"
      "v_mfma_f32_16x16x32_bf16 v[136:139], v[204:207], v[68:71], v[136:139]\n"
      "ds_read_b128 v[68:71], v233 offset:32768\n"
      "s_mul_i32 s79, s79, s100\n"
      "v_mfma_f32_16x16x32_bf16 v[136:139], a[24:27], v[84:87], v[136:139]\n"
      "ds_read_b128 v[84:87], v234 offset:32768\n"
      "v_add_u32 v247, s79, v247\n"
      "v_mfma_f32_16x16x32_bf16 v[136:139], a[28:31], v[88:91], v[136:139]\n"
      "ds_read_b128 v[88:91], v235 offset:32768\n"
      "s_mul_i32 s79, s79, s101\n"
      "v_mfma_f32_16x16x32_bf16 v[136:139], a[32:35], v[92:95], v[136:139]\n"
      "ds_read_b128 v[92:95], v232 offset:40960\n"
      "v_add_u32 v253, s79, v253\n"
      "v_mfma_f32_16x16x32_bf16 v[136:139], a[36:39], v[108:111], v[136:139]\n"
      "ds_read_b128 v[108:111], v233 offset:40960\n"
      "v_mfma_f32_16x16x32_bf16 v[136:139], a[40:43], v[112:115], v[136:139]\n"
      "ds_read_b128 v[112:115], v234 offset:40960\n"
      "v_mfma_f32_16x16x32_bf16 v[136:139], a[44:47], v[116:119], v[136:139]\n"
      "ds_read_b128 v[116:119], v235 offset:40960\n"
      "s_waitcnt lgkmcnt(8)\n"
      "v_mfma_f32_16x16x32_bf16 v[132:135], a[240:243], v[36:39], v[0:3]\n"
      "ds_write_b64 v244, v[208:209] offset:128\n"
      "v_mfma_f32_16x16x32_bf16 v[132:135], a[244:247], v[40:43], v[132:135]\n"
      "s_add_u32 s54, s30, s36\n"
      "s_addc_u32 s55, s31, 0\n"
      "v_mfma_f32_16x16x32_bf16 v[132:135], a[248:251], v[44:47], v[132:135]\n"
      "s_cbranch_vccnz LT6\n"
      "LS6:\n"
      "v_mfma_f32_16x16x32_bf16 v[132:135], a[252:255], v[60:63], v[132:135]\n"
      "v_cvt_pk_bf16_f32 v210, v136, v137\n"
      "s_waitcnt lgkmcnt(5)\n"
      "v_mfma_f32_16x16x32_bf16 v[132:135], a[48:51], v[64:67], v[132:135]\n"
      "v_cvt_pk_bf16_f32 v211, v138, v139\n"
      "v_mfma_f32_16x16x32_bf16 v[132:135], a[52:55], v[68:71], v[132:135]\n"
      "s_mul_i32 s78, s95, s19\n"
      "v_mfma_f32_16x16x32_bf16 v[132:135], a[56:59], v[84:87], v[132:135]\n"
      "ds_read_b128 v[216:219], v246 offset:24576\n"
      "v_add_u32 v236, s78, v236\n"
      "v_mfma_f32_16x16x32_bf16 v[132:135], a[60:63], v[88:91], v[132:135]\n"
      "ds_read_b128 v[220:223], v246 offset:25600\n"
      "s_waitcnt lgkmcnt(3)\n"
      "v_mfma_f32_16x16x32_bf16 v[132:135], a[64:67], v[92:95], v[132:135]\n"
      "ds_read_b128 v[224:227], v246 offset:26624\n"
      "v_mfma_f32_16x16x32_bf16 v[132:135], a[68:71], v[108:111], v[132:135]\n"
      "ds_write_b64 v244, v[210:211] offset:136\n"
      "v_mfma_f32_16x16x32_bf16 v[132:135], a[72:75], v[112:115], v[132:135]\n"
      "s_mul_i32 s78, s78, s99\n"
      "v_mfma_f32_16x16x32_bf16 v[132:135], a[76:79], v[116:119], v[132:135]\n"
      "v_add_u32 v237, s78, v237\n"
      "v_mfma_f32_16x16x32_bf16 v[136:139], a[80:83], v[36:39], v[4:7]\n"
      "v_mfma_f32_16x16x32_bf16 v[136:139], a[84:87], v[40:43], v[136:139]\n"
      "v_mfma_f32_16x16x32_bf16 v[136:139], a[88:91], v[44:47], v[136:139]\n"
      "v_mfma_f32_16x16x32_bf16 v[136:139], a[92:95], v[60:63], v[136:139]\n"
      "v_cvt_pk_bf16_f32 v208, v132, v133\n"
      "v_mfma_f32_16x16x32_bf16 v[136:139], a[96:99], v[64:67], v[136:139]\n"
      "v_cvt_pk_bf16_f32 v209, v134, v135\n"
      "v_mfma_f32_16x16x32_bf16 v[136:139], a[100:103], v[68:71], v[136:139]\n"
      "s_waitcnt lgkmcnt(3)\n"
      "v_mfma_f32_16x16x32_bf16 v[136:139], a[104:107], v[84:87], v[136:139]\n"
      "global_store_dwordx4 v247, v[216:219], s[54:55] offset:0 sc1\n"
      "v_mfma_f32_16x16x32_bf16 v[136:139], a[108:111], v[88:91], v[136:139]\n"
      "s_add_u32 s52, s20, s38\n"
      "s_addc_u32 s53, s21, 0\n"
      "v_mfma_f32_16x16x32_bf16 v[136:139], a[112:115], v[92:95], v[136:139]\n"
      "s_add_u32 m0, s17, 0x0\n"
      "v_mfma_f32_16x16x32_bf16 v[136:139], a[116:119], v[108:111], v[136:139]\n"
      "ds_write_b64 v244, v[208:209] offset:12288\n"
      "v_mfma_f32_16x16x32_bf16 v[136:139], a[120:123], v[112:115], v[136:139]\n"
      "global_load_lds_dwordx4 v236, s[52:53] offset:0 nt\n"
      "v_mfma_f32_16x16x32_bf16 v[136:139], a[124:127], v[116:119], v[136:139]\n"
      "v_mfma_f32_16x16x32_bf16 v[132:135], a[128:131], v[36:39], v[8:11]\n"
      "s_cbranch_vccnz LT7\n"
      "LS7:\n"
      "v_mfma_f32_16x16x32_bf16 v[132:135], a[132:135], v[40:43], v[132:135]\n"
      "v_mfma_f32_16x16x32_bf16 v[132:135], a[136:139], v[44:47], v[132:135]\n"
      "v_mfma_f32_16x16x32_bf16 v[132:135], a[140:143], v[60:63], v[132:135]\n"
      "v_cvt_pk_bf16_f32 v210, v136, v137\n"
      "v_mfma_f32_16x16x32_bf16 v[132:135], a[144:147], v[64:67], v[132:135]\n"
      "v_cvt_pk_bf16_f32 v211, v138, v139\n"
      "v_mfma_f32_16x16x32_bf16 v[132:135], a[148:151], v[68:71], v[132:135]\n"
      "v_mfma_f32_16x16x32_bf16 v[132:135], a[152:155], v[84:87], v[132:135]\n"
      "s_waitcnt lgkmcnt(3)\n"
      "v_mfma_f32_16x16x32_bf16 v[132:135], a[156:159], v[88:91], v[132:135]\n"
      "global_store_dwordx4 v252, v[220:223], s[54:55] offset:0 sc1\n"
      "v_mfma_f32_16x16x32_bf16 v[132:135], a[160:163], v[92:95], v[132:135]\n"
      "s_add_u32 m0, s17, 0x1f00\n"
      "v_mfma_f32_16x16x32_bf16 v[132:135], a[164:167], v[108:111], v[132:135]\n"
      "ds_write_b64 v244, v[210:211] offset:12296\n"
      "v_mfma_f32_16x16x32_bf16 v[132:135], a[168:171], v[112:115], v[132:135]\n"
      "global_load_lds_dwordx4 v236, s[52:53] offset:256 nt\n"
      "v_mfma_f32_16x16x32_bf16 v[132:135], a[172:175], v[116:119], v[132:135]\n"
      "v_mfma_f32_16x16x32_bf16 v[136:139], a[176:179], v[36:39], v[12:15]\n"
      "v_mfma_f32_16x16x32_bf16 v[136:139], a[180:183], v[40:43], v[136:139]\n"
      "v_mfma_f32_16x16x32_bf16 v[136:139], a[184:187], v[44:47], v[136:139]\n"
      "v_mfma_f32_16x16x32_bf16 v[136:139], a[188:191], v[60:63], v[136:139]\n"
      "v_cvt_pk_bf16_f32 v212, v132, v133\n"
      "v_mfma_f32_16x16x32_bf16 v[136:139], a[192:195], v[64:67], v[136:139]\n"
      "v_cvt_pk_bf16_f32 v213, v134, v135\n"
      "v_mfma_f32_16x16x32_bf16 v[136:139], a[196:199], v[68:71], v[136:139]\n"
      "v_mfma_f32_16x16x32_bf16 v[136:139], a[200:203], v[84:87], v[136:139]\n"
      "v_mfma_f32_16x16x32_bf16 v[136:139], a[204:207], v[88:91], v[136:139]\n"
      "s_waitcnt lgkmcnt(3)\n"
      "v_mfma_f32_16x16x32_bf16 v[136:139], a[208:211], v[92:95], v[136:139]\n"
      "global_store_dwordx4 v253, v[224:227], s[54:55] offset:0 sc1\n"
      "v_mfma_f32_16x16x32_bf16 v[136:139], a[212:215], v[108:111], v[136:139]\n"
      "ds_write_b64 v245, v[212:213] offset:12288\n"
      "v_mfma_f32_16x16x32_bf16 v[136:139], a[216:219], v[112:115], v[136:139]\n"
      "s_add_u32 m0, s17, 0x3e00\n"
      "v_mfma_f32_16x16x32_bf16 v[136:139], a[220:223], v[116:119], v[136:139]\n"
      "global_load_lds_dwordx4 v236, s[52:53] offset:512 nt\n"
      "v_mfma_f32_16x16x32_bf16 v[132:135], a[224:227], v[36:39], v[16:19]\n"
      "s_cbranch_vccnz LT8\n"
      "LS8:\n"
      "v_mfma_f32_16x16x32_bf16 v[132:135], a[228:231], v[40:43], v[132:135]\n"
      "v_mfma_f32_16x16x32_bf16 v[132:135], a[232:235], v[44:47], v[132:135]\n"
      "v_mfma_f32_16x16x32_bf16 v[132:135], a[236:239], v[60:63], v[132:135]\n"
      "v_cvt_pk_bf16_f32 v214, v136, v137\n"
      "v_mfma_f32_16x16x32_bf16 v[132:135], a[0:3], v[64:67], v[132:135]\n"
      "v_cvt_pk_bf16_f32 v215, v138, v139\n"
      "v_mfma_f32_16x16x32_bf16 v[132:135], a[4:7], v[68:71], v[132:135]\n"
      "v_mfma_f32_16x16x32_bf16 v[132:135], a[8:11], v[84:87], v[132:135]\n"
      "v_mfma_f32_16x16x32_bf16 v[132:135], a[12:15], v[88:91], v[132:135]\n"
      "v_mfma_f32_16x16x32_bf16 v[132:135], a[16:19], v[92:95], v[132:135]\n"
      "v_mfma_f32_16x16x32_bf16 v[132:135], a[20:23], v[108:111], v[132:135]\n"
      "ds_write_b64 v245, v[214:215] offset:12296\n"
      "v_mfma_f32_16x16x32_bf16 v[132:135], v[140:143], v[112:115], v[132:135]\n"
      "v_mfma_f32_16x16x32_bf16 v[132:135], v[156:159], v[116:119], v[132:135]\n"
      "s_waitcnt lgkmcnt(4)\n"
      "s_barrier\n"
      "v_mfma_f32_16x16x32_bf16 v[136:139], v[160:163], v[36:39], v[20:23]\n"
      "ds_read_b128 v[36:39], v232 offset:24832\n"
      "v_mfma_f32_16x16x32_bf16 v[136:139], v[164:167], v[40:43], v[136:139]\n"
      "ds_read_b128 v[40:43], v233 offset:24832\n"
      "LPD12:\n"
      "s_cbranch_execz LPD15\n"
      "v_mfma_f32_16x16x32_bf16 v[136:139], v[180:183], v[44:47], v[136:139]\n"
      "ds_read_b128 v[44:47], v234 offset:24832\n"
      "v_mfma_f32_16x16x32_bf16 v[136:139], v[184:187], v[60:63], v[136:139]\n"
      "ds_read_b128 v[60:63], v235 offset:24832\n"
      "v_cvt_pk_bf16_f32 v208, v132, v133\n"
      "v_mfma_f32_16x16x32_bf16 v[136:139], v[188:191], v[64:67], v[136:139]\n"
      "ds_read_b128 v[64:67], v232 offset:33024\n"
      "v_cvt_pk_bf16_f32 v209, v134, v135\n"
      "v_mfma_f32_16x16x32_bf16 v[136:139], v[204:207], v[68:71], v[136:139]\n"
      "ds_read_b128 v[68:71], v233 offset:33024\n"
      "v_mfma_f32_16x16x32_bf16 v[136:139], a[24:27], v[84:87], v[136:139]\n"
      "ds_read_b128 v[84:87], v234 offset:33024\n"
      "v_mfma_f32_16x16x32_bf16 v[136:139], a[28:31], v[88:91], v[136:139]\n"
      "ds_read_b128 v[88:91], v235 offset:33024\n"
      "v_mfma_f32_16x16x32_bf16 v[136:139], a[32:35], v[92:95], v[136:139]\n"
      "ds_read_b128 v[92:95], v232 offset:41216\n"
      "v_mfma_f32_16x16x32_bf16 v[136:139], a[36:39], v[108:111], v[136:139]\n"
      "ds_read_b128 v[108:111], v233 offset:41216\n"
      "ds_write_b64 v244, v[208:209] offset:12416\n"
      "v_mfma_f32_16x16x32_bf16 v[136:139], a[40:43], v[112:115], v[136:139]\n"
      "ds_read_b128 v[112:115], v234 offset:41216\n"
      "v_mfma_f32_16x16x32_bf16 v[136:139], a[44:47], v[116:119], v[136:139]\n"
      "ds_read_b128 v[116:119], v235 offset:41216\n"
      "s_waitcnt lgkmcnt(9)\n"
      "v_mfma_f32_16x16x32_bf16 v[132:135], a[240:243], v[36:39], v[0:3]\n"
      "v_add_u32 v232, 0xc000, v232\n"
      "s_cbranch_vccnz LT9\n"
      "LS9:\n"
      "v_mfma_f32_16x16x32_bf16 v[132:135], a[244:247], v[40:43], v[132:135]\n"
      "v_add_u32 v233, 0xc000, v233\n"
      "v_mfma_f32_16x16x32_bf16 v[132:135], a[248:251], v[44:47], v[132:135]\n"
      "v_add_u32 v234, 0xc000, v234\n"
      "v_mfma_f32_16x16x32_bf16 v[132:135], a[252:255], v[60:63], v[132:135]\n"
      "v_add_u32 v235, 0xc000, v235\n"
      "s_waitcnt lgkmcnt(5)\n"
      "v_mfma_f32_16x16x32_bf16 v[132:135], a[48:51], v[64:67], v[132:135]\n"
      "v_cvt_pk_bf16_f32 v210, v136, v137\n"
      "v_mfma_f32_16x16x32_bf16 v[132:135], a[52:55], v[68:71], v[132:135]\n"
      "v_cvt_pk_bf16_f32 v211, v138, v139\n"
      "v_mfma_f32_16x16x32_bf16 v[132:135], a[56:59], v[84:87], v[132:135]\n"
      "ds_read_b128 v[216:219], v246 offset:0\n"
      "v_mfma_f32_16x16x32_bf16 v[132:135], a[60:63], v[88:91], v[132:135]\n"
      "ds_read_b128 v[220:223], v246 offset:1024\n"
      "s_waitcnt lgkmcnt(2)\n"
      "v_mfma_f32_16x16x32_bf16 v[132:135], a[64:67], v[92:95], v[132:135]\n"
      "ds_read_b128 v[224:227], v246 offset:2048\n"
      "v_mfma_f32_16x16x32_bf16 v[132:135], a[68:71], v[108:111], v[132:135]\n"
      "v_lshlrev_b32 v0, 16, v120\n"
      "v_mfma_f32_16x16x32_bf16 v[132:135], a[72:75], v[112:115], v[132:135]\n"
      "ds_write_b64 v244, v[210:211] offset:12424\n"
      "v_mfma_f32_16x16x32_bf16 v[132:135], a[76:79], v[116:119], v[132:135]\n"
      "v_and_b32 v1, 0xffff0000, v120\n"
      "v_mfma_f32_16x16x32_bf16 v[136:139], a[80:83], v[36:39], v[4:7]\n"
      "v_lshlrev_b32 v2, 16, v121\n"
      "v_mfma_f32_16x16x32_bf16 v[136:139], a[84:87], v[40:43], v[136:139]\n"
      "v_and_b32 v3, 0xffff0000, v121\n"
      "v_mfma_f32_16x16x32_bf16 v[136:139], a[88:91], v[44:47], v[136:139]\n"
      "v_mfma_f32_16x16x32_bf16 v[136:139], a[92:95], v[60:63], v[136:139]\n"
      "v_cvt_pk_bf16_f32 v208, v132, v133\n"
      "v_mfma_f32_16x16x32_bf16 v[136:139], a[96:99], v[64:67], v[136:139]\n"
      "v_cvt_pk_bf16_f32 v209, v134, v135\n"
      "v_mfma_f32_16x16x32_bf16 v[136:139], a[100:103], v[68:71], v[136:139]\n"
      "s_waitcnt lgkmcnt(3)\n"
      "v_mfma_f32_16x16x32_bf16 v[136:139], a[104:107], v[84:87], v[136:139]\n"
      "global_store_dwordx4 v247, v[216:219], s[54:55] offset:768 sc1\n"
      "v_mfma_f32_16x16x32_bf16 v[136:139], a[108:111], v[88:91], v[136:139]\n"
      "s_add_u32 m0, s17, 0x1000\n"
      "v_mfma_f32_16x16x32_bf16 v[136:139], a[112:115], v[92:95], v[136:139]\n"
      "global_load_lds_dwordx4 v237, s[52:53] offset:0 nt\n"
      "v_mfma_f32_16x16x32_bf16 v[136:139], a[116:119], v[108:111], v[136:139]\n"
      "ds_write_b64 v244, v[208:209] offset:24576\n"
      "v_mfma_f32_16x16x32_bf16 v[136:139], a[120:123], v[112:115], v[136:139]\n"
      "v_lshlrev_b32 v4, 16, v122\n"
      "v_mfma_f32_16x16x32_bf16 v[136:139], a[124:127], v[116:119], v[136:139]\n"
      "v_and_b32 v5, 0xffff0000, v122\n"
      "v_mfma_f32_16x16x32_bf16 v[132:135], a[128:131], v[36:39], v[8:11]\n"
      "v_lshlrev_b32 v6, 16, v123\n"
      "v_mfma_f32_16x16x32_bf16 v[132:135], a[132:135], v[40:43], v[132:135]\n"
      "v_and_b32 v7, 0xffff0000, v123\n"
      "v_mfma_f32_16x16x32_bf16 v[132:135], a[136:139], v[44:47], v[132:135]\n"
      "s_cbranch_vccnz LT10\n"
      "LS10:\n"
      "v_mfma_f32_16x16x32_bf16 v[132:135], a[140:143], v[60:63], v[132:135]\n"
      "v_cvt_pk_bf16_f32 v210, v136, v137\n"
      "v_mfma_f32_16x16x32_bf16 v[132:135], a[144:147], v[64:67], v[132:135]\n"
      "v_cvt_pk_bf16_f32 v211, v138, v139\n"
      "v_mfma_f32_16x16x32_bf16 v[132:135], a[148:151], v[68:71], v[132:135]\n"
      "v_lshlrev_b32 v8, 16, v124\n"
      "v_mfma_f32_16x16x32_bf16 v[132:135], a[152:155], v[84:87], v[132:135]\n"
      "s_waitcnt lgkmcnt(3)\n"
      "v_mfma_f32_16x16x32_bf16 v[132:135], a[156:159], v[88:91], v[132:135]\n"
      "global_store_dwordx4 v252, v[220:223], s[54:55] offset:768 sc1\n"
      "v_mfma_f32_16x16x32_bf16 v[132:135], a[160:163], v[92:95], v[132:135]\n"
      "s_add_u32 m0, s17, 0x2f00\n"
      "v_mfma_f32_16x16x32_bf16 v[132:135], a[164:167], v[108:111], v[132:135]\n"
      "ds_write_b64 v244, v[210:211] offset:24584\n"
      "v_mfma_f32_16x16x32_bf16 v[132:135], a[168:171], v[112:115], v[132:135]\n"
      "global_load_lds_dwordx4 v237, s[52:53] offset:256 nt\n"
      "v_mfma_f32_16x16x32_bf16 v[132:135], a[172:175], v[116:119], v[132:135]\n"
      "v_and_b32 v9, 0xffff0000, v124\n"
      "v_mfma_f32_16x16x32_bf16 v[136:139], a[176:179], v[36:39], v[12:15]\n"
      "v_lshlrev_b32 v10, 16, v125\n"
      "v_mfma_f32_16x16x32_bf16 v[136:139], a[180:183], v[40:43], v[136:139]\n"
      "v_and_b32 v11, 0xffff0000, v125\n"
      "v_mfma_f32_16x16x32_bf16 v[136:139], a[184:187], v[44:47], v[136:139]\n"
      "v_mfma_f32_16x16x32_bf16 v[136:139], a[188:191], v[60:63], v[136:139]\n"
      "v_cvt_pk_bf16_f32 v212, v132, v133\n"
      "v_mfma_f32_16x16x32_bf16 v[136:139], a[192:195], v[64:67], v[136:139]\n"
      "v_cvt_pk_bf16_f32 v213, v134, v135\n"
      "v_mfma_f32_16x16x32_bf16 v[136:139], a[196:199], v[68:71], v[136:139]\n"
      "v_lshlrev_b32 v12, 16, v126\n"
      "v_mfma_f32_16x16x32_bf16 v[136:139], a[200:203], v[84:87], v[136:139]\n"
      "v_and_b32 v13, 0xffff0000, v126\n"
      "v_mfma_f32_16x16x32_bf16 v[136:139], a[204:207], v[88:91], v[136:139]\n"
      "s_waitcnt lgkmcnt(3)\n"
      "v_mfma_f32_16x16x32_bf16 v[136:139], a[208:211], v[92:95], v[136:139]\n"
      "global_store_dwordx4 v253, v[224:227], s[54:55] offset:768 sc1\n"
      "v_mfma_f32_16x16x32_bf16 v[136:139], a[212:215], v[108:111], v[136:139]\n"
      "ds_write_b64 v245, v[212:213] offset:24576\n"
      "v_mfma_f32_16x16x32_bf16 v[136:139], a[216:219], v[112:115], v[136:139]\n"
      "s_add_u32 m0, s17, 0x4e00\n"
      "v_mfma_f32_16x16x32_bf16 v[136:139], a[220:223], v[116:119], v[136:139]\n"
      "global_load_lds_dwordx4 v237, s[52:53] offset:512 nt\n"
      "v_mfma_f32_16x16x32_bf16 v[132:135], a[224:227], v[36:39], v[16:19]\n"
      "v_lshlrev_b32 v14, 16, v127\n"
      "v_mfma_f32_16x16x32_bf16 v[132:135], a[228:231], v[40:43], v[132:135]\n"
      "v_and_b32 v15, 0xffff0000, v127\n"
      "v_mfma_f32_16x16x32_bf16 v[132:135], a[232:235], v[44:47], v[132:135]\n"
      "s_cbranch_vccnz LT11\n"
      "LS11:\n"
      "v_mfma_f32_16x16x32_bf16 v[132:135], a[236:239], v[60:63], v[132:135]\n"
      "s_memrealtime s[84:85]\n"
      "v_mfma_f32_16x16x32_bf16 v[132:135], a[0:3], v[64:67], v[132:135]\n"
      "v_cvt_pk_bf16_f32 v214, v136, v137\n"
      "v_mfma_f32_16x16x32_bf16 v[132:135], a[4:7], v[68:71], v[132:135]\n"
      "v_cvt_pk_bf16_f32 v215, v138, v139\n"
      "v_mfma_f32_16x16x32_bf16 v[132:135], a[8:11], v[84:87], v[132:135]\n"
      "v_lshlrev_b32 v16, 16, v128\n"
      "v_mfma_f32_16x16x32_bf16 v[132:135], a[12:15], v[88:91], v[132:135]\n"
      "s_add_u32 s83, s83, s86\n"
      "v_mfma_f32_16x16x32_bf16 v[132:135], a[16:19], v[92:95], v[132:135]\n"
      "v_and_b32 v17, 0xffff0000, v128\n"
      "v_mfma_f32_16x16x32_bf16 v[132:135], a[20:23], v[108:111], v[132:135]\n"
      "v_lshlrev_b32 v18, 16, v129\n"
      "v_mfma_f32_16x16x32_bf16 v[132:135], v[140:143], v[112:115], v[132:135]\n"
      "s_waitcnt lgkmcnt(0)\n"
      "s_sub_u32 s84, s84, s83\n"
      "v_mfma_f32_16x16x32_bf16 v[132:135], v[156:159], v[116:119], v[132:135]\n"
      "s_cmp_lt_i32 s84, 0\n"
      "s_cselect_b64 vcc, -1, 0\n"
      "s_waitcnt vmcnt(24)\n"
      "s_barrier\n"
      "v_mfma_f32_16x16x32_bf16 v[136:139], v[160:163], v[36:39], v[20:23]\n"
      "ds_read_b128 v[36:39], v232 offset:0\n"
      "ds_write_b64 v245, v[214:215] offset:24584\n"
      "v_mfma_f32_16x16x32_bf16 v[136:139], v[164:167], v[40:43], v[136:139]\n"
      "ds_read_b128 v[40:43], v233 offset:0\n"
      "v_mfma_f32_16x16x32_bf16 v[136:139], v[180:183], v[44:47], v[136:139]\n"
      "ds_read_b128 v[44:47], v234 offset:0\n"
      "v_mfma_f32_16x16x32_bf16 v[136:139], v[184:187], v[60:63], v[136:139]\n"
      "ds_read_b128 v[60:63], v235 offset:0\n"
      "v_cvt_pk_bf16_f32 v208, v132, v133\n"
      "v_mfma_f32_16x16x32_bf16 v[136:139], v[188:191], v[64:67], v[136:139]\n"
      "ds_read_b128 v[64:67], v232 offset:8192\n"
      "v_cvt_pk_bf16_f32 v209, v134, v135\n"
      "v_mfma_f32_16x16x32_bf16 v[136:139], v[204:207], v[68:71], v[136:139]\n"
      "ds_read_b128 v[68:71], v233 offset:8192\n"
      "v_mfma_f32_16x16x32_bf16 v[136:139], a[24:27], v[84:87], v[136:139]\n"
      "ds_read_b128 v[84:87], v234 offset:8192\n"
      "v_mfma_f32_16x16x32_bf16 v[136:139], a[28:31], v[88:91], v[136:139]\n"
      "ds_read_b128 v[88:91], v235 offset:8192\n"
      "v_mfma_f32_16x16x32_bf16 v[136:139], a[32:35], v[92:95], v[136:139]\n"
      "ds_read_b128 v[92:95], v232 offset:16384\n"
      "v_mfma_f32_16x16x32_bf16 v[136:139], a[36:39], v[108:111], v[136:139]\n"
      "ds_read_b128 v[108:111], v233 offset:16384\n"
      "v_mfma_f32_16x16x32_bf16 v[136:139], a[40:43], v[112:115], v[136:139]\n"
      "ds_read_b128 v[112:115], v234 offset:16384\n"
      "v_mfma_f32_16x16x32_bf16 v[136:139], a[44:47], v[116:119], v[136:139]\n"
      "ds_read_b128 v[116:119], v235 offset:16384\n"
      "s_waitcnt lgkmcnt(8)\n"
      "v_mfma_f32_16x16x32_bf16 v[132:135], a[240:243], v[36:39], v[0:3]\n"
      "ds_write_b64 v244, v[208:209] offset:24704\n"
      "s_cbranch_vccnz LT12\n"
      "LS12:\n"
      "v_mfma_f32_16x16x32_bf16 v[132:135], a[244:247], v[40:43], v[132:135]\n"
      "v_and_b32 v19, 0xffff0000, v129\n"
      "v_mfma_f32_16x16x32_bf16 v[132:135], a[248:251], v[44:47], v[132:135]\n"
      "v_lshlrev_b32 v20, 16, v130\n"
      "v_mfma_f32_16x16x32_bf16 v[132:135], a[252:255], v[60:63], v[132:135]\n"
      "v_and_b32 v21, 0xffff0000, v130\n"
      "s_waitcnt lgkmcnt(5)\n"
      "v_mfma_f32_16x16x32_bf16 v[132:135], a[48:51], v[64:67], v[132:135]\n"
      "v_lshlrev_b32 v22, 16, v131\n"
      "v_mfma_f32_16x16x32_bf16 v[132:135], a[52:55], v[68:71], v[132:135]\n"
      "v_and_b32 v23, 0xffff0000, v131\n"
      "v_mfma_f32_16x16x32_bf16 v[132:135], a[56:59], v[84:87], v[132:135]\n"
      "v_cvt_pk_bf16_f32 v210, v136, v137\n"
      "v_mfma_f32_16x16x32_bf16 v[132:135], a[60:63], v[88:91], v[132:135]\n"
      "v_cvt_pk_bf16_f32 v211, v138, v139\n"
      "s_waitcnt lgkmcnt(1)\n"
      "v_mfma_f32_16x16x32_bf16 v[132:135], a[64:67], v[92:95], v[132:135]\n"
      "s_add_u32 s54, s44, s36\n"
      "s_addc_u32 s55, s45, 0\n"
      "v_mfma_f32_16x16x32_bf16 v[132:135], a[68:71], v[108:111], v[132:135]\n"
      "ds_read_b128 v[216:219], v246 offset:12288\n"
      "v_mfma_f32_16x16x32_bf16 v[132:135], a[72:75], v[112:115], v[132:135]\n"
      "ds_read_b128 v[220:223], v246 offset:13312\n"
      "v_mfma_f32_16x16x32_bf16 v[132:135], a[76:79], v[116:119], v[132:135]\n"
      "ds_read_b128 v[224:227], v246 offset:14336\n"
      "v_mfma_f32_16x16x32_bf16 v[136:139], a[80:83], v[36:39], v[4:7]\n"
      "ds_write_b64 v244, v[210:211] offset:24712\n"
      "v_mfma_f32_16x16x32_bf16 v[136:139], a[84:87], v[40:43], v[136:139]\n"
      "v_mfma_f32_16x16x32_bf16 v[136:139], a[88:91], v[44:47], v[136:139]\n"
      "v_mfma_f32_16x16x32_bf16 v[136:139], a[92:95], v[60:63], v[136:139]\n"
      "v_cvt_pk_bf16_f32 v208, v132, v133\n"
      "v_mfma_f32_16x16x32_bf16 v[136:139], a[96:99], v[64:67], v[136:139]\n"
      "v_cvt_pk_bf16_f32 v209, v134, v135\n"
      "v_mfma_f32_16x16x32_bf16 v[136:139], a[100:103], v[68:71], v[136:139]\n"
      "s_waitcnt lgkmcnt(3)\n"
      "v_mfma_f32_16x16x32_bf16 v[136:139], a[104:107], v[84:87], v[136:139]\n"
      "global_store_dwordx4 v247, v[216:219], s[54:55] offset:0 sc1\n"
      "v_mfma_f32_16x16x32_bf16 v[136:139], a[108:111], v[88:91], v[136:139]\n"
      "s_add_u32 s52, s42, s38\n"
      "s_addc_u32 s53, s43, 0\n"
      "v_mfma_f32_16x16x32_bf16 v[136:139], a[112:115], v[92:95], v[136:139]\n"
      "s_add_u32 m0, s17, 0x6000\n"
      "v_mfma_f32_16x16x32_bf16 v[136:139], a[116:119], v[108:111], v[136:139]\n"
      "ds_write_b64 v244, v[208:209] offset:0\n"
      "v_mfma_f32_16x16x32_bf16 v[136:139], a[120:123], v[112:115], v[136:139]\n"
      "global_load_lds_dwordx4 v236, s[52:53] offset:0 nt\n"
      "v_mfma_f32_16x16x32_bf16 v[136:139], a[124:127], v[116:119], v[136:139]\n"
      "v_mfma_f32_16x16x32_bf16 v[132:135], a[128:131], v[36:39], v[8:11]\n"
      "s_cbranch_vccnz LT13\n"
      "LS13:\n"
      "v_mfma_f32_16x16x32_bf16 v[132:135], a[132:135], v[40:43], v[132:135]\n"
      "v_mfma_f32_16x16x32_bf16 v[132:135], a[136:139], v[44:47], v[132:135]\n"
      "v_mfma_f32_16x16x32_bf16 v[132:135], a[140:143], v[60:63], v[132:135]\n"
      "v_cvt_pk_bf16_f32 v210, v136, v137\n"
      "v_mfma_f32_16x16x32_bf16 v[132:135], a[144:147], v[64:67], v[132:135]\n"
      "v_cvt_pk_bf16_f32 v211, v138, v139\n"
      "v_mfma_f32_16x16x32_bf16 v[132:135], a[148:151], v[68:71], v[132:135]\n"
      "v_mfma_f32_16x16x32_bf16 v[132:135], a[152:155], v[84:87], v[132:135]\n"
      "s_waitcnt lgkmcnt(3)\n"
      "v_mfma_f32_16x16x32_bf16 v[132:135], a[156:159], v[88:91], v[132:135]\n"
      "global_store_dwordx4 v252, v[220:223], s[54:55] offset:0 sc1\n"
      "v_mfma_f32_16x16x32_bf16 v[132:135], a[160:163], v[92:95], v[132:135]\n"
      "s_add_u32 m0, s17, 0x7f00\n"
      "v_mfma_f32_16x16x32_bf16 v[132:135], a[164:167], v[108:111], v[132:135]\n"
      "ds_write_b64 v244, v[210:211] offset:8\n"
      "v_mfma_f32_16x16x32_bf16 v[132:135], a[168:171], v[112:115], v[132:135]\n"
      "global_load_lds_dwordx4 v236, s[52:53] offset:256 nt\n"
      "v_mfma_f32_16x16x32_bf16 v[132:135], a[172:175], v[116:119], v[132:135]\n"
      "v_mfma_f32_16x16x32_bf16 v[136:139], a[176:179], v[36:39], v[12:15]\n"
      "v_mfma_f32_16x16x32_bf16 v[136:139], a[180:183], v[40:43], v[136:139]\n"
      "v_mfma_f32_16x16x32_bf16 v[136:139], a[184:187], v[44:47], v[136:139]\n"
      "v_mfma_f32_16x16x32_bf16 v[136:139], a[188:191], v[60:63], v[136:139]\n"
      "v_cvt_pk_bf16_f32 v212, v132, v133\n"
      "v_mfma_f32_16x16x32_bf16 v[136:139], a[192:195], v[64:67], v[136:139]\n"
      "v_cvt_pk_bf16_f32 v213, v134, v135\n"
      "v_mfma_f32_16x16x32_bf16 v[136:139], a[196:199], v[68:71], v[136:139]\n"
      "v_mfma_f32_16x16x32_bf16 v[136:139], a[200:203], v[84:87], v[136:139]\n"
      "v_mfma_f32_16x16x32_bf16 v[136:139], a[204:207], v[88:91], v[136:139]\n"
      "s_waitcnt lgkmcnt(3)\n"
      "v_mfma_f32_16x16x32_bf16 v[136:139], a[208:211], v[92:95], v[136:139]\n"
      "global_store_dwordx4 v253, v[224:227], s[54:55] offset:0 sc1\n"
      "v_mfma_f32_16x16x32_bf16 v[136:139], a[212:215], v[108:111], v[136:139]\n"
      "ds_write_b64 v245, v[212:213] offset:0\n"
      "v_mfma_f32_16x16x32_bf16 v[136:139], a[216:219], v[112:115], v[136:139]\n"
      "s_add_u32 m0, s17, 0x9e00\n"
      "v_mfma_f32_16x16x32_bf16 v[136:139], a[220:223], v[116:119], v[136:139]\n"
      "global_load_lds_dwordx4 v236, s[52:53] offset:512 nt\n"
      "v_mfma_f32_16x16x32_bf16 v[132:135], a[224:227], v[36:39], v[16:19]\n"
      "s_cbranch_vccnz LT14\n"
      "LS14:\n"
      "v_mfma_f32_16x16x32_bf16 v[132:135], a[228:231], v[40:43], v[132:135]\n"
      "v_mfma_f32_16x16x32_bf16 v[132:135], a[232:235], v[44:47], v[132:135]\n"
      "v_mfma_f32_16x16x32_bf16 v[132:135], a[236:239], v[60:63], v[132:135]\n"
      "v_cvt_pk_bf16_f32 v214, v136, v137\n"
      "v_mfma_f32_16x16x32_bf16 v[132:135], a[0:3], v[64:67], v[132:135]\n"
      "v_cvt_pk_bf16_f32 v215, v138, v139\n"
      "v_mfma_f32_16x16x32_bf16 v[132:135], a[4:7], v[68:71], v[132:135]\n"
      "v_mfma_f32_16x16x32_bf16 v[132:135], a[8:11], v[84:87], v[132:135]\n"
      "v_mfma_f32_16x16x32_bf16 v[132:135], a[12:15], v[88:91], v[132:135]\n"
      "v_mfma_f32_16x16x32_bf16 v[132:135], a[16:19], v[92:95], v[132:135]\n"
      "v_mfma_f32_16x16x32_bf16 v[132:135], a[20:23], v[108:111], v[132:135]\n"
      "ds_write_b64 v245, v[214:215] offset:8\n"
      "v_mfma_f32_16x16x32_bf16 v[132:135], v[140:143], v[112:115], v[132:135]\n"
      "v_mfma_f32_16x16x32_bf16 v[132:135], v[156:159], v[116:119], v[132:135]\n"
      "s_waitcnt lgkmcnt(4)\n"
      "s_barrier\n"
      "v_mfma_f32_16x16x32_bf16 v[136:139], v[160:163], v[36:39], v[20:23]\n"
      "ds_read_b128 v[36:39], v232 offset:256\n"
      "v_mfma_f32_16x16x32_bf16 v[136:139], v[164:167], v[40:43], v[136:139]\n"
      "ds_read_b128 v[40:43], v233 offset:256\n"
      "v_mfma_f32_16x16x32_bf16 v[136:139], v[180:183], v[44:47], v[136:139]\n"
      "ds_read_b128 v[44:47], v234 offset:256\n"
      "v_mfma_f32_16x16x32_bf16 v[136:139], v[184:187], v[60:63], v[136:139]\n"
      "ds_read_b128 v[60:63], v235 offset:256\n"
      "v_cvt_pk_bf16_f32 v208, v132, v133\n"
      "v_mfma_f32_16x16x32_bf16 v[136:139], v[188:191], v[64:67], v[136:139]\n"
      "ds_read_b128 v[64:67], v232 offset:8448\n"
      "v_cvt_pk_bf16_f32 v209, v134, v135\n"
      "v_mfma_f32_16x16x32_bf16 v[136:139], v[204:207], v[68:71], v[136:139]\n"
      "ds_read_b128 v[68:71], v233 offset:8448\n"
      "v_mfma_f32_16x16x32_bf16 v[136:139], a[24:27], v[84:87], v[136:139]\n"
      "ds_read_b128 v[84:87], v234 offset:8448\n"
      "v_mfma_f32_16x16x32_bf16 v[136:139], a[28:31], v[88:91], v[136:139]\n"
      "ds_read_b128 v[88:91], v235 offset:8448\n"
      "v_mfma_f32_16x16x32_bf16 v[136:139], a[32:35], v[92:95], v[136:139]\n"
      "ds_read_b128 v[92:95], v232 offset:16640\n"
      "v_mfma_f32_16x16x32_bf16 v[136:139], a[36:39], v[108:111], v[136:139]\n"
      "ds_read_b128 v[108:111], v233 offset:16640\n"
      "v_mfma_f32_16x16x32_bf16 v[136:139], a[40:43], v[112:115], v[136:139]\n"
      "ds_read_b128 v[112:115], v234 offset:16640\n"
      "v_mfma_f32_16x16x32_bf16 v[136:139], a[44:47], v[116:119], v[136:139]\n"
      "ds_read_b128 v[116:119], v235 offset:16640\n"
      "s_waitcnt lgkmcnt(8)\n"
      "v_mfma_f32_16x16x32_bf16 v[132:135], a[240:243], v[36:39], v[0:3]\n"
      "ds_write_b64 v244, v[208:209] offset:128\n"
      "v_mfma_f32_16x16x32_bf16 v[132:135], a[244:247], v[40:43], v[132:135]\n"
      "s_cbranch_vccnz LT15\n"
      "LS15:\n"
      "v_mfma_f32_16x16x32_bf16 v[132:135], a[248:251], v[44:47], v[132:135]\n"
      "v_mfma_f32_16x16x32_bf16 v[132:135], a[252:255], v[60:63], v[132:135]\n"
      "v_cvt_pk_bf16_f32 v210, v136, v137\n"
      "s_waitcnt lgkmcnt(5)\n"
      "v_mfma_f32_16x16x32_bf16 v[132:135], a[48:51], v[64:67], v[132:135]\n"
      "v_cvt_pk_bf16_f32 v211, v138, v139\n"
      "v_mfma_f32_16x16x32_bf16 v[132:135], a[52:55], v[68:71], v[132:135]\n"
      "v_mfma_f32_16x16x32_bf16 v[132:135], a[56:59], v[84:87], v[132:135]\n"
      "ds_read_b128 v[216:219], v246 offset:24576\n"
      "v_mfma_f32_16x16x32_bf16 v[132:135], a[60:63], v[88:91], v[132:135]\n"
      "ds_read_b128 v[220:223], v246 offset:25600\n"
      "s_waitcnt lgkmcnt(3)\n"
      "v_mfma_f32_16x16x32_bf16 v[132:135], a[64:67], v[92:95], v[132:135]\n"
      "ds_read_b128 v[224:227], v246 offset:26624\n"
      "v_mfma_f32_16x16x32_bf16 v[132:135], a[68:71], v[108:111], v[132:135]\n"
      "ds_write_b64 v244, v[210:211] offset:136\n"
      "v_mfma_f32_16x16x32_bf16 v[132:135], a[72:75], v[112:115], v[132:135]\n"
      "v_mfma_f32_16x16x32_bf16 v[132:135], a[76:79], v[116:119], v[132:135]\n"
      "v_mfma_f32_16x16x32_bf16 v[136:139], a[80:83], v[36:39], v[4:7]\n"
      "v_mfma_f32_16x16x32_bf16 v[136:139], a[84:87], v[40:43], v[136:139]\n"
      "v_mfma_f32_16x16x32_bf16 v[136:139], a[88:91], v[44:47], v[136:139]\n"
      "v_mfma_f32_16x16x32_bf16 v[136:139], a[92:95], v[60:63], v[136:139]\n"
      "v_cvt_pk_bf16_f32 v208, v132, v133\n"
      "v_mfma_f32_16x16x32_bf16 v[136:139], a[96:99], v[64:67], v[136:139]\n"
      "v_cvt_pk_bf16_f32 v209, v134, v135\n"
      "v_mfma_f32_16x16x32_bf16 v[136:139], a[100:103], v[68:71], v[136:139]\n"
      "s_waitcnt lgkmcnt(3)\n"
      "v_mfma_f32_16x16x32_bf16 v[136:139], a[104:107], v[84:87], v[136:139]\n"
      "global_store_dwordx4 v247, v[216:219], s[54:55] offset:768 sc1\n"
      "v_mfma_f32_16x16x32_bf16 v[136:139], a[108:111], v[88:91], v[136:139]\n"
      "s_add_u32 m0, s17, 0x7000\n"
      "v_mfma_f32_16x16x32_bf16 v[136:139], a[112:115], v[92:95], v[136:139]\n"
      "global_load_lds_dwordx4 v237, s[52:53] offset:0 nt\n"
      "v_mfma_f32_16x16x32_bf16 v[136:139], a[116:119], v[108:111], v[136:139]\n"
      "ds_write_b64 v244, v[208:209] offset:12288\n"
      "v_mfma_f32_16x16x32_bf16 v[136:139], a[120:123], v[112:115], v[136:139]\n"
      "v_mfma_f32_16x16x32_bf16 v[136:139], a[124:127], v[116:119], v[136:139]\n"
      "v_mfma_f32_16x16x32_bf16 v[132:135], a[128:131], v[36:39], v[8:11]\n"
      "s_cbranch_vccnz LT16\n"
      "LS16:\n"
      "v_mfma_f32_16x16x32_bf16 v[132:135], a[132:135], v[40:43], v[132:135]\n"
      "v_mfma_f32_16x16x32_bf16 v[132:135], a[136:139], v[44:47], v[132:135]\n"
      "v_mfma_f32_16x16x32_bf16 v[132:135], a[140:143], v[60:63], v[132:135]\n"
      "v_cvt_pk_bf16_f32 v210, v136, v137\n"
      "v_mfma_f32_16x16x32_bf16 v[132:135], a[144:147], v[64:67], v[132:135]\n"
      "v_cvt_pk_bf16_f32 v211, v138, v139\n"
      "v_mfma_f32_16x16x32_bf16 v[132:135], a[148:151], v[68:71], v[132:135]\n"
      "v_mfma_f32_16x16x32_bf16 v[132:135], a[152:155], v[84:87], v[132:135]\n"
      "s_waitcnt lgkmcnt(3)\n"
      "v_mfma_f32_16x16x32_bf16 v[132:135], a[156:159], v[88:91], v[132:135]\n"
      "global_store_dwordx4 v252, v[220:223], s[54:55] offset:768 sc1\n"
      "v_mfma_f32_16x16x32_bf16 v[132:135], a[160:163], v[92:95], v[132:135]\n"
      "s_add_u32 m0, s17, 0x8f00\n"
      "v_mfma_f32_16x16x32_bf16 v[132:135], a[164:167], v[108:111], v[132:135]\n"
      "ds_write_b64 v244, v[210:211] offset:12296\n"
      "v_mfma_f32_16x16x32_bf16 v[132:135], a[168:171], v[112:115], v[132:135]\n"
      "global_load_lds_dwordx4 v237, s[52:53] offset:256 nt\n"
      "v_mfma_f32_16x16x32_bf16 v[132:135], a[172:175], v[116:119], v[132:135]\n"
      "v_mfma_f32_16x16x32_bf16 v[136:139], a[176:179], v[36:39], v[12:15]\n"
      "v_mfma_f32_16x16x32_bf16 v[136:139], a[180:183], v[40:43], v[136:139]\n"
      "v_mfma_f32_16x16x32_bf16 v[136:139], a[184:187], v[44:47], v[136:139]\n"
      "v_mfma_f32_16x16x32_bf16 v[136:139], a[188:191], v[60:63], v[136:139]\n"
      "v_cvt_pk_bf16_f32 v212, v132, v133\n"
      "v_mfma_f32_16x16x32_bf16 v[136:139], a[192:195], v[64:67], v[136:139]\n"
      "v_cvt_pk_bf16_f32 v213, v134, v135\n"
      "v_mfma_f32_16x16x32_bf16 v[136:139], a[196:199], v[68:71], v[136:139]\n"
      "v_mfma_f32_16x16x32_bf16 v[136:139], a[200:203], v[84:87], v[136:139]\n"
      "v_mfma_f32_16x16x32_bf16 v[136:139], a[204:207], v[88:91], v[136:139]\n"
      "s_waitcnt lgkmcnt(3)\n"
      "v_mfma_f32_16x16x32_bf16 v[136:139], a[208:211], v[92:95], v[136:139]\n"
      "global_store_dwordx4 v253, v[224:227], s[54:55] offset:768 sc1\n"
      "v_mfma_f32_16x16x32_bf16 v[136:139], a[212:215], v[108:111], v[136:139]\n"
      "ds_write_b64 v245, v[212:213] offset:12288\n"
      "v_mfma_f32_16x16x32_bf16 v[136:139], a[216:219], v[112:115], v[136:139]\n"
      "s_add_u32 m0, s17, 0xae00\n"
      "v_mfma_f32_16x16x32_bf16 v[136:139], a[220:223], v[116:119], v[136:139]\n"
      "global_load_lds_dwordx4 v237, s[52:53] offset:512 nt\n"
      "v_mfma_f32_16x16x32_bf16 v[132:135], a[224:227], v[36:39], v[16:19]\n"
      "s_cbranch_vccnz LT17\n"
      "LS17:\n"
      "v_mfma_f32_16x16x32_bf16 v[132:135], a[228:231], v[40:43], v[132:135]\n"
      "v_mfma_f32_16x16x32_bf16 v[132:135], a[232:235], v[44:47], v[132:135]\n"
      "v_mfma_f32_16x16x32_bf16 v[132:135], a[236:239], v[60:63], v[132:135]\n"
      "s_memrealtime s[84:85]\n"
      "v_mfma_f32_16x16x32_bf16 v[132:135], a[0:3], v[64:67], v[132:135]\n"
      "v_cvt_pk_bf16_f32 v214, v136, v137\n"
      "v_mfma_f32_16x16x32_bf16 v[132:135], a[4:7], v[68:71], v[132:135]\n"
      "v_cvt_pk_bf16_f32 v215, v138, v139\n"
      "v_mfma_f32_16x16x32_bf16 v[132:135], a[8:11], v[84:87], v[132:135]\n"
      "v_mfma_f32_16x16x32_bf16 v[132:135], a[12:15], v[88:91], v[132:135]\n"
      "s_add_u32 s83, s83, s86\n"
      "v_mfma_f32_16x16x32_bf16 v[132:135], a[16:19], v[92:95], v[132:135]\n"
      "v_mfma_f32_16x16x32_bf16 v[132:135], a[20:23], v[108:111], v[132:135]\n"
      "v_mfma_f32_16x16x32_bf16 v[132:135], v[140:143], v[112:115], v[132:135]\n"
      "s_waitcnt lgkmcnt(0)\n"
      "s_sub_u32 s84, s84, s83\n"
      "v_mfma_f32_16x16x32_bf16 v[132:135], v[156:159], v[116:119], v[132:135]\n"
      "s_cmp_lt_i32 s84, 0\n"
      "s_cselect_b64 vcc, -1, 0\n"
      "s_waitcnt vmcnt(24)\n"
      "s_barrier\n"
      "v_mfma_f32_16x16x32_bf16 v[136:139], v[160:163], v[36:39], v[20:23]\n"
      "ds_read_b128 v[36:39], v232 offset:24576\n"
      "ds_write_b64 v245, v[214:215] offset:12296\n"
      "v_mfma_f32_16x16x32_bf16 v[136:139], v[164:167], v[40:43], v[136:139]\n"
      "ds_read_b128 v[40:43], v233 offset:24576\n"
      "s_lshl_b32 s79, s94, 1\n"
      "v_mfma_f32_16x16x32_bf16 v[136:139], v[180:183], v[44:47], v[136:139]\n"
      "ds_read_b128 v[44:47], v234 offset:24576\n"
      "v_mad_i32_i24 v252, v251, s79, v252\n"
      "v_mfma_f32_16x16x32_bf16 v[136:139], v[184:187], v[60:63], v[136:139]\n"
      "ds_read_b128 v[60:63], v235 offset:24576\n"
      "v_cvt_pk_bf16_f32 v208, v132, v133\n"
      "v_mfma_f32_16x16x32_bf16 v[136:139], v[188:191], v[64:67], v[136:139]\n"
      "ds_read_b128 v[64:67], v232 offset:32768\n"
      "v_cvt_pk_bf16_f32 v209, v134, v135\n"
      "v_mfma_f32_16x16x32_bf16 v[136:139], v[204:207], v[68:71], v[136:139]\n"
      "ds_read_b128 v[68:71], v233 offset:32768\n"
      "s_mul_i32 s79, s79, s100\n"
      "v_mfma_f32_16x16x32_bf16 v[136:139], a[24:27], v[84:87], v[136:139]\n"
      "ds_read_b128 v[84:87], v234 offset:32768\n"
      "v_add_u32 v247, s79, v247\n"
      "v_mfma_f32_16x16x32_bf16 v[136:139], a[28:31], v[88:91], v[136:139]\n"
      "ds_read_b128 v[88:91], v235 offset:32768\n"
      "s_mul_i32 s79, s79, s101\n"
      "v_mfma_f32_16x16x32_bf16 v[136:139], a[32:35], v[92:95], v[136:139]\n"
      "ds_read_b128 v[92:95], v232 offset:40960\n"
      "v_add_u32 v253, s79, v253\n"
      "v_mfma_f32_16x16x32_bf16 v[136:139], a[36:39], v[108:111], v[136:139]\n"
      "ds_read_b128 v[108:111], v233 offset:40960\n"
      "v_mfma_f32_16x16x32_bf16 v[136:139], a[40:43], v[112:115], v[136:139]\n"
      "ds_read_b128 v[112:115], v234 offset:40960\n"
      "v_mfma_f32_16x16x32_bf16 v[136:139], a[44:47], v[116:119], v[136:139]\n"
      "ds_read_b128 v[116:119], v235 offset:40960\n"
      "s_waitcnt lgkmcnt(8)\n"
      "v_mfma_f32_16x16x32_bf16 v[132:135], a[240:243], v[36:39], v[0:3]\n"
      "ds_write_b64 v244, v[208:209] offset:12416\n"
      "v_mfma_f32_16x16x32_bf16 v[132:135], a[244:247], v[40:43], v[132:135]\n"
      "s_add_u32 s54, s30, s37\n"
      "s_addc_u32 s55, s31, 0\n"
      "v_mfma_f32_16x16x32_bf16 v[132:135], a[248:251], v[44:47], v[132:135]\n"
      "s_cbranch_vccnz LT18\n"
      "LS18:\n"
      "v_mfma_f32_16x16x32_bf16 v[132:135], a[252:255], v[60:63], v[132:135]\n"
      "v_cvt_pk_bf16_f32 v210, v136, v137\n"
      "s_waitcnt lgkmcnt(5)\n"
      "v_mfma_f32_16x16x32_bf16 v[132:135], a[48:51], v[64:67], v[132:135]\n"
      "v_cvt_pk_bf16_f32 v211, v138, v139\n"
      "v_mfma_f32_16x16x32_bf16 v[132:135], a[52:55], v[68:71], v[132:135]\n"
      "s_mul_i32 s78, s96, s19\n"
      "v_mfma_f32_16x16x32_bf16 v[132:135], a[56:59], v[84:87], v[132:135]\n"
      "ds_read_b128 v[216:219], v246 offset:0\n"
      "v_add_u32 v236, s78, v236\n"
      "v_mfma_f32_16x16x32_bf16 v[132:135], a[60:63], v[88:91], v[132:135]\n"
      "ds_read_b128 v[220:223], v246 offset:1024\n"
      "s_waitcnt lgkmcnt(3)\n"
      "v_mfma_f32_16x16x32_bf16 v[132:135], a[64:67], v[92:95], v[132:135]\n"
      "ds_read_b128 v[224:227], v246 offset:2048\n"
      "v_mfma_f32_16x16x32_bf16 v[132:135], a[68:71], v[108:111], v[132:135]\n"
      "ds_write_b64 v244, v[210:211] offset:12424\n"
      "v_mfma_f32_16x16x32_bf16 v[132:135], a[72:75], v[112:115], v[132:135]\n"
      "s_mul_i32 s78, s78, s99\n"
      "v_mfma_f32_16x16x32_bf16 v[132:135], a[76:79], v[116:119], v[132:135]\n"
      "v_add_u32 v237, s78, v237\n"
      "v_mfma_f32_16x16x32_bf16 v[136:139], a[80:83], v[36:39], v[4:7]\n"
      "v_mfma_f32_16x16x32_bf16 v[136:139], a[84:87], v[40:43], v[136:139]\n"
      "v_mfma_f32_16x16x32_bf16 v[136:139], a[88:91], v[44:47], v[136:139]\n"
      "v_mfma_f32_16x16x32_bf16 v[136:139], a[92:95], v[60:63], v[136:139]\n"
      "v_cvt_pk_bf16_f32 v208, v132, v133\n"
      "v_mfma_f32_16x16x32_bf16 v[136:139], a[96:99], v[64:67], v[136:139]\n"
      "v_cvt_pk_bf16_f32 v209, v134, v135\n"
      "v_mfma_f32_16x16x32_bf16 v[136:139], a[100:103], v[68:71], v[136:139]\n"
      "s_waitcnt lgkmcnt(3)\n"
      "v_mfma_f32_16x16x32_bf16 v[136:139], a[104:107], v[84:87], v[136:139]\n"
      "global_store_dwordx4 v247, v[216:219], s[54:55] offset:0 sc1\n"
      "v_mfma_f32_16x16x32_bf16 v[136:139], a[108:111], v[88:91], v[136:139]\n"
      "s_add_u32 s52, s20, s39\n"
      "s_addc_u32 s53, s21, 0\n"
      "v_mfma_f32_16x16x32_bf16 v[136:139], a[112:115], v[92:95], v[136:139]\n"
      "s_add_u32 m0, s17, 0xc000\n"
      "v_mfma_f32_16x16x32_bf16 v[136:139], a[116:119], v[108:111], v[136:139]\n"
      "ds_write_b64 v244, v[208:209] offset:24576\n"
      "v_mfma_f32_16x16x32_bf16 v[136:139], a[120:123], v[112:115], v[136:139]\n"
      "global_load_lds_dwordx4 v236, s[52:53] offset:0 nt\n"
      "v_mfma_f32_16x16x32_bf16 v[136:139], a[124:127], v[116:119], v[136:139]\n"
      "v_mfma_f32_16x16x32_bf16 v[132:135], a[128:131], v[36:39], v[8:11]\n"
      "s_cbranch_vccnz LT19\n"
      "LS19:\n"
      "v_mfma_f32_16x16x32_bf16 v[132:135], a[132:135], v[40:43], v[132:135]\n"
      "v_mfma_f32_16x16x32_bf16 v[132:135], a[136:139], v[44:47], v[132:135]\n"
      "v_mfma_f32_16x16x32_bf16 v[132:135], a[140:143], v[60:63], v[132:135]\n"
      "v_cvt_pk_bf16_f32 v210, v136, v137\n"
      "v_mfma_f32_16x16x32_bf16 v[132:135], a[144:147], v[64:67], v[132:135]\n"
      "v_cvt_pk_bf16_f32 v211, v138, v139\n"
      "v_mfma_f32_16x16x32_bf16 v[132:135], a[148:151], v[68:71], v[132:135]\n"
      "v_mfma_f32_16x16x32_bf16 v[132:135], a[152:155], v[84:87], v[132:135]\n"
      "s_waitcnt lgkmcnt(3)\n"
      "v_mfma_f32_16x16x32_bf16 v[132:135], a[156:159], v[88:91], v[132:135]\n"
      "global_store_dwordx4 v252, v[220:223], s[54:55] offset:0 sc1\n"
      "v_mfma_f32_16x16x32_bf16 v[132:135], a[160:163], v[92:95], v[132:135]\n"
      "s_add_u32 m0, s17, 0xdf00\n"
      "v_mfma_f32_16x16x32_bf16 v[132:135], a[164:167], v[108:111], v[132:135]\n"
      "ds_write_b64 v244, v[210:211] offset:24584\n"
      "v_mfma_f32_16x16x32_bf16 v[132:135], a[168:171], v[112:115], v[132:135]\n"
      "global_load_lds_dwordx4 v236, s[52:53] offset:256 nt\n"
      "v_mfma_f32_16x16x32_bf16 v[132:135], a[172:175], v[116:119], v[132:135]\n"
      "v_mfma_f32_16x16x32_bf16 v[136:139], a[176:179], v[36:39], v[12:15]\n"
      "v_mfma_f32_16x16x32_bf16 v[136:139], a[180:183], v[40:43], v[136:139]\n"
      "v_mfma_f32_16x16x32_bf16 v[136:139], a[184:187], v[44:47], v[136:139]\n"
      "v_mfma_f32_16x16x32_bf16 v[136:139], a[188:191], v[60:63], v[136:139]\n"
      "v_cvt_pk_bf16_f32 v212, v132, v133\n"
      "v_mfma_f32_16x16x32_bf16 v[136:139], a[192:195], v[64:67], v[136:139]\n"
      "v_cvt_pk_bf16_f32 v213, v134, v135\n"
      "v_mfma_f32_16x16x32_bf16 v[136:139], a[196:199], v[68:71], v[136:139]\n"
      "v_mfma_f32_16x16x32_bf16 v[136:139], a[200:203], v[84:87], v[136:139]\n"
      "v_mfma_f32_16x16x32_bf16 v[136:139], a[204:207], v[88:91], v[136:139]\n"
      "s_waitcnt lgkmcnt(3)\n"
      "v_mfma_f32_16x16x32_bf16 v[136:139], a[208:211], v[92:95], v[136:139]\n"
      "global_store_dwordx4 v253, v[224:227], s[54:55] offset:0 sc1\n"
      "v_mfma_f32_16x16x32_bf16 v[136:139], a[212:215], v[108:111], v[136:139]\n"
      "ds_write_b64 v245, v[212:213] offset:24576\n"
      "v_mfma_f32_16x16x32_bf16 v[136:139], a[216:219], v[112:115], v[136:139]\n"
      "s_add_u32 m0, s17, 0xfe00\n"
      "v_mfma_f32_16x16x32_bf16 v[136:139], a[220:223], v[116:119], v[136:139]\n"
      "global_load_lds_dwordx4 v236, s[52:53] offset:512 nt\n"
      "v_mfma_f32_16x16x32_bf16 v[132:135], a[224:227], v[36:39], v[16:19]\n"
      "s_cbranch_vccnz LT20\n"
      "LS20:\n"
      "v_mfma_f32_16x16x32_bf16 v[132:135], a[228:231], v[40:43], v[132:135]\n"
      "v_mfma_f32_16x16x32_bf16 v[132:135], a[232:235], v[44:47], v[132:135]\n"
      "v_mfma_f32_16x16x32_bf16 v[132:135], a[236:239], v[60:63], v[132:135]\n"
      "v_cvt_pk_bf16_f32 v214, v136, v137\n"
      "v_mfma_f32_16x16x32_bf16 v[132:135], a[0:3], v[64:67], v[132:135]\n"
      "v_cvt_pk_bf16_f32 v215, v138, v139\n"
      "v_mfma_f32_16x16x32_bf16 v[132:135], a[4:7], v[68:71], v[132:135]\n"
      "v_mfma_f32_16x16x32_bf16 v[132:135], a[8:11], v[84:87], v[132:135]\n"
      "v_mfma_f32_16x16x32_bf16 v[132:135], a[12:15], v[88:91], v[132:135]\n"
      "v_mfma_f32_16x16x32_bf16 v[132:135], a[16:19], v[92:95], v[132:135]\n"
      "v_mfma_f32_16x16x32_bf16 v[132:135], a[20:23], v[108:111], v[132:135]\n"
      "ds_write_b64 v245, v[214:215] offset:24584\n"
      "v_mfma_f32_16x16x32_bf16 v[132:135], v[140:143], v[112:115], v[132:135]\n"
      "v_mfma_f32_16x16x32_bf16 v[132:135], v[156:159], v[116:119], v[132:135]\n"
      "LPD13:\n"
      "s_cbranch_execz LPD16\n"
      "s_waitcnt lgkmcnt(4)\n"
      "s_barrier\n"
      "v_mfma_f32_16x16x32_bf16 v[136:139], v[160:163], v[36:39], v[20:23]\n"
      "ds_read_b128 v[36:39], v232 offset:24832\n"
      "v_mfma_f32_16x16x32_bf16 v[136:139], v[164:167], v[40:43], v[136:139]\n"
      "ds_read_b128 v[40:43], v233 offset:24832\n"
      "v_mfma_f32_16x16x32_bf16 v[136:139], v[180:183], v[44:47], v[136:139]\n"
      "ds_read_b128 v[44:47], v234 offset:24832\n"
      "v_mfma_f32_16x16x32_bf16 v[136:139], v[184:187], v[60:63], v[136:139]\n"
      "ds_read_b128 v[60:63], v235 offset:24832\n"
      "v_cvt_pk_bf16_f32 v208, v132, v133\n"
      "v_mfma_f32_16x16x32_bf16 v[136:139], v[188:191], v[64:67], v[136:139]\n"
      "ds_read_b128 v[64:67], v232 offset:33024\n"
      "v_cvt_pk_bf16_f32 v209, v134, v135\n"
      "v_mfma_f32_16x16x32_bf16 v[136:139], v[204:207], v[68:71], v[136:139]\n"
      "ds_read_b128 v[68:71], v233 offset:33024\n"
      "v_mfma_f32_16x16x32_bf16 v[136:139], a[24:27], v[84:87], v[136:139]\n"
      "ds_read_b128 v[84:87], v234 offset:33024\n"
      "v_mfma_f32_16x16x32_bf16 v[136:139], a[28:31], v[88:91], v[136:139]\n"
      "ds_read_b128 v[88:91], v235 offset:33024\n"
      "v_mfma_f32_16x16x32_bf16 v[136:139], a[32:35], v[92:95], v[136:139]\n"
      "ds_read_b128 v[92:95], v232 offset:41216\n"
      "v_mfma_f32_16x16x32_bf16 v[136:139], a[36:39], v[108:111], v[136:139]\n"
      "ds_read_b128 v[108:111], v233 offset:41216\n"
      "ds_write_b64 v244, v[208:209] offset:24704\n"
      "v_mfma_f32_16x16x32_bf16 v[136:139], a[40:43], v[112:115], v[136:139]\n"
      "ds_read_b128 v[112:115], v234 offset:41216\n"
      "v_mfma_f32_16x16x32_bf16 v[136:139], a[44:47], v[116:119], v[136:139]\n"
      "ds_read_b128 v[116:119], v235 offset:41216\n"
      "s_waitcnt lgkmcnt(9)\n"
      "v_mfma_f32_16x16x32_bf16 v[132:135], a[240:243], v[36:39], v[0:3]\n"
      "v_add_u32 v232, 0xffff4000, v232\n"
      "s_cbranch_vccnz LT21\n"
      "LS21:\n"
      "v_mfma_f32_16x16x32_bf16 v[132:135], a[244:247], v[40:43], v[132:135]\n"
      "v_add_u32 v233, 0xffff4000, v233\n"
      "v_mfma_f32_16x16x32_bf16 v[132:135], a[248:251], v[44:47], v[132:135]\n"
      "v_add_u32 v234, 0xffff4000, v234\n"
      "v_mfma_f32_16x16x32_bf16 v[132:135], a[252:255], v[60:63], v[132:135]\n"
      "v_add_u32 v235, 0xffff4000, v235\n"
      "s_waitcnt lgkmcnt(5)\n"
      "v_mfma_f32_16x16x32_bf16 v[132:135], a[48:51], v[64:67], v[132:135]\n"
      "v_cvt_pk_bf16_f32 v210, v136, v137\n"
      "v_mfma_f32_16x16x32_bf16 v[132:135], a[52:55], v[68:71], v[132:135]\n"
      "v_cvt_pk_bf16_f32 v211, v138, v139\n"
      "v_mfma_f32_16x16x32_bf16 v[132:135], a[56:59], v[84:87], v[132:135]\n"
      "ds_read_b128 v[216:219], v246 offset:12288\n"
      "v_mfma_f32_16x16x32_bf16 v[132:135], a[60:63], v[88:91], v[132:135]\n"
      "ds_read_b128 v[220:223], v246 offset:13312\n"
      "s_waitcnt lgkmcnt(2)\n"
      "v_mfma_f32_16x16x32_bf16 v[132:135], a[64:67], v[92:95], v[132:135]\n"
      "ds_read_b128 v[224:227], v246 offset:14336\n"
      "v_mfma_f32_16x16x32_bf16 v[132:135], a[68:71], v[108:111], v[132:135]\n"
      "v_lshlrev_b32 v0, 16, v144\n"
      "v_mfma_f32_16x16x32_bf16 v[132:135], a[72:75], v[112:115], v[132:135]\n"
      "ds_write_b64 v244, v[210:211] offset:24712\n"
      "v_mfma_f32_16x16x32_bf16 v[132:135], a[76:79], v[116:119], v[132:135]\n"
      "v_and_b32 v1, 0xffff0000, v144\n"
      "v_mfma_f32_16x16x32_bf16 v[136:139], a[80:83], v[36:39], v[4:7]\n"
      "v_lshlrev_b32 v2, 16, v145\n"
      "v_mfma_f32_16x16x32_bf16 v[136:139], a[84:87], v[40:43], v[136:139]\n"
      "v_and_b32 v3, 0xffff0000, v145\n"
      "v_mfma_f32_16x16x32_bf16 v[136:139], a[88:91], v[44:47], v[136:139]\n"
      "v_mfma_f32_16x16x32_bf16 v[136:139], a[92:95], v[60:63], v[136:139]\n"
      "v_cvt_pk_bf16_f32 v208, v132, v133\n"
      "v_mfma_f32_16x16x32_bf16 v[136:139], a[96:99], v[64:67], v[136:139]\n"
      "v_cvt_pk_bf16_f32 v209, v134, v135\n"
      "v_mfma_f32_16x16x32_bf16 v[136:139], a[100:103], v[68:71], v[136:139]\n"
      "s_waitcnt lgkmcnt(3)\n"
      "v_mfma_f32_16x16x32_bf16 v[136:139], a[104:107], v[84:87], v[136:139]\n"
      "global_store_dwordx4 v247, v[216:219], s[54:55] offset:768 sc1\n"
      "v_mfma_f32_16x16x32_bf16 v[136:139], a[108:111], v[88:91], v[136:139]\n"
      "s_add_u32 m0, s17, 0xd000\n"
      "v_mfma_f32_16x16x32_bf16 v[136:139], a[112:115], v[92:95], v[136:139]\n"
      "global_load_lds_dwordx4 v237, s[52:53] offset:0 nt\n"
      "v_mfma_f32_16x16x32_bf16 v[136:139], a[116:119], v[108:111], v[136:139]\n"
      "ds_write_b64 v244, v[208:209] offset:0\n"
      "v_mfma_f32_16x16x32_bf16 v[136:139], a[120:123], v[112:115], v[136:139]\n"
      "v_lshlrev_b32 v4, 16, v146\n"
      "v_mfma_f32_16x16x32_bf16 v[136:139], a[124:127], v[116:119], v[136:139]\n"
      "v_and_b32 v5, 0xffff0000, v146\n"
      "v_mfma_f32_16x16x32_bf16 v[132:135], a[128:131], v[36:39], v[8:11]\n"
      "v_lshlrev_b32 v6, 16, v147\n"
      "v_mfma_f32_16x16x32_bf16 v[132:135], a[132:135], v[40:43], v[132:135]\n"
      "v_and_b32 v7, 0xffff0000, v147\n"
      "v_mfma_f32_16x16x32_bf16 v[132:135], a[136:139], v[44:47], v[132:135]\n"
      "s_cbranch_vccnz LT22\n"
      "LS22:\n"
      "v_mfma_f32_16x16x32_bf16 v[132:135], a[140:143], v[60:63], v[132:135]\n"
      "v_cvt_pk_bf16_f32 v210, v136, v137\n"
      "v_mfma_f32_16x16x32_bf16 v[132:135], a[144:147], v[64:67], v[132:135]\n"
      "v_cvt_pk_bf16_f32 v211, v138, v139\n"
      "v_mfma_f32_16x16x32_bf16 v[132:135], a[148:151], v[68:71], v[132:135]\n"
      "v_lshlrev_b32 v8, 16, v148\n"
      "v_mfma_f32_16x16x32_bf16 v[132:135], a[152:155], v[84:87], v[132:135]\n"
      "s_waitcnt lgkmcnt(3)\n"
      "v_mfma_f32_16x16x32_bf16 v[132:135], a[156:159], v[88:91], v[132:135]\n"
      "global_store_dwordx4 v252, v[220:223], s[54:55] offset:768 sc1\n"
      "v_mfma_f32_16x16x32_bf16 v[132:135], a[160:163], v[92:95], v[132:135]\n"
      "s_add_u32 m0, s17, 0xef00\n"
      "v_mfma_f32_16x16x32_bf16 v[132:135], a[164:167], v[108:111], v[132:135]\n"
      "ds_write_b64 v244, v[210:211] offset:8\n"
      "v_mfma_f32_16x16x32_bf16 v[132:135], a[168:171], v[112:115], v[132:135]\n"
      "global_load_lds_dwordx4 v237, s[52:53] offset:256 nt\n"
      "v_mfma_f32_16x16x32_bf16 v[132:135], a[172:175], v[116:119], v[132:135]\n"
      "v_and_b32 v9, 0xffff0000, v148\n"
      "v_mfma_f32_16x16x32_bf16 v[136:139], a[176:179], v[36:39], v[12:15]\n"
      "v_lshlrev_b32 v10, 16, v149\n"
      "v_mfma_f32_16x16x32_bf16 v[136:139], a[180:183], v[40:43], v[136:139]\n"
      "v_and_b32 v11, 0xffff0000, v149\n"
      "v_mfma_f32_16x16x32_bf16 v[136:139], a[184:187], v[44:47], v[136:139]\n"
      "v_mfma_f32_16x16x32_bf16 v[136:139], a[188:191], v[60:63], v[136:139]\n"
      "v_cvt_pk_bf16_f32 v212, v132, v133\n"
      "v_mfma_f32_16x16x32_bf16 v[136:139], a[192:195], v[64:67], v[136:139]\n"
      "v_cvt_pk_bf16_f32 v213, v134, v135\n"
      "v_mfma_f32_16x16x32_bf16 v[136:139], a[196:199], v[68:71], v[136:139]\n"
      "v_lshlrev_b32 v12, 16, v150\n"
      "v_mfma_f32_16x16x32_bf16 v[136:139], a[200:203], v[84:87], v[136:139]\n"
      "v_and_b32 v13, 0xffff0000, v150\n"
      "v_mfma_f32_16x16x32_bf16 v[136:139], a[204:207], v[88:91], v[136:139]\n"
      "s_waitcnt lgkmcnt(3)\n"
      "v_mfma_f32_16x16x32_bf16 v[136:139], a[208:211], v[92:95], v[136:139]\n"
      "global_store_dwordx4 v253, v[224:227], s[54:55] offset:768 sc1\n"
      "v_mfma_f32_16x16x32_bf16 v[136:139], a[212:215], v[108:111], v[136:139]\n"
      "ds_write_b64 v245, v[212:213] offset:0\n"
      "v_mfma_f32_16x16x32_bf16 v[136:139], a[216:219], v[112:115], v[136:139]\n"
      "s_add_u32 m0, s17, 0x10e00\n"
      "v_mfma_f32_16x16x32_bf16 v[136:139], a[220:223], v[116:119], v[136:139]\n"
      "global_load_lds_dwordx4 v237, s[52:53] offset:512 nt\n"
      "v_mfma_f32_16x16x32_bf16 v[132:135], a[224:227], v[36:39], v[16:19]\n"
      "v_lshlrev_b32 v14, 16, v151\n"
      "v_mfma_f32_16x16x32_bf16 v[132:135], a[228:231], v[40:43], v[132:135]\n"
      "v_and_b32 v15, 0xffff0000, v151\n"
      "v_mfma_f32_16x16x32_bf16 v[132:135], a[232:235], v[44:47], v[132:135]\n"
      "s_cbranch_vccnz LT23\n"
      "LS23:\n"
      "v_mfma_f32_16x16x32_bf16 v[132:135], a[236:239], v[60:63], v[132:135]\n"
      "s_memrealtime s[84:85]\n"
      "v_mfma_f32_16x16x32_bf16 v[132:135], a[0:3], v[64:67], v[132:135]\n"
      "v_cvt_pk_bf16_f32 v214, v136, v137\n"
      "v_mfma_f32_16x16x32_bf16 v[132:135], a[4:7], v[68:71], v[132:135]\n"
      "v_cvt_pk_bf16_f32 v215, v138, v139\n"
      "v_mfma_f32_16x16x32_bf16 v[132:135], a[8:11], v[84:87], v[132:135]\n"
      "v_lshlrev_b32 v16, 16, v152\n"
      "v_mfma_f32_16x16x32_bf16 v[132:135], a[12:15], v[88:91], v[132:135]\n"
      "s_add_u32 s83, s83, s86\n"
      "v_mfma_f32_16x16x32_bf16 v[132:135], a[16:19], v[92:95], v[132:135]\n"
      "v_and_b32 v17, 0xffff0000, v152\n"
      "v_mfma_f32_16x16x32_bf16 v[132:135], a[20:23], v[108:111], v[132:135]\n"
      "v_lshlrev_b32 v18, 16, v153\n"
      "v_mfma_f32_16x16x32_bf16 v[132:135], v[140:143], v[112:115], v[132:135]\n"
      "s_waitcnt lgkmcnt(0)\n"
      "s_sub_u32 s84, s84, s83\n"
      "v_mfma_f32_16x16x32_bf16 v[132:135], v[156:159], v[116:119], v[132:135]\n"
      "s_cmp_lt_i32 s84, 0\n"
      "s_cselect_b64 vcc, -1, 0\n"
      "s_waitcnt vmcnt(24)\n"
      "s_barrier\n"
      "v_mfma_f32_16x16x32_bf16 v[136:139], v[160:163], v[36:39], v[20:23]\n"
      "ds_read_b128 v[36:39], v232 offset:0\n"
      "ds_write_b64 v245, v[214:215] offset:8\n"
      "v_mfma_f32_16x16x32_bf16 v[136:139], v[164:167], v[40:43], v[136:139]\n"
      "ds_read_b128 v[40:43], v233 offset:0\n"
      "v_mfma_f32_16x16x32_bf16 v[136:139], v[180:183], v[44:47], v[136:139]\n"
      "ds_read_b128 v[44:47], v234 offset:0\n"
      "v_mfma_f32_16x16x32_bf16 v[136:139], v[184:187], v[60:63], v[136:139]\n"
      "ds_read_b128 v[60:63], v235 offset:0\n"
      "v_cvt_pk_bf16_f32 v208, v132, v133\n"
      "v_mfma_f32_16x16x32_bf16 v[136:139], v[188:191], v[64:67], v[136:139]\n"
      "ds_read_b128 v[64:67], v232 offset:8192\n"
      "v_cvt_pk_bf16_f32 v209, v134, v135\n"
      "v_mfma_f32_16x16x32_bf16 v[136:139], v[204:207], v[68:71], v[136:139]\n"
      "ds_read_b128 v[68:71], v233 offset:8192\n"
      "v_mfma_f32_16x16x32_bf16 v[136:139], a[24:27], v[84:87], v[136:139]\n"
      "ds_read_b128 v[84:87], v234 offset:8192\n"
      "v_mfma_f32_16x16x32_bf16 v[136:139], a[28:31], v[88:91], v[136:139]\n"
      "ds_read_b128 v[88:91], v235 offset:8192\n"
      "v_mfma_f32_16x16x32_bf16 v[136:139], a[32:35], v[92:95], v[136:139]\n"
      "ds_read_b128 v[92:95], v232 offset:16384\n"
      "v_mfma_f32_16x16x32_bf16 v[136:139], a[36:39], v[108:111], v[136:139]\n"
      "ds_read_b128 v[108:111], v233 offset:16384\n"
      "v_mfma_f32_16x16x32_bf16 v[136:139], a[40:43], v[112:115], v[136:139]\n"
      "ds_read_b128 v[112:115], v234 offset:16384\n"
      "v_mfma_f32_16x16x32_bf16 v[136:139], a[44:47], v[116:119], v[136:139]\n"
      "ds_read_b128 v[116:119], v235 offset:16384\n"
      "s_waitcnt lgkmcnt(8)\n"
      "v_mfma_f32_16x16x32_bf16 v[132:135], a[240:243], v[36:39], v[0:3]\n"
      "ds_write_b64 v244, v[208:209] offset:128\n"
      "s_cbranch_vccnz LT24\n"
      "LS24:\n"
      "v_mfma_f32_16x16x32_bf16 v[132:135], a[244:247], v[40:43], v[132:135]\n"
      "v_and_b32 v19, 0xffff0000, v153\n"
      "v_mfma_f32_16x16x32_bf16 v[132:135], a[248:251], v[44:47], v[132:135]\n"
      "v_lshlrev_b32 v20, 16, v154\n"
      "v_mfma_f32_16x16x32_bf16 v[132:135], a[252:255], v[60:63], v[132:135]\n"
      "v_and_b32 v21, 0xffff0000, v154\n"
      "s_waitcnt lgkmcnt(5)\n"
      "v_mfma_f32_16x16x32_bf16 v[132:135], a[48:51], v[64:67], v[132:135]\n"
      "v_lshlrev_b32 v22, 16, v155\n"
      "v_mfma_f32_16x16x32_bf16 v[132:135], a[52:55], v[68:71], v[132:135]\n"
      "v_and_b32 v23, 0xffff0000, v155\n"
      "v_mfma_f32_16x16x32_bf16 v[132:135], a[56:59], v[84:87], v[132:135]\n"
      "v_cvt_pk_bf16_f32 v210, v136, v137\n"
      "v_mfma_f32_16x16x32_bf16 v[132:135], a[60:63], v[88:91], v[132:135]\n"
      "v_cvt_pk_bf16_f32 v211, v138, v139\n"
      "s_waitcnt lgkmcnt(1)\n"
      "v_mfma_f32_16x16x32_bf16 v[132:135], a[64:67], v[92:95], v[132:135]\n"
      "s_add_u32 s54, s44, s37\n"
      "s_addc_u32 s55, s45, 0\n"
      "v_mfma_f32_16x16x32_bf16 v[132:135], a[68:71], v[108:111], v[132:135]\n"
      "ds_read_b128 v[216:219], v246 offset:24576\n"
      "v_mfma_f32_16x16x32_bf16 v[132:135], a[72:75], v[112:115], v[132:135]\n"
      "ds_read_b128 v[220:223], v246 offset:25600\n"
      "v_mfma_f32_16x16x32_bf16 v[132:135], a[76:79], v[116:119], v[132:135]\n"
      "ds_read_b128 v[224:227], v246 offset:26624\n"
      "v_mfma_f32_16x16x32_bf16 v[136:139], a[80:83], v[36:39], v[4:7]\n"
      "ds_write_b64 v244, v[210:211] offset:136\n"
      "v_mfma_f32_16x16x32_bf16 v[136:139], a[84:87], v[40:43], v[136:139]\n"
      "v_mfma_f32_16x16x32_bf16 v[136:139], a[88:91], v[44:47], v[136:139]\n"
      "v_mfma_f32_16x16x32_bf16 v[136:139], a[92:95], v[60:63], v[136:139]\n"
      "v_cvt_pk_bf16_f32 v208, v132, v133\n"
      "v_mfma_f32_16x16x32_bf16 v[136:139], a[96:99], v[64:67], v[136:139]\n"
      "v_cvt_pk_bf16_f32 v209, v134, v135\n"
      "v_mfma_f32_16x16x32_bf16 v[136:139], a[100:103], v[68:71], v[136:139]\n"
      "s_waitcnt lgkmcnt(3)\n"
      "v_mfma_f32_16x16x32_bf16 v[136:139], a[104:107], v[84:87], v[136:139]\n"
      "global_store_dwordx4 v247, v[216:219], s[54:55] offset:0 sc1\n"
      "v_mfma_f32_16x16x32_bf16 v[136:139], a[108:111], v[88:91], v[136:139]\n"
      "s_add_u32 s52, s42, s39\n"
      "s_addc_u32 s53, s43, 0\n"
      "v_mfma_f32_16x16x32_bf16 v[136:139], a[112:115], v[92:95], v[136:139]\n"
      "s_add_u32 m0, s17, 0x12000\n"
      "v_mfma_f32_16x16x32_bf16 v[136:139], a[116:119], v[108:111], v[136:139]\n"
      "ds_write_b64 v244, v[208:209] offset:12288\n"
      "v_mfma_f32_16x16x32_bf16 v[136:139], a[120:123], v[112:115], v[136:139]\n"
      "global_load_lds_dwordx4 v236, s[52:53] offset:0 nt\n"
      "v_mfma_f32_16x16x32_bf16 v[136:139], a[124:127], v[116:119], v[136:139]\n"
      "v_mfma_f32_16x16x32_bf16 v[132:135], a[128:131], v[36:39], v[8:11]\n"
      "s_cbranch_vccnz LT25\n"
      "LS25:\n"
      "v_mfma_f32_16x16x32_bf16 v[132:135], a[132:135], v[40:43], v[132:135]\n"
      "v_mfma_f32_16x16x32_bf16 v[132:135], a[136:139], v[44:47], v[132:135]\n"
      "v_mfma_f32_16x16x32_bf16 v[132:135], a[140:143], v[60:63], v[132:135]\n"
      "v_cvt_pk_bf16_f32 v210, v136, v137\n"
      "v_mfma_f32_16x16x32_bf16 v[132:135], a[144:147], v[64:67], v[132:135]\n"
      "v_cvt_pk_bf16_f32 v211, v138, v139\n"
      "v_mfma_f32_16x16x32_bf16 v[132:135], a[148:151], v[68:71], v[132:135]\n"
      "v_mfma_f32_16x16x32_bf16 v[132:135], a[152:155], v[84:87], v[132:135]\n"
      "s_waitcnt lgkmcnt(3)\n"
      "v_mfma_f32_16x16x32_bf16 v[132:135], a[156:159], v[88:91], v[132:135]\n"
      "global_store_dwordx4 v252, v[220:223], s[54:55] offset:0 sc1\n"
      "v_mfma_f32_16x16x32_bf16 v[132:135], a[160:163], v[92:95], v[132:135]\n"
      "s_add_u32 m0, s17, 0x13f00\n"
      "v_mfma_f32_16x16x32_bf16 v[132:135], a[164:167], v[108:111], v[132:135]\n"
      "ds_write_b64 v244, v[210:211] offset:12296\n"
      "v_mfma_f32_16x16x32_bf16 v[132:135], a[168:171], v[112:115], v[132:135]\n"
      "global_load_lds_dwordx4 v236, s[52:53] offset:256 nt\n"
      "v_mfma_f32_16x16x32_bf16 v[132:135], a[172:175], v[116:119], v[132:135]\n"
      "v_mfma_f32_16x16x32_bf16 v[136:139], a[176:179], v[36:39], v[12:15]\n"
      "v_mfma_f32_16x16x32_bf16 v[136:139], a[180:183], v[40:43], v[136:139]\n"
      "v_mfma_f32_16x16x32_bf16 v[136:139], a[184:187], v[44:47], v[136:139]\n"
      "v_mfma_f32_16x16x32_bf16 v[136:139], a[188:191], v[60:63], v[136:139]\n"
      "v_cvt_pk_bf16_f32 v212, v132, v133\n"
      "v_mfma_f32_16x16x32_bf16 v[136:139], a[192:195], v[64:67], v[136:139]\n"
      "v_cvt_pk_bf16_f32 v213, v134, v135\n"
      "v_mfma_f32_16x16x32_bf16 v[136:139], a[196:199], v[68:71], v[136:139]\n"
      "v_mfma_f32_16x16x32_bf16 v[136:139], a[200:203], v[84:87], v[136:139]\n"
      "v_mfma_f32_16x16x32_bf16 v[136:139], a[204:207], v[88:91], v[136:139]\n"
      "s_waitcnt lgkmcnt(3)\n"
      "v_mfma_f32_16x16x32_bf16 v[136:139], a[208:211], v[92:95], v[136:139]\n"
      "global_store_dwordx4 v253, v[224:227], s[54:55] offset:0 sc1\n"
      "v_mfma_f32_16x16x32_bf16 v[136:139], a[212:215], v[108:111], v[136:139]\n"
      "ds_write_b64 v245, v[212:213] offset:12288\n"
      "v_mfma_f32_16x16x32_bf16 v[136:139], a[216:219], v[112:115], v[136:139]\n"
      "s_add_u32 m0, s17, 0x15e00\n"
      "v_mfma_f32_16x16x32_bf16 v[136:139], a[220:223], v[116:119], v[136:139]\n"
      "global_load_lds_dwordx4 v236, s[52:53] offset:512 nt\n"
      "v_mfma_f32_16x16x32_bf16 v[132:135], a[224:227], v[36:39], v[16:19]\n"
      "s_cbranch_vccnz LT26\n"
      "LS26:\n"
      "v_mfma_f32_16x16x32_bf16 v[132:135], a[228:231], v[40:43], v[132:135]\n"
      "v_mfma_f32_16x16x32_bf16 v[132:135], a[232:235], v[44:47], v[132:135]\n"
      "v_mfma_f32_16x16x32_bf16 v[132:135], a[236:239], v[60:63], v[132:135]\n"
      "v_cvt_pk_bf16_f32 v214, v136, v137\n"
      "v_mfma_f32_16x16x32_bf16 v[132:135], a[0:3], v[64:67], v[132:135]\n"
      "v_cvt_pk_bf16_f32 v215, v138, v139\n"
      "v_mfma_f32_16x16x32_bf16 v[132:135], a[4:7], v[68:71], v[132:135]\n"
      "v_mfma_f32_16x16x32_bf16 v[132:135], a[8:11], v[84:87], v[132:135]\n"
      "v_mfma_f32_16x16x32_bf16 v[132:135], a[12:15], v[88:91], v[132:135]\n"
      "v_mfma_f32_16x16x32_bf16 v[132:135], a[16:19], v[92:95], v[132:135]\n"
      "v_mfma_f32_16x16x32_bf16 v[132:135], a[20:23], v[108:111], v[132:135]\n"
      "ds_write_b64 v245, v[214:215] offset:12296\n"
      "v_mfma_f32_16x16x32_bf16 v[132:135], v[140:143], v[112:115], v[132:135]\n"
      "v_mfma_f32_16x16x32_bf16 v[132:135], v[156:159], v[116:119], v[132:135]\n"
      "s_waitcnt lgkmcnt(4)\n"
      "s_barrier\n"
      "v_mfma_f32_16x16x32_bf16 v[136:139], v[160:163], v[36:39], v[20:23]\n"
      "ds_read_b128 v[36:39], v232 offset:256\n"
      "v_mfma_f32_16x16x32_bf16 v[136:139], v[164:167], v[40:43], v[136:139]\n"
      "ds_read_b128 v[40:43], v233 offset:256\n"
      "v_mfma_f32_16x16x32_bf16 v[136:139], v[180:183], v[44:47], v[136:139]\n"
      "ds_read_b128 v[44:47], v234 offset:256\n"
      "v_mfma_f32_16x16x32_bf16 v[136:139], v[184:187], v[60:63], v[136:139]\n"
      "ds_read_b128 v[60:63], v235 offset:256\n"
      "v_cvt_pk_bf16_f32 v208, v132, v133\n"
      "v_mfma_f32_16x16x32_bf16 v[136:139], v[188:191], v[64:67], v[136:139]\n"
      "ds_read_b128 v[64:67], v232 offset:8448\n"
      "v_cvt_pk_bf16_f32 v209, v134, v135\n"
      "v_mfma_f32_16x16x32_bf16 v[136:139], v[204:207], v[68:71], v[136:139]\n"
      "ds_read_b128 v[68:71], v233 offset:8448\n"
      "v_mfma_f32_16x16x32_bf16 v[136:139], a[24:27], v[84:87], v[136:139]\n"
      "ds_read_b128 v[84:87], v234 offset:8448\n"
      "v_mfma_f32_16x16x32_bf16 v[136:139], a[28:31], v[88:91], v[136:139]\n"
      "ds_read_b128 v[88:91], v235 offset:8448\n"
      "v_mfma_f32_16x16x32_bf16 v[136:139], a[32:35], v[92:95], v[136:139]\n"
      "ds_read_b128 v[92:95], v232 offset:16640\n"
      "v_mfma_f32_16x16x32_bf16 v[136:139], a[36:39], v[108:111], v[136:139]\n"
      "ds_read_b128 v[108:111], v233 offset:16640\n"
      "v_mfma_f32_16x16x32_bf16 v[136:139], a[40:43], v[112:115], v[136:139]\n"
      "ds_read_b128 v[112:115], v234 offset:16640\n"
      "v_mfma_f32_16x16x32_bf16 v[136:139], a[44:47], v[116:119], v[136:139]\n"
      "ds_read_b128 v[116:119], v235 offset:16640\n"
      "s_waitcnt lgkmcnt(8)\n"
      "v_mfma_f32_16x16x32_bf16 v[132:135], a[240:243], v[36:39], v[0:3]\n"
      "ds_write_b64 v244, v[208:209] offset:12416\n"
      "v_mfma_f32_16x16x32_bf16 v[132:135], a[244:247], v[40:43], v[132:135]\n"
      "s_cbranch_vccnz LT27\n"
      "LS27:\n"
      "v_mfma_f32_16x16x32_bf16 v[132:135], a[248:251], v[44:47], v[132:135]\n"
      "v_mfma_f32_16x16x32_bf16 v[132:135], a[252:255], v[60:63], v[132:135]\n"
      "v_cvt_pk_bf16_f32 v210, v136, v137\n"
      "s_waitcnt lgkmcnt(5)\n"
      "v_mfma_f32_16x16x32_bf16 v[132:135], a[48:51], v[64:67], v[132:135]\n"
      "v_cvt_pk_bf16_f32 v211, v138, v139\n"
      "v_mfma_f32_16x16x32_bf16 v[132:135], a[52:55], v[68:71], v[132:135]\n"
      "v_mfma_f32_16x16x32_bf16 v[132:135], a[56:59], v[84:87], v[132:135]\n"
      "ds_read_b128 v[216:219], v246 offset:0\n"
      "v_mfma_f32_16x16x32_bf16 v[132:135], a[60:63], v[88:91], v[132:135]\n"
      "ds_read_b128 v[220:223], v246 offset:1024\n"
      "s_waitcnt lgkmcnt(3)\n"
      "v_mfma_f32_16x16x32_bf16 v[132:135], a[64:67], v[92:95], v[132:135]\n"
      "ds_read_b128 v[224:227], v246 offset:2048\n"
      "v_mfma_f32_16x16x32_bf16 v[132:135], a[68:71], v[108:111], v[132:135]\n"
      "ds_write_b64 v244, v[210:211] offset:12424\n"
      "v_mfma_f32_16x16x32_bf16 v[132:135], a[72:75], v[112:115], v[132:135]\n"
      "v_mfma_f32_16x16x32_bf16 v[132:135], a[76:79], v[116:119], v[132:135]\n"
      "v_mfma_f32_16x16x32_bf16 v[136:139], a[80:83], v[36:39], v[4:7]\n"
      "v_mfma_f32_16x16x32_bf16 v[136:139], a[84:87], v[40:43], v[136:139]\n"
      "v_mfma_f32_16x16x32_bf16 v[136:139], a[88:91], v[44:47], v[136:139]\n"
      "v_mfma_f32_16x16x32_bf16 v[136:139], a[92:95], v[60:63], v[136:139]\n"
      "v_cvt_pk_bf16_f32 v208, v132, v133\n"
      "v_mfma_f32_16x16x32_bf16 v[136:139], a[96:99], v[64:67], v[136:139]\n"
      "v_cvt_pk_bf16_f32 v209, v134, v135\n"
      "v_mfma_f32_16x16x32_bf16 v[136:139], a[100:103], v[68:71], v[136:139]\n"
      "s_waitcnt lgkmcnt(3)\n"
      "v_mfma_f32_16x16x32_bf16 v[136:139], a[104:107], v[84:87], v[136:139]\n"
      "global_store_dwordx4 v247, v[216:219], s[54:55] offset:768 sc1\n"
      "v_mfma_f32_16x16x32_bf16 v[136:139], a[108:111], v[88:91], v[136:139]\n"
      "s_add_u32 m0, s17, 0x13000\n"
      "v_mfma_f32_16x16x32_bf16 v[136:139], a[112:115], v[92:95], v[136:139]\n"
      "global_load_lds_dwordx4 v237, s[52:53] offset:0 nt\n"
      "v_mfma_f32_16x16x32_bf16 v[136:139], a[116:119], v[108:111], v[136:139]\n"
      "ds_write_b64 v244, v[208:209] offset:24576\n"
      "v_mfma_f32_16x16x32_bf16 v[136:139], a[120:123], v[112:115], v[136:139]\n"
      "v_mfma_f32_16x16x32_bf16 v[136:139], a[124:127], v[116:119], v[136:139]\n"
      "v_mfma_f32_16x16x32_bf16 v[132:135], a[128:131], v[36:39], v[8:11]\n"
      "s_cbranch_vccnz LT28\n"
      "LS28:\n"
      "v_mfma_f32_16x16x32_bf16 v[132:135], a[132:135], v[40:43], v[132:135]\n"
      "v_mfma_f32_16x16x32_bf16 v[132:135], a[136:139], v[44:47], v[132:135]\n"
      "v_mfma_f32_16x16x32_bf16 v[132:135], a[140:143], v[60:63], v[132:135]\n"
      "v_cvt_pk_bf16_f32 v210, v136, v137\n"
      "v_mfma_f32_16x16x32_bf16 v[132:135], a[144:147], v[64:67], v[132:135]\n"
      "v_cvt_pk_bf16_f32 v211, v138, v139\n"
      "v_mfma_f32_16x16x32_bf16 v[132:135], a[148:151], v[68:71], v[132:135]\n"
      "v_mfma_f32_16x16x32_bf16 v[132:135], a[152:155], v[84:87], v[132:135]\n"
      "s_waitcnt lgkmcnt(3)\n"
      "v_mfma_f32_16x16x32_bf16 v[132:135], a[156:159], v[88:91], v[132:135]\n"
      "global_store_dwordx4 v252, v[220:223], s[54:55] offset:768 sc1\n"
      "v_mfma_f32_16x16x32_bf16 v[132:135], a[160:163], v[92:95], v[132:135]\n"
      "s_add_u32 m0, s17, 0x14f00\n"
      "v_mfma_f32_16x16x32_bf16 v[132:135], a[164:167], v[108:111], v[132:135]\n"
      "ds_write_b64 v244, v[210:211] offset:24584\n"
      "v_mfma_f32_16x16x32_bf16 v[132:135], a[168:171], v[112:115], v[132:135]\n"
      "global_load_lds_dwordx4 v237, s[52:53] offset:256 nt\n"
      "v_mfma_f32_16x16x32_bf16 v[132:135], a[172:175], v[116:119], v[132:135]\n"
      "v_mfma_f32_16x16x32_bf16 v[136:139], a[176:179], v[36:39], v[12:15]\n"
      "v_mfma_f32_16x16x32_bf16 v[136:139], a[180:183], v[40:43], v[136:139]\n"
      "v_mfma_f32_16x16x32_bf16 v[136:139], a[184:187], v[44:47], v[136:139]\n"
      "v_mfma_f32_16x16x32_bf16 v[136:139], a[188:191], v[60:63], v[136:139]\n"
      "v_cvt_pk_bf16_f32 v212, v132, v133\n"
      "v_mfma_f32_16x16x32_bf16 v[136:139], a[192:195], v[64:67], v[136:139]\n"
      "v_cvt_pk_bf16_f32 v213, v134, v135\n"
      "v_mfma_f32_16x16x32_bf16 v[136:139], a[196:199], v[68:71], v[136:139]\n"
      "v_mfma_f32_16x16x32_bf16 v[136:139], a[200:203], v[84:87], v[136:139]\n"
      "v_mfma_f32_16x16x32_bf16 v[136:139], a[204:207], v[88:91], v[136:139]\n"
      "s_waitcnt lgkmcnt(3)\n"
      "v_mfma_f32_16x16x32_bf16 v[136:139], a[208:211], v[92:95], v[136:139]\n"
      "global_store_dwordx4 v253, v[224:227], s[54:55] offset:768 sc1\n"
      "v_mfma_f32_16x16x32_bf16 v[136:139], a[212:215], v[108:111], v[136:139]\n"
      "ds_write_b64 v245, v[212:213] offset:24576\n"
      "v_mfma_f32_16x16x32_bf16 v[136:139], a[216:219], v[112:115], v[136:139]\n"
      "s_add_u32 m0, s17, 0x16e00\n"
      "v_mfma_f32_16x16x32_bf16 v[136:139], a[220:223], v[116:119], v[136:139]\n"
      "global_load_lds_dwordx4 v237, s[52:53] offset:512 nt\n"
      "v_mfma_f32_16x16x32_bf16 v[132:135], a[224:227], v[36:39], v[16:19]\n"
      "s_cbranch_vccnz LT29\n"
      "LS29:\n"
      "v_mfma_f32_16x16x32_bf16 v[132:135], a[228:231], v[40:43], v[132:135]\n"
      "v_mfma_f32_16x16x32_bf16 v[132:135], a[232:235], v[44:47], v[132:135]\n"
      "v_mfma_f32_16x16x32_bf16 v[132:135], a[236:239], v[60:63], v[132:135]\n"
      "s_memrealtime s[84:85]\n"
      "v_mfma_f32_16x16x32_bf16 v[132:135], a[0:3], v[64:67], v[132:135]\n"
      "v_cvt_pk_bf16_f32 v214, v136, v137\n"
      "v_mfma_f32_16x16x32_bf16 v[132:135], a[4:7], v[68:71], v[132:135]\n"
      "v_cvt_pk_bf16_f32 v215, v138, v139\n"
      "v_mfma_f32_16x16x32_bf16 v[132:135], a[8:11], v[84:87], v[132:135]\n"
      "v_mfma_f32_16x16x32_bf16 v[132:135], a[12:15], v[88:91], v[132:135]\n"
      "s_add_u32 s83, s83, s86\n"
      "v_mfma_f32_16x16x32_bf16 v[132:135], a[16:19], v[92:95], v[132:135]\n"
      "v_mfma_f32_16x16x32_bf16 v[132:135], a[20:23], v[108:111], v[132:135]\n"
      "v_mfma_f32_16x16x32_bf16 v[132:135], v[140:143], v[112:115], v[132:135]\n"
      "s_waitcnt lgkmcnt(0)\n"
      "s_sub_u32 s84, s84, s83\n"
      "v_mfma_f32_16x16x32_bf16 v[132:135], v[156:159], v[116:119], v[132:135]\n"
      "s_cmp_lt_i32 s84, 0\n"
      "s_cselect_b64 vcc, -1, 0\n"
      "s_waitcnt vmcnt(24)\n"
      "s_barrier\n"
      "v_mfma_f32_16x16x32_bf16 v[136:139], v[160:163], v[36:39], v[20:23]\n"
      "ds_read_b128 v[36:39], v232 offset:24576\n"
      "ds_write_b64 v245, v[214:215] offset:24584\n"
      "v_mfma_f32_16x16x32_bf16 v[136:139], v[164:167], v[40:43], v[136:139]\n"
      "ds_read_b128 v[40:43], v233 offset:24576\n"
      "v_mfma_f32_16x16x32_bf16 v[136:139], v[180:183], v[44:47], v[136:139]\n"
      "ds_read_b128 v[44:47], v234 offset:24576\n"
      "v_mfma_f32_16x16x32_bf16 v[136:139], v[184:187], v[60:63], v[136:139]\n"
      "ds_read_b128 v[60:63], v235 offset:24576\n"
      "v_cvt_pk_bf16_f32 v208, v132, v133\n"
      "v_mfma_f32_16x16x32_bf16 v[136:139], v[188:191], v[64:67], v[136:139]\n"
      "ds_read_b128 v[64:67], v232 offset:32768\n"
      "v_cvt_pk_bf16_f32 v209, v134, v135\n"
      "v_mfma_f32_16x16x32_bf16 v[136:139], v[204:207], v[68:71], v[136:139]\n"
      "ds_read_b128 v[68:71], v233 offset:32768\n"
      "v_mfma_f32_16x16x32_bf16 v[136:139], a[24:27], v[84:87], v[136:139]\n"
      "ds_read_b128 v[84:87], v234 offset:32768\n"
      "v_mfma_f32_16x16x32_bf16 v[136:139], a[28:31], v[88:91], v[136:139]\n"
      "ds_read_b128 v[88:91], v235 offset:32768\n"
      "v_mfma_f32_16x16x32_bf16 v[136:139], a[32:35], v[92:95], v[136:139]\n"
      "ds_read_b128 v[92:95], v232 offset:40960\n"
      "v_mfma_f32_16x16x32_bf16 v[136:139], a[36:39], v[108:111], v[136:139]\n"
      "ds_read_b128 v[108:111], v233 offset:40960\n"
      "v_mfma_f32_16x16x32_bf16 v[136:139], a[40:43], v[112:115], v[136:139]\n"
      "ds_read_b128 v[112:115], v234 offset:40960\n"
      "v_mfma_f32_16x16x32_bf16 v[136:139], a[44:47], v[116:119], v[136:139]\n"
      "ds_read_b128 v[116:119], v235 offset:40960\n"
      "s_waitcnt lgkmcnt(8)\n"
      "v_mfma_f32_16x16x32_bf16 v[132:135], a[240:243], v[36:39], v[0:3]\n"
      "ds_write_b64 v244, v[208:209] offset:24704\n"
      "v_mfma_f32_16x16x32_bf16 v[132:135], a[244:247], v[40:43], v[132:135]\n"
      "s_cbranch_vccnz LT30\n"
      "LS30:\n"
      "v_mfma_f32_16x16x32_bf16 v[132:135], a[248:251], v[44:47], v[132:135]\n"
      "v_mfma_f32_16x16x32_bf16 v[132:135], a[252:255], v[60:63], v[132:135]\n"
      "v_cvt_pk_bf16_f32 v210, v136, v137\n"
      "s_lshl_b32 s79, s95, 1\n"
      "s_waitcnt lgkmcnt(5)\n"
      "v_mfma_f32_16x16x32_bf16 v[132:135], a[48:51], v[64:67], v[132:135]\n"
      "v_cvt_pk_bf16_f32 v211, v138, v139\n"
      "s_mul_i32 s78, s97, s19\n"
      "v_mfma_f32_16x16x32_bf16 v[132:135], a[52:55], v[68:71], v[132:135]\n"
      "v_mad_i32_i24 v252, v251, s79, v252\n"
      "v_add_u32 v236, s78, v236\n"
      "v_mfma_f32_16x16x32_bf16 v[132:135], a[56:59], v[84:87], v[132:135]\n"
      "ds_read_b128 v[24:27], v246 offset:12288\n"
      "s_mul_i32 s79, s79, s100\n"
      "v_mfma_f32_16x16x32_bf16 v[132:135], a[60:63], v[88:91], v[132:135]\n"
      "ds_read_b128 v[28:31], v246 offset:13312\n"
      "v_add_u32 v247, s79, v247\n"
      "s_waitcnt lgkmcnt(3)\n"
      "v_mfma_f32_16x16x32_bf16 v[132:135], a[64:67], v[92:95], v[132:135]\n"
      "ds_read_b128 v[32:35], v246 offset:14336\n"
      "s_mul_i32 s79, s79, s101\n"
      "v_mfma_f32_16x16x32_bf16 v[132:135], a[68:71], v[108:111], v[132:135]\n"
      "ds_write_b64 v244, v[210:211] offset:24712\n"
      "s_mul_i32 s78, s78, s99\n"
      "v_mfma_f32_16x16x32_bf16 v[132:135], a[72:75], v[112:115], v[132:135]\n"
      "v_add_u32 v253, s79, v253\n"
      "v_add_u32 v237, s78, v237\n"
      "v_mfma_f32_16x16x32_bf16 v[132:135], a[76:79], v[116:119], v[132:135]\n"
      "s_add_u32 s54, s30, s38\n"
      "s_addc_u32 s55, s31, 0\n"
      "v_mfma_f32_16x16x32_bf16 v[136:139], a[80:83], v[36:39], v[4:7]\n"
      "s_waitcnt lgkmcnt(3)\n"
      "v_mfma_f32_16x16x32_bf16 v[136:139], a[84:87], v[40:43], v[136:139]\n"
      "global_store_dwordx4 v247, v[24:27], s[54:55] offset:0 sc1\n"
      "v_mfma_f32_16x16x32_bf16 v[136:139], a[88:91], v[44:47], v[136:139]\n"
      "v_mfma_f32_16x16x32_bf16 v[136:139], a[92:95], v[60:63], v[136:139]\n"
      "v_cvt_pk_bf16_f32 v208, v132, v133\n"
      "v_mfma_f32_16x16x32_bf16 v[136:139], a[96:99], v[64:67], v[136:139]\n"
      "v_cvt_pk_bf16_f32 v209, v134, v135\n"
      "v_mfma_f32_16x16x32_bf16 v[136:139], a[100:103], v[68:71], v[136:139]\n"
      "v_mfma_f32_16x16x32_bf16 v[136:139], a[104:107], v[84:87], v[136:139]\n"
      "v_mfma_f32_16x16x32_bf16 v[136:139], a[108:111], v[88:91], v[136:139]\n"
      "s_add_u32 s52, s20, s40\n"
      "s_addc_u32 s53, s21, 0\n"
      "v_mfma_f32_16x16x32_bf16 v[136:139], a[112:115], v[92:95], v[136:139]\n"
      "s_add_u32 m0, s17, 0x0\n"
      "v_mfma_f32_16x16x32_bf16 v[136:139], a[116:119], v[108:111], v[136:139]\n"
      "ds_write_b64 v244, v[208:209] offset:0\n"
      "v_mfma_f32_16x16x32_bf16 v[136:139], a[120:123], v[112:115], v[136:139]\n"
      "global_load_lds_dwordx4 v236, s[52:53] offset:0 nt\n"
      "v_mfma_f32_16x16x32_bf16 v[136:139], a[124:127], v[116:119], v[136:139]\n"
      "v_mfma_f32_16x16x32_bf16 v[132:135], a[128:131], v[36:39], v[8:11]\n"
      "s_cbranch_vccnz LT31\n"
      "LS31:\n"
      "v_mfma_f32_16x16x32_bf16 v[132:135], a[132:135], v[40:43], v[132:135]\n"
      "v_mfma_f32_16x16x32_bf16 v[132:135], a[136:139], v[44:47], v[132:135]\n"
      "v_mfma_f32_16x16x32_bf16 v[132:135], a[140:143], v[60:63], v[132:135]\n"
      "v_cvt_pk_bf16_f32 v210, v136, v137\n"
      "v_mfma_f32_16x16x32_bf16 v[132:135], a[144:147], v[64:67], v[132:135]\n"
      "v_cvt_pk_bf16_f32 v211, v138, v139\n"
      "v_mfma_f32_16x16x32_bf16 v[132:135], a[148:151], v[68:71], v[132:135]\n"
      "v_mfma_f32_16x16x32_bf16 v[132:135], a[152:155], v[84:87], v[132:135]\n"
      "s_waitcnt lgkmcnt(3)\n"
      "v_mfma_f32_16x16x32_bf16 v[132:135], a[156:159], v[88:91], v[132:135]\n"
      "global_store_dwordx4 v252, v[28:31], s[54:55] offset:0 sc1\n"
      "v_mfma_f32_16x16x32_bf16 v[132:135], a[160:163], v[92:95], v[132:135]\n"
      "s_add_u32 m0, s17, 0x1f00\n"
      "v_mfma_f32_16x16x32_bf16 v[132:135], a[164:167], v[108:111], v[132:135]\n"
      "ds_write_b64 v244, v[210:211] offset:8\n"
      "v_mfma_f32_16x16x32_bf16 v[132:135], a[168:171], v[112:115], v[132:135]\n"
      "global_load_lds_dwordx4 v236, s[52:53] offset:256 nt\n"
      "v_mfma_f32_16x16x32_bf16 v[132:135], a[172:175], v[116:119], v[132:135]\n"
      "v_mfma_f32_16x16x32_bf16 v[136:139], a[176:179], v[36:39], v[12:15]\n"
      "v_mfma_f32_16x16x32_bf16 v[136:139], a[180:183], v[40:43], v[136:139]\n"
      "v_mfma_f32_16x16x32_bf16 v[136:139], a[184:187], v[44:47], v[136:139]\n"
      "v_mfma_f32_16x16x32_bf16 v[136:139], a[188:191], v[60:63], v[136:139]\n"
      "v_cvt_pk_bf16_f32 v212, v132, v133\n"
      "v_mfma_f32_16x16x32_bf16 v[136:139], a[192:195], v[64:67], v[136:139]\n"
      "v_cvt_pk_bf16_f32 v213, v134, v135\n"
      "v_mfma_f32_16x16x32_bf16 v[136:139], a[196:199], v[68:71], v[136:139]\n"
      "v_mfma_f32_16x16x32_bf16 v[136:139], a[200:203], v[84:87], v[136:139]\n"
      "v_mfma_f32_16x16x32_bf16 v[136:139], a[204:207], v[88:91], v[136:139]\n"
      "s_waitcnt lgkmcnt(3)\n"
      "v_mfma_f32_16x16x32_bf16 v[136:139], a[208:211], v[92:95], v[136:139]\n"
      "global_store_dwordx4 v253, v[32:35], s[54:55] offset:0 sc1\n"
      "v_mfma_f32_16x16x32_bf16 v[136:139], a[212:215], v[108:111], v[136:139]\n"
      "ds_write_b64 v245, v[212:213] offset:0\n"
      "v_mfma_f32_16x16x32_bf16 v[136:139], a[216:219], v[112:115], v[136:139]\n"
      "s_add_u32 m0, s17, 0x3e00\n"
      "v_mfma_f32_16x16x32_bf16 v[136:139], a[220:223], v[116:119], v[136:139]\n"
      "global_load_lds_dwordx4 v236, s[52:53] offset:512 nt\n"
      "v_mfma_f32_16x16x32_bf16 v[132:135], a[224:227], v[36:39], v[16:19]\n"
      "s_cbranch_vccnz LT32\n"
      "LS32:\n"
      "v_mfma_f32_16x16x32_bf16 v[132:135], a[228:231], v[40:43], v[132:135]\n"
      "v_mfma_f32_16x16x32_bf16 v[132:135], a[232:235], v[44:47], v[132:135]\n"
      "v_mfma_f32_16x16x32_bf16 v[132:135], a[236:239], v[60:63], v[132:135]\n"
      "v_cvt_pk_bf16_f32 v214, v136, v137\n"
      "v_mfma_f32_16x16x32_bf16 v[132:135], a[0:3], v[64:67], v[132:135]\n"
      "v_cvt_pk_bf16_f32 v215, v138, v139\n"
      "v_mfma_f32_16x16x32_bf16 v[132:135], a[4:7], v[68:71], v[132:135]\n"
      "v_mfma_f32_16x16x32_bf16 v[132:135], a[8:11], v[84:87], v[132:135]\n"
      "v_mfma_f32_16x16x32_bf16 v[132:135], a[12:15], v[88:91], v[132:135]\n"
      "v_mfma_f32_16x16x32_bf16 v[132:135], a[16:19], v[92:95], v[132:135]\n"
      "LPD14:\n"
      "s_cbranch_execz LRET\n"
      "v_mfma_f32_16x16x32_bf16 v[132:135], a[20:23], v[108:111], v[132:135]\n"
      "ds_write_b64 v245, v[214:215] offset:8\n"
      "v_mfma_f32_16x16x32_bf16 v[132:135], v[140:143], v[112:115], v[132:135]\n"
      "v_mfma_f32_16x16x32_bf16 v[132:135], v[156:159], v[116:119], v[132:135]\n"
      "s_waitcnt lgkmcnt(4)\n"
      "s_barrier\n"
      "v_mfma_f32_16x16x32_bf16 v[136:139], v[160:163], v[36:39], v[20:23]\n"
      "ds_read_b128 v[36:39], v232 offset:24832\n"
      "v_mfma_f32_16x16x32_bf16 v[136:139], v[164:167], v[40:43], v[136:139]\n"
      "ds_read_b128 v[40:43], v233 offset:24832\n"
      "v_mfma_f32_16x16x32_bf16 v[136:139], v[180:183], v[44:47], v[136:139]\n"
      "ds_read_b128 v[44:47], v234 offset:24832\n"
      "v_mfma_f32_16x16x32_bf16 v[136:139], v[184:187], v[60:63], v[136:139]\n"
      "ds_read_b128 v[60:63], v235 offset:24832\n"
      "v_cvt_pk_bf16_f32 v208, v132, v133\n"
      "v_mfma_f32_16x16x32_bf16 v[136:139], v[188:191], v[64:67], v[136:139]\n"
      "ds_read_b128 v[64:67], v232 offset:33024\n"
      "v_cvt_pk_bf16_f32 v209, v134, v135\n"
      "v_mfma_f32_16x16x32_bf16 v[136:139], v[204:207], v[68:71], v[136:139]\n"
      "ds_read_b128 v[68:71], v233 offset:33024\n"
      "v_mfma_f32_16x16x32_bf16 v[136:139], a[24:27], v[84:87], v[136:139]\n"
      "ds_read_b128 v[84:87], v234 offset:33024\n"
      "v_mfma_f32_16x16x32_bf16 v[136:139], a[28:31], v[88:91], v[136:139]\n"
      "ds_read_b128 v[88:91], v235 offset:33024\n"
      "v_mfma_f32_16x16x32_bf16 v[136:139], a[32:35], v[92:95], v[136:139]\n"
      "ds_read_b128 v[92:95], v232 offset:41216\n"
      "v_mfma_f32_16x16x32_bf16 v[136:139], a[36:39], v[108:111], v[136:139]\n"
      "ds_read_b128 v[108:111], v233 offset:41216\n"
      "ds_write_b64 v244, v[208:209] offset:128\n"
      "v_mfma_f32_16x16x32_bf16 v[136:139], a[40:43], v[112:115], v[136:139]\n"
      "ds_read_b128 v[112:115], v234 offset:41216\n"
      "v_mfma_f32_16x16x32_bf16 v[136:139], a[44:47], v[116:119], v[136:139]\n"
      "ds_read_b128 v[116:119], v235 offset:41216\n"
      "s_waitcnt lgkmcnt(9)\n"
      "v_mfma_f32_16x16x32_bf16 v[132:135], a[240:243], v[36:39], v[0:3]\n"
      "v_add_u32 v232, 0xc000, v232\n"
      "s_cbranch_vccnz LT33\n"
      "LS33:\n"
      "v_mfma_f32_16x16x32_bf16 v[132:135], a[244:247], v[40:43], v[132:135]\n"
      "v_add_u32 v233, 0xc000, v233\n"
      "v_mfma_f32_16x16x32_bf16 v[132:135], a[248:251], v[44:47], v[132:135]\n"
      "v_add_u32 v234, 0xc000, v234\n"
      "v_mfma_f32_16x16x32_bf16 v[132:135], a[252:255], v[60:63], v[132:135]\n"
      "v_add_u32 v235, 0xc000, v235\n"
      "s_waitcnt lgkmcnt(5)\n"
      "v_mfma_f32_16x16x32_bf16 v[132:135], a[48:51], v[64:67], v[132:135]\n"
      "v_cvt_pk_bf16_f32 v210, v136, v137\n"
      "v_mfma_f32_16x16x32_bf16 v[132:135], a[52:55], v[68:71], v[132:135]\n"
      "v_cvt_pk_bf16_f32 v211, v138, v139\n"
      "v_mfma_f32_16x16x32_bf16 v[132:135], a[56:59], v[84:87], v[132:135]\n"
      "ds_read_b128 v[48:51], v246 offset:24576\n"
      "v_mfma_f32_16x16x32_bf16 v[132:135], a[60:63], v[88:91], v[132:135]\n"
      "ds_read_b128 v[52:55], v246 offset:25600\n"
      "s_waitcnt lgkmcnt(2)\n"
      "v_mfma_f32_16x16x32_bf16 v[132:135], a[64:67], v[92:95], v[132:135]\n"
      "ds_read_b128 v[56:59], v246 offset:26624\n"
      "v_mfma_f32_16x16x32_bf16 v[132:135], a[68:71], v[108:111], v[132:135]\n"
      "v_lshlrev_b32 v0, 16, v168\n"
      "v_mfma_f32_16x16x32_bf16 v[132:135], a[72:75], v[112:115], v[132:135]\n"
      "ds_write_b64 v244, v[210:211] offset:136\n"
      "v_mfma_f32_16x16x32_bf16 v[132:135], a[76:79], v[116:119], v[132:135]\n"
      "v_and_b32 v1, 0xffff0000, v168\n"
      "v_mfma_f32_16x16x32_bf16 v[136:139], a[80:83], v[36:39], v[4:7]\n"
      "v_lshlrev_b32 v2, 16, v169\n"
      "v_mfma_f32_16x16x32_bf16 v[136:139], a[84:87], v[40:43], v[136:139]\n"
      "v_and_b32 v3, 0xffff0000, v169\n"
      "v_mfma_f32_16x16x32_bf16 v[136:139], a[88:91], v[44:47], v[136:139]\n"
      "v_mfma_f32_16x16x32_bf16 v[136:139], a[92:95], v[60:63], v[136:139]\n"
      "v_cvt_pk_bf16_f32 v208, v132, v133\n"
      "v_mfma_f32_16x16x32_bf16 v[136:139], a[96:99], v[64:67], v[136:139]\n"
      "v_cvt_pk_bf16_f32 v209, v134, v135\n"
      "v_mfma_f32_16x16x32_bf16 v[136:139], a[100:103], v[68:71], v[136:139]\n"
      "s_waitcnt lgkmcnt(3)\n"
      "v_mfma_f32_16x16x32_bf16 v[136:139], a[104:107], v[84:87], v[136:139]\n"
      "global_store_dwordx4 v247, v[48:51], s[54:55] offset:768 sc1\n"
      "v_mfma_f32_16x16x32_bf16 v[136:139], a[108:111], v[88:91], v[136:139]\n"
      "s_add_u32 m0, s17, 0x1000\n"
      "v_mfma_f32_16x16x32_bf16 v[136:139], a[112:115], v[92:95], v[136:139]\n"
      "global_load_lds_dwordx4 v237, s[52:53] offset:0 nt\n"
      "v_mfma_f32_16x16x32_bf16 v[136:139], a[116:119], v[108:111], v[136:139]\n"
      "ds_write_b64 v244, v[208:209] offset:12288\n"
      "v_mfma_f32_16x16x32_bf16 v[136:139], a[120:123], v[112:115], v[136:139]\n"
      "v_lshlrev_b32 v4, 16, v170\n"
      "v_mfma_f32_16x16x32_bf16 v[136:139], a[124:127], v[116:119], v[136:139]\n"
      "v_and_b32 v5, 0xffff0000, v170\n"
      "v_mfma_f32_16x16x32_bf16 v[132:135], a[128:131], v[36:39], v[8:11]\n"
      "v_lshlrev_b32 v6, 16, v171\n"
      "v_mfma_f32_16x16x32_bf16 v[132:135], a[132:135], v[40:43], v[132:135]\n"
      "v_and_b32 v7, 0xffff0000, v171\n"
      "v_mfma_f32_16x16x32_bf16 v[132:135], a[136:139], v[44:47], v[132:135]\n"
      "s_cbranch_vccnz LT34\n"
      "LS34:\n"
      "v_mfma_f32_16x16x32_bf16 v[132:135], a[140:143], v[60:63], v[132:135]\n"
      "v_cvt_pk_bf16_f32 v210, v136, v137\n"
      "v_mfma_f32_16x16x32_bf16 v[132:135], a[144:147], v[64:67], v[132:135]\n"
      "v_cvt_pk_bf16_f32 v211, v138, v139\n"
      "v_mfma_f32_16x16x32_bf16 v[132:135], a[148:151], v[68:71], v[132:135]\n"
      "v_lshlrev_b32 v8, 16, v172\n"
      "v_mfma_f32_16x16x32_bf16 v[132:135], a[152:155], v[84:87], v[132:135]\n"
      "s_waitcnt lgkmcnt(3)\n"
      "v_mfma_f32_16x16x32_bf16 v[132:135], a[156:159], v[88:91], v[132:135]\n"
      "global_store_dwordx4 v252, v[52:55], s[54:55] offset:768 sc1\n"
      "v_mfma_f32_16x16x32_bf16 v[132:135], a[160:163], v[92:95], v[132:135]\n"
      "s_add_u32 m0, s17, 0x2f00\n"
      "v_mfma_f32_16x16x32_bf16 v[132:135], a[164:167], v[108:111], v[132:135]\n"
      "ds_write_b64 v244, v[210:211] offset:12296\n"
      "v_mfma_f32_16x16x32_bf16 v[132:135], a[168:171], v[112:115], v[132:135]\n"
      "global_load_lds_dwordx4 v237, s[52:53] offset:256 nt\n"
      "v_mfma_f32_16x16x32_bf16 v[132:135], a[172:175], v[116:119], v[132:135]\n"
      "v_and_b32 v9, 0xffff0000, v172\n"
      "v_mfma_f32_16x16x32_bf16 v[136:139], a[176:179], v[36:39], v[12:15]\n"
      "v_lshlrev_b32 v10, 16, v173\n"
      "v_mfma_f32_16x16x32_bf16 v[136:139], a[180:183], v[40:43], v[136:139]\n"
      "v_and_b32 v11, 0xffff0000, v173\n"
      "v_mfma_f32_16x16x32_bf16 v[136:139], a[184:187], v[44:47], v[136:139]\n"
      "v_mfma_f32_16x16x32_bf16 v[136:139], a[188:191], v[60:63], v[136:139]\n"
      "v_cvt_pk_bf16_f32 v212, v132, v133\n"
      "v_mfma_f32_16x16x32_bf16 v[136:139], a[192:195], v[64:67], v[136:139]\n"
      "v_cvt_pk_bf16_f32 v213, v134, v135\n"
      "v_mfma_f32_16x16x32_bf16 v[136:139], a[196:199], v[68:71], v[136:139]\n"
      "v_lshlrev_b32 v12, 16, v174\n"
      "v_mfma_f32_16x16x32_bf16 v[136:139], a[200:203], v[84:87], v[136:139]\n"
      "v_and_b32 v13, 0xffff0000, v174\n"
      "v_mfma_f32_16x16x32_bf16 v[136:139], a[204:207], v[88:91], v[136:139]\n"
      "s_waitcnt lgkmcnt(3)\n"
      "v_mfma_f32_16x16x32_bf16 v[136:139], a[208:211], v[92:95], v[136:139]\n"
      "global_store_dwordx4 v253, v[56:59], s[54:55] offset:768 sc1\n"
      "v_mfma_f32_16x16x32_bf16 v[136:139], a[212:215], v[108:111], v[136:139]\n"
      "ds_write_b64 v245, v[212:213] offset:12288\n"
      "v_mfma_f32_16x16x32_bf16 v[136:139], a[216:219], v[112:115], v[136:139]\n"
      "s_add_u32 m0, s17, 0x4e00\n"
      "v_mfma_f32_16x16x32_bf16 v[136:139], a[220:223], v[116:119], v[136:139]\n"
      "global_load_lds_dwordx4 v237, s[52:53] offset:512 nt\n"
      "v_mfma_f32_16x16x32_bf16 v[132:135], a[224:227], v[36:39], v[16:19]\n"
      "v_lshlrev_b32 v14, 16, v175\n"
      "v_mfma_f32_16x16x32_bf16 v[132:135], a[228:231], v[40:43], v[132:135]\n"
      "v_and_b32 v15, 0xffff0000, v175\n"
      "v_mfma_f32_16x16x32_bf16 v[132:135], a[232:235], v[44:47], v[132:135]\n"
      "s_cbranch_vccnz LT35\n"
      "LS35:\n"
      "v_mfma_f32_16x16x32_bf16 v[132:135], a[236:239], v[60:63], v[132:135]\n"
      "s_memrealtime s[84:85]\n"
      "v_mfma_f32_16x16x32_bf16 v[132:135], a[0:3], v[64:67], v[132:135]\n"
      "v_cvt_pk_bf16_f32 v214, v136, v137\n"
      "v_mfma_f32_16x16x32_bf16 v[132:135], a[4:7], v[68:71], v[132:135]\n"
      "v_cvt_pk_bf16_f32 v215, v138, v139\n"
      "v_mfma_f32_16x16x32_bf16 v[132:135], a[8:11], v[84:87], v[132:135]\n"
      "v_lshlrev_b32 v16, 16, v176\n"
      "v_mfma_f32_16x16x32_bf16 v[132:135], a[12:15], v[88:91], v[132:135]\n"
      "s_add_u32 s83, s83, s86\n"
      "v_mfma_f32_16x16x32_bf16 v[132:135], a[16:19], v[92:95], v[132:135]\n"
      "v_and_b32 v17, 0xffff0000, v176\n"
      "v_mfma_f32_16x16x32_bf16 v[132:135], a[20:23], v[108:111], v[132:135]\n"
      "v_lshlrev_b32 v18, 16, v177\n"
      "v_mfma_f32_16x16x32_bf16 v[132:135], v[140:143], v[112:115], v[132:135]\n"
      "s_waitcnt lgkmcnt(0)\n"
      "s_sub_u32 s84, s84, s83\n"
      "v_mfma_f32_16x16x32_bf16 v[132:135], v[156:159], v[116:119], v[132:135]\n"
      "s_cmp_lt_i32 s84, 0\n"
      "s_cselect_b64 vcc, -1, 0\n"
      "s_waitcnt vmcnt(24)\n"
      "s_barrier\n"
      "v_mfma_f32_16x16x32_bf16 v[136:139], v[160:163], v[36:39], v[20:23]\n"
      "ds_read_b128 v[36:39], v232 offset:0\n"
      "ds_write_b64 v245, v[214:215] offset:12296\n"
      "v_mfma_f32_16x16x32_bf16 v[136:139], v[164:167], v[40:43], v[136:139]\n"
      "ds_read_b128 v[40:43], v233 offset:0\n"
      "v_mfma_f32_16x16x32_bf16 v[136:139], v[180:183], v[44:47], v[136:139]\n"
      "ds_read_b128 v[44:47], v234 offset:0\n"
      "v_mfma_f32_16x16x32_bf16 v[136:139], v[184:187], v[60:63], v[136:139]\n"
      "ds_read_b128 v[60:63], v235 offset:0\n"
      "v_cvt_pk_bf16_f32 v208, v132, v133\n"
      "v_mfma_f32_16x16x32_bf16 v[136:139], v[188:191], v[64:67], v[136:139]\n"
      "ds_read_b128 v[64:67], v232 offset:8192\n"
      "v_cvt_pk_bf16_f32 v209, v134, v135\n"
      "v_mfma_f32_16x16x32_bf16 v[136:139], v[204:207], v[68:71], v[136:139]\n"
      "ds_read_b128 v[68:71], v233 offset:8192\n"
      "v_mfma_f32_16x16x32_bf16 v[136:139], a[24:27], v[84:87], v[136:139]\n"
      "ds_read_b128 v[84:87], v234 offset:8192\n"
      "v_mfma_f32_16x16x32_bf16 v[136:139], a[28:31], v[88:91], v[136:139]\n"
      "ds_read_b128 v[88:91], v235 offset:8192\n"
      "v_mfma_f32_16x16x32_bf16 v[136:139], a[32:35], v[92:95], v[136:139]\n"
      "ds_read_b128 v[92:95], v232 offset:16384\n"
      "v_mfma_f32_16x16x32_bf16 v[136:139], a[36:39], v[108:111], v[136:139]\n"
      "ds_read_b128 v[108:111], v233 offset:16384\n"
      "v_mfma_f32_16x16x32_bf16 v[136:139], a[40:43], v[112:115], v[136:139]\n"
      "ds_read_b128 v[112:115], v234 offset:16384\n"
      "v_mfma_f32_16x16x32_bf16 v[136:139], a[44:47], v[116:119], v[136:139]\n"
      "ds_read_b128 v[116:119], v235 offset:16384\n"
      "s_waitcnt lgkmcnt(8)\n"
      "v_mfma_f32_16x16x32_bf16 v[132:135], a[240:243], v[36:39], v[0:3]\n"
      "ds_write_b64 v244, v[208:209] offset:12416\n"
      "s_cbranch_vccnz LT36\n"
      "LS36:\n"
      "v_mfma_f32_16x16x32_bf16 v[132:135], a[244:247], v[40:43], v[132:135]\n"
      "v_and_b32 v19, 0xffff0000, v177\n"
      "v_mfma_f32_16x16x32_bf16 v[132:135], a[248:251], v[44:47], v[132:135]\n"
      "v_lshlrev_b32 v20, 16, v178\n"
      "v_mfma_f32_16x16x32_bf16 v[132:135], a[252:255], v[60:63], v[132:135]\n"
      "v_and_b32 v21, 0xffff0000, v178\n"
      "s_waitcnt lgkmcnt(5)\n"
      "v_mfma_f32_16x16x32_bf16 v[132:135], a[48:51], v[64:67], v[132:135]\n"
      "v_lshlrev_b32 v22, 16, v179\n"
      "v_mfma_f32_16x16x32_bf16 v[132:135], a[52:55], v[68:71], v[132:135]\n"
      "v_and_b32 v23, 0xffff0000, v179\n"
      "v_mfma_f32_16x16x32_bf16 v[132:135], a[56:59], v[84:87], v[132:135]\n"
      "v_cvt_pk_bf16_f32 v210, v136, v137\n"
      "v_mfma_f32_16x16x32_bf16 v[132:135], a[60:63], v[88:91], v[132:135]\n"
      "v_cvt_pk_bf16_f32 v211, v138, v139\n"
      "s_waitcnt lgkmcnt(1)\n"
      "v_mfma_f32_16x16x32_bf16 v[132:135], a[64:67], v[92:95], v[132:135]\n"
      "ds_read_b128 v[72:75], v246 offset:0\n"
      "v_mfma_f32_16x16x32_bf16 v[132:135], a[68:71], v[108:111], v[132:135]\n"
      "ds_read_b128 v[76:79], v246 offset:1024\n"
      "v_mfma_f32_16x16x32_bf16 v[132:135], a[72:75], v[112:115], v[132:135]\n"
      "ds_read_b128 v[80:83], v246 offset:2048\n"
      "v_mfma_f32_16x16x32_bf16 v[132:135], a[76:79], v[116:119], v[132:135]\n"
      "v_mfma_f32_16x16x32_bf16 v[136:139], a[80:83], v[36:39], v[4:7]\n"
      "ds_write_b64 v244, v[210:211] offset:12424\n"
      "v_mfma_f32_16x16x32_bf16 v[136:139], a[84:87], v[40:43], v[136:139]\n"
      "v_mfma_f32_16x16x32_bf16 v[136:139], a[88:91], v[44:47], v[136:139]\n"
      "v_mfma_f32_16x16x32_bf16 v[136:139], a[92:95], v[60:63], v[136:139]\n"
      "v_cvt_pk_bf16_f32 v208, v132, v133\n"
      "s_add_u32 s54, s44, s38\n"
      "s_addc_u32 s55, s45, 0\n"
      "v_mfma_f32_16x16x32_bf16 v[136:139], a[96:99], v[64:67], v[136:139]\n"
      "v_cvt_pk_bf16_f32 v209, v134, v135\n"
      "v_mfma_f32_16x16x32_bf16 v[136:139], a[100:103], v[68:71], v[136:139]\n"
      "s_waitcnt lgkmcnt(3)\n"
      "v_mfma_f32_16x16x32_bf16 v[136:139], a[104:107], v[84:87], v[136:139]\n"
      "global_store_dwordx4 v247, v[72:75], s[54:55] offset:0 sc1\n"
      "v_mfma_f32_16x16x32_bf16 v[136:139], a[108:111], v[88:91], v[136:139]\n"
      "s_add_u32 s52, s42, s40\n"
      "s_addc_u32 s53, s43, 0\n"
      "v_mfma_f32_16x16x32_bf16 v[136:139], a[112:115], v[92:95], v[136:139]\n"
      "s_add_u32 m0, s17, 0x6000\n"
      "v_mfma_f32_16x16x32_bf16 v[136:139], a[116:119], v[108:111], v[136:139]\n"
      "ds_write_b64 v244, v[208:209] offset:24576\n"
      "v_mfma_f32_16x16x32_bf16 v[136:139], a[120:123], v[112:115], v[136:139]\n"
      "global_load_lds_dwordx4 v236, s[52:53] offset:0 nt\n"
      "v_mfma_f32_16x16x32_bf16 v[136:139], a[124:127], v[116:119], v[136:139]\n"
      "v_mfma_f32_16x16x32_bf16 v[132:135], a[128:131], v[36:39], v[8:11]\n"
      "s_cbranch_vccnz LT37\n"
      "LS37:\n"
      "v_mfma_f32_16x16x32_bf16 v[132:135], a[132:135], v[40:43], v[132:135]\n"
      "v_mfma_f32_16x16x32_bf16 v[132:135], a[136:139], v[44:47], v[132:135]\n"
      "v_mfma_f32_16x16x32_bf16 v[132:135], a[140:143], v[60:63], v[132:135]\n"
      "v_cvt_pk_bf16_f32 v210, v136, v137\n"
      "v_mfma_f32_16x16x32_bf16 v[132:135], a[144:147], v[64:67], v[132:135]\n"
      "v_cvt_pk_bf16_f32 v211, v138, v139\n"
      "v_mfma_f32_16x16x32_bf16 v[132:135], a[148:151], v[68:71], v[132:135]\n"
      "v_mfma_f32_16x16x32_bf16 v[132:135], a[152:155], v[84:87], v[132:135]\n"
      "s_waitcnt lgkmcnt(3)\n"
      "v_mfma_f32_16x16x32_bf16 v[132:135], a[156:159], v[88:91], v[132:135]\n"
      "global_store_dwordx4 v252, v[76:79], s[54:55] offset:0 sc1\n"
      "v_mfma_f32_16x16x32_bf16 v[132:135], a[160:163], v[92:95], v[132:135]\n"
      "s_add_u32 m0, s17, 0x7f00\n"
      "v_mfma_f32_16x16x32_bf16 v[132:135], a[164:167], v[108:111], v[132:135]\n"
      "ds_write_b64 v244, v[210:211] offset:24584\n"
      "v_mfma_f32_16x16x32_bf16 v[132:135], a[168:171], v[112:115], v[132:135]\n"
      "global_load_lds_dwordx4 v236, s[52:53] offset:256 nt\n"
      "v_mfma_f32_16x16x32_bf16 v[132:135], a[172:175], v[116:119], v[132:135]\n"
      "v_mfma_f32_16x16x32_bf16 v[136:139], a[176:179], v[36:39], v[12:15]\n"
      "v_mfma_f32_16x16x32_bf16 v[136:139], a[180:183], v[40:43], v[136:139]\n"
      "v_mfma_f32_16x16x32_bf16 v[136:139], a[184:187], v[44:47], v[136:139]\n"
      "v_mfma_f32_16x16x32_bf16 v[136:139], a[188:191], v[60:63], v[136:139]\n"
      "v_cvt_pk_bf16_f32 v212, v132, v133\n"
      "v_mfma_f32_16x16x32_bf16 v[136:139], a[192:195], v[64:67], v[136:139]\n"
      "v_cvt_pk_bf16_f32 v213, v134, v135\n"
      "v_mfma_f32_16x16x32_bf16 v[136:139], a[196:199], v[68:71], v[136:139]\n"
      "v_mfma_f32_16x16x32_bf16 v[136:139], a[200:203], v[84:87], v[136:139]\n"
      "v_mfma_f32_16x16x32_bf16 v[136:139], a[204:207], v[88:91], v[136:139]\n"
      "s_waitcnt lgkmcnt(3)\n"
      "v_mfma_f32_16x16x32_bf16 v[136:139], a[208:211], v[92:95], v[136:139]\n"
      "global_store_dwordx4 v253, v[80:83], s[54:55] offset:0 sc1\n"
      "v_mfma_f32_16x16x32_bf16 v[136:139], a[212:215], v[108:111], v[136:139]\n"
      "ds_write_b64 v245, v[212:213] offset:24576\n"
      "v_mfma_f32_16x16x32_bf16 v[136:139], a[216:219], v[112:115], v[136:139]\n"
      "s_add_u32 m0, s17, 0x9e00\n"
      "v_mfma_f32_16x16x32_bf16 v[136:139], a[220:223], v[116:119], v[136:139]\n"
      "global_load_lds_dwordx4 v236, s[52:53] offset:512 nt\n"
      "v_mfma_f32_16x16x32_bf16 v[132:135], a[224:227], v[36:39], v[16:19]\n"
      "s_cbranch_vccnz LT38\n"
      "LS38:\n"
      "v_mfma_f32_16x16x32_bf16 v[132:135], a[228:231], v[40:43], v[132:135]\n"
      "v_mfma_f32_16x16x32_bf16 v[132:135], a[232:235], v[44:47], v[132:135]\n"
      "v_mfma_f32_16x16x32_bf16 v[132:135], a[236:239], v[60:63], v[132:135]\n"
      "v_cvt_pk_bf16_f32 v214, v136, v137\n"
      "v_mfma_f32_16x16x32_bf16 v[132:135], a[0:3], v[64:67], v[132:135]\n"
      "v_cvt_pk_bf16_f32 v215, v138, v139\n"
      "v_mfma_f32_16x16x32_bf16 v[132:135], a[4:7], v[68:71], v[132:135]\n"
      "v_mfma_f32_16x16x32_bf16 v[132:135], a[8:11], v[84:87], v[132:135]\n"
      "v_mfma_f32_16x16x32_bf16 v[132:135], a[12:15], v[88:91], v[132:135]\n"
      "v_mfma_f32_16x16x32_bf16 v[132:135], a[16:19], v[92:95], v[132:135]\n"
      "v_mfma_f32_16x16x32_bf16 v[132:135], a[20:23], v[108:111], v[132:135]\n"
      "ds_write_b64 v245, v[214:215] offset:24584\n"
      "v_mfma_f32_16x16x32_bf16 v[132:135], v[140:143], v[112:115], v[132:135]\n"
      "v_mfma_f32_16x16x32_bf16 v[132:135], v[156:159], v[116:119], v[132:135]\n"
      "s_waitcnt lgkmcnt(4)\n"
      "s_barrier\n"
      "v_mfma_f32_16x16x32_bf16 v[136:139], v[160:163], v[36:39], v[20:23]\n"
      "ds_read_b128 v[36:39], v232 offset:256\n"
      "v_mfma_f32_16x16x32_bf16 v[136:139], v[164:167], v[40:43], v[136:139]\n"
      "ds_read_b128 v[40:43], v233 offset:256\n"
      "v_mfma_f32_16x16x32_bf16 v[136:139], v[180:183], v[44:47], v[136:139]\n"
      "ds_read_b128 v[44:47], v234 offset:256\n"
      "v_mfma_f32_16x16x32_bf16 v[136:139], v[184:187], v[60:63], v[136:139]\n"
      "ds_read_b128 v[60:63], v235 offset:256\n"
      "v_cvt_pk_bf16_f32 v208, v132, v133\n"
      "v_mfma_f32_16x16x32_bf16 v[136:139], v[188:191], v[64:67], v[136:139]\n"
      "ds_read_b128 v[64:67], v232 offset:8448\n"
      "v_cvt_pk_bf16_f32 v209, v134, v135\n"
      "v_mfma_f32_16x16x32_bf16 v[136:139], v[204:207], v[68:71], v[136:139]\n"
      "ds_read_b128 v[68:71], v233 offset:8448\n"
      "v_mfma_f32_16x16x32_bf16 v[136:139], a[24:27], v[84:87], v[136:139]\n"
      "ds_read_b128 v[84:87], v234 offset:8448\n"
      "v_mfma_f32_16x16x32_bf16 v[136:139], a[28:31], v[88:91], v[136:139]\n"
      "ds_read_b128 v[88:91], v235 offset:8448\n"
      "v_mfma_f32_16x16x32_bf16 v[136:139], a[32:35], v[92:95], v[136:139]\n"
      "ds_read_b128 v[92:95], v232 offset:16640\n"
      "v_mfma_f32_16x16x32_bf16 v[136:139], a[36:39], v[108:111], v[136:139]\n"
      "ds_read_b128 v[108:111], v233 offset:16640\n"
      "v_mfma_f32_16x16x32_bf16 v[136:139], a[40:43], v[112:115], v[136:139]\n"
      "ds_read_b128 v[112:115], v234 offset:16640\n"
      "v_mfma_f32_16x16x32_bf16 v[136:139], a[44:47], v[116:119], v[136:139]\n"
      "ds_read_b128 v[116:119], v235 offset:16640\n"
      "s_waitcnt lgkmcnt(8)\n"
      "v_mfma_f32_16x16x32_bf16 v[132:135], a[240:243], v[36:39], v[0:3]\n"
      "ds_write_b64 v244, v[208:209] offset:24704\n"
      "v_mfma_f32_16x16x32_bf16 v[132:135], a[244:247], v[40:43], v[132:135]\n"
      "s_cbranch_vccnz LT39\n"
      "LS39:\n"
      "v_mfma_f32_16x16x32_bf16 v[132:135], a[248:251], v[44:47], v[132:135]\n"
      "v_mfma_f32_16x16x32_bf16 v[132:135], a[252:255], v[60:63], v[132:135]\n"
      "v_cvt_pk_bf16_f32 v210, v136, v137\n"
      "s_waitcnt lgkmcnt(5)\n"
      "v_mfma_f32_16x16x32_bf16 v[132:135], a[48:51], v[64:67], v[132:135]\n"
      "v_cvt_pk_bf16_f32 v211, v138, v139\n"
      "v_mfma_f32_16x16x32_bf16 v[132:135], a[52:55], v[68:71], v[132:135]\n"
      "v_mfma_f32_16x16x32_bf16 v[132:135], a[56:59], v[84:87], v[132:135]\n"
      "ds_read_b128 v[96:99], v246 offset:12288\n"
      "v_mfma_f32_16x16x32_bf16 v[132:135], a[60:63], v[88:91], v[132:135]\n"
      "ds_read_b128 v[100:103], v246 offset:13312\n"
      "s_waitcnt lgkmcnt(3)\n"
      "v_mfma_f32_16x16x32_bf16 v[132:135], a[64:67], v[92:95], v[132:135]\n"
      "ds_read_b128 v[104:107], v246 offset:14336\n"
      "v_mfma_f32_16x16x32_bf16 v[132:135], a[68:71], v[108:111], v[132:135]\n"
      "ds_write_b64 v244, v[210:211] offset:24712\n"
      "v_mfma_f32_16x16x32_bf16 v[132:135], a[72:75], v[112:115], v[132:135]\n"
      "v_mfma_f32_16x16x32_bf16 v[132:135], a[76:79], v[116:119], v[132:135]\n"
      "v_mfma_f32_16x16x32_bf16 v[136:139], a[80:83], v[36:39], v[4:7]\n"
      "v_mfma_f32_16x16x32_bf16 v[136:139], a[84:87], v[40:43], v[136:139]\n"
      "v_mfma_f32_16x16x32_bf16 v[136:139], a[88:91], v[44:47], v[136:139]\n"
      "v_mfma_f32_16x16x32_bf16 v[136:139], a[92:95], v[60:63], v[136:139]\n"
      "v_cvt_pk_bf16_f32 v208, v132, v133\n"
      "v_mfma_f32_16x16x32_bf16 v[136:139], a[96:99], v[64:67], v[136:139]\n"
      "v_cvt_pk_bf16_f32 v209, v134, v135\n"
      "v_mfma_f32_16x16x32_bf16 v[136:139], a[100:103], v[68:71], v[136:139]\n"
      "v_mfma_f32_16x16x32_bf16 v[136:139], a[104:107], v[84:87], v[136:139]\n"
      "v_mfma_f32_16x16x32_bf16 v[136:139], a[108:111], v[88:91], v[136:139]\n"
      "s_add_u32 m0, s17, 0x7000\n"
      "v_mfma_f32_16x16x32_bf16 v[136:139], a[112:115], v[92:95], v[136:139]\n"
      "global_load_lds_dwordx4 v237, s[52:53] offset:0 nt\n"
      "v_mfma_f32_16x16x32_bf16 v[136:139], a[116:119], v[108:111], v[136:139]\n"
      "ds_write_b64 v244, v[208:209] offset:0\n"
      "v_mfma_f32_16x16x32_bf16 v[136:139], a[120:123], v[112:115], v[136:139]\n"
      "v_mfma_f32_16x16x32_bf16 v[136:139], a[124:127], v[116:119], v[136:139]\n"
      "v_mfma_f32_16x16x32_bf16 v[132:135], a[128:131], v[36:39], v[8:11]\n"
      "s_cbranch_vccnz LT40\n"
      "LS40:\n"
      "v_mfma_f32_16x16x32_bf16 v[132:135], a[132:135], v[40:43], v[132:135]\n"
      "v_mfma_f32_16x16x32_bf16 v[132:135], a[136:139], v[44:47], v[132:135]\n"
      "v_mfma_f32_16x16x32_bf16 v[132:135], a[140:143], v[60:63], v[132:135]\n"
      "v_cvt_pk_bf16_f32 v210, v136, v137\n"
      "v_mfma_f32_16x16x32_bf16 v[132:135], a[144:147], v[64:67], v[132:135]\n"
      "v_cvt_pk_bf16_f32 v211, v138, v139\n"
      "v_mfma_f32_16x16x32_bf16 v[132:135], a[148:151], v[68:71], v[132:135]\n"
      "v_mfma_f32_16x16x32_bf16 v[132:135], a[152:155], v[84:87], v[132:135]\n"
      "s_add_u32 m0, s17, 0x8f00\n"
      "v_mfma_f32_16x16x32_bf16 v[132:135], a[156:159], v[88:91], v[132:135]\n"
      "global_load_lds_dwordx4 v237, s[52:53] offset:256 nt\n"
      "v_mfma_f32_16x16x32_bf16 v[132:135], a[160:163], v[92:95], v[132:135]\n"
      "v_mfma_f32_16x16x32_bf16 v[132:135], a[164:167], v[108:111], v[132:135]\n"
      "ds_write_b64 v244, v[210:211] offset:8\n"
      "v_mfma_f32_16x16x32_bf16 v[132:135], a[168:171], v[112:115], v[132:135]\n"
      "v_mfma_f32_16x16x32_bf16 v[132:135], a[172:175], v[116:119], v[132:135]\n"
      "v_mfma_f32_16x16x32_bf16 v[136:139], a[176:179], v[36:39], v[12:15]\n"
      "v_mfma_f32_16x16x32_bf16 v[136:139], a[180:183], v[40:43], v[136:139]\n"
      "v_mfma_f32_16x16x32_bf16 v[136:139], a[184:187], v[44:47], v[136:139]\n"
      "v_mfma_f32_16x16x32_bf16 v[136:139], a[188:191], v[60:63], v[136:139]\n"
      "v_cvt_pk_bf16_f32 v212, v132, v133\n"
      "v_mfma_f32_16x16x32_bf16 v[136:139], a[192:195], v[64:67], v[136:139]\n"
      "v_cvt_pk_bf16_f32 v213, v134, v135\n"
      "v_mfma_f32_16x16x32_bf16 v[136:139], a[196:199], v[68:71], v[136:139]\n"
      "s_add_u32 m0, s17, 0xae00\n"
      "v_mfma_f32_16x16x32_bf16 v[136:139], a[200:203], v[84:87], v[136:139]\n"
      "global_load_lds_dwordx4 v237, s[52:53] offset:512 nt\n"
      "v_mfma_f32_16x16x32_bf16 v[136:139], a[204:207], v[88:91], v[136:139]\n"
      "v_mfma_f32_16x16x32_bf16 v[136:139], a[208:211], v[92:95], v[136:139]\n"
      "v_mfma_f32_16x16x32_bf16 v[136:139], a[212:215], v[108:111], v[136:139]\n"
      "ds_write_b64 v245, v[212:213] offset:0\n"
      "v_mfma_f32_16x16x32_bf16 v[136:139], a[216:219], v[112:115], v[136:139]\n"
      "v_mfma_f32_16x16x32_bf16 v[136:139], a[220:223], v[116:119], v[136:139]\n"
      "v_mfma_f32_16x16x32_bf16 v[132:135], a[224:227], v[36:39], v[16:19]\n"
      "s_cbranch_vccnz LT41\n"
      "LS41:\n"
      "v_mfma_f32_16x16x32_bf16 v[132:135], a[228:231], v[40:43], v[132:135]\n"
      "v_mfma_f32_16x16x32_bf16 v[132:135], a[232:235], v[44:47], v[132:135]\n"
      "v_mfma_f32_16x16x32_bf16 v[132:135], a[236:239], v[60:63], v[132:135]\n"
      "v_cvt_pk_bf16_f32 v214, v136, v137\n"
      "v_mfma_f32_16x16x32_bf16 v[132:135], a[0:3], v[64:67], v[132:135]\n"
      "v_cvt_pk_bf16_f32 v215, v138, v139\n"
      "v_mfma_f32_16x16x32_bf16 v[132:135], a[4:7], v[68:71], v[132:135]\n"
      "v_mfma_f32_16x16x32_bf16 v[132:135], a[8:11], v[84:87], v[132:135]\n"
      "v_mfma_f32_16x16x32_bf16 v[132:135], a[12:15], v[88:91], v[132:135]\n"
      "v_mfma_f32_16x16x32_bf16 v[132:135], a[16:19], v[92:95], v[132:135]\n"
      "v_mfma_f32_16x16x32_bf16 v[132:135], a[20:23], v[108:111], v[132:135]\n"
      "ds_write_b64 v245, v[214:215] offset:8\n"
      "v_mfma_f32_16x16x32_bf16 v[132:135], v[140:143], v[112:115], v[132:135]\n"
      "v_mfma_f32_16x16x32_bf16 v[132:135], v[156:159], v[116:119], v[132:135]\n"
      "s_waitcnt vmcnt(21) lgkmcnt(4)\n"
      "s_barrier\n"
      "v_mfma_f32_16x16x32_bf16 v[136:139], v[160:163], v[36:39], v[20:23]\n"
      "ds_read_b128 v[36:39], v232 offset:24576\n"
      "v_mfma_f32_16x16x32_bf16 v[136:139], v[164:167], v[40:43], v[136:139]\n"
      "ds_read_b128 v[40:43], v233 offset:24576\n"
      "v_mfma_f32_16x16x32_bf16 v[136:139], v[180:183], v[44:47], v[136:139]\n"
      "ds_read_b128 v[44:47], v234 offset:24576\n"
      "v_mfma_f32_16x16x32_bf16 v[136:139], v[184:187], v[60:63], v[136:139]\n"
      "ds_read_b128 v[60:63], v235 offset:24576\n"
      "v_cvt_pk_bf16_f32 v208, v132, v133\n"
      "v_mfma_f32_16x16x32_bf16 v[136:139], v[188:191], v[64:67], v[136:139]\n"
      "ds_read_b128 v[64:67], v232 offset:32768\n"
      "v_cvt_pk_bf16_f32 v209, v134, v135\n"
      "v_mfma_f32_16x16x32_bf16 v[136:139], v[204:207], v[68:71], v[136:139]\n"
      "ds_read_b128 v[68:71], v233 offset:32768\n"
      "v_mfma_f32_16x16x32_bf16 v[136:139], a[24:27], v[84:87], v[136:139]\n"
      "ds_read_b128 v[84:87], v234 offset:32768\n"
      "v_mfma_f32_16x16x32_bf16 v[136:139], a[28:31], v[88:91], v[136:139]\n"
      "ds_read_b128 v[88:91], v235 offset:32768\n"
      "v_mfma_f32_16x16x32_bf16 v[136:139], a[32:35], v[92:95], v[136:139]\n"
      "ds_read_b128 v[92:95], v232 offset:40960\n"
      "v_mfma_f32_16x16x32_bf16 v[136:139], a[36:39], v[108:111], v[136:139]\n"
      "ds_read_b128 v[108:111], v233 offset:40960\n"
      "v_mfma_f32_16x16x32_bf16 v[136:139], a[40:43], v[112:115], v[136:139]\n"
      "ds_read_b128 v[112:115], v234 offset:40960\n"
      "v_mfma_f32_16x16x32_bf16 v[136:139], a[44:47], v[116:119], v[136:139]\n"
      "ds_read_b128 v[116:119], v235 offset:40960\n"
      "s_waitcnt lgkmcnt(8)\n"
      "v_mfma_f32_16x16x32_bf16 v[132:135], a[240:243], v[36:39], v[0:3]\n"
      "ds_write_b64 v244, v[208:209] offset:128\n"
      "v_mfma_f32_16x16x32_bf16 v[132:135], a[244:247], v[40:43], v[132:135]\n"
      "v_mfma_f32_16x16x32_bf16 v[132:135], a[248:251], v[44:47], v[132:135]\n"
      "v_mfma_f32_16x16x32_bf16 v[132:135], a[252:255], v[60:63], v[132:135]\n"
      "v_cvt_pk_bf16_f32 v210, v136, v137\n"
      "s_waitcnt lgkmcnt(5)\n"
      "v_mfma_f32_16x16x32_bf16 v[132:135], a[48:51], v[64:67], v[132:135]\n"
      "v_cvt_pk_bf16_f32 v211, v138, v139\n"
      "v_mfma_f32_16x16x32_bf16 v[132:135], a[52:55], v[68:71], v[132:135]\n"
      "v_mfma_f32_16x16x32_bf16 v[132:135], a[56:59], v[84:87], v[132:135]\n"
      "ds_read_b128 v[120:123], v246 offset:24576\n"
      "v_mfma_f32_16x16x32_bf16 v[132:135], a[60:63], v[88:91], v[132:135]\n"
      "ds_read_b128 v[124:127], v246 offset:25600\n"
      "s_waitcnt lgkmcnt(3)\n"
      "v_mfma_f32_16x16x32_bf16 v[132:135], a[64:67], v[92:95], v[132:135]\n"
      "ds_read_b128 v[128:131], v246 offset:26624\n"
      "v_mfma_f32_16x16x32_bf16 v[132:135], a[68:71], v[108:111], v[132:135]\n"
      "ds_write_b64 v244, v[210:211] offset:136\n"
      "v_mfma_f32_16x16x32_bf16 v[132:135], a[72:75], v[112:115], v[132:135]\n"
      "v_mfma_f32_16x16x32_bf16 v[132:135], a[76:79], v[116:119], v[132:135]\n"
      "v_mfma_f32_16x16x32_bf16 v[136:139], a[80:83], v[36:39], v[4:7]\n"
      "v_mfma_f32_16x16x32_bf16 v[136:139], a[84:87], v[40:43], v[136:139]\n"
      "v_mfma_f32_16x16x32_bf16 v[136:139], a[88:91], v[44:47], v[136:139]\n"
      "v_mfma_f32_16x16x32_bf16 v[136:139], a[92:95], v[60:63], v[136:139]\n"
      "v_cvt_pk_bf16_f32 v208, v132, v133\n"
      "v_mfma_f32_16x16x32_bf16 v[136:139], a[96:99], v[64:67], v[136:139]\n"
      "v_cvt_pk_bf16_f32 v209, v134, v135\n"
      "v_mfma_f32_16x16x32_bf16 v[136:139], a[100:103], v[68:71], v[136:139]\n"
      "v_mfma_f32_16x16x32_bf16 v[136:139], a[104:107], v[84:87], v[136:139]\n"
      "v_mfma_f32_16x16x32_bf16 v[136:139], a[108:111], v[88:91], v[136:139]\n"
      "v_mfma_f32_16x16x32_bf16 v[136:139], a[112:115], v[92:95], v[136:139]\n"
      "v_mfma_f32_16x16x32_bf16 v[136:139], a[116:119], v[108:111], v[136:139]\n"
      "ds_write_b64 v244, v[208:209] offset:12288\n"
      "v_mfma_f32_16x16x32_bf16 v[136:139], a[120:123], v[112:115], v[136:139]\n"
      "v_mfma_f32_16x16x32_bf16 v[136:139], a[124:127], v[116:119], v[136:139]\n"
      "v_mfma_f32_16x16x32_bf16 v[132:135], a[128:131], v[36:39], v[8:11]\n"
      "v_mfma_f32_16x16x32_bf16 v[132:135], a[132:135], v[40:43], v[132:135]\n"
      "v_mfma_f32_16x16x32_bf16 v[132:135], a[136:139], v[44:47], v[132:135]\n"
      "v_mfma_f32_16x16x32_bf16 v[132:135], a[140:143], v[60:63], v[132:135]\n"
      "v_cvt_pk_bf16_f32 v210, v136, v137\n"
      "v_mfma_f32_16x16x32_bf16 v[132:135], a[144:147], v[64:67], v[132:135]\n"
      "v_cvt_pk_bf16_f32 v211, v138, v139\n"
      "v_mfma_f32_16x16x32_bf16 v[132:135], a[148:151], v[68:71], v[132:135]\n"
      "v_mfma_f32_16x16x32_bf16 v[132:135], a[152:155], v[84:87], v[132:135]\n"
      "v_mfma_f32_16x16x32_bf16 v[132:135], a[156:159], v[88:91], v[132:135]\n"
      "v_mfma_f32_16x16x32_bf16 v[132:135], a[160:163], v[92:95], v[132:135]\n"
      "v_mfma_f32_16x16x32_bf16 v[132:135], a[164:167], v[108:111], v[132:135]\n"
      "ds_write_b64 v244, v[210:211] offset:12296\n"
      "v_mfma_f32_16x16x32_bf16 v[132:135], a[168:171], v[112:115], v[132:135]\n"
      "v_mfma_f32_16x16x32_bf16 v[132:135], a[172:175], v[116:119], v[132:135]\n"
      "v_mfma_f32_16x16x32_bf16 v[136:139], a[176:179], v[36:39], v[12:15]\n"
      "v_mfma_f32_16x16x32_bf16 v[136:139], a[180:183], v[40:43], v[136:139]\n"
      "v_mfma_f32_16x16x32_bf16 v[136:139], a[184:187], v[44:47], v[136:139]\n"
      "v_mfma_f32_16x16x32_bf16 v[136:139], a[188:191], v[60:63], v[136:139]\n"
      "v_cvt_pk_bf16_f32 v212, v132, v133\n"
      "v_mfma_f32_16x16x32_bf16 v[136:139], a[192:195], v[64:67], v[136:139]\n"
      "v_cvt_pk_bf16_f32 v213, v134, v135\n"
      "v_mfma_f32_16x16x32_bf16 v[136:139], a[196:199], v[68:71], v[136:139]\n"
      "v_mfma_f32_16x16x32_bf16 v[136:139], a[200:203], v[84:87], v[136:139]\n"
      "v_mfma_f32_16x16x32_bf16 v[136:139], a[204:207], v[88:91], v[136:139]\n"
      "v_mfma_f32_16x16x32_bf16 v[136:139], a[208:211], v[92:95], v[136:139]\n"
      "v_mfma_f32_16x16x32_bf16 v[136:139], a[212:215], v[108:111], v[136:139]\n"
      "ds_write_b64 v245, v[212:213] offset:12288\n"
      "v_mfma_f32_16x16x32_bf16 v[136:139], a[216:219], v[112:115], v[136:139]\n"
      "v_mfma_f32_16x16x32_bf16 v[136:139], a[220:223], v[116:119], v[136:139]\n"
      "v_mfma_f32_16x16x32_bf16 v[132:135], a[224:227], v[36:39], v[16:19]\n"
      "v_mfma_f32_16x16x32_bf16 v[132:135], a[228:231], v[40:43], v[132:135]\n"
      "v_mfma_f32_16x16x32_bf16 v[132:135], a[232:235], v[44:47], v[132:135]\n"
      "v_mfma_f32_16x16x32_bf16 v[132:135], a[236:239], v[60:63], v[132:135]\n"
      "v_cvt_pk_bf16_f32 v214, v136, v137\n"
      "v_mfma_f32_16x16x32_bf16 v[132:135], a[0:3], v[64:67], v[132:135]\n"
      "v_cvt_pk_bf16_f32 v215, v138, v139\n"
      "v_mfma_f32_16x16x32_bf16 v[132:135], a[4:7], v[68:71], v[132:135]\n"
      "v_mfma_f32_16x16x32_bf16 v[132:135], a[8:11], v[84:87], v[132:135]\n"
      "v_mfma_f32_16x16x32_bf16 v[132:135], a[12:15], v[88:91], v[132:135]\n"
      "v_mfma_f32_16x16x32_bf16 v[132:135], a[16:19], v[92:95], v[132:135]\n"
      "v_mfma_f32_16x16x32_bf16 v[132:135], a[20:23], v[108:111], v[132:135]\n"
      "ds_write_b64 v245, v[214:215] offset:12296\n"
      "v_mfma_f32_16x16x32_bf16 v[132:135], v[140:143], v[112:115], v[132:135]\n"
      "v_mfma_f32_16x16x32_bf16 v[132:135], v[156:159], v[116:119], v[132:135]\n"
      "s_waitcnt lgkmcnt(4)\n"
      "s_barrier\n"
      "v_mfma_f32_16x16x32_bf16 v[136:139], v[160:163], v[36:39], v[20:23]\n"
      "ds_read_b128 v[36:39], v232 offset:24832\n"
      "v_mfma_f32_16x16x32_bf16 v[136:139], v[164:167], v[40:43], v[136:139]\n"
      "ds_read_b128 v[40:43], v233 offset:24832\n"
      "v_mfma_f32_16x16x32_bf16 v[136:139], v[180:183], v[44:47], v[136:139]\n"
      "ds_read_b128 v[44:47], v234 offset:24832\n"
      "v_mfma_f32_16x16x32_bf16 v[136:139], v[184:187], v[60:63], v[136:139]\n"
      "ds_read_b128 v[60:63], v235 offset:24832\n"
      "v_cvt_pk_bf16_f32 v208, v132, v133\n"
      "v_mfma_f32_16x16x32_bf16 v[136:139], v[188:191], v[64:67], v[136:139]\n"
      "ds_read_b128 v[64:67], v232 offset:33024\n"
      "v_cvt_pk_bf16_f32 v209, v134, v135\n"
      "v_mfma_f32_16x16x32_bf16 v[136:139], v[204:207], v[68:71], v[136:139]\n"
      "ds_read_b128 v[68:71], v233 offset:33024\n"
      "v_mfma_f32_16x16x32_bf16 v[136:139], a[24:27], v[84:87], v[136:139]\n"
      "ds_read_b128 v[84:87], v234 offset:33024\n"
      "v_mfma_f32_16x16x32_bf16 v[136:139], a[28:31], v[88:91], v[136:139]\n"
      "LPD15:\n"
      "s_cbranch_execz LRET\n"
      "ds_read_b128 v[88:91], v235 offset:33024\n"
      "v_mfma_f32_16x16x32_bf16 v[136:139], a[32:35], v[92:95], v[136:139]\n"
      "ds_read_b128 v[92:95], v232 offset:41216\n"
      "v_mfma_f32_16x16x32_bf16 v[136:139], a[36:39], v[108:111], v[136:139]\n"
      "ds_read_b128 v[108:111], v233 offset:41216\n"
      "ds_write_b64 v244, v[208:209] offset:12416\n"
      "v_mfma_f32_16x16x32_bf16 v[136:139], a[40:43], v[112:115], v[136:139]\n"
      "ds_read_b128 v[112:115], v234 offset:41216\n"
      "v_mfma_f32_16x16x32_bf16 v[136:139], a[44:47], v[116:119], v[136:139]\n"
      "ds_read_b128 v[116:119], v235 offset:41216\n"
      "s_waitcnt lgkmcnt(9)\n"
      "v_mfma_f32_16x16x32_bf16 v[132:135], a[240:243], v[36:39], v[0:3]\n"
      "v_add_u32 v232, 0xffff4000, v232\n"
      "v_mfma_f32_16x16x32_bf16 v[132:135], a[244:247], v[40:43], v[132:135]\n"
      "v_add_u32 v233, 0xffff4000, v233\n"
      "v_mfma_f32_16x16x32_bf16 v[132:135], a[248:251], v[44:47], v[132:135]\n"
      "v_add_u32 v234, 0xffff4000, v234\n"
      "v_mfma_f32_16x16x32_bf16 v[132:135], a[252:255], v[60:63], v[132:135]\n"
      "v_add_u32 v235, 0xffff4000, v235\n"
      "s_waitcnt lgkmcnt(5)\n"
      "v_mfma_f32_16x16x32_bf16 v[132:135], a[48:51], v[64:67], v[132:135]\n"
      "v_cvt_pk_bf16_f32 v210, v136, v137\n"
      "v_mfma_f32_16x16x32_bf16 v[132:135], a[52:55], v[68:71], v[132:135]\n"
      "v_cvt_pk_bf16_f32 v211, v138, v139\n"
      "v_mfma_f32_16x16x32_bf16 v[132:135], a[56:59], v[84:87], v[132:135]\n"
      "ds_read_b128 v[144:147], v246 offset:0\n"
      "v_mfma_f32_16x16x32_bf16 v[132:135], a[60:63], v[88:91], v[132:135]\n"
      "ds_read_b128 v[148:151], v246 offset:1024\n"
      "s_waitcnt lgkmcnt(2)\n"
      "v_mfma_f32_16x16x32_bf16 v[132:135], a[64:67], v[92:95], v[132:135]\n"
      "ds_read_b128 v[152:155], v246 offset:2048\n"
      "v_mfma_f32_16x16x32_bf16 v[132:135], a[68:71], v[108:111], v[132:135]\n"
      "v_lshlrev_b32 v0, 16, v192\n"
      "v_mfma_f32_16x16x32_bf16 v[132:135], a[72:75], v[112:115], v[132:135]\n"
      "ds_write_b64 v244, v[210:211] offset:12424\n"
      "v_mfma_f32_16x16x32_bf16 v[132:135], a[76:79], v[116:119], v[132:135]\n"
      "v_and_b32 v1, 0xffff0000, v192\n"
      "v_mfma_f32_16x16x32_bf16 v[136:139], a[80:83], v[36:39], v[4:7]\n"
      "v_lshlrev_b32 v2, 16, v193\n"
      "v_mfma_f32_16x16x32_bf16 v[136:139], a[84:87], v[40:43], v[136:139]\n"
      "v_and_b32 v3, 0xffff0000, v193\n"
      "v_mfma_f32_16x16x32_bf16 v[136:139], a[88:91], v[44:47], v[136:139]\n"
      "v_mfma_f32_16x16x32_bf16 v[136:139], a[92:95], v[60:63], v[136:139]\n"
      "v_cvt_pk_bf16_f32 v208, v132, v133\n"
      "v_mfma_f32_16x16x32_bf16 v[136:139], a[96:99], v[64:67], v[136:139]\n"
      "v_cvt_pk_bf16_f32 v209, v134, v135\n"
      "v_mfma_f32_16x16x32_bf16 v[136:139], a[100:103], v[68:71], v[136:139]\n"
      "v_lshlrev_b32 v4, 16, v194\n"
      "v_mfma_f32_16x16x32_bf16 v[136:139], a[104:107], v[84:87], v[136:139]\n"
      "v_and_b32 v5, 0xffff0000, v194\n"
      "v_mfma_f32_16x16x32_bf16 v[136:139], a[108:111], v[88:91], v[136:139]\n"
      "v_lshlrev_b32 v6, 16, v195\n"
      "v_mfma_f32_16x16x32_bf16 v[136:139], a[112:115], v[92:95], v[136:139]\n"
      "v_and_b32 v7, 0xffff0000, v195\n"
      "v_mfma_f32_16x16x32_bf16 v[136:139], a[116:119], v[108:111], v[136:139]\n"
      "ds_write_b64 v244, v[208:209] offset:24576\n"
      "v_mfma_f32_16x16x32_bf16 v[136:139], a[120:123], v[112:115], v[136:139]\n"
      "v_mfma_f32_16x16x32_bf16 v[136:139], a[124:127], v[116:119], v[136:139]\n"
      "v_mfma_f32_16x16x32_bf16 v[132:135], a[128:131], v[36:39], v[8:11]\n"
      "v_mfma_f32_16x16x32_bf16 v[132:135], a[132:135], v[40:43], v[132:135]\n"
      "v_mfma_f32_16x16x32_bf16 v[132:135], a[136:139], v[44:47], v[132:135]\n"
      "v_mfma_f32_16x16x32_bf16 v[132:135], a[140:143], v[60:63], v[132:135]\n"
      "v_cvt_pk_bf16_f32 v210, v136, v137\n"
      "v_mfma_f32_16x16x32_bf16 v[132:135], a[144:147], v[64:67], v[132:135]\n"
      "v_cvt_pk_bf16_f32 v211, v138, v139\n"
      "v_mfma_f32_16x16x32_bf16 v[132:135], a[148:151], v[68:71], v[132:135]\n"
      "v_lshlrev_b32 v8, 16, v196\n"
      "v_mfma_f32_16x16x32_bf16 v[132:135], a[152:155], v[84:87], v[132:135]\n"
      "v_and_b32 v9, 0xffff0000, v196\n"
      "v_mfma_f32_16x16x32_bf16 v[132:135], a[156:159], v[88:91], v[132:135]\n"
      "v_lshlrev_b32 v10, 16, v197\n"
      "v_mfma_f32_16x16x32_bf16 v[132:135], a[160:163], v[92:95], v[132:135]\n"
      "v_and_b32 v11, 0xffff0000, v197\n"
      "v_mfma_f32_16x16x32_bf16 v[132:135], a[164:167], v[108:111], v[132:135]\n"
      "ds_write_b64 v244, v[210:211] offset:24584\n"
      "v_mfma_f32_16x16x32_bf16 v[132:135], a[168:171], v[112:115], v[132:135]\n"
      "v_mfma_f32_16x16x32_bf16 v[132:135], a[172:175], v[116:119], v[132:135]\n"
      "v_mfma_f32_16x16x32_bf16 v[136:139], a[176:179], v[36:39], v[12:15]\n"
      "v_mfma_f32_16x16x32_bf16 v[136:139], a[180:183], v[40:43], v[136:139]\n"
      "v_mfma_f32_16x16x32_bf16 v[136:139], a[184:187], v[44:47], v[136:139]\n"
      "v_mfma_f32_16x16x32_bf16 v[136:139], a[188:191], v[60:63], v[136:139]\n"
      "v_cvt_pk_bf16_f32 v212, v132, v133\n"
      "v_mfma_f32_16x16x32_bf16 v[136:139], a[192:195], v[64:67], v[136:139]\n"
      "v_cvt_pk_bf16_f32 v213, v134, v135\n"
      "v_mfma_f32_16x16x32_bf16 v[136:139], a[196:199], v[68:71], v[136:139]\n"
      "v_lshlrev_b32 v12, 16, v198\n"
      "v_mfma_f32_16x16x32_bf16 v[136:139], a[200:203], v[84:87], v[136:139]\n"
      "v_and_b32 v13, 0xffff0000, v198\n"
      "v_mfma_f32_16x16x32_bf16 v[136:139], a[204:207], v[88:91], v[136:139]\n"
      "v_lshlrev_b32 v14, 16, v199\n"
      "v_mfma_f32_16x16x32_bf16 v[136:139], a[208:211], v[92:95], v[136:139]\n"
      "v_and_b32 v15, 0xffff0000, v199\n"
      "v_mfma_f32_16x16x32_bf16 v[136:139], a[212:215], v[108:111], v[136:139]\n"
      "ds_write_b64 v245, v[212:213] offset:24576\n"
      "v_mfma_f32_16x16x32_bf16 v[136:139], a[216:219], v[112:115], v[136:139]\n"
      "v_mfma_f32_16x16x32_bf16 v[136:139], a[220:223], v[116:119], v[136:139]\n"
      "v_mfma_f32_16x16x32_bf16 v[132:135], a[224:227], v[36:39], v[16:19]\n"
      "v_mfma_f32_16x16x32_bf16 v[132:135], a[228:231], v[40:43], v[132:135]\n"
      "v_mfma_f32_16x16x32_bf16 v[132:135], a[232:235], v[44:47], v[132:135]\n"
      "v_mfma_f32_16x16x32_bf16 v[132:135], a[236:239], v[60:63], v[132:135]\n"
      "v_cvt_pk_bf16_f32 v214, v136, v137\n"
      "v_mfma_f32_16x16x32_bf16 v[132:135], a[0:3], v[64:67], v[132:135]\n"
      "v_cvt_pk_bf16_f32 v215, v138, v139\n"
      "v_mfma_f32_16x16x32_bf16 v[132:135], a[4:7], v[68:71], v[132:135]\n"
      "v_lshlrev_b32 v16, 16, v200\n"
      "v_mfma_f32_16x16x32_bf16 v[132:135], a[8:11], v[84:87], v[132:135]\n"
      "v_and_b32 v17, 0xffff0000, v200\n"
      "v_mfma_f32_16x16x32_bf16 v[132:135], a[12:15], v[88:91], v[132:135]\n"
      "v_lshlrev_b32 v18, 16, v201\n"
      "v_mfma_f32_16x16x32_bf16 v[132:135], a[16:19], v[92:95], v[132:135]\n"
      "v_and_b32 v19, 0xffff0000, v201\n"
      "v_mfma_f32_16x16x32_bf16 v[132:135], a[20:23], v[108:111], v[132:135]\n"
      "ds_write_b64 v245, v[214:215] offset:24584\n"
      "v_mfma_f32_16x16x32_bf16 v[132:135], v[140:143], v[112:115], v[132:135]\n"
      "v_mfma_f32_16x16x32_bf16 v[132:135], v[156:159], v[116:119], v[132:135]\n"
      "s_waitcnt vmcnt(9) lgkmcnt(4)\n"
      "s_barrier\n"
      "v_mfma_f32_16x16x32_bf16 v[136:139], v[160:163], v[36:39], v[20:23]\n"
      "ds_read_b128 v[36:39], v232 offset:0\n"
      "v_mfma_f32_16x16x32_bf16 v[136:139], v[164:167], v[40:43], v[136:139]\n"
      "ds_read_b128 v[40:43], v233 offset:0\n"
      "v_mfma_f32_16x16x32_bf16 v[136:139], v[180:183], v[44:47], v[136:139]\n"
      "ds_read_b128 v[44:47], v234 offset:0\n"
      "v_mfma_f32_16x16x32_bf16 v[136:139], v[184:187], v[60:63], v[136:139]\n"
      "ds_read_b128 v[60:63], v235 offset:0\n"
      "v_cvt_pk_bf16_f32 v208, v132, v133\n"
      "v_mfma_f32_16x16x32_bf16 v[136:139], v[188:191], v[64:67], v[136:139]\n"
      "ds_read_b128 v[64:67], v232 offset:8192\n"
      "v_cvt_pk_bf16_f32 v209, v134, v135\n"
      "v_mfma_f32_16x16x32_bf16 v[136:139], v[204:207], v[68:71], v[136:139]\n"
      "ds_read_b128 v[68:71], v233 offset:8192\n"
      "v_mfma_f32_16x16x32_bf16 v[136:139], a[24:27], v[84:87], v[136:139]\n"
      "ds_read_b128 v[84:87], v234 offset:8192\n"
      "v_mfma_f32_16x16x32_bf16 v[136:139], a[28:31], v[88:91], v[136:139]\n"
      "ds_read_b128 v[88:91], v235 offset:8192\n"
      "v_mfma_f32_16x16x32_bf16 v[136:139], a[32:35], v[92:95], v[136:139]\n"
      "ds_read_b128 v[92:95], v232 offset:16384\n"
      "v_mfma_f32_16x16x32_bf16 v[136:139], a[36:39], v[108:111], v[136:139]\n"
      "ds_read_b128 v[108:111], v233 offset:16384\n"
      "v_mfma_f32_16x16x32_bf16 v[136:139], a[40:43], v[112:115], v[136:139]\n"
      "ds_read_b128 v[112:115], v234 offset:16384\n"
      "v_mfma_f32_16x16x32_bf16 v[136:139], a[44:47], v[116:119], v[136:139]\n"
      "ds_read_b128 v[116:119], v235 offset:16384\n"
      "s_waitcnt lgkmcnt(8)\n"
      "v_mfma_f32_16x16x32_bf16 v[132:135], a[240:243], v[36:39], v[0:3]\n"
      "ds_write_b64 v244, v[208:209] offset:24704\n"
      "v_mfma_f32_16x16x32_bf16 v[132:135], a[244:247], v[40:43], v[132:135]\n"
      "v_lshlrev_b32 v20, 16, v202\n"
      "v_mfma_f32_16x16x32_bf16 v[132:135], a[248:251], v[44:47], v[132:135]\n"
      "v_and_b32 v21, 0xffff0000, v202\n"
      "v_mfma_f32_16x16x32_bf16 v[132:135], a[252:255], v[60:63], v[132:135]\n"
      "v_lshlrev_b32 v22, 16, v203\n"
      "global_store_dwordx4 v247, v[96:99], s[54:55] offset:768 sc1\n"
      "s_waitcnt lgkmcnt(5)\n"
      "v_mfma_f32_16x16x32_bf16 v[132:135], a[48:51], v[64:67], v[132:135]\n"
      "v_and_b32 v23, 0xffff0000, v203\n"
      "v_mfma_f32_16x16x32_bf16 v[132:135], a[52:55], v[68:71], v[132:135]\n"
      "v_cvt_pk_bf16_f32 v210, v136, v137\n"
      "v_mfma_f32_16x16x32_bf16 v[132:135], a[56:59], v[84:87], v[132:135]\n"
      "v_cvt_pk_bf16_f32 v211, v138, v139\n"
      "v_mfma_f32_16x16x32_bf16 v[132:135], a[60:63], v[88:91], v[132:135]\n"
      "ds_read_b128 v[24:27], v246 offset:12288\n"
      "s_waitcnt lgkmcnt(2)\n"
      "v_mfma_f32_16x16x32_bf16 v[132:135], a[64:67], v[92:95], v[132:135]\n"
      "ds_read_b128 v[28:31], v246 offset:13312\n"
      "v_mfma_f32_16x16x32_bf16 v[132:135], a[68:71], v[108:111], v[132:135]\n"
      "ds_read_b128 v[32:35], v246 offset:14336\n"
      "v_mfma_f32_16x16x32_bf16 v[132:135], a[72:75], v[112:115], v[132:135]\n"
      "v_mfma_f32_16x16x32_bf16 v[132:135], a[76:79], v[116:119], v[132:135]\n"
      "ds_write_b64 v244, v[210:211] offset:24712\n"
      "v_mfma_f32_16x16x32_bf16 v[136:139], a[80:83], v[36:39], v[4:7]\n"
      "v_mfma_f32_16x16x32_bf16 v[136:139], a[84:87], v[40:43], v[136:139]\n"
      "v_mfma_f32_16x16x32_bf16 v[136:139], a[88:91], v[44:47], v[136:139]\n"
      "v_mfma_f32_16x16x32_bf16 v[136:139], a[92:95], v[60:63], v[136:139]\n"
      "v_cvt_pk_bf16_f32 v208, v132, v133\n"
      "global_store_dwordx4 v252, v[100:103], s[54:55] offset:768 sc1\n"
      "v_mfma_f32_16x16x32_bf16 v[136:139], a[96:99], v[64:67], v[136:139]\n"
      "v_cvt_pk_bf16_f32 v209, v134, v135\n"
      "v_mfma_f32_16x16x32_bf16 v[136:139], a[100:103], v[68:71], v[136:139]\n"
      "v_mfma_f32_16x16x32_bf16 v[136:139], a[104:107], v[84:87], v[136:139]\n"
      "v_mfma_f32_16x16x32_bf16 v[136:139], a[108:111], v[88:91], v[136:139]\n"
      "v_mfma_f32_16x16x32_bf16 v[136:139], a[112:115], v[92:95], v[136:139]\n"
      "v_mfma_f32_16x16x32_bf16 v[136:139], a[116:119], v[108:111], v[136:139]\n"
      "ds_write_b64 v244, v[208:209] offset:0\n"
      "v_mfma_f32_16x16x32_bf16 v[136:139], a[120:123], v[112:115], v[136:139]\n"
      "v_mfma_f32_16x16x32_bf16 v[136:139], a[124:127], v[116:119], v[136:139]\n"
      "v_mfma_f32_16x16x32_bf16 v[132:135], a[128:131], v[36:39], v[8:11]\n"
      "v_mfma_f32_16x16x32_bf16 v[132:135], a[132:135], v[40:43], v[132:135]\n"
      "v_mfma_f32_16x16x32_bf16 v[132:135], a[136:139], v[44:47], v[132:135]\n"
      "v_mfma_f32_16x16x32_bf16 v[132:135], a[140:143], v[60:63], v[132:135]\n"
      "v_cvt_pk_bf16_f32 v210, v136, v137\n"
      "v_mfma_f32_16x16x32_bf16 v[132:135], a[144:147], v[64:67], v[132:135]\n"
      "v_cvt_pk_bf16_f32 v211, v138, v139\n"
      "v_mfma_f32_16x16x32_bf16 v[132:135], a[148:151], v[68:71], v[132:135]\n"
      "global_store_dwordx4 v253, v[104:107], s[54:55] offset:768 sc1\n"
      "v_mfma_f32_16x16x32_bf16 v[132:135], a[152:155], v[84:87], v[132:135]\n"
      "v_mfma_f32_16x16x32_bf16 v[132:135], a[156:159], v[88:91], v[132:135]\n"
      "s_lshl_b32 s79, s96, 1\n"
      "v_mfma_f32_16x16x32_bf16 v[132:135], a[160:163], v[92:95], v[132:135]\n"
      "v_mad_i32_i24 v252, v251, s79, v252\n"
      "v_mfma_f32_16x16x32_bf16 v[132:135], a[164:167], v[108:111], v[132:135]\n"
      "ds_write_b64 v244, v[210:211] offset:8\n"
      "v_mfma_f32_16x16x32_bf16 v[132:135], a[168:171], v[112:115], v[132:135]\n"
      "s_mul_i32 s79, s79, s100\n"
      "v_mfma_f32_16x16x32_bf16 v[132:135], a[172:175], v[116:119], v[132:135]\n"
      "v_add_u32 v247, s79, v247\n"
      "v_mfma_f32_16x16x32_bf16 v[136:139], a[176:179], v[36:39], v[12:15]\n"
      "s_mul_i32 s79, s79, s101\n"
      "v_mfma_f32_16x16x32_bf16 v[136:139], a[180:183], v[40:43], v[136:139]\n"
      "v_add_u32 v253, s79, v253\n"
      "v_mfma_f32_16x16x32_bf16 v[136:139], a[184:187], v[44:47], v[136:139]\n"
      "s_add_u32 s54, s30, s39\n"
      "s_addc_u32 s55, s31, 0\n"
      "v_mfma_f32_16x16x32_bf16 v[136:139], a[188:191], v[60:63], v[136:139]\n"
      "v_cvt_pk_bf16_f32 v212, v132, v133\n"
      "v_mfma_f32_16x16x32_bf16 v[136:139], a[192:195], v[64:67], v[136:139]\n"
      "v_cvt_pk_bf16_f32 v213, v134, v135\n"
      "v_mfma_f32_16x16x32_bf16 v[136:139], a[196:199], v[68:71], v[136:139]\n"
      "global_store_dwordx4 v247, v[120:123], s[54:55] offset:0 sc1\n"
      "v_mfma_f32_16x16x32_bf16 v[136:139], a[200:203], v[84:87], v[136:139]\n"
      "v_mfma_f32_16x16x32_bf16 v[136:139], a[204:207], v[88:91], v[136:139]\n"
      "v_mfma_f32_16x16x32_bf16 v[136:139], a[208:211], v[92:95], v[136:139]\n"
      "v_mfma_f32_16x16x32_bf16 v[136:139], a[212:215], v[108:111], v[136:139]\n"
      "ds_write_b64 v245, v[212:213] offset:0\n"
      "v_mfma_f32_16x16x32_bf16 v[136:139], a[216:219], v[112:115], v[136:139]\n"
      "v_mfma_f32_16x16x32_bf16 v[136:139], a[220:223], v[116:119], v[136:139]\n"
      "v_mfma_f32_16x16x32_bf16 v[132:135], a[224:227], v[36:39], v[16:19]\n"
      "v_mfma_f32_16x16x32_bf16 v[132:135], a[228:231], v[40:43], v[132:135]\n"
      "v_mfma_f32_16x16x32_bf16 v[132:135], a[232:235], v[44:47], v[132:135]\n"
      "v_mfma_f32_16x16x32_bf16 v[132:135], a[236:239], v[60:63], v[132:135]\n"
      "v_cvt_pk_bf16_f32 v214, v136, v137\n"
      "v_mfma_f32_16x16x32_bf16 v[132:135], a[0:3], v[64:67], v[132:135]\n"
      "v_cvt_pk_bf16_f32 v215, v138, v139\n"
      "v_mfma_f32_16x16x32_bf16 v[132:135], a[4:7], v[68:71], v[132:135]\n"
      "v_mfma_f32_16x16x32_bf16 v[132:135], a[8:11], v[84:87], v[132:135]\n"
      "global_store_dwordx4 v252, v[124:127], s[54:55] offset:0 sc1\n"
      "v_mfma_f32_16x16x32_bf16 v[132:135], a[12:15], v[88:91], v[132:135]\n"
      "v_mfma_f32_16x16x32_bf16 v[132:135], a[16:19], v[92:95], v[132:135]\n"
      "v_mfma_f32_16x16x32_bf16 v[132:135], a[20:23], v[108:111], v[132:135]\n"
      "ds_write_b64 v245, v[214:215] offset:8\n"
      "v_mfma_f32_16x16x32_bf16 v[132:135], v[140:143], v[112:115], v[132:135]\n"
      "v_mfma_f32_16x16x32_bf16 v[132:135], v[156:159], v[116:119], v[132:135]\n"
      "s_waitcnt lgkmcnt(4)\n"
      "s_barrier\n"
      "v_mfma_f32_16x16x32_bf16 v[136:139], v[160:163], v[36:39], v[20:23]\n"
      "ds_read_b128 v[36:39], v232 offset:256\n"
      "v_mfma_f32_16x16x32_bf16 v[136:139], v[164:167], v[40:43], v[136:139]\n"
      "ds_read_b128 v[40:43], v233 offset:256\n"
      "v_mfma_f32_16x16x32_bf16 v[136:139], v[180:183], v[44:47], v[136:139]\n"
      "ds_read_b128 v[44:47], v234 offset:256\n"
      "v_mfma_f32_16x16x32_bf16 v[136:139], v[184:187], v[60:63], v[136:139]\n"
      "ds_read_b128 v[60:63], v235 offset:256\n"
      "v_cvt_pk_bf16_f32 v208, v132, v133\n"
      "v_mfma_f32_16x16x32_bf16 v[136:139], v[188:191], v[64:67], v[136:139]\n"
      "ds_read_b128 v[64:67], v232 offset:8448\n"
      "v_cvt_pk_bf16_f32 v209, v134, v135\n"
      "v_mfma_f32_16x16x32_bf16 v[136:139], v[204:207], v[68:71], v[136:139]\n"
      "ds_read_b128 v[68:71], v233 offset:8448\n"
      "v_mfma_f32_16x16x32_bf16 v[136:139], a[24:27], v[84:87], v[136:139]\n"
      "ds_read_b128 v[84:87], v234 offset:8448\n"
      "v_mfma_f32_16x16x32_bf16 v[136:139], a[28:31], v[88:91], v[136:139]\n"
      "ds_read_b128 v[88:91], v235 offset:8448\n"
      "v_mfma_f32_16x16x32_bf16 v[136:139], a[32:35], v[92:95], v[136:139]\n"
      "ds_read_b128 v[92:95], v232 offset:16640\n"
      "v_mfma_f32_16x16x32_bf16 v[136:139], a[36:39], v[108:111], v[136:139]\n"
      "ds_read_b128 v[108:111], v233 offset:16640\n"
      "v_mfma_f32_16x16x32_bf16 v[136:139], a[40:43], v[112:115], v[136:139]\n"
      "ds_read_b128 v[112:115], v234 offset:16640\n"
      "v_mfma_f32_16x16x32_bf16 v[136:139], a[44:47], v[116:119], v[136:139]\n"
      "ds_read_b128 v[116:119], v235 offset:16640\n"
      "s_waitcnt lgkmcnt(8)\n"
      "v_mfma_f32_16x16x32_bf16 v[132:135], a[240:243], v[36:39], v[0:3]\n"
      "ds_write_b64 v244, v[208:209] offset:128\n"
      "v_mfma_f32_16x16x32_bf16 v[132:135], a[244:247], v[40:43], v[132:135]\n"
      "v_mfma_f32_16x16x32_bf16 v[132:135], a[248:251], v[44:47], v[132:135]\n"
      "v_mfma_f32_16x16x32_bf16 v[132:135], a[252:255], v[60:63], v[132:135]\n"
      "v_cvt_pk_bf16_f32 v210, v136, v137\n"
      "global_store_dwordx4 v253, v[128:131], s[54:55] offset:0 sc1\n"
      "s_waitcnt lgkmcnt(5)\n"
      "v_mfma_f32_16x16x32_bf16 v[132:135], a[48:51], v[64:67], v[132:135]\n"
      "v_cvt_pk_bf16_f32 v211, v138, v139\n"
      "v_mfma_f32_16x16x32_bf16 v[132:135], a[52:55], v[68:71], v[132:135]\n"
      "v_mfma_f32_16x16x32_bf16 v[132:135], a[56:59], v[84:87], v[132:135]\n"
      "ds_read_b128 v[48:51], v246 offset:24576\n"
      "v_mfma_f32_16x16x32_bf16 v[132:135], a[60:63], v[88:91], v[132:135]\n"
      "ds_read_b128 v[52:55], v246 offset:25600\n"
      "s_waitcnt lgkmcnt(3)\n"
      "v_mfma_f32_16x16x32_bf16 v[132:135], a[64:67], v[92:95], v[132:135]\n"
      "ds_read_b128 v[56:59], v246 offset:26624\n"
      "v_mfma_f32_16x16x32_bf16 v[132:135], a[68:71], v[108:111], v[132:135]\n"
      "ds_write_b64 v244, v[210:211] offset:136\n"
      "v_mfma_f32_16x16x32_bf16 v[132:135], a[72:75], v[112:115], v[132:135]\n"
      "v_mfma_f32_16x16x32_bf16 v[132:135], a[76:79], v[116:119], v[132:135]\n"
      "v_mfma_f32_16x16x32_bf16 v[136:139], a[80:83], v[36:39], v[4:7]\n"
      "v_mfma_f32_16x16x32_bf16 v[136:139], a[84:87], v[40:43], v[136:139]\n"
      "v_mfma_f32_16x16x32_bf16 v[136:139], a[88:91], v[44:47], v[136:139]\n"
      "v_mfma_f32_16x16x32_bf16 v[136:139], a[92:95], v[60:63], v[136:139]\n"
      "v_cvt_pk_bf16_f32 v208, v132, v133\n"
      "global_store_dwordx4 v247, v[144:147], s[54:55] offset:768 sc1\n"
      "v_mfma_f32_16x16x32_bf16 v[136:139], a[96:99], v[64:67], v[136:139]\n"
      "v_cvt_pk_bf16_f32 v209, v134, v135\n"
      "v_mfma_f32_16x16x32_bf16 v[136:139], a[100:103], v[68:71], v[136:139]\n"
      "v_mfma_f32_16x16x32_bf16 v[136:139], a[104:107], v[84:87], v[136:139]\n"
      "v_mfma_f32_16x16x32_bf16 v[136:139], a[108:111], v[88:91], v[136:139]\n"
      "v_mfma_f32_16x16x32_bf16 v[136:139], a[112:115], v[92:95], v[136:139]\n"
      "v_mfma_f32_16x16x32_bf16 v[136:139], a[116:119], v[108:111], v[136:139]\n"
      "ds_write_b64 v244, v[208:209] offset:12288\n"
      "v_mfma_f32_16x16x32_bf16 v[136:139], a[120:123], v[112:115], v[136:139]\n"
      "v_mfma_f32_16x16x32_bf16 v[136:139], a[124:127], v[116:119], v[136:139]\n"
      "v_mfma_f32_16x16x32_bf16 v[132:135], a[128:131], v[36:39], v[8:11]\n"
      "v_mfma_f32_16x16x32_bf16 v[132:135], a[132:135], v[40:43], v[132:135]\n"
      "v_mfma_f32_16x16x32_bf16 v[132:135], a[136:139], v[44:47], v[132:135]\n"
      "v_mfma_f32_16x16x32_bf16 v[132:135], a[140:143], v[60:63], v[132:135]\n"
      "v_cvt_pk_bf16_f32 v210, v136, v137\n"
      "v_mfma_f32_16x16x32_bf16 v[132:135], a[144:147], v[64:67], v[132:135]\n"
      "v_cvt_pk_bf16_f32 v211, v138, v139\n"
      "v_mfma_f32_16x16x32_bf16 v[132:135], a[148:151], v[68:71], v[132:135]\n"
      "global_store_dwordx4 v252, v[148:151], s[54:55] offset:768 sc1\n"
      "v_mfma_f32_16x16x32_bf16 v[132:135], a[152:155], v[84:87], v[132:135]\n"
      "v_mfma_f32_16x16x32_bf16 v[132:135], a[156:159], v[88:91], v[132:135]\n"
      "v_mfma_f32_16x16x32_bf16 v[132:135], a[160:163], v[92:95], v[132:135]\n"
      "v_mfma_f32_16x16x32_bf16 v[132:135], a[164:167], v[108:111], v[132:135]\n"
      "ds_write_b64 v244, v[210:211] offset:12296\n"
      "v_mfma_f32_16x16x32_bf16 v[132:135], a[168:171], v[112:115], v[132:135]\n"
      "v_mfma_f32_16x16x32_bf16 v[132:135], a[172:175], v[116:119], v[132:135]\n"
      "v_mfma_f32_16x16x32_bf16 v[136:139], a[176:179], v[36:39], v[12:15]\n"
      "v_mfma_f32_16x16x32_bf16 v[136:139], a[180:183], v[40:43], v[136:139]\n"
      "v_mfma_f32_16x16x32_bf16 v[136:139], a[184:187], v[44:47], v[136:139]\n"
      "v_mfma_f32_16x16x32_bf16 v[136:139], a[188:191], v[60:63], v[136:139]\n"
      "v_cvt_pk_bf16_f32 v212, v132, v133\n"
      "v_mfma_f32_16x16x32_bf16 v[136:139], a[192:195], v[64:67], v[136:139]\n"
      "v_cvt_pk_bf16_f32 v213, v134, v135\n"
      "v_mfma_f32_16x16x32_bf16 v[136:139], a[196:199], v[68:71], v[136:139]\n"
      "v_mfma_f32_16x16x32_bf16 v[136:139], a[200:203], v[84:87], v[136:139]\n"
      "global_store_dwordx4 v253, v[152:155], s[54:55] offset:768 sc1\n"
      "v_mfma_f32_16x16x32_bf16 v[136:139], a[204:207], v[88:91], v[136:139]\n"
      "v_mfma_f32_16x16x32_bf16 v[136:139], a[208:211], v[92:95], v[136:139]\n"
      "v_mfma_f32_16x16x32_bf16 v[136:139], a[212:215], v[108:111], v[136:139]\n"
      "ds_write_b64 v245, v[212:213] offset:12288\n"
      "v_mfma_f32_16x16x32_bf16 v[136:139], a[216:219], v[112:115], v[136:139]\n"
      "v_mfma_f32_16x16x32_bf16 v[136:139], a[220:223], v[116:119], v[136:139]\n"
      "v_mfma_f32_16x16x32_bf16 v[132:135], a[224:227], v[36:39], v[16:19]\n"
      "v_mfma_f32_16x16x32_bf16 v[132:135], a[228:231], v[40:43], v[132:135]\n"
      "v_mfma_f32_16x16x32_bf16 v[132:135], a[232:235], v[44:47], v[132:135]\n"
      "v_mfma_f32_16x16x32_bf16 v[132:135], a[236:239], v[60:63], v[132:135]\n"
      "v_cvt_pk_bf16_f32 v214, v136, v137\n"
      "s_add_u32 s54, s44, s39\n"
      "s_addc_u32 s55, s45, 0\n"
      "v_mfma_f32_16x16x32_bf16 v[132:135], a[0:3], v[64:67], v[132:135]\n"
      "v_cvt_pk_bf16_f32 v215, v138, v139\n"
      "v_mfma_f32_16x16x32_bf16 v[132:135], a[4:7], v[68:71], v[132:135]\n"
      "v_mfma_f32_16x16x32_bf16 v[132:135], a[8:11], v[84:87], v[132:135]\n"
      "global_store_dwordx4 v247, v[24:27], s[54:55] offset:0 sc1\n"
      "v_mfma_f32_16x16x32_bf16 v[132:135], a[12:15], v[88:91], v[132:135]\n"
      "v_mfma_f32_16x16x32_bf16 v[132:135], a[16:19], v[92:95], v[132:135]\n"
      "v_mfma_f32_16x16x32_bf16 v[132:135], a[20:23], v[108:111], v[132:135]\n"
      "ds_write_b64 v245, v[214:215] offset:12296\n"
      "v_mfma_f32_16x16x32_bf16 v[132:135], v[140:143], v[112:115], v[132:135]\n"
      "v_mfma_f32_16x16x32_bf16 v[132:135], v[156:159], v[116:119], v[132:135]\n"
      "s_waitcnt vmcnt(10) lgkmcnt(4)\n"
      "s_barrier\n"
      "v_mfma_f32_16x16x32_bf16 v[136:139], v[160:163], v[36:39], v[20:23]\n"
      "ds_read_b128 v[36:39], v232 offset:24576\n"
      "v_mfma_f32_16x16x32_bf16 v[136:139], v[164:167], v[40:43], v[136:139]\n"
      "ds_read_b128 v[40:43], v233 offset:24576\n"
      "v_mfma_f32_16x16x32_bf16 v[136:139], v[180:183], v[44:47], v[136:139]\n"
      "ds_read_b128 v[44:47], v234 offset:24576\n"
      "v_mfma_f32_16x16x32_bf16 v[136:139], v[184:187], v[60:63], v[136:139]\n"
      "ds_read_b128 v[60:63], v235 offset:24576\n"
      "v_cvt_pk_bf16_f32 v208, v132, v133\n"
      "v_mfma_f32_16x16x32_bf16 v[136:139], v[188:191], v[64:67], v[136:139]\n"
      "ds_read_b128 v[64:67], v232 offset:32768\n"
      "v_cvt_pk_bf16_f32 v209, v134, v135\n"
      "v_mfma_f32_16x16x32_bf16 v[136:139], v[204:207], v[68:71], v[136:139]\n"
      "ds_read_b128 v[68:71], v233 offset:32768\n"
      "v_mfma_f32_16x16x32_bf16 v[136:139], a[24:27], v[84:87], v[136:139]\n"
      "ds_read_b128 v[84:87], v234 offset:32768\n"
      "v_mfma_f32_16x16x32_bf16 v[136:139], a[28:31], v[88:91], v[136:139]\n"
      "ds_read_b128 v[88:91], v235 offset:32768\n"
      "v_mfma_f32_16x16x32_bf16 v[136:139], a[32:35], v[92:95], v[136:139]\n"
      "ds_read_b128 v[92:95], v232 offset:40960\n"
      "v_mfma_f32_16x16x32_bf16 v[136:139], a[36:39], v[108:111], v[136:139]\n"
      "ds_read_b128 v[108:111], v233 offset:40960\n"
      "v_mfma_f32_16x16x32_bf16 v[136:139], a[40:43], v[112:115], v[136:139]\n"
      "ds_read_b128 v[112:115], v234 offset:40960\n"
      "v_mfma_f32_16x16x32_bf16 v[136:139], a[44:47], v[116:119], v[136:139]\n"
      "ds_read_b128 v[116:119], v235 offset:40960\n"
      "s_waitcnt lgkmcnt(8)\n"
      "v_mfma_f32_16x16x32_bf16 v[132:135], a[240:243], v[36:39], v[0:3]\n"
      "ds_write_b64 v244, v[208:209] offset:12416\n"
      "v_mfma_f32_16x16x32_bf16 v[132:135], a[244:247], v[40:43], v[132:135]\n"
      "v_mfma_f32_16x16x32_bf16 v[132:135], a[248:251], v[44:47], v[132:135]\n"
      "v_mfma_f32_16x16x32_bf16 v[132:135], a[252:255], v[60:63], v[132:135]\n"
      "v_cvt_pk_bf16_f32 v210, v136, v137\n"
      "global_store_dwordx4 v252, v[28:31], s[54:55] offset:0 sc1\n"
      "s_waitcnt lgkmcnt(5)\n"
      "v_mfma_f32_16x16x32_bf16 v[132:135], a[48:51], v[64:67], v[132:135]\n"
      "v_cvt_pk_bf16_f32 v211, v138, v139\n"
      "v_mfma_f32_16x16x32_bf16 v[132:135], a[52:55], v[68:71], v[132:135]\n"
      "v_mfma_f32_16x16x32_bf16 v[132:135], a[56:59], v[84:87], v[132:135]\n"
      "ds_read_b128 v[72:75], v246 offset:0\n"
      "v_mfma_f32_16x16x32_bf16 v[132:135], a[60:63], v[88:91], v[132:135]\n"
      "ds_read_b128 v[76:79], v246 offset:1024\n"
      "s_waitcnt lgkmcnt(3)\n"
      "v_mfma_f32_16x16x32_bf16 v[132:135], a[64:67], v[92:95], v[132:135]\n"
      "ds_read_b128 v[80:83], v246 offset:2048\n"
      "v_mfma_f32_16x16x32_bf16 v[132:135], a[68:71], v[108:111], v[132:135]\n"
      "ds_write_b64 v244, v[210:211] offset:12424\n"
      "v_mfma_f32_16x16x32_bf16 v[132:135], a[72:75], v[112:115], v[132:135]\n"
      "v_mfma_f32_16x16x32_bf16 v[132:135], a[76:79], v[116:119], v[132:135]\n"
      "v_mfma_f32_16x16x32_bf16 v[136:139], a[80:83], v[36:39], v[4:7]\n"
      "v_mfma_f32_16x16x32_bf16 v[136:139], a[84:87], v[40:43], v[136:139]\n"
      "v_mfma_f32_16x16x32_bf16 v[136:139], a[88:91], v[44:47], v[136:139]\n"
      "v_mfma_f32_16x16x32_bf16 v[136:139], a[92:95], v[60:63], v[136:139]\n"
      "v_cvt_pk_bf16_f32 v208, v132, v133\n"
      "global_store_dwordx4 v253, v[32:35], s[54:55] offset:0 sc1\n"
      "v_mfma_f32_16x16x32_bf16 v[136:139], a[96:99], v[64:67], v[136:139]\n"
      "v_cvt_pk_bf16_f32 v209, v134, v135\n"
      "v_mfma_f32_16x16x32_bf16 v[136:139], a[100:103], v[68:71], v[136:139]\n"
      "v_mfma_f32_16x16x32_bf16 v[136:139], a[104:107], v[84:87], v[136:139]\n"
      "v_mfma_f32_16x16x32_bf16 v[136:139], a[108:111], v[88:91], v[136:139]\n"
      "v_mfma_f32_16x16x32_bf16 v[136:139], a[112:115], v[92:95], v[136:139]\n"
      "v_mfma_f32_16x16x32_bf16 v[136:139], a[116:119], v[108:111], v[136:139]\n"
      "ds_write_b64 v244, v[208:209] offset:24576\n"
      "v_mfma_f32_16x16x32_bf16 v[136:139], a[120:123], v[112:115], v[136:139]\n"
      "v_mfma_f32_16x16x32_bf16 v[136:139], a[124:127], v[116:119], v[136:139]\n"
      "v_mfma_f32_16x16x32_bf16 v[132:135], a[128:131], v[36:39], v[8:11]\n"
      "v_mfma_f32_16x16x32_bf16 v[132:135], a[132:135], v[40:43], v[132:135]\n"
      "v_mfma_f32_16x16x32_bf16 v[132:135], a[136:139], v[44:47], v[132:135]\n"
      "v_mfma_f32_16x16x32_bf16 v[132:135], a[140:143], v[60:63], v[132:135]\n"
      "v_cvt_pk_bf16_f32 v210, v136, v137\n"
      "v_mfma_f32_16x16x32_bf16 v[132:135], a[144:147], v[64:67], v[132:135]\n"
      "v_cvt_pk_bf16_f32 v211, v138, v139\n"
      "v_mfma_f32_16x16x32_bf16 v[132:135], a[148:151], v[68:71], v[132:135]\n"
      "global_store_dwordx4 v247, v[48:51], s[54:55] offset:768 sc1\n"
      "v_mfma_f32_16x16x32_bf16 v[132:135], a[152:155], v[84:87], v[132:135]\n"
      "v_mfma_f32_16x16x32_bf16 v[132:135], a[156:159], v[88:91], v[132:135]\n"
      "v_mfma_f32_16x16x32_bf16 v[132:135], a[160:163], v[92:95], v[132:135]\n"
      "v_mfma_f32_16x16x32_bf16 v[132:135], a[164:167], v[108:111], v[132:135]\n"
      "ds_write_b64 v244, v[210:211] offset:24584\n"
      "v_mfma_f32_16x16x32_bf16 v[132:135], a[168:171], v[112:115], v[132:135]\n"
      "v_mfma_f32_16x16x32_bf16 v[132:135], a[172:175], v[116:119], v[132:135]\n"
      "v_mfma_f32_16x16x32_bf16 v[136:139], a[176:179], v[36:39], v[12:15]\n"
      "v_mfma_f32_16x16x32_bf16 v[136:139], a[180:183], v[40:43], v[136:139]\n"
      "v_mfma_f32_16x16x32_bf16 v[136:139], a[184:187], v[44:47], v[136:139]\n"
      "v_mfma_f32_16x16x32_bf16 v[136:139], a[188:191], v[60:63], v[136:139]\n"
      "v_cvt_pk_bf16_f32 v212, v132, v133\n"
      "v_mfma_f32_16x16x32_bf16 v[136:139], a[192:195], v[64:67], v[136:139]\n"
      "v_cvt_pk_bf16_f32 v213, v134, v135\n"
      "v_mfma_f32_16x16x32_bf16 v[136:139], a[196:199], v[68:71], v[136:139]\n"
      "v_mfma_f32_16x16x32_bf16 v[136:139], a[200:203], v[84:87], v[136:139]\n"
      "global_store_dwordx4 v252, v[52:55], s[54:55] offset:768 sc1\n"
      "v_mfma_f32_16x16x32_bf16 v[136:139], a[204:207], v[88:91], v[136:139]\n"
      "v_mfma_f32_16x16x32_bf16 v[136:139], a[208:211], v[92:95], v[136:139]\n"
      "v_mfma_f32_16x16x32_bf16 v[136:139], a[212:215], v[108:111], v[136:139]\n"
      "ds_write_b64 v245, v[212:213] offset:24576\n"
      "v_mfma_f32_16x16x32_bf16 v[136:139], a[216:219], v[112:115], v[136:139]\n"
      "v_mfma_f32_16x16x32_bf16 v[136:139], a[220:223], v[116:119], v[136:139]\n"
      "v_mfma_f32_16x16x32_bf16 v[132:135], a[224:227], v[36:39], v[16:19]\n"
      "v_mfma_f32_16x16x32_bf16 v[132:135], a[228:231], v[40:43], v[132:135]\n"
      "v_mfma_f32_16x16x32_bf16 v[132:135], a[232:235], v[44:47], v[132:135]\n"
      "v_mfma_f32_16x16x32_bf16 v[132:135], a[236:239], v[60:63], v[132:135]\n"
      "v_cvt_pk_bf16_f32 v214, v136, v137\n"
      "v_mfma_f32_16x16x32_bf16 v[132:135], a[0:3], v[64:67], v[132:135]\n"
      "v_cvt_pk_bf16_f32 v215, v138, v139\n"
      "v_mfma_f32_16x16x32_bf16 v[132:135], a[4:7], v[68:71], v[132:135]\n"
      "v_mfma_f32_16x16x32_bf16 v[132:135], a[8:11], v[84:87], v[132:135]\n"
      "global_store_dwordx4 v253, v[56:59], s[54:55] offset:768 sc1\n"
      "v_mfma_f32_16x16x32_bf16 v[132:135], a[12:15], v[88:91], v[132:135]\n"
      "v_mfma_f32_16x16x32_bf16 v[132:135], a[16:19], v[92:95], v[132:135]\n"
      "v_mfma_f32_16x16x32_bf16 v[132:135], a[20:23], v[108:111], v[132:135]\n"
      "ds_write_b64 v245, v[214:215] offset:24584\n"
      "v_mfma_f32_16x16x32_bf16 v[132:135], v[140:143], v[112:115], v[132:135]\n"
      "v_mfma_f32_16x16x32_bf16 v[132:135], v[156:159], v[116:119], v[132:135]\n"
      "s_waitcnt lgkmcnt(4)\n"
      "s_barrier\n"
      "v_mfma_f32_16x16x32_bf16 v[136:139], v[160:163], v[36:39], v[20:23]\n"
      "ds_read_b128 v[36:39], v232 offset:24832\n"
      "s_lshl_b32 s79, s97, 1\n"
      "v_mfma_f32_16x16x32_bf16 v[136:139], v[164:167], v[40:43], v[136:139]\n"
      "ds_read_b128 v[40:43], v233 offset:24832\n"
      "v_mad_i32_i24 v252, v251, s79, v252\n"
      "v_mfma_f32_16x16x32_bf16 v[136:139], v[180:183], v[44:47], v[136:139]\n"
      "ds_read_b128 v[44:47], v234 offset:24832\n"
      "s_mul_i32 s79, s79, s100\n"
      "v_mfma_f32_16x16x32_bf16 v[136:139], v[184:187], v[60:63], v[136:139]\n"
      "ds_read_b128 v[60:63], v235 offset:24832\n"
      "v_cvt_pk_bf16_f32 v208, v132, v133\n"
      "v_add_u32 v247, s79, v247\n"
      "v_mfma_f32_16x16x32_bf16 v[136:139], v[188:191], v[64:67], v[136:139]\n"
      "ds_read_b128 v[64:67], v232 offset:33024\n"
      "v_cvt_pk_bf16_f32 v209, v134, v135\n"
      "v_mfma_f32_16x16x32_bf16 v[136:139], v[204:207], v[68:71], v[136:139]\n"
      "ds_read_b128 v[68:71], v233 offset:33024\n"
      "s_mul_i32 s79, s79, s101\n"
      "v_mfma_f32_16x16x32_bf16 v[136:139], a[24:27], v[84:87], v[136:139]\n"
      "ds_read_b128 v[84:87], v234 offset:33024\n"
      "v_add_u32 v253, s79, v253\n"
      "v_mfma_f32_16x16x32_bf16 v[136:139], a[28:31], v[88:91], v[136:139]\n"
      "ds_read_b128 v[88:91], v235 offset:33024\n"
      "s_add_u32 s54, s30, s40\n"
      "s_addc_u32 s55, s31, 0\n"
      "v_mfma_f32_16x16x32_bf16 v[136:139], a[32:35], v[92:95], v[136:139]\n"
      "ds_read_b128 v[92:95], v232 offset:41216\n"
      "v_mfma_f32_16x16x32_bf16 v[136:139], a[36:39], v[108:111], v[136:139]\n"
      "ds_read_b128 v[108:111], v233 offset:41216\n"
      "global_store_dwordx4 v247, v[72:75], s[54:55] offset:0 sc1\n"
      "v_mfma_f32_16x16x32_bf16 v[136:139], a[40:43], v[112:115], v[136:139]\n"
      "ds_read_b128 v[112:115], v234 offset:41216\n"
      "v_mfma_f32_16x16x32_bf16 v[136:139], a[44:47], v[116:119], v[136:139]\n"
      "ds_read_b128 v[116:119], v235 offset:41216\n"
      "s_waitcnt lgkmcnt(8)\n"
      "v_mfma_f32_16x16x32_bf16 v[132:135], a[240:243], v[36:39], v[0:3]\n"
      "ds_write_b64 v244, v[208:209] offset:24704\n"
      "v_mfma_f32_16x16x32_bf16 v[132:135], a[244:247], v[40:43], v[132:135]\n"
      "v_mfma_f32_16x16x32_bf16 v[132:135], a[248:251], v[44:47], v[132:135]\n"
      "v_mfma_f32_16x16x32_bf16 v[132:135], a[252:255], v[60:63], v[132:135]\n"
      "v_cvt_pk_bf16_f32 v210, v136, v137\n"
      "s_waitcnt lgkmcnt(5)\n"
      "v_mfma_f32_16x16x32_bf16 v[132:135], a[48:51], v[64:67], v[132:135]\n"
      "v_cvt_pk_bf16_f32 v211, v138, v139\n"
      "v_mfma_f32_16x16x32_bf16 v[132:135], a[52:55], v[68:71], v[132:135]\n"
      "v_mfma_f32_16x16x32_bf16 v[132:135], a[56:59], v[84:87], v[132:135]\n"
      "ds_read_b128 v[96:99], v246 offset:12288\n"
      "v_mfma_f32_16x16x32_bf16 v[132:135], a[60:63], v[88:91], v[132:135]\n"
      "ds_read_b128 v[100:103], v246 offset:13312\n"
      "s_waitcnt lgkmcnt(3)\n"
      "v_mfma_f32_16x16x32_bf16 v[132:135], a[64:67], v[92:95], v[132:135]\n"
      "ds_read_b128 v[104:107], v246 offset:14336\n"
      "v_mfma_f32_16x16x32_bf16 v[132:135], a[68:71], v[108:111], v[132:135]\n"
      "ds_write_b64 v244, v[210:211] offset:24712\n"
      "v_mfma_f32_16x16x32_bf16 v[132:135], a[72:75], v[112:115], v[132:135]\n"
      "v_mfma_f32_16x16x32_bf16 v[132:135], a[76:79], v[116:119], v[132:135]\n"
      "v_mfma_f32_16x16x32_bf16 v[136:139], a[80:83], v[36:39], v[4:7]\n"
      "v_mfma_f32_16x16x32_bf16 v[136:139], a[84:87], v[40:43], v[136:139]\n"
      "global_store_dwordx4 v252, v[76:79], s[54:55] offset:0 sc1\n"
      "v_mfma_f32_16x16x32_bf16 v[136:139], a[88:91], v[44:47], v[136:139]\n"
      "LPD16:\n"
      "s_cbranch_execz LRET\n"
      "v_mfma_f32_16x16x32_bf16 v[136:139], a[92:95], v[60:63], v[136:139]\n"
      "v_cvt_pk_bf16_f32 v208, v132, v133\n"
      "v_mfma_f32_16x16x32_bf16 v[136:139], a[96:99], v[64:67], v[136:139]\n"
      "v_cvt_pk_bf16_f32 v209, v134, v135\n"
      "v_mfma_f32_16x16x32_bf16 v[136:139], a[100:103], v[68:71], v[136:139]\n"
      "v_mfma_f32_16x16x32_bf16 v[136:139], a[104:107], v[84:87], v[136:139]\n"
      "v_mfma_f32_16x16x32_bf16 v[136:139], a[108:111], v[88:91], v[136:139]\n"
      "v_mfma_f32_16x16x32_bf16 v[136:139], a[112:115], v[92:95], v[136:139]\n"
      "v_mfma_f32_16x16x32_bf16 v[136:139], a[116:119], v[108:111], v[136:139]\n"
      "ds_write_b64 v244, v[208:209] offset:0\n"
      "v_mfma_f32_16x16x32_bf16 v[136:139], a[120:123], v[112:115], v[136:139]\n"
      "v_mfma_f32_16x16x32_bf16 v[136:139], a[124:127], v[116:119], v[136:139]\n"
      "v_mfma_f32_16x16x32_bf16 v[132:135], a[128:131], v[36:39], v[8:11]\n"
      "global_store_dwordx4 v253, v[80:83], s[54:55] offset:0 sc1\n"
      "v_mfma_f32_16x16x32_bf16 v[132:135], a[132:135], v[40:43], v[132:135]\n"
      "v_mfma_f32_16x16x32_bf16 v[132:135], a[136:139], v[44:47], v[132:135]\n"
      "v_mfma_f32_16x16x32_bf16 v[132:135], a[140:143], v[60:63], v[132:135]\n"
      "v_cvt_pk_bf16_f32 v210, v136, v137\n"
      "v_mfma_f32_16x16x32_bf16 v[132:135], a[144:147], v[64:67], v[132:135]\n"
      "v_cvt_pk_bf16_f32 v211, v138, v139\n"
      "v_mfma_f32_16x16x32_bf16 v[132:135], a[148:151], v[68:71], v[132:135]\n"
      "v_mfma_f32_16x16x32_bf16 v[132:135], a[152:155], v[84:87], v[132:135]\n"
      "v_mfma_f32_16x16x32_bf16 v[132:135], a[156:159], v[88:91], v[132:135]\n"
      "v_mfma_f32_16x16x32_bf16 v[132:135], a[160:163], v[92:95], v[132:135]\n"
      "v_mfma_f32_16x16x32_bf16 v[132:135], a[164:167], v[108:111], v[132:135]\n"
      "ds_write_b64 v244, v[210:211] offset:8\n"
      "v_mfma_f32_16x16x32_bf16 v[132:135], a[168:171], v[112:115], v[132:135]\n"
      "s_waitcnt lgkmcnt(5)\n"
      "v_mfma_f32_16x16x32_bf16 v[132:135], a[172:175], v[116:119], v[132:135]\n"
      "global_store_dwordx4 v247, v[96:99], s[54:55] offset:768 sc1\n"
      "v_mfma_f32_16x16x32_bf16 v[136:139], a[176:179], v[36:39], v[12:15]\n"
      "v_mfma_f32_16x16x32_bf16 v[136:139], a[180:183], v[40:43], v[136:139]\n"
      "v_mfma_f32_16x16x32_bf16 v[136:139], a[184:187], v[44:47], v[136:139]\n"
      "v_mfma_f32_16x16x32_bf16 v[136:139], a[188:191], v[60:63], v[136:139]\n"
      "v_cvt_pk_bf16_f32 v212, v132, v133\n"
      "v_mfma_f32_16x16x32_bf16 v[136:139], a[192:195], v[64:67], v[136:139]\n"
      "v_cvt_pk_bf16_f32 v213, v134, v135\n"
      "v_mfma_f32_16x16x32_bf16 v[136:139], a[196:199], v[68:71], v[136:139]\n"
      "v_mfma_f32_16x16x32_bf16 v[136:139], a[200:203], v[84:87], v[136:139]\n"
      "v_mfma_f32_16x16x32_bf16 v[136:139], a[204:207], v[88:91], v[136:139]\n"
      "v_mfma_f32_16x16x32_bf16 v[136:139], a[208:211], v[92:95], v[136:139]\n"
      "s_waitcnt lgkmcnt(4)\n"
      "v_mfma_f32_16x16x32_bf16 v[136:139], a[212:215], v[108:111], v[136:139]\n"
      "ds_write_b64 v245, v[212:213] offset:0\n"
      "v_mfma_f32_16x16x32_bf16 v[136:139], a[216:219], v[112:115], v[136:139]\n"
      "global_store_dwordx4 v252, v[100:103], s[54:55] offset:768 sc1\n"
      "v_mfma_f32_16x16x32_bf16 v[136:139], a[220:223], v[116:119], v[136:139]\n"
      "v_mfma_f32_16x16x32_bf16 v[132:135], a[224:227], v[36:39], v[16:19]\n"
      "v_mfma_f32_16x16x32_bf16 v[132:135], a[228:231], v[40:43], v[132:135]\n"
      "v_mfma_f32_16x16x32_bf16 v[132:135], a[232:235], v[44:47], v[132:135]\n"
      "v_mfma_f32_16x16x32_bf16 v[132:135], a[236:239], v[60:63], v[132:135]\n"
      "v_cvt_pk_bf16_f32 v214, v136, v137\n"
      "v_mfma_f32_16x16x32_bf16 v[132:135], a[0:3], v[64:67], v[132:135]\n"
      "v_cvt_pk_bf16_f32 v215, v138, v139\n"
      "v_mfma_f32_16x16x32_bf16 v[132:135], a[4:7], v[68:71], v[132:135]\n"
      "v_mfma_f32_16x16x32_bf16 v[132:135], a[8:11], v[84:87], v[132:135]\n"
      "v_mfma_f32_16x16x32_bf16 v[132:135], a[12:15], v[88:91], v[132:135]\n"
      "s_waitcnt lgkmcnt(4)\n"
      "v_mfma_f32_16x16x32_bf16 v[132:135], a[16:19], v[92:95], v[132:135]\n"
      "global_store_dwordx4 v253, v[104:107], s[54:55] offset:768 sc1\n"
      "v_mfma_f32_16x16x32_bf16 v[132:135], a[20:23], v[108:111], v[132:135]\n"
      "ds_write_b64 v245, v[214:215] offset:8\n"
      "v_mfma_f32_16x16x32_bf16 v[132:135], v[140:143], v[112:115], v[132:135]\n"
      "v_mfma_f32_16x16x32_bf16 v[132:135], v[156:159], v[116:119], v[132:135]\n"
      "s_waitcnt lgkmcnt(4)\n"
      "s_barrier\n"
      "v_mfma_f32_16x16x32_bf16 v[136:139], v[160:163], v[36:39], v[20:23]\n"
      "v_mfma_f32_16x16x32_bf16 v[136:139], v[164:167], v[40:43], v[136:139]\n"
      "v_mfma_f32_16x16x32_bf16 v[136:139], v[180:183], v[44:47], v[136:139]\n"
      "v_mfma_f32_16x16x32_bf16 v[136:139], v[184:187], v[60:63], v[136:139]\n"
      "v_cvt_pk_bf16_f32 v208, v132, v133\n"
      "v_mfma_f32_16x16x32_bf16 v[136:139], v[188:191], v[64:67], v[136:139]\n"
      "v_cvt_pk_bf16_f32 v209, v134, v135\n"
      "v_mfma_f32_16x16x32_bf16 v[136:139], v[204:207], v[68:71], v[136:139]\n"
      "v_mfma_f32_16x16x32_bf16 v[136:139], a[24:27], v[84:87], v[136:139]\n"
      "v_mfma_f32_16x16x32_bf16 v[136:139], a[28:31], v[88:91], v[136:139]\n"
      "v_mfma_f32_16x16x32_bf16 v[136:139], a[32:35], v[92:95], v[136:139]\n"
      "v_mfma_f32_16x16x32_bf16 v[136:139], a[36:39], v[108:111], v[136:139]\n"
      "ds_write_b64 v244, v[208:209] offset:128\n"
      "v_mfma_f32_16x16x32_bf16 v[136:139], a[40:43], v[112:115], v[136:139]\n"
      "v_mfma_f32_16x16x32_bf16 v[136:139], a[44:47], v[116:119], v[136:139]\n"
      "ds_read_b128 v[120:123], v246 offset:24576\n"
      "ds_read_b128 v[124:127], v246 offset:25600\n"
      "ds_read_b128 v[128:131], v246 offset:26624\n"
      "s_add_u32 s54, s44, s40\n"
      "s_addc_u32 s55, s45, 0\n"
      "s_nop 3\n"
      "v_cvt_pk_bf16_f32 v210, v136, v137\n"
      "v_cvt_pk_bf16_f32 v211, v138, v139\n"
      "ds_write_b128 v244, v[208:211] offset:128\n"
      "s_waitcnt lgkmcnt(3)\n"
      "global_store_dwordx4 v247, v[120:123], s[54:55] offset:0 sc1\n"
      "s_waitcnt lgkmcnt(2)\n"
      "global_store_dwordx4 v252, v[124:127], s[54:55] offset:0 sc1\n"
      "s_waitcnt lgkmcnt(1)\n"
      "global_store_dwordx4 v253, v[128:131], s[54:55] offset:0 sc1\n"
      "s_waitcnt lgkmcnt(0)\n"
      "s_barrier\n"
      "ds_read_b128 v[144:147], v246 offset:0\n"
      "ds_read_b128 v[148:151], v246 offset:1024\n"
      "ds_read_b128 v[152:155], v246 offset:2048\n"
      "s_waitcnt lgkmcnt(2)\n"
      "global_store_dwordx4 v247, v[144:147], s[54:55] offset:768 sc1\n"
      "s_waitcnt lgkmcnt(1)\n"
      "global_store_dwordx4 v252, v[148:151], s[54:55] offset:768 sc1\n"
      "s_waitcnt lgkmcnt(0)\n"
      "global_store_dwordx4 v253, v[152:155], s[54:55] offset:768 sc1\n"
      "s_cmp_lg_u32 s87, 0\n"
      "s_cbranch_scc1 LE1\n"
      "v_min_u32 v0, 0x370, v239\n"
      "v_add_u32 v0, 0x2480, v0\n"
      "s_mov_b32 s82, 0x40000\n"
      "LE0:\n"
      "global_load_dwordx4 v[4:7], v0, s[28:29] sc1\n"
      "s_sub_u32 s82, s82, 1\n"
      "s_cbranch_scc1 LE1\n"
      "s_waitcnt vmcnt(0)\n"
      "v_cmp_ne_u64 vcc, s[80:81], v[4:5]\n"
      "v_cmp_ne_u64 s[56:57], s[80:81], v[6:7]\n"
      "s_nop 3\n"
      "s_or_b64 vcc, vcc, s[56:57]\n"
      "s_cbranch_scc1 LE0\n"
      "v_mov_b32 v0, 0\n"
      "v_mov_b32 v2, s80\n"
      "v_mov_b32 v3, s81\n"
      "s_mov_b64 exec, 1\n"
      "global_store_dwordx2 v0, v[2:3], s[28:29] sc1\n"
      "s_mov_b64 exec, -1\n"
      "LE1:\n"
      "s_waitcnt vmcnt(0)\n"
      "s_branch LCEND\n"
      "LPS:\n"
      "s_mov_b64 exec, 1\n"
      "global_store_dword v229, v230, s[28:29] sc1\n"
      "s_mov_b64 exec, -1\n"
      "s_branch LPR\n"
      "LT0:\n"
      "s_nop 15\n"
      "s_nop 3\n"
      "s_branch LS0\n"
      "LT1:\n"
      "s_nop 15\n"
      "s_nop 3\n"
      "s_branch LS1\n"
      "LT2:\n"
      "s_nop 15\n"
      "s_nop 3\n"
      "s_branch LS2\n"
      "LT3:\n"
      "s_nop 15\n"
      "s_nop 3\n"
      "s_branch LS3\n"
      "LT4:\n"
      "s_nop 15\n"
      "s_nop 3\n"
      "s_branch LS4\n"
      "LT5:\n"
      "s_nop 15\n"
      "s_nop 3\n"
      "s_branch LS5\n"
      "LT6:\n"
      "s_nop 15\n"
      "s_nop 3\n"
      "s_branch LS6\n"
      "LT7:\n"
      "s_nop 15\n"
      "s_nop 3\n"
      "s_branch LS7\n"
      "LT8:\n"
      "s_nop 15\n"
      "s_nop 3\n"
      "s_branch LS8\n"
      "LT9:\n"
      "s_nop 15\n"
      "s_nop 3\n"
      "s_branch LS9\n"
      "LT10:\n"
      "s_nop 15\n"
      "s_nop 3\n"
      "s_branch LS10\n"
      "LT11:\n"
      "s_nop 15\n"
      "s_nop 3\n"
      "s_branch LS11\n"
      "LT12:\n"
      "s_nop 15\n"
      "s_nop 3\n"
      "s_branch LS12\n"
      "LT13:\n"
      "s_nop 15\n"
      "s_nop 3\n"
      "s_branch LS13\n"
      "LT14:\n"
      "s_nop 15\n"
      "s_nop 3\n"
      "s_branch LS14\n"
      "LT15:\n"
      "s_nop 15\n"
      "s_nop 3\n"
      "s_branch LS15\n"
      "LT16:\n"
      "s_nop 15\n"
      "s_nop 3\n"
      "s_branch LS16\n"
      "LT17:\n"
      "s_nop 15\n"
      "s_nop 3\n"
      "s_branch LS17\n"
      "LT18:\n"
      "s_nop 15\n"
      "s_nop 3\n"
      "s_branch LS18\n"
      "LT19:\n"
      "s_nop 15\n"
      "s_nop 3\n"
      "s_branch LS19\n"
      "LT20:\n"
      "s_nop 15\n"
      "s_nop 3\n"
      "s_branch LS20\n"
      "LT21:\n"
      "s_nop 15\n"
      "s_nop 3\n"
      "s_branch LS21\n"
      "LT22:\n"
      "s_nop 15\n"
      "s_nop 3\n"
      "s_branch LS22\n"
      "LT23:\n"
      "s_nop 15\n"
      "s_nop 3\n"
      "s_branch LS23\n"
      "LT24:\n"
      "s_nop 15\n"
      "s_nop 3\n"
      "s_branch LS24\n"
      "LT25:\n"
      "s_nop 15\n"
      "s_nop 3\n"
      "s_branch LS25\n"
      "LT26:\n"
      "s_nop 15\n"
      "s_nop 3\n"
      "s_branch LS26\n"
      "LT27:\n"
      "s_nop 15\n"
      "s_nop 3\n"
      "s_branch LS27\n"
      "LT28:\n"
      "s_nop 15\n"
      "s_nop 3\n"
      "s_branch LS28\n"
      "LT29:\n"
      "s_nop 15\n"
      "s_nop 3\n"
      "s_branch LS29\n"
      "LT30:\n"
      "s_nop 15\n"
      "s_nop 3\n"
      "s_branch LS30\n"
      "LT31:\n"
      "s_nop 15\n"
      "s_nop 3\n"
      "s_branch LS31\n"
      "LT32:\n"
      "s_nop 15\n"
      "s_nop 3\n"
      "s_branch LS32\n"
      "LT33:\n"
      "s_nop 15\n"
      "s_nop 3\n"
      "s_branch LS33\n"
      "LT34:\n"
      "s_nop 15\n"
      "s_nop 3\n"
      "s_branch LS34\n"
      "LT35:\n"
      "s_nop 15\n"
      "s_nop 3\n"
      "s_branch LS35\n"
      "LT36:\n"
      "s_nop 15\n"
      "s_nop 3\n"
      "s_branch LS36\n"
      "LT37:\n"
      "s_nop 15\n"
      "s_nop 3\n"
      "s_branch LS37\n"
      "LT38:\n"
      "s_nop 15\n"
      "s_nop 3\n"
      "s_branch LS38\n"
      "LT39:\n"
      "s_nop 15\n"
      "s_nop 3\n"
      "s_branch LS39\n"
      "LT40:\n"
      "s_nop 15\n"
      "s_nop 3\n"
      "s_branch LS40\n"
      "LT41:\n"
      "s_nop 15\n"
      "s_nop 3\n"
      "s_branch LS41\n"
      "LCEND:\n"

      :
      : "{v232}"(cB0), "{v233}"(cB0 ^ 64), "{v234}"(cB0 ^ 128), "{v235}"(cB0 ^ 192), "{v238}"(pvo),
        "{v239}"(lo16), "{v240}"(wro[0]), "{v241}"(wro[1]), "{v242}"(wro[2]), "{v243}"(tra), "{v248}"(fla), "{v249}"(c256),
        "{v250}"(pk), "{v105}"(two), "{v106}"(tcode), "{v[252:253]}"(ep),
        "{s[20:21]}"(cf), "{s[22:23]}"(pc), "{s[24:25]}"(wb), "{s[30:31]}"(out), "{s[28:29]}"(ws), "{s18}"(mP), "{s17}"(mX),
        "{s32}"(xo[0]), "{s33}"(xo[1]), "{s34}"(xo[2]), "{s35}"(xo[3]), "{s36}"(xo[4]), "{s37}"(xo[5]), "{s38}"(xo[6]),
        "{s39}"(xo[7]), "{s40}"(xo[8]), "{s[42:43]}"(cf + 294912), "{s[44:45]}"(out + 294912), "{s[46:47]}"(tab), "{s50}"(var), "{s51}"(b),
        "{s[64:65]}"(w2w), "{s74}"(mW), "{s76}"(wid), "{s77}"(jfl), "{v251}"(sgn),
        "{s89}"(dj[0]), "{s90}"(dj[1]), "{s91}"(dj[2]), "{s92}"(dj[3]), "{s93}"(dj[4]), "{s94}"(dj[5]), "{s95}"(dj[6]),
        "{s96}"(dj[7]), "{s97}"(dj[8]), "{s98}"(d0), "{s99}"(wid ? -1 : 1), "{s100}"(wid < 3 ? 1 : -1), "{s101}"(wid == 2 ? -1 : 1),
        "{v102}"(jt), "{v103}"(jrw), "{v104}"(jrr), "{v[110:111]}"(cd), "{v[112:113]}"(cr), "{v107}"(sg1), "{v108}"(scr), "{s[72:73]}"(jo),
        "{v[4:7]}"(ja0), "{v[8:11]}"(ja1), "{v[12:15]}"(ja2), "{v[16:19]}"(ja3), "{v[20:23]}"(ja4), "{v[24:27]}"(ja5),
        "{s83}"(t_start + (0) + var * ((0) - (0))), "{s86}"(158 + var * ((158) - (158)))
      : "memory", "scc", "vcc", "v0", "v1", "v2", "v3", "v28", "v29", "v30", "v31", "v32", "v33", "v34", "v35", "v36", "v37", "v38", "v39", "v40", "v41", "v42", "v43", "v44", "v45", "v46", "v47", "v48", "v49", "v50", "v51", "v52", "v53", "v54", "v55", "v56", "v57", "v58", "v59", "v60", "v61", "v62", "v63", "v64", "v65", "v66", "v67", "v68", "v69", "v70", "v71", "v72", "v73", "v74", "v75", "v76", "v77", "v78", "v79", "v80", "v81", "v82", "v83", "v84", "v85", "v86", "v87", "v88", "v89", "v90", "v91", "v92", "v93", "v94", "v95", "v96", "v97", "v98", "v99", "v100", "v101", "v109", "v114", "v115", "v116", "v117", "v118", "v119", "v120", "v121", "v122", "v123", "v124", "v125", "v126", "v127", "v128", "v129", "v130", "v131", "v132", "v133", "v134", "v135", "v136", "v137", "v138", "v139", "v140", "v141", "v142", "v143", "v144", "v145", "v146", "v147", "v148", "v149", "v150", "v151", "v152", "v153", "v154", "v155", "v156", "v157", "v158", "v159", "v160", "v161", "v162", "v163", "v164", "v165", "v166", "v167", "v168", "v169", "v170", "v171", "v172", "v173", "v174", "v175", "v176", "v177", "v178", "v179", "v180", "v181", "v182", "v183", "v184", "v185", "v186", "v187", "v188", "v189", "v190", "v191", "v192", "v193", "v194", "v195", "v196", "v197", "v198", "v199", "v200", "v201", "v202", "v203", "v204", "v205", "v206", "v207", "v208", "v209", "v210", "v211", "v212", "v213", "v214", "v215", "v216", "v217", "v218", "v219", "v220", "v221", "v222", "v223", "v224", "v225", "v226", "v227", "v228", "v229", "v230", "v231", "v236", "v237", "v244", "v245", "v246", "v247", "v254", "v255", "a0", "a1", "a2", "a3", "a4", "a5", "a6", "a7", "a8", "a9", "a10", "a11", "a12", "a13", "a14", "a15", "a16", "a17", "a18", "a19", "a20", "a21", "a22", "a23", "a24", "a25", "a26", "a27", "a28", "a29", "a30", "a31", "a32", "a33", "a34", "a35", "a36", "a37", "a38", "a39", "a40", "a41", "a42", "a43", "a44", "a45", "a46", "a47", "a48", "a49", "a50", "a51", "a52", "a53", "a54", "a55", "a56", "a57", "a58", "a59", "a60", "a61", "a62", "a63", "a64", "a65", "a66", "a67", "a68", "a69", "a70", "a71", "a72", "a73", "a74", "a75", "a76", "a77", "a78", "a79", "a80", "a81", "a82", "a83", "a84", "a85", "a86", "a87", "a88", "a89", "a90", "a91", "a92", "a93", "a94", "a95", "a96", "a97", "a98", "a99", "a100", "a101", "a102", "a103", "a104", "a105", "a106", "a107", "a108", "a109", "a110", "a111", "a112", "a113", "a114", "a115", "a116", "a117", "a118", "a119", "a120", "a121", "a122", "a123", "a124", "a125", "a126", "a127", "a128", "a129", "a130", "a131", "a132", "a133", "a134", "a135", "a136", "a137", "a138", "a139", "a140", "a141", "a142", "a143", "a144", "a145", "a146", "a147", "a148", "a149", "a150", "a151", "a152", "a153", "a154", "a155", "a156", "a157", "a158", "a159", "a160", "a161", "a162", "a163", "a164", "a165", "a166", "a167", "a168", "a169", "a170", "a171", "a172", "a173", "a174", "a175", "a176", "a177", "a178", "a179", "a180", "a181", "a182", "a183", "a184", "a185", "a186", "a187", "a188", "a189", "a190", "a191", "a192", "a193", "a194", "a195", "a196", "a197", "a198", "a199", "a200", "a201", "a202", "a203", "a204", "a205", "a206", "a207", "a208", "a209", "a210", "a211", "a212", "a213", "a214", "a215", "a216", "a217", "a218", "a219", "a220", "a221", "a222", "a223", "a224", "a225", "a226", "a227", "a228", "a229", "a230", "a231", "a232", "a233", "a234", "a235", "a236", "a237", "a238", "a239", "a240", "a241", "a242", "a243", "a244", "a245", "a246", "a247", "a248", "a249", "a250", "a251", "a252", "a253", "a254", "a255", "s48", "s49", "s66", "s67", "s68", "s69", "s70", "s71", "s52", "s53", "s54", "s55", "s56", "s57", "s58", "s59", "s60", "s61", "s62", "s63", "s78", "s79", "s80", "s81", "s82", "s84", "s85", "s87", "s19");
}

extern "C" void kernel_launch(void* const* d_in, const int* in_sizes, int n_in,
                              void* d_out, int out_size, void* d_ws, size_t ws_size,
                              hipStream_t stream) {
  (void)hipFuncSetAttribute((const void*)fpn_kernel, hipFuncAttributeMaxDynamicSharedMemorySize, 159808);
  fpn_kernel<<<256, 256, 159808, stream>>>((const char*)d_in[0], (const char*)d_in[1], (const char*)d_in[2],
                                                (const char*)d_in[3], (char*)d_ws, (char*)d_out);
}
```

```cpp
#include <hip/hip_runtime.h>
#include <hip/hip_bf16.h>

typedef __attribute__((ext_vector_type(4))) unsigned u32x4;
typedef __attribute__((ext_vector_type(2))) unsigned u32x2;
#define NSTAMPS 0

__device__ __forceinline__ unsigned col0(unsigned i) { return i + (i >= 5 ? 6 : 0) + (i >= 10 ? 5 : 0); }

static __device__ const unsigned __attribute__((aligned(128))) lane_tab[2048] = {
  98304, 98368, 98304, 0, 99088, 99152, 98320, 16, 99872, 99936, 98336, 32, 100656, 100720, 98352, 48,
  101440, 101376, 98368, 64, 102224, 102160, 98384, 80, 103008, 102944, 98400, 96, 103792, 103728, 98416, 112,
  104448, 104512, 98432, 128, 105232, 105296, 98448, 144, 106016, 106080, 98464, 160, 106800, 106864, 98480, 176,
  107584, 107520, 98496, 192, 108368, 108304, 98512, 208, 109152, 109088, 98528, 224, 109936, 109872, 98544, 240,
  98320, 98384, 98560, 256, 99072, 99136, 98576, 272, 99888, 99952, 98592, 288, 100640, 100704, 98608, 304,
  101456, 101392, 98624, 320, 102208, 102144, 98640, 336, 103024, 102960, 98656, 352, 103776, 103712, 98672, 368,
  104464, 104528, 98688, 384, 105216, 105280, 98704, 400, 106032, 106096, 98720, 416, 106784, 106848, 98736, 432,
  107600, 107536, 98752, 448, 108352, 108288, 98768, 464, 109168, 109104, 98784, 480, 109920, 109856, 98800, 496,
  98336, 98400, 98816, 512, 99120, 99184, 98832, 528, 99840, 99904, 98848, 544, 100624, 100688, 98864, 560,
  101472, 101408, 98880, 576, 102256, 102192, 98896, 592, 102976, 102912, 98912, 608, 103760, 103696, 98928, 624,
  104480, 104544, 98944, 640, 105264, 105328, 98960, 656, 105984, 106048, 98976, 672, 106768, 106832, 98992, 688,
  107616, 107552, 99008, 704, 108400, 108336, 99024, 720, 109120, 109056, 99040, 736, 109904, 109840, 99056, 752,
  98352, 98416, 99072, 1552, 99104, 99168, 99088, 1536, 99856, 99920, 99104, 1584, 100608, 100672, 99120, 1568,
  101488, 101424, 99136, 1616, 102240, 102176, 99152, 1600, 102992, 102928, 99168, 1648, 103744, 103680, 99184, 1632,
  104496, 104560, 99200, 1680, 105248, 105312, 99216, 1664, 106000, 106064, 99232, 1712, 106752, 106816, 99248, 1696,
  107632, 107568, 99264, 1744, 108384, 108320, 99280, 1728, 109136, 109072, 99296, 1776, 109888, 109824, 99312, 1760,
  98496, 98560, 101376, 6208, 99280, 99344, 101392, 6224, 100064, 100128, 101408, 6240, 100848, 100912, 101424, 6256,
  101504, 101696, 101440, 6144, 102288, 102480, 101456, 6160, 103072, 103264, 101472, 6176, 103856, 104048, 101488, 6192,
  104640, 104704, 101504, 6336, 105424, 105488, 101520, 6352, 106208, 106272, 101536, 6368, 106992, 107056, 101552, 6384,
  107648, 107840, 101568, 6272, 108432, 108624, 101584, 6288, 109216, 109408, 101600, 6304, 110000, 110192, 101616, 6320,
  98512, 98576, 101632, 6464, 99264, 99328, 101648, 6480, 100080, 100144, 101664, 6496, 100832, 100896, 101680, 6512,
  101520, 101712, 101696, 6400, 102272, 102464, 101712, 6416, 103088, 103280, 101728, 6432, 103840, 104032, 101744, 6448,
  104656, 104720, 101760, 6592, 105408, 105472, 101776, 6608, 106224, 106288, 101792, 6624, 106976, 107040, 101808, 6640,
  107664, 107856, 101824, 6528, 108416, 108608, 101840, 6544, 109232, 109424, 101856, 6560, 109984, 110176, 101872, 6576,
  98528, 98592, 101888, 6720, 99312, 99376, 101904, 6736, 100032, 100096, 101920, 6752, 100816, 100880, 101936, 6768,
  101536, 101728, 101952, 6656, 102320, 102512, 101968, 6672, 103040, 103232, 101984, 6688, 103824, 104016, 102000, 6704,
  104672, 104736, 102016, 6848, 105456, 105520, 102032, 6864, 106176, 106240, 102048, 6880, 106960, 107024, 102064, 6896,
  107680, 107872, 102080, 6784, 108464, 108656, 102096, 6800, 109184, 109376, 102112, 6816, 109968, 110160, 102128, 6832,
  98544, 98608, 102144, 16976, 99296, 99360, 102160, 16960, 100048, 100112, 102176, 17008, 100800, 100864, 102192, 16992,
  101552, 101744, 102208, 16912, 102304, 102496, 102224, 16896, 103056, 103248, 102240, 16944, 103808, 104000, 102256, 16928,
  104688, 104752, 102272, 17104, 105440, 105504, 102288, 17088, 106192, 106256, 102304, 17136, 106944, 107008, 102320, 17120,
  107696, 107888, 102336, 17040, 108448, 108640, 102352, 17024, 109200, 109392, 102368, 17072, 109952, 110144, 102384, 17056,
  1808, 3616, 0, 21632, 1792, 3632, 16, 21648, 1840, 3584, 32, 21664, 1824, 3600, 48, 21680,
  1872, 3680, 64, 21696, 1856, 3696, 80, 21712, 1904, 3648, 96, 21728, 1888, 3664, 112, 21744,
  1936, 3744, 128, 21504, 1920, 3760, 144, 21520, 1968, 3712, 160, 21536, 1952, 3728, 176, 21552,
  2000, 3808, 192, 21568, 1984, 3824, 208, 21584, 2032, 3776, 224, 21600, 2016, 3792, 240, 21616,
  2064, 4656, 768, 22400, 2048, 4640, 784, 22416, 2096, 4624, 800, 22432, 2080, 4608, 816, 22448,
  2128, 4720, 832, 22464, 2112, 4704, 848, 22480, 2160, 4688, 864, 22496, 2144, 4672, 880, 22512,
  2192, 4784, 896, 22272, 2176, 4768, 912, 22288, 2224, 4752, 928, 22304, 2208, 4736, 944, 22320,
  2256, 4848, 960, 22336, 2240, 4832, 976, 22352, 2288, 4816, 992, 22368, 2272, 4800, 1008, 22384,
  3104, 4912, 1552, 23184, 3120, 4896, 1536, 23168, 3072, 4880, 1584, 23216, 3088, 4864, 1568, 23200,
  3168, 4976, 1616, 23248, 3184, 4960, 1600, 23232, 3136, 4944, 1648, 23280, 3152, 4928, 1632, 23264,
  3232, 5040, 1680, 23056, 3248, 5024, 1664, 23040, 3200, 5008, 1712, 23088, 3216, 4992, 1696, 23072,
  3296, 5104, 1744, 23120, 3312, 5088, 1728, 23104, 3264, 5072, 1776, 23152, 3280, 5056, 1760, 23136,
  3360, 5168, 2320, 23952, 3376, 5152, 2304, 23936, 3328, 5136, 2352, 23984, 3344, 5120, 2336, 23968,
  3424, 5232, 2384, 24016, 3440, 5216, 2368, 24000, 3392, 5200, 2416, 24048, 3408, 5184, 2400, 24032,
  3488, 5296, 2448, 23824, 3504, 5280, 2432, 23808, 3456, 5264, 2480, 23856, 3472, 5248, 2464, 23840,
  3552, 5360, 2512, 23888, 3568, 5344, 2496, 23872, 3520, 5328, 2544, 23920, 3536, 5312, 2528, 23904,
  17232, 19040, 3104, 32416, 17216, 19056, 3120, 32432, 17264, 19008, 3072, 32384, 17248, 19024, 3088, 32400,
  17168, 18976, 3168, 32480, 17152, 18992, 3184, 32496, 17200, 18944, 3136, 32448, 17184, 18960, 3152, 32464,
  17360, 19168, 3232, 32288, 17344, 19184, 3248, 32304, 17392, 19136, 3200, 32256, 17376, 19152, 3216, 32272,
  17296, 19104, 3296, 32352, 17280, 19120, 3312, 32368, 17328, 19072, 3264, 32320, 17312, 19088, 3280, 32336,
  17488, 20080, 3872, 33184, 17472, 20064, 3888, 33200, 17520, 20048, 3840, 33152, 17504, 20032, 3856, 33168,
  17424, 20016, 3936, 33248, 17408, 20000, 3952, 33264, 17456, 19984, 3904, 33216, 17440, 19968, 3920, 33232,
  17616, 20208, 4000, 33056, 17600, 20192, 4016, 33072, 17648, 20176, 3968, 33024, 17632, 20160, 3984, 33040,
  17552, 20144, 4064, 33120, 17536, 20128, 4080, 33136, 17584, 20112, 4032, 33088, 17568, 20096, 4048, 33104,
  18528, 20336, 4656, 33968, 18544, 20320, 4640, 33952, 18496, 20304, 4624, 33936, 18512, 20288, 4608, 33920,
  18464, 20272, 4720, 34032, 18480, 20256, 4704, 34016, 18432, 20240, 4688, 34000, 18448, 20224, 4672, 33984,
  18656, 20464, 4784, 33840, 18672, 20448, 4768, 33824, 18624, 20432, 4752, 33808, 18640, 20416, 4736, 33792,
  18592, 20400, 4848, 33904, 18608, 20384, 4832, 33888, 18560, 20368, 4816, 33872, 18576, 20352, 4800, 33856,
  18784, 20592, 5424, 34736, 18800, 20576, 5408, 34720, 18752, 20560, 5392, 34704, 18768, 20544, 5376, 34688,
  18720, 20528, 5488, 34800, 18736, 20512, 5472, 34784, 18688, 20496, 5456, 34768, 18704, 20480, 5440, 34752,
  18912, 20720, 5552, 34608, 18928, 20704, 5536, 34592, 18880, 20688, 5520, 34576, 18896, 20672, 5504, 34560,
  18848, 20656, 5616, 34672, 18864, 20640, 5600, 34656, 18816, 20624, 5584, 34640, 18832, 20608, 5568, 34624,
  98688, 98752, 104448, 21504, 99472, 99536, 104464, 21520, 100256, 100320, 104480, 21536, 101040, 101104, 104496, 21552,
  101824, 101760, 104512, 21568, 102608, 102544, 104528, 21584, 103392, 103328, 104544, 21600, 104176, 104112, 104560, 21616,
  104832, 104896, 104576, 21632, 105616, 105680, 104592, 21648, 106400, 106464, 104608, 21664, 107184, 107248, 104624, 21680,
  107968, 107904, 104640, 21696, 108752, 108688, 104656, 21712, 109536, 109472, 104672, 21728, 110320, 110256, 104688, 21744,
  98704, 98768, 104704, 21760, 99456, 99520, 104720, 21776, 100272, 100336, 104736, 21792, 101024, 101088, 104752, 21808,
  101840, 101776, 104768, 21824, 102592, 102528, 104784, 21840, 103408, 103344, 104800, 21856, 104160, 104096, 104816, 21872,
  104848, 104912, 104832, 21888, 105600, 105664, 104848, 21904, 106416, 106480, 104864, 21920, 107168, 107232, 104880, 21936,
  107984, 107920, 104896, 21952, 108736, 108672, 104912, 21968, 109552, 109488, 104928, 21984, 110304, 110240, 104944, 22000,
  98720, 98784, 104960, 22016, 99504, 99568, 104976, 22032, 100224, 100288, 104992, 22048, 101008, 101072, 105008, 22064,
  101856, 101792, 105024, 22080, 102640, 102576, 105040, 22096, 103360, 103296, 105056, 22112, 104144, 104080, 105072, 22128,
  104864, 104928, 105088, 22144, 105648, 105712, 105104, 22160, 106368, 106432, 105120, 22176, 107152, 107216, 105136, 22192,
  108000, 107936, 105152, 22208, 108784, 108720, 105168, 22224, 109504, 109440, 105184, 22240, 110288, 110224, 105200, 22256,
  98736, 98800, 105216, 23056, 99488, 99552, 105232, 23040, 100240, 100304, 105248, 23088, 100992, 101056, 105264, 23072,
  101872, 101808, 105280, 23120, 102624, 102560, 105296, 23104, 103376, 103312, 105312, 23152, 104128, 104064, 105328, 23136,
  104880, 104944, 105344, 23184, 105632, 105696, 105360, 23168, 106384, 106448, 105376, 23216, 107136, 107200, 105392, 23200,
  108016, 107952, 105408, 23248, 108768, 108704, 105424, 23232, 109520, 109456, 105440, 23280, 110272, 110208, 105456, 23264,
  98880, 98944, 107520, 35392, 99664, 99728, 107536, 35408, 100448, 100512, 107552, 35424, 101232, 101296, 107568, 35440,
  101888, 102080, 107584, 35328, 102672, 102864, 107600, 35344, 103456, 103648, 107616, 35360, 104240, 104432, 107632, 35376,
  105024, 105088, 107648, 35520, 105808, 105872, 107664, 35536, 106592, 106656, 107680, 35552, 107376, 107440, 107696, 35568,
  108032, 108224, 107712, 35456, 108816, 109008, 107728, 35472, 109600, 109792, 107744, 35488, 110384, 110576, 107760, 35504,
  98896, 98960, 107776, 35648, 99648, 99712, 107792, 35664, 100464, 100528, 107808, 35680, 101216, 101280, 107824, 35696,
  101904, 102096, 107840, 35584, 102656, 102848, 107856, 35600, 103472, 103664, 107872, 35616, 104224, 104416, 107888, 35632,
  105040, 105104, 107904, 35776, 105792, 105856, 107920, 35792, 106608, 106672, 107936, 35808, 107360, 107424, 107952, 35824,
  108048, 108240, 107968, 35712, 108800, 108992, 107984, 35728, 109616, 109808, 108000, 35744, 110368, 110560, 108016, 35760,
  98912, 98976, 108032, 35904, 99696, 99760, 108048, 35920, 100416, 100480, 108064, 35936, 101200, 101264, 108080, 35952,
  101920, 102112, 108096, 35840, 102704, 102896, 108112, 35856, 103424, 103616, 108128, 35872, 104208, 104400, 108144, 35888,
  105056, 105120, 108160, 36032, 105840, 105904, 108176, 36048, 106560, 106624, 108192, 36064, 107344, 107408, 108208, 36080,
  108064, 108256, 108224, 35968, 108848, 109040, 108240, 35984, 109568, 109760, 108256, 36000, 110352, 110544, 108272, 36016,
  98928, 98992, 108288, 36944, 99680, 99744, 108304, 36928, 100432, 100496, 108320, 36976, 101184, 101248, 108336, 36960,
  101936, 102128, 108352, 36880, 102688, 102880, 108368, 36864, 103440, 103632, 108384, 36912, 104192, 104384, 108400, 36896,
  105072, 105136, 108416, 37072, 105824, 105888, 108432, 37056, 106576, 106640, 108448, 37104, 107328, 107392, 108464, 37088,
  108080, 108272, 108480, 37008, 108832, 109024, 108496, 36992, 109584, 109776, 108512, 37040, 110336, 110528, 108528, 37024,
  23312, 32800, 6208, 35520, 23296, 32816, 6224, 35536, 23344, 32768, 6240, 35552, 23328, 32784, 6256, 35568,
  23376, 32864, 6144, 35456, 23360, 32880, 6160, 35472, 23408, 32832, 6176, 35488, 23392, 32848, 6192, 35504,
  23440, 32928, 6336, 35392, 23424, 32944, 6352, 35408, 23472, 32896, 6368, 35424, 23456, 32912, 6384, 35440,
  23504, 32992, 6272, 35328, 23488, 33008, 6288, 35344, 23536, 32960, 6304, 35360, 23520, 32976, 6320, 35376,
  23568, 33840, 6976, 36288, 23552, 33824, 6992, 36304, 23600, 33808, 7008, 36320, 23584, 33792, 7024, 36336,
  23632, 33904, 6912, 36224, 23616, 33888, 6928, 36240, 23664, 33872, 6944, 36256, 23648, 33856, 6960, 36272,
  23696, 33968, 7104, 36160, 23680, 33952, 7120, 36176, 23728, 33936, 7136, 36192, 23712, 33920, 7152, 36208,
  23760, 34032, 7040, 36096, 23744, 34016, 7056, 36112, 23792, 34000, 7072, 36128, 23776, 33984, 7088, 36144,
  32288, 34096, 16976, 37072, 32304, 34080, 16960, 37056, 32256, 34064, 17008, 37104, 32272, 34048, 16992, 37088,
  32352, 34160, 16912, 37008, 32368, 34144, 16896, 36992, 32320, 34128, 16944, 37040, 32336, 34112, 16928, 37024,
  32416, 34224, 17104, 36944, 32432, 34208, 17088, 36928, 32384, 34192, 17136, 36976, 32400, 34176, 17120, 36960,
  32480, 34288, 17040, 36880, 32496, 34272, 17024, 36864, 32448, 34256, 17072, 36912, 32464, 34240, 17056, 36896,
  32544, 34352, 17744, 37840, 32560, 34336, 17728, 37824, 32512, 34320, 17776, 37872, 32528, 34304, 17760, 37856,
  32608, 34416, 17680, 37776, 32624, 34400, 17664, 37760, 32576, 34384, 17712, 37808, 32592, 34368, 17696, 37792,
  32672, 34480, 17872, 37712, 32688, 34464, 17856, 37696, 32640, 34448, 17904, 37744, 32656, 34432, 17888, 37728,
  32736, 34544, 17808, 37648, 32752, 34528, 17792, 37632, 32704, 34512, 17840, 37680, 32720, 34496, 17824, 37664,
  37200, 39008, 18528, 38624, 37184, 39024, 18544, 38640, 37232, 38976, 18496, 38592, 37216, 38992, 18512, 38608,
  37136, 38944, 18464, 38560, 37120, 38960, 18480, 38576, 37168, 38912, 18432, 38528, 37152, 38928, 18448, 38544,
  37328, 39136, 18656, 38496, 37312, 39152, 18672, 38512, 37360, 39104, 18624, 38464, 37344, 39120, 18640, 38480,
  37264, 39072, 18592, 38432, 37248, 39088, 18608, 38448, 37296, 39040, 18560, 38400, 37280, 39056, 18576, 38416,
  37456, 40048, 19296, 39392, 37440, 40032, 19312, 39408, 37488, 40016, 19264, 39360, 37472, 40000, 19280, 39376,
  37392, 39984, 19232, 39328, 37376, 39968, 19248, 39344, 37424, 39952, 19200, 39296, 37408, 39936, 19216, 39312,
  37584, 40176, 19424, 39264, 37568, 40160, 19440, 39280, 37616, 40144, 19392, 39232, 37600, 40128, 19408, 39248,
  37520, 40112, 19360, 39200, 37504, 40096, 19376, 39216, 37552, 40080, 19328, 39168, 37536, 40064, 19344, 39184,
  38496, 40304, 20080, 40176, 38512, 40288, 20064, 40160, 38464, 40272, 20048, 40144, 38480, 40256, 20032, 40128,
  38432, 40240, 20016, 40112, 38448, 40224, 20000, 40096, 38400, 40208, 19984, 40080, 38416, 40192, 19968, 40064,
  38624, 40432, 20208, 40048, 38640, 40416, 20192, 40032, 38592, 40400, 20176, 40016, 38608, 40384, 20160, 40000,
  38560, 40368, 20144, 39984, 38576, 40352, 20128, 39968, 38528, 40336, 20112, 39952, 38544, 40320, 20096, 39936,
  38752, 40560, 20848, 40944, 38768, 40544, 20832, 40928, 38720, 40528, 20816, 40912, 38736, 40512, 20800, 40896,
  38688, 40496, 20784, 40880, 38704, 40480, 20768, 40864, 38656, 40464, 20752, 40848, 38672, 40448, 20736, 40832,
  38880, 40688, 20976, 40816, 38896, 40672, 20960, 40800, 38848, 40656, 20944, 40784, 38864, 40640, 20928, 40768,
  38816, 40624, 20912, 40752, 38832, 40608, 20896, 40736, 38784, 40592, 20880, 40720, 38800, 40576, 20864, 40704};

__global__ __attribute__((aligned(4096))) __launch_bounds__(256, 1) void fpn_kernel(const char* __restrict__ cf, const char* __restrict__ pc,
                                                     const char* __restrict__ w1, const char* __restrict__ w2,
                                                     char* __restrict__ ws, char* __restrict__ out) {
  unsigned lane = threadIdx.x & 63;
  unsigned wid = __builtin_amdgcn_readfirstlane(threadIdx.x >> 6);
  unsigned b = blockIdx.x;
  unsigned long long t64;
  asm volatile("s_memrealtime %0" : "={s[88:89]}"(t64), "+s"(b), "+s"(wid));
  const unsigned t_start = (unsigned)t64;
  const unsigned pr = (b >> 3) * 4 + (b & 3);
  unsigned xo[9], pg[9];
#pragma unroll
  for (int j = 0; j < 9; ++j) {
    xo[j] = ((3 * pr) >> 1) * 589824u + (pr & 1) * (147456u + (j >= 3 && j < 6 ? 294912u : 0)) + j * 49152u + (j >= 6 ? 294912u : 0);
    pg[j] = xo[j] >> 21;
  }
  int dj[9];
#pragma unroll
  for (int j = 1; j < 9; ++j) dj[j] = (int)(pg[j] - pg[j - 1]) * (j <= 4 ? -12288 : 12288);
  const int d0 = (int)(pg[4] - pg[0]) * 12288;
  dj[0] = d0 - (int)(pg[8] - pg[4]) * 12288;
  const unsigned scr = 159744 + wid * 4;
  const int sgn = (4 * wid + (lane >> 4)) < 10 ? 1 : -1;
  const unsigned mP = 73728 + wid * 1024;
  unsigned pvo = __umul24(col0(4 * wid + (lane >> 4)), 768) + ((((lane & 15) ^ (lane >> 4) ^ (4 * wid)) & 15) << 4)
                 + pr * (9 * 24576);
  int sg1;
  u32x2 cd, cr, po;
  po[0] = xo[4] + lane * 1024 + (b & 4) * 128;
  po[1] = po[0] + 8192;
  const unsigned mJ = 110592 + wid * 6144;
  const unsigned var = (b + 112) >> 8;
  u32x4 ja0, ja1, ja2, ja3, ja4, ja5;
  u32x2 ep;
  unsigned two = min((((b >> 3) - 18) * 4 + wid) * 42 + min(lane, 41u), 2303u) * 128;
  unsigned tcode = ((((b >> 3) - 18) * 4 + wid) * 11 + min(lane, 10u)) * 128;
  const unsigned jb = b < 144 ? b : 0;
  const unsigned G32 = (jb * 171) >> 11, jcb = jb - 12 * G32;
  {
    const unsigned jw = __umul24(lane >> 2, 768) + (lane & 3) * 16 + 64 * G32 + wid * (96 * 768);
    const unsigned ja = __umul24(8 * ((lane & 15) >> 2) + (lane & 3), 768) + 16 * (lane >> 4) + jcb * (32 * 768) + wid * 192;
    const unsigned cla = 1048576 + pr * 128;
    asm volatile(
      "s_nop 4\n"
      "s_cmp_lg_u32 s76, 0\n"
      "s_cbranch_scc1 LQ0\n"
      "s_mul_i32 s62, s88, 0x1080\n"
      "s_and_b32 s62, s62, 0x4180\n"
      "s_add_u32 s56, s30, s62\n"
      "s_addc_u32 s57, s31, 0\n"
      "s_cmp_lg_u32 s76, 0\n"
      "s_mov_b64 exec, 0x1\n"
      "global_load_dword v116, v118, s[56:57] nt\n"
      "global_load_dword v117, v119, s[56:57] nt\n"
      "s_mov_b64 exec, -1\n"
      "LQ0:\n"
      "v_mov_b32 v0, 0\n"
      "s_barrier\n"
      "global_load_dwordx2 v[252:253], v0, s[28:29] sc1\n"
      "s_cbranch_scc1 LQ1\n"
      ".p2align 5\n"
      "s_waitcnt vmcnt(2)\n"
      "s_memtime s[60:61]\n"
      "s_waitcnt vmcnt(1)\n"
      "s_memtime s[62:63]\n"
      "s_waitcnt vmcnt(0) lgkmcnt(0)\n"
      "s_sub_u32 s62, s62, s60\n"
      "s_bfe_u32 s63, s51, 0x10002\n"
      "s_cmp_lt_u32 s62, 30\n"
      "s_cselect_b32 s62, 1, 0\n"
      "v_mov_b32 v116, s62\n"
      "ds_write_b32 v108, v116 offset:16\n"
      "s_xor_b32 s62, s62, s63\n"
      "v_add_co_u32 v110, vcc, 0x7f4a7c15, v252\n"
      "v_mov_b32 v111, 0x9e3779b9\n"
      "v_addc_co_u32 v111, vcc, v111, v253, vcc\n"
      "v_and_or_b32 v110, v110, -2, s62\n"
      "s_mov_b64 exec, 1\n"
      "global_atomic_swap_x2 v[112:113], v114, v[110:111], s[28:29] sc0 sc1\n"
      "s_mov_b64 exec, -1\n"
      "LQ1:\n"
      "s_cmp_lg_u32 s50, 0\n"
      "s_cbranch_scc0 LU1\n"
      "s_getpc_b64 s[56:57]\n"
      "LC0_1:\n"
      "v_min_u32 v106, LCEND-LC0_1-4, v106\n"
      "global_load_dword v106, v106, s[56:57]\n"
      "global_load_dword v105, v105, s[26:27]\n"
      "v_and_b32 v117, -16, v108\n"
      "s_waitcnt lgkmcnt(0)\n"
      "s_barrier\n"
      "ds_read_b32 v116, v117 offset:16\n"
      "s_waitcnt lgkmcnt(0)\n"
      "v_readfirstlane_b32 s62, v116\n"
      "s_nop 3\n"
      "s_mul_i32 s63, s62, 0x3000\n"
      "s_sub_u32 s82, s98, s63\n"
      "s_lshl_b32 s62, s62, 1\n"
      "s_sub_u32 s62, 1, s62\n"
      "v_mul_i32_i24 v107, s62, v251\n"
      "v_mad_i32_i24 v238, v107, s82, v238\n"
      "s_add_u32 s56, s22, 0x0\n"
      "s_addc_u32 s57, s23, 0\n"
      "s_add_u32 m0, s18, 0x0\n"
      "s_nop 0\n"
      "global_load_lds_dwordx4 v238, s[56:57] offset:0 \n"
      "v_mad_i32_i24 v238, v107, s90, v238\n"
      "s_add_u32 s56, s22, 0x6000\n"
      "s_addc_u32 s57, s23, 0\n"
      "s_add_u32 m0, s18, 0x1000\n"
      "s_nop 0\n"
      "global_load_lds_dwordx4 v238, s[56:57] offset:0 \n"
      "v_mad_i32_i24 v238, v107, s91, v238\n"
      "s_add_u32 s56, s22, 0xc000\n"
      "s_addc_u32 s57, s23, 0\n"
      "s_add_u32 m0, s18, 0x2000\n"
      "s_nop 0\n"
      "global_load_lds_dwordx4 v238, s[56:57] offset:0 \n"
      "v_mad_i32_i24 v238, v107, s92, v238\n"
      "s_add_u32 s56, s22, 0x12000\n"
      "s_addc_u32 s57, s23, 0\n"
      "s_add_u32 m0, s18, 0x3000\n"
      "s_nop 0\n"
      "global_load_lds_dwordx4 v238, s[56:57] offset:0 \n"
      "v_mad_i32_i24 v238, v107, s93, v238\n"
      "s_add_u32 s56, s22, 0x18000\n"
      "s_addc_u32 s57, s23, 0\n"
      "s_add_u32 m0, s18, 0x4000\n"
      "s_nop 0\n"
      "global_load_lds_dwordx4 v238, s[56:57] offset:0 \n"
      "v_mad_i32_i24 v238, v107, s94, v238\n"
      "s_add_u32 s56, s22, 0x1e000\n"
      "s_addc_u32 s57, s23, 0\n"
      "s_add_u32 m0, s18, 0x5000\n"
      "s_nop 0\n"
      "global_load_lds_dwordx4 v238, s[56:57] offset:0 \n"
      "v_mad_i32_i24 v238, v107, s95, v238\n"
      "s_add_u32 s56, s22, 0x24000\n"
      "s_addc_u32 s57, s23, 0\n"
      "s_add_u32 m0, s18, 0x6000\n"
      "s_nop 0\n"
      "global_load_lds_dwordx4 v238, s[56:57] offset:0 \n"
      "v_mad_i32_i24 v238, v107, s96, v238\n"
      "s_add_u32 s56, s22, 0x2a000\n"
      "s_addc_u32 s57, s23, 0\n"
      "s_add_u32 m0, s18, 0x7000\n"
      "s_nop 0\n"
      "global_load_lds_dwordx4 v238, s[56:57] offset:0 \n"
      "v_mad_i32_i24 v238, v107, s97, v238\n"
      "s_add_u32 s56, s22, 0x30000\n"
      "s_addc_u32 s57, s23, 0\n"
      "s_add_u32 m0, s18, 0x8000\n"
      "s_nop 0\n"
      "global_load_lds_dwordx4 v238, s[56:57] offset:0 \n"
      "s_branch LUE\n"
      "LU1:\n"
      "s_add_u32 s56, s26, 0x0\n"
      "s_addc_u32 s57, s27, 0\n"
      "s_add_u32 m0, s75, 0x0\n"
      "s_nop 0\n"
      "global_load_lds_dwordx4 v100, s[56:57]\n"
      "s_add_u32 s56, s26, 0x3000\n"
      "s_addc_u32 s57, s27, 0\n"
      "s_add_u32 m0, s75, 0x400\n"
      "s_nop 0\n"
      "global_load_lds_dwordx4 v100, s[56:57]\n"
      "s_add_u32 s56, s26, 0x6000\n"
      "s_addc_u32 s57, s27, 0\n"
      "s_add_u32 m0, s75, 0x800\n"
      "s_nop 0\n"
      "global_load_lds_dwordx4 v100, s[56:57]\n"
      "s_add_u32 s56, s26, 0x9000\n"
      "s_addc_u32 s57, s27, 0\n"
      "s_add_u32 m0, s75, 0xc00\n"
      "s_nop 0\n"
      "global_load_lds_dwordx4 v100, s[56:57]\n"
      "s_add_u32 s56, s26, 0xc000\n"
      "s_addc_u32 s57, s27, 0\n"
      "s_add_u32 m0, s75, 0x1000\n"
      "s_nop 0\n"
      "global_load_lds_dwordx4 v100, s[56:57]\n"
      "s_add_u32 s56, s26, 0xf000\n"
      "s_addc_u32 s57, s27, 0\n"
      "s_add_u32 m0, s75, 0x1400\n"
      "s_nop 0\n"
      "global_load_lds_dwordx4 v100, s[56:57]\n"
      "global_load_dwordx4 v[4:7], v101, s[24:25] offset:0\n"
      "global_load_dwordx4 v[8:11], v101, s[24:25] offset:3072\n"
      "global_load_dwordx4 v[12:15], v101, s[24:25] offset:64\n"
      "global_load_dwordx4 v[16:19], v101, s[24:25] offset:3136\n"
      "global_load_dwordx4 v[20:23], v101, s[24:25] offset:128\n"
      "global_load_dwordx4 v[24:27], v101, s[24:25] offset:3200\n"
      "LUE:\n"

      : "={v[4:7]}"(ja0), "={v[8:11]}"(ja1), "={v[12:15]}"(ja2), "={v[16:19]}"(ja3), "={v[20:23]}"(ja4), "={v[24:27]}"(ja5),
        "={v[252:253]}"(ep), "+{v105}"(two), "+{v106}"(tcode), "={v[110:111]}"(cd), "={v[112:113]}"(cr), "+{v[118:119]}"(po),
        "+{v238}"(pvo), "={v107}"(sg1)
      : "{v114}"(cla), "{s[88:89]}"(t64), "{v100}"(jw), "{v101}"(ja), "{s[20:21]}"(cf), "{s[24:25]}"(w1), "{s[26:27]}"(w2), "{s[28:29]}"(ws), "{s[30:31]}"(out),
        "{s75}"(mJ), "{s51}"(b), "{s50}"(var), "{s76}"(wid), "{s[22:23]}"(pc), "{s18}"(mP), "{v108}"(scr), "{v251}"(sgn),
        "{s90}"(dj[1]), "{s91}"(dj[2]), "{s92}"(dj[3]), "{s93}"(dj[4]), "{s94}"(dj[5]), "{s95}"(dj[6]), "{s96}"(dj[7]), "{s97}"(dj[8]), "{s98}"(d0)
      : "memory", "scc", "vcc", "v0", "v116", "v117", "s56", "s57", "s60", "s61", "s62", "s63", "s82");
  }
  asm volatile("" : "+v"(lane));
  asm volatile("" : "+s"(wid));
  unsigned wro[3];
#pragma unroll
  for (int i = 0; i < 3; ++i) { const unsigned x = lane + 64 * i, q = __umul24(x, 171) >> 11; wro[i] = __umul24(q, 576) + x * 16; }
  const unsigned mW = wid * 6144;
  const char* w2w = w2 + wid * 192;
  const unsigned lo16 = lane * 16;
  const char* wb = ws + (65536 + wid * 73728);
  const unsigned c = lane & 15, g = lane >> 4;
  const unsigned cB0 = 73728 + c * 256 + ((g ^ c) << 4);
  const unsigned r8 = 8 * g + (c >> 2), c3 = (c & 3) * 16;
  const unsigned tra = mW + __umul24(r8, 192) + c3;
  const unsigned mX = wid * 1024;
  const unsigned fla = 4096 + wid * 288 + min(lo16, 272u);
  const unsigned c256 = c * 256 - 73728;
  const unsigned jfl = (b < 144 ? 4096 : 8192) + 8 * b;
  char* jo = ws + (65536 + (24 * G32 + jcb) * 1024);
  const unsigned jt = mJ + r8 * 64 + c3;
  const unsigned jrr = 135168 + lo16;
  const unsigned jrw = jrr + wid * 4096;
  const unsigned pk = 151552 + mX + lo16;
  const char* tab = (const char*)lane_tab + ((wid >> 1) * 4096 + (wid & 1) * 1024);
  asm volatile(
      "s_nop 4\n"
      "LBODY:\n"
      "s_branch LB0\n"
      "LRET:\n"
      "s_setpc_b64 s[56:57]\n"
      "LB0:\n"
      "s_cmp_lg_u32 s50, 0\n"
      "s_cbranch_scc1 LV1\n"
      "s_getpc_b64 s[56:57]\n"
      "LTR_0:\n"
      "s_add_u32 s56, s56, LTE_0-LTR_0\n"
      "s_addc_u32 s57, s57, 0\n"
      "s_mov_b64 exec, 0\n"
      "s_cmp_eq_u32 s76, 3\n"
      "s_cbranch_scc1 LPD2\n"
      "s_cmp_eq_u32 s76, 2\n"
      "s_cbranch_scc1 LPD1\n"
      "s_cmp_eq_u32 s76, 1\n"
      "s_cbranch_scc1 LPD0\n"
      "LTE_0:\n"
      "s_mov_b64 exec, -1\n"
      "s_waitcnt vmcnt(12)\n"
      "v_readfirstlane_b32 s80, v252\n"
      "v_readfirstlane_b32 s81, v253\n"
      "s_nop 3\n"
      "s_add_u32 s80, s80, 0x7f4a7c15\n"
      "s_addc_u32 s81, s81, 0x9e3779b9\n"
      "v_mov_b32 v252, s80\n"
      "v_mov_b32 v253, s81\n"
      "v_xor_b32 v116, v112, v110\n"
      "v_xor_b32 v117, v113, v111\n"
      "v_and_or_b32 v116, v116, -2, v117\n"
      "v_cmp_eq_u32 vcc, 0, v116\n"
      "v_cndmask_b32 v116, v110, v112, vcc\n"
      "s_mov_b64 exec, 1\n"
      "ds_write_b32 v108, v116\n"
      "s_mov_b64 exec, -1\n"
      "v_and_b32 v108, -16, v108\n"
      "s_waitcnt lgkmcnt(0)\n"
      "s_barrier\n"
      "ds_read_b32 v116, v108\n"
      "s_waitcnt lgkmcnt(0)\n"
      "v_readfirstlane_b32 s62, v116\n"
      "s_lshr_b32 s63, s51, 2\n"
      "s_nop 2\n"
      "s_xor_b32 s62, s62, s63\n"
      "s_and_b32 s62, s62, 1\n"
      "s_mul_i32 s63, s62, 0x3000\n"
      "s_sub_u32 s98, s98, s63\n"
      "s_lshl_b32 s62, s62, 1\n"
      "s_sub_u32 s62, 1, s62\n"
      "v_mul_i32_i24 v251, s62, v251\n"
      "s_lshl_b32 s19, s62, 1\n"
      "s_mul_i32 s100, s100, s62\n"
      "v_mad_i32_i24 v238, v251, s98, v238\n"
      "s_add_u32 s56, s22, 0x0\n"
      "s_addc_u32 s57, s23, 0\n"
      "s_add_u32 m0, s18, 0x0\n"
      "s_nop 0\n"
      "global_load_lds_dwordx4 v238, s[56:57] offset:0 \n"
      "v_mad_i32_i24 v238, v251, s90, v238\n"
      "s_add_u32 s56, s22, 0x6000\n"
      "s_addc_u32 s57, s23, 0\n"
      "s_add_u32 m0, s18, 0x1000\n"
      "s_nop 0\n"
      "global_load_lds_dwordx4 v238, s[56:57] offset:0 \n"
      "v_mad_i32_i24 v238, v251, s91, v238\n"
      "s_add_u32 s56, s22, 0xc000\n"
      "s_addc_u32 s57, s23, 0\n"
      "s_add_u32 m0, s18, 0x2000\n"
      "s_nop 0\n"
      "global_load_lds_dwordx4 v238, s[56:57] offset:0 \n"
      "v_mad_i32_i24 v238, v251, s92, v238\n"
      "s_add_u32 s56, s22, 0x12000\n"
      "s_addc_u32 s57, s23, 0\n"
      "s_add_u32 m0, s18, 0x3000\n"
      "s_nop 0\n"
      "global_load_lds_dwordx4 v238, s[56:57] offset:0 \n"
      "v_mad_i32_i24 v238, v251, s93, v238\n"
      "s_add_u32 s56, s22, 0x18000\n"
      "s_addc_u32 s57, s23, 0\n"
      "s_add_u32 m0, s18, 0x4000\n"
      "s_nop 0\n"
      "global_load_lds_dwordx4 v238, s[56:57] offset:0 \n"
      "v_mad_i32_i24 v238, v251, s94, v238\n"
      "s_add_u32 s56, s22, 0x1e000\n"
      "s_addc_u32 s57, s23, 0\n"
      "s_add_u32 m0, s18, 0x5000\n"
      "s_nop 0\n"
      "global_load_lds_dwordx4 v238, s[56:57] offset:0 \n"
      "v_mad_i32_i24 v238, v251, s95, v238\n"
      "s_add_u32 s56, s22, 0x24000\n"
      "s_addc_u32 s57, s23, 0\n"
      "s_add_u32 m0, s18, 0x6000\n"
      "s_nop 0\n"
      "global_load_lds_dwordx4 v238, s[56:57] offset:0 \n"
      "v_mad_i32_i24 v238, v251, s96, v238\n"
      "s_add_u32 s56, s22, 0x2a000\n"
      "s_addc_u32 s57, s23, 0\n"
      "s_add_u32 m0, s18, 0x7000\n"
      "s_nop 0\n"
      "global_load_lds_dwordx4 v238, s[56:57] offset:0 \n"
      "v_mad_i32_i24 v238, v251, s97, v238\n"
      "s_add_u32 s56, s22, 0x30000\n"
      "s_addc_u32 s57, s23, 0\n"
      "s_add_u32 m0, s18, 0x8000\n"
      "s_nop 0\n"
      "global_load_lds_dwordx4 v238, s[56:57] offset:0 \n"
      "s_waitcnt vmcnt(9)\n"
      "ds_read_b64_tr_b16 v[28:29], v102 offset:0\n"
      "ds_read_b64_tr_b16 v[30:31], v102 offset:256\n"
      "ds_read_b64_tr_b16 v[32:33], v102 offset:8\n"
      "ds_read_b64_tr_b16 v[34:35], v102 offset:264\n"
      "ds_read_b64_tr_b16 v[36:37], v102 offset:2048\n"
      "ds_read_b64_tr_b16 v[38:39], v102 offset:2304\n"
      "ds_read_b64_tr_b16 v[40:41], v102 offset:2056\n"
      "ds_read_b64_tr_b16 v[42:43], v102 offset:2312\n"
      "ds_read_b64_tr_b16 v[44:45], v102 offset:4096\n"
      "ds_read_b64_tr_b16 v[46:47], v102 offset:4352\n"
      "ds_read_b64_tr_b16 v[48:49], v102 offset:4104\n"
      "ds_read_b64_tr_b16 v[50:51], v102 offset:4360\n"
      "s_waitcnt lgkmcnt(0)\n"
      "v_mfma_f32_16x16x32_bf16 v[52:55], v[4:7], v[28:31], 0\n"
      "v_mfma_f32_16x16x32_bf16 v[56:59], v[8:11], v[28:31], 0\n"
      "v_mfma_f32_16x16x32_bf16 v[60:63], v[4:7], v[32:35], 0\n"
      "v_mfma_f32_16x16x32_bf16 v[64:67], v[8:11], v[32:35], 0\n"
      "v_mfma_f32_16x16x32_bf16 v[52:55], v[12:15], v[36:39], v[52:55]\n"
      "v_mfma_f32_16x16x32_bf16 v[56:59], v[16:19], v[36:39], v[56:59]\n"
      "v_mfma_f32_16x16x32_bf16 v[60:63], v[12:15], v[40:43], v[60:63]\n"
      "v_mfma_f32_16x16x32_bf16 v[64:67], v[16:19], v[40:43], v[64:67]\n"
      "v_mfma_f32_16x16x32_bf16 v[52:55], v[20:23], v[44:47], v[52:55]\n"
      "v_mfma_f32_16x16x32_bf16 v[56:59], v[24:27], v[44:47], v[56:59]\n"
      "v_mfma_f32_16x16x32_bf16 v[60:63], v[20:23], v[48:51], v[60:63]\n"
      "v_mfma_f32_16x16x32_bf16 v[64:67], v[24:27], v[48:51], v[64:67]\n"
      "s_nop 7\n"
      "ds_write_b128 v103, v[52:55] offset:0\n"
      "ds_write_b128 v103, v[56:59] offset:1024\n"
      "ds_write_b128 v103, v[60:63] offset:2048\n"
      "ds_write_b128 v103, v[64:67] offset:3072\n"
      "s_waitcnt lgkmcnt(0)\n"
      "s_barrier\n"
      "s_cmp_lg_u32 s76, 1\n"
      "s_cbranch_scc1 LJ1\n"
      "ds_read_b128 v[4:7], v104 offset:0\n"
      "ds_read_b128 v[8:11], v104 offset:1024\n"
      "ds_read_b128 v[12:15], v104 offset:2048\n"
      "ds_read_b128 v[16:19], v104 offset:3072\n"
      "ds_read_b128 v[20:23], v104 offset:4096\n"
      "ds_read_b128 v[24:27], v104 offset:5120\n"
      "ds_read_b128 v[28:31], v104 offset:6144\n"
      "ds_read_b128 v[32:35], v104 offset:7168\n"
      "ds_read_b128 v[36:39], v104 offset:8192\n"
      "ds_read_b128 v[40:43], v104 offset:9216\n"
      "ds_read_b128 v[44:47], v104 offset:10240\n"
      "ds_read_b128 v[48:51], v104 offset:11264\n"
      "ds_read_b128 v[52:55], v104 offset:12288\n"
      "ds_read_b128 v[56:59], v104 offset:13312\n"
      "ds_read_b128 v[60:63], v104 offset:14336\n"
      "ds_read_b128 v[64:67], v104 offset:15360\n"
      "s_waitcnt lgkmcnt(8)\n"
      "v_add_f32 v4, v4, v20\n"
      "v_add_f32 v5, v5, v21\n"
      "v_add_f32 v6, v6, v22\n"
      "v_add_f32 v7, v7, v23\n"
      "v_add_f32 v8, v8, v24\n"
      "v_add_f32 v9, v9, v25\n"
      "v_add_f32 v10, v10, v26\n"
      "v_add_f32 v11, v11, v27\n"
      "v_add_f32 v12, v12, v28\n"
      "v_add_f32 v13, v13, v29\n"
      "v_add_f32 v14, v14, v30\n"
      "v_add_f32 v15, v15, v31\n"
      "v_add_f32 v16, v16, v32\n"
      "v_add_f32 v17, v17, v33\n"
      "v_add_f32 v18, v18, v34\n"
      "v_add_f32 v19, v19, v35\n"
      "s_waitcnt lgkmcnt(4)\n"
      "v_add_f32 v4, v4, v36\n"
      "v_add_f32 v5, v5, v37\n"
      "v_add_f32 v6, v6, v38\n"
      "v_add_f32 v7, v7, v39\n"
      "v_add_f32 v8, v8, v40\n"
      "v_add_f32 v9, v9, v41\n"
      "v_add_f32 v10, v10, v42\n"
      "v_add_f32 v11, v11, v43\n"
      "v_add_f32 v12, v12, v44\n"
      "v_add_f32 v13, v13, v45\n"
      "v_add_f32 v14, v14, v46\n"
      "v_add_f32 v15, v15, v47\n"
      "v_add_f32 v16, v16, v48\n"
      "v_add_f32 v17, v17, v49\n"
      "v_add_f32 v18, v18, v50\n"
      "v_add_f32 v19, v19, v51\n"
      "s_waitcnt lgkmcnt(0)\n"
      "v_add_f32 v4, v4, v52\n"
      "v_add_f32 v5, v5, v53\n"
      "v_add_f32 v6, v6, v54\n"
      "v_add_f32 v7, v7, v55\n"
      "v_add_f32 v8, v8, v56\n"
      "v_add_f32 v9, v9, v57\n"
      "v_add_f32 v10, v10, v58\n"
      "v_add_f32 v11, v11, v59\n"
      "v_add_f32 v12, v12, v60\n"
      "v_add_f32 v13, v13, v61\n"
      "v_add_f32 v14, v14, v62\n"
      "v_add_f32 v15, v15, v63\n"
      "v_add_f32 v16, v16, v64\n"
      "v_add_f32 v17, v17, v65\n"
      "v_add_f32 v18, v18, v66\n"
      "v_add_f32 v19, v19, v67\n"
      "v_cvt_pk_bf16_f32 v68, v4, v5\n"
      "v_cvt_pk_bf16_f32 v69, v6, v7\n"
      "v_cvt_pk_bf16_f32 v70, v8, v9\n"
      "v_cvt_pk_bf16_f32 v71, v10, v11\n"
      "v_cvt_pk_bf16_f32 v72, v12, v13\n"
      "v_cvt_pk_bf16_f32 v73, v14, v15\n"
      "v_cvt_pk_bf16_f32 v74, v16, v17\n"
      "v_cvt_pk_bf16_f32 v75, v18, v19\n"
      "global_store_dwordx4 v239, v[68:71], s[72:73] sc1\n"
      "s_add_u32 s72, s72, 0x3000\n"
      "s_addc_u32 s73, s73, 0\n"
      "global_store_dwordx4 v239, v[72:75], s[72:73] sc1\n"
      "LJ1:\n"
      "s_add_u32 s78, s64, 0x0\n"
      "s_addc_u32 s79, s65, 0\n"
      "s_add_u32 m0, s74, 0x0\n"
      "s_nop 0\n"
      "global_load_lds_dwordx4 v240, s[78:79]\n"
      "s_add_u32 m0, s74, 0x400\n"
      "s_nop 0\n"
      "global_load_lds_dwordx4 v241, s[78:79]\n"
      "s_add_u32 m0, s74, 0x800\n"
      "s_nop 0\n"
      "global_load_lds_dwordx4 v242, s[78:79]\n"
      "s_add_u32 s78, s64, 0x3000\n"
      "s_addc_u32 s79, s65, 0\n"
      "s_add_u32 m0, s74, 0xc00\n"
      "s_nop 0\n"
      "global_load_lds_dwordx4 v240, s[78:79]\n"
      "s_add_u32 m0, s74, 0x1000\n"
      "s_nop 0\n"
      "global_load_lds_dwordx4 v241, s[78:79]\n"
      "s_add_u32 m0, s74, 0x1400\n"
      "s_nop 0\n"
      "global_load_lds_dwordx4 v242, s[78:79]\n"
      "s_add_u32 s78, s64, 0x6000\n"
      "s_addc_u32 s79, s65, 0\n"
      "s_add_u32 m0, s74, 0x6000\n"
      "s_nop 0\n"
      "global_load_lds_dwordx4 v240, s[78:79]\n"
      "s_add_u32 m0, s74, 0x6400\n"
      "s_nop 0\n"
      "global_load_lds_dwordx4 v241, s[78:79]\n"
      "s_add_u32 m0, s74, 0x6800\n"
      "s_nop 0\n"
      "global_load_lds_dwordx4 v242, s[78:79]\n"
      "s_add_u32 s78, s64, 0x9000\n"
      "s_addc_u32 s79, s65, 0\n"
      "s_add_u32 m0, s74, 0x6c00\n"
      "s_nop 0\n"
      "global_load_lds_dwordx4 v240, s[78:79]\n"
      "s_add_u32 m0, s74, 0x7000\n"
      "s_nop 0\n"
      "global_load_lds_dwordx4 v241, s[78:79]\n"
      "s_add_u32 m0, s74, 0x7400\n"
      "s_nop 0\n"
      "global_load_lds_dwordx4 v242, s[78:79]\n"
      "s_add_u32 s78, s64, 0xc000\n"
      "s_addc_u32 s79, s65, 0\n"
      "s_add_u32 m0, s74, 0xc000\n"
      "s_nop 0\n"
      "global_load_lds_dwordx4 v240, s[78:79]\n"
      "s_add_u32 m0, s74, 0xc400\n"
      "s_nop 0\n"
      "global_load_lds_dwordx4 v241, s[78:79]\n"
      "s_add_u32 m0, s74, 0xc800\n"
      "s_nop 0\n"
      "global_load_lds_dwordx4 v242, s[78:79]\n"
      "s_add_u32 s78, s64, 0xf000\n"
      "s_addc_u32 s79, s65, 0\n"
      "s_add_u32 m0, s74, 0xcc00\n"
      "s_nop 0\n"
      "global_load_lds_dwordx4 v240, s[78:79]\n"
      "s_add_u32 m0, s74, 0xd000\n"
      "s_nop 0\n"
      "global_load_lds_dwordx4 v241, s[78:79]\n"
      "s_add_u32 m0, s74, 0xd400\n"
      "s_nop 0\n"
      "global_load_lds_dwordx4 v242, s[78:79]\n"
      "s_add_u32 m0, s17, 0x25000\n"
      "s_nop 0\n"
      "global_load_lds_dwordx4 v239, s[46:47] offset:0\n"
      "s_add_u32 m0, s17, 0x25800\n"
      "s_nop 0\n"
      "global_load_lds_dwordx4 v239, s[46:47] offset:2048\n"
      "s_waitcnt vmcnt(14)\n"
      "ds_read_b64_tr_b16 a[0:1], v243 offset:0\n"
      "ds_read_b64_tr_b16 a[2:3], v243 offset:768\n"
      "ds_read_b64_tr_b16 a[4:5], v243 offset:8\n"
      "ds_read_b64_tr_b16 a[6:7], v243 offset:776\n"
      "ds_read_b64_tr_b16 a[8:9], v243 offset:64\n"
      "ds_read_b64_tr_b16 a[10:11], v243 offset:832\n"
      "ds_read_b64_tr_b16 a[12:13], v243 offset:72\n"
      "ds_read_b64_tr_b16 a[14:15], v243 offset:840\n"
      "ds_read_b64_tr_b16 a[16:17], v243 offset:128\n"
      "ds_read_b64_tr_b16 a[18:19], v243 offset:896\n"
      "ds_read_b64_tr_b16 a[20:21], v243 offset:136\n"
      "ds_read_b64_tr_b16 a[22:23], v243 offset:904\n"
      "s_barrier\n"
      "s_cmp_lg_u32 s76, 1\n"
      "s_cbranch_scc1 LA_0\n"
      "v_mov_b32 v244, s77\n"
      "s_mov_b64 exec, 1\n"
      "global_store_dwordx2 v244, v[252:253], s[28:29] sc1\n"
      "s_mov_b64 exec, -1\n"
      "s_nop 4\n"
      "LA_0:\n"
      "ds_read_b128 v[216:219], v232 offset:0\n"
      "ds_read_b128 v[220:223], v232 offset:4096\n"
      "ds_read_b128 v[224:227], v232 offset:8192\n"
      "s_waitcnt lgkmcnt(2)\n"
      "v_mfma_f32_16x16x32_bf16 v[0:3], a[0:3], v[216:219], 0\n"
      "s_add_u32 s78, s64, 0x12000\n"
      "s_addc_u32 s79, s65, 0\n"
      "s_add_u32 m0, s74, 0x0\n"
      "v_mfma_f32_16x16x32_bf16 v[4:7], a[4:7], v[216:219], 0\n"
      "global_load_lds_dwordx4 v240, s[78:79]\n"
      "v_mfma_f32_16x16x32_bf16 v[8:11], a[8:11], v[216:219], 0\n"
      "ds_read_b128 v[228:231], v232 offset:12288\n"
      "v_mfma_f32_16x16x32_bf16 v[12:15], a[12:15], v[216:219], 0\n"
      "v_mfma_f32_16x16x32_bf16 v[16:19], a[16:19], v[216:219], 0\n"
      "s_add_u32 m0, s74, 0x400\n"
      "v_mfma_f32_16x16x32_bf16 v[20:23], a[20:23], v[216:219], 0\n"
      "global_load_lds_dwordx4 v241, s[78:79]\n"
      "s_waitcnt lgkmcnt(2)\n"
      "v_mfma_f32_16x16x32_bf16 v[24:27], a[0:3], v[220:223], 0\n"
      "s_waitcnt vmcnt(10)\n"
      "v_mfma_f32_16x16x32_bf16 v[28:31], a[4:7], v[220:223], 0\n"
      "ds_read_b64_tr_b16 a[24:25], v243 offset:24576\n"
      "v_mfma_f32_16x16x32_bf16 v[32:35], a[8:11], v[220:223], 0\n"
      "ds_read_b128 v[216:219], v232 offset:16384\n"
      "s_add_u32 m0, s74, 0x800\n"
      "v_mfma_f32_16x16x32_bf16 v[36:39], a[12:15], v[220:223], 0\n"
      "ds_read_b64_tr_b16 a[26:27], v243 offset:25344\n"
      "global_load_lds_dwordx4 v242, s[78:79]\n"
      "v_mfma_f32_16x16x32_bf16 v[40:43], a[16:19], v[220:223], 0\n"
      "LPD0:\n"
      "s_cbranch_execz LPD3\n"
      "ds_read_b64_tr_b16 a[28:29], v243 offset:24584\n"
      "v_mfma_f32_16x16x32_bf16 v[44:47], a[20:23], v[220:223], 0\n"
      "s_waitcnt lgkmcnt(5)\n"
      "v_mfma_f32_16x16x32_bf16 v[48:51], a[0:3], v[224:227], 0\n"
      "ds_read_b64_tr_b16 a[30:31], v243 offset:25352\n"
      "s_add_u32 s78, s64, 0x15000\n"
      "s_addc_u32 s79, s65, 0\n"
      "s_add_u32 m0, s74, 0xc00\n"
      "v_mfma_f32_16x16x32_bf16 v[52:55], a[4:7], v[224:227], 0\n"
      "ds_read_b64_tr_b16 a[32:33], v243 offset:24640\n"
      "global_load_lds_dwordx4 v240, s[78:79]\n"
      "v_mfma_f32_16x16x32_bf16 v[56:59], a[8:11], v[224:227], 0\n"
      "ds_read_b128 v[220:223], v232 offset:20480\n"
      "v_mfma_f32_16x16x32_bf16 v[60:63], a[12:15], v[224:227], 0\n"
      "ds_read_b64_tr_b16 a[34:35], v243 offset:25408\n"
      "v_mfma_f32_16x16x32_bf16 v[64:67], a[16:19], v[224:227], 0\n"
      "ds_read_b64_tr_b16 a[36:37], v243 offset:24648\n"
      "s_add_u32 m0, s74, 0x1000\n"
      "v_mfma_f32_16x16x32_bf16 v[68:71], a[20:23], v[224:227], 0\n"
      "global_load_lds_dwordx4 v241, s[78:79]\n"
      "s_waitcnt lgkmcnt(9)\n"
      "v_mfma_f32_16x16x32_bf16 v[72:75], a[0:3], v[228:231], 0\n"
      "ds_read_b64_tr_b16 a[38:39], v243 offset:25416\n"
      "v_mfma_f32_16x16x32_bf16 v[76:79], a[4:7], v[228:231], 0\n"
      "ds_read_b64_tr_b16 a[40:41], v243 offset:24704\n"
      "v_mfma_f32_16x16x32_bf16 v[80:83], a[8:11], v[228:231], 0\n"
      "ds_read_b128 v[224:227], v232 offset:24576\n"
      "s_add_u32 m0, s74, 0x1400\n"
      "v_mfma_f32_16x16x32_bf16 v[84:87], a[12:15], v[228:231], 0\n"
      "ds_read_b64_tr_b16 a[42:43], v243 offset:25472\n"
      "global_load_lds_dwordx4 v242, s[78:79]\n"
      "v_mfma_f32_16x16x32_bf16 v[88:91], a[16:19], v[228:231], 0\n"
      "ds_read_b64_tr_b16 a[44:45], v243 offset:24712\n"
      "v_mfma_f32_16x16x32_bf16 v[92:95], a[20:23], v[228:231], 0\n"
      "s_waitcnt lgkmcnt(12)\n"
      "v_mfma_f32_16x16x32_bf16 v[96:99], a[0:3], v[216:219], 0\n"
      "ds_read_b64_tr_b16 a[46:47], v243 offset:25480\n"
      "v_mfma_f32_16x16x32_bf16 v[100:103], a[4:7], v[216:219], 0\n"
      "s_waitcnt vmcnt(8)\n"
      "v_mfma_f32_16x16x32_bf16 v[104:107], a[8:11], v[216:219], 0\n"
      "ds_read_b128 v[228:231], v232 offset:28672\n"
      "v_mfma_f32_16x16x32_bf16 v[108:111], a[12:15], v[216:219], 0\n"
      "ds_read_b64_tr_b16 a[48:49], v243 offset:49152\n"
      "v_mfma_f32_16x16x32_bf16 v[112:115], a[16:19], v[216:219], 0\n"
      "ds_read_b64_tr_b16 a[50:51], v243 offset:49920\n"
      "v_mfma_f32_16x16x32_bf16 v[116:119], a[20:23], v[216:219], 0\n"
      "s_waitcnt lgkmcnt(11)\n"
      "v_mfma_f32_16x16x32_bf16 v[120:123], a[0:3], v[220:223], 0\n"
      "ds_read_b64_tr_b16 a[52:53], v243 offset:49160\n"
      "v_mfma_f32_16x16x32_bf16 v[124:127], a[4:7], v[220:223], 0\n"
      "ds_read_b64_tr_b16 a[54:55], v243 offset:49928\n"
      "v_mfma_f32_16x16x32_bf16 v[128:131], a[8:11], v[220:223], 0\n"
      "ds_read_b128 v[216:219], v232 offset:32768\n"
      "v_add_u32 v232, 0x9000, v232\n"
      "v_mfma_f32_16x16x32_bf16 v[132:135], a[12:15], v[220:223], 0\n"
      "ds_read_b64_tr_b16 a[56:57], v243 offset:49216\n"
      "v_mfma_f32_16x16x32_bf16 v[136:139], a[16:19], v[220:223], 0\n"
      "ds_read_b64_tr_b16 a[58:59], v243 offset:49984\n"
      "s_waitcnt lgkmcnt(8)\n"
      "s_add_u32 s78, s64, 0x18000\n"
      "s_addc_u32 s79, s65, 0\n"
      "s_add_u32 m0, s74, 0x6000\n"
      "v_mfma_f32_16x16x32_bf16 v[140:143], a[20:23], v[220:223], 0\n"
      "global_load_lds_dwordx4 v240, s[78:79]\n"
      "v_mfma_f32_16x16x32_bf16 v[144:147], a[0:3], v[224:227], 0\n"
      "ds_read_b64_tr_b16 a[60:61], v243 offset:49224\n"
      "v_mfma_f32_16x16x32_bf16 v[148:151], a[4:7], v[224:227], 0\n"
      "ds_read_b64_tr_b16 a[62:63], v243 offset:49992\n"
      "v_mfma_f32_16x16x32_bf16 v[152:155], a[8:11], v[224:227], 0\n"
      "ds_read_b128 v[220:223], v233 offset:0\n"
      "s_add_u32 m0, s74, 0x6400\n"
      "v_mfma_f32_16x16x32_bf16 v[156:159], a[12:15], v[224:227], 0\n"
      "ds_read_b64_tr_b16 a[64:65], v243 offset:49280\n"
      "global_load_lds_dwordx4 v241, s[78:79]\n"
      "v_mfma_f32_16x16x32_bf16 v[160:163], a[16:19], v[224:227], 0\n"
      "ds_read_b64_tr_b16 a[66:67], v243 offset:50048\n"
      "v_mfma_f32_16x16x32_bf16 v[164:167], a[20:23], v[224:227], 0\n"
      "s_waitcnt lgkmcnt(12)\n"
      "v_mfma_f32_16x16x32_bf16 v[168:171], a[0:3], v[228:231], 0\n"
      "ds_read_b64_tr_b16 a[68:69], v243 offset:49288\n"
      "s_add_u32 m0, s74, 0x6800\n"
      "v_mfma_f32_16x16x32_bf16 v[172:175], a[4:7], v[228:231], 0\n"
      "ds_read_b64_tr_b16 a[70:71], v243 offset:50056\n"
      "global_load_lds_dwordx4 v242, s[78:79]\n"
      "v_mfma_f32_16x16x32_bf16 v[176:179], a[8:11], v[228:231], 0\n"
      "ds_read_b128 v[224:227], v233 offset:4096\n"
      "v_mfma_f32_16x16x32_bf16 v[180:183], a[12:15], v[228:231], 0\n"
      "v_mfma_f32_16x16x32_bf16 v[184:187], a[16:19], v[228:231], 0\n"
      "s_add_u32 s78, s64, 0x1b000\n"
      "s_addc_u32 s79, s65, 0\n"
      "s_add_u32 m0, s74, 0x6c00\n"
      "v_mfma_f32_16x16x32_bf16 v[188:191], a[20:23], v[228:231], 0\n"
      "global_load_lds_dwordx4 v240, s[78:79]\n"
      "s_waitcnt lgkmcnt(10)\n"
      "v_mfma_f32_16x16x32_bf16 v[192:195], a[0:3], v[216:219], 0\n"
      "v_mfma_f32_16x16x32_bf16 v[196:199], a[4:7], v[216:219], 0\n"
      "v_mfma_f32_16x16x32_bf16 v[200:203], a[8:11], v[216:219], 0\n"
      "ds_read_b128 v[228:231], v233 offset:8192\n"
      "s_add_u32 m0, s74, 0x7000\n"
      "v_mfma_f32_16x16x32_bf16 v[204:207], a[12:15], v[216:219], 0\n"
      "global_load_lds_dwordx4 v241, s[78:79]\n"
      "v_mfma_f32_16x16x32_bf16 v[208:211], a[16:19], v[216:219], 0\n"
      "v_mfma_f32_16x16x32_bf16 v[212:215], a[20:23], v[216:219], 0\n"
      "s_waitcnt lgkmcnt(6)\n"
      "v_mfma_f32_16x16x32_bf16 v[0:3], a[24:27], v[220:223], v[0:3]\n"
      "s_add_u32 m0, s74, 0x7400\n"
      "v_mfma_f32_16x16x32_bf16 v[4:7], a[28:31], v[220:223], v[4:7]\n"
      "global_load_lds_dwordx4 v242, s[78:79]\n"
      "v_mfma_f32_16x16x32_bf16 v[8:11], a[32:35], v[220:223], v[8:11]\n"
      "ds_read_b128 v[216:219], v233 offset:12288\n"
      "s_waitcnt vmcnt(6)\n"
      "v_mfma_f32_16x16x32_bf16 v[12:15], a[36:39], v[220:223], v[12:15]\n"
      "ds_read_b64_tr_b16 a[72:73], v243 offset:0\n"
      "v_mfma_f32_16x16x32_bf16 v[16:19], a[40:43], v[220:223], v[16:19]\n"
      "ds_read_b64_tr_b16 a[74:75], v243 offset:768\n"
      "v_mad_i32_i24 v238, v251, s89, v238\n"
      "s_add_u32 s56, s22, 0x0\n"
      "s_addc_u32 s57, s23, 0\n"
      "s_add_u32 m0, s18, 0x8f00\n"
      "v_mfma_f32_16x16x32_bf16 v[20:23], a[44:47], v[220:223], v[20:23]\n"
      "global_load_lds_dwordx4 v238, s[56:57] offset:256 \n"
      "s_waitcnt lgkmcnt(4)\n"
      "v_mfma_f32_16x16x32_bf16 v[24:27], a[24:27], v[224:227], v[24:27]\n"
      "ds_read_b64_tr_b16 a[76:77], v243 offset:8\n"
      "v_mfma_f32_16x16x32_bf16 v[28:31], a[28:31], v[224:227], v[28:31]\n"
      "ds_read_b64_tr_b16 a[78:79], v243 offset:776\n"
      "v_mad_i32_i24 v238, v251, s90, v238\n"
      "s_add_u32 s56, s22, 0x6000\n"
      "s_addc_u32 s57, s23, 0\n"
      "s_add_u32 m0, s18, 0x9f00\n"
      "v_mfma_f32_16x16x32_bf16 v[32:35], a[32:35], v[224:227], v[32:35]\n"
      "ds_read_b128 v[220:223], v233 offset:16384\n"
      "global_load_lds_dwordx4 v238, s[56:57] offset:256 \n"
      "v_mfma_f32_16x16x32_bf16 v[36:39], a[36:39], v[224:227], v[36:39]\n"
      "ds_read_b64_tr_b16 a[80:81], v243 offset:64\n"
      "v_mfma_f32_16x16x32_bf16 v[40:43], a[40:43], v[224:227], v[40:43]\n"
      "ds_read_b64_tr_b16 a[82:83], v243 offset:832\n"
      "v_mad_i32_i24 v238, v251, s91, v238\n"
      "s_add_u32 s56, s22, 0xc000\n"
      "s_addc_u32 s57, s23, 0\n"
      "s_add_u32 m0, s18, 0xaf00\n"
      "v_mfma_f32_16x16x32_bf16 v[44:47], a[44:47], v[224:227], v[44:47]\n"
      "global_load_lds_dwordx4 v238, s[56:57] offset:256 \n"
      "s_waitcnt lgkmcnt(8)\n"
      "v_mfma_f32_16x16x32_bf16 v[48:51], a[24:27], v[228:231], v[48:51]\n"
      "ds_read_b64_tr_b16 a[84:85], v243 offset:72\n"
      "v_mfma_f32_16x16x32_bf16 v[52:55], a[28:31], v[228:231], v[52:55]\n"
      "ds_read_b64_tr_b16 a[86:87], v243 offset:840\n"
      "v_mad_i32_i24 v238, v251, s92, v238\n"
      "s_add_u32 s56, s22, 0x12000\n"
      "s_addc_u32 s57, s23, 0\n"
      "s_add_u32 m0, s18, 0xbf00\n"
      "v_mfma_f32_16x16x32_bf16 v[56:59], a[32:35], v[228:231], v[56:59]\n"
      "ds_read_b128 v[224:227], v233 offset:20480\n"
      "global_load_lds_dwordx4 v238, s[56:57] offset:256 \n"
      "v_mfma_f32_16x16x32_bf16 v[60:63], a[36:39], v[228:231], v[60:63]\n"
      "ds_read_b64_tr_b16 a[88:89], v243 offset:128\n"
      "v_mfma_f32_16x16x32_bf16 v[64:67], a[40:43], v[228:231], v[64:67]\n"
      "ds_read_b64_tr_b16 a[90:91], v243 offset:896\n"
      "v_mad_i32_i24 v238, v251, s93, v238\n"
      "s_add_u32 s56, s22, 0x18000\n"
      "s_addc_u32 s57, s23, 0\n"
      "s_add_u32 m0, s18, 0xcf00\n"
      "v_mfma_f32_16x16x32_bf16 v[68:71], a[44:47], v[228:231], v[68:71]\n"
      "global_load_lds_dwordx4 v238, s[56:57] offset:256 \n"
      "s_waitcnt lgkmcnt(12)\n"
      "v_mfma_f32_16x16x32_bf16 v[72:75], a[24:27], v[216:219], v[72:75]\n"
      "ds_read_b64_tr_b16 a[92:93], v243 offset:136\n"
      "v_mfma_f32_16x16x32_bf16 v[76:79], a[28:31], v[216:219], v[76:79]\n"
      "ds_read_b64_tr_b16 a[94:95], v243 offset:904\n"
      "v_mad_i32_i24 v238, v251, s94, v238\n"
      "s_add_u32 s56, s22, 0x1e000\n"
      "s_addc_u32 s57, s23, 0\n"
      "s_add_u32 m0, s18, 0xdf00\n"
      "v_mfma_f32_16x16x32_bf16 v[80:83], a[32:35], v[216:219], v[80:83]\n"
      "ds_read_b128 v[228:231], v233 offset:24576\n"
      "global_load_lds_dwordx4 v238, s[56:57] offset:256 \n"
      "v_mfma_f32_16x16x32_bf16 v[84:87], a[36:39], v[216:219], v[84:87]\n"
      "v_mfma_f32_16x16x32_bf16 v[88:91], a[40:43], v[216:219], v[88:91]\n"
      "v_mad_i32_i24 v238, v251, s95, v238\n"
      "s_add_u32 s56, s22, 0x24000\n"
      "s_addc_u32 s57, s23, 0\n"
      "s_add_u32 m0, s18, 0xef00\n"
      "v_mfma_f32_16x16x32_bf16 v[92:95], a[44:47], v[216:219], v[92:95]\n"
      "global_load_lds_dwordx4 v238, s[56:57] offset:256 \n"
      "s_waitcnt lgkmcnt(10)\n"
      "v_mfma_f32_16x16x32_bf16 v[96:99], a[24:27], v[220:223], v[96:99]\n"
      "v_mfma_f32_16x16x32_bf16 v[100:103], a[28:31], v[220:223], v[100:103]\n"
      "v_mad_i32_i24 v238, v251, s96, v238\n"
      "s_add_u32 s56, s22, 0x2a000\n"
      "s_addc_u32 s57, s23, 0\n"
      "s_add_u32 m0, s18, 0xff00\n"
      "v_mfma_f32_16x16x32_bf16 v[104:107], a[32:35], v[220:223], v[104:107]\n"
      "ds_read_b128 v[216:219], v233 offset:28672\n"
      "global_load_lds_dwordx4 v238, s[56:57] offset:256 \n"
      "v_mfma_f32_16x16x32_bf16 v[108:111], a[36:39], v[220:223], v[108:111]\n"
      "v_mfma_f32_16x16x32_bf16 v[112:115], a[40:43], v[220:223], v[112:115]\n"
      "v_mad_i32_i24 v238, v251, s97, v238\n"
      "s_add_u32 s56, s22, 0x30000\n"
      "s_addc_u32 s57, s23, 0\n"
      "s_add_u32 m0, s18, 0x10f00\n"
      "v_mfma_f32_16x16x32_bf16 v[116:119], a[44:47], v[220:223], v[116:119]\n"
      "global_load_lds_dwordx4 v238, s[56:57] offset:256 \n"
      "s_waitcnt lgkmcnt(6)\n"
      "v_mfma_f32_16x16x32_bf16 v[120:123], a[24:27], v[224:227], v[120:123]\n"
      "v_mfma_f32_16x16x32_bf16 v[124:127], a[28:31], v[224:227], v[124:127]\n"
      "v_mfma_f32_16x16x32_bf16 v[128:131], a[32:35], v[224:227], v[128:131]\n"
      "ds_read_b128 v[220:223], v233 offset:32768\n"
      "v_add_u32 v233, 0x9000, v233\n"
      "s_add_u32 s78, s64, 0x1e000\n"
      "s_addc_u32 s79, s65, 0\n"
      "s_add_u32 m0, s74, 0xc000\n"
      "v_mfma_f32_16x16x32_bf16 v[132:135], a[36:39], v[224:227], v[132:135]\n"
      "global_load_lds_dwordx4 v240, s[78:79]\n"
      "v_mfma_f32_16x16x32_bf16 v[136:139], a[40:43], v[224:227], v[136:139]\n"
      "v_mfma_f32_16x16x32_bf16 v[140:143], a[44:47], v[224:227], v[140:143]\n"
      "s_waitcnt lgkmcnt(2)\n"
      "v_mfma_f32_16x16x32_bf16 v[144:147], a[24:27], v[228:231], v[144:147]\n"
      "s_waitcnt vmcnt(10)\n"
      "s_add_u32 m0, s74, 0xc400\n"
      "v_mfma_f32_16x16x32_bf16 v[148:151], a[28:31], v[228:231], v[148:151]\n"
      "ds_read_b64_tr_b16 a[96:97], v243 offset:24576\n"
      "global_load_lds_dwordx4 v241, s[78:79]\n"
      "v_mfma_f32_16x16x32_bf16 v[152:155], a[32:35], v[228:231], v[152:155]\n"
      "ds_read_b128 v[224:227], v234 offset:0\n"
      "v_mfma_f32_16x16x32_bf16 v[156:159], a[36:39], v[228:231], v[156:159]\n"
      "ds_read_b64_tr_b16 a[98:99], v243 offset:25344\n"
      "v_mfma_f32_16x16x32_bf16 v[160:163], a[40:43], v[228:231], v[160:163]\n"
      "ds_read_b64_tr_b16 a[100:101], v243 offset:24584\n"
      "s_add_u32 m0, s74, 0xc800\n"
      "v_mfma_f32_16x16x32_bf16 v[164:167], a[44:47], v[228:231], v[164:167]\n"
      "global_load_lds_dwordx4 v242, s[78:79]\n"
      "s_waitcnt lgkmcnt(5)\n"
      "v_mfma_f32_16x16x32_bf16 v[168:171], a[24:27], v[216:219], v[168:171]\n"
      "ds_read_b64_tr_b16 a[102:103], v243 offset:25352\n"
      "v_mfma_f32_16x16x32_bf16 v[172:175], a[28:31], v[216:219], v[172:175]\n"
      "ds_read_b64_tr_b16 a[104:105], v243 offset:24640\n"
      "v_mfma_f32_16x16x32_bf16 v[176:179], a[32:35], v[216:219], v[176:179]\n"
      "ds_read_b128 v[228:231], v234 offset:4096\n"
      "s_add_u32 s78, s64, 0x21000\n"
      "s_addc_u32 s79, s65, 0\n"
      "s_add_u32 m0, s74, 0xcc00\n"
      "v_mfma_f32_16x16x32_bf16 v[180:183], a[36:39], v[216:219], v[180:183]\n"
      "ds_read_b64_tr_b16 a[106:107], v243 offset:25408\n"
      "global_load_lds_dwordx4 v240, s[78:79]\n"
      "v_mfma_f32_16x16x32_bf16 v[184:187], a[40:43], v[216:219], v[184:187]\n"
      "ds_read_b64_tr_b16 a[108:109], v243 offset:24648\n"
      "v_mfma_f32_16x16x32_bf16 v[188:191], a[44:47], v[216:219], v[188:191]\n"
      "s_waitcnt lgkmcnt(9)\n"
      "v_mfma_f32_16x16x32_bf16 v[192:195], a[24:27], v[220:223], v[192:195]\n"
      "ds_read_b64_tr_b16 a[110:111], v243 offset:25416\n"
      "s_add_u32 m0, s74, 0xd000\n"
      "v_mfma_f32_16x16x32_bf16 v[196:199], a[28:31], v[220:223], v[196:199]\n"
      "ds_read_b64_tr_b16 a[112:113], v243 offset:24704\n"
      "global_load_lds_dwordx4 v241, s[78:79]\n"
      "v_mfma_f32_16x16x32_bf16 v[200:203], a[32:35], v[220:223], v[200:203]\n"
      "ds_read_b128 v[216:219], v234 offset:8192\n"
      "v_mfma_f32_16x16x32_bf16 v[204:207], a[36:39], v[220:223], v[204:207]\n"
      "ds_read_b64_tr_b16 a[114:115], v243 offset:25472\n"
      "v_mfma_f32_16x16x32_bf16 v[208:211], a[40:43], v[220:223], v[208:211]\n"
      "ds_read_b64_tr_b16 a[116:117], v243 offset:24712\n"
      "s_add_u32 m0, s74, 0xd400\n"
      "v_mfma_f32_16x16x32_bf16 v[212:215], a[44:47], v[220:223], v[212:215]\n"
      "global_load_lds_dwordx4 v242, s[78:79]\n"
      "s_waitcnt lgkmcnt(12)\n"
      "v_mfma_f32_16x16x32_bf16 v[0:3], a[48:51], v[224:227], v[0:3]\n"
      "ds_read_b64_tr_b16 a[118:119], v243 offset:25480\n"
      "v_mfma_f32_16x16x32_bf16 v[4:7], a[52:55], v[224:227], v[4:7]\n"
      "v_mfma_f32_16x16x32_bf16 v[8:11], a[56:59], v[224:227], v[8:11]\n"
      "ds_read_b128 v[220:223], v234 offset:12288\n"
      "s_add_u32 s78, s64, 0x24000\n"
      "s_addc_u32 s79, s65, 0\n"
      "s_add_u32 m0, s74, 0x0\n"
      "v_mfma_f32_16x16x32_bf16 v[12:15], a[60:63], v[224:227], v[12:15]\n"
      "global_load_lds_dwordx4 v240, s[78:79]\n"
      "v_mfma_f32_16x16x32_bf16 v[16:19], a[64:67], v[224:227], v[16:19]\n"
      "v_mfma_f32_16x16x32_bf16 v[20:23], a[68:71], v[224:227], v[20:23]\n"
      "s_waitcnt lgkmcnt(9)\n"
      "v_mfma_f32_16x16x32_bf16 v[24:27], a[48:51], v[228:231], v[24:27]\n"
      "s_add_u32 m0, s74, 0x400\n"
      "v_mfma_f32_16x16x32_bf16 v[28:31], a[52:55], v[228:231], v[28:31]\n"
      "global_load_lds_dwordx4 v241, s[78:79]\n"
      "v_mfma_f32_16x16x32_bf16 v[32:35], a[56:59], v[228:231], v[32:35]\n"
      "ds_read_b128 v[224:227], v234 offset:16384\n"
      "v_mfma_f32_16x16x32_bf16 v[36:39], a[60:63], v[228:231], v[36:39]\n"
      "v_mfma_f32_16x16x32_bf16 v[40:43], a[64:67], v[228:231], v[40:43]\n"
      "s_add_u32 m0, s74, 0x800\n"
      "v_mfma_f32_16x16x32_bf16 v[44:47], a[68:71], v[228:231], v[44:47]\n"
      "global_load_lds_dwordx4 v242, s[78:79]\n"
      "s_waitcnt lgkmcnt(5)\n"
      "v_mfma_f32_16x16x32_bf16 v[48:51], a[48:51], v[216:219], v[48:51]\n"
      "v_mfma_f32_16x16x32_bf16 v[52:55], a[52:55], v[216:219], v[52:55]\n"
      "v_mfma_f32_16x16x32_bf16 v[56:59], a[56:59], v[216:219], v[56:59]\n"
      "ds_read_b128 v[228:231], v234 offset:20480\n"
      "s_add_u32 s78, s64, 0x27000\n"
      "s_addc_u32 s79, s65, 0\n"
      "s_add_u32 m0, s74, 0xc00\n"
      "v_mfma_f32_16x16x32_bf16 v[60:63], a[60:63], v[216:219], v[60:63]\n"
      "global_load_lds_dwordx4 v240, s[78:79]\n"
      "v_mfma_f32_16x16x32_bf16 v[64:67], a[64:67], v[216:219], v[64:67]\n"
      "v_mfma_f32_16x16x32_bf16 v[68:71], a[68:71], v[216:219], v[68:71]\n"
      "s_waitcnt lgkmcnt(2)\n"
      "v_mfma_f32_16x16x32_bf16 v[72:75], a[48:51], v[220:223], v[72:75]\n"
      "s_add_u32 m0, s74, 0x1000\n"
      "v_mfma_f32_16x16x32_bf16 v[76:79], a[52:55], v[220:223], v[76:79]\n"
      "global_load_lds_dwordx4 v241, s[78:79]\n"
      "v_mfma_f32_16x16x32_bf16 v[80:83], a[56:59], v[220:223], v[80:83]\n"
      "ds_read_b128 v[216:219], v234 offset:24576\n"
      "v_mfma_f32_16x16x32_bf16 v[84:87], a[60:63], v[220:223], v[84:87]\n"
      "v_mfma_f32_16x16x32_bf16 v[88:91], a[64:67], v[220:223], v[88:91]\n"
      "s_add_u32 m0, s74, 0x1400\n"
      "v_mfma_f32_16x16x32_bf16 v[92:95], a[68:71], v[220:223], v[92:95]\n"
      "global_load_lds_dwordx4 v242, s[78:79]\n"
      "s_waitcnt lgkmcnt(2)\n"
      "v_mfma_f32_16x16x32_bf16 v[96:99], a[48:51], v[224:227], v[96:99]\n"
      "v_mfma_f32_16x16x32_bf16 v[100:103], a[52:55], v[224:227], v[100:103]\n"
      "v_mfma_f32_16x16x32_bf16 v[104:107], a[56:59], v[224:227], v[104:107]\n"
      "ds_read_b128 v[220:223], v234 offset:28672\n"
      "s_add_u32 s78, s64, 0x2a000\n"
      "s_addc_u32 s79, s65, 0\n"
      "s_add_u32 m0, s74, 0x6000\n"
      "v_mfma_f32_16x16x32_bf16 v[108:111], a[60:63], v[224:227], v[108:111]\n"
      "global_load_lds_dwordx4 v240, s[78:79]\n"
      "v_mfma_f32_16x16x32_bf16 v[112:115], a[64:67], v[224:227], v[112:115]\n"
      "v_mfma_f32_16x16x32_bf16 v[116:119], a[68:71], v[224:227], v[116:119]\n"
      "s_waitcnt lgkmcnt(2)\n"
      "v_mfma_f32_16x16x32_bf16 v[120:123], a[48:51], v[228:231], v[120:123]\n"
      "s_add_u32 m0, s74, 0x6400\n"
      "v_mfma_f32_16x16x32_bf16 v[124:127], a[52:55], v[228:231], v[124:127]\n"
      "global_load_lds_dwordx4 v241, s[78:79]\n"
      "v_mfma_f32_16x16x32_bf16 v[128:131], a[56:59], v[228:231], v[128:131]\n"
      "ds_read_b128 v[224:227], v234 offset:32768\n"
      "v_add_u32 v234, 0x9000, v234\n"
      "v_mfma_f32_16x16x32_bf16 v[132:135], a[60:63], v[228:231], v[132:135]\n"
      "v_mfma_f32_16x16x32_bf16 v[136:139], a[64:67], v[228:231], v[136:139]\n"
      "s_waitcnt vmcnt(8)\n"
      "s_add_u32 m0, s74, 0x6800\n"
      "v_mfma_f32_16x16x32_bf16 v[140:143], a[68:71], v[228:231], v[140:143]\n"
      "global_load_lds_dwordx4 v242, s[78:79]\n"
      "s_waitcnt lgkmcnt(2)\n"
      "v_mfma_f32_16x16x32_bf16 v[144:147], a[48:51], v[216:219], v[144:147]\n"
      "ds_read_b64_tr_b16 a[120:121], v243 offset:49152\n"
      "v_mfma_f32_16x16x32_bf16 v[148:151], a[52:55], v[216:219], v[148:151]\n"
      "ds_read_b64_tr_b16 a[122:123], v243 offset:49920\n"
      "v_mfma_f32_16x16x32_bf16 v[152:155], a[56:59], v[216:219], v[152:155]\n"
      "ds_read_b128 v[228:231], v235 offset:0\n"
      "s_add_u32 s78, s64, 0x2d000\n"
      "s_addc_u32 s79, s65, 0\n"
      "s_add_u32 m0, s74, 0x6c00\n"
      "v_mfma_f32_16x16x32_bf16 v[156:159], a[60:63], v[216:219], v[156:159]\n"
      "ds_read_b64_tr_b16 a[124:125], v243 offset:49160\n"
      "global_load_lds_dwordx4 v240, s[78:79]\n"
      "v_mfma_f32_16x16x32_bf16 v[160:163], a[64:67], v[216:219], v[160:163]\n"
      "ds_read_b64_tr_b16 a[126:127], v243 offset:49928\n"
      "v_mfma_f32_16x16x32_bf16 v[164:167], a[68:71], v[216:219], v[164:167]\n"
      "s_waitcnt lgkmcnt(6)\n"
      "v_mfma_f32_16x16x32_bf16 v[168:171], a[48:51], v[220:223], v[168:171]\n"
      "ds_read_b64_tr_b16 a[128:129], v243 offset:49216\n"
      "s_add_u32 m0, s74, 0x7000\n"
      "v_mfma_f32_16x16x32_bf16 v[172:175], a[52:55], v[220:223], v[172:175]\n"
      "ds_read_b64_tr_b16 a[130:131], v243 offset:49984\n"
      "global_load_lds_dwordx4 v241, s[78:79]\n"
      "v_mfma_f32_16x16x32_bf16 v[176:179], a[56:59], v[220:223], v[176:179]\n"
      "ds_read_b128 v[216:219], v235 offset:4096\n"
      "v_mfma_f32_16x16x32_bf16 v[180:183], a[60:63], v[220:223], v[180:183]\n"
      "ds_read_b64_tr_b16 a[132:133], v243 offset:49224\n"
      "v_mfma_f32_16x16x32_bf16 v[184:187], a[64:67], v[220:223], v[184:187]\n"
      "ds_read_b64_tr_b16 a[134:135], v243 offset:49992\n"
      "s_add_u32 m0, s74, 0x7400\n"
      "v_mfma_f32_16x16x32_bf16 v[188:191], a[68:71], v[220:223], v[188:191]\n"
      "global_load_lds_dwordx4 v242, s[78:79]\n"
      "s_waitcnt lgkmcnt(10)\n"
      "v_mfma_f32_16x16x32_bf16 v[192:195], a[48:51], v[224:227], v[192:195]\n"
      "ds_read_b64_tr_b16 a[136:137], v243 offset:49280\n"
      "v_mfma_f32_16x16x32_bf16 v[196:199], a[52:55], v[224:227], v[196:199]\n"
      "ds_read_b64_tr_b16 a[138:139], v243 offset:50048\n"
      "v_mfma_f32_16x16x32_bf16 v[200:203], a[56:59], v[224:227], v[200:203]\n"
      "ds_read_b128 v[220:223], v235 offset:8192\n"
      "global_load_dwordx4 v[244:247], v248, s[28:29] sc1\n"
      "v_mfma_f32_16x16x32_bf16 v[204:207], a[60:63], v[224:227], v[204:207]\n"
      "ds_read_b64_tr_b16 a[140:141], v243 offset:49288\n"
      "v_mfma_f32_16x16x32_bf16 v[208:211], a[64:67], v[224:227], v[208:211]\n"
      "ds_read_b64_tr_b16 a[142:143], v243 offset:50056\n"
      "v_mfma_f32_16x16x32_bf16 v[212:215], a[68:71], v[224:227], v[212:215]\n"
      "s_waitcnt lgkmcnt(12)\n"
      "v_mfma_f32_16x16x32_bf16 v[0:3], a[72:75], v[228:231], v[0:3]\n"
      "v_mfma_f32_16x16x32_bf16 v[4:7], a[76:79], v[228:231], v[4:7]\n"
      "v_mfma_f32_16x16x32_bf16 v[8:11], a[80:83], v[228:231], v[8:11]\n"
      "ds_read_b128 v[224:227], v235 offset:12288\n"
      "v_mfma_f32_16x16x32_bf16 v[12:15], a[84:87], v[228:231], v[12:15]\n"
      "v_mfma_f32_16x16x32_bf16 v[16:19], a[88:91], v[228:231], v[16:19]\n"
      "s_waitcnt vmcnt(7)\n"
      "v_mfma_f32_16x16x32_bf16 v[20:23], a[92:95], v[228:231], v[20:23]\n"
      "s_waitcnt lgkmcnt(8)\n"
      "v_mfma_f32_16x16x32_bf16 v[24:27], a[72:75], v[216:219], v[24:27]\n"
      "ds_read_b64_tr_b16 a[144:145], v243 offset:0\n"
      "v_mfma_f32_16x16x32_bf16 v[28:31], a[76:79], v[216:219], v[28:31]\n"
      "ds_read_b64_tr_b16 a[146:147], v243 offset:768\n"
      "v_mfma_f32_16x16x32_bf16 v[32:35], a[80:83], v[216:219], v[32:35]\n"
      "ds_read_b128 v[228:231], v235 offset:16384\n"
      "s_waitcnt lgkmcnt(4)\n"
      "s_add_u32 s78, s64, 0x30000\n"
      "s_addc_u32 s79, s65, 0\n"
      "s_add_u32 m0, s74, 0xc000\n"
      "v_mfma_f32_16x16x32_bf16 v[36:39], a[84:87], v[216:219], v[36:39]\n"
      "ds_read_b64_tr_b16 a[148:149], v243 offset:8\n"
      "global_load_lds_dwordx4 v240, s[78:79]\n"
      "v_mfma_f32_16x16x32_bf16 v[40:43], a[88:91], v[216:219], v[40:43]\n"
      "ds_read_b64_tr_b16 a[150:151], v243 offset:776\n"
      "v_mfma_f32_16x16x32_bf16 v[44:47], a[92:95], v[216:219], v[44:47]\n"
      "v_mfma_f32_16x16x32_bf16 v[48:51], a[72:75], v[220:223], v[48:51]\n"
      "ds_read_b64_tr_b16 a[152:153], v243 offset:64\n"
      "s_add_u32 m0, s74, 0xc400\n"
      "v_mfma_f32_16x16x32_bf16 v[52:55], a[76:79], v[220:223], v[52:55]\n"
      "ds_read_b64_tr_b16 a[154:155], v243 offset:832\n"
      "global_load_lds_dwordx4 v241, s[78:79]\n"
      "v_mfma_f32_16x16x32_bf16 v[56:59], a[80:83], v[220:223], v[56:59]\n"
      "ds_read_b128 v[216:219], v235 offset:20480\n"
      "v_mfma_f32_16x16x32_bf16 v[60:63], a[84:87], v[220:223], v[60:63]\n"
      "ds_read_b64_tr_b16 a[156:157], v243 offset:72\n"
      "v_mfma_f32_16x16x32_bf16 v[64:67], a[88:91], v[220:223], v[64:67]\n"
      "ds_read_b64_tr_b16 a[158:159], v243 offset:840\n"
      "s_add_u32 m0, s74, 0xc800\n"
      "v_mfma_f32_16x16x32_bf16 v[68:71], a[92:95], v[220:223], v[68:71]\n"
      "global_load_lds_dwordx4 v242, s[78:79]\n"
      "s_waitcnt lgkmcnt(10)\n"
      "v_mfma_f32_16x16x32_bf16 v[72:75], a[72:75], v[224:227], v[72:75]\n"
      "ds_read_b64_tr_b16 a[160:161], v243 offset:128\n"
      "v_mfma_f32_16x16x32_bf16 v[76:79], a[76:79], v[224:227], v[76:79]\n"
      "ds_read_b64_tr_b16 a[162:163], v243 offset:896\n"
      "v_mfma_f32_16x16x32_bf16 v[80:83], a[80:83], v[224:227], v[80:83]\n"
      "ds_read_b128 v[220:223], v235 offset:24576\n"
      "s_add_u32 s78, s64, 0x33000\n"
      "s_addc_u32 s79, s65, 0\n"
      "s_add_u32 m0, s74, 0xcc00\n"
      "v_mfma_f32_16x16x32_bf16 v[84:87], a[84:87], v[224:227], v[84:87]\n"
      "ds_read_b64_tr_b16 a[164:165], v243 offset:136\n"
      "global_load_lds_dwordx4 v240, s[78:79]\n"
      "v_mfma_f32_16x16x32_bf16 v[88:91], a[88:91], v[224:227], v[88:91]\n"
      "ds_read_b64_tr_b16 a[166:167], v243 offset:904\n"
      "v_mfma_f32_16x16x32_bf16 v[92:95], a[92:95], v[224:227], v[92:95]\n"
      "s_waitcnt lgkmcnt(12)\n"
      "v_mfma_f32_16x16x32_bf16 v[96:99], a[72:75], v[228:231], v[96:99]\n"
      "s_add_u32 m0, s74, 0xd000\n"
      "v_mfma_f32_16x16x32_bf16 v[100:103], a[76:79], v[228:231], v[100:103]\n"
      "global_load_lds_dwordx4 v241, s[78:79]\n"
      "v_mfma_f32_16x16x32_bf16 v[104:107], a[80:83], v[228:231], v[104:107]\n"
      "ds_read_b128 v[224:227], v235 offset:28672\n"
      "v_mfma_f32_16x16x32_bf16 v[108:111], a[84:87], v[228:231], v[108:111]\n"
      "v_mfma_f32_16x16x32_bf16 v[112:115], a[88:91], v[228:231], v[112:115]\n"
      "s_waitcnt vmcnt(6)\n"
      "s_add_u32 m0, s74, 0xd400\n"
      "v_mfma_f32_16x16x32_bf16 v[116:119], a[92:95], v[228:231], v[116:119]\n"
      "global_load_lds_dwordx4 v242, s[78:79]\n"
      "s_waitcnt lgkmcnt(8)\n"
      "v_mfma_f32_16x16x32_bf16 v[120:123], a[72:75], v[216:219], v[120:123]\n"
      "ds_read_b64_tr_b16 a[168:169], v243 offset:24576\n"
      "v_mfma_f32_16x16x32_bf16 v[124:127], a[76:79], v[216:219], v[124:127]\n"
      "ds_read_b64_tr_b16 a[170:171], v243 offset:25344\n"
      "v_mfma_f32_16x16x32_bf16 v[128:131], a[80:83], v[216:219], v[128:131]\n"
      "ds_read_b128 v[228:231], v235 offset:32768\n"
      "v_add_u32 v235, 0x9000, v235\n"
      "s_waitcnt lgkmcnt(4)\n"
      "s_add_u32 s78, s64, 0x36000\n"
      "s_addc_u32 s79, s65, 0\n"
      "s_add_u32 m0, s74, 0x0\n"
      "v_mfma_f32_16x16x32_bf16 v[132:135], a[84:87], v[216:219], v[132:135]\n"
      "ds_read_b64_tr_b16 a[172:173], v243 offset:24584\n"
      "global_load_lds_dwordx4 v240, s[78:79]\n"
      "v_mfma_f32_16x16x32_bf16 v[136:139], a[88:91], v[216:219], v[136:139]\n"
      "ds_read_b64_tr_b16 a[174:175], v243 offset:25352\n"
      "v_mfma_f32_16x16x32_bf16 v[140:143], a[92:95], v[216:219], v[140:143]\n"
      "v_mfma_f32_16x16x32_bf16 v[144:147], a[72:75], v[220:223], v[144:147]\n"
      "ds_read_b64_tr_b16 a[176:177], v243 offset:24640\n"
      "s_waitcnt vmcnt(7)\n"
      "v_cmp_ne_u64 vcc, s[80:81], v[244:245]\n"
      "v_cmp_ne_u64 s[56:57], s[80:81], v[246:247]\n"
      "s_nop 3\n"
      "s_or_b64 vcc, vcc, s[56:57]\n"
      "s_cbranch_scc0 LP1_0\n"
      "s_mov_b32 s82, 0x40000\n"
      "LP0_0:\n"
      "s_sleep 2\n"
      "global_load_dwordx4 v[244:247], v248, s[28:29] sc1\n"
      "s_sub_u32 s82, s82, 1\n"
      "s_cbranch_scc1 LP2_0\n"
      "s_waitcnt vmcnt(0)\n"
      "v_cmp_ne_u64 vcc, s[80:81], v[244:245]\n"
      "v_cmp_ne_u64 s[56:57], s[80:81], v[246:247]\n"
      "s_nop 3\n"
      "s_or_b64 vcc, vcc, s[56:57]\n"
      "s_cbranch_scc1 LP0_0\n"
      "LP2_0:\n"
      "s_waitcnt vmcnt(0)\n"
      "LP1_0:\n"
      "s_nop 0\n"
      "v_mfma_f32_16x16x32_bf16 v[148:151], a[76:79], v[220:223], v[148:151]\n"
      "s_barrier\n"
      "s_add_u32 m0, s74, 0x400\n"
      "v_mfma_f32_16x16x32_bf16 v[152:155], a[80:83], v[220:223], v[152:155]\n"
      "ds_read_b128 v[216:219], v232 offset:0\n"
      "global_load_lds_dwordx4 v241, s[78:79]\n"
      "v_mfma_f32_16x16x32_bf16 v[156:159], a[84:87], v[220:223], v[156:159]\n"
      "ds_read_b64_tr_b16 a[178:179], v243 offset:25408\n"
      "v_mfma_f32_16x16x32_bf16 v[160:163], a[88:91], v[220:223], v[160:163]\n"
      "ds_read_b64_tr_b16 a[180:181], v243 offset:24648\n"
      "v_mfma_f32_16x16x32_bf16 v[164:167], a[92:95], v[220:223], v[164:167]\n"
      "s_add_u32 m0, s74, 0x800\n"
      "s_waitcnt lgkmcnt(9)\n"
      "v_mfma_f32_16x16x32_bf16 v[168:171], a[72:75], v[224:227], v[168:171]\n"
      "ds_read_b64_tr_b16 a[182:183], v243 offset:25416\n"
      "global_load_lds_dwordx4 v242, s[78:79]\n"
      "v_mfma_f32_16x16x32_bf16 v[172:175], a[76:79], v[224:227], v[172:175]\n"
      "ds_read_b64_tr_b16 a[184:185], v243 offset:24704\n"
      "v_mfma_f32_16x16x32_bf16 v[176:179], a[80:83], v[224:227], v[176:179]\n"
      "ds_read_b128 v[220:223], v232 offset:4096\n"
      "v_mfma_f32_16x16x32_bf16 v[180:183], a[84:87], v[224:227], v[180:183]\n"
      "ds_read_b64_tr_b16 a[186:187], v243 offset:25472\n"
      "s_add_u32 s78, s64, 0x39000\n"
      "s_addc_u32 s79, s65, 0\n"
      "s_add_u32 m0, s74, 0xc00\n"
      "v_mfma_f32_16x16x32_bf16 v[184:187], a[88:91], v[224:227], v[184:187]\n"
      "ds_read_b64_tr_b16 a[188:189], v243 offset:24712\n"
      "global_load_lds_dwordx4 v240, s[78:79]\n"
      "v_mfma_f32_16x16x32_bf16 v[188:191], a[92:95], v[224:227], v[188:191]\n"
      "s_waitcnt lgkmcnt(11)\n"
      "v_mfma_f32_16x16x32_bf16 v[192:195], a[72:75], v[228:231], v[192:195]\n"
      "ds_read_b64_tr_b16 a[190:191], v243 offset:25480\n"
      "v_mfma_f32_16x16x32_bf16 v[196:199], a[76:79], v[228:231], v[196:199]\n"
      "s_add_u32 m0, s74, 0x1000\n"
      "v_mfma_f32_16x16x32_bf16 v[200:203], a[80:83], v[228:231], v[200:203]\n"
      "ds_read_b128 v[224:227], v232 offset:8192\n"
      "global_load_lds_dwordx4 v241, s[78:79]\n"
      "v_mfma_f32_16x16x32_bf16 v[204:207], a[84:87], v[228:231], v[204:207]\n"
      "v_mfma_f32_16x16x32_bf16 v[208:211], a[88:91], v[228:231], v[208:211]\n"
      "v_mfma_f32_16x16x32_bf16 v[212:215], a[92:95], v[228:231], v[212:215]\n"
      "s_add_u32 m0, s74, 0x1400\n"
      "s_waitcnt lgkmcnt(9)\n"
      "v_mfma_f32_16x16x32_bf16 v[0:3], a[96:99], v[216:219], v[0:3]\n"
      "global_load_lds_dwordx4 v242, s[78:79]\n"
      "v_mfma_f32_16x16x32_bf16 v[4:7], a[100:103], v[216:219], v[4:7]\n"
      "s_barrier\n"
      "v_mfma_f32_16x16x32_bf16 v[8:11], a[104:107], v[216:219], v[8:11]\n"
      "ds_read_b128 v[228:231], v232 offset:12288\n"
      "v_mfma_f32_16x16x32_bf16 v[12:15], a[108:111], v[216:219], v[12:15]\n"
      "s_add_u32 s58, s24, 0x0\n"
      "s_addc_u32 s59, s25, 0\n"
      "global_load_dwordx4 a[240:243], v239, s[58:59] offset:0 sc1\n"
      "v_mfma_f32_16x16x32_bf16 v[16:19], a[112:115], v[216:219], v[16:19]\n"
      "v_mfma_f32_16x16x32_bf16 v[20:23], a[116:119], v[216:219], v[20:23]\n"
      "s_waitcnt lgkmcnt(5)\n"
      "v_mfma_f32_16x16x32_bf16 v[24:27], a[96:99], v[220:223], v[24:27]\n"
      "v_mfma_f32_16x16x32_bf16 v[28:31], a[100:103], v[220:223], v[28:31]\n"
      "s_waitcnt lgkmcnt(2)\n"
      "s_add_u32 s78, s64, 0x3c000\n"
      "s_addc_u32 s79, s65, 0\n"
      "s_add_u32 m0, s74, 0x6000\n"
      "v_mfma_f32_16x16x32_bf16 v[32:35], a[104:107], v[220:223], v[32:35]\n"
      "ds_read_b128 v[216:219], v232 offset:16384\n"
      "LPD1:\n"
      "s_cbranch_execz LPD8\n"
      "global_load_lds_dwordx4 v240, s[78:79]\n"
      "v_mfma_f32_16x16x32_bf16 v[36:39], a[108:111], v[220:223], v[36:39]\n"
      "v_mfma_f32_16x16x32_bf16 v[40:43], a[112:115], v[220:223], v[40:43]\n"
      "s_waitcnt vmcnt(8)\n"
      "v_mfma_f32_16x16x32_bf16 v[44:47], a[116:119], v[220:223], v[44:47]\n"
      "s_add_u32 m0, s74, 0x6400\n"
      "s_waitcnt lgkmcnt(2)\n"
      "v_mfma_f32_16x16x32_bf16 v[48:51], a[96:99], v[224:227], v[48:51]\n"
      "ds_read_b64_tr_b16 a[192:193], v243 offset:49152\n"
      "global_load_lds_dwordx4 v241, s[78:79]\n"
      "v_mfma_f32_16x16x32_bf16 v[52:55], a[100:103], v[224:227], v[52:55]\n"
      "ds_read_b64_tr_b16 a[194:195], v243 offset:49920\n"
      "v_mfma_f32_16x16x32_bf16 v[56:59], a[104:107], v[224:227], v[56:59]\n"
      "ds_read_b128 v[220:223], v232 offset:20480\n"
      "v_mfma_f32_16x16x32_bf16 v[60:63], a[108:111], v[224:227], v[60:63]\n"
      "ds_read_b64_tr_b16 a[196:197], v243 offset:49160\n"
      "s_add_u32 m0, s74, 0x6800\n"
      "v_mfma_f32_16x16x32_bf16 v[64:67], a[112:115], v[224:227], v[64:67]\n"
      "ds_read_b64_tr_b16 a[198:199], v243 offset:49928\n"
      "global_load_lds_dwordx4 v242, s[78:79]\n"
      "v_mfma_f32_16x16x32_bf16 v[68:71], a[116:119], v[224:227], v[68:71]\n"
      "s_waitcnt lgkmcnt(6)\n"
      "v_mfma_f32_16x16x32_bf16 v[72:75], a[96:99], v[228:231], v[72:75]\n"
      "ds_read_b64_tr_b16 a[200:201], v243 offset:49216\n"
      "v_mfma_f32_16x16x32_bf16 v[76:79], a[100:103], v[228:231], v[76:79]\n"
      "ds_read_b64_tr_b16 a[202:203], v243 offset:49984\n"
      "s_add_u32 s78, s64, 0x3f000\n"
      "s_addc_u32 s79, s65, 0\n"
      "s_add_u32 m0, s74, 0x6c00\n"
      "v_mfma_f32_16x16x32_bf16 v[80:83], a[104:107], v[228:231], v[80:83]\n"
      "ds_read_b128 v[224:227], v232 offset:24576\n"
      "global_load_lds_dwordx4 v240, s[78:79]\n"
      "v_mfma_f32_16x16x32_bf16 v[84:87], a[108:111], v[228:231], v[84:87]\n"
      "ds_read_b64_tr_b16 a[204:205], v243 offset:49224\n"
      "v_mfma_f32_16x16x32_bf16 v[88:91], a[112:115], v[228:231], v[88:91]\n"
      "ds_read_b64_tr_b16 a[206:207], v243 offset:49992\n"
      "v_mfma_f32_16x16x32_bf16 v[92:95], a[116:119], v[228:231], v[92:95]\n"
      "s_add_u32 m0, s74, 0x7000\n"
      "s_waitcnt lgkmcnt(10)\n"
      "v_mfma_f32_16x16x32_bf16 v[96:99], a[96:99], v[216:219], v[96:99]\n"
      "ds_read_b64_tr_b16 a[208:209], v243 offset:49280\n"
      "global_load_lds_dwordx4 v241, s[78:79]\n"
      "v_mfma_f32_16x16x32_bf16 v[100:103], a[100:103], v[216:219], v[100:103]\n"
      "ds_read_b64_tr_b16 a[210:211], v243 offset:50048\n"
      "v_mfma_f32_16x16x32_bf16 v[104:107], a[104:107], v[216:219], v[104:107]\n"
      "ds_read_b128 v[228:231], v232 offset:28672\n"
      "v_mfma_f32_16x16x32_bf16 v[108:111], a[108:111], v[216:219], v[108:111]\n"
      "ds_read_b64_tr_b16 a[212:213], v243 offset:49288\n"
      "s_add_u32 m0, s74, 0x7400\n"
      "v_mfma_f32_16x16x32_bf16 v[112:115], a[112:115], v[216:219], v[112:115]\n"
      "ds_read_b64_tr_b16 a[214:215], v243 offset:50056\n"
      "global_load_lds_dwordx4 v242, s[78:79]\n"
      "v_mfma_f32_16x16x32_bf16 v[116:119], a[116:119], v[216:219], v[116:119]\n"
      "s_waitcnt lgkmcnt(12)\n"
      "v_mfma_f32_16x16x32_bf16 v[120:123], a[96:99], v[220:223], v[120:123]\n"
      "v_mfma_f32_16x16x32_bf16 v[124:127], a[100:103], v[220:223], v[124:127]\n"
      "global_load_dwordx4 a[244:247], v239, s[58:59] offset:1024 sc1\n"
      "v_mfma_f32_16x16x32_bf16 v[128:131], a[104:107], v[220:223], v[128:131]\n"
      "ds_read_b128 v[216:219], v232 offset:32768\n"
      "v_add_u32 v232, 0xffff7000, v232\n"
      "v_mfma_f32_16x16x32_bf16 v[132:135], a[108:111], v[220:223], v[132:135]\n"
      "v_mfma_f32_16x16x32_bf16 v[136:139], a[112:115], v[220:223], v[136:139]\n"
      "v_mfma_f32_16x16x32_bf16 v[140:143], a[116:119], v[220:223], v[140:143]\n"
      "global_load_dwordx4 a[248:251], v239, s[58:59] offset:2048 sc1\n"
      "s_waitcnt lgkmcnt(8)\n"
      "v_mfma_f32_16x16x32_bf16 v[144:147], a[96:99], v[224:227], v[144:147]\n"
      "s_waitcnt vmcnt(9)\n"
      "v_mfma_f32_16x16x32_bf16 v[148:151], a[100:103], v[224:227], v[148:151]\n"
      "ds_read_b64_tr_b16 a[216:217], v243 offset:0\n"
      "v_mfma_f32_16x16x32_bf16 v[152:155], a[104:107], v[224:227], v[152:155]\n"
      "ds_read_b128 v[220:223], v233 offset:0\n"
      "v_mfma_f32_16x16x32_bf16 v[156:159], a[108:111], v[224:227], v[156:159]\n"
      "ds_read_b64_tr_b16 a[218:219], v243 offset:768\n"
      "s_waitcnt lgkmcnt(4)\n"
      "s_add_u32 s78, s64, 0x42000\n"
      "s_addc_u32 s79, s65, 0\n"
      "s_add_u32 m0, s74, 0xc000\n"
      "v_mfma_f32_16x16x32_bf16 v[160:163], a[112:115], v[224:227], v[160:163]\n"
      "ds_read_b64_tr_b16 a[220:221], v243 offset:8\n"
      "global_load_lds_dwordx4 v240, s[78:79]\n"
      "v_mfma_f32_16x16x32_bf16 v[164:167], a[116:119], v[224:227], v[164:167]\n"
      "v_mfma_f32_16x16x32_bf16 v[168:171], a[96:99], v[228:231], v[168:171]\n"
      "ds_read_b64_tr_b16 a[222:223], v243 offset:776\n"
      "v_mfma_f32_16x16x32_bf16 v[172:175], a[100:103], v[228:231], v[172:175]\n"
      "ds_read_b64_tr_b16 a[224:225], v243 offset:64\n"
      "s_add_u32 m0, s74, 0xc400\n"
      "v_mfma_f32_16x16x32_bf16 v[176:179], a[104:107], v[228:231], v[176:179]\n"
      "ds_read_b128 v[224:227], v233 offset:4096\n"
      "global_load_lds_dwordx4 v241, s[78:79]\n"
      "v_mfma_f32_16x16x32_bf16 v[180:183], a[108:111], v[228:231], v[180:183]\n"
      "ds_read_b64_tr_b16 a[226:227], v243 offset:832\n"
      "v_mfma_f32_16x16x32_bf16 v[184:187], a[112:115], v[228:231], v[184:187]\n"
      "ds_read_b64_tr_b16 a[228:229], v243 offset:72\n"
      "v_mfma_f32_16x16x32_bf16 v[188:191], a[116:119], v[228:231], v[188:191]\n"
      "s_add_u32 m0, s74, 0xc800\n"
      "s_waitcnt lgkmcnt(9)\n"
      "v_mfma_f32_16x16x32_bf16 v[192:195], a[96:99], v[216:219], v[192:195]\n"
      "ds_read_b64_tr_b16 a[230:231], v243 offset:840\n"
      "global_load_lds_dwordx4 v242, s[78:79]\n"
      "v_mfma_f32_16x16x32_bf16 v[196:199], a[100:103], v[216:219], v[196:199]\n"
      "ds_read_b64_tr_b16 a[232:233], v243 offset:128\n"
      "v_mfma_f32_16x16x32_bf16 v[200:203], a[104:107], v[216:219], v[200:203]\n"
      "ds_read_b128 v[228:231], v233 offset:8192\n"
      "v_mfma_f32_16x16x32_bf16 v[204:207], a[108:111], v[216:219], v[204:207]\n"
      "ds_read_b64_tr_b16 a[234:235], v243 offset:896\n"
      "s_add_u32 s78, s64, 0x45000\n"
      "s_addc_u32 s79, s65, 0\n"
      "s_add_u32 m0, s74, 0xcc00\n"
      "v_mfma_f32_16x16x32_bf16 v[208:211], a[112:115], v[216:219], v[208:211]\n"
      "ds_read_b64_tr_b16 a[236:237], v243 offset:136\n"
      "global_load_lds_dwordx4 v240, s[78:79]\n"
      "v_mfma_f32_16x16x32_bf16 v[212:215], a[116:119], v[216:219], v[212:215]\n"
      "s_waitcnt lgkmcnt(12)\n"
      "v_mfma_f32_16x16x32_bf16 v[0:3], a[120:123], v[220:223], v[0:3]\n"
      "ds_read_b64_tr_b16 a[238:239], v243 offset:904\n"
      "v_mfma_f32_16x16x32_bf16 v[4:7], a[124:127], v[220:223], v[4:7]\n"
      "s_add_u32 m0, s74, 0xd000\n"
      "v_mfma_f32_16x16x32_bf16 v[8:11], a[128:131], v[220:223], v[8:11]\n"
      "ds_read_b128 v[216:219], v233 offset:12288\n"
      "global_load_lds_dwordx4 v241, s[78:79]\n"
      "v_mfma_f32_16x16x32_bf16 v[12:15], a[132:135], v[220:223], v[12:15]\n"
      "v_mfma_f32_16x16x32_bf16 v[16:19], a[136:139], v[220:223], v[16:19]\n"
      "v_mfma_f32_16x16x32_bf16 v[20:23], a[140:143], v[220:223], v[20:23]\n"
      "s_add_u32 m0, s74, 0xd400\n"
      "s_waitcnt lgkmcnt(9)\n"
      "v_mfma_f32_16x16x32_bf16 v[24:27], a[120:123], v[224:227], v[24:27]\n"
      "global_load_lds_dwordx4 v242, s[78:79]\n"
      "v_mfma_f32_16x16x32_bf16 v[28:31], a[124:127], v[224:227], v[28:31]\n"
      "v_mfma_f32_16x16x32_bf16 v[32:35], a[128:131], v[224:227], v[32:35]\n"
      "ds_read_b128 v[220:223], v233 offset:16384\n"
      "v_mfma_f32_16x16x32_bf16 v[36:39], a[132:135], v[224:227], v[36:39]\n"
      "s_waitcnt vmcnt(8)\n"
      "v_mad_i32_i24 v238, v251, s89, v238\n"
      "s_add_u32 s56, s22, 0x0\n"
      "s_addc_u32 s57, s23, 0\n"
      "s_add_u32 m0, s18, 0xfffffe00\n"
      "v_mfma_f32_16x16x32_bf16 v[40:43], a[136:139], v[224:227], v[40:43]\n"
      "ds_read_b64_tr_b16 a[0:1], v243 offset:24576\n"
      "global_load_lds_dwordx4 v238, s[56:57] offset:512 \n"
      "v_mfma_f32_16x16x32_bf16 v[44:47], a[140:143], v[224:227], v[44:47]\n"
      "s_waitcnt lgkmcnt(6)\n"
      "v_mfma_f32_16x16x32_bf16 v[48:51], a[120:123], v[228:231], v[48:51]\n"
      "ds_read_b64_tr_b16 a[2:3], v243 offset:25344\n"
      "v_mad_i32_i24 v238, v251, s90, v238\n"
      "s_add_u32 s56, s22, 0x6000\n"
      "s_addc_u32 s57, s23, 0\n"
      "s_add_u32 m0, s18, 0xe00\n"
      "v_mfma_f32_16x16x32_bf16 v[52:55], a[124:127], v[228:231], v[52:55]\n"
      "ds_read_b64_tr_b16 a[4:5], v243 offset:24584\n"
      "global_load_lds_dwordx4 v238, s[56:57] offset:512 \n"
      "v_mfma_f32_16x16x32_bf16 v[56:59], a[128:131], v[228:231], v[56:59]\n"
      "ds_read_b128 v[224:227], v233 offset:20480\n"
      "v_mfma_f32_16x16x32_bf16 v[60:63], a[132:135], v[228:231], v[60:63]\n"
      "ds_read_b64_tr_b16 a[6:7], v243 offset:25352\n"
      "v_mad_i32_i24 v238, v251, s91, v238\n"
      "s_add_u32 s56, s22, 0xc000\n"
      "s_addc_u32 s57, s23, 0\n"
      "s_add_u32 m0, s18, 0x1e00\n"
      "v_mfma_f32_16x16x32_bf16 v[64:67], a[136:139], v[228:231], v[64:67]\n"
      "ds_read_b64_tr_b16 a[8:9], v243 offset:24640\n"
      "global_load_lds_dwordx4 v238, s[56:57] offset:512 \n"
      "v_mfma_f32_16x16x32_bf16 v[68:71], a[140:143], v[228:231], v[68:71]\n"
      "s_waitcnt lgkmcnt(7)\n"
      "v_mfma_f32_16x16x32_bf16 v[72:75], a[120:123], v[216:219], v[72:75]\n"
      "ds_read_b64_tr_b16 a[10:11], v243 offset:25408\n"
      "v_mad_i32_i24 v238, v251, s92, v238\n"
      "s_add_u32 s56, s22, 0x12000\n"
      "s_addc_u32 s57, s23, 0\n"
      "s_add_u32 m0, s18, 0x2e00\n"
      "v_mfma_f32_16x16x32_bf16 v[76:79], a[124:127], v[216:219], v[76:79]\n"
      "ds_read_b64_tr_b16 a[12:13], v243 offset:24648\n"
      "global_load_lds_dwordx4 v238, s[56:57] offset:512 \n"
      "v_mfma_f32_16x16x32_bf16 v[80:83], a[128:131], v[216:219], v[80:83]\n"
      "ds_read_b128 v[228:231], v233 offset:24576\n"
      "v_mfma_f32_16x16x32_bf16 v[84:87], a[132:135], v[216:219], v[84:87]\n"
      "ds_read_b64_tr_b16 a[14:15], v243 offset:25416\n"
      "v_mad_i32_i24 v238, v251, s93, v238\n"
      "s_add_u32 s56, s22, 0x18000\n"
      "s_addc_u32 s57, s23, 0\n"
      "s_add_u32 m0, s18, 0x3e00\n"
      "v_mfma_f32_16x16x32_bf16 v[88:91], a[136:139], v[216:219], v[88:91]\n"
      "ds_read_b64_tr_b16 a[16:17], v243 offset:24704\n"
      "global_load_lds_dwordx4 v238, s[56:57] offset:512 \n"
      "v_mfma_f32_16x16x32_bf16 v[92:95], a[140:143], v[216:219], v[92:95]\n"
      "s_waitcnt lgkmcnt(11)\n"
      "v_mfma_f32_16x16x32_bf16 v[96:99], a[120:123], v[220:223], v[96:99]\n"
      "ds_read_b64_tr_b16 a[18:19], v243 offset:25472\n"
      "v_mad_i32_i24 v238, v251, s94, v238\n"
      "s_add_u32 s56, s22, 0x1e000\n"
      "s_addc_u32 s57, s23, 0\n"
      "s_add_u32 m0, s18, 0x4e00\n"
      "v_mfma_f32_16x16x32_bf16 v[100:103], a[124:127], v[220:223], v[100:103]\n"
      "ds_read_b64_tr_b16 a[20:21], v243 offset:24712\n"
      "global_load_lds_dwordx4 v238, s[56:57] offset:512 \n"
      "v_mfma_f32_16x16x32_bf16 v[104:107], a[128:131], v[220:223], v[104:107]\n"
      "ds_read_b128 v[216:219], v233 offset:28672\n"
      "v_mfma_f32_16x16x32_bf16 v[108:111], a[132:135], v[220:223], v[108:111]\n"
      "ds_read_b64_tr_b16 a[22:23], v243 offset:25480\n"
      "v_mad_i32_i24 v238, v251, s95, v238\n"
      "s_add_u32 s56, s22, 0x24000\n"
      "s_addc_u32 s57, s23, 0\n"
      "s_add_u32 m0, s18, 0x5e00\n"
      "v_mfma_f32_16x16x32_bf16 v[112:115], a[136:139], v[220:223], v[112:115]\n"
      "global_load_lds_dwordx4 v238, s[56:57] offset:512 \n"
      "v_mfma_f32_16x16x32_bf16 v[116:119], a[140:143], v[220:223], v[116:119]\n"
      "s_waitcnt lgkmcnt(11)\n"
      "v_mfma_f32_16x16x32_bf16 v[120:123], a[120:123], v[224:227], v[120:123]\n"
      "v_mad_i32_i24 v238, v251, s96, v238\n"
      "s_add_u32 s56, s22, 0x2a000\n"
      "s_addc_u32 s57, s23, 0\n"
      "s_add_u32 m0, s18, 0x6e00\n"
      "v_mfma_f32_16x16x32_bf16 v[124:127], a[124:127], v[224:227], v[124:127]\n"
      "global_load_lds_dwordx4 v238, s[56:57] offset:512 \n"
      "v_mfma_f32_16x16x32_bf16 v[128:131], a[128:131], v[224:227], v[128:131]\n"
      "ds_read_b128 v[220:223], v233 offset:32768\n"
      "v_add_u32 v233, 0xffff7000, v233\n"
      "v_mfma_f32_16x16x32_bf16 v[132:135], a[132:135], v[224:227], v[132:135]\n"
      "v_mad_i32_i24 v238, v251, s97, v238\n"
      "s_add_u32 s56, s22, 0x30000\n"
      "s_addc_u32 s57, s23, 0\n"
      "s_add_u32 m0, s18, 0x7e00\n"
      "v_mfma_f32_16x16x32_bf16 v[136:139], a[136:139], v[224:227], v[136:139]\n"
      "global_load_lds_dwordx4 v238, s[56:57] offset:512 \n"
      "v_mfma_f32_16x16x32_bf16 v[140:143], a[140:143], v[224:227], v[140:143]\n"
      "s_waitcnt lgkmcnt(7)\n"
      "v_mfma_f32_16x16x32_bf16 v[144:147], a[120:123], v[228:231], v[144:147]\n"
      "v_mfma_f32_16x16x32_bf16 v[148:151], a[124:127], v[228:231], v[148:151]\n"
      "global_load_dwordx4 a[252:255], v239, s[58:59] offset:3072 sc1\n"
      "v_mfma_f32_16x16x32_bf16 v[152:155], a[128:131], v[228:231], v[152:155]\n"
      "ds_read_b128 v[224:227], v234 offset:0\n"
      "v_mfma_f32_16x16x32_bf16 v[156:159], a[132:135], v[228:231], v[156:159]\n"
      "v_mfma_f32_16x16x32_bf16 v[160:163], a[136:139], v[228:231], v[160:163]\n"
      "v_mfma_f32_16x16x32_bf16 v[164:167], a[140:143], v[228:231], v[164:167]\n"
      "s_add_u32 s58, s24, 0x1000\n"
      "s_addc_u32 s59, s25, 0\n"
      "global_load_dwordx4 a[48:51], v239, s[58:59] offset:0 sc1\n"
      "s_waitcnt lgkmcnt(3)\n"
      "v_mfma_f32_16x16x32_bf16 v[168:171], a[120:123], v[216:219], v[168:171]\n"
      "s_waitcnt vmcnt(11)\n"
      "v_mfma_f32_16x16x32_bf16 v[172:175], a[124:127], v[216:219], v[172:175]\n"
      "ds_read_b64_tr_b16 a[24:25], v243 offset:49152\n"
      "v_mfma_f32_16x16x32_bf16 v[176:179], a[128:131], v[216:219], v[176:179]\n"
      "ds_read_b128 v[228:231], v234 offset:4096\n"
      "v_mfma_f32_16x16x32_bf16 v[180:183], a[132:135], v[216:219], v[180:183]\n"
      "ds_read_b64_tr_b16 a[26:27], v243 offset:49920\n"
      "global_load_dwordx4 a[52:55], v239, s[58:59] offset:1024 sc1\n"
      "v_mfma_f32_16x16x32_bf16 v[184:187], a[136:139], v[216:219], v[184:187]\n"
      "ds_read_b64_tr_b16 a[28:29], v243 offset:49160\n"
      "v_mfma_f32_16x16x32_bf16 v[188:191], a[140:143], v[216:219], v[188:191]\n"
      "s_waitcnt lgkmcnt(5)\n"
      "v_mfma_f32_16x16x32_bf16 v[192:195], a[120:123], v[220:223], v[192:195]\n"
      "ds_read_b64_tr_b16 a[30:31], v243 offset:49928\n"
      "v_mfma_f32_16x16x32_bf16 v[196:199], a[124:127], v[220:223], v[196:199]\n"
      "ds_read_b64_tr_b16 a[32:33], v243 offset:49216\n"
      "global_load_dwordx4 a[56:59], v239, s[58:59] offset:2048 sc1\n"
      "v_mfma_f32_16x16x32_bf16 v[200:203], a[128:131], v[220:223], v[200:203]\n"
      "ds_read_b128 v[216:219], v234 offset:8192\n"
      "v_mfma_f32_16x16x32_bf16 v[204:207], a[132:135], v[220:223], v[204:207]\n"
      "ds_read_b64_tr_b16 a[34:35], v243 offset:49984\n"
      "v_mfma_f32_16x16x32_bf16 v[208:211], a[136:139], v[220:223], v[208:211]\n"
      "ds_read_b64_tr_b16 a[36:37], v243 offset:49224\n"
      "v_mfma_f32_16x16x32_bf16 v[212:215], a[140:143], v[220:223], v[212:215]\n"
      "global_load_dwordx4 a[60:63], v239, s[58:59] offset:3072 sc1\n"
      "s_waitcnt lgkmcnt(9)\n"
      "v_mfma_f32_16x16x32_bf16 v[0:3], a[144:147], v[224:227], v[0:3]\n"
      "ds_read_b64_tr_b16 a[38:39], v243 offset:49992\n"
      "v_mfma_f32_16x16x32_bf16 v[4:7], a[148:151], v[224:227], v[4:7]\n"
      "ds_read_b64_tr_b16 a[40:41], v243 offset:49280\n"
      "v_mfma_f32_16x16x32_bf16 v[8:11], a[152:155], v[224:227], v[8:11]\n"
      "ds_read_b128 v[220:223], v234 offset:12288\n"
      "v_mfma_f32_16x16x32_bf16 v[12:15], a[156:159], v[224:227], v[12:15]\n"
      "ds_read_b64_tr_b16 a[42:43], v243 offset:50048\n"
      "s_add_u32 s58, s24, 0x2000\n"
      "s_addc_u32 s59, s25, 0\n"
      "global_load_dwordx4 a[64:67], v239, s[58:59] offset:0 sc1\n"
      "v_mfma_f32_16x16x32_bf16 v[16:19], a[160:163], v[224:227], v[16:19]\n"
      "ds_read_b64_tr_b16 a[44:45], v243 offset:49288\n"
      "v_mfma_f32_16x16x32_bf16 v[20:23], a[164:167], v[224:227], v[20:23]\n"
      "s_waitcnt lgkmcnt(12)\n"
      "v_mfma_f32_16x16x32_bf16 v[24:27], a[144:147], v[228:231], v[24:27]\n"
      "ds_read_b64_tr_b16 a[46:47], v243 offset:50056\n"
      "v_mfma_f32_16x16x32_bf16 v[28:31], a[148:151], v[228:231], v[28:31]\n"
      "global_load_dwordx4 a[68:71], v239, s[58:59] offset:1024 sc1\n"
      "v_mfma_f32_16x16x32_bf16 v[32:35], a[152:155], v[228:231], v[32:35]\n"
      "ds_read_b128 v[224:227], v234 offset:16384\n"
      "v_mfma_f32_16x16x32_bf16 v[36:39], a[156:159], v[228:231], v[36:39]\n"
      "v_mfma_f32_16x16x32_bf16 v[40:43], a[160:163], v[228:231], v[40:43]\n"
      "v_mfma_f32_16x16x32_bf16 v[44:47], a[164:167], v[228:231], v[44:47]\n"
      "global_load_dwordx4 a[72:75], v239, s[58:59] offset:2048 sc1\n"
      "s_waitcnt lgkmcnt(9)\n"
      "v_mfma_f32_16x16x32_bf16 v[48:51], a[144:147], v[216:219], v[48:51]\n"
      "v_mfma_f32_16x16x32_bf16 v[52:55], a[148:151], v[216:219], v[52:55]\n"
      "v_mfma_f32_16x16x32_bf16 v[56:59], a[152:155], v[216:219], v[56:59]\n"
      "ds_read_b128 v[228:231], v234 offset:20480\n"
      "v_mfma_f32_16x16x32_bf16 v[60:63], a[156:159], v[216:219], v[60:63]\n"
      "global_load_dwordx4 a[76:79], v239, s[58:59] offset:3072 sc1\n"
      "v_mfma_f32_16x16x32_bf16 v[64:67], a[160:163], v[216:219], v[64:67]\n"
      "v_mfma_f32_16x16x32_bf16 v[68:71], a[164:167], v[216:219], v[68:71]\n"
      "s_waitcnt lgkmcnt(5)\n"
      "v_mfma_f32_16x16x32_bf16 v[72:75], a[144:147], v[220:223], v[72:75]\n"
      "v_mfma_f32_16x16x32_bf16 v[76:79], a[148:151], v[220:223], v[76:79]\n"
      "s_add_u32 s58, s24, 0x3000\n"
      "s_addc_u32 s59, s25, 0\n"
      "global_load_dwordx4 a[80:83], v239, s[58:59] offset:0 sc1\n"
      "v_mfma_f32_16x16x32_bf16 v[80:83], a[152:155], v[220:223], v[80:83]\n"
      "ds_read_b128 v[216:219], v234 offset:24576\n"
      "v_mfma_f32_16x16x32_bf16 v[84:87], a[156:159], v[220:223], v[84:87]\n"
      "v_mfma_f32_16x16x32_bf16 v[88:91], a[160:163], v[220:223], v[88:91]\n"
      "v_mfma_f32_16x16x32_bf16 v[92:95], a[164:167], v[220:223], v[92:95]\n"
      "global_load_dwordx4 a[84:87], v239, s[58:59] offset:1024 sc1\n"
      "s_waitcnt lgkmcnt(2)\n"
      "v_mfma_f32_16x16x32_bf16 v[96:99], a[144:147], v[224:227], v[96:99]\n"
      "v_mfma_f32_16x16x32_bf16 v[100:103], a[148:151], v[224:227], v[100:103]\n"
      "v_mfma_f32_16x16x32_bf16 v[104:107], a[152:155], v[224:227], v[104:107]\n"
      "ds_read_b128 v[220:223], v234 offset:28672\n"
      "v_mfma_f32_16x16x32_bf16 v[108:111], a[156:159], v[224:227], v[108:111]\n"
      "global_load_dwordx4 a[88:91], v239, s[58:59] offset:2048 sc1\n"
      "v_mfma_f32_16x16x32_bf16 v[112:115], a[160:163], v[224:227], v[112:115]\n"
      "v_mfma_f32_16x16x32_bf16 v[116:119], a[164:167], v[224:227], v[116:119]\n"
      "s_waitcnt lgkmcnt(2)\n"
      "v_mfma_f32_16x16x32_bf16 v[120:123], a[144:147], v[228:231], v[120:123]\n"
      "v_mfma_f32_16x16x32_bf16 v[124:127], a[148:151], v[228:231], v[124:127]\n"
      "global_load_dwordx4 a[92:95], v239, s[58:59] offset:3072 sc1\n"
      "v_mfma_f32_16x16x32_bf16 v[128:131], a[152:155], v[228:231], v[128:131]\n"
      "ds_read_b128 v[224:227], v234 offset:32768\n"
      "v_add_u32 v234, 0xffff7000, v234\n"
      "v_mfma_f32_16x16x32_bf16 v[132:135], a[156:159], v[228:231], v[132:135]\n"
      "v_mfma_f32_16x16x32_bf16 v[136:139], a[160:163], v[228:231], v[136:139]\n"
      "v_mfma_f32_16x16x32_bf16 v[140:143], a[164:167], v[228:231], v[140:143]\n"
      "s_add_u32 s58, s24, 0x4000\n"
      "s_addc_u32 s59, s25, 0\n"
      "global_load_dwordx4 a[96:99], v239, s[58:59] offset:0 sc1\n"
      "s_waitcnt lgkmcnt(2)\n"
      "v_mfma_f32_16x16x32_bf16 v[144:147], a[144:147], v[216:219], v[144:147]\n"
      "ds_read_b64 v[236:237], v250 offset:4104\n"
      "v_mfma_f32_16x16x32_bf16 v[148:151], a[148:151], v[216:219], v[148:151]\n"
      "v_mfma_f32_16x16x32_bf16 v[152:155], a[152:155], v[216:219], v[152:155]\n"
      "ds_read_b128 v[228:231], v235 offset:0\n"
      "v_mfma_f32_16x16x32_bf16 v[156:159], a[156:159], v[216:219], v[156:159]\n"
      "global_load_dwordx4 a[100:103], v239, s[58:59] offset:1024 sc1\n"
      "v_mfma_f32_16x16x32_bf16 v[160:163], a[160:163], v[216:219], v[160:163]\n"
      "v_mfma_f32_16x16x32_bf16 v[164:167], a[164:167], v[216:219], v[164:167]\n"
      "s_waitcnt lgkmcnt(3)\n"
      "v_mfma_f32_16x16x32_bf16 v[168:171], a[144:147], v[220:223], v[168:171]\n"
      "v_mfma_f32_16x16x32_bf16 v[172:175], a[148:151], v[220:223], v[172:175]\n"
      "global_load_dwordx4 a[104:107], v239, s[58:59] offset:2048 sc1\n"
      "v_mfma_f32_16x16x32_bf16 v[176:179], a[152:155], v[220:223], v[176:179]\n"
      "ds_read_b128 v[216:219], v235 offset:4096\n"
      "v_mfma_f32_16x16x32_bf16 v[180:183], a[156:159], v[220:223], v[180:183]\n"
      "v_mfma_f32_16x16x32_bf16 v[184:187], a[160:163], v[220:223], v[184:187]\n"
      "v_mfma_f32_16x16x32_bf16 v[188:191], a[164:167], v[220:223], v[188:191]\n"
      "global_load_dwordx4 a[108:111], v239, s[58:59] offset:3072 sc1\n"
      "s_waitcnt lgkmcnt(3)\n"
      "v_mfma_f32_16x16x32_bf16 v[192:195], a[144:147], v[224:227], v[192:195]\n"
      "v_mfma_f32_16x16x32_bf16 v[196:199], a[148:151], v[224:227], v[196:199]\n"
      "v_mfma_f32_16x16x32_bf16 v[200:203], a[152:155], v[224:227], v[200:203]\n"
      "ds_read_b128 v[220:223], v235 offset:8192\n"
      "v_mfma_f32_16x16x32_bf16 v[204:207], a[156:159], v[224:227], v[204:207]\n"
      "s_add_u32 s58, s24, 0x5000\n"
      "s_addc_u32 s59, s25, 0\n"
      "global_load_dwordx4 a[112:115], v239, s[58:59] offset:0 sc1\n"
      "v_mfma_f32_16x16x32_bf16 v[208:211], a[160:163], v[224:227], v[208:211]\n"
      "v_mfma_f32_16x16x32_bf16 v[212:215], a[164:167], v[224:227], v[212:215]\n"
      "s_waitcnt lgkmcnt(2)\n"
      "v_mfma_f32_16x16x32_bf16 v[0:3], a[168:171], v[228:231], v[0:3]\n"
      "v_mfma_f32_16x16x32_bf16 v[4:7], a[172:175], v[228:231], v[4:7]\n"
      "global_load_dwordx4 a[116:119], v239, s[58:59] offset:1024 sc1\n"
      "v_mfma_f32_16x16x32_bf16 v[8:11], a[176:179], v[228:231], v[8:11]\n"
      "ds_read_b128 v[224:227], v235 offset:12288\n"
      "v_mfma_f32_16x16x32_bf16 v[12:15], a[180:183], v[228:231], v[12:15]\n"
      "v_mfma_f32_16x16x32_bf16 v[16:19], a[184:187], v[228:231], v[16:19]\n"
      "v_mfma_f32_16x16x32_bf16 v[20:23], a[188:191], v[228:231], v[20:23]\n"
      "global_load_dwordx4 a[120:123], v239, s[58:59] offset:2048 sc1\n"
      "s_waitcnt lgkmcnt(2)\n"
      "v_mfma_f32_16x16x32_bf16 v[24:27], a[168:171], v[216:219], v[24:27]\n"
      "v_mfma_f32_16x16x32_bf16 v[28:31], a[172:175], v[216:219], v[28:31]\n"
      "v_mfma_f32_16x16x32_bf16 v[32:35], a[176:179], v[216:219], v[32:35]\n"
      "ds_read_b128 v[228:231], v235 offset:16384\n"
      "v_mfma_f32_16x16x32_bf16 v[36:39], a[180:183], v[216:219], v[36:39]\n"
      "global_load_dwordx4 a[124:127], v239, s[58:59] offset:3072 sc1\n"
      "v_mfma_f32_16x16x32_bf16 v[40:43], a[184:187], v[216:219], v[40:43]\n"
      "v_mfma_f32_16x16x32_bf16 v[44:47], a[188:191], v[216:219], v[44:47]\n"
      "s_waitcnt lgkmcnt(2)\n"
      "v_mfma_f32_16x16x32_bf16 v[48:51], a[168:171], v[220:223], v[48:51]\n"
      "v_mfma_f32_16x16x32_bf16 v[52:55], a[172:175], v[220:223], v[52:55]\n"
      "s_add_u32 s58, s24, 0x6000\n"
      "s_addc_u32 s59, s25, 0\n"
      "global_load_dwordx4 a[128:131], v239, s[58:59] offset:0 sc1\n"
      "v_mfma_f32_16x16x32_bf16 v[56:59], a[176:179], v[220:223], v[56:59]\n"
      "ds_read_b128 v[216:219], v235 offset:20480\n"
      "v_mfma_f32_16x16x32_bf16 v[60:63], a[180:183], v[220:223], v[60:63]\n"
      "v_mfma_f32_16x16x32_bf16 v[64:67], a[184:187], v[220:223], v[64:67]\n"
      "v_mfma_f32_16x16x32_bf16 v[68:71], a[188:191], v[220:223], v[68:71]\n"
      "global_load_dwordx4 a[132:135], v239, s[58:59] offset:1024 sc1\n"
      "s_waitcnt lgkmcnt(2)\n"
      "v_mfma_f32_16x16x32_bf16 v[72:75], a[168:171], v[224:227], v[72:75]\n"
      "v_mfma_f32_16x16x32_bf16 v[76:79], a[172:175], v[224:227], v[76:79]\n"
      "v_mfma_f32_16x16x32_bf16 v[80:83], a[176:179], v[224:227], v[80:83]\n"
      "ds_read_b128 v[220:223], v235 offset:24576\n"
      "v_mfma_f32_16x16x32_bf16 v[84:87], a[180:183], v[224:227], v[84:87]\n"
      "global_load_dwordx4 a[136:139], v239, s[58:59] offset:2048 sc1\n"
      "v_mfma_f32_16x16x32_bf16 v[88:91], a[184:187], v[224:227], v[88:91]\n"
      "v_mfma_f32_16x16x32_bf16 v[92:95], a[188:191], v[224:227], v[92:95]\n"
      "s_waitcnt lgkmcnt(2)\n"
      "v_mfma_f32_16x16x32_bf16 v[96:99], a[168:171], v[228:231], v[96:99]\n"
      "v_mfma_f32_16x16x32_bf16 v[100:103], a[172:175], v[228:231], v[100:103]\n"
      "global_load_dwordx4 a[140:143], v239, s[58:59] offset:3072 sc1\n"
      "v_mfma_f32_16x16x32_bf16 v[104:107], a[176:179], v[228:231], v[104:107]\n"
      "ds_read_b128 v[224:227], v235 offset:28672\n"
      "v_mfma_f32_16x16x32_bf16 v[108:111], a[180:183], v[228:231], v[108:111]\n"
      "v_mfma_f32_16x16x32_bf16 v[112:115], a[184:187], v[228:231], v[112:115]\n"
      "v_mfma_f32_16x16x32_bf16 v[116:119], a[188:191], v[228:231], v[116:119]\n"
      "s_add_u32 s58, s24, 0x7000\n"
      "s_addc_u32 s59, s25, 0\n"
      "global_load_dwordx4 a[144:147], v239, s[58:59] offset:0 sc1\n"
      "s_waitcnt lgkmcnt(2)\n"
      "v_mfma_f32_16x16x32_bf16 v[120:123], a[168:171], v[216:219], v[120:123]\n"
      "v_mfma_f32_16x16x32_bf16 v[124:127], a[172:175], v[216:219], v[124:127]\n"
      "v_mfma_f32_16x16x32_bf16 v[128:131], a[176:179], v[216:219], v[128:131]\n"
      "ds_read_b128 v[228:231], v235 offset:32768\n"
      "v_add_u32 v235, 0xffff7000, v235\n"
      "v_mfma_f32_16x16x32_bf16 v[132:135], a[180:183], v[216:219], v[132:135]\n"
      "global_load_dwordx4 a[148:151], v239, s[58:59] offset:1024 sc1\n"
      "v_mfma_f32_16x16x32_bf16 v[136:139], a[184:187], v[216:219], v[136:139]\n"
      "v_mfma_f32_16x16x32_bf16 v[140:143], a[188:191], v[216:219], v[140:143]\n"
      "s_waitcnt lgkmcnt(2)\n"
      "v_mfma_f32_16x16x32_bf16 v[144:147], a[168:171], v[220:223], v[144:147]\n"
      "v_mfma_f32_16x16x32_bf16 v[148:151], a[172:175], v[220:223], v[148:151]\n"
      "s_waitcnt vmcnt(27)\n"
      "s_barrier\n"
      "global_load_dwordx4 a[152:155], v239, s[58:59] offset:2048 sc1\n"
      "v_mfma_f32_16x16x32_bf16 v[152:155], a[176:179], v[220:223], v[152:155]\n"
      "ds_read_b128 v[216:219], v232 offset:0\n"
      "v_mfma_f32_16x16x32_bf16 v[156:159], a[180:183], v[220:223], v[156:159]\n"
      "v_mfma_f32_16x16x32_bf16 v[160:163], a[184:187], v[220:223], v[160:163]\n"
      "v_mfma_f32_16x16x32_bf16 v[164:167], a[188:191], v[220:223], v[164:167]\n"
      "global_load_dwordx4 a[156:159], v239, s[58:59] offset:3072 sc1\n"
      "s_waitcnt lgkmcnt(2)\n"
      "v_mfma_f32_16x16x32_bf16 v[168:171], a[168:171], v[224:227], v[168:171]\n"
      "v_mfma_f32_16x16x32_bf16 v[172:175], a[172:175], v[224:227], v[172:175]\n"
      "v_mfma_f32_16x16x32_bf16 v[176:179], a[176:179], v[224:227], v[176:179]\n"
      "ds_read_b128 v[220:223], v232 offset:4096\n"
      "v_mfma_f32_16x16x32_bf16 v[180:183], a[180:183], v[224:227], v[180:183]\n"
      "s_add_u32 s58, s24, 0x8000\n"
      "s_addc_u32 s59, s25, 0\n"
      "global_load_dwordx4 a[160:163], v239, s[58:59] offset:0 sc1\n"
      "v_mfma_f32_16x16x32_bf16 v[184:187], a[184:187], v[224:227], v[184:187]\n"
      "v_mfma_f32_16x16x32_bf16 v[188:191], a[188:191], v[224:227], v[188:191]\n"
      "s_waitcnt lgkmcnt(2)\n"
      "v_mfma_f32_16x16x32_bf16 v[192:195], a[168:171], v[228:231], v[192:195]\n"
      "v_mfma_f32_16x16x32_bf16 v[196:199], a[172:175], v[228:231], v[196:199]\n"
      "global_load_dwordx4 a[164:167], v239, s[58:59] offset:1024 sc1\n"
      "v_mfma_f32_16x16x32_bf16 v[200:203], a[176:179], v[228:231], v[200:203]\n"
      "ds_read_b128 v[224:227], v232 offset:8192\n"
      "v_mfma_f32_16x16x32_bf16 v[204:207], a[180:183], v[228:231], v[204:207]\n"
      "v_mfma_f32_16x16x32_bf16 v[208:211], a[184:187], v[228:231], v[208:211]\n"
      "v_mfma_f32_16x16x32_bf16 v[212:215], a[188:191], v[228:231], v[212:215]\n"
      "s_waitcnt lgkmcnt(2)\n"
      "v_mfma_f32_16x16x32_bf16 v[0:3], a[192:195], v[216:219], v[0:3]\n"
      "global_load_dwordx4 a[168:171], v239, s[58:59] offset:2048 sc1\n"
      "v_mfma_f32_16x16x32_bf16 v[4:7], a[196:199], v[216:219], v[4:7]\n"
      "v_mfma_f32_16x16x32_bf16 v[8:11], a[200:203], v[216:219], v[8:11]\n"
      "ds_read_b128 v[228:231], v232 offset:12288\n"
      "v_mfma_f32_16x16x32_bf16 v[12:15], a[204:207], v[216:219], v[12:15]\n"
      "v_mfma_f32_16x16x32_bf16 v[16:19], a[208:211], v[216:219], v[16:19]\n"
      "s_mul_i32 s78, s98, s19\n"
      "v_mfma_f32_16x16x32_bf16 v[20:23], a[212:215], v[216:219], v[20:23]\n"
      "v_add_u32 v236, s78, v236\n"
      "s_waitcnt lgkmcnt(2)\n"
      "v_mfma_f32_16x16x32_bf16 v[24:27], a[192:195], v[220:223], v[24:27]\n"
      "s_mul_i32 s78, s78, s99\n"
      "v_mfma_f32_16x16x32_bf16 v[28:31], a[196:199], v[220:223], v[28:31]\n"
      "v_add_u32 v237, s78, v237\n"
      "v_mfma_f32_16x16x32_bf16 v[32:35], a[200:203], v[220:223], v[32:35]\n"
      "ds_read_b128 v[216:219], v232 offset:16384\n"
      "s_add_u32 s52, s20, s32\n"
      "s_addc_u32 s53, s21, 0\n"
      "s_add_u32 m0, s17, 0x0\n"
      "v_mfma_f32_16x16x32_bf16 v[36:39], a[204:207], v[220:223], v[36:39]\n"
      "global_load_lds_dwordx4 v236, s[52:53] offset:0 \n"
      "v_mfma_f32_16x16x32_bf16 v[40:43], a[208:211], v[220:223], v[40:43]\n"
      "v_mfma_f32_16x16x32_bf16 v[44:47], a[212:215], v[220:223], v[44:47]\n"
      "s_add_u32 m0, s17, 0x1f00\n"
      "s_waitcnt lgkmcnt(2)\n"
      "v_mfma_f32_16x16x32_bf16 v[48:51], a[192:195], v[224:227], v[48:51]\n"
      "global_load_lds_dwordx4 v236, s[52:53] offset:256 \n"
      "v_mfma_f32_16x16x32_bf16 v[52:55], a[196:199], v[224:227], v[52:55]\n"
      "v_mfma_f32_16x16x32_bf16 v[56:59], a[200:203], v[224:227], v[56:59]\n"
      "ds_read_b128 v[220:223], v232 offset:20480\n"
      "s_add_u32 m0, s17, 0x3e00\n"
      "v_mfma_f32_16x16x32_bf16 v[60:63], a[204:207], v[224:227], v[60:63]\n"
      "global_load_lds_dwordx4 v236, s[52:53] offset:512 \n"
      "v_mfma_f32_16x16x32_bf16 v[64:67], a[208:211], v[224:227], v[64:67]\n"
      "v_mfma_f32_16x16x32_bf16 v[68:71], a[212:215], v[224:227], v[68:71]\n"
      "s_add_u32 m0, s17, 0x1000\n"
      "s_waitcnt lgkmcnt(2)\n"
      "v_mfma_f32_16x16x32_bf16 v[72:75], a[192:195], v[228:231], v[72:75]\n"
      "global_load_lds_dwordx4 v237, s[52:53] offset:0 \n"
      "v_mfma_f32_16x16x32_bf16 v[76:79], a[196:199], v[228:231], v[76:79]\n"
      "v_mfma_f32_16x16x32_bf16 v[80:83], a[200:203], v[228:231], v[80:83]\n"
      "ds_read_b128 v[224:227], v232 offset:24576\n"
      "s_add_u32 m0, s17, 0x2f00\n"
      "v_mfma_f32_16x16x32_bf16 v[84:87], a[204:207], v[228:231], v[84:87]\n"
      "global_load_lds_dwordx4 v237, s[52:53] offset:256 \n"
      "v_mfma_f32_16x16x32_bf16 v[88:91], a[208:211], v[228:231], v[88:91]\n"
      "v_mfma_f32_16x16x32_bf16 v[92:95], a[212:215], v[228:231], v[92:95]\n"
      "s_add_u32 m0, s17, 0x4e00\n"
      "s_waitcnt lgkmcnt(2)\n"
      "v_mfma_f32_16x16x32_bf16 v[96:99], a[192:195], v[216:219], v[96:99]\n"
      "global_load_lds_dwordx4 v237, s[52:53] offset:512 \n"
      "v_mfma_f32_16x16x32_bf16 v[100:103], a[196:199], v[216:219], v[100:103]\n"
      "v_mfma_f32_16x16x32_bf16 v[104:107], a[200:203], v[216:219], v[104:107]\n"
      "ds_read_b128 v[228:231], v232 offset:28672\n"
      "v_mfma_f32_16x16x32_bf16 v[108:111], a[204:207], v[216:219], v[108:111]\n"
      "v_mfma_f32_16x16x32_bf16 v[112:115], a[208:211], v[216:219], v[112:115]\n"
      "v_mfma_f32_16x16x32_bf16 v[116:119], a[212:215], v[216:219], v[116:119]\n"
      "s_waitcnt lgkmcnt(2)\n"
      "v_mfma_f32_16x16x32_bf16 v[120:123], a[192:195], v[220:223], v[120:123]\n"
      "v_mfma_f32_16x16x32_bf16 v[124:127], a[196:199], v[220:223], v[124:127]\n"
      "v_mfma_f32_16x16x32_bf16 v[128:131], a[200:203], v[220:223], v[128:131]\n"
      "ds_read_b128 v[216:219], v232 offset:32768\n"
      "v_add_u32 v232, v249, v232\n"
      "v_mfma_f32_16x16x32_bf16 v[132:135], a[204:207], v[220:223], v[132:135]\n"
      "v_mfma_f32_16x16x32_bf16 v[136:139], a[208:211], v[220:223], v[136:139]\n"
      "v_mfma_f32_16x16x32_bf16 v[140:143], a[212:215], v[220:223], v[140:143]\n"
      "s_waitcnt lgkmcnt(2)\n"
      "v_mfma_f32_16x16x32_bf16 v[144:147], a[192:195], v[224:227], v[144:147]\n"
      "v_mfma_f32_16x16x32_bf16 v[148:151], a[196:199], v[224:227], v[148:151]\n"
      "v_mfma_f32_16x16x32_bf16 v[152:155], a[200:203], v[224:227], v[152:155]\n"
      "ds_read_b128 v[220:223], v233 offset:0\n"
      "v_mfma_f32_16x16x32_bf16 v[156:159], a[204:207], v[224:227], v[156:159]\n"
      "v_mfma_f32_16x16x32_bf16 v[160:163], a[208:211], v[224:227], v[160:163]\n"
      "v_mfma_f32_16x16x32_bf16 v[164:167], a[212:215], v[224:227], v[164:167]\n"
      "s_waitcnt lgkmcnt(2)\n"
      "v_mfma_f32_16x16x32_bf16 v[168:171], a[192:195], v[228:231], v[168:171]\n"
      "v_mfma_f32_16x16x32_bf16 v[172:175], a[196:199], v[228:231], v[172:175]\n"
      "v_mfma_f32_16x16x32_bf16 v[176:179], a[200:203], v[228:231], v[176:179]\n"
      "ds_read_b128 v[224:227], v233 offset:4096\n"
      "v_mfma_f32_16x16x32_bf16 v[180:183], a[204:207], v[228:231], v[180:183]\n"
      "v_mfma_f32_16x16x32_bf16 v[184:187], a[208:211], v[228:231], v[184:187]\n"
      "v_mfma_f32_16x16x32_bf16 v[188:191], a[212:215], v[228:231], v[188:191]\n"
      "s_waitcnt lgkmcnt(2)\n"
      "v_mfma_f32_16x16x32_bf16 v[192:195], a[192:195], v[216:219], v[192:195]\n"
      "v_mfma_f32_16x16x32_bf16 v[196:199], a[196:199], v[216:219], v[196:199]\n"
      "v_mfma_f32_16x16x32_bf16 v[200:203], a[200:203], v[216:219], v[200:203]\n"
      "ds_read_b128 v[228:231], v233 offset:8192\n"
      "v_mfma_f32_16x16x32_bf16 v[204:207], a[204:207], v[216:219], v[204:207]\n"
      "v_mfma_f32_16x16x32_bf16 v[208:211], a[208:211], v[216:219], v[208:211]\n"
      "v_mfma_f32_16x16x32_bf16 v[212:215], a[212:215], v[216:219], v[212:215]\n"
      "s_waitcnt lgkmcnt(2)\n"
      "v_mfma_f32_16x16x32_bf16 v[0:3], a[216:219], v[220:223], v[0:3]\n"
      "LPD2:\n"
      "s_cbranch_execz LPD7\n"
      "v_mfma_f32_16x16x32_bf16 v[4:7], a[220:223], v[220:223], v[4:7]\n"
      "v_mfma_f32_16x16x32_bf16 v[8:11], a[224:227], v[220:223], v[8:11]\n"
      "ds_read_b128 v[216:219], v233 offset:12288\n"
      "v_mfma_f32_16x16x32_bf16 v[12:15], a[228:231], v[220:223], v[12:15]\n"
      "v_mfma_f32_16x16x32_bf16 v[16:19], a[232:235], v[220:223], v[16:19]\n"
      "s_add_u32 s52, s42, s32\n"
      "s_addc_u32 s53, s43, 0\n"
      "s_add_u32 m0, s17, 0x6000\n"
      "v_mfma_f32_16x16x32_bf16 v[20:23], a[236:239], v[220:223], v[20:23]\n"
      "global_load_lds_dwordx4 v236, s[52:53] offset:0 \n"
      "s_waitcnt lgkmcnt(2)\n"
      "v_mfma_f32_16x16x32_bf16 v[24:27], a[216:219], v[224:227], v[24:27]\n"
      "v_mfma_f32_16x16x32_bf16 v[28:31], a[220:223], v[224:227], v[28:31]\n"
      "s_add_u32 m0, s17, 0x7f00\n"
      "v_mfma_f32_16x16x32_bf16 v[32:35], a[224:227], v[224:227], v[32:35]\n"
      "ds_read_b128 v[220:223], v233 offset:16384\n"
      "global_load_lds_dwordx4 v236, s[52:53] offset:256 \n"
      "v_mfma_f32_16x16x32_bf16 v[36:39], a[228:231], v[224:227], v[36:39]\n"
      "v_mfma_f32_16x16x32_bf16 v[40:43], a[232:235], v[224:227], v[40:43]\n"
      "s_add_u32 m0, s17, 0x9e00\n"
      "v_mfma_f32_16x16x32_bf16 v[44:47], a[236:239], v[224:227], v[44:47]\n"
      "global_load_lds_dwordx4 v236, s[52:53] offset:512 \n"
      "s_waitcnt lgkmcnt(2)\n"
      "v_mfma_f32_16x16x32_bf16 v[48:51], a[216:219], v[228:231], v[48:51]\n"
      "v_mfma_f32_16x16x32_bf16 v[52:55], a[220:223], v[228:231], v[52:55]\n"
      "s_add_u32 m0, s17, 0x7000\n"
      "v_mfma_f32_16x16x32_bf16 v[56:59], a[224:227], v[228:231], v[56:59]\n"
      "ds_read_b128 v[224:227], v233 offset:20480\n"
      "global_load_lds_dwordx4 v237, s[52:53] offset:0 \n"
      "v_mfma_f32_16x16x32_bf16 v[60:63], a[228:231], v[228:231], v[60:63]\n"
      "v_mfma_f32_16x16x32_bf16 v[64:67], a[232:235], v[228:231], v[64:67]\n"
      "s_add_u32 m0, s17, 0x8f00\n"
      "v_mfma_f32_16x16x32_bf16 v[68:71], a[236:239], v[228:231], v[68:71]\n"
      "global_load_lds_dwordx4 v237, s[52:53] offset:256 \n"
      "s_waitcnt lgkmcnt(2)\n"
      "v_mfma_f32_16x16x32_bf16 v[72:75], a[216:219], v[216:219], v[72:75]\n"
      "v_mfma_f32_16x16x32_bf16 v[76:79], a[220:223], v[216:219], v[76:79]\n"
      "s_add_u32 m0, s17, 0xae00\n"
      "v_mfma_f32_16x16x32_bf16 v[80:83], a[224:227], v[216:219], v[80:83]\n"
      "ds_read_b128 v[228:231], v233 offset:24576\n"
      "global_load_lds_dwordx4 v237, s[52:53] offset:512 \n"
      "v_mfma_f32_16x16x32_bf16 v[84:87], a[228:231], v[216:219], v[84:87]\n"
      "v_mfma_f32_16x16x32_bf16 v[88:91], a[232:235], v[216:219], v[88:91]\n"
      "v_mfma_f32_16x16x32_bf16 v[92:95], a[236:239], v[216:219], v[92:95]\n"
      "s_waitcnt lgkmcnt(2)\n"
      "v_mfma_f32_16x16x32_bf16 v[96:99], a[216:219], v[220:223], v[96:99]\n"
      "v_mfma_f32_16x16x32_bf16 v[100:103], a[220:223], v[220:223], v[100:103]\n"
      "v_mfma_f32_16x16x32_bf16 v[104:107], a[224:227], v[220:223], v[104:107]\n"
      "ds_read_b128 v[216:219], v233 offset:28672\n"
      "v_mfma_f32_16x16x32_bf16 v[108:111], a[228:231], v[220:223], v[108:111]\n"
      "v_mfma_f32_16x16x32_bf16 v[112:115], a[232:235], v[220:223], v[112:115]\n"
      "v_mfma_f32_16x16x32_bf16 v[116:119], a[236:239], v[220:223], v[116:119]\n"
      "s_waitcnt lgkmcnt(2)\n"
      "v_mfma_f32_16x16x32_bf16 v[120:123], a[216:219], v[224:227], v[120:123]\n"
      "v_mfma_f32_16x16x32_bf16 v[124:127], a[220:223], v[224:227], v[124:127]\n"
      "v_mfma_f32_16x16x32_bf16 v[128:131], a[224:227], v[224:227], v[128:131]\n"
      "ds_read_b128 v[220:223], v233 offset:32768\n"
      "v_add_u32 v233, v249, v233\n"
      "v_mfma_f32_16x16x32_bf16 v[132:135], a[228:231], v[224:227], v[132:135]\n"
      "v_mfma_f32_16x16x32_bf16 v[136:139], a[232:235], v[224:227], v[136:139]\n"
      "v_mfma_f32_16x16x32_bf16 v[140:143], a[236:239], v[224:227], v[140:143]\n"
      "s_waitcnt lgkmcnt(2)\n"
      "v_mfma_f32_16x16x32_bf16 v[144:147], a[216:219], v[228:231], v[144:147]\n"
      "v_mfma_f32_16x16x32_bf16 v[148:151], a[220:223], v[228:231], v[148:151]\n"
      "v_mfma_f32_16x16x32_bf16 v[152:155], a[224:227], v[228:231], v[152:155]\n"
      "ds_read_b128 v[224:227], v234 offset:0\n"
      "v_mfma_f32_16x16x32_bf16 v[156:159], a[228:231], v[228:231], v[156:159]\n"
      "v_mfma_f32_16x16x32_bf16 v[160:163], a[232:235], v[228:231], v[160:163]\n"
      "v_mfma_f32_16x16x32_bf16 v[164:167], a[236:239], v[228:231], v[164:167]\n"
      "s_waitcnt lgkmcnt(2)\n"
      "v_mfma_f32_16x16x32_bf16 v[168:171], a[216:219], v[216:219], v[168:171]\n"
      "v_mfma_f32_16x16x32_bf16 v[172:175], a[220:223], v[216:219], v[172:175]\n"
      "v_mfma_f32_16x16x32_bf16 v[176:179], a[224:227], v[216:219], v[176:179]\n"
      "ds_read_b128 v[228:231], v234 offset:4096\n"
      "v_mfma_f32_16x16x32_bf16 v[180:183], a[228:231], v[216:219], v[180:183]\n"
      "v_mfma_f32_16x16x32_bf16 v[184:187], a[232:235], v[216:219], v[184:187]\n"
      "v_mfma_f32_16x16x32_bf16 v[188:191], a[236:239], v[216:219], v[188:191]\n"
      "s_waitcnt lgkmcnt(2)\n"
      "v_mfma_f32_16x16x32_bf16 v[192:195], a[216:219], v[220:223], v[192:195]\n"
      "v_mfma_f32_16x16x32_bf16 v[196:199], a[220:223], v[220:223], v[196:199]\n"
      "v_mfma_f32_16x16x32_bf16 v[200:203], a[224:227], v[220:223], v[200:203]\n"
      "ds_read_b128 v[216:219], v234 offset:8192\n"
      "v_mfma_f32_16x16x32_bf16 v[204:207], a[228:231], v[220:223], v[204:207]\n"
      "v_mfma_f32_16x16x32_bf16 v[208:211], a[232:235], v[220:223], v[208:211]\n"
      "v_mfma_f32_16x16x32_bf16 v[212:215], a[236:239], v[220:223], v[212:215]\n"
      "s_waitcnt lgkmcnt(2)\n"
      "v_mfma_f32_16x16x32_bf16 v[0:3], a[0:3], v[224:227], v[0:3]\n"
      "ds_read_b128 v[244:247], v250\n"
      "v_mfma_f32_16x16x32_bf16 v[4:7], a[4:7], v[224:227], v[4:7]\n"
      "ds_read_b64 v[252:253], v250 offset:4096\n"
      "v_mfma_f32_16x16x32_bf16 v[8:11], a[8:11], v[224:227], v[8:11]\n"
      "ds_read_b128 v[220:223], v234 offset:12288\n"
      "v_mfma_f32_16x16x32_bf16 v[12:15], a[12:15], v[224:227], v[12:15]\n"
      "v_mfma_f32_16x16x32_bf16 v[16:19], a[16:19], v[224:227], v[16:19]\n"
      "v_mfma_f32_16x16x32_bf16 v[20:23], a[20:23], v[224:227], v[20:23]\n"
      "s_waitcnt lgkmcnt(4)\n"
      "v_mfma_f32_16x16x32_bf16 v[24:27], a[0:3], v[228:231], v[24:27]\n"
      "v_mfma_f32_16x16x32_bf16 v[28:31], a[4:7], v[228:231], v[28:31]\n"
      "v_mfma_f32_16x16x32_bf16 v[32:35], a[8:11], v[228:231], v[32:35]\n"
      "ds_read_b128 v[224:227], v234 offset:16384\n"
      "v_mfma_f32_16x16x32_bf16 v[36:39], a[12:15], v[228:231], v[36:39]\n"
      "global_load_dwordx4 a[172:175], v239, s[58:59] offset:3072 sc1\n"
      "v_mfma_f32_16x16x32_bf16 v[40:43], a[16:19], v[228:231], v[40:43]\n"
      "v_mfma_f32_16x16x32_bf16 v[44:47], a[20:23], v[228:231], v[44:47]\n"
      "s_waitcnt lgkmcnt(4)\n"
      "v_mfma_f32_16x16x32_bf16 v[48:51], a[0:3], v[216:219], v[48:51]\n"
      "v_mfma_f32_16x16x32_bf16 v[52:55], a[4:7], v[216:219], v[52:55]\n"
      "s_add_u32 s58, s24, 0x9000\n"
      "s_addc_u32 s59, s25, 0\n"
      "global_load_dwordx4 a[176:179], v239, s[58:59] offset:0 sc1\n"
      "v_mfma_f32_16x16x32_bf16 v[56:59], a[8:11], v[216:219], v[56:59]\n"
      "ds_read_b128 v[228:231], v234 offset:20480\n"
      "v_mfma_f32_16x16x32_bf16 v[60:63], a[12:15], v[216:219], v[60:63]\n"
      "v_mfma_f32_16x16x32_bf16 v[64:67], a[16:19], v[216:219], v[64:67]\n"
      "v_mfma_f32_16x16x32_bf16 v[68:71], a[20:23], v[216:219], v[68:71]\n"
      "global_load_dwordx4 a[180:183], v239, s[58:59] offset:1024 sc1\n"
      "s_waitcnt lgkmcnt(2)\n"
      "v_mfma_f32_16x16x32_bf16 v[72:75], a[0:3], v[220:223], v[72:75]\n"
      "v_mfma_f32_16x16x32_bf16 v[76:79], a[4:7], v[220:223], v[76:79]\n"
      "v_mfma_f32_16x16x32_bf16 v[80:83], a[8:11], v[220:223], v[80:83]\n"
      "ds_read_b128 v[216:219], v234 offset:24576\n"
      "v_mfma_f32_16x16x32_bf16 v[84:87], a[12:15], v[220:223], v[84:87]\n"
      "global_load_dwordx4 a[184:187], v239, s[58:59] offset:2048 sc1\n"
      "v_mfma_f32_16x16x32_bf16 v[88:91], a[16:19], v[220:223], v[88:91]\n"
      "v_mfma_f32_16x16x32_bf16 v[92:95], a[20:23], v[220:223], v[92:95]\n"
      "s_waitcnt lgkmcnt(2)\n"
      "v_mfma_f32_16x16x32_bf16 v[96:99], a[0:3], v[224:227], v[96:99]\n"
      "v_mfma_f32_16x16x32_bf16 v[100:103], a[4:7], v[224:227], v[100:103]\n"
      "global_load_dwordx4 a[188:191], v239, s[58:59] offset:3072 sc1\n"
      "v_mfma_f32_16x16x32_bf16 v[104:107], a[8:11], v[224:227], v[104:107]\n"
      "ds_read_b128 v[220:223], v234 offset:28672\n"
      "v_mfma_f32_16x16x32_bf16 v[108:111], a[12:15], v[224:227], v[108:111]\n"
      "v_mfma_f32_16x16x32_bf16 v[112:115], a[16:19], v[224:227], v[112:115]\n"
      "v_mfma_f32_16x16x32_bf16 v[116:119], a[20:23], v[224:227], v[116:119]\n"
      "s_add_u32 s58, s24, 0xa000\n"
      "s_addc_u32 s59, s25, 0\n"
      "global_load_dwordx4 a[192:195], v239, s[58:59] offset:0 sc1\n"
      "s_waitcnt lgkmcnt(2)\n"
      "v_mfma_f32_16x16x32_bf16 v[120:123], a[0:3], v[228:231], v[120:123]\n"
      "v_mfma_f32_16x16x32_bf16 v[124:127], a[4:7], v[228:231], v[124:127]\n"
      "v_mfma_f32_16x16x32_bf16 v[128:131], a[8:11], v[228:231], v[128:131]\n"
      "ds_read_b128 v[224:227], v234 offset:32768\n"
      "v_add_u32 v234, v249, v234\n"
      "v_mfma_f32_16x16x32_bf16 v[132:135], a[12:15], v[228:231], v[132:135]\n"
      "global_load_dwordx4 a[196:199], v239, s[58:59] offset:1024 sc1\n"
      "v_mfma_f32_16x16x32_bf16 v[136:139], a[16:19], v[228:231], v[136:139]\n"
      "v_mfma_f32_16x16x32_bf16 v[140:143], a[20:23], v[228:231], v[140:143]\n"
      "s_waitcnt lgkmcnt(2)\n"
      "v_mfma_f32_16x16x32_bf16 v[144:147], a[0:3], v[216:219], v[144:147]\n"
      "v_mfma_f32_16x16x32_bf16 v[148:151], a[4:7], v[216:219], v[148:151]\n"
      "global_load_dwordx4 a[200:203], v239, s[58:59] offset:2048 sc1\n"
      "v_mfma_f32_16x16x32_bf16 v[152:155], a[8:11], v[216:219], v[152:155]\n"
      "ds_read_b128 v[228:231], v235 offset:4096\n"
      "v_mfma_f32_16x16x32_bf16 v[156:159], a[12:15], v[216:219], v[156:159]\n"
      "v_mfma_f32_16x16x32_bf16 v[160:163], a[16:19], v[216:219], v[160:163]\n"
      "v_mfma_f32_16x16x32_bf16 v[164:167], a[20:23], v[216:219], v[164:167]\n"
      "global_load_dwordx4 a[204:207], v239, s[58:59] offset:3072 sc1\n"
      "s_waitcnt lgkmcnt(2)\n"
      "v_mfma_f32_16x16x32_bf16 v[168:171], a[0:3], v[220:223], v[168:171]\n"
      "v_mfma_f32_16x16x32_bf16 v[172:175], a[4:7], v[220:223], v[172:175]\n"
      "v_mfma_f32_16x16x32_bf16 v[176:179], a[8:11], v[220:223], v[176:179]\n"
      "ds_read_b128 v[216:219], v235 offset:8192\n"
      "v_mfma_f32_16x16x32_bf16 v[180:183], a[12:15], v[220:223], v[180:183]\n"
      "s_add_u32 s58, s24, 0xb000\n"
      "s_addc_u32 s59, s25, 0\n"
      "global_load_dwordx4 a[208:211], v239, s[58:59] offset:0 sc1\n"
      "v_mfma_f32_16x16x32_bf16 v[184:187], a[16:19], v[220:223], v[184:187]\n"
      "v_mfma_f32_16x16x32_bf16 v[188:191], a[20:23], v[220:223], v[188:191]\n"
      "s_waitcnt lgkmcnt(2)\n"
      "v_mfma_f32_16x16x32_bf16 v[192:195], a[0:3], v[224:227], v[192:195]\n"
      "v_mfma_f32_16x16x32_bf16 v[196:199], a[4:7], v[224:227], v[196:199]\n"
      "global_load_dwordx4 a[212:215], v239, s[58:59] offset:1024 sc1\n"
      "v_mfma_f32_16x16x32_bf16 v[200:203], a[8:11], v[224:227], v[200:203]\n"
      "ds_read_b128 v[220:223], v235 offset:12288\n"
      "v_mfma_f32_16x16x32_bf16 v[204:207], a[12:15], v[224:227], v[204:207]\n"
      "v_mfma_f32_16x16x32_bf16 v[208:211], a[16:19], v[224:227], v[208:211]\n"
      "v_mfma_f32_16x16x32_bf16 v[212:215], a[20:23], v[224:227], v[212:215]\n"
      "global_load_dwordx4 a[216:219], v239, s[58:59] offset:2048 sc1\n"
      "s_waitcnt lgkmcnt(2)\n"
      "v_mfma_f32_16x16x32_bf16 v[24:27], a[24:27], v[228:231], v[24:27]\n"
      "v_mfma_f32_16x16x32_bf16 v[28:31], a[28:31], v[228:231], v[28:31]\n"
      "v_mfma_f32_16x16x32_bf16 v[32:35], a[32:35], v[228:231], v[32:35]\n"
      "ds_read_b128 v[224:227], v235 offset:16384\n"
      "v_mfma_f32_16x16x32_bf16 v[36:39], a[36:39], v[228:231], v[36:39]\n"
      "global_load_dwordx4 a[220:223], v239, s[58:59] offset:3072 sc1\n"
      "v_mfma_f32_16x16x32_bf16 v[40:43], a[40:43], v[228:231], v[40:43]\n"
      "v_mfma_f32_16x16x32_bf16 v[44:47], a[44:47], v[228:231], v[44:47]\n"
      "s_waitcnt lgkmcnt(2)\n"
      "v_mfma_f32_16x16x32_bf16 v[48:51], a[24:27], v[216:219], v[48:51]\n"
      "v_cvt_pk_bf16_f32 v24, v24, v25\n"
      "v_cvt_pk_bf16_f32 v25, v26, v27\n"
      "v_mfma_f32_16x16x32_bf16 v[52:55], a[28:31], v[216:219], v[52:55]\n"
      "v_cvt_pk_bf16_f32 v26, v28, v29\n"
      "v_cvt_pk_bf16_f32 v27, v30, v31\n"
      "s_add_u32 s58, s24, 0xc000\n"
      "s_addc_u32 s59, s25, 0\n"
      "global_load_dwordx4 a[224:227], v239, s[58:59] offset:0 sc1\n"
      "v_mfma_f32_16x16x32_bf16 v[56:59], a[32:35], v[216:219], v[56:59]\n"
      "ds_read_b128 v[228:231], v235 offset:20480\n"
      "v_cvt_pk_bf16_f32 v28, v32, v33\n"
      "v_cvt_pk_bf16_f32 v29, v34, v35\n"
      "v_mfma_f32_16x16x32_bf16 v[60:63], a[36:39], v[216:219], v[60:63]\n"
      "v_cvt_pk_bf16_f32 v30, v36, v37\n"
      "v_cvt_pk_bf16_f32 v31, v38, v39\n"
      "v_mfma_f32_16x16x32_bf16 v[64:67], a[40:43], v[216:219], v[64:67]\n"
      "v_cvt_pk_bf16_f32 v32, v40, v41\n"
      "v_cvt_pk_bf16_f32 v33, v42, v43\n"
      "v_mfma_f32_16x16x32_bf16 v[68:71], a[44:47], v[216:219], v[68:71]\n"
      "v_cvt_pk_bf16_f32 v34, v44, v45\n"
      "v_cvt_pk_bf16_f32 v35, v46, v47\n"
      "global_load_dwordx4 a[228:231], v239, s[58:59] offset:1024 sc1\n"
      "s_waitcnt lgkmcnt(2)\n"
      "v_mfma_f32_16x16x32_bf16 v[72:75], a[24:27], v[220:223], v[72:75]\n"
      "v_cvt_pk_bf16_f32 v48, v48, v49\n"
      "v_cvt_pk_bf16_f32 v49, v50, v51\n"
      "v_mfma_f32_16x16x32_bf16 v[76:79], a[28:31], v[220:223], v[76:79]\n"
      "v_cvt_pk_bf16_f32 v50, v52, v53\n"
      "v_cvt_pk_bf16_f32 v51, v54, v55\n"
      "v_mfma_f32_16x16x32_bf16 v[80:83], a[32:35], v[220:223], v[80:83]\n"
      "ds_read_b128 v[216:219], v235 offset:24576\n"
      "v_cvt_pk_bf16_f32 v52, v56, v57\n"
      "v_cvt_pk_bf16_f32 v53, v58, v59\n"
      "v_mfma_f32_16x16x32_bf16 v[84:87], a[36:39], v[220:223], v[84:87]\n"
      "v_cvt_pk_bf16_f32 v54, v60, v61\n"
      "v_cvt_pk_bf16_f32 v55, v62, v63\n"
      "global_load_dwordx4 a[232:235], v239, s[58:59] offset:2048 sc1\n"
      "v_mfma_f32_16x16x32_bf16 v[88:91], a[40:43], v[220:223], v[88:91]\n"
      "v_cvt_pk_bf16_f32 v56, v64, v65\n"
      "v_cvt_pk_bf16_f32 v57, v66, v67\n"
      "v_mfma_f32_16x16x32_bf16 v[92:95], a[44:47], v[220:223], v[92:95]\n"
      "v_cvt_pk_bf16_f32 v58, v68, v69\n"
      "v_cvt_pk_bf16_f32 v59, v70, v71\n"
      "s_waitcnt lgkmcnt(2)\n"
      "v_mfma_f32_16x16x32_bf16 v[96:99], a[24:27], v[224:227], v[96:99]\n"
      "v_cvt_pk_bf16_f32 v72, v72, v73\n"
      "v_cvt_pk_bf16_f32 v73, v74, v75\n"
      "v_mfma_f32_16x16x32_bf16 v[100:103], a[28:31], v[224:227], v[100:103]\n"
      "v_cvt_pk_bf16_f32 v74, v76, v77\n"
      "v_cvt_pk_bf16_f32 v75, v78, v79\n"
      "global_load_dwordx4 a[236:239], v239, s[58:59] offset:3072 sc1\n"
      "v_mfma_f32_16x16x32_bf16 v[104:107], a[32:35], v[224:227], v[104:107]\n"
      "ds_read_b128 v[220:223], v235 offset:28672\n"
      "v_cvt_pk_bf16_f32 v76, v80, v81\n"
      "v_cvt_pk_bf16_f32 v77, v82, v83\n"
      "v_mfma_f32_16x16x32_bf16 v[108:111], a[36:39], v[224:227], v[108:111]\n"
      "v_cvt_pk_bf16_f32 v78, v84, v85\n"
      "v_cvt_pk_bf16_f32 v79, v86, v87\n"
      "v_mfma_f32_16x16x32_bf16 v[112:115], a[40:43], v[224:227], v[112:115]\n"
      "v_cvt_pk_bf16_f32 v80, v88, v89\n"
      "v_cvt_pk_bf16_f32 v81, v90, v91\n"
      "v_mfma_f32_16x16x32_bf16 v[116:119], a[44:47], v[224:227], v[116:119]\n"
      "v_cvt_pk_bf16_f32 v82, v92, v93\n"
      "v_cvt_pk_bf16_f32 v83, v94, v95\n"
      "s_add_u32 s58, s24, 0xd000\n"
      "s_addc_u32 s59, s25, 0\n"
      "global_load_dwordx4 a[0:3], v239, s[58:59] offset:0 sc1\n"
      "s_waitcnt lgkmcnt(2)\n"
      "v_mfma_f32_16x16x32_bf16 v[120:123], a[24:27], v[228:231], v[120:123]\n"
      "v_cvt_pk_bf16_f32 v96, v96, v97\n"
      "v_cvt_pk_bf16_f32 v97, v98, v99\n"
      "v_mfma_f32_16x16x32_bf16 v[124:127], a[28:31], v[228:231], v[124:127]\n"
      "v_cvt_pk_bf16_f32 v98, v100, v101\n"
      "v_cvt_pk_bf16_f32 v99, v102, v103\n"
      "v_mfma_f32_16x16x32_bf16 v[128:131], a[32:35], v[228:231], v[128:131]\n"
      "ds_read_b128 v[224:227], v235 offset:32768\n"
      "v_cvt_pk_bf16_f32 v100, v104, v105\n"
      "v_cvt_pk_bf16_f32 v101, v106, v107\n"
      "v_mfma_f32_16x16x32_bf16 v[132:135], a[36:39], v[228:231], v[132:135]\n"
      "v_cvt_pk_bf16_f32 v102, v108, v109\n"
      "v_cvt_pk_bf16_f32 v103, v110, v111\n"
      "global_load_dwordx4 a[4:7], v239, s[58:59] offset:1024 sc1\n"
      "v_mfma_f32_16x16x32_bf16 v[136:139], a[40:43], v[228:231], v[136:139]\n"
      "v_cvt_pk_bf16_f32 v104, v112, v113\n"
      "v_cvt_pk_bf16_f32 v105, v114, v115\n"
      "v_mfma_f32_16x16x32_bf16 v[140:143], a[44:47], v[228:231], v[140:143]\n"
      "v_cvt_pk_bf16_f32 v106, v116, v117\n"
      "v_cvt_pk_bf16_f32 v107, v118, v119\n"
      "s_waitcnt lgkmcnt(2)\n"
      "v_mfma_f32_16x16x32_bf16 v[144:147], a[24:27], v[216:219], v[144:147]\n"
      "v_cvt_pk_bf16_f32 v120, v120, v121\n"
      "v_cvt_pk_bf16_f32 v121, v122, v123\n"
      "v_mfma_f32_16x16x32_bf16 v[148:151], a[28:31], v[216:219], v[148:151]\n"
      "v_cvt_pk_bf16_f32 v122, v124, v125\n"
      "v_cvt_pk_bf16_f32 v123, v126, v127\n"
      "global_load_dwordx4 a[8:11], v239, s[58:59] offset:2048 sc1\n"
      "v_mfma_f32_16x16x32_bf16 v[152:155], a[32:35], v[216:219], v[152:155]\n"
      "ds_read_b128 v[228:231], v235 offset:0\n"
      "v_add_u32 v235, v249, v235\n"
      "s_waitcnt vmcnt(26)\n"
      "s_barrier\n"
      "v_cvt_pk_bf16_f32 v124, v128, v129\n"
      "v_cvt_pk_bf16_f32 v125, v130, v131\n"
      "v_mfma_f32_16x16x32_bf16 v[156:159], a[36:39], v[216:219], v[156:159]\n"
      "ds_read_b128 v[36:39], v232 offset:0\n"
      "v_cvt_pk_bf16_f32 v126, v132, v133\n"
      "v_cvt_pk_bf16_f32 v127, v134, v135\n"
      "v_mfma_f32_16x16x32_bf16 v[160:163], a[40:43], v[216:219], v[160:163]\n"
      "v_cvt_pk_bf16_f32 v128, v136, v137\n"
      "v_cvt_pk_bf16_f32 v129, v138, v139\n"
      "v_mfma_f32_16x16x32_bf16 v[164:167], a[44:47], v[216:219], v[164:167]\n"
      "ds_read_b128 v[40:43], v233 offset:0\n"
      "v_cvt_pk_bf16_f32 v130, v140, v141\n"
      "v_cvt_pk_bf16_f32 v131, v142, v143\n"
      "global_load_dwordx4 a[12:15], v239, s[58:59] offset:3072 sc1\n"
      "s_waitcnt lgkmcnt(4)\n"
      "v_mfma_f32_16x16x32_bf16 v[168:171], a[24:27], v[220:223], v[168:171]\n"
      "v_cvt_pk_bf16_f32 v144, v144, v145\n"
      "v_cvt_pk_bf16_f32 v145, v146, v147\n"
      "v_mfma_f32_16x16x32_bf16 v[172:175], a[28:31], v[220:223], v[172:175]\n"
      "ds_read_b128 v[44:47], v234 offset:0\n"
      "v_cvt_pk_bf16_f32 v146, v148, v149\n"
      "v_cvt_pk_bf16_f32 v147, v150, v151\n"
      "v_mfma_f32_16x16x32_bf16 v[176:179], a[32:35], v[220:223], v[176:179]\n"
      "v_cvt_pk_bf16_f32 v148, v152, v153\n"
      "v_cvt_pk_bf16_f32 v149, v154, v155\n"
      "v_mfma_f32_16x16x32_bf16 v[180:183], a[36:39], v[220:223], v[180:183]\n"
      "ds_read_b128 v[60:63], v235 offset:0\n"
      "v_cvt_pk_bf16_f32 v150, v156, v157\n"
      "v_cvt_pk_bf16_f32 v151, v158, v159\n"
      "s_add_u32 s58, s24, 0xe000\n"
      "s_addc_u32 s59, s25, 0\n"
      "global_load_dwordx4 a[16:19], v239, s[58:59] offset:0 sc1\n"
      "v_mfma_f32_16x16x32_bf16 v[184:187], a[40:43], v[220:223], v[184:187]\n"
      "v_cvt_pk_bf16_f32 v152, v160, v161\n"
      "v_cvt_pk_bf16_f32 v153, v162, v163\n"
      "v_mfma_f32_16x16x32_bf16 v[188:191], a[44:47], v[220:223], v[188:191]\n"
      "ds_read_b128 v[64:67], v232 offset:8192\n"
      "v_cvt_pk_bf16_f32 v154, v164, v165\n"
      "v_cvt_pk_bf16_f32 v155, v166, v167\n"
      "s_waitcnt lgkmcnt(6)\n"
      "v_mfma_f32_16x16x32_bf16 v[192:195], a[24:27], v[224:227], v[192:195]\n"
      "v_cvt_pk_bf16_f32 v168, v168, v169\n"
      "v_cvt_pk_bf16_f32 v169, v170, v171\n"
      "v_mfma_f32_16x16x32_bf16 v[196:199], a[28:31], v[224:227], v[196:199]\n"
      "ds_read_b128 v[68:71], v233 offset:8192\n"
      "v_cvt_pk_bf16_f32 v170, v172, v173\n"
      "v_cvt_pk_bf16_f32 v171, v174, v175\n"
      "global_load_dwordx4 a[20:23], v239, s[58:59] offset:1024 sc1\n"
      "v_mfma_f32_16x16x32_bf16 v[200:203], a[32:35], v[224:227], v[200:203]\n"
      "v_cvt_pk_bf16_f32 v172, v176, v177\n"
      "v_cvt_pk_bf16_f32 v173, v178, v179\n"
      "v_mfma_f32_16x16x32_bf16 v[204:207], a[36:39], v[224:227], v[204:207]\n"
      "ds_read_b128 v[84:87], v234 offset:8192\n"
      "v_cvt_pk_bf16_f32 v174, v180, v181\n"
      "v_cvt_pk_bf16_f32 v175, v182, v183\n"
      "v_mfma_f32_16x16x32_bf16 v[208:211], a[40:43], v[224:227], v[208:211]\n"
      "v_cvt_pk_bf16_f32 v176, v184, v185\n"
      "v_cvt_pk_bf16_f32 v177, v186, v187\n"
      "v_mfma_f32_16x16x32_bf16 v[212:215], a[44:47], v[224:227], v[212:215]\n"
      "ds_read_b128 v[88:91], v235 offset:8192\n"
      "v_cvt_pk_bf16_f32 v178, v188, v189\n"
      "v_cvt_pk_bf16_f32 v179, v190, v191\n"
      "global_load_dwordx4 v[140:143], v239, s[58:59] offset:2048 sc1\n"
      "s_waitcnt lgkmcnt(8)\n"
      "v_mfma_f32_16x16x32_bf16 v[0:3], a[24:27], v[228:231], v[0:3]\n"
      "v_cvt_pk_bf16_f32 v192, v192, v193\n"
      "v_cvt_pk_bf16_f32 v193, v194, v195\n"
      "v_mfma_f32_16x16x32_bf16 v[4:7], a[28:31], v[228:231], v[4:7]\n"
      "ds_read_b128 v[92:95], v232 offset:16384\n"
      "v_cvt_pk_bf16_f32 v194, v196, v197\n"
      "v_cvt_pk_bf16_f32 v195, v198, v199\n"
      "v_mfma_f32_16x16x32_bf16 v[8:11], a[32:35], v[228:231], v[8:11]\n"
      "v_cvt_pk_bf16_f32 v196, v200, v201\n"
      "v_cvt_pk_bf16_f32 v197, v202, v203\n"
      "v_mfma_f32_16x16x32_bf16 v[12:15], a[36:39], v[228:231], v[12:15]\n"
      "ds_read_b128 v[108:111], v233 offset:16384\n"
      "v_cvt_pk_bf16_f32 v198, v204, v205\n"
      "v_cvt_pk_bf16_f32 v199, v206, v207\n"
      "global_load_dwordx4 v[156:159], v239, s[58:59] offset:3072 sc1\n"
      "v_mfma_f32_16x16x32_bf16 v[16:19], a[40:43], v[228:231], v[16:19]\n"
      "ds_read_b128 v[112:115], v234 offset:16384\n"
      "v_cvt_pk_bf16_f32 v200, v208, v209\n"
      "v_cvt_pk_bf16_f32 v201, v210, v211\n"
      "v_mfma_f32_16x16x32_bf16 v[20:23], a[44:47], v[228:231], v[20:23]\n"
      "ds_read_b128 v[116:119], v235 offset:16384\n"
      "v_cvt_pk_bf16_f32 v202, v212, v213\n"
      "v_cvt_pk_bf16_f32 v203, v214, v215\n"
      "s_waitcnt lgkmcnt(8)\n"
      "v_mfma_f32_16x16x32_bf16 v[132:135], a[240:243], v[36:39], v[0:3]\n"
      "s_mov_b64 vcc, 0\n"
      "v_mfma_f32_16x16x32_bf16 v[132:135], a[244:247], v[40:43], v[132:135]\n"
      "s_add_u32 s58, s24, 0xf000\n"
      "s_addc_u32 s59, s25, 0\n"
      "global_load_dwordx4 v[160:163], v239, s[58:59] offset:0 sc1\n"
      "v_mfma_f32_16x16x32_bf16 v[132:135], a[248:251], v[44:47], v[132:135]\n"
      "v_mfma_f32_16x16x32_bf16 v[132:135], a[252:255], v[60:63], v[132:135]\n"
      "s_waitcnt lgkmcnt(4)\n"
      "v_mfma_f32_16x16x32_bf16 v[132:135], a[48:51], v[64:67], v[132:135]\n"
      "global_load_dwordx4 v[164:167], v239, s[58:59] offset:1024 sc1\n"
      "v_mfma_f32_16x16x32_bf16 v[132:135], a[52:55], v[68:71], v[132:135]\n"
      "v_mfma_f32_16x16x32_bf16 v[132:135], a[56:59], v[84:87], v[132:135]\n"
      "v_mfma_f32_16x16x32_bf16 v[132:135], a[60:63], v[88:91], v[132:135]\n"
      "global_load_dwordx4 v[180:183], v239, s[58:59] offset:2048 sc1\n"
      "s_waitcnt lgkmcnt(0)\n"
      "v_mfma_f32_16x16x32_bf16 v[132:135], a[64:67], v[92:95], v[132:135]\n"
      "v_mfma_f32_16x16x32_bf16 v[132:135], a[68:71], v[108:111], v[132:135]\n"
      "v_mfma_f32_16x16x32_bf16 v[132:135], a[72:75], v[112:115], v[132:135]\n"
      "global_load_dwordx4 v[184:187], v239, s[58:59] offset:3072 sc1\n"
      "v_mfma_f32_16x16x32_bf16 v[132:135], a[76:79], v[116:119], v[132:135]\n"
      "v_mfma_f32_16x16x32_bf16 v[136:139], a[80:83], v[36:39], v[4:7]\n"
      "v_mfma_f32_16x16x32_bf16 v[136:139], a[84:87], v[40:43], v[136:139]\n"
      "s_add_u32 s58, s24, 0x10000\n"
      "s_addc_u32 s59, s25, 0\n"
      "global_load_dwordx4 v[188:191], v239, s[58:59] offset:0 sc1\n"
      "v_mfma_f32_16x16x32_bf16 v[136:139], a[88:91], v[44:47], v[136:139]\n"
      "v_mfma_f32_16x16x32_bf16 v[136:139], a[92:95], v[60:63], v[136:139]\n"
      "v_cvt_pk_bf16_f32 v208, v132, v133\n"
      "v_mfma_f32_16x16x32_bf16 v[136:139], a[96:99], v[64:67], v[136:139]\n"
      "global_load_dwordx4 v[204:207], v239, s[58:59] offset:1024 sc1\n"
      "v_mfma_f32_16x16x32_bf16 v[136:139], a[100:103], v[68:71], v[136:139]\n"
      "v_cvt_pk_bf16_f32 v209, v134, v135\n"
      "v_mfma_f32_16x16x32_bf16 v[136:139], a[104:107], v[84:87], v[136:139]\n"
      "v_mfma_f32_16x16x32_bf16 v[136:139], a[108:111], v[88:91], v[136:139]\n"
      "global_load_dwordx4 a[24:27], v239, s[58:59] offset:2048 sc1\n"
      "v_mfma_f32_16x16x32_bf16 v[136:139], a[112:115], v[92:95], v[136:139]\n"
      "v_mfma_f32_16x16x32_bf16 v[136:139], a[116:119], v[108:111], v[136:139]\n"
      "v_mfma_f32_16x16x32_bf16 v[136:139], a[120:123], v[112:115], v[136:139]\n"
      "global_load_dwordx4 a[28:31], v239, s[58:59] offset:3072 sc1\n"
      "v_mfma_f32_16x16x32_bf16 v[136:139], a[124:127], v[116:119], v[136:139]\n"
      "ds_write_b64 v244, v[208:209] offset:12288\n"
      "v_mfma_f32_16x16x32_bf16 v[132:135], a[128:131], v[36:39], v[8:11]\n"
      "v_mfma_f32_16x16x32_bf16 v[132:135], a[132:135], v[40:43], v[132:135]\n"
      "s_add_u32 s58, s24, 0x11000\n"
      "s_addc_u32 s59, s25, 0\n"
      "global_load_dwordx4 a[32:35], v239, s[58:59] offset:0 sc1\n"
      "v_mfma_f32_16x16x32_bf16 v[132:135], a[136:139], v[44:47], v[132:135]\n"
      "v_mfma_f32_16x16x32_bf16 v[132:135], a[140:143], v[60:63], v[132:135]\n"
      "v_cvt_pk_bf16_f32 v210, v136, v137\n"
      "v_mfma_f32_16x16x32_bf16 v[132:135], a[144:147], v[64:67], v[132:135]\n"
      "global_load_dwordx4 a[36:39], v239, s[58:59] offset:1024 sc1\n"
      "v_mfma_f32_16x16x32_bf16 v[132:135], a[148:151], v[68:71], v[132:135]\n"
      "v_cvt_pk_bf16_f32 v211, v138, v139\n"
      "v_mfma_f32_16x16x32_bf16 v[132:135], a[152:155], v[84:87], v[132:135]\n"
      "v_mfma_f32_16x16x32_bf16 v[132:135], a[156:159], v[88:91], v[132:135]\n"
      "global_load_dwordx4 a[40:43], v239, s[58:59] offset:2048 sc1\n"
      "s_waitcnt vmcnt(35)\n"
      "v_mfma_f32_16x16x32_bf16 v[132:135], a[160:163], v[92:95], v[132:135]\n"
      "v_mfma_f32_16x16x32_bf16 v[132:135], a[164:167], v[108:111], v[132:135]\n"
      "v_mfma_f32_16x16x32_bf16 v[132:135], a[168:171], v[112:115], v[132:135]\n"
      "global_load_dwordx4 a[44:47], v239, s[58:59] offset:3072 sc1\n"
      "v_mfma_f32_16x16x32_bf16 v[132:135], a[172:175], v[116:119], v[132:135]\n"
      "ds_write_b64 v244, v[210:211] offset:12296\n"
      "s_waitcnt vmcnt(32)\n"
      "v_mfma_f32_16x16x32_bf16 v[136:139], a[176:179], v[36:39], v[12:15]\n"
      "v_mfma_f32_16x16x32_bf16 v[136:139], a[180:183], v[40:43], v[136:139]\n"
      "s_mul_i32 s78, s90, s19\n"
      "v_mfma_f32_16x16x32_bf16 v[136:139], a[184:187], v[44:47], v[136:139]\n"
      "v_add_u32 v236, s78, v236\n"
      "v_mfma_f32_16x16x32_bf16 v[136:139], a[188:191], v[60:63], v[136:139]\n"
      "s_mul_i32 s78, s78, s99\n"
      "s_waitcnt vmcnt(28)\n"
      "v_mfma_f32_16x16x32_bf16 v[136:139], a[192:195], v[64:67], v[136:139]\n"
      "v_add_u32 v237, s78, v237\n"
      "v_mfma_f32_16x16x32_bf16 v[136:139], a[196:199], v[68:71], v[136:139]\n"
      "s_add_u32 s52, s20, s33\n"
      "s_addc_u32 s53, s21, 0\n"
      "s_add_u32 m0, s17, 0xc000\n"
      "v_mfma_f32_16x16x32_bf16 v[136:139], a[200:203], v[84:87], v[136:139]\n"
      "global_load_lds_dwordx4 v236, s[52:53] offset:0 \n"
      "v_mfma_f32_16x16x32_bf16 v[136:139], a[204:207], v[88:91], v[136:139]\n"
      "v_cvt_pk_bf16_f32 v212, v132, v133\n"
      "s_waitcnt vmcnt(25)\n"
      "v_mfma_f32_16x16x32_bf16 v[136:139], a[208:211], v[92:95], v[136:139]\n"
      "v_cvt_pk_bf16_f32 v213, v134, v135\n"
      "v_mfma_f32_16x16x32_bf16 v[136:139], a[212:215], v[108:111], v[136:139]\n"
      "v_mfma_f32_16x16x32_bf16 v[136:139], a[216:219], v[112:115], v[136:139]\n"
      "v_mfma_f32_16x16x32_bf16 v[136:139], a[220:223], v[116:119], v[136:139]\n"
      "s_add_u32 m0, s17, 0xdf00\n"
      "s_waitcnt vmcnt(21)\n"
      "v_mfma_f32_16x16x32_bf16 v[132:135], a[224:227], v[36:39], v[16:19]\n"
      "global_load_lds_dwordx4 v236, s[52:53] offset:256 \n"
      "v_mfma_f32_16x16x32_bf16 v[132:135], a[228:231], v[40:43], v[132:135]\n"
      "ds_write_b64 v245, v[212:213] offset:12288\n"
      "v_mfma_f32_16x16x32_bf16 v[132:135], a[232:235], v[44:47], v[132:135]\n"
      "v_mfma_f32_16x16x32_bf16 v[132:135], a[236:239], v[60:63], v[132:135]\n"
      "v_cvt_pk_bf16_f32 v214, v136, v137\n"
      "s_waitcnt vmcnt(18)\n"
      "v_mfma_f32_16x16x32_bf16 v[132:135], a[0:3], v[64:67], v[132:135]\n"
      "v_cvt_pk_bf16_f32 v215, v138, v139\n"
      "v_mfma_f32_16x16x32_bf16 v[132:135], a[4:7], v[68:71], v[132:135]\n"
      "s_add_u32 m0, s17, 0xfe00\n"
      "v_mfma_f32_16x16x32_bf16 v[132:135], a[8:11], v[84:87], v[132:135]\n"
      "global_load_lds_dwordx4 v236, s[52:53] offset:512 \n"
      "v_mfma_f32_16x16x32_bf16 v[132:135], a[12:15], v[88:91], v[132:135]\n"
      "s_waitcnt vmcnt(15)\n"
      "v_mfma_f32_16x16x32_bf16 v[132:135], a[16:19], v[92:95], v[132:135]\n"
      "v_mfma_f32_16x16x32_bf16 v[132:135], a[20:23], v[108:111], v[132:135]\n"
      "ds_write_b64 v245, v[214:215] offset:12296\n"
      "v_mfma_f32_16x16x32_bf16 v[132:135], v[140:143], v[112:115], v[132:135]\n"
      "v_mfma_f32_16x16x32_bf16 v[132:135], v[156:159], v[116:119], v[132:135]\n"
      "s_barrier\n"
      "s_add_u32 m0, s17, 0xd000\n"
      "s_waitcnt vmcnt(11)\n"
      "v_mfma_f32_16x16x32_bf16 v[136:139], v[160:163], v[36:39], v[20:23]\n"
      "ds_read_b128 v[36:39], v232 offset:256\n"
      "global_load_lds_dwordx4 v237, s[52:53] offset:0 \n"
      "v_mfma_f32_16x16x32_bf16 v[136:139], v[164:167], v[40:43], v[136:139]\n"
      "ds_read_b128 v[40:43], v233 offset:256\n"
      "v_mfma_f32_16x16x32_bf16 v[136:139], v[180:183], v[44:47], v[136:139]\n"
      "ds_read_b128 v[44:47], v234 offset:256\n"
      "v_mfma_f32_16x16x32_bf16 v[136:139], v[184:187], v[60:63], v[136:139]\n"
      "ds_read_b128 v[60:63], v235 offset:256\n"
      "v_cvt_pk_bf16_f32 v208, v132, v133\n"
      "s_waitcnt vmcnt(8)\n"
      "v_mfma_f32_16x16x32_bf16 v[136:139], v[188:191], v[64:67], v[136:139]\n"
      "ds_read_b128 v[64:67], v232 offset:8448\n"
      "v_cvt_pk_bf16_f32 v209, v134, v135\n"
      "v_mfma_f32_16x16x32_bf16 v[136:139], v[204:207], v[68:71], v[136:139]\n"
      "ds_read_b128 v[68:71], v233 offset:8448\n"
      "s_add_u32 m0, s17, 0xef00\n"
      "v_mfma_f32_16x16x32_bf16 v[136:139], a[24:27], v[84:87], v[136:139]\n"
      "ds_read_b128 v[84:87], v234 offset:8448\n"
      "global_load_lds_dwordx4 v237, s[52:53] offset:256 \n"
      "v_mfma_f32_16x16x32_bf16 v[136:139], a[28:31], v[88:91], v[136:139]\n"
      "ds_read_b128 v[88:91], v235 offset:8448\n"
      "s_waitcnt vmcnt(5)\n"
      "v_mfma_f32_16x16x32_bf16 v[136:139], a[32:35], v[92:95], v[136:139]\n"
      "ds_read_b128 v[92:95], v232 offset:16640\n"
      "v_mfma_f32_16x16x32_bf16 v[136:139], a[36:39], v[108:111], v[136:139]\n"
      "ds_read_b128 v[108:111], v233 offset:16640\n"
      "ds_write_b64 v244, v[208:209] offset:12416\n"
      "v_mfma_f32_16x16x32_bf16 v[136:139], a[40:43], v[112:115], v[136:139]\n"
      "ds_read_b128 v[112:115], v234 offset:16640\n"
      "v_mfma_f32_16x16x32_bf16 v[136:139], a[44:47], v[116:119], v[136:139]\n"
      "ds_read_b128 v[116:119], v235 offset:16640\n"
      "s_add_u32 m0, s17, 0x10e00\n"
      "s_waitcnt lgkmcnt(9)\n"
      "v_mfma_f32_16x16x32_bf16 v[132:135], a[240:243], v[36:39], v[0:3]\n"
      "global_load_lds_dwordx4 v237, s[52:53] offset:512 \n"
      "v_mfma_f32_16x16x32_bf16 v[132:135], a[244:247], v[40:43], v[132:135]\n"
      "v_mfma_f32_16x16x32_bf16 v[132:135], a[248:251], v[44:47], v[132:135]\n"
      "v_mfma_f32_16x16x32_bf16 v[132:135], a[252:255], v[60:63], v[132:135]\n"
      "v_cvt_pk_bf16_f32 v210, v136, v137\n"
      "s_waitcnt lgkmcnt(5)\n"
      "v_mfma_f32_16x16x32_bf16 v[132:135], a[48:51], v[64:67], v[132:135]\n"
      "v_cvt_pk_bf16_f32 v211, v138, v139\n"
      "v_mfma_f32_16x16x32_bf16 v[132:135], a[52:55], v[68:71], v[132:135]\n"
      "v_mfma_f32_16x16x32_bf16 v[132:135], a[56:59], v[84:87], v[132:135]\n"
      "s_add_u32 s52, s42, s33\n"
      "s_addc_u32 s53, s43, 0\n"
      "s_add_u32 m0, s17, 0x12000\n"
      "v_mfma_f32_16x16x32_bf16 v[132:135], a[60:63], v[88:91], v[132:135]\n"
      "global_load_lds_dwordx4 v236, s[52:53] offset:0 \n"
      "s_waitcnt lgkmcnt(0)\n"
      "v_mfma_f32_16x16x32_bf16 v[132:135], a[64:67], v[92:95], v[132:135]\n"
      "LPD3:\n"
      "s_cbranch_execz LPD9\n"
      "v_mfma_f32_16x16x32_bf16 v[132:135], a[68:71], v[108:111], v[132:135]\n"
      "ds_write_b64 v244, v[210:211] offset:12424\n"
      "v_mfma_f32_16x16x32_bf16 v[132:135], a[72:75], v[112:115], v[132:135]\n"
      "v_mfma_f32_16x16x32_bf16 v[132:135], a[76:79], v[116:119], v[132:135]\n"
      "v_mfma_f32_16x16x32_bf16 v[136:139], a[80:83], v[36:39], v[4:7]\n"
      "s_add_u32 m0, s17, 0x13f00\n"
      "v_mfma_f32_16x16x32_bf16 v[136:139], a[84:87], v[40:43], v[136:139]\n"
      "global_load_lds_dwordx4 v236, s[52:53] offset:256 \n"
      "v_mfma_f32_16x16x32_bf16 v[136:139], a[88:91], v[44:47], v[136:139]\n"
      "v_mfma_f32_16x16x32_bf16 v[136:139], a[92:95], v[60:63], v[136:139]\n"
      "v_cvt_pk_bf16_f32 v208, v132, v133\n"
      "v_mfma_f32_16x16x32_bf16 v[136:139], a[96:99], v[64:67], v[136:139]\n"
      "v_cvt_pk_bf16_f32 v209, v134, v135\n"
      "v_mfma_f32_16x16x32_bf16 v[136:139], a[100:103], v[68:71], v[136:139]\n"
      "v_mfma_f32_16x16x32_bf16 v[136:139], a[104:107], v[84:87], v[136:139]\n"
      "s_add_u32 m0, s17, 0x15e00\n"
      "v_mfma_f32_16x16x32_bf16 v[136:139], a[108:111], v[88:91], v[136:139]\n"
      "global_load_lds_dwordx4 v236, s[52:53] offset:512 \n"
      "v_mfma_f32_16x16x32_bf16 v[136:139], a[112:115], v[92:95], v[136:139]\n"
      "v_mfma_f32_16x16x32_bf16 v[136:139], a[116:119], v[108:111], v[136:139]\n"
      "ds_write_b64 v244, v[208:209] offset:24576\n"
      "v_mfma_f32_16x16x32_bf16 v[136:139], a[120:123], v[112:115], v[136:139]\n"
      "v_mfma_f32_16x16x32_bf16 v[136:139], a[124:127], v[116:119], v[136:139]\n"
      "v_mfma_f32_16x16x32_bf16 v[132:135], a[128:131], v[36:39], v[8:11]\n"
      "s_add_u32 m0, s17, 0x13000\n"
      "v_mfma_f32_16x16x32_bf16 v[132:135], a[132:135], v[40:43], v[132:135]\n"
      "global_load_lds_dwordx4 v237, s[52:53] offset:0 \n"
      "v_mfma_f32_16x16x32_bf16 v[132:135], a[136:139], v[44:47], v[132:135]\n"
      "v_mfma_f32_16x16x32_bf16 v[132:135], a[140:143], v[60:63], v[132:135]\n"
      "v_cvt_pk_bf16_f32 v210, v136, v137\n"
      "v_mfma_f32_16x16x32_bf16 v[132:135], a[144:147], v[64:67], v[132:135]\n"
      "v_cvt_pk_bf16_f32 v211, v138, v139\n"
      "v_mfma_f32_16x16x32_bf16 v[132:135], a[148:151], v[68:71], v[132:135]\n"
      "v_mfma_f32_16x16x32_bf16 v[132:135], a[152:155], v[84:87], v[132:135]\n"
      "s_add_u32 m0, s17, 0x14f00\n"
      "v_mfma_f32_16x16x32_bf16 v[132:135], a[156:159], v[88:91], v[132:135]\n"
      "global_load_lds_dwordx4 v237, s[52:53] offset:256 \n"
      "v_mfma_f32_16x16x32_bf16 v[132:135], a[160:163], v[92:95], v[132:135]\n"
      "v_mfma_f32_16x16x32_bf16 v[132:135], a[164:167], v[108:111], v[132:135]\n"
      "ds_write_b64 v244, v[210:211] offset:24584\n"
      "v_mfma_f32_16x16x32_bf16 v[132:135], a[168:171], v[112:115], v[132:135]\n"
      "v_mfma_f32_16x16x32_bf16 v[132:135], a[172:175], v[116:119], v[132:135]\n"
      "v_mfma_f32_16x16x32_bf16 v[136:139], a[176:179], v[36:39], v[12:15]\n"
      "s_add_u32 m0, s17, 0x16e00\n"
      "v_mfma_f32_16x16x32_bf16 v[136:139], a[180:183], v[40:43], v[136:139]\n"
      "global_load_lds_dwordx4 v237, s[52:53] offset:512 \n"
      "v_mfma_f32_16x16x32_bf16 v[136:139], a[184:187], v[44:47], v[136:139]\n"
      "v_mfma_f32_16x16x32_bf16 v[136:139], a[188:191], v[60:63], v[136:139]\n"
      "v_cvt_pk_bf16_f32 v212, v132, v133\n"
      "v_mfma_f32_16x16x32_bf16 v[136:139], a[192:195], v[64:67], v[136:139]\n"
      "v_cvt_pk_bf16_f32 v213, v134, v135\n"
      "v_mfma_f32_16x16x32_bf16 v[136:139], a[196:199], v[68:71], v[136:139]\n"
      "v_mfma_f32_16x16x32_bf16 v[136:139], a[200:203], v[84:87], v[136:139]\n"
      "v_mfma_f32_16x16x32_bf16 v[136:139], a[204:207], v[88:91], v[136:139]\n"
      "v_mfma_f32_16x16x32_bf16 v[136:139], a[208:211], v[92:95], v[136:139]\n"
      "v_mfma_f32_16x16x32_bf16 v[136:139], a[212:215], v[108:111], v[136:139]\n"
      "ds_write_b64 v245, v[212:213] offset:24576\n"
      "v_mfma_f32_16x16x32_bf16 v[136:139], a[216:219], v[112:115], v[136:139]\n"
      "v_mfma_f32_16x16x32_bf16 v[136:139], a[220:223], v[116:119], v[136:139]\n"
      "v_mfma_f32_16x16x32_bf16 v[132:135], a[224:227], v[36:39], v[16:19]\n"
      "v_mfma_f32_16x16x32_bf16 v[132:135], a[228:231], v[40:43], v[132:135]\n"
      "v_mfma_f32_16x16x32_bf16 v[132:135], a[232:235], v[44:47], v[132:135]\n"
      "v_mfma_f32_16x16x32_bf16 v[132:135], a[236:239], v[60:63], v[132:135]\n"
      "v_cvt_pk_bf16_f32 v214, v136, v137\n"
      "v_mfma_f32_16x16x32_bf16 v[132:135], a[0:3], v[64:67], v[132:135]\n"
      "v_cvt_pk_bf16_f32 v215, v138, v139\n"
      "v_mfma_f32_16x16x32_bf16 v[132:135], a[4:7], v[68:71], v[132:135]\n"
      "v_mfma_f32_16x16x32_bf16 v[132:135], a[8:11], v[84:87], v[132:135]\n"
      "v_mfma_f32_16x16x32_bf16 v[132:135], a[12:15], v[88:91], v[132:135]\n"
      "v_mfma_f32_16x16x32_bf16 v[132:135], a[16:19], v[92:95], v[132:135]\n"
      "v_mfma_f32_16x16x32_bf16 v[132:135], a[20:23], v[108:111], v[132:135]\n"
      "ds_write_b64 v245, v[214:215] offset:24584\n"
      "v_mfma_f32_16x16x32_bf16 v[132:135], v[140:143], v[112:115], v[132:135]\n"
      "s_lshl_b32 s79, s98, 1\n"
      "v_mfma_f32_16x16x32_bf16 v[132:135], v[156:159], v[116:119], v[132:135]\n"
      "s_waitcnt lgkmcnt(4)\n"
      "s_barrier\n"
      "v_mad_i32_i24 v252, v251, s79, v252\n"
      "v_mfma_f32_16x16x32_bf16 v[136:139], v[160:163], v[36:39], v[20:23]\n"
      "ds_read_b128 v[36:39], v232 offset:24576\n"
      "s_mul_i32 s79, s79, s100\n"
      "v_mfma_f32_16x16x32_bf16 v[136:139], v[164:167], v[40:43], v[136:139]\n"
      "ds_read_b128 v[40:43], v233 offset:24576\n"
      "v_add_u32 v247, s79, v247\n"
      "v_mfma_f32_16x16x32_bf16 v[136:139], v[180:183], v[44:47], v[136:139]\n"
      "ds_read_b128 v[44:47], v234 offset:24576\n"
      "s_mul_i32 s79, s79, s101\n"
      "v_mfma_f32_16x16x32_bf16 v[136:139], v[184:187], v[60:63], v[136:139]\n"
      "ds_read_b128 v[60:63], v235 offset:24576\n"
      "v_cvt_pk_bf16_f32 v208, v132, v133\n"
      "v_mfma_f32_16x16x32_bf16 v[136:139], v[188:191], v[64:67], v[136:139]\n"
      "ds_read_b128 v[64:67], v232 offset:32768\n"
      "v_cvt_pk_bf16_f32 v209, v134, v135\n"
      "v_mfma_f32_16x16x32_bf16 v[136:139], v[204:207], v[68:71], v[136:139]\n"
      "ds_read_b128 v[68:71], v233 offset:32768\n"
      "v_add_u32 v253, s79, v253\n"
      "v_mfma_f32_16x16x32_bf16 v[136:139], a[24:27], v[84:87], v[136:139]\n"
      "ds_read_b128 v[84:87], v234 offset:32768\n"
      "v_mfma_f32_16x16x32_bf16 v[136:139], a[28:31], v[88:91], v[136:139]\n"
      "ds_read_b128 v[88:91], v235 offset:32768\n"
      "v_mfma_f32_16x16x32_bf16 v[136:139], a[32:35], v[92:95], v[136:139]\n"
      "ds_read_b128 v[92:95], v232 offset:40960\n"
      "v_mfma_f32_16x16x32_bf16 v[136:139], a[36:39], v[108:111], v[136:139]\n"
      "ds_read_b128 v[108:111], v233 offset:40960\n"
      "v_mfma_f32_16x16x32_bf16 v[136:139], a[40:43], v[112:115], v[136:139]\n"
      "ds_read_b128 v[112:115], v234 offset:40960\n"
      "v_mfma_f32_16x16x32_bf16 v[136:139], a[44:47], v[116:119], v[136:139]\n"
      "ds_read_b128 v[116:119], v235 offset:40960\n"
      "s_branch LJOIN\n"
      "LF_1:\n"
      "s_mul_i32 s62, s62, 0x3000\n"
      "s_sub_u32 s62, s98, s62\n"
      "s_sub_u32 s62, s62, s89\n"
      "v_mad_i32_i24 v238, v251, s62, v238\n"
      "v_mad_i32_i24 v238, v251, s98, v238\n"
      "s_add_u32 s56, s22, 0x0\n"
      "s_addc_u32 s57, s23, 0\n"
      "s_add_u32 m0, s18, 0x0\n"
      "s_nop 0\n"
      "global_load_lds_dwordx4 v238, s[56:57] offset:0 \n"
      "v_mad_i32_i24 v238, v251, s90, v238\n"
      "s_add_u32 s56, s22, 0x6000\n"
      "s_addc_u32 s57, s23, 0\n"
      "s_add_u32 m0, s18, 0x1000\n"
      "s_nop 0\n"
      "global_load_lds_dwordx4 v238, s[56:57] offset:0 \n"
      "v_mad_i32_i24 v238, v251, s91, v238\n"
      "s_add_u32 s56, s22, 0xc000\n"
      "s_addc_u32 s57, s23, 0\n"
      "s_add_u32 m0, s18, 0x2000\n"
      "s_nop 0\n"
      "global_load_lds_dwordx4 v238, s[56:57] offset:0 \n"
      "v_mad_i32_i24 v238, v251, s92, v238\n"
      "s_add_u32 s56, s22, 0x12000\n"
      "s_addc_u32 s57, s23, 0\n"
      "s_add_u32 m0, s18, 0x3000\n"
      "s_nop 0\n"
      "global_load_lds_dwordx4 v238, s[56:57] offset:0 \n"
      "v_mad_i32_i24 v238, v251, s93, v238\n"
      "s_add_u32 s56, s22, 0x18000\n"
      "s_addc_u32 s57, s23, 0\n"
      "s_add_u32 m0, s18, 0x4000\n"
      "s_nop 0\n"
      "global_load_lds_dwordx4 v238, s[56:57] offset:0 \n"
      "v_mad_i32_i24 v238, v251, s94, v238\n"
      "s_add_u32 s56, s22, 0x1e000\n"
      "s_addc_u32 s57, s23, 0\n"
      "s_add_u32 m0, s18, 0x5000\n"
      "s_nop 0\n"
      "global_load_lds_dwordx4 v238, s[56:57] offset:0 \n"
      "v_mad_i32_i24 v238, v251, s95, v238\n"
      "s_add_u32 s56, s22, 0x24000\n"
      "s_addc_u32 s57, s23, 0\n"
      "s_add_u32 m0, s18, 0x6000\n"
      "s_nop 0\n"
      "global_load_lds_dwordx4 v238, s[56:57] offset:0 \n"
      "v_mad_i32_i24 v238, v251, s96, v238\n"
      "s_add_u32 s56, s22, 0x2a000\n"
      "s_addc_u32 s57, s23, 0\n"
      "s_add_u32 m0, s18, 0x7000\n"
      "s_nop 0\n"
      "global_load_lds_dwordx4 v238, s[56:57] offset:0 \n"
      "v_mad_i32_i24 v238, v251, s97, v238\n"
      "s_add_u32 s56, s22, 0x30000\n"
      "s_addc_u32 s57, s23, 0\n"
      "s_add_u32 m0, s18, 0x8000\n"
      "s_nop 0\n"
      "global_load_lds_dwordx4 v238, s[56:57] offset:0 \n"
      "s_branch LR_1\n"
      "LV1:\n"
      "s_add_u32 s78, s64, 0x0\n"
      "s_addc_u32 s79, s65, 0\n"
      "s_add_u32 m0, s74, 0x0\n"
      "s_nop 0\n"
      "global_load_lds_dwordx4 v240, s[78:79]\n"
      "s_add_u32 m0, s74, 0x400\n"
      "s_nop 0\n"
      "global_load_lds_dwordx4 v241, s[78:79]\n"
      "s_add_u32 m0, s74, 0x800\n"
      "s_nop 0\n"
      "global_load_lds_dwordx4 v242, s[78:79]\n"
      "s_add_u32 s78, s64, 0x3000\n"
      "s_addc_u32 s79, s65, 0\n"
      "s_add_u32 m0, s74, 0xc00\n"
      "s_nop 0\n"
      "global_load_lds_dwordx4 v240, s[78:79]\n"
      "s_add_u32 m0, s74, 0x1000\n"
      "s_nop 0\n"
      "global_load_lds_dwordx4 v241, s[78:79]\n"
      "s_add_u32 m0, s74, 0x1400\n"
      "s_nop 0\n"
      "global_load_lds_dwordx4 v242, s[78:79]\n"
      "s_getpc_b64 s[56:57]\n"
      "LTR_1:\n"
      "s_add_u32 s56, s56, LTE_1-LTR_1\n"
      "s_addc_u32 s57, s57, 0\n"
      "s_mov_b64 exec, 0\n"
      "s_cmp_eq_u32 s76, 3\n"
      "s_cbranch_scc1 LPD6\n"
      "s_cmp_eq_u32 s76, 2\n"
      "s_cbranch_scc1 LPD5\n"
      "s_cmp_eq_u32 s76, 1\n"
      "s_cbranch_scc1 LPD4\n"
      "LTE_1:\n"
      "s_mov_b64 exec, -1\n"
      "s_waitcnt vmcnt(17)\n"
      "v_readfirstlane_b32 s80, v252\n"
      "v_readfirstlane_b32 s81, v253\n"
      "s_nop 3\n"
      "s_add_u32 s80, s80, 0x7f4a7c15\n"
      "s_addc_u32 s81, s81, 0x9e3779b9\n"
      "v_mov_b32 v252, s80\n"
      "v_mov_b32 v253, s81\n"
      "v_xor_b32 v116, v112, v110\n"
      "v_xor_b32 v117, v113, v111\n"
      "v_and_or_b32 v116, v116, -2, v117\n"
      "v_cmp_eq_u32 vcc, 0, v116\n"
      "v_cndmask_b32 v116, v110, v112, vcc\n"
      "s_mov_b64 exec, 1\n"
      "ds_write_b32 v108, v116\n"
      "s_mov_b64 exec, -1\n"
      "v_and_b32 v108, -16, v108\n"
      "s_waitcnt lgkmcnt(0)\n"
      "s_barrier\n"
      "ds_read_b32 v116, v108\n"
      "s_waitcnt lgkmcnt(0)\n"
      "v_readfirstlane_b32 s62, v116\n"
      "s_lshr_b32 s63, s51, 2\n"
      "s_nop 2\n"
      "s_xor_b32 s62, s62, s63\n"
      "s_and_b32 s62, s62, 1\n"
      "s_mul_i32 s63, s62, 0x3000\n"
      "s_sub_u32 s98, s98, s63\n"
      "s_lshl_b32 s62, s62, 1\n"
      "s_sub_u32 s62, 1, s62\n"
      "v_mul_i32_i24 v251, s62, v251\n"
      "s_lshl_b32 s19, s62, 1\n"
      "s_mul_i32 s100, s100, s62\n"
      "v_cmp_ne_u32 vcc, v107, v251\n"
      "s_nop 3\n"
      "s_cbranch_vccnz LF_1\n"
      "LR_1:\n"
      "s_nop 0\n"
      "s_add_u32 s78, s64, 0x6000\n"
      "s_addc_u32 s79, s65, 0\n"
      "s_add_u32 m0, s74, 0x6000\n"
      "s_nop 0\n"
      "global_load_lds_dwordx4 v240, s[78:79]\n"
      "s_add_u32 m0, s74, 0x6400\n"
      "s_nop 0\n"
      "global_load_lds_dwordx4 v241, s[78:79]\n"
      "s_add_u32 m0, s74, 0x6800\n"
      "s_nop 0\n"
      "global_load_lds_dwordx4 v242, s[78:79]\n"
      "s_add_u32 s78, s64, 0x9000\n"
      "s_addc_u32 s79, s65, 0\n"
      "s_add_u32 m0, s74, 0x6c00\n"
      "s_nop 0\n"
      "global_load_lds_dwordx4 v240, s[78:79]\n"
      "s_add_u32 m0, s74, 0x7000\n"
      "s_nop 0\n"
      "global_load_lds_dwordx4 v241, s[78:79]\n"
      "s_add_u32 m0, s74, 0x7400\n"
      "s_nop 0\n"
      "global_load_lds_dwordx4 v242, s[78:79]\n"
      "s_add_u32 s78, s64, 0xc000\n"
      "s_addc_u32 s79, s65, 0\n"
      "s_add_u32 m0, s74, 0xc000\n"
      "s_nop 0\n"
      "global_load_lds_dwordx4 v240, s[78:79]\n"
      "s_add_u32 m0, s74, 0xc400\n"
      "s_nop 0\n"
      "global_load_lds_dwordx4 v241, s[78:79]\n"
      "s_add_u32 m0, s74, 0xc800\n"
      "s_nop 0\n"
      "global_load_lds_dwordx4 v242, s[78:79]\n"
      "s_add_u32 s78, s64, 0xf000\n"
      "s_addc_u32 s79, s65, 0\n"
      "s_add_u32 m0, s74, 0xcc00\n"
      "s_nop 0\n"
      "global_load_lds_dwordx4 v240, s[78:79]\n"
      "s_add_u32 m0, s74, 0xd000\n"
      "s_nop 0\n"
      "global_load_lds_dwordx4 v241, s[78:79]\n"
      "s_add_u32 m0, s74, 0xd400\n"
      "s_nop 0\n"
      "global_load_lds_dwordx4 v242, s[78:79]\n"
      "s_add_u32 m0, s17, 0x25000\n"
      "s_nop 0\n"
      "global_load_lds_dwordx4 v239, s[46:47] offset:0\n"
      "s_add_u32 m0, s17, 0x25800\n"
      "s_nop 0\n"
      "global_load_lds_dwordx4 v239, s[46:47] offset:2048\n"
      "s_waitcnt vmcnt(14)\n"
      "ds_read_b64_tr_b16 a[0:1], v243 offset:0\n"
      "ds_read_b64_tr_b16 a[2:3], v243 offset:768\n"
      "ds_read_b64_tr_b16 a[4:5], v243 offset:8\n"
      "ds_read_b64_tr_b16 a[6:7], v243 offset:776\n"
      "ds_read_b64_tr_b16 a[8:9], v243 offset:64\n"
      "ds_read_b64_tr_b16 a[10:11], v243 offset:832\n"
      "ds_read_b64_tr_b16 a[12:13], v243 offset:72\n"
      "ds_read_b64_tr_b16 a[14:15], v243 offset:840\n"
      "ds_read_b64_tr_b16 a[16:17], v243 offset:128\n"
      "ds_read_b64_tr_b16 a[18:19], v243 offset:896\n"
      "ds_read_b64_tr_b16 a[20:21], v243 offset:136\n"
      "ds_read_b64_tr_b16 a[22:23], v243 offset:904\n"
      "s_waitcnt vmcnt(13)\n"
      "s_barrier\n"
      "ds_read_b128 v[216:219], v232 offset:0\n"
      "ds_read_b128 v[220:223], v232 offset:4096\n"
      "ds_read_b128 v[224:227], v232 offset:8192\n"
      "s_waitcnt lgkmcnt(2)\n"
      "v_mfma_f32_16x16x32_bf16 v[0:3], a[0:3], v[216:219], 0\n"
      "s_add_u32 s78, s64, 0x12000\n"
      "s_addc_u32 s79, s65, 0\n"
      "s_add_u32 m0, s74, 0x0\n"
      "v_mfma_f32_16x16x32_bf16 v[4:7], a[4:7], v[216:219], 0\n"
      "global_load_lds_dwordx4 v240, s[78:79]\n"
      "v_mfma_f32_16x16x32_bf16 v[8:11], a[8:11], v[216:219], 0\n"
      "ds_read_b128 v[228:231], v232 offset:12288\n"
      "v_mfma_f32_16x16x32_bf16 v[12:15], a[12:15], v[216:219], 0\n"
      "v_mfma_f32_16x16x32_bf16 v[16:19], a[16:19], v[216:219], 0\n"
      "s_add_u32 m0, s74, 0x400\n"
      "v_mfma_f32_16x16x32_bf16 v[20:23], a[20:23], v[216:219], 0\n"
      "global_load_lds_dwordx4 v241, s[78:79]\n"
      "s_waitcnt lgkmcnt(2)\n"
      "v_mfma_f32_16x16x32_bf16 v[24:27], a[0:3], v[220:223], 0\n"
      "s_waitcnt vmcnt(10)\n"
      "s_cmp_lg_u32 s76, 1\n"
      "s_cbranch_scc1 LA_1\n"
      "v_mov_b32 v244, s77\n"
      "s_mov_b64 exec, 1\n"
      "global_store_dwordx2 v244, v[252:253], s[28:29] sc1\n"
      "s_mov_b64 exec, -1\n"
      "s_nop 4\n"
      "LA_1:\n"
      "v_mfma_f32_16x16x32_bf16 v[28:31], a[4:7], v[220:223], 0\n"
      "ds_read_b64_tr_b16 a[24:25], v243 offset:24576\n"
      "v_mfma_f32_16x16x32_bf16 v[32:35], a[8:11], v[220:223], 0\n"
      "ds_read_b128 v[216:219], v232 offset:16384\n"
      "s_add_u32 m0, s74, 0x800\n"
      "v_mfma_f32_16x16x32_bf16 v[36:39], a[12:15], v[220:223], 0\n"
      "ds_read_b64_tr_b16 a[26:27], v243 offset:25344\n"
      "global_load_lds_dwordx4 v242, s[78:79]\n"
      "v_mfma_f32_16x16x32_bf16 v[40:43], a[16:19], v[220:223], 0\n"
      "ds_read_b64_tr_b16 a[28:29], v243 offset:24584\n"
      "v_mfma_f32_16x16x32_bf16 v[44:47], a[20:23], v[220:223], 0\n"
      "s_waitcnt lgkmcnt(5)\n"
      "v_mfma_f32_16x16x32_bf16 v[48:51], a[0:3], v[224:227], 0\n"
      "ds_read_b64_tr_b16 a[30:31], v243 offset:25352\n"
      "s_add_u32 s78, s64, 0x15000\n"
      "s_addc_u32 s79, s65, 0\n"
      "s_add_u32 m0, s74, 0xc00\n"
      "v_mfma_f32_16x16x32_bf16 v[52:55], a[4:7], v[224:227], 0\n"
      "ds_read_b64_tr_b16 a[32:33], v243 offset:24640\n"
      "global_load_lds_dwordx4 v240, s[78:79]\n"
      "v_mfma_f32_16x16x32_bf16 v[56:59], a[8:11], v[224:227], 0\n"
      "ds_read_b128 v[220:223], v232 offset:20480\n"
      "v_mfma_f32_16x16x32_bf16 v[60:63], a[12:15], v[224:227], 0\n"
      "ds_read_b64_tr_b16 a[34:35], v243 offset:25408\n"
      "v_mfma_f32_16x16x32_bf16 v[64:67], a[16:19], v[224:227], 0\n"
      "ds_read_b64_tr_b16 a[36:37], v243 offset:24648\n"
      "s_add_u32 m0, s74, 0x1000\n"
      "v_mfma_f32_16x16x32_bf16 v[68:71], a[20:23], v[224:227], 0\n"
      "global_load_lds_dwordx4 v241, s[78:79]\n"
      "s_waitcnt lgkmcnt(9)\n"
      "v_mfma_f32_16x16x32_bf16 v[72:75], a[0:3], v[228:231], 0\n"
      "ds_read_b64_tr_b16 a[38:39], v243 offset:25416\n"
      "v_mfma_f32_16x16x32_bf16 v[76:79], a[4:7], v[228:231], 0\n"
      "ds_read_b64_tr_b16 a[40:41], v243 offset:24704\n"
      "v_mfma_f32_16x16x32_bf16 v[80:83], a[8:11], v[228:231], 0\n"
      "ds_read_b128 v[224:227], v232 offset:24576\n"
      "s_add_u32 m0, s74, 0x1400\n"
      "v_mfma_f32_16x16x32_bf16 v[84:87], a[12:15], v[228:231], 0\n"
      "ds_read_b64_tr_b16 a[42:43], v243 offset:25472\n"
      "global_load_lds_dwordx4 v242, s[78:79]\n"
      "v_mfma_f32_16x16x32_bf16 v[88:91], a[16:19], v[228:231], 0\n"
      "ds_read_b64_tr_b16 a[44:45], v243 offset:24712\n"
      "v_mfma_f32_16x16x32_bf16 v[92:95], a[20:23], v[228:231], 0\n"
      "s_waitcnt lgkmcnt(12)\n"
      "v_mfma_f32_16x16x32_bf16 v[96:99], a[0:3], v[216:219], 0\n"
      "ds_read_b64_tr_b16 a[46:47], v243 offset:25480\n"
      "v_mfma_f32_16x16x32_bf16 v[100:103], a[4:7], v[216:219], 0\n"
      "s_waitcnt vmcnt(8)\n"
      "v_mfma_f32_16x16x32_bf16 v[104:107], a[8:11], v[216:219], 0\n"
      "ds_read_b128 v[228:231], v232 offset:28672\n"
      "v_mfma_f32_16x16x32_bf16 v[108:111], a[12:15], v[216:219], 0\n"
      "ds_read_b64_tr_b16 a[48:49], v243 offset:49152\n"
      "v_mfma_f32_16x16x32_bf16 v[112:115], a[16:19], v[216:219], 0\n"
      "ds_read_b64_tr_b16 a[50:51], v243 offset:49920\n"
      "v_mfma_f32_16x16x32_bf16 v[116:119], a[20:23], v[216:219], 0\n"
      "s_waitcnt lgkmcnt(11)\n"
      "v_mfma_f32_16x16x32_bf16 v[120:123], a[0:3], v[220:223], 0\n"
      "ds_read_b64_tr_b16 a[52:53], v243 offset:49160\n"
      "v_mfma_f32_16x16x32_bf16 v[124:127], a[4:7], v[220:223], 0\n"
      "ds_read_b64_tr_b16 a[54:55], v243 offset:49928\n"
      "v_mfma_f32_16x16x32_bf16 v[128:131], a[8:11], v[220:223], 0\n"
      "ds_read_b128 v[216:219], v232 offset:32768\n"
      "v_add_u32 v232, 0x9000, v232\n"
      "v_mfma_f32_16x16x32_bf16 v[132:135], a[12:15], v[220:223], 0\n"
      "ds_read_b64_tr_b16 a[56:57], v243 offset:49216\n"
      "v_mfma_f32_16x16x32_bf16 v[136:139], a[16:19], v[220:223], 0\n"
      "ds_read_b64_tr_b16 a[58:59], v243 offset:49984\n"
      "s_waitcnt lgkmcnt(8)\n"
      "s_add_u32 s78, s64, 0x18000\n"
      "s_addc_u32 s79, s65, 0\n"
      "s_add_u32 m0, s74, 0x6000\n"
      "v_mfma_f32_16x16x32_bf16 v[140:143], a[20:23], v[220:223], 0\n"
      "global_load_lds_dwordx4 v240, s[78:79]\n"
      "v_mfma_f32_16x16x32_bf16 v[144:147], a[0:3], v[224:227], 0\n"
      "ds_read_b64_tr_b16 a[60:61], v243 offset:49224\n"
      "v_mfma_f32_16x16x32_bf16 v[148:151], a[4:7], v[224:227], 0\n"
      "ds_read_b64_tr_b16 a[62:63], v243 offset:49992\n"
      "v_mfma_f32_16x16x32_bf16 v[152:155], a[8:11], v[224:227], 0\n"
      "ds_read_b128 v[220:223], v233 offset:0\n"
      "s_add_u32 m0, s74, 0x6400\n"
      "v_mfma_f32_16x16x32_bf16 v[156:159], a[12:15], v[224:227], 0\n"
      "ds_read_b64_tr_b16 a[64:65], v243 offset:49280\n"
      "global_load_lds_dwordx4 v241, s[78:79]\n"
      "v_mfma_f32_16x16x32_bf16 v[160:163], a[16:19], v[224:227], 0\n"
      "ds_read_b64_tr_b16 a[66:67], v243 offset:50048\n"
      "v_mfma_f32_16x16x32_bf16 v[164:167], a[20:23], v[224:227], 0\n"
      "s_waitcnt lgkmcnt(12)\n"
      "v_mfma_f32_16x16x32_bf16 v[168:171], a[0:3], v[228:231], 0\n"
      "ds_read_b64_tr_b16 a[68:69], v243 offset:49288\n"
      "s_add_u32 m0, s74, 0x6800\n"
      "v_mfma_f32_16x16x32_bf16 v[172:175], a[4:7], v[228:231], 0\n"
      "ds_read_b64_tr_b16 a[70:71], v243 offset:50056\n"
      "global_load_lds_dwordx4 v242, s[78:79]\n"
      "v_mfma_f32_16x16x32_bf16 v[176:179], a[8:11], v[228:231], 0\n"
      "ds_read_b128 v[224:227], v233 offset:4096\n"
      "v_mfma_f32_16x16x32_bf16 v[180:183], a[12:15], v[228:231], 0\n"
      "v_mfma_f32_16x16x32_bf16 v[184:187], a[16:19], v[228:231], 0\n"
      "s_add_u32 s78, s64, 0x1b000\n"
      "s_addc_u32 s79, s65, 0\n"
      "s_add_u32 m0, s74, 0x6c00\n"
      "v_mfma_f32_16x16x32_bf16 v[188:191], a[20:23], v[228:231], 0\n"
      "global_load_lds_dwordx4 v240, s[78:79]\n"
      "s_waitcnt lgkmcnt(10)\n"
      "v_mfma_f32_16x16x32_bf16 v[192:195], a[0:3], v[216:219], 0\n"
      "v_mfma_f32_16x16x32_bf16 v[196:199], a[4:7], v[216:219], 0\n"
      "v_mfma_f32_16x16x32_bf16 v[200:203], a[8:11], v[216:219], 0\n"
      "ds_read_b128 v[228:231], v233 offset:8192\n"
      "s_add_u32 m0, s74, 0x7000\n"
      "v_mfma_f32_16x16x32_bf16 v[204:207], a[12:15], v[216:219], 0\n"
      "global_load_lds_dwordx4 v241, s[78:79]\n"
      "v_mfma_f32_16x16x32_bf16 v[208:211], a[16:19], v[216:219], 0\n"
      "v_mfma_f32_16x16x32_bf16 v[212:215], a[20:23], v[216:219], 0\n"
      "s_waitcnt lgkmcnt(6)\n"
      "v_mfma_f32_16x16x32_bf16 v[0:3], a[24:27], v[220:223], v[0:3]\n"
      "s_add_u32 m0, s74, 0x7400\n"
      "v_mfma_f32_16x16x32_bf16 v[4:7], a[28:31], v[220:223], v[4:7]\n"
      "global_load_lds_dwordx4 v242, s[78:79]\n"
      "v_mfma_f32_16x16x32_bf16 v[8:11], a[32:35], v[220:223], v[8:11]\n"
      "ds_read_b128 v[216:219], v233 offset:12288\n"
      "s_waitcnt vmcnt(6)\n"
      "v_mfma_f32_16x16x32_bf16 v[12:15], a[36:39], v[220:223], v[12:15]\n"
      "ds_read_b64_tr_b16 a[72:73], v243 offset:0\n"
      "v_mfma_f32_16x16x32_bf16 v[16:19], a[40:43], v[220:223], v[16:19]\n"
      "ds_read_b64_tr_b16 a[74:75], v243 offset:768\n"
      "v_mad_i32_i24 v238, v251, s89, v238\n"
      "s_add_u32 s56, s22, 0x0\n"
      "s_addc_u32 s57, s23, 0\n"
      "s_add_u32 m0, s18, 0x8f00\n"
      "v_mfma_f32_16x16x32_bf16 v[20:23], a[44:47], v[220:223], v[20:23]\n"
      "global_load_lds_dwordx4 v238, s[56:57] offset:256 \n"
      "s_waitcnt lgkmcnt(4)\n"
      "v_mfma_f32_16x16x32_bf16 v[24:27], a[24:27], v[224:227], v[24:27]\n"
      "ds_read_b64_tr_b16 a[76:77], v243 offset:8\n"
      "v_mfma_f32_16x16x32_bf16 v[28:31], a[28:31], v[224:227], v[28:31]\n"
      "ds_read_b64_tr_b16 a[78:79], v243 offset:776\n"
      "v_mad_i32_i24 v238, v251, s90, v238\n"
      "s_add_u32 s56, s22, 0x6000\n"
      "s_addc_u32 s57, s23, 0\n"
      "s_add_u32 m0, s18, 0x9f00\n"
      "v_mfma_f32_16x16x32_bf16 v[32:35], a[32:35], v[224:227], v[32:35]\n"
      "ds_read_b128 v[220:223], v233 offset:16384\n"
      "global_load_lds_dwordx4 v238, s[56:57] offset:256 \n"
      "v_mfma_f32_16x16x32_bf16 v[36:39], a[36:39], v[224:227], v[36:39]\n"
      "ds_read_b64_tr_b16 a[80:81], v243 offset:64\n"
      "v_mfma_f32_16x16x32_bf16 v[40:43], a[40:43], v[224:227], v[40:43]\n"
      "ds_read_b64_tr_b16 a[82:83], v243 offset:832\n"
      "v_mad_i32_i24 v238, v251, s91, v238\n"
      "s_add_u32 s56, s22, 0xc000\n"
      "s_addc_u32 s57, s23, 0\n"
      "s_add_u32 m0, s18, 0xaf00\n"
      "v_mfma_f32_16x16x32_bf16 v[44:47], a[44:47], v[224:227], v[44:47]\n"
      "global_load_lds_dwordx4 v238, s[56:57] offset:256 \n"
      "s_waitcnt lgkmcnt(8)\n"
      "v_mfma_f32_16x16x32_bf16 v[48:51], a[24:27], v[228:231], v[48:51]\n"
      "ds_read_b64_tr_b16 a[84:85], v243 offset:72\n"
      "v_mfma_f32_16x16x32_bf16 v[52:55], a[28:31], v[228:231], v[52:55]\n"
      "ds_read_b64_tr_b16 a[86:87], v243 offset:840\n"
      "v_mad_i32_i24 v238, v251, s92, v238\n"
      "s_add_u32 s56, s22, 0x12000\n"
      "s_addc_u32 s57, s23, 0\n"
      "s_add_u32 m0, s18, 0xbf00\n"
      "v_mfma_f32_16x16x32_bf16 v[56:59], a[32:35], v[228:231], v[56:59]\n"
      "ds_read_b128 v[224:227], v233 offset:20480\n"
      "global_load_lds_dwordx4 v238, s[56:57] offset:256 \n"
      "v_mfma_f32_16x16x32_bf16 v[60:63], a[36:39], v[228:231], v[60:63]\n"
      "ds_read_b64_tr_b16 a[88:89], v243 offset:128\n"
      "v_mfma_f32_16x16x32_bf16 v[64:67], a[40:43], v[228:231], v[64:67]\n"
      "ds_read_b64_tr_b16 a[90:91], v243 offset:896\n"
      "v_mad_i32_i24 v238, v251, s93, v238\n"
      "s_add_u32 s56, s22, 0x18000\n"
      "s_addc_u32 s57, s23, 0\n"
      "s_add_u32 m0, s18, 0xcf00\n"
      "v_mfma_f32_16x16x32_bf16 v[68:71], a[44:47], v[228:231], v[68:71]\n"
      "global_load_lds_dwordx4 v238, s[56:57] offset:256 \n"
      "s_waitcnt lgkmcnt(12)\n"
      "v_mfma_f32_16x16x32_bf16 v[72:75], a[24:27], v[216:219], v[72:75]\n"
      "ds_read_b64_tr_b16 a[92:93], v243 offset:136\n"
      "v_mfma_f32_16x16x32_bf16 v[76:79], a[28:31], v[216:219], v[76:79]\n"
      "ds_read_b64_tr_b16 a[94:95], v243 offset:904\n"
      "v_mad_i32_i24 v238, v251, s94, v238\n"
      "s_add_u32 s56, s22, 0x1e000\n"
      "s_addc_u32 s57, s23, 0\n"
      "s_add_u32 m0, s18, 0xdf00\n"
      "v_mfma_f32_16x16x32_bf16 v[80:83], a[32:35], v[216:219], v[80:83]\n"
      "ds_read_b128 v[228:231], v233 offset:24576\n"
      "global_load_lds_dwordx4 v238, s[56:57] offset:256 \n"
      "v_mfma_f32_16x16x32_bf16 v[84:87], a[36:39], v[216:219], v[84:87]\n"
      "v_mfma_f32_16x16x32_bf16 v[88:91], a[40:43], v[216:219], v[88:91]\n"
      "v_mad_i32_i24 v238, v251, s95, v238\n"
      "s_add_u32 s56, s22, 0x24000\n"
      "s_addc_u32 s57, s23, 0\n"
      "s_add_u32 m0, s18, 0xef00\n"
      "v_mfma_f32_16x16x32_bf16 v[92:95], a[44:47], v[216:219], v[92:95]\n"
      "global_load_lds_dwordx4 v238, s[56:57] offset:256 \n"
      "s_waitcnt lgkmcnt(10)\n"
      "v_mfma_f32_16x16x32_bf16 v[96:99], a[24:27], v[220:223], v[96:99]\n"
      "v_mfma_f32_16x16x32_bf16 v[100:103], a[28:31], v[220:223], v[100:103]\n"
      "v_mad_i32_i24 v238, v251, s96, v238\n"
      "s_add_u32 s56, s22, 0x2a000\n"
      "s_addc_u32 s57, s23, 0\n"
      "s_add_u32 m0, s18, 0xff00\n"
      "v_mfma_f32_16x16x32_bf16 v[104:107], a[32:35], v[220:223], v[104:107]\n"
      "ds_read_b128 v[216:219], v233 offset:28672\n"
      "global_load_lds_dwordx4 v238, s[56:57] offset:256 \n"
      "v_mfma_f32_16x16x32_bf16 v[108:111], a[36:39], v[220:223], v[108:111]\n"
      "v_mfma_f32_16x16x32_bf16 v[112:115], a[40:43], v[220:223], v[112:115]\n"
      "v_mad_i32_i24 v238, v251, s97, v238\n"
      "s_add_u32 s56, s22, 0x30000\n"
      "s_addc_u32 s57, s23, 0\n"
      "s_add_u32 m0, s18, 0x10f00\n"
      "v_mfma_f32_16x16x32_bf16 v[116:119], a[44:47], v[220:223], v[116:119]\n"
      "global_load_lds_dwordx4 v238, s[56:57] offset:256 \n"
      "s_waitcnt lgkmcnt(6)\n"
      "v_mfma_f32_16x16x32_bf16 v[120:123], a[24:27], v[224:227], v[120:123]\n"
      "v_mfma_f32_16x16x32_bf16 v[124:127], a[28:31], v[224:227], v[124:127]\n"
      "v_mfma_f32_16x16x32_bf16 v[128:131], a[32:35], v[224:227], v[128:131]\n"
      "ds_read_b128 v[220:223], v233 offset:32768\n"
      "v_add_u32 v233, 0x9000, v233\n"
      "s_add_u32 s78, s64, 0x1e000\n"
      "s_addc_u32 s79, s65, 0\n"
      "s_add_u32 m0, s74, 0xc000\n"
      "v_mfma_f32_16x16x32_bf16 v[132:135], a[36:39], v[224:227], v[132:135]\n"
      "global_load_lds_dwordx4 v240, s[78:79]\n"
      "v_mfma_f32_16x16x32_bf16 v[136:139], a[40:43], v[224:227], v[136:139]\n"
      "v_mfma_f32_16x16x32_bf16 v[140:143], a[44:47], v[224:227], v[140:143]\n"
      "s_waitcnt lgkmcnt(2)\n"
      "v_mfma_f32_16x16x32_bf16 v[144:147], a[24:27], v[228:231], v[144:147]\n"
      "s_waitcnt vmcnt(10)\n"
      "s_add_u32 m0, s74, 0xc400\n"
      "v_mfma_f32_16x16x32_bf16 v[148:151], a[28:31], v[228:231], v[148:151]\n"
      "ds_read_b64_tr_b16 a[96:97], v243 offset:24576\n"
      "global_load_lds_dwordx4 v241, s[78:79]\n"
      "v_mfma_f32_16x16x32_bf16 v[152:155], a[32:35], v[228:231], v[152:155]\n"
      "ds_read_b128 v[224:227], v234 offset:0\n"
      "v_mfma_f32_16x16x32_bf16 v[156:159], a[36:39], v[228:231], v[156:159]\n"
      "ds_read_b64_tr_b16 a[98:99], v243 offset:25344\n"
      "v_mfma_f32_16x16x32_bf16 v[160:163], a[40:43], v[228:231], v[160:163]\n"
      "ds_read_b64_tr_b16 a[100:101], v243 offset:24584\n"
      "s_add_u32 m0, s74, 0xc800\n"
      "v_mfma_f32_16x16x32_bf16 v[164:167], a[44:47], v[228:231], v[164:167]\n"
      "global_load_lds_dwordx4 v242, s[78:79]\n"
      "s_waitcnt lgkmcnt(5)\n"
      "v_mfma_f32_16x16x32_bf16 v[168:171], a[24:27], v[216:219], v[168:171]\n"
      "ds_read_b64_tr_b16 a[102:103], v243 offset:25352\n"
      "v_mfma_f32_16x16x32_bf16 v[172:175], a[28:31], v[216:219], v[172:175]\n"
      "ds_read_b64_tr_b16 a[104:105], v243 offset:24640\n"
      "v_mfma_f32_16x16x32_bf16 v[176:179], a[32:35], v[216:219], v[176:179]\n"
      "ds_read_b128 v[228:231], v234 offset:4096\n"
      "s_add_u32 s78, s64, 0x21000\n"
      "s_addc_u32 s79, s65, 0\n"
      "s_add_u32 m0, s74, 0xcc00\n"
      "v_mfma_f32_16x16x32_bf16 v[180:183], a[36:39], v[216:219], v[180:183]\n"
      "ds_read_b64_tr_b16 a[106:107], v243 offset:25408\n"
      "global_load_lds_dwordx4 v240, s[78:79]\n"
      "v_mfma_f32_16x16x32_bf16 v[184:187], a[40:43], v[216:219], v[184:187]\n"
      "LPD4:\n"
      "s_cbranch_execz LPD9\n"
      "ds_read_b64_tr_b16 a[108:109], v243 offset:24648\n"
      "v_mfma_f32_16x16x32_bf16 v[188:191], a[44:47], v[216:219], v[188:191]\n"
      "s_waitcnt lgkmcnt(9)\n"
      "v_mfma_f32_16x16x32_bf16 v[192:195], a[24:27], v[220:223], v[192:195]\n"
      "ds_read_b64_tr_b16 a[110:111], v243 offset:25416\n"
      "s_add_u32 m0, s74, 0xd000\n"
      "v_mfma_f32_16x16x32_bf16 v[196:199], a[28:31], v[220:223], v[196:199]\n"
      "ds_read_b64_tr_b16 a[112:113], v243 offset:24704\n"
      "global_load_lds_dwordx4 v241, s[78:79]\n"
      "v_mfma_f32_16x16x32_bf16 v[200:203], a[32:35], v[220:223], v[200:203]\n"
      "ds_read_b128 v[216:219], v234 offset:8192\n"
      "v_mfma_f32_16x16x32_bf16 v[204:207], a[36:39], v[220:223], v[204:207]\n"
      "ds_read_b64_tr_b16 a[114:115], v243 offset:25472\n"
      "v_mfma_f32_16x16x32_bf16 v[208:211], a[40:43], v[220:223], v[208:211]\n"
      "ds_read_b64_tr_b16 a[116:117], v243 offset:24712\n"
      "s_add_u32 m0, s74, 0xd400\n"
      "v_mfma_f32_16x16x32_bf16 v[212:215], a[44:47], v[220:223], v[212:215]\n"
      "global_load_lds_dwordx4 v242, s[78:79]\n"
      "s_waitcnt lgkmcnt(12)\n"
      "v_mfma_f32_16x16x32_bf16 v[0:3], a[48:51], v[224:227], v[0:3]\n"
      "ds_read_b64_tr_b16 a[118:119], v243 offset:25480\n"
      "v_mfma_f32_16x16x32_bf16 v[4:7], a[52:55], v[224:227], v[4:7]\n"
      "v_mfma_f32_16x16x32_bf16 v[8:11], a[56:59], v[224:227], v[8:11]\n"
      "ds_read_b128 v[220:223], v234 offset:12288\n"
      "s_add_u32 s78, s64, 0x24000\n"
      "s_addc_u32 s79, s65, 0\n"
      "s_add_u32 m0, s74, 0x0\n"
      "v_mfma_f32_16x16x32_bf16 v[12:15], a[60:63], v[224:227], v[12:15]\n"
      "global_load_lds_dwordx4 v240, s[78:79]\n"
      "v_mfma_f32_16x16x32_bf16 v[16:19], a[64:67], v[224:227], v[16:19]\n"
      "v_mfma_f32_16x16x32_bf16 v[20:23], a[68:71], v[224:227], v[20:23]\n"
      "s_waitcnt lgkmcnt(9)\n"
      "v_mfma_f32_16x16x32_bf16 v[24:27], a[48:51], v[228:231], v[24:27]\n"
      "s_add_u32 m0, s74, 0x400\n"
      "v_mfma_f32_16x16x32_bf16 v[28:31], a[52:55], v[228:231], v[28:31]\n"
      "global_load_lds_dwordx4 v241, s[78:79]\n"
      "v_mfma_f32_16x16x32_bf16 v[32:35], a[56:59], v[228:231], v[32:35]\n"
      "ds_read_b128 v[224:227], v234 offset:16384\n"
      "v_mfma_f32_16x16x32_bf16 v[36:39], a[60:63], v[228:231], v[36:39]\n"
      "v_mfma_f32_16x16x32_bf16 v[40:43], a[64:67], v[228:231], v[40:43]\n"
      "s_add_u32 m0, s74, 0x800\n"
      "v_mfma_f32_16x16x32_bf16 v[44:47], a[68:71], v[228:231], v[44:47]\n"
      "global_load_lds_dwordx4 v242, s[78:79]\n"
      "s_waitcnt lgkmcnt(5)\n"
      "v_mfma_f32_16x16x32_bf16 v[48:51], a[48:51], v[216:219], v[48:51]\n"
      "v_mfma_f32_16x16x32_bf16 v[52:55], a[52:55], v[216:219], v[52:55]\n"
      "v_mfma_f32_16x16x32_bf16 v[56:59], a[56:59], v[216:219], v[56:59]\n"
      "ds_read_b128 v[228:231], v234 offset:20480\n"
      "s_add_u32 s78, s64, 0x27000\n"
      "s_addc_u32 s79, s65, 0\n"
      "s_add_u32 m0, s74, 0xc00\n"
      "v_mfma_f32_16x16x32_bf16 v[60:63], a[60:63], v[216:219], v[60:63]\n"
      "global_load_lds_dwordx4 v240, s[78:79]\n"
      "v_mfma_f32_16x16x32_bf16 v[64:67], a[64:67], v[216:219], v[64:67]\n"
      "v_mfma_f32_16x16x32_bf16 v[68:71], a[68:71], v[216:219], v[68:71]\n"
      "s_waitcnt lgkmcnt(2)\n"
      "v_mfma_f32_16x16x32_bf16 v[72:75], a[48:51], v[220:223], v[72:75]\n"
      "s_add_u32 m0, s74, 0x1000\n"
      "v_mfma_f32_16x16x32_bf16 v[76:79], a[52:55], v[220:223], v[76:79]\n"
      "global_load_lds_dwordx4 v241, s[78:79]\n"
      "v_mfma_f32_16x16x32_bf16 v[80:83], a[56:59], v[220:223], v[80:83]\n"
      "ds_read_b128 v[216:219], v234 offset:24576\n"
      "v_mfma_f32_16x16x32_bf16 v[84:87], a[60:63], v[220:223], v[84:87]\n"
      "v_mfma_f32_16x16x32_bf16 v[88:91], a[64:67], v[220:223], v[88:91]\n"
      "s_add_u32 m0, s74, 0x1400\n"
      "v_mfma_f32_16x16x32_bf16 v[92:95], a[68:71], v[220:223], v[92:95]\n"
      "global_load_lds_dwordx4 v242, s[78:79]\n"
      "s_waitcnt lgkmcnt(2)\n"
      "v_mfma_f32_16x16x32_bf16 v[96:99], a[48:51], v[224:227], v[96:99]\n"
      "v_mfma_f32_16x16x32_bf16 v[100:103], a[52:55], v[224:227], v[100:103]\n"
      "v_mfma_f32_16x16x32_bf16 v[104:107], a[56:59], v[224:227], v[104:107]\n"
      "ds_read_b128 v[220:223], v234 offset:28672\n"
      "s_add_u32 s78, s64, 0x2a000\n"
      "s_addc_u32 s79, s65, 0\n"
      "s_add_u32 m0, s74, 0x6000\n"
      "v_mfma_f32_16x16x32_bf16 v[108:111], a[60:63], v[224:227], v[108:111]\n"
      "global_load_lds_dwordx4 v240, s[78:79]\n"
      "v_mfma_f32_16x16x32_bf16 v[112:115], a[64:67], v[224:227], v[112:115]\n"
      "v_mfma_f32_16x16x32_bf16 v[116:119], a[68:71], v[224:227], v[116:119]\n"
      "s_waitcnt lgkmcnt(2)\n"
      "v_mfma_f32_16x16x32_bf16 v[120:123], a[48:51], v[228:231], v[120:123]\n"
      "s_add_u32 m0, s74, 0x6400\n"
      "v_mfma_f32_16x16x32_bf16 v[124:127], a[52:55], v[228:231], v[124:127]\n"
      "global_load_lds_dwordx4 v241, s[78:79]\n"
      "v_mfma_f32_16x16x32_bf16 v[128:131], a[56:59], v[228:231], v[128:131]\n"
      "ds_read_b128 v[224:227], v234 offset:32768\n"
      "v_add_u32 v234, 0x9000, v234\n"
      "v_mfma_f32_16x16x32_bf16 v[132:135], a[60:63], v[228:231], v[132:135]\n"
      "v_mfma_f32_16x16x32_bf16 v[136:139], a[64:67], v[228:231], v[136:139]\n"
      "s_waitcnt vmcnt(8)\n"
      "s_add_u32 m0, s74, 0x6800\n"
      "v_mfma_f32_16x16x32_bf16 v[140:143], a[68:71], v[228:231], v[140:143]\n"
      "global_load_lds_dwordx4 v242, s[78:79]\n"
      "s_waitcnt lgkmcnt(2)\n"
      "v_mfma_f32_16x16x32_bf16 v[144:147], a[48:51], v[216:219], v[144:147]\n"
      "ds_read_b64_tr_b16 a[120:121], v243 offset:49152\n"
      "v_mfma_f32_16x16x32_bf16 v[148:151], a[52:55], v[216:219], v[148:151]\n"
      "ds_read_b64_tr_b16 a[122:123], v243 offset:49920\n"
      "v_mfma_f32_16x16x32_bf16 v[152:155], a[56:59], v[216:219], v[152:155]\n"
      "ds_read_b128 v[228:231], v235 offset:0\n"
      "s_add_u32 s78, s64, 0x2d000\n"
      "s_addc_u32 s79, s65, 0\n"
      "s_add_u32 m0, s74, 0x6c00\n"
      "v_mfma_f32_16x16x32_bf16 v[156:159], a[60:63], v[216:219], v[156:159]\n"
      "ds_read_b64_tr_b16 a[124:125], v243 offset:49160\n"
      "global_load_lds_dwordx4 v240, s[78:79]\n"
      "v_mfma_f32_16x16x32_bf16 v[160:163], a[64:67], v[216:219], v[160:163]\n"
      "ds_read_b64_tr_b16 a[126:127], v243 offset:49928\n"
      "v_mfma_f32_16x16x32_bf16 v[164:167], a[68:71], v[216:219], v[164:167]\n"
      "s_waitcnt lgkmcnt(6)\n"
      "v_mfma_f32_16x16x32_bf16 v[168:171], a[48:51], v[220:223], v[168:171]\n"
      "ds_read_b64_tr_b16 a[128:129], v243 offset:49216\n"
      "s_add_u32 m0, s74, 0x7000\n"
      "v_mfma_f32_16x16x32_bf16 v[172:175], a[52:55], v[220:223], v[172:175]\n"
      "ds_read_b64_tr_b16 a[130:131], v243 offset:49984\n"
      "global_load_lds_dwordx4 v241, s[78:79]\n"
      "v_mfma_f32_16x16x32_bf16 v[176:179], a[56:59], v[220:223], v[176:179]\n"
      "ds_read_b128 v[216:219], v235 offset:4096\n"
      "v_mfma_f32_16x16x32_bf16 v[180:183], a[60:63], v[220:223], v[180:183]\n"
      "ds_read_b64_tr_b16 a[132:133], v243 offset:49224\n"
      "v_mfma_f32_16x16x32_bf16 v[184:187], a[64:67], v[220:223], v[184:187]\n"
      "ds_read_b64_tr_b16 a[134:135], v243 offset:49992\n"
      "s_add_u32 m0, s74, 0x7400\n"
      "v_mfma_f32_16x16x32_bf16 v[188:191], a[68:71], v[220:223], v[188:191]\n"
      "global_load_lds_dwordx4 v242, s[78:79]\n"
      "s_waitcnt lgkmcnt(10)\n"
      "v_mfma_f32_16x16x32_bf16 v[192:195], a[48:51], v[224:227], v[192:195]\n"
      "ds_read_b64_tr_b16 a[136:137], v243 offset:49280\n"
      "v_mfma_f32_16x16x32_bf16 v[196:199], a[52:55], v[224:227], v[196:199]\n"
      "ds_read_b64_tr_b16 a[138:139], v243 offset:50048\n"
      "v_mfma_f32_16x16x32_bf16 v[200:203], a[56:59], v[224:227], v[200:203]\n"
      "ds_read_b128 v[220:223], v235 offset:8192\n"
      "global_load_dwordx4 v[244:247], v248, s[28:29] sc1\n"
      "v_mfma_f32_16x16x32_bf16 v[204:207], a[60:63], v[224:227], v[204:207]\n"
      "ds_read_b64_tr_b16 a[140:141], v243 offset:49288\n"
      "v_mfma_f32_16x16x32_bf16 v[208:211], a[64:67], v[224:227], v[208:211]\n"
      "ds_read_b64_tr_b16 a[142:143], v243 offset:50056\n"
      "v_mfma_f32_16x16x32_bf16 v[212:215], a[68:71], v[224:227], v[212:215]\n"
      "s_waitcnt lgkmcnt(12)\n"
      "v_mfma_f32_16x16x32_bf16 v[0:3], a[72:75], v[228:231], v[0:3]\n"
      "v_mfma_f32_16x16x32_bf16 v[4:7], a[76:79], v[228:231], v[4:7]\n"
      "v_mfma_f32_16x16x32_bf16 v[8:11], a[80:83], v[228:231], v[8:11]\n"
      "ds_read_b128 v[224:227], v235 offset:12288\n"
      "v_mfma_f32_16x16x32_bf16 v[12:15], a[84:87], v[228:231], v[12:15]\n"
      "v_mfma_f32_16x16x32_bf16 v[16:19], a[88:91], v[228:231], v[16:19]\n"
      "s_waitcnt vmcnt(7)\n"
      "v_mfma_f32_16x16x32_bf16 v[20:23], a[92:95], v[228:231], v[20:23]\n"
      "s_waitcnt lgkmcnt(8)\n"
      "v_mfma_f32_16x16x32_bf16 v[24:27], a[72:75], v[216:219], v[24:27]\n"
      "ds_read_b64_tr_b16 a[144:145], v243 offset:0\n"
      "v_mfma_f32_16x16x32_bf16 v[28:31], a[76:79], v[216:219], v[28:31]\n"
      "ds_read_b64_tr_b16 a[146:147], v243 offset:768\n"
      "v_mfma_f32_16x16x32_bf16 v[32:35], a[80:83], v[216:219], v[32:35]\n"
      "ds_read_b128 v[228:231], v235 offset:16384\n"
      "s_waitcnt lgkmcnt(4)\n"
      "s_add_u32 s78, s64, 0x30000\n"
      "s_addc_u32 s79, s65, 0\n"
      "s_add_u32 m0, s74, 0xc000\n"
      "v_mfma_f32_16x16x32_bf16 v[36:39], a[84:87], v[216:219], v[36:39]\n"
      "ds_read_b64_tr_b16 a[148:149], v243 offset:8\n"
      "global_load_lds_dwordx4 v240, s[78:79]\n"
      "v_mfma_f32_16x16x32_bf16 v[40:43], a[88:91], v[216:219], v[40:43]\n"
      "ds_read_b64_tr_b16 a[150:151], v243 offset:776\n"
      "v_mfma_f32_16x16x32_bf16 v[44:47], a[92:95], v[216:219], v[44:47]\n"
      "v_mfma_f32_16x16x32_bf16 v[48:51], a[72:75], v[220:223], v[48:51]\n"
      "ds_read_b64_tr_b16 a[152:153], v243 offset:64\n"
      "s_add_u32 m0, s74, 0xc400\n"
      "v_mfma_f32_16x16x32_bf16 v[52:55], a[76:79], v[220:223], v[52:55]\n"
      "ds_read_b64_tr_b16 a[154:155], v243 offset:832\n"
      "global_load_lds_dwordx4 v241, s[78:79]\n"
      "v_mfma_f32_16x16x32_bf16 v[56:59], a[80:83], v[220:223], v[56:59]\n"
      "ds_read_b128 v[216:219], v235 offset:20480\n"
      "v_mfma_f32_16x16x32_bf16 v[60:63], a[84:87], v[220:223], v[60:63]\n"
      "ds_read_b64_tr_b16 a[156:157], v243 offset:72\n"
      "v_mfma_f32_16x16x32_bf16 v[64:67], a[88:91], v[220:223], v[64:67]\n"
      "ds_read_b64_tr_b16 a[158:159], v243 offset:840\n"
      "s_add_u32 m0, s74, 0xc800\n"
      "v_mfma_f32_16x16x32_bf16 v[68:71], a[92:95], v[220:223], v[68:71]\n"
      "global_load_lds_dwordx4 v242, s[78:79]\n"
      "s_waitcnt lgkmcnt(10)\n"
      "v_mfma_f32_16x16x32_bf16 v[72:75], a[72:75], v[224:227], v[72:75]\n"
      "ds_read_b64_tr_b16 a[160:161], v243 offset:128\n"
      "v_mfma_f32_16x16x32_bf16 v[76:79], a[76:79], v[224:227], v[76:79]\n"
      "ds_read_b64_tr_b16 a[162:163], v243 offset:896\n"
      "v_mfma_f32_16x16x32_bf16 v[80:83], a[80:83], v[224:227], v[80:83]\n"
      "ds_read_b128 v[220:223], v235 offset:24576\n"
      "s_add_u32 s78, s64, 0x33000\n"
      "s_addc_u32 s79, s65, 0\n"
      "s_add_u32 m0, s74, 0xcc00\n"
      "v_mfma_f32_16x16x32_bf16 v[84:87], a[84:87], v[224:227], v[84:87]\n"
      "ds_read_b64_tr_b16 a[164:165], v243 offset:136\n"
      "global_load_lds_dwordx4 v240, s[78:79]\n"
      "v_mfma_f32_16x16x32_bf16 v[88:91], a[88:91], v[224:227], v[88:91]\n"
      "ds_read_b64_tr_b16 a[166:167], v243 offset:904\n"
      "v_mfma_f32_16x16x32_bf16 v[92:95], a[92:95], v[224:227], v[92:95]\n"
      "s_waitcnt lgkmcnt(12)\n"
      "v_mfma_f32_16x16x32_bf16 v[96:99], a[72:75], v[228:231], v[96:99]\n"
      "s_add_u32 m0, s74, 0xd000\n"
      "v_mfma_f32_16x16x32_bf16 v[100:103], a[76:79], v[228:231], v[100:103]\n"
      "global_load_lds_dwordx4 v241, s[78:79]\n"
      "v_mfma_f32_16x16x32_bf16 v[104:107], a[80:83], v[228:231], v[104:107]\n"
      "ds_read_b128 v[224:227], v235 offset:28672\n"
      "v_mfma_f32_16x16x32_bf16 v[108:111], a[84:87], v[228:231], v[108:111]\n"
      "v_mfma_f32_16x16x32_bf16 v[112:115], a[88:91], v[228:231], v[112:115]\n"
      "s_waitcnt vmcnt(6)\n"
      "s_add_u32 m0, s74, 0xd400\n"
      "v_mfma_f32_16x16x32_bf16 v[116:119], a[92:95], v[228:231], v[116:119]\n"
      "global_load_lds_dwordx4 v242, s[78:79]\n"
      "s_waitcnt lgkmcnt(8)\n"
      "v_mfma_f32_16x16x32_bf16 v[120:123], a[72:75], v[216:219], v[120:123]\n"
      "ds_read_b64_tr_b16 a[168:169], v243 offset:24576\n"
      "v_mfma_f32_16x16x32_bf16 v[124:127], a[76:79], v[216:219], v[124:127]\n"
      "ds_read_b64_tr_b16 a[170:171], v243 offset:25344\n"
      "v_mfma_f32_16x16x32_bf16 v[128:131], a[80:83], v[216:219], v[128:131]\n"
      "ds_read_b128 v[228:231], v235 offset:32768\n"
      "v_add_u32 v235, 0x9000, v235\n"
      "s_waitcnt lgkmcnt(4)\n"
      "s_add_u32 s78, s64, 0x36000\n"
      "s_addc_u32 s79, s65, 0\n"
      "s_add_u32 m0, s74, 0x0\n"
      "v_mfma_f32_16x16x32_bf16 v[132:135], a[84:87], v[216:219], v[132:135]\n"
      "ds_read_b64_tr_b16 a[172:173], v243 offset:24584\n"
      "global_load_lds_dwordx4 v240, s[78:79]\n"
      "v_mfma_f32_16x16x32_bf16 v[136:139], a[88:91], v[216:219], v[136:139]\n"
      "ds_read_b64_tr_b16 a[174:175], v243 offset:25352\n"
      "v_mfma_f32_16x16x32_bf16 v[140:143], a[92:95], v[216:219], v[140:143]\n"
      "v_mfma_f32_16x16x32_bf16 v[144:147], a[72:75], v[220:223], v[144:147]\n"
      "ds_read_b64_tr_b16 a[176:177], v243 offset:24640\n"
      "s_waitcnt vmcnt(7)\n"
      "v_cmp_ne_u64 vcc, s[80:81], v[244:245]\n"
      "v_cmp_ne_u64 s[56:57], s[80:81], v[246:247]\n"
      "s_nop 3\n"
      "s_or_b64 vcc, vcc, s[56:57]\n"
      "s_cbranch_scc0 LP1_1\n"
      "s_mov_b32 s82, 0x40000\n"
      "LP0_1:\n"
      "s_sleep 2\n"
      "global_load_dwordx4 v[244:247], v248, s[28:29] sc1\n"
      "s_sub_u32 s82, s82, 1\n"
      "s_cbranch_scc1 LP2_1\n"
      "s_waitcnt vmcnt(0)\n"
      "v_cmp_ne_u64 vcc, s[80:81], v[244:245]\n"
      "v_cmp_ne_u64 s[56:57], s[80:81], v[246:247]\n"
      "s_nop 3\n"
      "s_or_b64 vcc, vcc, s[56:57]\n"
      "s_cbranch_scc1 LP0_1\n"
      "LP2_1:\n"
      "s_waitcnt vmcnt(0)\n"
      "LP1_1:\n"
      "s_nop 0\n"
      "v_mfma_f32_16x16x32_bf16 v[148:151], a[76:79], v[220:223], v[148:151]\n"
      "s_barrier\n"
      "s_add_u32 m0, s74, 0x400\n"
      "v_mfma_f32_16x16x32_bf16 v[152:155], a[80:83], v[220:223], v[152:155]\n"
      "ds_read_b128 v[216:219], v232 offset:0\n"
      "global_load_lds_dwordx4 v241, s[78:79]\n"
      "v_mfma_f32_16x16x32_bf16 v[156:159], a[84:87], v[220:223], v[156:159]\n"
      "ds_read_b64_tr_b16 a[178:179], v243 offset:25408\n"
      "v_mfma_f32_16x16x32_bf16 v[160:163], a[88:91], v[220:223], v[160:163]\n"
      "ds_read_b64_tr_b16 a[180:181], v243 offset:24648\n"
      "v_mfma_f32_16x16x32_bf16 v[164:167], a[92:95], v[220:223], v[164:167]\n"
      "s_add_u32 m0, s74, 0x800\n"
      "s_waitcnt lgkmcnt(9)\n"
      "v_mfma_f32_16x16x32_bf16 v[168:171], a[72:75], v[224:227], v[168:171]\n"
      "ds_read_b64_tr_b16 a[182:183], v243 offset:25416\n"
      "global_load_lds_dwordx4 v242, s[78:79]\n"
      "v_mfma_f32_16x16x32_bf16 v[172:175], a[76:79], v[224:227], v[172:175]\n"
      "ds_read_b64_tr_b16 a[184:185], v243 offset:24704\n"
      "v_mfma_f32_16x16x32_bf16 v[176:179], a[80:83], v[224:227], v[176:179]\n"
      "ds_read_b128 v[220:223], v232 offset:4096\n"
      "v_mfma_f32_16x16x32_bf16 v[180:183], a[84:87], v[224:227], v[180:183]\n"
      "ds_read_b64_tr_b16 a[186:187], v243 offset:25472\n"
      "s_add_u32 s78, s64, 0x39000\n"
      "s_addc_u32 s79, s65, 0\n"
      "s_add_u32 m0, s74, 0xc00\n"
      "v_mfma_f32_16x16x32_bf16 v[184:187], a[88:91], v[224:227], v[184:187]\n"
      "ds_read_b64_tr_b16 a[188:189], v243 offset:24712\n"
      "global_load_lds_dwordx4 v240, s[78:79]\n"
      "v_mfma_f32_16x16x32_bf16 v[188:191], a[92:95], v[224:227], v[188:191]\n"
      "s_waitcnt lgkmcnt(11)\n"
      "v_mfma_f32_16x16x32_bf16 v[192:195], a[72:75], v[228:231], v[192:195]\n"
      "ds_read_b64_tr_b16 a[190:191], v243 offset:25480\n"
      "v_mfma_f32_16x16x32_bf16 v[196:199], a[76:79], v[228:231], v[196:199]\n"
      "s_add_u32 m0, s74, 0x1000\n"
      "v_mfma_f32_16x16x32_bf16 v[200:203], a[80:83], v[228:231], v[200:203]\n"
      "ds_read_b128 v[224:227], v232 offset:8192\n"
      "global_load_lds_dwordx4 v241, s[78:79]\n"
      "v_mfma_f32_16x16x32_bf16 v[204:207], a[84:87], v[228:231], v[204:207]\n"
      "v_mfma_f32_16x16x32_bf16 v[208:211], a[88:91], v[228:231], v[208:211]\n"
      "v_mfma_f32_16x16x32_bf16 v[212:215], a[92:95], v[228:231], v[212:215]\n"
      "s_add_u32 m0, s74, 0x1400\n"
      "s_waitcnt lgkmcnt(9)\n"
      "v_mfma_f32_16x16x32_bf16 v[0:3], a[96:99], v[216:219], v[0:3]\n"
      "global_load_lds_dwordx4 v242, s[78:79]\n"
      "v_mfma_f32_16x16x32_bf16 v[4:7], a[100:103], v[216:219], v[4:7]\n"
      "s_barrier\n"
      "v_mfma_f32_16x16x32_bf16 v[8:11], a[104:107], v[216:219], v[8:11]\n"
      "ds_read_b128 v[228:231], v232 offset:12288\n"
      "v_mfma_f32_16x16x32_bf16 v[12:15], a[108:111], v[216:219], v[12:15]\n"
      "s_add_u32 s58, s24, 0x0\n"
      "s_addc_u32 s59, s25, 0\n"
      "global_load_dwordx4 a[240:243], v239, s[58:59] offset:0 sc1\n"
      "v_mfma_f32_16x16x32_bf16 v[16:19], a[112:115], v[216:219], v[16:19]\n"
      "v_mfma_f32_16x16x32_bf16 v[20:23], a[116:119], v[216:219], v[20:23]\n"
      "s_waitcnt lgkmcnt(5)\n"
      "v_mfma_f32_16x16x32_bf16 v[24:27], a[96:99], v[220:223], v[24:27]\n"
      "v_mfma_f32_16x16x32_bf16 v[28:31], a[100:103], v[220:223], v[28:31]\n"
      "s_waitcnt lgkmcnt(2)\n"
      "s_add_u32 s78, s64, 0x3c000\n"
      "s_addc_u32 s79, s65, 0\n"
      "s_add_u32 m0, s74, 0x6000\n"
      "v_mfma_f32_16x16x32_bf16 v[32:35], a[104:107], v[220:223], v[32:35]\n"
      "ds_read_b128 v[216:219], v232 offset:16384\n"
      "global_load_lds_dwordx4 v240, s[78:79]\n"
      "v_mfma_f32_16x16x32_bf16 v[36:39], a[108:111], v[220:223], v[36:39]\n"
      "v_mfma_f32_16x16x32_bf16 v[40:43], a[112:115], v[220:223], v[40:43]\n"
      "s_waitcnt vmcnt(8)\n"
      "v_mfma_f32_16x16x32_bf16 v[44:47], a[116:119], v[220:223], v[44:47]\n"
      "s_add_u32 m0, s74, 0x6400\n"
      "s_waitcnt lgkmcnt(2)\n"
      "v_mfma_f32_16x16x32_bf16 v[48:51], a[96:99], v[224:227], v[48:51]\n"
      "ds_read_b64_tr_b16 a[192:193], v243 offset:49152\n"
      "global_load_lds_dwordx4 v241, s[78:79]\n"
      "v_mfma_f32_16x16x32_bf16 v[52:55], a[100:103], v[224:227], v[52:55]\n"
      "ds_read_b64_tr_b16 a[194:195], v243 offset:49920\n"
      "v_mfma_f32_16x16x32_bf16 v[56:59], a[104:107], v[224:227], v[56:59]\n"
      "ds_read_b128 v[220:223], v232 offset:20480\n"
      "v_mfma_f32_16x16x32_bf16 v[60:63], a[108:111], v[224:227], v[60:63]\n"
      "ds_read_b64_tr_b16 a[196:197], v243 offset:49160\n"
      "s_add_u32 m0, s74, 0x6800\n"
      "v_mfma_f32_16x16x32_bf16 v[64:67], a[112:115], v[224:227], v[64:67]\n"
      "ds_read_b64_tr_b16 a[198:199], v243 offset:49928\n"
      "global_load_lds_dwordx4 v242, s[78:79]\n"
      "v_mfma_f32_16x16x32_bf16 v[68:71], a[116:119], v[224:227], v[68:71]\n"
      "s_waitcnt lgkmcnt(6)\n"
      "v_mfma_f32_16x16x32_bf16 v[72:75], a[96:99], v[228:231], v[72:75]\n"
      "ds_read_b64_tr_b16 a[200:201], v243 offset:49216\n"
      "v_mfma_f32_16x16x32_bf16 v[76:79], a[100:103], v[228:231], v[76:79]\n"
      "ds_read_b64_tr_b16 a[202:203], v243 offset:49984\n"
      "s_add_u32 s78, s64, 0x3f000\n"
      "s_addc_u32 s79, s65, 0\n"
      "s_add_u32 m0, s74, 0x6c00\n"
      "v_mfma_f32_16x16x32_bf16 v[80:83], a[104:107], v[228:231], v[80:83]\n"
      "ds_read_b128 v[224:227], v232 offset:24576\n"
      "global_load_lds_dwordx4 v240, s[78:79]\n"
      "v_mfma_f32_16x16x32_bf16 v[84:87], a[108:111], v[228:231], v[84:87]\n"
      "ds_read_b64_tr_b16 a[204:205], v243 offset:49224\n"
      "v_mfma_f32_16x16x32_bf16 v[88:91], a[112:115], v[228:231], v[88:91]\n"
      "ds_read_b64_tr_b16 a[206:207], v243 offset:49992\n"
      "v_mfma_f32_16x16x32_bf16 v[92:95], a[116:119], v[228:231], v[92:95]\n"
      "s_add_u32 m0, s74, 0x7000\n"
      "s_waitcnt lgkmcnt(10)\n"
      "v_mfma_f32_16x16x32_bf16 v[96:99], a[96:99], v[216:219], v[96:99]\n"
      "ds_read_b64_tr_b16 a[208:209], v243 offset:49280\n"
      "global_load_lds_dwordx4 v241, s[78:79]\n"
      "v_mfma_f32_16x16x32_bf16 v[100:103], a[100:103], v[216:219], v[100:103]\n"
      "ds_read_b64_tr_b16 a[210:211], v243 offset:50048\n"
      "v_mfma_f32_16x16x32_bf16 v[104:107], a[104:107], v[216:219], v[104:107]\n"
      "ds_read_b128 v[228:231], v232 offset:28672\n"
      "v_mfma_f32_16x16x32_bf16 v[108:111], a[108:111], v[216:219], v[108:111]\n"
      "ds_read_b64_tr_b16 a[212:213], v243 offset:49288\n"
      "s_add_u32 m0, s74, 0x7400\n"
      "v_mfma_f32_16x16x32_bf16 v[112:115], a[112:115], v[216:219], v[112:115]\n"
      "ds_read_b64_tr_b16 a[214:215], v243 offset:50056\n"
      "global_load_lds_dwordx4 v242, s[78:79]\n"
      "v_mfma_f32_16x16x32_bf16 v[116:119], a[116:119], v[216:219], v[116:119]\n"
      "s_waitcnt lgkmcnt(12)\n"
      "v_mfma_f32_16x16x32_bf16 v[120:123], a[96:99], v[220:223], v[120:123]\n"
      "v_mfma_f32_16x16x32_bf16 v[124:127], a[100:103], v[220:223], v[124:127]\n"
      "global_load_dwordx4 a[244:247], v239, s[58:59] offset:1024 sc1\n"
      "v_mfma_f32_16x16x32_bf16 v[128:131], a[104:107], v[220:223], v[128:131]\n"
      "ds_read_b128 v[216:219], v232 offset:32768\n"
      "v_add_u32 v232, 0xffff7000, v232\n"
      "v_mfma_f32_16x16x32_bf16 v[132:135], a[108:111], v[220:223], v[132:135]\n"
      "v_mfma_f32_16x16x32_bf16 v[136:139], a[112:115], v[220:223], v[136:139]\n"
      "v_mfma_f32_16x16x32_bf16 v[140:143], a[116:119], v[220:223], v[140:143]\n"
      "global_load_dwordx4 a[248:251], v239, s[58:59] offset:2048 sc1\n"
      "s_waitcnt lgkmcnt(8)\n"
      "v_mfma_f32_16x16x32_bf16 v[144:147], a[96:99], v[224:227], v[144:147]\n"
      "s_waitcnt vmcnt(9)\n"
      "v_mfma_f32_16x16x32_bf16 v[148:151], a[100:103], v[224:227], v[148:151]\n"
      "ds_read_b64_tr_b16 a[216:217], v243 offset:0\n"
      "v_mfma_f32_16x16x32_bf16 v[152:155], a[104:107], v[224:227], v[152:155]\n"
      "ds_read_b128 v[220:223], v233 offset:0\n"
      "v_mfma_f32_16x16x32_bf16 v[156:159], a[108:111], v[224:227], v[156:159]\n"
      "ds_read_b64_tr_b16 a[218:219], v243 offset:768\n"
      "s_waitcnt lgkmcnt(4)\n"
      "s_add_u32 s78, s64, 0x42000\n"
      "s_addc_u32 s79, s65, 0\n"
      "s_add_u32 m0, s74, 0xc000\n"
      "v_mfma_f32_16x16x32_bf16 v[160:163], a[112:115], v[224:227], v[160:163]\n"
      "ds_read_b64_tr_b16 a[220:221], v243 offset:8\n"
      "global_load_lds_dwordx4 v240, s[78:79]\n"
      "v_mfma_f32_16x16x32_bf16 v[164:167], a[116:119], v[224:227], v[164:167]\n"
      "v_mfma_f32_16x16x32_bf16 v[168:171], a[96:99], v[228:231], v[168:171]\n"
      "ds_read_b64_tr_b16 a[222:223], v243 offset:776\n"
      "v_mfma_f32_16x16x32_bf16 v[172:175], a[100:103], v[228:231], v[172:175]\n"
      "ds_read_b64_tr_b16 a[224:225], v243 offset:64\n"
      "s_add_u32 m0, s74, 0xc400\n"
      "v_mfma_f32_16x16x32_bf16 v[176:179], a[104:107], v[228:231], v[176:179]\n"
      "ds_read_b128 v[224:227], v233 offset:4096\n"
      "global_load_lds_dwordx4 v241, s[78:79]\n"
      "v_mfma_f32_16x16x32_bf16 v[180:183], a[108:111], v[228:231], v[180:183]\n"
      "ds_read_b64_tr_b16 a[226:227], v243 offset:832\n"
      "v_mfma_f32_16x16x32_bf16 v[184:187], a[112:115], v[228:231], v[184:187]\n"
      "ds_read_b64_tr_b16 a[228:229], v243 offset:72\n"
      "v_mfma_f32_16x16x32_bf16 v[188:191], a[116:119], v[228:231], v[188:191]\n"
      "s_add_u32 m0, s74, 0xc800\n"
      "s_waitcnt lgkmcnt(9)\n"
      "v_mfma_f32_16x16x32_bf16 v[192:195], a[96:99], v[216:219], v[192:195]\n"
      "ds_read_b64_tr_b16 a[230:231], v243 offset:840\n"
      "global_load_lds_dwordx4 v242, s[78:79]\n"
      "v_mfma_f32_16x16x32_bf16 v[196:199], a[100:103], v[216:219], v[196:199]\n"
      "ds_read_b64_tr_b16 a[232:233], v243 offset:128\n"
      "v_mfma_f32_16x16x32_bf16 v[200:203], a[104:107], v[216:219], v[200:203]\n"
      "ds_read_b128 v[228:231], v233 offset:8192\n"
      "v_mfma_f32_16x16x32_bf16 v[204:207], a[108:111], v[216:219], v[204:207]\n"
      "ds_read_b64_tr_b16 a[234:235], v243 offset:896\n"
      "s_add_u32 s78, s64, 0x45000\n"
      "s_addc_u32 s79, s65, 0\n"
      "s_add_u32 m0, s74, 0xcc00\n"
      "v_mfma_f32_16x16x32_bf16 v[208:211], a[112:115], v[216:219], v[208:211]\n"
      "ds_read_b64_tr_b16 a[236:237], v243 offset:136\n"
      "global_load_lds_dwordx4 v240, s[78:79]\n"
      "v_mfma_f32_16x16x32_bf16 v[212:215], a[116:119], v[216:219], v[212:215]\n"
      "s_waitcnt lgkmcnt(12)\n"
      "v_mfma_f32_16x16x32_bf16 v[0:3], a[120:123], v[220:223], v[0:3]\n"
      "ds_read_b64_tr_b16 a[238:239], v243 offset:904\n"
      "v_mfma_f32_16x16x32_bf16 v[4:7], a[124:127], v[220:223], v[4:7]\n"
      "s_add_u32 m0, s74, 0xd000\n"
      "v_mfma_f32_16x16x32_bf16 v[8:11], a[128:131], v[220:223], v[8:11]\n"
      "ds_read_b128 v[216:219], v233 offset:12288\n"
      "global_load_lds_dwordx4 v241, s[78:79]\n"
      "v_mfma_f32_16x16x32_bf16 v[12:15], a[132:135], v[220:223], v[12:15]\n"
      "v_mfma_f32_16x16x32_bf16 v[16:19], a[136:139], v[220:223], v[16:19]\n"
      "v_mfma_f32_16x16x32_bf16 v[20:23], a[140:143], v[220:223], v[20:23]\n"
      "s_add_u32 m0, s74, 0xd400\n"
      "s_waitcnt lgkmcnt(9)\n"
      "v_mfma_f32_16x16x32_bf16 v[24:27], a[120:123], v[224:227], v[24:27]\n"
      "global_load_lds_dwordx4 v242, s[78:79]\n"
      "v_mfma_f32_16x16x32_bf16 v[28:31], a[124:127], v[224:227], v[28:31]\n"
      "v_mfma_f32_16x16x32_bf16 v[32:35], a[128:131], v[224:227], v[32:35]\n"
      "ds_read_b128 v[220:223], v233 offset:16384\n"
      "v_mfma_f32_16x16x32_bf16 v[36:39], a[132:135], v[224:227], v[36:39]\n"
      "s_waitcnt vmcnt(8)\n"
      "v_mad_i32_i24 v238, v251, s89, v238\n"
      "s_add_u32 s56, s22, 0x0\n"
      "s_addc_u32 s57, s23, 0\n"
      "s_add_u32 m0, s18, 0xfffffe00\n"
      "v_mfma_f32_16x16x32_bf16 v[40:43], a[136:139], v[224:227], v[40:43]\n"
      "ds_read_b64_tr_b16 a[0:1], v243 offset:24576\n"
      "global_load_lds_dwordx4 v238, s[56:57] offset:512 \n"
      "v_mfma_f32_16x16x32_bf16 v[44:47], a[140:143], v[224:227], v[44:47]\n"
      "s_waitcnt lgkmcnt(6)\n"
      "v_mfma_f32_16x16x32_bf16 v[48:51], a[120:123], v[228:231], v[48:51]\n"
      "ds_read_b64_tr_b16 a[2:3], v243 offset:25344\n"
      "v_mad_i32_i24 v238, v251, s90, v238\n"
      "s_add_u32 s56, s22, 0x6000\n"
      "s_addc_u32 s57, s23, 0\n"
      "s_add_u32 m0, s18, 0xe00\n"
      "v_mfma_f32_16x16x32_bf16 v[52:55], a[124:127], v[228:231], v[52:55]\n"
      "ds_read_b64_tr_b16 a[4:5], v243 offset:24584\n"
      "global_load_lds_dwordx4 v238, s[56:57] offset:512 \n"
      "v_mfma_f32_16x16x32_bf16 v[56:59], a[128:131], v[228:231], v[56:59]\n"
      "ds_read_b128 v[224:227], v233 offset:20480\n"
      "v_mfma_f32_16x16x32_bf16 v[60:63], a[132:135], v[228:231], v[60:63]\n"
      "ds_read_b64_tr_b16 a[6:7], v243 offset:25352\n"
      "v_mad_i32_i24 v238, v251, s91, v238\n"
      "s_add_u32 s56, s22, 0xc000\n"
      "s_addc_u32 s57, s23, 0\n"
      "s_add_u32 m0, s18, 0x1e00\n"
      "v_mfma_f32_16x16x32_bf16 v[64:67], a[136:139], v[228:231], v[64:67]\n"
      "ds_read_b64_tr_b16 a[8:9], v243 offset:24640\n"
      "global_load_lds_dwordx4 v238, s[56:57] offset:512 \n"
      "v_mfma_f32_16x16x32_bf16 v[68:71], a[140:143], v[228:231], v[68:71]\n"
      "s_waitcnt lgkmcnt(7)\n"
      "v_mfma_f32_16x16x32_bf16 v[72:75], a[120:123], v[216:219], v[72:75]\n"
      "ds_read_b64_tr_b16 a[10:11], v243 offset:25408\n"
      "v_mad_i32_i24 v238, v251, s92, v238\n"
      "s_add_u32 s56, s22, 0x12000\n"
      "s_addc_u32 s57, s23, 0\n"
      "s_add_u32 m0, s18, 0x2e00\n"
      "v_mfma_f32_16x16x32_bf16 v[76:79], a[124:127], v[216:219], v[76:79]\n"
      "ds_read_b64_tr_b16 a[12:13], v243 offset:24648\n"
      "global_load_lds_dwordx4 v238, s[56:57] offset:512 \n"
      "v_mfma_f32_16x16x32_bf16 v[80:83], a[128:131], v[216:219], v[80:83]\n"
      "ds_read_b128 v[228:231], v233 offset:24576\n"
      "v_mfma_f32_16x16x32_bf16 v[84:87], a[132:135], v[216:219], v[84:87]\n"
      "ds_read_b64_tr_b16 a[14:15], v243 offset:25416\n"
      "v_mad_i32_i24 v238, v251, s93, v238\n"
      "s_add_u32 s56, s22, 0x18000\n"
      "s_addc_u32 s57, s23, 0\n"
      "s_add_u32 m0, s18, 0x3e00\n"
      "v_mfma_f32_16x16x32_bf16 v[88:91], a[136:139], v[216:219], v[88:91]\n"
      "ds_read_b64_tr_b16 a[16:17], v243 offset:24704\n"
      "global_load_lds_dwordx4 v238, s[56:57] offset:512 \n"
      "v_mfma_f32_16x16x32_bf16 v[92:95], a[140:143], v[216:219], v[92:95]\n"
      "s_waitcnt lgkmcnt(11)\n"
      "v_mfma_f32_16x16x32_bf16 v[96:99], a[120:123], v[220:223], v[96:99]\n"
      "ds_read_b64_tr_b16 a[18:19], v243 offset:25472\n"
      "v_mad_i32_i24 v238, v251, s94, v238\n"
      "s_add_u32 s56, s22, 0x1e000\n"
      "s_addc_u32 s57, s23, 0\n"
      "s_add_u32 m0, s18, 0x4e00\n"
      "v_mfma_f32_16x16x32_bf16 v[100:103], a[124:127], v[220:223], v[100:103]\n"
      "ds_read_b64_tr_b16 a[20:21], v243 offset:24712\n"
      "global_load_lds_dwordx4 v238, s[56:57] offset:512 \n"
      "v_mfma_f32_16x16x32_bf16 v[104:107], a[128:131], v[220:223], v[104:107]\n"
      "ds_read_b128 v[216:219], v233 offset:28672\n"
      "v_mfma_f32_16x16x32_bf16 v[108:111], a[132:135], v[220:223], v[108:111]\n"
      "ds_read_b64_tr_b16 a[22:23], v243 offset:25480\n"
      "v_mad_i32_i24 v238, v251, s95, v238\n"
      "s_add_u32 s56, s22, 0x24000\n"
      "s_addc_u32 s57, s23, 0\n"
      "s_add_u32 m0, s18, 0x5e00\n"
      "v_mfma_f32_16x16x32_bf16 v[112:115], a[136:139], v[220:223], v[112:115]\n"
      "global_load_lds_dwordx4 v238, s[56:57] offset:512 \n"
      "v_mfma_f32_16x16x32_bf16 v[116:119], a[140:143], v[220:223], v[116:119]\n"
      "s_waitcnt lgkmcnt(11)\n"
      "v_mfma_f32_16x16x32_bf16 v[120:123], a[120:123], v[224:227], v[120:123]\n"
      "v_mad_i32_i24 v238, v251, s96, v238\n"
      "s_add_u32 s56, s22, 0x2a000\n"
      "s_addc_u32 s57, s23, 0\n"
      "s_add_u32 m0, s18, 0x6e00\n"
      "v_mfma_f32_16x16x32_bf16 v[124:127], a[124:127], v[224:227], v[124:127]\n"
      "global_load_lds_dwordx4 v238, s[56:57] offset:512 \n"
      "v_mfma_f32_16x16x32_bf16 v[128:131], a[128:131], v[224:227], v[128:131]\n"
      "ds_read_b128 v[220:223], v233 offset:32768\n"
      "v_add_u32 v233, 0xffff7000, v233\n"
      "v_mfma_f32_16x16x32_bf16 v[132:135], a[132:135], v[224:227], v[132:135]\n"
      "v_mad_i32_i24 v238, v251, s97, v238\n"
      "s_add_u32 s56, s22, 0x30000\n"
      "s_addc_u32 s57, s23, 0\n"
      "s_add_u32 m0, s18, 0x7e00\n"
      "v_mfma_f32_16x16x32_bf16 v[136:139], a[136:139], v[224:227], v[136:139]\n"
      "global_load_lds_dwordx4 v238, s[56:57] offset:512 \n"
      "v_mfma_f32_16x16x32_bf16 v[140:143], a[140:143], v[224:227], v[140:143]\n"
      "s_waitcnt lgkmcnt(7)\n"
      "v_mfma_f32_16x16x32_bf16 v[144:147], a[120:123], v[228:231], v[144:147]\n"
      "v_mfma_f32_16x16x32_bf16 v[148:151], a[124:127], v[228:231], v[148:151]\n"
      "global_load_dwordx4 a[252:255], v239, s[58:59] offset:3072 sc1\n"
      "v_mfma_f32_16x16x32_bf16 v[152:155], a[128:131], v[228:231], v[152:155]\n"
      "ds_read_b128 v[224:227], v234 offset:0\n"
      "v_mfma_f32_16x16x32_bf16 v[156:159], a[132:135], v[228:231], v[156:159]\n"
      "v_mfma_f32_16x16x32_bf16 v[160:163], a[136:139], v[228:231], v[160:163]\n"
      "v_mfma_f32_16x16x32_bf16 v[164:167], a[140:143], v[228:231], v[164:167]\n"
      "s_add_u32 s58, s24, 0x1000\n"
      "s_addc_u32 s59, s25, 0\n"
      "global_load_dwordx4 a[48:51], v239, s[58:59] offset:0 sc1\n"
      "s_waitcnt lgkmcnt(3)\n"
      "v_mfma_f32_16x16x32_bf16 v[168:171], a[120:123], v[216:219], v[168:171]\n"
      "s_waitcnt vmcnt(11)\n"
      "v_mfma_f32_16x16x32_bf16 v[172:175], a[124:127], v[216:219], v[172:175]\n"
      "ds_read_b64_tr_b16 a[24:25], v243 offset:49152\n"
      "v_mfma_f32_16x16x32_bf16 v[176:179], a[128:131], v[216:219], v[176:179]\n"
      "ds_read_b128 v[228:231], v234 offset:4096\n"
      "v_mfma_f32_16x16x32_bf16 v[180:183], a[132:135], v[216:219], v[180:183]\n"
      "ds_read_b64_tr_b16 a[26:27], v243 offset:49920\n"
      "global_load_dwordx4 a[52:55], v239, s[58:59] offset:1024 sc1\n"
      "LPD5:\n"
      "s_cbranch_execz LPD8\n"
      "v_mfma_f32_16x16x32_bf16 v[184:187], a[136:139], v[216:219], v[184:187]\n"
      "ds_read_b64_tr_b16 a[28:29], v243 offset:49160\n"
      "v_mfma_f32_16x16x32_bf16 v[188:191], a[140:143], v[216:219], v[188:191]\n"
      "s_waitcnt lgkmcnt(5)\n"
      "v_mfma_f32_16x16x32_bf16 v[192:195], a[120:123], v[220:223], v[192:195]\n"
      "ds_read_b64_tr_b16 a[30:31], v243 offset:49928\n"
      "v_mfma_f32_16x16x32_bf16 v[196:199], a[124:127], v[220:223], v[196:199]\n"
      "ds_read_b64_tr_b16 a[32:33], v243 offset:49216\n"
      "global_load_dwordx4 a[56:59], v239, s[58:59] offset:2048 sc1\n"
      "v_mfma_f32_16x16x32_bf16 v[200:203], a[128:131], v[220:223], v[200:203]\n"
      "ds_read_b128 v[216:219], v234 offset:8192\n"
      "v_mfma_f32_16x16x32_bf16 v[204:207], a[132:135], v[220:223], v[204:207]\n"
      "ds_read_b64_tr_b16 a[34:35], v243 offset:49984\n"
      "v_mfma_f32_16x16x32_bf16 v[208:211], a[136:139], v[220:223], v[208:211]\n"
      "ds_read_b64_tr_b16 a[36:37], v243 offset:49224\n"
      "v_mfma_f32_16x16x32_bf16 v[212:215], a[140:143], v[220:223], v[212:215]\n"
      "global_load_dwordx4 a[60:63], v239, s[58:59] offset:3072 sc1\n"
      "s_waitcnt lgkmcnt(9)\n"
      "v_mfma_f32_16x16x32_bf16 v[0:3], a[144:147], v[224:227], v[0:3]\n"
      "ds_read_b64_tr_b16 a[38:39], v243 offset:49992\n"
      "v_mfma_f32_16x16x32_bf16 v[4:7], a[148:151], v[224:227], v[4:7]\n"
      "ds_read_b64_tr_b16 a[40:41], v243 offset:49280\n"
      "v_mfma_f32_16x16x32_bf16 v[8:11], a[152:155], v[224:227], v[8:11]\n"
      "ds_read_b128 v[220:223], v234 offset:12288\n"
      "v_mfma_f32_16x16x32_bf16 v[12:15], a[156:159], v[224:227], v[12:15]\n"
      "ds_read_b64_tr_b16 a[42:43], v243 offset:50048\n"
      "s_add_u32 s58, s24, 0x2000\n"
      "s_addc_u32 s59, s25, 0\n"
      "global_load_dwordx4 a[64:67], v239, s[58:59] offset:0 sc1\n"
      "v_mfma_f32_16x16x32_bf16 v[16:19], a[160:163], v[224:227], v[16:19]\n"
      "ds_read_b64_tr_b16 a[44:45], v243 offset:49288\n"
      "v_mfma_f32_16x16x32_bf16 v[20:23], a[164:167], v[224:227], v[20:23]\n"
      "s_waitcnt lgkmcnt(12)\n"
      "v_mfma_f32_16x16x32_bf16 v[24:27], a[144:147], v[228:231], v[24:27]\n"
      "ds_read_b64_tr_b16 a[46:47], v243 offset:50056\n"
      "v_mfma_f32_16x16x32_bf16 v[28:31], a[148:151], v[228:231], v[28:31]\n"
      "global_load_dwordx4 a[68:71], v239, s[58:59] offset:1024 sc1\n"
      "v_mfma_f32_16x16x32_bf16 v[32:35], a[152:155], v[228:231], v[32:35]\n"
      "ds_read_b128 v[224:227], v234 offset:16384\n"
      "v_mfma_f32_16x16x32_bf16 v[36:39], a[156:159], v[228:231], v[36:39]\n"
      "v_mfma_f32_16x16x32_bf16 v[40:43], a[160:163], v[228:231], v[40:43]\n"
      "v_mfma_f32_16x16x32_bf16 v[44:47], a[164:167], v[228:231], v[44:47]\n"
      "global_load_dwordx4 a[72:75], v239, s[58:59] offset:2048 sc1\n"
      "s_waitcnt lgkmcnt(9)\n"
      "v_mfma_f32_16x16x32_bf16 v[48:51], a[144:147], v[216:219], v[48:51]\n"
      "v_mfma_f32_16x16x32_bf16 v[52:55], a[148:151], v[216:219], v[52:55]\n"
      "v_mfma_f32_16x16x32_bf16 v[56:59], a[152:155], v[216:219], v[56:59]\n"
      "ds_read_b128 v[228:231], v234 offset:20480\n"
      "v_mfma_f32_16x16x32_bf16 v[60:63], a[156:159], v[216:219], v[60:63]\n"
      "global_load_dwordx4 a[76:79], v239, s[58:59] offset:3072 sc1\n"
      "v_mfma_f32_16x16x32_bf16 v[64:67], a[160:163], v[216:219], v[64:67]\n"
      "v_mfma_f32_16x16x32_bf16 v[68:71], a[164:167], v[216:219], v[68:71]\n"
      "s_waitcnt lgkmcnt(5)\n"
      "v_mfma_f32_16x16x32_bf16 v[72:75], a[144:147], v[220:223], v[72:75]\n"
      "v_mfma_f32_16x16x32_bf16 v[76:79], a[148:151], v[220:223], v[76:79]\n"
      "s_add_u32 s58, s24, 0x3000\n"
      "s_addc_u32 s59, s25, 0\n"
      "global_load_dwordx4 a[80:83], v239, s[58:59] offset:0 sc1\n"
      "v_mfma_f32_16x16x32_bf16 v[80:83], a[152:155], v[220:223], v[80:83]\n"
      "ds_read_b128 v[216:219], v234 offset:24576\n"
      "v_mfma_f32_16x16x32_bf16 v[84:87], a[156:159], v[220:223], v[84:87]\n"
      "v_mfma_f32_16x16x32_bf16 v[88:91], a[160:163], v[220:223], v[88:91]\n"
      "v_mfma_f32_16x16x32_bf16 v[92:95], a[164:167], v[220:223], v[92:95]\n"
      "global_load_dwordx4 a[84:87], v239, s[58:59] offset:1024 sc1\n"
      "s_waitcnt lgkmcnt(2)\n"
      "v_mfma_f32_16x16x32_bf16 v[96:99], a[144:147], v[224:227], v[96:99]\n"
      "v_mfma_f32_16x16x32_bf16 v[100:103], a[148:151], v[224:227], v[100:103]\n"
      "v_mfma_f32_16x16x32_bf16 v[104:107], a[152:155], v[224:227], v[104:107]\n"
      "ds_read_b128 v[220:223], v234 offset:28672\n"
      "v_mfma_f32_16x16x32_bf16 v[108:111], a[156:159], v[224:227], v[108:111]\n"
      "global_load_dwordx4 a[88:91], v239, s[58:59] offset:2048 sc1\n"
      "v_mfma_f32_16x16x32_bf16 v[112:115], a[160:163], v[224:227], v[112:115]\n"
      "v_mfma_f32_16x16x32_bf16 v[116:119], a[164:167], v[224:227], v[116:119]\n"
      "s_waitcnt lgkmcnt(2)\n"
      "v_mfma_f32_16x16x32_bf16 v[120:123], a[144:147], v[228:231], v[120:123]\n"
      "v_mfma_f32_16x16x32_bf16 v[124:127], a[148:151], v[228:231], v[124:127]\n"
      "global_load_dwordx4 a[92:95], v239, s[58:59] offset:3072 sc1\n"
      "v_mfma_f32_16x16x32_bf16 v[128:131], a[152:155], v[228:231], v[128:131]\n"
      "ds_read_b128 v[224:227], v234 offset:32768\n"
      "v_add_u32 v234, 0xffff7000, v234\n"
      "v_mfma_f32_16x16x32_bf16 v[132:135], a[156:159], v[228:231], v[132:135]\n"
      "v_mfma_f32_16x16x32_bf16 v[136:139], a[160:163], v[228:231], v[136:139]\n"
      "v_mfma_f32_16x16x32_bf16 v[140:143], a[164:167], v[228:231], v[140:143]\n"
      "s_add_u32 s58, s24, 0x4000\n"
      "s_addc_u32 s59, s25, 0\n"
      "global_load_dwordx4 a[96:99], v239, s[58:59] offset:0 sc1\n"
      "s_waitcnt lgkmcnt(2)\n"
      "v_mfma_f32_16x16x32_bf16 v[144:147], a[144:147], v[216:219], v[144:147]\n"
      "ds_read_b64 v[236:237], v250 offset:4104\n"
      "v_mfma_f32_16x16x32_bf16 v[148:151], a[148:151], v[216:219], v[148:151]\n"
      "v_mfma_f32_16x16x32_bf16 v[152:155], a[152:155], v[216:219], v[152:155]\n"
      "ds_read_b128 v[228:231], v235 offset:0\n"
      "v_mfma_f32_16x16x32_bf16 v[156:159], a[156:159], v[216:219], v[156:159]\n"
      "global_load_dwordx4 a[100:103], v239, s[58:59] offset:1024 sc1\n"
      "v_mfma_f32_16x16x32_bf16 v[160:163], a[160:163], v[216:219], v[160:163]\n"
      "v_mfma_f32_16x16x32_bf16 v[164:167], a[164:167], v[216:219], v[164:167]\n"
      "s_waitcnt lgkmcnt(3)\n"
      "v_mfma_f32_16x16x32_bf16 v[168:171], a[144:147], v[220:223], v[168:171]\n"
      "v_mfma_f32_16x16x32_bf16 v[172:175], a[148:151], v[220:223], v[172:175]\n"
      "global_load_dwordx4 a[104:107], v239, s[58:59] offset:2048 sc1\n"
      "v_mfma_f32_16x16x32_bf16 v[176:179], a[152:155], v[220:223], v[176:179]\n"
      "ds_read_b128 v[216:219], v235 offset:4096\n"
      "v_mfma_f32_16x16x32_bf16 v[180:183], a[156:159], v[220:223], v[180:183]\n"
      "v_mfma_f32_16x16x32_bf16 v[184:187], a[160:163], v[220:223], v[184:187]\n"
      "v_mfma_f32_16x16x32_bf16 v[188:191], a[164:167], v[220:223], v[188:191]\n"
      "global_load_dwordx4 a[108:111], v239, s[58:59] offset:3072 sc1\n"
      "s_waitcnt lgkmcnt(3)\n"
      "v_mfma_f32_16x16x32_bf16 v[192:195], a[144:147], v[224:227], v[192:195]\n"
      "v_mfma_f32_16x16x32_bf16 v[196:199], a[148:151], v[224:227], v[196:199]\n"
      "v_mfma_f32_16x16x32_bf16 v[200:203], a[152:155], v[224:227], v[200:203]\n"
      "ds_read_b128 v[220:223], v235 offset:8192\n"
      "v_mfma_f32_16x16x32_bf16 v[204:207], a[156:159], v[224:227], v[204:207]\n"
      "s_add_u32 s58, s24, 0x5000\n"
      "s_addc_u32 s59, s25, 0\n"
      "global_load_dwordx4 a[112:115], v239, s[58:59] offset:0 sc1\n"
      "v_mfma_f32_16x16x32_bf16 v[208:211], a[160:163], v[224:227], v[208:211]\n"
      "v_mfma_f32_16x16x32_bf16 v[212:215], a[164:167], v[224:227], v[212:215]\n"
      "s_waitcnt lgkmcnt(2)\n"
      "v_mfma_f32_16x16x32_bf16 v[0:3], a[168:171], v[228:231], v[0:3]\n"
      "v_mfma_f32_16x16x32_bf16 v[4:7], a[172:175], v[228:231], v[4:7]\n"
      "global_load_dwordx4 a[116:119], v239, s[58:59] offset:1024 sc1\n"
      "v_mfma_f32_16x16x32_bf16 v[8:11], a[176:179], v[228:231], v[8:11]\n"
      "ds_read_b128 v[224:227], v235 offset:12288\n"
      "v_mfma_f32_16x16x32_bf16 v[12:15], a[180:183], v[228:231], v[12:15]\n"
      "v_mfma_f32_16x16x32_bf16 v[16:19], a[184:187], v[228:231], v[16:19]\n"
      "v_mfma_f32_16x16x32_bf16 v[20:23], a[188:191], v[228:231], v[20:23]\n"
      "global_load_dwordx4 a[120:123], v239, s[58:59] offset:2048 sc1\n"
      "s_waitcnt lgkmcnt(2)\n"
      "v_mfma_f32_16x16x32_bf16 v[24:27], a[168:171], v[216:219], v[24:27]\n"
      "v_mfma_f32_16x16x32_bf16 v[28:31], a[172:175], v[216:219], v[28:31]\n"
      "v_mfma_f32_16x16x32_bf16 v[32:35], a[176:179], v[216:219], v[32:35]\n"
      "ds_read_b128 v[228:231], v235 offset:16384\n"
      "v_mfma_f32_16x16x32_bf16 v[36:39], a[180:183], v[216:219], v[36:39]\n"
      "global_load_dwordx4 a[124:127], v239, s[58:59] offset:3072 sc1\n"
      "v_mfma_f32_16x16x32_bf16 v[40:43], a[184:187], v[216:219], v[40:43]\n"
      "v_mfma_f32_16x16x32_bf16 v[44:47], a[188:191], v[216:219], v[44:47]\n"
      "s_waitcnt lgkmcnt(2)\n"
      "v_mfma_f32_16x16x32_bf16 v[48:51], a[168:171], v[220:223], v[48:51]\n"
      "v_mfma_f32_16x16x32_bf16 v[52:55], a[172:175], v[220:223], v[52:55]\n"
      "s_add_u32 s58, s24, 0x6000\n"
      "s_addc_u32 s59, s25, 0\n"
      "global_load_dwordx4 a[128:131], v239, s[58:59] offset:0 sc1\n"
      "v_mfma_f32_16x16x32_bf16 v[56:59], a[176:179], v[220:223], v[56:59]\n"
      "ds_read_b128 v[216:219], v235 offset:20480\n"
      "v_mfma_f32_16x16x32_bf16 v[60:63], a[180:183], v[220:223], v[60:63]\n"
      "v_mfma_f32_16x16x32_bf16 v[64:67], a[184:187], v[220:223], v[64:67]\n"
      "v_mfma_f32_16x16x32_bf16 v[68:71], a[188:191], v[220:223], v[68:71]\n"
      "global_load_dwordx4 a[132:135], v239, s[58:59] offset:1024 sc1\n"
      "s_waitcnt lgkmcnt(2)\n"
      "v_mfma_f32_16x16x32_bf16 v[72:75], a[168:171], v[224:227], v[72:75]\n"
      "v_mfma_f32_16x16x32_bf16 v[76:79], a[172:175], v[224:227], v[76:79]\n"
      "v_mfma_f32_16x16x32_bf16 v[80:83], a[176:179], v[224:227], v[80:83]\n"
      "ds_read_b128 v[220:223], v235 offset:24576\n"
      "v_mfma_f32_16x16x32_bf16 v[84:87], a[180:183], v[224:227], v[84:87]\n"
      "global_load_dwordx4 a[136:139], v239, s[58:59] offset:2048 sc1\n"
      "v_mfma_f32_16x16x32_bf16 v[88:91], a[184:187], v[224:227], v[88:91]\n"
      "v_mfma_f32_16x16x32_bf16 v[92:95], a[188:191], v[224:227], v[92:95]\n"
      "s_waitcnt lgkmcnt(2)\n"
      "v_mfma_f32_16x16x32_bf16 v[96:99], a[168:171], v[228:231], v[96:99]\n"
      "v_mfma_f32_16x16x32_bf16 v[100:103], a[172:175], v[228:231], v[100:103]\n"
      "global_load_dwordx4 a[140:143], v239, s[58:59] offset:3072 sc1\n"
      "v_mfma_f32_16x16x32_bf16 v[104:107], a[176:179], v[228:231], v[104:107]\n"
      "ds_read_b128 v[224:227], v235 offset:28672\n"
      "v_mfma_f32_16x16x32_bf16 v[108:111], a[180:183], v[228:231], v[108:111]\n"
      "v_mfma_f32_16x16x32_bf16 v[112:115], a[184:187], v[228:231], v[112:115]\n"
      "v_mfma_f32_16x16x32_bf16 v[116:119], a[188:191], v[228:231], v[116:119]\n"
      "s_add_u32 s58, s24, 0x7000\n"
      "s_addc_u32 s59, s25, 0\n"
      "global_load_dwordx4 a[144:147], v239, s[58:59] offset:0 sc1\n"
      "s_waitcnt lgkmcnt(2)\n"
      "v_mfma_f32_16x16x32_bf16 v[120:123], a[168:171], v[216:219], v[120:123]\n"
      "v_mfma_f32_16x16x32_bf16 v[124:127], a[172:175], v[216:219], v[124:127]\n"
      "v_mfma_f32_16x16x32_bf16 v[128:131], a[176:179], v[216:219], v[128:131]\n"
      "ds_read_b128 v[228:231], v235 offset:32768\n"
      "v_add_u32 v235, 0xffff7000, v235\n"
      "v_mfma_f32_16x16x32_bf16 v[132:135], a[180:183], v[216:219], v[132:135]\n"
      "global_load_dwordx4 a[148:151], v239, s[58:59] offset:1024 sc1\n"
      "v_mfma_f32_16x16x32_bf16 v[136:139], a[184:187], v[216:219], v[136:139]\n"
      "v_mfma_f32_16x16x32_bf16 v[140:143], a[188:191], v[216:219], v[140:143]\n"
      "s_waitcnt lgkmcnt(2)\n"
      "v_mfma_f32_16x16x32_bf16 v[144:147], a[168:171], v[220:223], v[144:147]\n"
      "v_mfma_f32_16x16x32_bf16 v[148:151], a[172:175], v[220:223], v[148:151]\n"
      "s_waitcnt vmcnt(27)\n"
      "s_barrier\n"
      "global_load_dwordx4 a[152:155], v239, s[58:59] offset:2048 sc1\n"
      "v_mfma_f32_16x16x32_bf16 v[152:155], a[176:179], v[220:223], v[152:155]\n"
      "ds_read_b128 v[216:219], v232 offset:0\n"
      "v_mfma_f32_16x16x32_bf16 v[156:159], a[180:183], v[220:223], v[156:159]\n"
      "v_mfma_f32_16x16x32_bf16 v[160:163], a[184:187], v[220:223], v[160:163]\n"
      "v_mfma_f32_16x16x32_bf16 v[164:167], a[188:191], v[220:223], v[164:167]\n"
      "global_load_dwordx4 a[156:159], v239, s[58:59] offset:3072 sc1\n"
      "s_waitcnt lgkmcnt(2)\n"
      "v_mfma_f32_16x16x32_bf16 v[168:171], a[168:171], v[224:227], v[168:171]\n"
      "v_mfma_f32_16x16x32_bf16 v[172:175], a[172:175], v[224:227], v[172:175]\n"
      "v_mfma_f32_16x16x32_bf16 v[176:179], a[176:179], v[224:227], v[176:179]\n"
      "ds_read_b128 v[220:223], v232 offset:4096\n"
      "v_mfma_f32_16x16x32_bf16 v[180:183], a[180:183], v[224:227], v[180:183]\n"
      "s_add_u32 s58, s24, 0x8000\n"
      "s_addc_u32 s59, s25, 0\n"
      "global_load_dwordx4 a[160:163], v239, s[58:59] offset:0 sc1\n"
      "v_mfma_f32_16x16x32_bf16 v[184:187], a[184:187], v[224:227], v[184:187]\n"
      "v_mfma_f32_16x16x32_bf16 v[188:191], a[188:191], v[224:227], v[188:191]\n"
      "s_waitcnt lgkmcnt(2)\n"
      "v_mfma_f32_16x16x32_bf16 v[192:195], a[168:171], v[228:231], v[192:195]\n"
      "v_mfma_f32_16x16x32_bf16 v[196:199], a[172:175], v[228:231], v[196:199]\n"
      "global_load_dwordx4 a[164:167], v239, s[58:59] offset:1024 sc1\n"
      "v_mfma_f32_16x16x32_bf16 v[200:203], a[176:179], v[228:231], v[200:203]\n"
      "ds_read_b128 v[224:227], v232 offset:8192\n"
      "v_mfma_f32_16x16x32_bf16 v[204:207], a[180:183], v[228:231], v[204:207]\n"
      "v_mfma_f32_16x16x32_bf16 v[208:211], a[184:187], v[228:231], v[208:211]\n"
      "v_mfma_f32_16x16x32_bf16 v[212:215], a[188:191], v[228:231], v[212:215]\n"
      "s_waitcnt lgkmcnt(2)\n"
      "v_mfma_f32_16x16x32_bf16 v[0:3], a[192:195], v[216:219], v[0:3]\n"
      "global_load_dwordx4 a[168:171], v239, s[58:59] offset:2048 sc1\n"
      "v_mfma_f32_16x16x32_bf16 v[4:7], a[196:199], v[216:219], v[4:7]\n"
      "v_mfma_f32_16x16x32_bf16 v[8:11], a[200:203], v[216:219], v[8:11]\n"
      "ds_read_b128 v[228:231], v232 offset:12288\n"
      "v_mfma_f32_16x16x32_bf16 v[12:15], a[204:207], v[216:219], v[12:15]\n"
      "v_mfma_f32_16x16x32_bf16 v[16:19], a[208:211], v[216:219], v[16:19]\n"
      "s_mul_i32 s78, s98, s19\n"
      "v_mfma_f32_16x16x32_bf16 v[20:23], a[212:215], v[216:219], v[20:23]\n"
      "v_add_u32 v236, s78, v236\n"
      "s_waitcnt lgkmcnt(2)\n"
      "v_mfma_f32_16x16x32_bf16 v[24:27], a[192:195], v[220:223], v[24:27]\n"
      "s_mul_i32 s78, s78, s99\n"
      "v_mfma_f32_16x16x32_bf16 v[28:31], a[196:199], v[220:223], v[28:31]\n"
      "v_add_u32 v237, s78, v237\n"
      "v_mfma_f32_16x16x32_bf16 v[32:35], a[200:203], v[220:223], v[32:35]\n"
      "ds_read_b128 v[216:219], v232 offset:16384\n"
      "s_add_u32 s52, s20, s32\n"
      "s_addc_u32 s53, s21, 0\n"
      "s_add_u32 m0, s17, 0x0\n"
      "v_mfma_f32_16x16x32_bf16 v[36:39], a[204:207], v[220:223], v[36:39]\n"
      "global_load_lds_dwordx4 v236, s[52:53] offset:0 \n"
      "v_mfma_f32_16x16x32_bf16 v[40:43], a[208:211], v[220:223], v[40:43]\n"
      "v_mfma_f32_16x16x32_bf16 v[44:47], a[212:215], v[220:223], v[44:47]\n"
      "s_add_u32 m0, s17, 0x1f00\n"
      "s_waitcnt lgkmcnt(2)\n"
      "v_mfma_f32_16x16x32_bf16 v[48:51], a[192:195], v[224:227], v[48:51]\n"
      "global_load_lds_dwordx4 v236, s[52:53] offset:256 \n"
      "v_mfma_f32_16x16x32_bf16 v[52:55], a[196:199], v[224:227], v[52:55]\n"
      "v_mfma_f32_16x16x32_bf16 v[56:59], a[200:203], v[224:227], v[56:59]\n"
      "ds_read_b128 v[220:223], v232 offset:20480\n"
      "s_add_u32 m0, s17, 0x3e00\n"
      "v_mfma_f32_16x16x32_bf16 v[60:63], a[204:207], v[224:227], v[60:63]\n"
      "global_load_lds_dwordx4 v236, s[52:53] offset:512 \n"
      "v_mfma_f32_16x16x32_bf16 v[64:67], a[208:211], v[224:227], v[64:67]\n"
      "v_mfma_f32_16x16x32_bf16 v[68:71], a[212:215], v[224:227], v[68:71]\n"
      "s_add_u32 m0, s17, 0x1000\n"
      "s_waitcnt lgkmcnt(2)\n"
      "v_mfma_f32_16x16x32_bf16 v[72:75], a[192:195], v[228:231], v[72:75]\n"
      "global_load_lds_dwordx4 v237, s[52:53] offset:0 \n"
      "v_mfma_f32_16x16x32_bf16 v[76:79], a[196:199], v[228:231], v[76:79]\n"
      "v_mfma_f32_16x16x32_bf16 v[80:83], a[200:203], v[228:231], v[80:83]\n"
      "ds_read_b128 v[224:227], v232 offset:24576\n"
      "s_add_u32 m0, s17, 0x2f00\n"
      "v_mfma_f32_16x16x32_bf16 v[84:87], a[204:207], v[228:231], v[84:87]\n"
      "global_load_lds_dwordx4 v237, s[52:53] offset:256 \n"
      "v_mfma_f32_16x16x32_bf16 v[88:91], a[208:211], v[228:231], v[88:91]\n"
      "v_mfma_f32_16x16x32_bf16 v[92:95], a[212:215], v[228:231], v[92:95]\n"
      "s_add_u32 m0, s17, 0x4e00\n"
      "s_waitcnt lgkmcnt(2)\n"
      "v_mfma_f32_16x16x32_bf16 v[96:99], a[192:195], v[216:219], v[96:99]\n"
      "global_load_lds_dwordx4 v237, s[52:53] offset:512 \n"
      "v_mfma_f32_16x16x32_bf16 v[100:103], a[196:199], v[216:219], v[100:103]\n"
      "v_mfma_f32_16x16x32_bf16 v[104:107], a[200:203], v[216:219], v[104:107]\n"
      "ds_read_b128 v[228:231], v232 offset:28672\n"
      "v_mfma_f32_16x16x32_bf16 v[108:111], a[204:207], v[216:219], v[108:111]\n"
      "v_mfma_f32_16x16x32_bf16 v[112:115], a[208:211], v[216:219], v[112:115]\n"
      "v_mfma_f32_16x16x32_bf16 v[116:119], a[212:215], v[216:219], v[116:119]\n"
      "s_waitcnt lgkmcnt(2)\n"
      "v_mfma_f32_16x16x32_bf16 v[120:123], a[192:195], v[220:223], v[120:123]\n"
      "v_mfma_f32_16x16x32_bf16 v[124:127], a[196:199], v[220:223], v[124:127]\n"
      "v_mfma_f32_16x16x32_bf16 v[128:131], a[200:203], v[220:223], v[128:131]\n"
      "ds_read_b128 v[216:219], v232 offset:32768\n"
      "v_add_u32 v232, v249, v232\n"
      "v_mfma_f32_16x16x32_bf16 v[132:135], a[204:207], v[220:223], v[132:135]\n"
      "v_mfma_f32_16x16x32_bf16 v[136:139], a[208:211], v[220:223], v[136:139]\n"
      "v_mfma_f32_16x16x32_bf16 v[140:143], a[212:215], v[220:223], v[140:143]\n"
      "s_waitcnt lgkmcnt(2)\n"
      "v_mfma_f32_16x16x32_bf16 v[144:147], a[192:195], v[224:227], v[144:147]\n"
      "v_mfma_f32_16x16x32_bf16 v[148:151], a[196:199], v[224:227], v[148:151]\n"
      "v_mfma_f32_16x16x32_bf16 v[152:155], a[200:203], v[224:227], v[152:155]\n"
      "ds_read_b128 v[220:223], v233 offset:0\n"
      "v_mfma_f32_16x16x32_bf16 v[156:159], a[204:207], v[224:227], v[156:159]\n"
      "v_mfma_f32_16x16x32_bf16 v[160:163], a[208:211], v[224:227], v[160:163]\n"
      "v_mfma_f32_16x16x32_bf16 v[164:167], a[212:215], v[224:227], v[164:167]\n"
      "s_waitcnt lgkmcnt(2)\n"
      "v_mfma_f32_16x16x32_bf16 v[168:171], a[192:195], v[228:231], v[168:171]\n"
      "v_mfma_f32_16x16x32_bf16 v[172:175], a[196:199], v[228:231], v[172:175]\n"
      "v_mfma_f32_16x16x32_bf16 v[176:179], a[200:203], v[228:231], v[176:179]\n"
      "ds_read_b128 v[224:227], v233 offset:4096\n"
      "v_mfma_f32_16x16x32_bf16 v[180:183], a[204:207], v[228:231], v[180:183]\n"
      "v_mfma_f32_16x16x32_bf16 v[184:187], a[208:211], v[228:231], v[184:187]\n"
      "v_mfma_f32_16x16x32_bf16 v[188:191], a[212:215], v[228:231], v[188:191]\n"
      "s_waitcnt lgkmcnt(2)\n"
      "v_mfma_f32_16x16x32_bf16 v[192:195], a[192:195], v[216:219], v[192:195]\n"
      "v_mfma_f32_16x16x32_bf16 v[196:199], a[196:199], v[216:219], v[196:199]\n"
      "v_mfma_f32_16x16x32_bf16 v[200:203], a[200:203], v[216:219], v[200:203]\n"
      "ds_read_b128 v[228:231], v233 offset:8192\n"
      "v_mfma_f32_16x16x32_bf16 v[204:207], a[204:207], v[216:219], v[204:207]\n"
      "v_mfma_f32_16x16x32_bf16 v[208:211], a[208:211], v[216:219], v[208:211]\n"
      "v_mfma_f32_16x16x32_bf16 v[212:215], a[212:215], v[216:219], v[212:215]\n"
      "s_waitcnt lgkmcnt(2)\n"
      "v_mfma_f32_16x16x32_bf16 v[0:3], a[216:219], v[220:223], v[0:3]\n"
      "v_mfma_f32_16x16x32_bf16 v[4:7], a[220:223], v[220:223], v[4:7]\n"
      "v_mfma_f32_16x16x32_bf16 v[8:11], a[224:227], v[220:223], v[8:11]\n"
      "ds_read_b128 v[216:219], v233 offset:12288\n"
      "v_mfma_f32_16x16x32_bf16 v[12:15], a[228:231], v[220:223], v[12:15]\n"
      "v_mfma_f32_16x16x32_bf16 v[16:19], a[232:235], v[220:223], v[16:19]\n"
      "s_add_u32 s52, s42, s32\n"
      "s_addc_u32 s53, s43, 0\n"
      "s_add_u32 m0, s17, 0x6000\n"
      "v_mfma_f32_16x16x32_bf16 v[20:23], a[236:239], v[220:223], v[20:23]\n"
      "global_load_lds_dwordx4 v236, s[52:53] offset:0 \n"
      "s_waitcnt lgkmcnt(2)\n"
      "v_mfma_f32_16x16x32_bf16 v[24:27], a[216:219], v[224:227], v[24:27]\n"
      "v_mfma_f32_16x16x32_bf16 v[28:31], a[220:223], v[224:227], v[28:31]\n"
      "s_add_u32 m0, s17, 0x7f00\n"
      "v_mfma_f32_16x16x32_bf16 v[32:35], a[224:227], v[224:227], v[32:35]\n"
      "ds_read_b128 v[220:223], v233 offset:16384\n"
      "global_load_lds_dwordx4 v236, s[52:53] offset:256 \n"
      "v_mfma_f32_16x16x32_bf16 v[36:39], a[228:231], v[224:227], v[36:39]\n"
      "v_mfma_f32_16x16x32_bf16 v[40:43], a[232:235], v[224:227], v[40:43]\n"
      "s_add_u32 m0, s17, 0x9e00\n"
      "v_mfma_f32_16x16x32_bf16 v[44:47], a[236:239], v[224:227], v[44:47]\n"
      "global_load_lds_dwordx4 v236, s[52:53] offset:512 \n"
      "s_waitcnt lgkmcnt(2)\n"
      "v_mfma_f32_16x16x32_bf16 v[48:51], a[216:219], v[228:231], v[48:51]\n"
      "v_mfma_f32_16x16x32_bf16 v[52:55], a[220:223], v[228:231], v[52:55]\n"
      "s_add_u32 m0, s17, 0x7000\n"
      "v_mfma_f32_16x16x32_bf16 v[56:59], a[224:227], v[228:231], v[56:59]\n"
      "ds_read_b128 v[224:227], v233 offset:20480\n"
      "global_load_lds_dwordx4 v237, s[52:53] offset:0 \n"
      "v_mfma_f32_16x16x32_bf16 v[60:63], a[228:231], v[228:231], v[60:63]\n"
      "v_mfma_f32_16x16x32_bf16 v[64:67], a[232:235], v[228:231], v[64:67]\n"
      "s_add_u32 m0, s17, 0x8f00\n"
      "v_mfma_f32_16x16x32_bf16 v[68:71], a[236:239], v[228:231], v[68:71]\n"
      "global_load_lds_dwordx4 v237, s[52:53] offset:256 \n"
      "s_waitcnt lgkmcnt(2)\n"
      "v_mfma_f32_16x16x32_bf16 v[72:75], a[216:219], v[216:219], v[72:75]\n"
      "v_mfma_f32_16x16x32_bf16 v[76:79], a[220:223], v[216:219], v[76:79]\n"
      "s_add_u32 m0, s17, 0xae00\n"
      "v_mfma_f32_16x16x32_bf16 v[80:83], a[224:227], v[216:219], v[80:83]\n"
      "ds_read_b128 v[228:231], v233 offset:24576\n"
      "global_load_lds_dwordx4 v237, s[52:53] offset:512 \n"
      "v_mfma_f32_16x16x32_bf16 v[84:87], a[228:231], v[216:219], v[84:87]\n"
      "v_mfma_f32_16x16x32_bf16 v[88:91], a[232:235], v[216:219], v[88:91]\n"
      "v_mfma_f32_16x16x32_bf16 v[92:95], a[236:239], v[216:219], v[92:95]\n"
      "s_waitcnt lgkmcnt(2)\n"
      "v_mfma_f32_16x16x32_bf16 v[96:99], a[216:219], v[220:223], v[96:99]\n"
      "v_mfma_f32_16x16x32_bf16 v[100:103], a[220:223], v[220:223], v[100:103]\n"
      "v_mfma_f32_16x16x32_bf16 v[104:107], a[224:227], v[220:223], v[104:107]\n"
      "ds_read_b128 v[216:219], v233 offset:28672\n"
      "v_mfma_f32_16x16x32_bf16 v[108:111], a[228:231], v[220:223], v[108:111]\n"
      "v_mfma_f32_16x16x32_bf16 v[112:115], a[232:235], v[220:223], v[112:115]\n"
      "v_mfma_f32_16x16x32_bf16 v[116:119], a[236:239], v[220:223], v[116:119]\n"
      "s_waitcnt lgkmcnt(2)\n"
      "v_mfma_f32_16x16x32_bf16 v[120:123], a[216:219], v[224:227], v[120:123]\n"
      "v_mfma_f32_16x16x32_bf16 v[124:127], a[220:223], v[224:227], v[124:127]\n"
      "v_mfma_f32_16x16x32_bf16 v[128:131], a[224:227], v[224:227], v[128:131]\n"
      "ds_read_b128 v[220:223], v233 offset:32768\n"
      "v_add_u32 v233, v249, v233\n"
      "v_mfma_f32_16x16x32_bf16 v[132:135], a[228:231], v[224:227], v[132:135]\n"
      "v_mfma_f32_16x16x32_bf16 v[136:139], a[232:235], v[224:227], v[136:139]\n"
      "v_mfma_f32_16x16x32_bf16 v[140:143], a[236:239], v[224:227], v[140:143]\n"
      "s_waitcnt lgkmcnt(2)\n"
      "v_mfma_f32_16x16x32_bf16 v[144:147], a[216:219], v[228:231], v[144:147]\n"
      "v_mfma_f32_16x16x32_bf16 v[148:151], a[220:223], v[228:231], v[148:151]\n"
      "v_mfma_f32_16x16x32_bf16 v[152:155], a[224:227], v[228:231], v[152:155]\n"
      "ds_read_b128 v[224:227], v234 offset:0\n"
      "v_mfma_f32_16x16x32_bf16 v[156:159], a[228:231], v[228:231], v[156:159]\n"
      "v_mfma_f32_16x16x32_bf16 v[160:163], a[232:235], v[228:231], v[160:163]\n"
      "v_mfma_f32_16x16x32_bf16 v[164:167], a[236:239], v[228:231], v[164:167]\n"
      "s_waitcnt lgkmcnt(2)\n"
      "v_mfma_f32_16x16x32_bf16 v[168:171], a[216:219], v[216:219], v[168:171]\n"
      "v_mfma_f32_16x16x32_bf16 v[172:175], a[220:223], v[216:219], v[172:175]\n"
      "v_mfma_f32_16x16x32_bf16 v[176:179], a[224:227], v[216:219], v[176:179]\n"
      "ds_read_b128 v[228:231], v234 offset:4096\n"
      "v_mfma_f32_16x16x32_bf16 v[180:183], a[228:231], v[216:219], v[180:183]\n"
      "v_mfma_f32_16x16x32_bf16 v[184:187], a[232:235], v[216:219], v[184:187]\n"
      "v_mfma_f32_16x16x32_bf16 v[188:191], a[236:239], v[216:219], v[188:191]\n"
      "s_waitcnt lgkmcnt(2)\n"
      "v_mfma_f32_16x16x32_bf16 v[192:195], a[216:219], v[220:223], v[192:195]\n"
      "v_mfma_f32_16x16x32_bf16 v[196:199], a[220:223], v[220:223], v[196:199]\n"
      "v_mfma_f32_16x16x32_bf16 v[200:203], a[224:227], v[220:223], v[200:203]\n"
      "ds_read_b128 v[216:219], v234 offset:8192\n"
      "v_mfma_f32_16x16x32_bf16 v[204:207], a[228:231], v[220:223], v[204:207]\n"
      "v_mfma_f32_16x16x32_bf16 v[208:211], a[232:235], v[220:223], v[208:211]\n"
      "v_mfma_f32_16x16x32_bf16 v[212:215], a[236:239], v[220:223], v[212:215]\n"
      "s_waitcnt lgkmcnt(2)\n"
      "v_mfma_f32_16x16x32_bf16 v[0:3], a[0:3], v[224:227], v[0:3]\n"
      "ds_read_b128 v[244:247], v250\n"
      "v_mfma_f32_16x16x32_bf16 v[4:7], a[4:7], v[224:227], v[4:7]\n"
      "ds_read_b64 v[252:253], v250 offset:4096\n"
      "v_mfma_f32_16x16x32_bf16 v[8:11], a[8:11], v[224:227], v[8:11]\n"
      "ds_read_b128 v[220:223], v234 offset:12288\n"
      "v_mfma_f32_16x16x32_bf16 v[12:15], a[12:15], v[224:227], v[12:15]\n"
      "v_mfma_f32_16x16x32_bf16 v[16:19], a[16:19], v[224:227], v[16:19]\n"
      "v_mfma_f32_16x16x32_bf16 v[20:23], a[20:23], v[224:227], v[20:23]\n"
      "s_waitcnt lgkmcnt(4)\n"
      "v_mfma_f32_16x16x32_bf16 v[24:27], a[0:3], v[228:231], v[24:27]\n"
      "v_mfma_f32_16x16x32_bf16 v[28:31], a[4:7], v[228:231], v[28:31]\n"
      "v_mfma_f32_16x16x32_bf16 v[32:35], a[8:11], v[228:231], v[32:35]\n"
      "ds_read_b128 v[224:227], v234 offset:16384\n"
      "v_mfma_f32_16x16x32_bf16 v[36:39], a[12:15], v[228:231], v[36:39]\n"
      "global_load_dwordx4 a[172:175], v239, s[58:59] offset:3072 sc1\n"
      "v_mfma_f32_16x16x32_bf16 v[40:43], a[16:19], v[228:231], v[40:43]\n"
      "v_mfma_f32_16x16x32_bf16 v[44:47], a[20:23], v[228:231], v[44:47]\n"
      "s_waitcnt lgkmcnt(4)\n"
      "v_mfma_f32_16x16x32_bf16 v[48:51], a[0:3], v[216:219], v[48:51]\n"
      "v_mfma_f32_16x16x32_bf16 v[52:55], a[4:7], v[216:219], v[52:55]\n"
      "s_add_u32 s58, s24, 0x9000\n"
      "s_addc_u32 s59, s25, 0\n"
      "global_load_dwordx4 a[176:179], v239, s[58:59] offset:0 sc1\n"
      "v_mfma_f32_16x16x32_bf16 v[56:59], a[8:11], v[216:219], v[56:59]\n"
      "ds_read_b128 v[228:231], v234 offset:20480\n"
      "v_mfma_f32_16x16x32_bf16 v[60:63], a[12:15], v[216:219], v[60:63]\n"
      "v_mfma_f32_16x16x32_bf16 v[64:67], a[16:19], v[216:219], v[64:67]\n"
      "v_mfma_f32_16x16x32_bf16 v[68:71], a[20:23], v[216:219], v[68:71]\n"
      "global_load_dwordx4 a[180:183], v239, s[58:59] offset:1024 sc1\n"
      "s_waitcnt lgkmcnt(2)\n"
      "v_mfma_f32_16x16x32_bf16 v[72:75], a[0:3], v[220:223], v[72:75]\n"
      "v_mfma_f32_16x16x32_bf16 v[76:79], a[4:7], v[220:223], v[76:79]\n"
      "v_mfma_f32_16x16x32_bf16 v[80:83], a[8:11], v[220:223], v[80:83]\n"
      "ds_read_b128 v[216:219], v234 offset:24576\n"
      "v_mfma_f32_16x16x32_bf16 v[84:87], a[12:15], v[220:223], v[84:87]\n"
      "global_load_dwordx4 a[184:187], v239, s[58:59] offset:2048 sc1\n"
      "v_mfma_f32_16x16x32_bf16 v[88:91], a[16:19], v[220:223], v[88:91]\n"
      "v_mfma_f32_16x16x32_bf16 v[92:95], a[20:23], v[220:223], v[92:95]\n"
      "s_waitcnt lgkmcnt(2)\n"
      "v_mfma_f32_16x16x32_bf16 v[96:99], a[0:3], v[224:227], v[96:99]\n"
      "v_mfma_f32_16x16x32_bf16 v[100:103], a[4:7], v[224:227], v[100:103]\n"
      "global_load_dwordx4 a[188:191], v239, s[58:59] offset:3072 sc1\n"
      "v_mfma_f32_16x16x32_bf16 v[104:107], a[8:11], v[224:227], v[104:107]\n"
      "ds_read_b128 v[220:223], v234 offset:28672\n"
      "v_mfma_f32_16x16x32_bf16 v[108:111], a[12:15], v[224:227], v[108:111]\n"
      "v_mfma_f32_16x16x32_bf16 v[112:115], a[16:19], v[224:227], v[112:115]\n"
      "v_mfma_f32_16x16x32_bf16 v[116:119], a[20:23], v[224:227], v[116:119]\n"
      "s_add_u32 s58, s24, 0xa000\n"
      "s_addc_u32 s59, s25, 0\n"
      "global_load_dwordx4 a[192:195], v239, s[58:59] offset:0 sc1\n"
      "s_waitcnt lgkmcnt(2)\n"
      "v_mfma_f32_16x16x32_bf16 v[120:123], a[0:3], v[228:231], v[120:123]\n"
      "v_mfma_f32_16x16x32_bf16 v[124:127], a[4:7], v[228:231], v[124:127]\n"
      "v_mfma_f32_16x16x32_bf16 v[128:131], a[8:11], v[228:231], v[128:131]\n"
      "ds_read_b128 v[224:227], v234 offset:32768\n"
      "v_add_u32 v234, v249, v234\n"
      "v_mfma_f32_16x16x32_bf16 v[132:135], a[12:15], v[228:231], v[132:135]\n"
      "global_load_dwordx4 a[196:199], v239, s[58:59] offset:1024 sc1\n"
      "v_mfma_f32_16x16x32_bf16 v[136:139], a[16:19], v[228:231], v[136:139]\n"
      "v_mfma_f32_16x16x32_bf16 v[140:143], a[20:23], v[228:231], v[140:143]\n"
      "s_waitcnt lgkmcnt(2)\n"
      "v_mfma_f32_16x16x32_bf16 v[144:147], a[0:3], v[216:219], v[144:147]\n"
      "v_mfma_f32_16x16x32_bf16 v[148:151], a[4:7], v[216:219], v[148:151]\n"
      "global_load_dwordx4 a[200:203], v239, s[58:59] offset:2048 sc1\n"
      "v_mfma_f32_16x16x32_bf16 v[152:155], a[8:11], v[216:219], v[152:155]\n"
      "ds_read_b128 v[228:231], v235 offset:4096\n"
      "v_mfma_f32_16x16x32_bf16 v[156:159], a[12:15], v[216:219], v[156:159]\n"
      "v_mfma_f32_16x16x32_bf16 v[160:163], a[16:19], v[216:219], v[160:163]\n"
      "v_mfma_f32_16x16x32_bf16 v[164:167], a[20:23], v[216:219], v[164:167]\n"
      "global_load_dwordx4 a[204:207], v239, s[58:59] offset:3072 sc1\n"
      "s_waitcnt lgkmcnt(2)\n"
      "v_mfma_f32_16x16x32_bf16 v[168:171], a[0:3], v[220:223], v[168:171]\n"
      "v_mfma_f32_16x16x32_bf16 v[172:175], a[4:7], v[220:223], v[172:175]\n"
      "v_mfma_f32_16x16x32_bf16 v[176:179], a[8:11], v[220:223], v[176:179]\n"
      "ds_read_b128 v[216:219], v235 offset:8192\n"
      "v_mfma_f32_16x16x32_bf16 v[180:183], a[12:15], v[220:223], v[180:183]\n"
      "s_add_u32 s58, s24, 0xb000\n"
      "s_addc_u32 s59, s25, 0\n"
      "global_load_dwordx4 a[208:211], v239, s[58:59] offset:0 sc1\n"
      "v_mfma_f32_16x16x32_bf16 v[184:187], a[16:19], v[220:223], v[184:187]\n"
      "v_mfma_f32_16x16x32_bf16 v[188:191], a[20:23], v[220:223], v[188:191]\n"
      "s_waitcnt lgkmcnt(2)\n"
      "v_mfma_f32_16x16x32_bf16 v[192:195], a[0:3], v[224:227], v[192:195]\n"
      "v_mfma_f32_16x16x32_bf16 v[196:199], a[4:7], v[224:227], v[196:199]\n"
      "global_load_dwordx4 a[212:215], v239, s[58:59] offset:1024 sc1\n"
      "v_mfma_f32_16x16x32_bf16 v[200:203], a[8:11], v[224:227], v[200:203]\n"
      "ds_read_b128 v[220:223], v235 offset:12288\n"
      "v_mfma_f32_16x16x32_bf16 v[204:207], a[12:15], v[224:227], v[204:207]\n"
      "v_mfma_f32_16x16x32_bf16 v[208:211], a[16:19], v[224:227], v[208:211]\n"
      "v_mfma_f32_16x16x32_bf16 v[212:215], a[20:23], v[224:227], v[212:215]\n"
      "global_load_dwordx4 a[216:219], v239, s[58:59] offset:2048 sc1\n"
      "s_waitcnt lgkmcnt(2)\n"
      "v_mfma_f32_16x16x32_bf16 v[24:27], a[24:27], v[228:231], v[24:27]\n"
      "v_mfma_f32_16x16x32_bf16 v[28:31], a[28:31], v[228:231], v[28:31]\n"
      "v_mfma_f32_16x16x32_bf16 v[32:35], a[32:35], v[228:231], v[32:35]\n"
      "ds_read_b128 v[224:227], v235 offset:16384\n"
      "v_mfma_f32_16x16x32_bf16 v[36:39], a[36:39], v[228:231], v[36:39]\n"
      "global_load_dwordx4 a[220:223], v239, s[58:59] offset:3072 sc1\n"
      "v_mfma_f32_16x16x32_bf16 v[40:43], a[40:43], v[228:231], v[40:43]\n"
      "v_mfma_f32_16x16x32_bf16 v[44:47], a[44:47], v[228:231], v[44:47]\n"
      "s_waitcnt lgkmcnt(2)\n"
      "v_mfma_f32_16x16x32_bf16 v[48:51], a[24:27], v[216:219], v[48:51]\n"
      "v_cvt_pk_bf16_f32 v24, v24, v25\n"
      "v_cvt_pk_bf16_f32 v25, v26, v27\n"
      "v_mfma_f32_16x16x32_bf16 v[52:55], a[28:31], v[216:219], v[52:55]\n"
      "v_cvt_pk_bf16_f32 v26, v28, v29\n"
      "v_cvt_pk_bf16_f32 v27, v30, v31\n"
      "s_add_u32 s58, s24, 0xc000\n"
      "s_addc_u32 s59, s25, 0\n"
      "global_load_dwordx4 a[224:227], v239, s[58:59] offset:0 sc1\n"
      "v_mfma_f32_16x16x32_bf16 v[56:59], a[32:35], v[216:219], v[56:59]\n"
      "ds_read_b128 v[228:231], v235 offset:20480\n"
      "v_cvt_pk_bf16_f32 v28, v32, v33\n"
      "v_cvt_pk_bf16_f32 v29, v34, v35\n"
      "v_mfma_f32_16x16x32_bf16 v[60:63], a[36:39], v[216:219], v[60:63]\n"
      "v_cvt_pk_bf16_f32 v30, v36, v37\n"
      "v_cvt_pk_bf16_f32 v31, v38, v39\n"
      "v_mfma_f32_16x16x32_bf16 v[64:67], a[40:43], v[216:219], v[64:67]\n"
      "v_cvt_pk_bf16_f32 v32, v40, v41\n"
      "v_cvt_pk_bf16_f32 v33, v42, v43\n"
      "v_mfma_f32_16x16x32_bf16 v[68:71], a[44:47], v[216:219], v[68:71]\n"
      "v_cvt_pk_bf16_f32 v34, v44, v45\n"
      "v_cvt_pk_bf16_f32 v35, v46, v47\n"
      "global_load_dwordx4 a[228:231], v239, s[58:59] offset:1024 sc1\n"
      "s_waitcnt lgkmcnt(2)\n"
      "v_mfma_f32_16x16x32_bf16 v[72:75], a[24:27], v[220:223], v[72:75]\n"
      "v_cvt_pk_bf16_f32 v48, v48, v49\n"
      "v_cvt_pk_bf16_f32 v49, v50, v51\n"
      "v_mfma_f32_16x16x32_bf16 v[76:79], a[28:31], v[220:223], v[76:79]\n"
      "v_cvt_pk_bf16_f32 v50, v52, v53\n"
      "v_cvt_pk_bf16_f32 v51, v54, v55\n"
      "v_mfma_f32_16x16x32_bf16 v[80:83], a[32:35], v[220:223], v[80:83]\n"
      "ds_read_b128 v[216:219], v235 offset:24576\n"
      "v_cvt_pk_bf16_f32 v52, v56, v57\n"
      "v_cvt_pk_bf16_f32 v53, v58, v59\n"
      "v_mfma_f32_16x16x32_bf16 v[84:87], a[36:39], v[220:223], v[84:87]\n"
      "v_cvt_pk_bf16_f32 v54, v60, v61\n"
      "v_cvt_pk_bf16_f32 v55, v62, v63\n"
      "global_load_dwordx4 a[232:235], v239, s[58:59] offset:2048 sc1\n"
      "v_mfma_f32_16x16x32_bf16 v[88:91], a[40:43], v[220:223], v[88:91]\n"
      "v_cvt_pk_bf16_f32 v56, v64, v65\n"
      "v_cvt_pk_bf16_f32 v57, v66, v67\n"
      "v_mfma_f32_16x16x32_bf16 v[92:95], a[44:47], v[220:223], v[92:95]\n"
      "v_cvt_pk_bf16_f32 v58, v68, v69\n"
      "v_cvt_pk_bf16_f32 v59, v70, v71\n"
      "s_waitcnt lgkmcnt(2)\n"
      "v_mfma_f32_16x16x32_bf16 v[96:99], a[24:27], v[224:227], v[96:99]\n"
      "v_cvt_pk_bf16_f32 v72, v72, v73\n"
      "v_cvt_pk_bf16_f32 v73, v74, v75\n"
      "v_mfma_f32_16x16x32_bf16 v[100:103], a[28:31], v[224:227], v[100:103]\n"
      "v_cvt_pk_bf16_f32 v74, v76, v77\n"
      "v_cvt_pk_bf16_f32 v75, v78, v79\n"
      "global_load_dwordx4 a[236:239], v239, s[58:59] offset:3072 sc1\n"
      "v_mfma_f32_16x16x32_bf16 v[104:107], a[32:35], v[224:227], v[104:107]\n"
      "ds_read_b128 v[220:223], v235 offset:28672\n"
      "v_cvt_pk_bf16_f32 v76, v80, v81\n"
      "v_cvt_pk_bf16_f32 v77, v82, v83\n"
      "LPD6:\n"
      "s_cbranch_execz LPD7\n"
      "v_mfma_f32_16x16x32_bf16 v[108:111], a[36:39], v[224:227], v[108:111]\n"
      "v_cvt_pk_bf16_f32 v78, v84, v85\n"
      "v_cvt_pk_bf16_f32 v79, v86, v87\n"
      "v_mfma_f32_16x16x32_bf16 v[112:115], a[40:43], v[224:227], v[112:115]\n"
      "v_cvt_pk_bf16_f32 v80, v88, v89\n"
      "v_cvt_pk_bf16_f32 v81, v90, v91\n"
      "v_mfma_f32_16x16x32_bf16 v[116:119], a[44:47], v[224:227], v[116:119]\n"
      "v_cvt_pk_bf16_f32 v82, v92, v93\n"
      "v_cvt_pk_bf16_f32 v83, v94, v95\n"
      "s_add_u32 s58, s24, 0xd000\n"
      "s_addc_u32 s59, s25, 0\n"
      "global_load_dwordx4 a[0:3], v239, s[58:59] offset:0 sc1\n"
      "s_waitcnt lgkmcnt(2)\n"
      "v_mfma_f32_16x16x32_bf16 v[120:123], a[24:27], v[228:231], v[120:123]\n"
      "v_cvt_pk_bf16_f32 v96, v96, v97\n"
      "v_cvt_pk_bf16_f32 v97, v98, v99\n"
      "v_mfma_f32_16x16x32_bf16 v[124:127], a[28:31], v[228:231], v[124:127]\n"
      "v_cvt_pk_bf16_f32 v98, v100, v101\n"
      "v_cvt_pk_bf16_f32 v99, v102, v103\n"
      "v_mfma_f32_16x16x32_bf16 v[128:131], a[32:35], v[228:231], v[128:131]\n"
      "ds_read_b128 v[224:227], v235 offset:32768\n"
      "v_cvt_pk_bf16_f32 v100, v104, v105\n"
      "v_cvt_pk_bf16_f32 v101, v106, v107\n"
      "v_mfma_f32_16x16x32_bf16 v[132:135], a[36:39], v[228:231], v[132:135]\n"
      "v_cvt_pk_bf16_f32 v102, v108, v109\n"
      "v_cvt_pk_bf16_f32 v103, v110, v111\n"
      "global_load_dwordx4 a[4:7], v239, s[58:59] offset:1024 sc1\n"
      "v_mfma_f32_16x16x32_bf16 v[136:139], a[40:43], v[228:231], v[136:139]\n"
      "v_cvt_pk_bf16_f32 v104, v112, v113\n"
      "v_cvt_pk_bf16_f32 v105, v114, v115\n"
      "v_mfma_f32_16x16x32_bf16 v[140:143], a[44:47], v[228:231], v[140:143]\n"
      "v_cvt_pk_bf16_f32 v106, v116, v117\n"
      "v_cvt_pk_bf16_f32 v107, v118, v119\n"
      "s_waitcnt lgkmcnt(2)\n"
      "v_mfma_f32_16x16x32_bf16 v[144:147], a[24:27], v[216:219], v[144:147]\n"
      "v_cvt_pk_bf16_f32 v120, v120, v121\n"
      "v_cvt_pk_bf16_f32 v121, v122, v123\n"
      "v_mfma_f32_16x16x32_bf16 v[148:151], a[28:31], v[216:219], v[148:151]\n"
      "v_cvt_pk_bf16_f32 v122, v124, v125\n"
      "v_cvt_pk_bf16_f32 v123, v126, v127\n"
      "global_load_dwordx4 a[8:11], v239, s[58:59] offset:2048 sc1\n"
      "v_mfma_f32_16x16x32_bf16 v[152:155], a[32:35], v[216:219], v[152:155]\n"
      "ds_read_b128 v[228:231], v235 offset:0\n"
      "v_add_u32 v235, v249, v235\n"
      "s_waitcnt vmcnt(26)\n"
      "s_barrier\n"
      "v_cvt_pk_bf16_f32 v124, v128, v129\n"
      "v_cvt_pk_bf16_f32 v125, v130, v131\n"
      "v_mfma_f32_16x16x32_bf16 v[156:159], a[36:39], v[216:219], v[156:159]\n"
      "ds_read_b128 v[36:39], v232 offset:0\n"
      "v_cvt_pk_bf16_f32 v126, v132, v133\n"
      "v_cvt_pk_bf16_f32 v127, v134, v135\n"
      "v_mfma_f32_16x16x32_bf16 v[160:163], a[40:43], v[216:219], v[160:163]\n"
      "v_cvt_pk_bf16_f32 v128, v136, v137\n"
      "v_cvt_pk_bf16_f32 v129, v138, v139\n"
      "v_mfma_f32_16x16x32_bf16 v[164:167], a[44:47], v[216:219], v[164:167]\n"
      "ds_read_b128 v[40:43], v233 offset:0\n"
      "v_cvt_pk_bf16_f32 v130, v140, v141\n"
      "v_cvt_pk_bf16_f32 v131, v142, v143\n"
      "global_load_dwordx4 a[12:15], v239, s[58:59] offset:3072 sc1\n"
      "s_waitcnt lgkmcnt(4)\n"
      "v_mfma_f32_16x16x32_bf16 v[168:171], a[24:27], v[220:223], v[168:171]\n"
      "v_cvt_pk_bf16_f32 v144, v144, v145\n"
      "v_cvt_pk_bf16_f32 v145, v146, v147\n"
      "v_mfma_f32_16x16x32_bf16 v[172:175], a[28:31], v[220:223], v[172:175]\n"
      "ds_read_b128 v[44:47], v234 offset:0\n"
      "v_cvt_pk_bf16_f32 v146, v148, v149\n"
      "v_cvt_pk_bf16_f32 v147, v150, v151\n"
      "v_mfma_f32_16x16x32_bf16 v[176:179], a[32:35], v[220:223], v[176:179]\n"
      "v_cvt_pk_bf16_f32 v148, v152, v153\n"
      "v_cvt_pk_bf16_f32 v149, v154, v155\n"
      "v_mfma_f32_16x16x32_bf16 v[180:183], a[36:39], v[220:223], v[180:183]\n"
      "ds_read_b128 v[60:63], v235 offset:0\n"
      "v_cvt_pk_bf16_f32 v150, v156, v157\n"
      "v_cvt_pk_bf16_f32 v151, v158, v159\n"
      "s_add_u32 s58, s24, 0xe000\n"
      "s_addc_u32 s59, s25, 0\n"
      "global_load_dwordx4 a[16:19], v239, s[58:59] offset:0 sc1\n"
      "v_mfma_f32_16x16x32_bf16 v[184:187], a[40:43], v[220:223], v[184:187]\n"
      "v_cvt_pk_bf16_f32 v152, v160, v161\n"
      "v_cvt_pk_bf16_f32 v153, v162, v163\n"
      "v_mfma_f32_16x16x32_bf16 v[188:191], a[44:47], v[220:223], v[188:191]\n"
      "ds_read_b128 v[64:67], v232 offset:8192\n"
      "v_cvt_pk_bf16_f32 v154, v164, v165\n"
      "v_cvt_pk_bf16_f32 v155, v166, v167\n"
      "s_waitcnt lgkmcnt(6)\n"
      "v_mfma_f32_16x16x32_bf16 v[192:195], a[24:27], v[224:227], v[192:195]\n"
      "v_cvt_pk_bf16_f32 v168, v168, v169\n"
      "v_cvt_pk_bf16_f32 v169, v170, v171\n"
      "v_mfma_f32_16x16x32_bf16 v[196:199], a[28:31], v[224:227], v[196:199]\n"
      "ds_read_b128 v[68:71], v233 offset:8192\n"
      "v_cvt_pk_bf16_f32 v170, v172, v173\n"
      "v_cvt_pk_bf16_f32 v171, v174, v175\n"
      "global_load_dwordx4 a[20:23], v239, s[58:59] offset:1024 sc1\n"
      "v_mfma_f32_16x16x32_bf16 v[200:203], a[32:35], v[224:227], v[200:203]\n"
      "v_cvt_pk_bf16_f32 v172, v176, v177\n"
      "v_cvt_pk_bf16_f32 v173, v178, v179\n"
      "v_mfma_f32_16x16x32_bf16 v[204:207], a[36:39], v[224:227], v[204:207]\n"
      "ds_read_b128 v[84:87], v234 offset:8192\n"
      "v_cvt_pk_bf16_f32 v174, v180, v181\n"
      "v_cvt_pk_bf16_f32 v175, v182, v183\n"
      "v_mfma_f32_16x16x32_bf16 v[208:211], a[40:43], v[224:227], v[208:211]\n"
      "v_cvt_pk_bf16_f32 v176, v184, v185\n"
      "v_cvt_pk_bf16_f32 v177, v186, v187\n"
      "v_mfma_f32_16x16x32_bf16 v[212:215], a[44:47], v[224:227], v[212:215]\n"
      "ds_read_b128 v[88:91], v235 offset:8192\n"
      "v_cvt_pk_bf16_f32 v178, v188, v189\n"
      "v_cvt_pk_bf16_f32 v179, v190, v191\n"
      "global_load_dwordx4 v[140:143], v239, s[58:59] offset:2048 sc1\n"
      "s_waitcnt lgkmcnt(8)\n"
      "v_mfma_f32_16x16x32_bf16 v[0:3], a[24:27], v[228:231], v[0:3]\n"
      "v_cvt_pk_bf16_f32 v192, v192, v193\n"
      "v_cvt_pk_bf16_f32 v193, v194, v195\n"
      "v_mfma_f32_16x16x32_bf16 v[4:7], a[28:31], v[228:231], v[4:7]\n"
      "ds_read_b128 v[92:95], v232 offset:16384\n"
      "v_cvt_pk_bf16_f32 v194, v196, v197\n"
      "v_cvt_pk_bf16_f32 v195, v198, v199\n"
      "v_mfma_f32_16x16x32_bf16 v[8:11], a[32:35], v[228:231], v[8:11]\n"
      "v_cvt_pk_bf16_f32 v196, v200, v201\n"
      "v_cvt_pk_bf16_f32 v197, v202, v203\n"
      "v_mfma_f32_16x16x32_bf16 v[12:15], a[36:39], v[228:231], v[12:15]\n"
      "ds_read_b128 v[108:111], v233 offset:16384\n"
      "v_cvt_pk_bf16_f32 v198, v204, v205\n"
      "v_cvt_pk_bf16_f32 v199, v206, v207\n"
      "global_load_dwordx4 v[156:159], v239, s[58:59] offset:3072 sc1\n"
      "v_mfma_f32_16x16x32_bf16 v[16:19], a[40:43], v[228:231], v[16:19]\n"
      "ds_read_b128 v[112:115], v234 offset:16384\n"
      "v_cvt_pk_bf16_f32 v200, v208, v209\n"
      "v_cvt_pk_bf16_f32 v201, v210, v211\n"
      "v_mfma_f32_16x16x32_bf16 v[20:23], a[44:47], v[228:231], v[20:23]\n"
      "ds_read_b128 v[116:119], v235 offset:16384\n"
      "v_cvt_pk_bf16_f32 v202, v212, v213\n"
      "v_cvt_pk_bf16_f32 v203, v214, v215\n"
      "s_waitcnt lgkmcnt(8)\n"
      "v_mfma_f32_16x16x32_bf16 v[132:135], a[240:243], v[36:39], v[0:3]\n"
      "s_mov_b64 vcc, 0\n"
      "v_mfma_f32_16x16x32_bf16 v[132:135], a[244:247], v[40:43], v[132:135]\n"
      "s_add_u32 s58, s24, 0xf000\n"
      "s_addc_u32 s59, s25, 0\n"
      "global_load_dwordx4 v[160:163], v239, s[58:59] offset:0 sc1\n"
      "v_mfma_f32_16x16x32_bf16 v[132:135], a[248:251], v[44:47], v[132:135]\n"
      "v_mfma_f32_16x16x32_bf16 v[132:135], a[252:255], v[60:63], v[132:135]\n"
      "s_waitcnt lgkmcnt(4)\n"
      "v_mfma_f32_16x16x32_bf16 v[132:135], a[48:51], v[64:67], v[132:135]\n"
      "global_load_dwordx4 v[164:167], v239, s[58:59] offset:1024 sc1\n"
      "v_mfma_f32_16x16x32_bf16 v[132:135], a[52:55], v[68:71], v[132:135]\n"
      "v_mfma_f32_16x16x32_bf16 v[132:135], a[56:59], v[84:87], v[132:135]\n"
      "v_mfma_f32_16x16x32_bf16 v[132:135], a[60:63], v[88:91], v[132:135]\n"
      "global_load_dwordx4 v[180:183], v239, s[58:59] offset:2048 sc1\n"
      "s_waitcnt lgkmcnt(0)\n"
      "v_mfma_f32_16x16x32_bf16 v[132:135], a[64:67], v[92:95], v[132:135]\n"
      "v_mfma_f32_16x16x32_bf16 v[132:135], a[68:71], v[108:111], v[132:135]\n"
      "v_mfma_f32_16x16x32_bf16 v[132:135], a[72:75], v[112:115], v[132:135]\n"
      "global_load_dwordx4 v[184:187], v239, s[58:59] offset:3072 sc1\n"
      "v_mfma_f32_16x16x32_bf16 v[132:135], a[76:79], v[116:119], v[132:135]\n"
      "v_mfma_f32_16x16x32_bf16 v[136:139], a[80:83], v[36:39], v[4:7]\n"
      "v_mfma_f32_16x16x32_bf16 v[136:139], a[84:87], v[40:43], v[136:139]\n"
      "s_add_u32 s58, s24, 0x10000\n"
      "s_addc_u32 s59, s25, 0\n"
      "global_load_dwordx4 v[188:191], v239, s[58:59] offset:0 sc1\n"
      "v_mfma_f32_16x16x32_bf16 v[136:139], a[88:91], v[44:47], v[136:139]\n"
      "v_mfma_f32_16x16x32_bf16 v[136:139], a[92:95], v[60:63], v[136:139]\n"
      "v_cvt_pk_bf16_f32 v208, v132, v133\n"
      "v_mfma_f32_16x16x32_bf16 v[136:139], a[96:99], v[64:67], v[136:139]\n"
      "global_load_dwordx4 v[204:207], v239, s[58:59] offset:1024 sc1\n"
      "v_mfma_f32_16x16x32_bf16 v[136:139], a[100:103], v[68:71], v[136:139]\n"
      "v_cvt_pk_bf16_f32 v209, v134, v135\n"
      "v_mfma_f32_16x16x32_bf16 v[136:139], a[104:107], v[84:87], v[136:139]\n"
      "v_mfma_f32_16x16x32_bf16 v[136:139], a[108:111], v[88:91], v[136:139]\n"
      "global_load_dwordx4 a[24:27], v239, s[58:59] offset:2048 sc1\n"
      "v_mfma_f32_16x16x32_bf16 v[136:139], a[112:115], v[92:95], v[136:139]\n"
      "v_mfma_f32_16x16x32_bf16 v[136:139], a[116:119], v[108:111], v[136:139]\n"
      "v_mfma_f32_16x16x32_bf16 v[136:139], a[120:123], v[112:115], v[136:139]\n"
      "global_load_dwordx4 a[28:31], v239, s[58:59] offset:3072 sc1\n"
      "v_mfma_f32_16x16x32_bf16 v[136:139], a[124:127], v[116:119], v[136:139]\n"
      "ds_write_b64 v244, v[208:209] offset:12288\n"
      "v_mfma_f32_16x16x32_bf16 v[132:135], a[128:131], v[36:39], v[8:11]\n"
      "v_mfma_f32_16x16x32_bf16 v[132:135], a[132:135], v[40:43], v[132:135]\n"
      "s_add_u32 s58, s24, 0x11000\n"
      "s_addc_u32 s59, s25, 0\n"
      "global_load_dwordx4 a[32:35], v239, s[58:59] offset:0 sc1\n"
      "v_mfma_f32_16x16x32_bf16 v[132:135], a[136:139], v[44:47], v[132:135]\n"
      "v_mfma_f32_16x16x32_bf16 v[132:135], a[140:143], v[60:63], v[132:135]\n"
      "v_cvt_pk_bf16_f32 v210, v136, v137\n"
      "v_mfma_f32_16x16x32_bf16 v[132:135], a[144:147], v[64:67], v[132:135]\n"
      "global_load_dwordx4 a[36:39], v239, s[58:59] offset:1024 sc1\n"
      "v_mfma_f32_16x16x32_bf16 v[132:135], a[148:151], v[68:71], v[132:135]\n"
      "v_cvt_pk_bf16_f32 v211, v138, v139\n"
      "v_mfma_f32_16x16x32_bf16 v[132:135], a[152:155], v[84:87], v[132:135]\n"
      "v_mfma_f32_16x16x32_bf16 v[132:135], a[156:159], v[88:91], v[132:135]\n"
      "global_load_dwordx4 a[40:43], v239, s[58:59] offset:2048 sc1\n"
      "s_waitcnt vmcnt(35)\n"
      "v_mfma_f32_16x16x32_bf16 v[132:135], a[160:163], v[92:95], v[132:135]\n"
      "v_mfma_f32_16x16x32_bf16 v[132:135], a[164:167], v[108:111], v[132:135]\n"
      "v_mfma_f32_16x16x32_bf16 v[132:135], a[168:171], v[112:115], v[132:135]\n"
      "global_load_dwordx4 a[44:47], v239, s[58:59] offset:3072 sc1\n"
      "v_mfma_f32_16x16x32_bf16 v[132:135], a[172:175], v[116:119], v[132:135]\n"
      "ds_write_b64 v244, v[210:211] offset:12296\n"
      "s_waitcnt vmcnt(32)\n"
      "v_mfma_f32_16x16x32_bf16 v[136:139], a[176:179], v[36:39], v[12:15]\n"
      "v_mfma_f32_16x16x32_bf16 v[136:139], a[180:183], v[40:43], v[136:139]\n"
      "s_mul_i32 s78, s90, s19\n"
      "v_mfma_f32_16x16x32_bf16 v[136:139], a[184:187], v[44:47], v[136:139]\n"
      "v_add_u32 v236, s78, v236\n"
      "v_mfma_f32_16x16x32_bf16 v[136:139], a[188:191], v[60:63], v[136:139]\n"
      "s_mul_i32 s78, s78, s99\n"
      "s_waitcnt vmcnt(28)\n"
      "v_mfma_f32_16x16x32_bf16 v[136:139], a[192:195], v[64:67], v[136:139]\n"
      "v_add_u32 v237, s78, v237\n"
      "v_mfma_f32_16x16x32_bf16 v[136:139], a[196:199], v[68:71], v[136:139]\n"
      "s_add_u32 s52, s20, s33\n"
      "s_addc_u32 s53, s21, 0\n"
      "s_add_u32 m0, s17, 0xc000\n"
      "v_mfma_f32_16x16x32_bf16 v[136:139], a[200:203], v[84:87], v[136:139]\n"
      "global_load_lds_dwordx4 v236, s[52:53] offset:0 \n"
      "v_mfma_f32_16x16x32_bf16 v[136:139], a[204:207], v[88:91], v[136:139]\n"
      "v_cvt_pk_bf16_f32 v212, v132, v133\n"
      "s_waitcnt vmcnt(25)\n"
      "v_mfma_f32_16x16x32_bf16 v[136:139], a[208:211], v[92:95], v[136:139]\n"
      "v_cvt_pk_bf16_f32 v213, v134, v135\n"
      "v_mfma_f32_16x16x32_bf16 v[136:139], a[212:215], v[108:111], v[136:139]\n"
      "v_mfma_f32_16x16x32_bf16 v[136:139], a[216:219], v[112:115], v[136:139]\n"
      "v_mfma_f32_16x16x32_bf16 v[136:139], a[220:223], v[116:119], v[136:139]\n"
      "s_add_u32 m0, s17, 0xdf00\n"
      "s_waitcnt vmcnt(21)\n"
      "v_mfma_f32_16x16x32_bf16 v[132:135], a[224:227], v[36:39], v[16:19]\n"
      "global_load_lds_dwordx4 v236, s[52:53] offset:256 \n"
      "v_mfma_f32_16x16x32_bf16 v[132:135], a[228:231], v[40:43], v[132:135]\n"
      "ds_write_b64 v245, v[212:213] offset:12288\n"
      "v_mfma_f32_16x16x32_bf16 v[132:135], a[232:235], v[44:47], v[132:135]\n"
      "v_mfma_f32_16x16x32_bf16 v[132:135], a[236:239], v[60:63], v[132:135]\n"
      "v_cvt_pk_bf16_f32 v214, v136, v137\n"
      "s_waitcnt vmcnt(18)\n"
      "v_mfma_f32_16x16x32_bf16 v[132:135], a[0:3], v[64:67], v[132:135]\n"
      "v_cvt_pk_bf16_f32 v215, v138, v139\n"
      "v_mfma_f32_16x16x32_bf16 v[132:135], a[4:7], v[68:71], v[132:135]\n"
      "s_add_u32 m0, s17, 0xfe00\n"
      "v_mfma_f32_16x16x32_bf16 v[132:135], a[8:11], v[84:87], v[132:135]\n"
      "global_load_lds_dwordx4 v236, s[52:53] offset:512 \n"
      "v_mfma_f32_16x16x32_bf16 v[132:135], a[12:15], v[88:91], v[132:135]\n"
      "s_waitcnt vmcnt(15)\n"
      "v_mfma_f32_16x16x32_bf16 v[132:135], a[16:19], v[92:95], v[132:135]\n"
      "v_mfma_f32_16x16x32_bf16 v[132:135], a[20:23], v[108:111], v[132:135]\n"
      "ds_write_b64 v245, v[214:215] offset:12296\n"
      "v_mfma_f32_16x16x32_bf16 v[132:135], v[140:143], v[112:115], v[132:135]\n"
      "v_mfma_f32_16x16x32_bf16 v[132:135], v[156:159], v[116:119], v[132:135]\n"
      "s_barrier\n"
      "s_add_u32 m0, s17, 0xd000\n"
      "s_waitcnt vmcnt(11)\n"
      "v_mfma_f32_16x16x32_bf16 v[136:139], v[160:163], v[36:39], v[20:23]\n"
      "ds_read_b128 v[36:39], v232 offset:256\n"
      "global_load_lds_dwordx4 v237, s[52:53] offset:0 \n"
      "v_mfma_f32_16x16x32_bf16 v[136:139], v[164:167], v[40:43], v[136:139]\n"
      "ds_read_b128 v[40:43], v233 offset:256\n"
      "v_mfma_f32_16x16x32_bf16 v[136:139], v[180:183], v[44:47], v[136:139]\n"
      "ds_read_b128 v[44:47], v234 offset:256\n"
      "v_mfma_f32_16x16x32_bf16 v[136:139], v[184:187], v[60:63], v[136:139]\n"
      "ds_read_b128 v[60:63], v235 offset:256\n"
      "v_cvt_pk_bf16_f32 v208, v132, v133\n"
      "s_waitcnt vmcnt(8)\n"
      "v_mfma_f32_16x16x32_bf16 v[136:139], v[188:191], v[64:67], v[136:139]\n"
      "ds_read_b128 v[64:67], v232 offset:8448\n"
      "v_cvt_pk_bf16_f32 v209, v134, v135\n"
      "v_mfma_f32_16x16x32_bf16 v[136:139], v[204:207], v[68:71], v[136:139]\n"
      "ds_read_b128 v[68:71], v233 offset:8448\n"
      "s_add_u32 m0, s17, 0xef00\n"
      "v_mfma_f32_16x16x32_bf16 v[136:139], a[24:27], v[84:87], v[136:139]\n"
      "ds_read_b128 v[84:87], v234 offset:8448\n"
      "global_load_lds_dwordx4 v237, s[52:53] offset:256 \n"
      "v_mfma_f32_16x16x32_bf16 v[136:139], a[28:31], v[88:91], v[136:139]\n"
      "ds_read_b128 v[88:91], v235 offset:8448\n"
      "s_waitcnt vmcnt(5)\n"
      "v_mfma_f32_16x16x32_bf16 v[136:139], a[32:35], v[92:95], v[136:139]\n"
      "ds_read_b128 v[92:95], v232 offset:16640\n"
      "v_mfma_f32_16x16x32_bf16 v[136:139], a[36:39], v[108:111], v[136:139]\n"
      "ds_read_b128 v[108:111], v233 offset:16640\n"
      "ds_write_b64 v244, v[208:209] offset:12416\n"
      "v_mfma_f32_16x16x32_bf16 v[136:139], a[40:43], v[112:115], v[136:139]\n"
      "ds_read_b128 v[112:115], v234 offset:16640\n"
      "v_mfma_f32_16x16x32_bf16 v[136:139], a[44:47], v[116:119], v[136:139]\n"
      "ds_read_b128 v[116:119], v235 offset:16640\n"
      "s_add_u32 m0, s17, 0x10e00\n"
      "s_waitcnt lgkmcnt(9)\n"
      "v_mfma_f32_16x16x32_bf16 v[132:135], a[240:243], v[36:39], v[0:3]\n"
      "global_load_lds_dwordx4 v237, s[52:53] offset:512 \n"
      "v_mfma_f32_16x16x32_bf16 v[132:135], a[244:247], v[40:43], v[132:135]\n"
      "v_mfma_f32_16x16x32_bf16 v[132:135], a[248:251], v[44:47], v[132:135]\n"
      "v_mfma_f32_16x16x32_bf16 v[132:135], a[252:255], v[60:63], v[132:135]\n"
      "v_cvt_pk_bf16_f32 v210, v136, v137\n"
      "s_waitcnt lgkmcnt(5)\n"
      "v_mfma_f32_16x16x32_bf16 v[132:135], a[48:51], v[64:67], v[132:135]\n"
      "v_cvt_pk_bf16_f32 v211, v138, v139\n"
      "v_mfma_f32_16x16x32_bf16 v[132:135], a[52:55], v[68:71], v[132:135]\n"
      "v_mfma_f32_16x16x32_bf16 v[132:135], a[56:59], v[84:87], v[132:135]\n"
      "s_add_u32 s52, s42, s33\n"
      "s_addc_u32 s53, s43, 0\n"
      "s_add_u32 m0, s17, 0x12000\n"
      "v_mfma_f32_16x16x32_bf16 v[132:135], a[60:63], v[88:91], v[132:135]\n"
      "global_load_lds_dwordx4 v236, s[52:53] offset:0 \n"
      "s_waitcnt lgkmcnt(0)\n"
      "v_mfma_f32_16x16x32_bf16 v[132:135], a[64:67], v[92:95], v[132:135]\n"
      "v_mfma_f32_16x16x32_bf16 v[132:135], a[68:71], v[108:111], v[132:135]\n"
      "ds_write_b64 v244, v[210:211] offset:12424\n"
      "v_mfma_f32_16x16x32_bf16 v[132:135], a[72:75], v[112:115], v[132:135]\n"
      "v_mfma_f32_16x16x32_bf16 v[132:135], a[76:79], v[116:119], v[132:135]\n"
      "v_mfma_f32_16x16x32_bf16 v[136:139], a[80:83], v[36:39], v[4:7]\n"
      "s_add_u32 m0, s17, 0x13f00\n"
      "v_mfma_f32_16x16x32_bf16 v[136:139], a[84:87], v[40:43], v[136:139]\n"
      "global_load_lds_dwordx4 v236, s[52:53] offset:256 \n"
      "v_mfma_f32_16x16x32_bf16 v[136:139], a[88:91], v[44:47], v[136:139]\n"
      "v_mfma_f32_16x16x32_bf16 v[136:139], a[92:95], v[60:63], v[136:139]\n"
      "v_cvt_pk_bf16_f32 v208, v132, v133\n"
      "v_mfma_f32_16x16x32_bf16 v[136:139], a[96:99], v[64:67], v[136:139]\n"
      "v_cvt_pk_bf16_f32 v209, v134, v135\n"
      "v_mfma_f32_16x16x32_bf16 v[136:139], a[100:103], v[68:71], v[136:139]\n"
      "v_mfma_f32_16x16x32_bf16 v[136:139], a[104:107], v[84:87], v[136:139]\n"
      "s_add_u32 m0, s17, 0x15e00\n"
      "v_mfma_f32_16x16x32_bf16 v[136:139], a[108:111], v[88:91], v[136:139]\n"
      "global_load_lds_dwordx4 v236, s[52:53] offset:512 \n"
      "v_mfma_f32_16x16x32_bf16 v[136:139], a[112:115], v[92:95], v[136:139]\n"
      "v_mfma_f32_16x16x32_bf16 v[136:139], a[116:119], v[108:111], v[136:139]\n"
      "ds_write_b64 v244, v[208:209] offset:24576\n"
      "v_mfma_f32_16x16x32_bf16 v[136:139], a[120:123], v[112:115], v[136:139]\n"
      "v_mfma_f32_16x16x32_bf16 v[136:139], a[124:127], v[116:119], v[136:139]\n"
      "v_mfma_f32_16x16x32_bf16 v[132:135], a[128:131], v[36:39], v[8:11]\n"
      "s_add_u32 m0, s17, 0x13000\n"
      "v_mfma_f32_16x16x32_bf16 v[132:135], a[132:135], v[40:43], v[132:135]\n"
      "global_load_lds_dwordx4 v237, s[52:53] offset:0 \n"
      "v_mfma_f32_16x16x32_bf16 v[132:135], a[136:139], v[44:47], v[132:135]\n"
      "v_mfma_f32_16x16x32_bf16 v[132:135], a[140:143], v[60:63], v[132:135]\n"
      "v_cvt_pk_bf16_f32 v210, v136, v137\n"
      "v_mfma_f32_16x16x32_bf16 v[132:135], a[144:147], v[64:67], v[132:135]\n"
      "v_cvt_pk_bf16_f32 v211, v138, v139\n"
      "v_mfma_f32_16x16x32_bf16 v[132:135], a[148:151], v[68:71], v[132:135]\n"
      "v_mfma_f32_16x16x32_bf16 v[132:135], a[152:155], v[84:87], v[132:135]\n"
      "s_add_u32 m0, s17, 0x14f00\n"
      "v_mfma_f32_16x16x32_bf16 v[132:135], a[156:159], v[88:91], v[132:135]\n"
      "global_load_lds_dwordx4 v237, s[52:53] offset:256 \n"
      "v_mfma_f32_16x16x32_bf16 v[132:135], a[160:163], v[92:95], v[132:135]\n"
      "v_mfma_f32_16x16x32_bf16 v[132:135], a[164:167], v[108:111], v[132:135]\n"
      "ds_write_b64 v244, v[210:211] offset:24584\n"
      "v_mfma_f32_16x16x32_bf16 v[132:135], a[168:171], v[112:115], v[132:135]\n"
      "v_mfma_f32_16x16x32_bf16 v[132:135], a[172:175], v[116:119], v[132:135]\n"
      "v_mfma_f32_16x16x32_bf16 v[136:139], a[176:179], v[36:39], v[12:15]\n"
      "s_add_u32 m0, s17, 0x16e00\n"
      "v_mfma_f32_16x16x32_bf16 v[136:139], a[180:183], v[40:43], v[136:139]\n"
      "global_load_lds_dwordx4 v237, s[52:53] offset:512 \n"
      "v_mfma_f32_16x16x32_bf16 v[136:139], a[184:187], v[44:47], v[136:139]\n"
      "v_mfma_f32_16x16x32_bf16 v[136:139], a[188:191], v[60:63], v[136:139]\n"
      "v_cvt_pk_bf16_f32 v212, v132, v133\n"
      "v_mfma_f32_16x16x32_bf16 v[136:139], a[192:195], v[64:67], v[136:139]\n"
      "v_cvt_pk_bf16_f32 v213, v134, v135\n"
      "v_mfma_f32_16x16x32_bf16 v[136:139], a[196:199], v[68:71], v[136:139]\n"
      "v_mfma_f32_16x16x32_bf16 v[136:139], a[200:203], v[84:87], v[136:139]\n"
      "v_mfma_f32_16x16x32_bf16 v[136:139], a[204:207], v[88:91], v[136:139]\n"
      "v_mfma_f32_16x16x32_bf16 v[136:139], a[208:211], v[92:95], v[136:139]\n"
      "v_mfma_f32_16x16x32_bf16 v[136:139], a[212:215], v[108:111], v[136:139]\n"
      "ds_write_b64 v245, v[212:213] offset:24576\n"
      "v_mfma_f32_16x16x32_bf16 v[136:139], a[216:219], v[112:115], v[136:139]\n"
      "v_mfma_f32_16x16x32_bf16 v[136:139], a[220:223], v[116:119], v[136:139]\n"
      "v_mfma_f32_16x16x32_bf16 v[132:135], a[224:227], v[36:39], v[16:19]\n"
      "v_mfma_f32_16x16x32_bf16 v[132:135], a[228:231], v[40:43], v[132:135]\n"
      "v_mfma_f32_16x16x32_bf16 v[132:135], a[232:235], v[44:47], v[132:135]\n"
      "v_mfma_f32_16x16x32_bf16 v[132:135], a[236:239], v[60:63], v[132:135]\n"
      "v_cvt_pk_bf16_f32 v214, v136, v137\n"
      "v_mfma_f32_16x16x32_bf16 v[132:135], a[0:3], v[64:67], v[132:135]\n"
      "v_cvt_pk_bf16_f32 v215, v138, v139\n"
      "v_mfma_f32_16x16x32_bf16 v[132:135], a[4:7], v[68:71], v[132:135]\n"
      "v_mfma_f32_16x16x32_bf16 v[132:135], a[8:11], v[84:87], v[132:135]\n"
      "v_mfma_f32_16x16x32_bf16 v[132:135], a[12:15], v[88:91], v[132:135]\n"
      "v_mfma_f32_16x16x32_bf16 v[132:135], a[16:19], v[92:95], v[132:135]\n"
      "v_mfma_f32_16x16x32_bf16 v[132:135], a[20:23], v[108:111], v[132:135]\n"
      "ds_write_b64 v245, v[214:215] offset:24584\n"
      "v_mfma_f32_16x16x32_bf16 v[132:135], v[140:143], v[112:115], v[132:135]\n"
      "s_lshl_b32 s79, s98, 1\n"
      "v_mfma_f32_16x16x32_bf16 v[132:135], v[156:159], v[116:119], v[132:135]\n"
      "s_waitcnt lgkmcnt(4)\n"
      "s_barrier\n"
      "v_mad_i32_i24 v252, v251, s79, v252\n"
      "v_mfma_f32_16x16x32_bf16 v[136:139], v[160:163], v[36:39], v[20:23]\n"
      "ds_read_b128 v[36:39], v232 offset:24576\n"
      "s_mul_i32 s79, s79, s100\n"
      "v_mfma_f32_16x16x32_bf16 v[136:139], v[164:167], v[40:43], v[136:139]\n"
      "ds_read_b128 v[40:43], v233 offset:24576\n"
      "v_add_u32 v247, s79, v247\n"
      "v_mfma_f32_16x16x32_bf16 v[136:139], v[180:183], v[44:47], v[136:139]\n"
      "ds_read_b128 v[44:47], v234 offset:24576\n"
      "s_mul_i32 s79, s79, s101\n"
      "v_mfma_f32_16x16x32_bf16 v[136:139], v[184:187], v[60:63], v[136:139]\n"
      "ds_read_b128 v[60:63], v235 offset:24576\n"
      "v_cvt_pk_bf16_f32 v208, v132, v133\n"
      "v_mfma_f32_16x16x32_bf16 v[136:139], v[188:191], v[64:67], v[136:139]\n"
      "ds_read_b128 v[64:67], v232 offset:32768\n"
      "v_cvt_pk_bf16_f32 v209, v134, v135\n"
      "v_mfma_f32_16x16x32_bf16 v[136:139], v[204:207], v[68:71], v[136:139]\n"
      "ds_read_b128 v[68:71], v233 offset:32768\n"
      "v_add_u32 v253, s79, v253\n"
      "v_mfma_f32_16x16x32_bf16 v[136:139], a[24:27], v[84:87], v[136:139]\n"
      "ds_read_b128 v[84:87], v234 offset:32768\n"
      "v_mfma_f32_16x16x32_bf16 v[136:139], a[28:31], v[88:91], v[136:139]\n"
      "ds_read_b128 v[88:91], v235 offset:32768\n"
      "v_mfma_f32_16x16x32_bf16 v[136:139], a[32:35], v[92:95], v[136:139]\n"
      "ds_read_b128 v[92:95], v232 offset:40960\n"
      "v_mfma_f32_16x16x32_bf16 v[136:139], a[36:39], v[108:111], v[136:139]\n"
      "ds_read_b128 v[108:111], v233 offset:40960\n"
      "v_mfma_f32_16x16x32_bf16 v[136:139], a[40:43], v[112:115], v[136:139]\n"
      "ds_read_b128 v[112:115], v234 offset:40960\n"
      "v_mfma_f32_16x16x32_bf16 v[136:139], a[44:47], v[116:119], v[136:139]\n"
      "ds_read_b128 v[116:119], v235 offset:40960\n"
      "LJOIN:\n"
      "LPD7:\n"
      "s_cbranch_execz LPD10\n"
      "s_waitcnt lgkmcnt(8)\n"
      "v_mfma_f32_16x16x32_bf16 v[132:135], a[240:243], v[36:39], v[0:3]\n"
      "ds_write_b64 v244, v[208:209] offset:24704\n"
      "v_mfma_f32_16x16x32_bf16 v[132:135], a[244:247], v[40:43], v[132:135]\n"
      "s_add_u32 s54, s30, s32\n"
      "s_addc_u32 s55, s31, 0\n"
      "v_mfma_f32_16x16x32_bf16 v[132:135], a[248:251], v[44:47], v[132:135]\n"
      "v_mfma_f32_16x16x32_bf16 v[132:135], a[252:255], v[60:63], v[132:135]\n"
      "v_cvt_pk_bf16_f32 v210, v136, v137\n"
      "s_waitcnt lgkmcnt(5)\n"
      "v_mfma_f32_16x16x32_bf16 v[132:135], a[48:51], v[64:67], v[132:135]\n"
      "v_cvt_pk_bf16_f32 v211, v138, v139\n"
      "v_mfma_f32_16x16x32_bf16 v[132:135], a[52:55], v[68:71], v[132:135]\n"
      "s_mul_i32 s78, s91, s19\n"
      "v_mfma_f32_16x16x32_bf16 v[132:135], a[56:59], v[84:87], v[132:135]\n"
      "ds_read_b128 v[216:219], v246 offset:12288\n"
      "v_add_u32 v236, s78, v236\n"
      "v_mfma_f32_16x16x32_bf16 v[132:135], a[60:63], v[88:91], v[132:135]\n"
      "ds_read_b128 v[220:223], v246 offset:13312\n"
      "s_waitcnt lgkmcnt(3)\n"
      "v_mfma_f32_16x16x32_bf16 v[132:135], a[64:67], v[92:95], v[132:135]\n"
      "ds_read_b128 v[224:227], v246 offset:14336\n"
      "v_mfma_f32_16x16x32_bf16 v[132:135], a[68:71], v[108:111], v[132:135]\n"
      "ds_write_b64 v244, v[210:211] offset:24712\n"
      "v_mfma_f32_16x16x32_bf16 v[132:135], a[72:75], v[112:115], v[132:135]\n"
      "s_mul_i32 s78, s78, s99\n"
      "v_mfma_f32_16x16x32_bf16 v[132:135], a[76:79], v[116:119], v[132:135]\n"
      "v_add_u32 v237, s78, v237\n"
      "v_mfma_f32_16x16x32_bf16 v[136:139], a[80:83], v[36:39], v[4:7]\n"
      "v_mfma_f32_16x16x32_bf16 v[136:139], a[84:87], v[40:43], v[136:139]\n"
      "v_mfma_f32_16x16x32_bf16 v[136:139], a[88:91], v[44:47], v[136:139]\n"
      "v_mfma_f32_16x16x32_bf16 v[136:139], a[92:95], v[60:63], v[136:139]\n"
      "v_cvt_pk_bf16_f32 v208, v132, v133\n"
      "v_mfma_f32_16x16x32_bf16 v[136:139], a[96:99], v[64:67], v[136:139]\n"
      "v_cvt_pk_bf16_f32 v209, v134, v135\n"
      "v_mfma_f32_16x16x32_bf16 v[136:139], a[100:103], v[68:71], v[136:139]\n"
      "s_waitcnt lgkmcnt(3)\n"
      "v_mfma_f32_16x16x32_bf16 v[136:139], a[104:107], v[84:87], v[136:139]\n"
      "global_store_dwordx4 v247, v[216:219], s[54:55] offset:0 sc1\n"
      "v_mfma_f32_16x16x32_bf16 v[136:139], a[108:111], v[88:91], v[136:139]\n"
      "s_add_u32 s52, s20, s34\n"
      "s_addc_u32 s53, s21, 0\n"
      "v_mfma_f32_16x16x32_bf16 v[136:139], a[112:115], v[92:95], v[136:139]\n"
      "s_add_u32 m0, s17, 0x0\n"
      "v_mfma_f32_16x16x32_bf16 v[136:139], a[116:119], v[108:111], v[136:139]\n"
      "ds_write_b64 v244, v[208:209] offset:0\n"
      "v_mfma_f32_16x16x32_bf16 v[136:139], a[120:123], v[112:115], v[136:139]\n"
      "global_load_lds_dwordx4 v236, s[52:53] offset:0 nt\n"
      "v_mfma_f32_16x16x32_bf16 v[136:139], a[124:127], v[116:119], v[136:139]\n"
      "v_mfma_f32_16x16x32_bf16 v[132:135], a[128:131], v[36:39], v[8:11]\n"
      "v_mfma_f32_16x16x32_bf16 v[132:135], a[132:135], v[40:43], v[132:135]\n"
      "v_mfma_f32_16x16x32_bf16 v[132:135], a[136:139], v[44:47], v[132:135]\n"
      "v_mfma_f32_16x16x32_bf16 v[132:135], a[140:143], v[60:63], v[132:135]\n"
      "v_cvt_pk_bf16_f32 v210, v136, v137\n"
      "v_mfma_f32_16x16x32_bf16 v[132:135], a[144:147], v[64:67], v[132:135]\n"
      "v_cvt_pk_bf16_f32 v211, v138, v139\n"
      "v_mfma_f32_16x16x32_bf16 v[132:135], a[148:151], v[68:71], v[132:135]\n"
      "v_mfma_f32_16x16x32_bf16 v[132:135], a[152:155], v[84:87], v[132:135]\n"
      "s_waitcnt lgkmcnt(3)\n"
      "v_mfma_f32_16x16x32_bf16 v[132:135], a[156:159], v[88:91], v[132:135]\n"
      "global_store_dwordx4 v252, v[220:223], s[54:55] offset:0 sc1\n"
      "v_mfma_f32_16x16x32_bf16 v[132:135], a[160:163], v[92:95], v[132:135]\n"
      "s_add_u32 m0, s17, 0x1f00\n"
      "v_mfma_f32_16x16x32_bf16 v[132:135], a[164:167], v[108:111], v[132:135]\n"
      "ds_write_b64 v244, v[210:211] offset:8\n"
      "v_mfma_f32_16x16x32_bf16 v[132:135], a[168:171], v[112:115], v[132:135]\n"
      "global_load_lds_dwordx4 v236, s[52:53] offset:256 nt\n"
      "v_mfma_f32_16x16x32_bf16 v[132:135], a[172:175], v[116:119], v[132:135]\n"
      "v_mfma_f32_16x16x32_bf16 v[136:139], a[176:179], v[36:39], v[12:15]\n"
      "v_mfma_f32_16x16x32_bf16 v[136:139], a[180:183], v[40:43], v[136:139]\n"
      "v_mfma_f32_16x16x32_bf16 v[136:139], a[184:187], v[44:47], v[136:139]\n"
      "v_mfma_f32_16x16x32_bf16 v[136:139], a[188:191], v[60:63], v[136:139]\n"
      "v_cvt_pk_bf16_f32 v212, v132, v133\n"
      "v_mfma_f32_16x16x32_bf16 v[136:139], a[192:195], v[64:67], v[136:139]\n"
      "v_cvt_pk_bf16_f32 v213, v134, v135\n"
      "v_mfma_f32_16x16x32_bf16 v[136:139], a[196:199], v[68:71], v[136:139]\n"
      "v_mfma_f32_16x16x32_bf16 v[136:139], a[200:203], v[84:87], v[136:139]\n"
      "v_mfma_f32_16x16x32_bf16 v[136:139], a[204:207], v[88:91], v[136:139]\n"
      "s_waitcnt lgkmcnt(3)\n"
      "v_mfma_f32_16x16x32_bf16 v[136:139], a[208:211], v[92:95], v[136:139]\n"
      "global_store_dwordx4 v253, v[224:227], s[54:55] offset:0 sc1\n"
      "v_mfma_f32_16x16x32_bf16 v[136:139], a[212:215], v[108:111], v[136:139]\n"
      "ds_write_b64 v245, v[212:213] offset:0\n"
      "v_mfma_f32_16x16x32_bf16 v[136:139], a[216:219], v[112:115], v[136:139]\n"
      "s_add_u32 m0, s17, 0x3e00\n"
      "v_mfma_f32_16x16x32_bf16 v[136:139], a[220:223], v[116:119], v[136:139]\n"
      "global_load_lds_dwordx4 v236, s[52:53] offset:512 nt\n"
      "v_mfma_f32_16x16x32_bf16 v[132:135], a[224:227], v[36:39], v[16:19]\n"
      "v_mfma_f32_16x16x32_bf16 v[132:135], a[228:231], v[40:43], v[132:135]\n"
      "v_mfma_f32_16x16x32_bf16 v[132:135], a[232:235], v[44:47], v[132:135]\n"
      "v_mfma_f32_16x16x32_bf16 v[132:135], a[236:239], v[60:63], v[132:135]\n"
      "v_cvt_pk_bf16_f32 v214, v136, v137\n"
      "v_mfma_f32_16x16x32_bf16 v[132:135], a[0:3], v[64:67], v[132:135]\n"
      "v_cvt_pk_bf16_f32 v215, v138, v139\n"
      "v_mfma_f32_16x16x32_bf16 v[132:135], a[4:7], v[68:71], v[132:135]\n"
      "v_mfma_f32_16x16x32_bf16 v[132:135], a[8:11], v[84:87], v[132:135]\n"
      "v_mfma_f32_16x16x32_bf16 v[132:135], a[12:15], v[88:91], v[132:135]\n"
      "v_mfma_f32_16x16x32_bf16 v[132:135], a[16:19], v[92:95], v[132:135]\n"
      "v_mfma_f32_16x16x32_bf16 v[132:135], a[20:23], v[108:111], v[132:135]\n"
      "ds_write_b64 v245, v[214:215] offset:8\n"
      "v_mfma_f32_16x16x32_bf16 v[132:135], v[140:143], v[112:115], v[132:135]\n"
      "v_mfma_f32_16x16x32_bf16 v[132:135], v[156:159], v[116:119], v[132:135]\n"
      "s_waitcnt lgkmcnt(4)\n"
      "s_barrier\n"
      "v_mfma_f32_16x16x32_bf16 v[136:139], v[160:163], v[36:39], v[20:23]\n"
      "ds_read_b128 v[36:39], v232 offset:24832\n"
      "v_mfma_f32_16x16x32_bf16 v[136:139], v[164:167], v[40:43], v[136:139]\n"
      "ds_read_b128 v[40:43], v233 offset:24832\n"
      "v_mfma_f32_16x16x32_bf16 v[136:139], v[180:183], v[44:47], v[136:139]\n"
      "ds_read_b128 v[44:47], v234 offset:24832\n"
      "v_mfma_f32_16x16x32_bf16 v[136:139], v[184:187], v[60:63], v[136:139]\n"
      "ds_read_b128 v[60:63], v235 offset:24832\n"
      "v_cvt_pk_bf16_f32 v208, v132, v133\n"
      "v_mfma_f32_16x16x32_bf16 v[136:139], v[188:191], v[64:67], v[136:139]\n"
      "ds_read_b128 v[64:67], v232 offset:33024\n"
      "v_cvt_pk_bf16_f32 v209, v134, v135\n"
      "v_mfma_f32_16x16x32_bf16 v[136:139], v[204:207], v[68:71], v[136:139]\n"
      "ds_read_b128 v[68:71], v233 offset:33024\n"
      "v_mfma_f32_16x16x32_bf16 v[136:139], a[24:27], v[84:87], v[136:139]\n"
      "ds_read_b128 v[84:87], v234 offset:33024\n"
      "v_mfma_f32_16x16x32_bf16 v[136:139], a[28:31], v[88:91], v[136:139]\n"
      "ds_read_b128 v[88:91], v235 offset:33024\n"
      "v_mfma_f32_16x16x32_bf16 v[136:139], a[32:35], v[92:95], v[136:139]\n"
      "ds_read_b128 v[92:95], v232 offset:41216\n"
      "v_mfma_f32_16x16x32_bf16 v[136:139], a[36:39], v[108:111], v[136:139]\n"
      "ds_read_b128 v[108:111], v233 offset:41216\n"
      "ds_write_b64 v244, v[208:209] offset:128\n"
      "v_mfma_f32_16x16x32_bf16 v[136:139], a[40:43], v[112:115], v[136:139]\n"
      "ds_read_b128 v[112:115], v234 offset:41216\n"
      "v_mfma_f32_16x16x32_bf16 v[136:139], a[44:47], v[116:119], v[136:139]\n"
      "ds_read_b128 v[116:119], v235 offset:41216\n"
      "s_waitcnt lgkmcnt(9)\n"
      "v_mfma_f32_16x16x32_bf16 v[132:135], a[240:243], v[36:39], v[0:3]\n"
      "v_add_u32 v232, 0xc000, v232\n"
      "LPD8:\n"
      "s_cbranch_execz LPD11\n"
      "v_mfma_f32_16x16x32_bf16 v[132:135], a[244:247], v[40:43], v[132:135]\n"
      "v_add_u32 v233, 0xc000, v233\n"
      "v_mfma_f32_16x16x32_bf16 v[132:135], a[248:251], v[44:47], v[132:135]\n"
      "v_add_u32 v234, 0xc000, v234\n"
      "v_mfma_f32_16x16x32_bf16 v[132:135], a[252:255], v[60:63], v[132:135]\n"
      "v_add_u32 v235, 0xc000, v235\n"
      "s_waitcnt lgkmcnt(5)\n"
      "v_mfma_f32_16x16x32_bf16 v[132:135], a[48:51], v[64:67], v[132:135]\n"
      "v_cvt_pk_bf16_f32 v210, v136, v137\n"
      "v_mfma_f32_16x16x32_bf16 v[132:135], a[52:55], v[68:71], v[132:135]\n"
      "v_cvt_pk_bf16_f32 v211, v138, v139\n"
      "v_mfma_f32_16x16x32_bf16 v[132:135], a[56:59], v[84:87], v[132:135]\n"
      "ds_read_b128 v[216:219], v246 offset:24576\n"
      "v_mfma_f32_16x16x32_bf16 v[132:135], a[60:63], v[88:91], v[132:135]\n"
      "ds_read_b128 v[220:223], v246 offset:25600\n"
      "s_waitcnt lgkmcnt(2)\n"
      "v_mfma_f32_16x16x32_bf16 v[132:135], a[64:67], v[92:95], v[132:135]\n"
      "ds_read_b128 v[224:227], v246 offset:26624\n"
      "v_mfma_f32_16x16x32_bf16 v[132:135], a[68:71], v[108:111], v[132:135]\n"
      "v_lshlrev_b32 v0, 16, v24\n"
      "v_mfma_f32_16x16x32_bf16 v[132:135], a[72:75], v[112:115], v[132:135]\n"
      "ds_write_b64 v244, v[210:211] offset:136\n"
      "v_mfma_f32_16x16x32_bf16 v[132:135], a[76:79], v[116:119], v[132:135]\n"
      "v_and_b32 v1, 0xffff0000, v24\n"
      "v_mfma_f32_16x16x32_bf16 v[136:139], a[80:83], v[36:39], v[4:7]\n"
      "v_lshlrev_b32 v2, 16, v25\n"
      "v_mfma_f32_16x16x32_bf16 v[136:139], a[84:87], v[40:43], v[136:139]\n"
      "v_and_b32 v3, 0xffff0000, v25\n"
      "v_mfma_f32_16x16x32_bf16 v[136:139], a[88:91], v[44:47], v[136:139]\n"
      "v_mfma_f32_16x16x32_bf16 v[136:139], a[92:95], v[60:63], v[136:139]\n"
      "v_cvt_pk_bf16_f32 v208, v132, v133\n"
      "v_mfma_f32_16x16x32_bf16 v[136:139], a[96:99], v[64:67], v[136:139]\n"
      "v_cvt_pk_bf16_f32 v209, v134, v135\n"
      "v_mfma_f32_16x16x32_bf16 v[136:139], a[100:103], v[68:71], v[136:139]\n"
      "s_waitcnt lgkmcnt(3)\n"
      "v_mfma_f32_16x16x32_bf16 v[136:139], a[104:107], v[84:87], v[136:139]\n"
      "global_store_dwordx4 v247, v[216:219], s[54:55] offset:768 sc1\n"
      "v_mfma_f32_16x16x32_bf16 v[136:139], a[108:111], v[88:91], v[136:139]\n"
      "s_add_u32 m0, s17, 0x1000\n"
      "v_mfma_f32_16x16x32_bf16 v[136:139], a[112:115], v[92:95], v[136:139]\n"
      "global_load_lds_dwordx4 v237, s[52:53] offset:0 nt\n"
      "v_mfma_f32_16x16x32_bf16 v[136:139], a[116:119], v[108:111], v[136:139]\n"
      "ds_write_b64 v244, v[208:209] offset:12288\n"
      "v_mfma_f32_16x16x32_bf16 v[136:139], a[120:123], v[112:115], v[136:139]\n"
      "v_lshlrev_b32 v4, 16, v26\n"
      "v_mfma_f32_16x16x32_bf16 v[136:139], a[124:127], v[116:119], v[136:139]\n"
      "v_and_b32 v5, 0xffff0000, v26\n"
      "v_mfma_f32_16x16x32_bf16 v[132:135], a[128:131], v[36:39], v[8:11]\n"
      "v_lshlrev_b32 v6, 16, v27\n"
      "v_mfma_f32_16x16x32_bf16 v[132:135], a[132:135], v[40:43], v[132:135]\n"
      "v_and_b32 v7, 0xffff0000, v27\n"
      "v_mfma_f32_16x16x32_bf16 v[132:135], a[136:139], v[44:47], v[132:135]\n"
      "v_mfma_f32_16x16x32_bf16 v[132:135], a[140:143], v[60:63], v[132:135]\n"
      "v_cvt_pk_bf16_f32 v210, v136, v137\n"
      "v_mfma_f32_16x16x32_bf16 v[132:135], a[144:147], v[64:67], v[132:135]\n"
      "v_cvt_pk_bf16_f32 v211, v138, v139\n"
      "v_mfma_f32_16x16x32_bf16 v[132:135], a[148:151], v[68:71], v[132:135]\n"
      "v_lshlrev_b32 v8, 16, v28\n"
      "v_mfma_f32_16x16x32_bf16 v[132:135], a[152:155], v[84:87], v[132:135]\n"
      "s_waitcnt lgkmcnt(3)\n"
      "v_mfma_f32_16x16x32_bf16 v[132:135], a[156:159], v[88:91], v[132:135]\n"
      "global_store_dwordx4 v252, v[220:223], s[54:55] offset:768 sc1\n"
      "v_mfma_f32_16x16x32_bf16 v[132:135], a[160:163], v[92:95], v[132:135]\n"
      "s_add_u32 m0, s17, 0x2f00\n"
      "v_mfma_f32_16x16x32_bf16 v[132:135], a[164:167], v[108:111], v[132:135]\n"
      "ds_write_b64 v244, v[210:211] offset:12296\n"
      "v_mfma_f32_16x16x32_bf16 v[132:135], a[168:171], v[112:115], v[132:135]\n"
      "global_load_lds_dwordx4 v237, s[52:53] offset:256 nt\n"
      "v_mfma_f32_16x16x32_bf16 v[132:135], a[172:175], v[116:119], v[132:135]\n"
      "v_and_b32 v9, 0xffff0000, v28\n"
      "v_mfma_f32_16x16x32_bf16 v[136:139], a[176:179], v[36:39], v[12:15]\n"
      "v_lshlrev_b32 v10, 16, v29\n"
      "v_mfma_f32_16x16x32_bf16 v[136:139], a[180:183], v[40:43], v[136:139]\n"
      "v_and_b32 v11, 0xffff0000, v29\n"
      "v_mfma_f32_16x16x32_bf16 v[136:139], a[184:187], v[44:47], v[136:139]\n"
      "v_mfma_f32_16x16x32_bf16 v[136:139], a[188:191], v[60:63], v[136:139]\n"
      "v_cvt_pk_bf16_f32 v212, v132, v133\n"
      "v_mfma_f32_16x16x32_bf16 v[136:139], a[192:195], v[64:67], v[136:139]\n"
      "v_cvt_pk_bf16_f32 v213, v134, v135\n"
      "v_mfma_f32_16x16x32_bf16 v[136:139], a[196:199], v[68:71], v[136:139]\n"
      "v_lshlrev_b32 v12, 16, v30\n"
      "v_mfma_f32_16x16x32_bf16 v[136:139], a[200:203], v[84:87], v[136:139]\n"
      "v_and_b32 v13, 0xffff0000, v30\n"
      "v_mfma_f32_16x16x32_bf16 v[136:139], a[204:207], v[88:91], v[136:139]\n"
      "s_waitcnt lgkmcnt(3)\n"
      "v_mfma_f32_16x16x32_bf16 v[136:139], a[208:211], v[92:95], v[136:139]\n"
      "global_store_dwordx4 v253, v[224:227], s[54:55] offset:768 sc1\n"
      "v_mfma_f32_16x16x32_bf16 v[136:139], a[212:215], v[108:111], v[136:139]\n"
      "ds_write_b64 v245, v[212:213] offset:12288\n"
      "v_mfma_f32_16x16x32_bf16 v[136:139], a[216:219], v[112:115], v[136:139]\n"
      "s_add_u32 m0, s17, 0x4e00\n"
      "v_mfma_f32_16x16x32_bf16 v[136:139], a[220:223], v[116:119], v[136:139]\n"
      "global_load_lds_dwordx4 v237, s[52:53] offset:512 nt\n"
      "v_mfma_f32_16x16x32_bf16 v[132:135], a[224:227], v[36:39], v[16:19]\n"
      "v_lshlrev_b32 v14, 16, v31\n"
      "v_mfma_f32_16x16x32_bf16 v[132:135], a[228:231], v[40:43], v[132:135]\n"
      "v_and_b32 v15, 0xffff0000, v31\n"
      "v_mfma_f32_16x16x32_bf16 v[132:135], a[232:235], v[44:47], v[132:135]\n"
      "v_mfma_f32_16x16x32_bf16 v[132:135], a[236:239], v[60:63], v[132:135]\n"
      "s_memrealtime s[84:85]\n"
      "v_mfma_f32_16x16x32_bf16 v[132:135], a[0:3], v[64:67], v[132:135]\n"
      "v_cvt_pk_bf16_f32 v214, v136, v137\n"
      "v_mfma_f32_16x16x32_bf16 v[132:135], a[4:7], v[68:71], v[132:135]\n"
      "v_cvt_pk_bf16_f32 v215, v138, v139\n"
      "v_mfma_f32_16x16x32_bf16 v[132:135], a[8:11], v[84:87], v[132:135]\n"
      "v_lshlrev_b32 v16, 16, v32\n"
      "v_mfma_f32_16x16x32_bf16 v[132:135], a[12:15], v[88:91], v[132:135]\n"
      "v_and_b32 v17, 0xffff0000, v32\n"
      "v_mfma_f32_16x16x32_bf16 v[132:135], a[16:19], v[92:95], v[132:135]\n"
      "v_lshlrev_b32 v18, 16, v33\n"
      "v_mfma_f32_16x16x32_bf16 v[132:135], a[20:23], v[108:111], v[132:135]\n"
      "v_and_b32 v19, 0xffff0000, v33\n"
      "v_mfma_f32_16x16x32_bf16 v[132:135], v[140:143], v[112:115], v[132:135]\n"
      "s_waitcnt lgkmcnt(0)\n"
      "s_mov_b32 s48, s84\n"
      "v_mfma_f32_16x16x32_bf16 v[132:135], v[156:159], v[116:119], v[132:135]\n"
      "s_waitcnt vmcnt(18)\n"
      "s_barrier\n"
      "ds_write_b64 v245, v[214:215] offset:12296\n"
      "v_mfma_f32_16x16x32_bf16 v[136:139], v[160:163], v[36:39], v[20:23]\n"
      "ds_read_b128 v[36:39], v232 offset:0\n"
      "v_mfma_f32_16x16x32_bf16 v[136:139], v[164:167], v[40:43], v[136:139]\n"
      "ds_read_b128 v[40:43], v233 offset:0\n"
      "v_mfma_f32_16x16x32_bf16 v[136:139], v[180:183], v[44:47], v[136:139]\n"
      "ds_read_b128 v[44:47], v234 offset:0\n"
      "v_mfma_f32_16x16x32_bf16 v[136:139], v[184:187], v[60:63], v[136:139]\n"
      "ds_read_b128 v[60:63], v235 offset:0\n"
      "v_cvt_pk_bf16_f32 v208, v132, v133\n"
      "v_mfma_f32_16x16x32_bf16 v[136:139], v[188:191], v[64:67], v[136:139]\n"
      "ds_read_b128 v[64:67], v232 offset:8192\n"
      "v_cvt_pk_bf16_f32 v209, v134, v135\n"
      "v_mfma_f32_16x16x32_bf16 v[136:139], v[204:207], v[68:71], v[136:139]\n"
      "ds_read_b128 v[68:71], v233 offset:8192\n"
      "v_mfma_f32_16x16x32_bf16 v[136:139], a[24:27], v[84:87], v[136:139]\n"
      "ds_read_b128 v[84:87], v234 offset:8192\n"
      "v_mfma_f32_16x16x32_bf16 v[136:139], a[28:31], v[88:91], v[136:139]\n"
      "ds_read_b128 v[88:91], v235 offset:8192\n"
      "v_mfma_f32_16x16x32_bf16 v[136:139], a[32:35], v[92:95], v[136:139]\n"
      "ds_read_b128 v[92:95], v232 offset:16384\n"
      "v_mfma_f32_16x16x32_bf16 v[136:139], a[36:39], v[108:111], v[136:139]\n"
      "ds_read_b128 v[108:111], v233 offset:16384\n"
      "v_mfma_f32_16x16x32_bf16 v[136:139], a[40:43], v[112:115], v[136:139]\n"
      "ds_read_b128 v[112:115], v234 offset:16384\n"
      "v_mfma_f32_16x16x32_bf16 v[136:139], a[44:47], v[116:119], v[136:139]\n"
      "ds_read_b128 v[116:119], v235 offset:16384\n"
      "s_waitcnt lgkmcnt(8)\n"
      "v_mfma_f32_16x16x32_bf16 v[132:135], a[240:243], v[36:39], v[0:3]\n"
      "ds_write_b64 v244, v[208:209] offset:12416\n"
      "v_mfma_f32_16x16x32_bf16 v[132:135], a[244:247], v[40:43], v[132:135]\n"
      "v_lshlrev_b32 v20, 16, v34\n"
      "v_mfma_f32_16x16x32_bf16 v[132:135], a[248:251], v[44:47], v[132:135]\n"
      "v_and_b32 v21, 0xffff0000, v34\n"
      "v_mfma_f32_16x16x32_bf16 v[132:135], a[252:255], v[60:63], v[132:135]\n"
      "v_lshlrev_b32 v22, 16, v35\n"
      "s_waitcnt lgkmcnt(5)\n"
      "v_mfma_f32_16x16x32_bf16 v[132:135], a[48:51], v[64:67], v[132:135]\n"
      "v_and_b32 v23, 0xffff0000, v35\n"
      "v_mfma_f32_16x16x32_bf16 v[132:135], a[52:55], v[68:71], v[132:135]\n"
      "v_cvt_pk_bf16_f32 v210, v136, v137\n"
      "v_mfma_f32_16x16x32_bf16 v[132:135], a[56:59], v[84:87], v[132:135]\n"
      "v_cvt_pk_bf16_f32 v211, v138, v139\n"
      "v_mfma_f32_16x16x32_bf16 v[132:135], a[60:63], v[88:91], v[132:135]\n"
      "s_add_u32 s54, s44, s32\n"
      "s_addc_u32 s55, s45, 0\n"
      "s_waitcnt lgkmcnt(1)\n"
      "v_mfma_f32_16x16x32_bf16 v[132:135], a[64:67], v[92:95], v[132:135]\n"
      "ds_read_b128 v[216:219], v246 offset:0\n"
      "v_mfma_f32_16x16x32_bf16 v[132:135], a[68:71], v[108:111], v[132:135]\n"
      "ds_read_b128 v[220:223], v246 offset:1024\n"
      "v_mfma_f32_16x16x32_bf16 v[132:135], a[72:75], v[112:115], v[132:135]\n"
      "ds_read_b128 v[224:227], v246 offset:2048\n"
      "v_mfma_f32_16x16x32_bf16 v[132:135], a[76:79], v[116:119], v[132:135]\n"
      "ds_write_b64 v244, v[210:211] offset:12424\n"
      "v_mfma_f32_16x16x32_bf16 v[136:139], a[80:83], v[36:39], v[4:7]\n"
      "v_mfma_f32_16x16x32_bf16 v[136:139], a[84:87], v[40:43], v[136:139]\n"
      "v_mfma_f32_16x16x32_bf16 v[136:139], a[88:91], v[44:47], v[136:139]\n"
      "v_mfma_f32_16x16x32_bf16 v[136:139], a[92:95], v[60:63], v[136:139]\n"
      "v_cvt_pk_bf16_f32 v208, v132, v133\n"
      "v_mfma_f32_16x16x32_bf16 v[136:139], a[96:99], v[64:67], v[136:139]\n"
      "v_cvt_pk_bf16_f32 v209, v134, v135\n"
      "v_mfma_f32_16x16x32_bf16 v[136:139], a[100:103], v[68:71], v[136:139]\n"
      "s_waitcnt lgkmcnt(3)\n"
      "v_mfma_f32_16x16x32_bf16 v[136:139], a[104:107], v[84:87], v[136:139]\n"
      "global_store_dwordx4 v247, v[216:219], s[54:55] offset:0 sc1\n"
      "v_mfma_f32_16x16x32_bf16 v[136:139], a[108:111], v[88:91], v[136:139]\n"
      "s_add_u32 s52, s42, s34\n"
      "s_addc_u32 s53, s43, 0\n"
      "v_mfma_f32_16x16x32_bf16 v[136:139], a[112:115], v[92:95], v[136:139]\n"
      "s_add_u32 m0, s17, 0x6000\n"
      "v_mfma_f32_16x16x32_bf16 v[136:139], a[116:119], v[108:111], v[136:139]\n"
      "ds_write_b64 v244, v[208:209] offset:24576\n"
      "v_mfma_f32_16x16x32_bf16 v[136:139], a[120:123], v[112:115], v[136:139]\n"
      "global_load_lds_dwordx4 v236, s[52:53] offset:0 nt\n"
      "v_mfma_f32_16x16x32_bf16 v[136:139], a[124:127], v[116:119], v[136:139]\n"
      "v_mfma_f32_16x16x32_bf16 v[132:135], a[128:131], v[36:39], v[8:11]\n"
      "v_mfma_f32_16x16x32_bf16 v[132:135], a[132:135], v[40:43], v[132:135]\n"
      "v_mfma_f32_16x16x32_bf16 v[132:135], a[136:139], v[44:47], v[132:135]\n"
      "v_mfma_f32_16x16x32_bf16 v[132:135], a[140:143], v[60:63], v[132:135]\n"
      "v_cvt_pk_bf16_f32 v210, v136, v137\n"
      "v_mfma_f32_16x16x32_bf16 v[132:135], a[144:147], v[64:67], v[132:135]\n"
      "v_cvt_pk_bf16_f32 v211, v138, v139\n"
      "v_mfma_f32_16x16x32_bf16 v[132:135], a[148:151], v[68:71], v[132:135]\n"
      "v_mfma_f32_16x16x32_bf16 v[132:135], a[152:155], v[84:87], v[132:135]\n"
      "s_waitcnt lgkmcnt(3)\n"
      "v_mfma_f32_16x16x32_bf16 v[132:135], a[156:159], v[88:91], v[132:135]\n"
      "global_store_dwordx4 v252, v[220:223], s[54:55] offset:0 sc1\n"
      "v_mfma_f32_16x16x32_bf16 v[132:135], a[160:163], v[92:95], v[132:135]\n"
      "s_add_u32 m0, s17, 0x7f00\n"
      "v_mfma_f32_16x16x32_bf16 v[132:135], a[164:167], v[108:111], v[132:135]\n"
      "ds_write_b64 v244, v[210:211] offset:24584\n"
      "v_mfma_f32_16x16x32_bf16 v[132:135], a[168:171], v[112:115], v[132:135]\n"
      "global_load_lds_dwordx4 v236, s[52:53] offset:256 nt\n"
      "v_mfma_f32_16x16x32_bf16 v[132:135], a[172:175], v[116:119], v[132:135]\n"
      "v_mfma_f32_16x16x32_bf16 v[136:139], a[176:179], v[36:39], v[12:15]\n"
      "v_mfma_f32_16x16x32_bf16 v[136:139], a[180:183], v[40:43], v[136:139]\n"
      "v_mfma_f32_16x16x32_bf16 v[136:139], a[184:187], v[44:47], v[136:139]\n"
      "v_mfma_f32_16x16x32_bf16 v[136:139], a[188:191], v[60:63], v[136:139]\n"
      "v_cvt_pk_bf16_f32 v212, v132, v133\n"
      "v_mfma_f32_16x16x32_bf16 v[136:139], a[192:195], v[64:67], v[136:139]\n"
      "v_cvt_pk_bf16_f32 v213, v134, v135\n"
      "v_mfma_f32_16x16x32_bf16 v[136:139], a[196:199], v[68:71], v[136:139]\n"
      "v_mfma_f32_16x16x32_bf16 v[136:139], a[200:203], v[84:87], v[136:139]\n"
      "v_mfma_f32_16x16x32_bf16 v[136:139], a[204:207], v[88:91], v[136:139]\n"
      "s_waitcnt lgkmcnt(3)\n"
      "v_mfma_f32_16x16x32_bf16 v[136:139], a[208:211], v[92:95], v[136:139]\n"
      "global_store_dwordx4 v253, v[224:227], s[54:55] offset:0 sc1\n"
      "v_mfma_f32_16x16x32_bf16 v[136:139], a[212:215], v[108:111], v[136:139]\n"
      "ds_write_b64 v245, v[212:213] offset:24576\n"
      "v_mfma_f32_16x16x32_bf16 v[136:139], a[216:219], v[112:115], v[136:139]\n"
      "s_add_u32 m0, s17, 0x9e00\n"
      "v_mfma_f32_16x16x32_bf16 v[136:139], a[220:223], v[116:119], v[136:139]\n"
      "global_load_lds_dwordx4 v236, s[52:53] offset:512 nt\n"
      "v_mfma_f32_16x16x32_bf16 v[132:135], a[224:227], v[36:39], v[16:19]\n"
      "v_mfma_f32_16x16x32_bf16 v[132:135], a[228:231], v[40:43], v[132:135]\n"
      "v_mfma_f32_16x16x32_bf16 v[132:135], a[232:235], v[44:47], v[132:135]\n"
      "v_mfma_f32_16x16x32_bf16 v[132:135], a[236:239], v[60:63], v[132:135]\n"
      "v_cvt_pk_bf16_f32 v214, v136, v137\n"
      "v_mfma_f32_16x16x32_bf16 v[132:135], a[0:3], v[64:67], v[132:135]\n"
      "v_cvt_pk_bf16_f32 v215, v138, v139\n"
      "v_mfma_f32_16x16x32_bf16 v[132:135], a[4:7], v[68:71], v[132:135]\n"
      "v_mfma_f32_16x16x32_bf16 v[132:135], a[8:11], v[84:87], v[132:135]\n"
      "v_mfma_f32_16x16x32_bf16 v[132:135], a[12:15], v[88:91], v[132:135]\n"
      "v_mfma_f32_16x16x32_bf16 v[132:135], a[16:19], v[92:95], v[132:135]\n"
      "v_mfma_f32_16x16x32_bf16 v[132:135], a[20:23], v[108:111], v[132:135]\n"
      "ds_write_b64 v245, v[214:215] offset:24584\n"
      "v_mfma_f32_16x16x32_bf16 v[132:135], v[140:143], v[112:115], v[132:135]\n"
      "v_mfma_f32_16x16x32_bf16 v[132:135], v[156:159], v[116:119], v[132:135]\n"
      "s_waitcnt lgkmcnt(4)\n"
      "s_barrier\n"
      "v_mfma_f32_16x16x32_bf16 v[136:139], v[160:163], v[36:39], v[20:23]\n"
      "ds_read_b128 v[36:39], v232 offset:256\n"
      "v_mfma_f32_16x16x32_bf16 v[136:139], v[164:167], v[40:43], v[136:139]\n"
      "ds_read_b128 v[40:43], v233 offset:256\n"
      "v_mfma_f32_16x16x32_bf16 v[136:139], v[180:183], v[44:47], v[136:139]\n"
      "ds_read_b128 v[44:47], v234 offset:256\n"
      "v_mfma_f32_16x16x32_bf16 v[136:139], v[184:187], v[60:63], v[136:139]\n"
      "ds_read_b128 v[60:63], v235 offset:256\n"
      "v_cvt_pk_bf16_f32 v208, v132, v133\n"
      "v_mfma_f32_16x16x32_bf16 v[136:139], v[188:191], v[64:67], v[136:139]\n"
      "ds_read_b128 v[64:67], v232 offset:8448\n"
      "v_cvt_pk_bf16_f32 v209, v134, v135\n"
      "v_mfma_f32_16x16x32_bf16 v[136:139], v[204:207], v[68:71], v[136:139]\n"
      "ds_read_b128 v[68:71], v233 offset:8448\n"
      "v_mfma_f32_16x16x32_bf16 v[136:139], a[24:27], v[84:87], v[136:139]\n"
      "ds_read_b128 v[84:87], v234 offset:8448\n"
      "v_mfma_f32_16x16x32_bf16 v[136:139], a[28:31], v[88:91], v[136:139]\n"
      "ds_read_b128 v[88:91], v235 offset:8448\n"
      "v_mfma_f32_16x16x32_bf16 v[136:139], a[32:35], v[92:95], v[136:139]\n"
      "ds_read_b128 v[92:95], v232 offset:16640\n"
      "v_mfma_f32_16x16x32_bf16 v[136:139], a[36:39], v[108:111], v[136:139]\n"
      "ds_read_b128 v[108:111], v233 offset:16640\n"
      "v_mfma_f32_16x16x32_bf16 v[136:139], a[40:43], v[112:115], v[136:139]\n"
      "ds_read_b128 v[112:115], v234 offset:16640\n"
      "v_mfma_f32_16x16x32_bf16 v[136:139], a[44:47], v[116:119], v[136:139]\n"
      "ds_read_b128 v[116:119], v235 offset:16640\n"
      "s_waitcnt lgkmcnt(8)\n"
      "v_mfma_f32_16x16x32_bf16 v[132:135], a[240:243], v[36:39], v[0:3]\n"
      "ds_write_b64 v244, v[208:209] offset:24704\n"
      "v_mfma_f32_16x16x32_bf16 v[132:135], a[244:247], v[40:43], v[132:135]\n"
      "v_mfma_f32_16x16x32_bf16 v[132:135], a[248:251], v[44:47], v[132:135]\n"
      "v_mfma_f32_16x16x32_bf16 v[132:135], a[252:255], v[60:63], v[132:135]\n"
      "v_cvt_pk_bf16_f32 v210, v136, v137\n"
      "s_waitcnt lgkmcnt(5)\n"
      "v_mfma_f32_16x16x32_bf16 v[132:135], a[48:51], v[64:67], v[132:135]\n"
      "v_cvt_pk_bf16_f32 v211, v138, v139\n"
      "v_mfma_f32_16x16x32_bf16 v[132:135], a[52:55], v[68:71], v[132:135]\n"
      "v_mfma_f32_16x16x32_bf16 v[132:135], a[56:59], v[84:87], v[132:135]\n"
      "ds_read_b128 v[216:219], v246 offset:12288\n"
      "v_mfma_f32_16x16x32_bf16 v[132:135], a[60:63], v[88:91], v[132:135]\n"
      "ds_read_b128 v[220:223], v246 offset:13312\n"
      "s_waitcnt lgkmcnt(3)\n"
      "v_mfma_f32_16x16x32_bf16 v[132:135], a[64:67], v[92:95], v[132:135]\n"
      "ds_read_b128 v[224:227], v246 offset:14336\n"
      "v_mfma_f32_16x16x32_bf16 v[132:135], a[68:71], v[108:111], v[132:135]\n"
      "ds_write_b64 v244, v[210:211] offset:24712\n"
      "v_mfma_f32_16x16x32_bf16 v[132:135], a[72:75], v[112:115], v[132:135]\n"
      "v_mfma_f32_16x16x32_bf16 v[132:135], a[76:79], v[116:119], v[132:135]\n"
      "v_mfma_f32_16x16x32_bf16 v[136:139], a[80:83], v[36:39], v[4:7]\n"
      "v_mfma_f32_16x16x32_bf16 v[136:139], a[84:87], v[40:43], v[136:139]\n"
      "v_mfma_f32_16x16x32_bf16 v[136:139], a[88:91], v[44:47], v[136:139]\n"
      "v_mfma_f32_16x16x32_bf16 v[136:139], a[92:95], v[60:63], v[136:139]\n"
      "v_cvt_pk_bf16_f32 v208, v132, v133\n"
      "v_mfma_f32_16x16x32_bf16 v[136:139], a[96:99], v[64:67], v[136:139]\n"
      "v_cvt_pk_bf16_f32 v209, v134, v135\n"
      "v_mfma_f32_16x16x32_bf16 v[136:139], a[100:103], v[68:71], v[136:139]\n"
      "s_waitcnt lgkmcnt(3)\n"
      "v_mfma_f32_16x16x32_bf16 v[136:139], a[104:107], v[84:87], v[136:139]\n"
      "global_store_dwordx4 v247, v[216:219], s[54:55] offset:768 sc1\n"
      "v_mfma_f32_16x16x32_bf16 v[136:139], a[108:111], v[88:91], v[136:139]\n"
      "s_add_u32 m0, s17, 0x7000\n"
      "v_mfma_f32_16x16x32_bf16 v[136:139], a[112:115], v[92:95], v[136:139]\n"
      "global_load_lds_dwordx4 v237, s[52:53] offset:0 nt\n"
      "v_mfma_f32_16x16x32_bf16 v[136:139], a[116:119], v[108:111], v[136:139]\n"
      "ds_write_b64 v244, v[208:209] offset:0\n"
      "v_mfma_f32_16x16x32_bf16 v[136:139], a[120:123], v[112:115], v[136:139]\n"
      "v_mfma_f32_16x16x32_bf16 v[136:139], a[124:127], v[116:119], v[136:139]\n"
      "v_mfma_f32_16x16x32_bf16 v[132:135], a[128:131], v[36:39], v[8:11]\n"
      "v_mfma_f32_16x16x32_bf16 v[132:135], a[132:135], v[40:43], v[132:135]\n"
      "v_mfma_f32_16x16x32_bf16 v[132:135], a[136:139], v[44:47], v[132:135]\n"
      "v_mfma_f32_16x16x32_bf16 v[132:135], a[140:143], v[60:63], v[132:135]\n"
      "v_cvt_pk_bf16_f32 v210, v136, v137\n"
      "v_mfma_f32_16x16x32_bf16 v[132:135], a[144:147], v[64:67], v[132:135]\n"
      "v_cvt_pk_bf16_f32 v211, v138, v139\n"
      "v_mfma_f32_16x16x32_bf16 v[132:135], a[148:151], v[68:71], v[132:135]\n"
      "v_mfma_f32_16x16x32_bf16 v[132:135], a[152:155], v[84:87], v[132:135]\n"
      "s_waitcnt lgkmcnt(3)\n"
      "v_mfma_f32_16x16x32_bf16 v[132:135], a[156:159], v[88:91], v[132:135]\n"
      "global_store_dwordx4 v252, v[220:223], s[54:55] offset:768 sc1\n"
      "v_mfma_f32_16x16x32_bf16 v[132:135], a[160:163], v[92:95], v[132:135]\n"
      "s_add_u32 m0, s17, 0x8f00\n"
      "v_mfma_f32_16x16x32_bf16 v[132:135], a[164:167], v[108:111], v[132:135]\n"
      "ds_write_b64 v244, v[210:211] offset:8\n"
      "v_mfma_f32_16x16x32_bf16 v[132:135], a[168:171], v[112:115], v[132:135]\n"
      "global_load_lds_dwordx4 v237, s[52:53] offset:256 nt\n"
      "v_mfma_f32_16x16x32_bf16 v[132:135], a[172:175], v[116:119], v[132:135]\n"
      "v_mfma_f32_16x16x32_bf16 v[136:139], a[176:179], v[36:39], v[12:15]\n"
      "v_mfma_f32_16x16x32_bf16 v[136:139], a[180:183], v[40:43], v[136:139]\n"
      "v_mfma_f32_16x16x32_bf16 v[136:139], a[184:187], v[44:47], v[136:139]\n"
      "v_mfma_f32_16x16x32_bf16 v[136:139], a[188:191], v[60:63], v[136:139]\n"
      "v_cvt_pk_bf16_f32 v212, v132, v133\n"
      "v_mfma_f32_16x16x32_bf16 v[136:139], a[192:195], v[64:67], v[136:139]\n"
      "v_cvt_pk_bf16_f32 v213, v134, v135\n"
      "v_mfma_f32_16x16x32_bf16 v[136:139], a[196:199], v[68:71], v[136:139]\n"
      "v_mfma_f32_16x16x32_bf16 v[136:139], a[200:203], v[84:87], v[136:139]\n"
      "v_mfma_f32_16x16x32_bf16 v[136:139], a[204:207], v[88:91], v[136:139]\n"
      "s_waitcnt lgkmcnt(3)\n"
      "v_mfma_f32_16x16x32_bf16 v[136:139], a[208:211], v[92:95], v[136:139]\n"
      "global_store_dwordx4 v253, v[224:227], s[54:55] offset:768 sc1\n"
      "v_mfma_f32_16x16x32_bf16 v[136:139], a[212:215], v[108:111], v[136:139]\n"
      "ds_write_b64 v245, v[212:213] offset:0\n"
      "v_mfma_f32_16x16x32_bf16 v[136:139], a[216:219], v[112:115], v[136:139]\n"
      "s_add_u32 m0, s17, 0xae00\n"
      "v_mfma_f32_16x16x32_bf16 v[136:139], a[220:223], v[116:119], v[136:139]\n"
      "global_load_lds_dwordx4 v237, s[52:53] offset:512 nt\n"
      "v_mfma_f32_16x16x32_bf16 v[132:135], a[224:227], v[36:39], v[16:19]\n"
      "v_mfma_f32_16x16x32_bf16 v[132:135], a[228:231], v[40:43], v[132:135]\n"
      "v_mfma_f32_16x16x32_bf16 v[132:135], a[232:235], v[44:47], v[132:135]\n"
      "v_mfma_f32_16x16x32_bf16 v[132:135], a[236:239], v[60:63], v[132:135]\n"
      "v_cvt_pk_bf16_f32 v214, v136, v137\n"
      "v_mfma_f32_16x16x32_bf16 v[132:135], a[0:3], v[64:67], v[132:135]\n"
      "v_cvt_pk_bf16_f32 v215, v138, v139\n"
      "v_mfma_f32_16x16x32_bf16 v[132:135], a[4:7], v[68:71], v[132:135]\n"
      "v_mfma_f32_16x16x32_bf16 v[132:135], a[8:11], v[84:87], v[132:135]\n"
      "v_mfma_f32_16x16x32_bf16 v[132:135], a[12:15], v[88:91], v[132:135]\n"
      "v_mfma_f32_16x16x32_bf16 v[132:135], a[16:19], v[92:95], v[132:135]\n"
      "v_mfma_f32_16x16x32_bf16 v[132:135], a[20:23], v[108:111], v[132:135]\n"
      "ds_write_b64 v245, v[214:215] offset:8\n"
      "v_mfma_f32_16x16x32_bf16 v[132:135], v[140:143], v[112:115], v[132:135]\n"
      "s_lshl_b32 s79, s90, 1\n"
      "v_mfma_f32_16x16x32_bf16 v[132:135], v[156:159], v[116:119], v[132:135]\n"
      "s_waitcnt vmcnt(24) lgkmcnt(4)\n"
      "s_barrier\n"
      "v_mad_i32_i24 v252, v251, s79, v252\n"
      "v_mfma_f32_16x16x32_bf16 v[136:139], v[160:163], v[36:39], v[20:23]\n"
      "ds_read_b128 v[36:39], v232 offset:24576\n"
      "s_mul_i32 s79, s79, s100\n"
      "v_mfma_f32_16x16x32_bf16 v[136:139], v[164:167], v[40:43], v[136:139]\n"
      "ds_read_b128 v[40:43], v233 offset:24576\n"
      "v_add_u32 v247, s79, v247\n"
      "v_mfma_f32_16x16x32_bf16 v[136:139], v[180:183], v[44:47], v[136:139]\n"
      "ds_read_b128 v[44:47], v234 offset:24576\n"
      "s_mul_i32 s79, s79, s101\n"
      "v_mfma_f32_16x16x32_bf16 v[136:139], v[184:187], v[60:63], v[136:139]\n"
      "ds_read_b128 v[60:63], v235 offset:24576\n"
      "v_cvt_pk_bf16_f32 v208, v132, v133\n"
      "v_mfma_f32_16x16x32_bf16 v[136:139], v[188:191], v[64:67], v[136:139]\n"
      "ds_read_b128 v[64:67], v232 offset:32768\n"
      "v_cvt_pk_bf16_f32 v209, v134, v135\n"
      "v_mfma_f32_16x16x32_bf16 v[136:139], v[204:207], v[68:71], v[136:139]\n"
      "ds_read_b128 v[68:71], v233 offset:32768\n"
      "v_add_u32 v253, s79, v253\n"
      "v_mfma_f32_16x16x32_bf16 v[136:139], a[24:27], v[84:87], v[136:139]\n"
      "ds_read_b128 v[84:87], v234 offset:32768\n"
      "v_mfma_f32_16x16x32_bf16 v[136:139], a[28:31], v[88:91], v[136:139]\n"
      "ds_read_b128 v[88:91], v235 offset:32768\n"
      "v_mfma_f32_16x16x32_bf16 v[136:139], a[32:35], v[92:95], v[136:139]\n"
      "ds_read_b128 v[92:95], v232 offset:40960\n"
      "v_mfma_f32_16x16x32_bf16 v[136:139], a[36:39], v[108:111], v[136:139]\n"
      "ds_read_b128 v[108:111], v233 offset:40960\n"
      "v_mfma_f32_16x16x32_bf16 v[136:139], a[40:43], v[112:115], v[136:139]\n"
      "ds_read_b128 v[112:115], v234 offset:40960\n"
      "v_mfma_f32_16x16x32_bf16 v[136:139], a[44:47], v[116:119], v[136:139]\n"
      "ds_read_b128 v[116:119], v235 offset:40960\n"
      "s_waitcnt lgkmcnt(8)\n"
      "v_mfma_f32_16x16x32_bf16 v[132:135], a[240:243], v[36:39], v[0:3]\n"
      "ds_write_b64 v244, v[208:209] offset:128\n"
      "v_mfma_f32_16x16x32_bf16 v[132:135], a[244:247], v[40:43], v[132:135]\n"
      "s_add_u32 s54, s30, s33\n"
      "s_addc_u32 s55, s31, 0\n"
      "v_mfma_f32_16x16x32_bf16 v[132:135], a[248:251], v[44:47], v[132:135]\n"
      "v_mfma_f32_16x16x32_bf16 v[132:135], a[252:255], v[60:63], v[132:135]\n"
      "v_cvt_pk_bf16_f32 v210, v136, v137\n"
      "s_waitcnt lgkmcnt(5)\n"
      "v_mfma_f32_16x16x32_bf16 v[132:135], a[48:51], v[64:67], v[132:135]\n"
      "v_cvt_pk_bf16_f32 v211, v138, v139\n"
      "v_mfma_f32_16x16x32_bf16 v[132:135], a[52:55], v[68:71], v[132:135]\n"
      "s_mul_i32 s78, s92, s19\n"
      "v_mfma_f32_16x16x32_bf16 v[132:135], a[56:59], v[84:87], v[132:135]\n"
      "ds_read_b128 v[216:219], v246 offset:24576\n"
      "v_add_u32 v236, s78, v236\n"
      "v_mfma_f32_16x16x32_bf16 v[132:135], a[60:63], v[88:91], v[132:135]\n"
      "ds_read_b128 v[220:223], v246 offset:25600\n"
      "s_waitcnt lgkmcnt(3)\n"
      "v_mfma_f32_16x16x32_bf16 v[132:135], a[64:67], v[92:95], v[132:135]\n"
      "ds_read_b128 v[224:227], v246 offset:26624\n"
      "v_mfma_f32_16x16x32_bf16 v[132:135], a[68:71], v[108:111], v[132:135]\n"
      "ds_write_b64 v244, v[210:211] offset:136\n"
      "v_mfma_f32_16x16x32_bf16 v[132:135], a[72:75], v[112:115], v[132:135]\n"
      "s_mul_i32 s78, s78, s99\n"
      "v_mfma_f32_16x16x32_bf16 v[132:135], a[76:79], v[116:119], v[132:135]\n"
      "v_add_u32 v237, s78, v237\n"
      "v_mfma_f32_16x16x32_bf16 v[136:139], a[80:83], v[36:39], v[4:7]\n"
      "v_mfma_f32_16x16x32_bf16 v[136:139], a[84:87], v[40:43], v[136:139]\n"
      "v_mfma_f32_16x16x32_bf16 v[136:139], a[88:91], v[44:47], v[136:139]\n"
      "v_mfma_f32_16x16x32_bf16 v[136:139], a[92:95], v[60:63], v[136:139]\n"
      "v_cvt_pk_bf16_f32 v208, v132, v133\n"
      "v_mfma_f32_16x16x32_bf16 v[136:139], a[96:99], v[64:67], v[136:139]\n"
      "v_cvt_pk_bf16_f32 v209, v134, v135\n"
      "v_mfma_f32_16x16x32_bf16 v[136:139], a[100:103], v[68:71], v[136:139]\n"
      "s_waitcnt lgkmcnt(3)\n"
      "v_mfma_f32_16x16x32_bf16 v[136:139], a[104:107], v[84:87], v[136:139]\n"
      "global_store_dwordx4 v247, v[216:219], s[54:55] offset:0 sc1\n"
      "v_mfma_f32_16x16x32_bf16 v[136:139], a[108:111], v[88:91], v[136:139]\n"
      "s_add_u32 s52, s20, s35\n"
      "s_addc_u32 s53, s21, 0\n"
      "v_mfma_f32_16x16x32_bf16 v[136:139], a[112:115], v[92:95], v[136:139]\n"
      "s_add_u32 m0, s17, 0xc000\n"
      "v_mfma_f32_16x16x32_bf16 v[136:139], a[116:119], v[108:111], v[136:139]\n"
      "ds_write_b64 v244, v[208:209] offset:12288\n"
      "v_mfma_f32_16x16x32_bf16 v[136:139], a[120:123], v[112:115], v[136:139]\n"
      "global_load_lds_dwordx4 v236, s[52:53] offset:0 nt\n"
      "v_mfma_f32_16x16x32_bf16 v[136:139], a[124:127], v[116:119], v[136:139]\n"
      "v_mfma_f32_16x16x32_bf16 v[132:135], a[128:131], v[36:39], v[8:11]\n"
      "v_min_u32 v28, 0x370, v239\n"
      "v_mfma_f32_16x16x32_bf16 v[132:135], a[132:135], v[40:43], v[132:135]\n"
      "v_mfma_f32_16x16x32_bf16 v[132:135], a[136:139], v[44:47], v[132:135]\n"
      "v_mfma_f32_16x16x32_bf16 v[132:135], a[140:143], v[60:63], v[132:135]\n"
      "v_cvt_pk_bf16_f32 v210, v136, v137\n"
      "v_mfma_f32_16x16x32_bf16 v[132:135], a[144:147], v[64:67], v[132:135]\n"
      "v_cvt_pk_bf16_f32 v211, v138, v139\n"
      "v_mfma_f32_16x16x32_bf16 v[132:135], a[148:151], v[68:71], v[132:135]\n"
      "v_add_u32 v28, 0x2480, v28\n"
      "v_mfma_f32_16x16x32_bf16 v[132:135], a[152:155], v[84:87], v[132:135]\n"
      "s_waitcnt lgkmcnt(3)\n"
      "global_load_dwordx4 v[24:27], v28, s[28:29] sc1\n"
      "v_mfma_f32_16x16x32_bf16 v[132:135], a[156:159], v[88:91], v[132:135]\n"
      "global_store_dwordx4 v252, v[220:223], s[54:55] offset:0 sc1\n"
      "v_mfma_f32_16x16x32_bf16 v[132:135], a[160:163], v[92:95], v[132:135]\n"
      "s_add_u32 m0, s17, 0xdf00\n"
      "v_mfma_f32_16x16x32_bf16 v[132:135], a[164:167], v[108:111], v[132:135]\n"
      "ds_write_b64 v244, v[210:211] offset:12296\n"
      "v_mfma_f32_16x16x32_bf16 v[132:135], a[168:171], v[112:115], v[132:135]\n"
      "global_load_lds_dwordx4 v236, s[52:53] offset:256 nt\n"
      "v_mfma_f32_16x16x32_bf16 v[132:135], a[172:175], v[116:119], v[132:135]\n"
      "v_mfma_f32_16x16x32_bf16 v[136:139], a[176:179], v[36:39], v[12:15]\n"
      "v_mfma_f32_16x16x32_bf16 v[136:139], a[180:183], v[40:43], v[136:139]\n"
      "v_mfma_f32_16x16x32_bf16 v[136:139], a[184:187], v[44:47], v[136:139]\n"
      "v_mfma_f32_16x16x32_bf16 v[136:139], a[188:191], v[60:63], v[136:139]\n"
      "v_cvt_pk_bf16_f32 v212, v132, v133\n"
      "v_mfma_f32_16x16x32_bf16 v[136:139], a[192:195], v[64:67], v[136:139]\n"
      "v_cvt_pk_bf16_f32 v213, v134, v135\n"
      "v_mfma_f32_16x16x32_bf16 v[136:139], a[196:199], v[68:71], v[136:139]\n"
      "v_mfma_f32_16x16x32_bf16 v[136:139], a[200:203], v[84:87], v[136:139]\n"
      "v_mfma_f32_16x16x32_bf16 v[136:139], a[204:207], v[88:91], v[136:139]\n"
      "s_waitcnt lgkmcnt(3)\n"
      "v_mfma_f32_16x16x32_bf16 v[136:139], a[208:211], v[92:95], v[136:139]\n"
      "global_store_dwordx4 v253, v[224:227], s[54:55] offset:0 sc1\n"
      "v_mfma_f32_16x16x32_bf16 v[136:139], a[212:215], v[108:111], v[136:139]\n"
      "ds_write_b64 v245, v[212:213] offset:12288\n"
      "v_mfma_f32_16x16x32_bf16 v[136:139], a[216:219], v[112:115], v[136:139]\n"
      "s_add_u32 m0, s17, 0xfe00\n"
      "v_mfma_f32_16x16x32_bf16 v[136:139], a[220:223], v[116:119], v[136:139]\n"
      "global_load_lds_dwordx4 v236, s[52:53] offset:512 nt\n"
      "v_mfma_f32_16x16x32_bf16 v[132:135], a[224:227], v[36:39], v[16:19]\n"
      "v_mfma_f32_16x16x32_bf16 v[132:135], a[228:231], v[40:43], v[132:135]\n"
      "v_mfma_f32_16x16x32_bf16 v[132:135], a[232:235], v[44:47], v[132:135]\n"
      "v_mfma_f32_16x16x32_bf16 v[132:135], a[236:239], v[60:63], v[132:135]\n"
      "v_cvt_pk_bf16_f32 v214, v136, v137\n"
      "v_mfma_f32_16x16x32_bf16 v[132:135], a[0:3], v[64:67], v[132:135]\n"
      "v_cvt_pk_bf16_f32 v215, v138, v139\n"
      "v_mfma_f32_16x16x32_bf16 v[132:135], a[4:7], v[68:71], v[132:135]\n"
      "v_mfma_f32_16x16x32_bf16 v[132:135], a[8:11], v[84:87], v[132:135]\n"
      "v_mfma_f32_16x16x32_bf16 v[132:135], a[12:15], v[88:91], v[132:135]\n"
      "v_mfma_f32_16x16x32_bf16 v[132:135], a[16:19], v[92:95], v[132:135]\n"
      "v_mfma_f32_16x16x32_bf16 v[132:135], a[20:23], v[108:111], v[132:135]\n"
      "ds_write_b64 v245, v[214:215] offset:12296\n"
      "v_mfma_f32_16x16x32_bf16 v[132:135], v[140:143], v[112:115], v[132:135]\n"
      "v_mfma_f32_16x16x32_bf16 v[132:135], v[156:159], v[116:119], v[132:135]\n"
      "s_waitcnt lgkmcnt(4)\n"
      "s_barrier\n"
      "v_mfma_f32_16x16x32_bf16 v[136:139], v[160:163], v[36:39], v[20:23]\n"
      "ds_read_b128 v[36:39], v232 offset:24832\n"
      "v_mfma_f32_16x16x32_bf16 v[136:139], v[164:167], v[40:43], v[136:139]\n"
      "ds_read_b128 v[40:43], v233 offset:24832\n"
      "v_mfma_f32_16x16x32_bf16 v[136:139], v[180:183], v[44:47], v[136:139]\n"
      "ds_read_b128 v[44:47], v234 offset:24832\n"
      "v_mfma_f32_16x16x32_bf16 v[136:139], v[184:187], v[60:63], v[136:139]\n"
      "ds_read_b128 v[60:63], v235 offset:24832\n"
      "v_cvt_pk_bf16_f32 v208, v132, v133\n"
      "v_mfma_f32_16x16x32_bf16 v[136:139], v[188:191], v[64:67], v[136:139]\n"
      "ds_read_b128 v[64:67], v232 offset:33024\n"
      "v_cvt_pk_bf16_f32 v209, v134, v135\n"
      "v_mfma_f32_16x16x32_bf16 v[136:139], v[204:207], v[68:71], v[136:139]\n"
      "ds_read_b128 v[68:71], v233 offset:33024\n"
      "v_mfma_f32_16x16x32_bf16 v[136:139], a[24:27], v[84:87], v[136:139]\n"
      "ds_read_b128 v[84:87], v234 offset:33024\n"
      "v_mfma_f32_16x16x32_bf16 v[136:139], a[28:31], v[88:91], v[136:139]\n"
      "ds_read_b128 v[88:91], v235 offset:33024\n"
      "v_mfma_f32_16x16x32_bf16 v[136:139], a[32:35], v[92:95], v[136:139]\n"
      "ds_read_b128 v[92:95], v232 offset:41216\n"
      "v_mfma_f32_16x16x32_bf16 v[136:139], a[36:39], v[108:111], v[136:139]\n"
      "ds_read_b128 v[108:111], v233 offset:41216\n"
      "ds_write_b64 v244, v[208:209] offset:12416\n"
      "v_mfma_f32_16x16x32_bf16 v[136:139], a[40:43], v[112:115], v[136:139]\n"
      "ds_read_b128 v[112:115], v234 offset:41216\n"
      "v_mfma_f32_16x16x32_bf16 v[136:139], a[44:47], v[116:119], v[136:139]\n"
      "ds_read_b128 v[116:119], v235 offset:41216\n"
      "s_waitcnt lgkmcnt(9)\n"
      "v_mfma_f32_16x16x32_bf16 v[132:135], a[240:243], v[36:39], v[0:3]\n"
      "v_add_u32 v232, 0xffff4000, v232\n"
      "v_mfma_f32_16x16x32_bf16 v[132:135], a[244:247], v[40:43], v[132:135]\n"
      "v_add_u32 v233, 0xffff4000, v233\n"
      "v_mfma_f32_16x16x32_bf16 v[132:135], a[248:251], v[44:47], v[132:135]\n"
      "LPD9:\n"
      "s_cbranch_execz LPD12\n"
      "v_add_u32 v234, 0xffff4000, v234\n"
      "v_mfma_f32_16x16x32_bf16 v[132:135], a[252:255], v[60:63], v[132:135]\n"
      "v_add_u32 v235, 0xffff4000, v235\n"
      "s_waitcnt lgkmcnt(5)\n"
      "v_mfma_f32_16x16x32_bf16 v[132:135], a[48:51], v[64:67], v[132:135]\n"
      "v_cvt_pk_bf16_f32 v210, v136, v137\n"
      "v_mfma_f32_16x16x32_bf16 v[132:135], a[52:55], v[68:71], v[132:135]\n"
      "v_cvt_pk_bf16_f32 v211, v138, v139\n"
      "v_mfma_f32_16x16x32_bf16 v[132:135], a[56:59], v[84:87], v[132:135]\n"
      "ds_read_b128 v[216:219], v246 offset:0\n"
      "v_mfma_f32_16x16x32_bf16 v[132:135], a[60:63], v[88:91], v[132:135]\n"
      "ds_read_b128 v[220:223], v246 offset:1024\n"
      "s_waitcnt lgkmcnt(2)\n"
      "v_mfma_f32_16x16x32_bf16 v[132:135], a[64:67], v[92:95], v[132:135]\n"
      "ds_read_b128 v[224:227], v246 offset:2048\n"
      "v_mfma_f32_16x16x32_bf16 v[132:135], a[68:71], v[108:111], v[132:135]\n"
      "v_lshlrev_b32 v0, 16, v48\n"
      "v_mfma_f32_16x16x32_bf16 v[132:135], a[72:75], v[112:115], v[132:135]\n"
      "ds_write_b64 v244, v[210:211] offset:12424\n"
      "v_mfma_f32_16x16x32_bf16 v[132:135], a[76:79], v[116:119], v[132:135]\n"
      "v_and_b32 v1, 0xffff0000, v48\n"
      "v_mfma_f32_16x16x32_bf16 v[136:139], a[80:83], v[36:39], v[4:7]\n"
      "v_lshlrev_b32 v2, 16, v49\n"
      "v_mfma_f32_16x16x32_bf16 v[136:139], a[84:87], v[40:43], v[136:139]\n"
      "v_and_b32 v3, 0xffff0000, v49\n"
      "v_mfma_f32_16x16x32_bf16 v[136:139], a[88:91], v[44:47], v[136:139]\n"
      "v_mfma_f32_16x16x32_bf16 v[136:139], a[92:95], v[60:63], v[136:139]\n"
      "v_cvt_pk_bf16_f32 v208, v132, v133\n"
      "v_mfma_f32_16x16x32_bf16 v[136:139], a[96:99], v[64:67], v[136:139]\n"
      "v_cvt_pk_bf16_f32 v209, v134, v135\n"
      "v_mfma_f32_16x16x32_bf16 v[136:139], a[100:103], v[68:71], v[136:139]\n"
      "s_waitcnt lgkmcnt(3)\n"
      "v_mfma_f32_16x16x32_bf16 v[136:139], a[104:107], v[84:87], v[136:139]\n"
      "global_store_dwordx4 v247, v[216:219], s[54:55] offset:768 sc1\n"
      "v_mfma_f32_16x16x32_bf16 v[136:139], a[108:111], v[88:91], v[136:139]\n"
      "s_add_u32 m0, s17, 0xd000\n"
      "v_mfma_f32_16x16x32_bf16 v[136:139], a[112:115], v[92:95], v[136:139]\n"
      "global_load_lds_dwordx4 v237, s[52:53] offset:0 nt\n"
      "v_mfma_f32_16x16x32_bf16 v[136:139], a[116:119], v[108:111], v[136:139]\n"
      "ds_write_b64 v244, v[208:209] offset:24576\n"
      "v_mfma_f32_16x16x32_bf16 v[136:139], a[120:123], v[112:115], v[136:139]\n"
      "v_lshlrev_b32 v4, 16, v50\n"
      "v_mfma_f32_16x16x32_bf16 v[136:139], a[124:127], v[116:119], v[136:139]\n"
      "v_and_b32 v5, 0xffff0000, v50\n"
      "v_mfma_f32_16x16x32_bf16 v[132:135], a[128:131], v[36:39], v[8:11]\n"
      "v_lshlrev_b32 v6, 16, v51\n"
      "v_mfma_f32_16x16x32_bf16 v[132:135], a[132:135], v[40:43], v[132:135]\n"
      "v_and_b32 v7, 0xffff0000, v51\n"
      "v_mfma_f32_16x16x32_bf16 v[132:135], a[136:139], v[44:47], v[132:135]\n"
      "v_mfma_f32_16x16x32_bf16 v[132:135], a[140:143], v[60:63], v[132:135]\n"
      "v_cvt_pk_bf16_f32 v210, v136, v137\n"
      "v_mfma_f32_16x16x32_bf16 v[132:135], a[144:147], v[64:67], v[132:135]\n"
      "v_cvt_pk_bf16_f32 v211, v138, v139\n"
      "v_mfma_f32_16x16x32_bf16 v[132:135], a[148:151], v[68:71], v[132:135]\n"
      "v_lshlrev_b32 v8, 16, v52\n"
      "v_mfma_f32_16x16x32_bf16 v[132:135], a[152:155], v[84:87], v[132:135]\n"
      "s_waitcnt lgkmcnt(3)\n"
      "v_mfma_f32_16x16x32_bf16 v[132:135], a[156:159], v[88:91], v[132:135]\n"
      "global_store_dwordx4 v252, v[220:223], s[54:55] offset:768 sc1\n"
      "v_mfma_f32_16x16x32_bf16 v[132:135], a[160:163], v[92:95], v[132:135]\n"
      "s_add_u32 m0, s17, 0xef00\n"
      "v_mfma_f32_16x16x32_bf16 v[132:135], a[164:167], v[108:111], v[132:135]\n"
      "ds_write_b64 v244, v[210:211] offset:24584\n"
      "v_mfma_f32_16x16x32_bf16 v[132:135], a[168:171], v[112:115], v[132:135]\n"
      "global_load_lds_dwordx4 v237, s[52:53] offset:256 nt\n"
      "v_mfma_f32_16x16x32_bf16 v[132:135], a[172:175], v[116:119], v[132:135]\n"
      "v_and_b32 v9, 0xffff0000, v52\n"
      "v_mfma_f32_16x16x32_bf16 v[136:139], a[176:179], v[36:39], v[12:15]\n"
      "v_lshlrev_b32 v10, 16, v53\n"
      "v_mfma_f32_16x16x32_bf16 v[136:139], a[180:183], v[40:43], v[136:139]\n"
      "v_and_b32 v11, 0xffff0000, v53\n"
      "v_mfma_f32_16x16x32_bf16 v[136:139], a[184:187], v[44:47], v[136:139]\n"
      "v_mfma_f32_16x16x32_bf16 v[136:139], a[188:191], v[60:63], v[136:139]\n"
      "v_cvt_pk_bf16_f32 v212, v132, v133\n"
      "v_mfma_f32_16x16x32_bf16 v[136:139], a[192:195], v[64:67], v[136:139]\n"
      "v_cvt_pk_bf16_f32 v213, v134, v135\n"
      "v_mfma_f32_16x16x32_bf16 v[136:139], a[196:199], v[68:71], v[136:139]\n"
      "v_lshlrev_b32 v12, 16, v54\n"
      "v_mfma_f32_16x16x32_bf16 v[136:139], a[200:203], v[84:87], v[136:139]\n"
      "v_and_b32 v13, 0xffff0000, v54\n"
      "v_mfma_f32_16x16x32_bf16 v[136:139], a[204:207], v[88:91], v[136:139]\n"
      "s_waitcnt lgkmcnt(3)\n"
      "v_mfma_f32_16x16x32_bf16 v[136:139], a[208:211], v[92:95], v[136:139]\n"
      "global_store_dwordx4 v253, v[224:227], s[54:55] offset:768 sc1\n"
      "v_mfma_f32_16x16x32_bf16 v[136:139], a[212:215], v[108:111], v[136:139]\n"
      "ds_write_b64 v245, v[212:213] offset:24576\n"
      "v_mfma_f32_16x16x32_bf16 v[136:139], a[216:219], v[112:115], v[136:139]\n"
      "s_add_u32 m0, s17, 0x10e00\n"
      "v_mfma_f32_16x16x32_bf16 v[136:139], a[220:223], v[116:119], v[136:139]\n"
      "global_load_lds_dwordx4 v237, s[52:53] offset:512 nt\n"
      "v_mfma_f32_16x16x32_bf16 v[132:135], a[224:227], v[36:39], v[16:19]\n"
      "v_lshlrev_b32 v14, 16, v55\n"
      "v_mfma_f32_16x16x32_bf16 v[132:135], a[228:231], v[40:43], v[132:135]\n"
      "v_and_b32 v15, 0xffff0000, v55\n"
      "v_mfma_f32_16x16x32_bf16 v[132:135], a[232:235], v[44:47], v[132:135]\n"
      "v_mfma_f32_16x16x32_bf16 v[132:135], a[236:239], v[60:63], v[132:135]\n"
      "v_cvt_pk_bf16_f32 v214, v136, v137\n"
      "v_mfma_f32_16x16x32_bf16 v[132:135], a[0:3], v[64:67], v[132:135]\n"
      "v_cvt_pk_bf16_f32 v215, v138, v139\n"
      "v_mfma_f32_16x16x32_bf16 v[132:135], a[4:7], v[68:71], v[132:135]\n"
      "v_lshlrev_b32 v16, 16, v56\n"
      "v_mfma_f32_16x16x32_bf16 v[132:135], a[8:11], v[84:87], v[132:135]\n"
      "v_and_b32 v17, 0xffff0000, v56\n"
      "v_mfma_f32_16x16x32_bf16 v[132:135], a[12:15], v[88:91], v[132:135]\n"
      "v_lshlrev_b32 v18, 16, v57\n"
      "v_mfma_f32_16x16x32_bf16 v[132:135], a[16:19], v[92:95], v[132:135]\n"
      "v_and_b32 v19, 0xffff0000, v57\n"
      "v_mfma_f32_16x16x32_bf16 v[132:135], a[20:23], v[108:111], v[132:135]\n"
      "ds_write_b64 v245, v[214:215] offset:24584\n"
      "v_mfma_f32_16x16x32_bf16 v[132:135], v[140:143], v[112:115], v[132:135]\n"
      "v_mfma_f32_16x16x32_bf16 v[132:135], v[156:159], v[116:119], v[132:135]\n"
      "s_waitcnt vmcnt(25) lgkmcnt(4)\n"
      "s_barrier\n"
      "v_mfma_f32_16x16x32_bf16 v[136:139], v[160:163], v[36:39], v[20:23]\n"
      "ds_read_b128 v[36:39], v232 offset:0\n"
      "v_mfma_f32_16x16x32_bf16 v[136:139], v[164:167], v[40:43], v[136:139]\n"
      "ds_read_b128 v[40:43], v233 offset:0\n"
      "v_mfma_f32_16x16x32_bf16 v[136:139], v[180:183], v[44:47], v[136:139]\n"
      "ds_read_b128 v[44:47], v234 offset:0\n"
      "v_mfma_f32_16x16x32_bf16 v[136:139], v[184:187], v[60:63], v[136:139]\n"
      "ds_read_b128 v[60:63], v235 offset:0\n"
      "v_cvt_pk_bf16_f32 v208, v132, v133\n"
      "v_mfma_f32_16x16x32_bf16 v[136:139], v[188:191], v[64:67], v[136:139]\n"
      "ds_read_b128 v[64:67], v232 offset:8192\n"
      "v_cvt_pk_bf16_f32 v209, v134, v135\n"
      "v_mfma_f32_16x16x32_bf16 v[136:139], v[204:207], v[68:71], v[136:139]\n"
      "ds_read_b128 v[68:71], v233 offset:8192\n"
      "v_mfma_f32_16x16x32_bf16 v[136:139], a[24:27], v[84:87], v[136:139]\n"
      "ds_read_b128 v[84:87], v234 offset:8192\n"
      "v_mfma_f32_16x16x32_bf16 v[136:139], a[28:31], v[88:91], v[136:139]\n"
      "ds_read_b128 v[88:91], v235 offset:8192\n"
      "v_mfma_f32_16x16x32_bf16 v[136:139], a[32:35], v[92:95], v[136:139]\n"
      "ds_read_b128 v[92:95], v232 offset:16384\n"
      "v_mfma_f32_16x16x32_bf16 v[136:139], a[36:39], v[108:111], v[136:139]\n"
      "ds_read_b128 v[108:111], v233 offset:16384\n"
      "v_mfma_f32_16x16x32_bf16 v[136:139], a[40:43], v[112:115], v[136:139]\n"
      "ds_read_b128 v[112:115], v234 offset:16384\n"
      "v_mfma_f32_16x16x32_bf16 v[136:139], a[44:47], v[116:119], v[136:139]\n"
      "ds_read_b128 v[116:119], v235 offset:16384\n"
      "s_waitcnt lgkmcnt(8)\n"
      "v_mfma_f32_16x16x32_bf16 v[132:135], a[240:243], v[36:39], v[0:3]\n"
      "ds_write_b64 v244, v[208:209] offset:24704\n"
      "v_mfma_f32_16x16x32_bf16 v[132:135], a[244:247], v[40:43], v[132:135]\n"
      "v_lshlrev_b32 v20, 16, v58\n"
      "v_mfma_f32_16x16x32_bf16 v[132:135], a[248:251], v[44:47], v[132:135]\n"
      "v_and_b32 v21, 0xffff0000, v58\n"
      "v_mfma_f32_16x16x32_bf16 v[132:135], a[252:255], v[60:63], v[132:135]\n"
      "v_lshlrev_b32 v22, 16, v59\n"
      "s_waitcnt lgkmcnt(5)\n"
      "v_mfma_f32_16x16x32_bf16 v[132:135], a[48:51], v[64:67], v[132:135]\n"
      "v_and_b32 v23, 0xffff0000, v59\n"
      "v_mfma_f32_16x16x32_bf16 v[132:135], a[52:55], v[68:71], v[132:135]\n"
      "v_cvt_pk_bf16_f32 v210, v136, v137\n"
      "v_mfma_f32_16x16x32_bf16 v[132:135], a[56:59], v[84:87], v[132:135]\n"
      "v_cvt_pk_bf16_f32 v211, v138, v139\n"
      "v_mfma_f32_16x16x32_bf16 v[132:135], a[60:63], v[88:91], v[132:135]\n"
      "s_add_u32 s54, s44, s33\n"
      "s_addc_u32 s55, s45, 0\n"
      "s_waitcnt lgkmcnt(1)\n"
      "v_mfma_f32_16x16x32_bf16 v[132:135], a[64:67], v[92:95], v[132:135]\n"
      "ds_read_b128 v[216:219], v246 offset:12288\n"
      "v_mfma_f32_16x16x32_bf16 v[132:135], a[68:71], v[108:111], v[132:135]\n"
      "ds_read_b128 v[220:223], v246 offset:13312\n"
      "v_mfma_f32_16x16x32_bf16 v[132:135], a[72:75], v[112:115], v[132:135]\n"
      "ds_read_b128 v[224:227], v246 offset:14336\n"
      "v_mfma_f32_16x16x32_bf16 v[132:135], a[76:79], v[116:119], v[132:135]\n"
      "ds_write_b64 v244, v[210:211] offset:24712\n"
      "v_mfma_f32_16x16x32_bf16 v[136:139], a[80:83], v[36:39], v[4:7]\n"
      "v_mfma_f32_16x16x32_bf16 v[136:139], a[84:87], v[40:43], v[136:139]\n"
      "v_mfma_f32_16x16x32_bf16 v[136:139], a[88:91], v[44:47], v[136:139]\n"
      "v_mfma_f32_16x16x32_bf16 v[136:139], a[92:95], v[60:63], v[136:139]\n"
      "v_cvt_pk_bf16_f32 v208, v132, v133\n"
      "v_mfma_f32_16x16x32_bf16 v[136:139], a[96:99], v[64:67], v[136:139]\n"
      "v_cvt_pk_bf16_f32 v209, v134, v135\n"
      "v_mfma_f32_16x16x32_bf16 v[136:139], a[100:103], v[68:71], v[136:139]\n"
      "s_waitcnt lgkmcnt(3)\n"
      "v_mfma_f32_16x16x32_bf16 v[136:139], a[104:107], v[84:87], v[136:139]\n"
      "global_store_dwordx4 v247, v[216:219], s[54:55] offset:0 sc1\n"
      "v_mfma_f32_16x16x32_bf16 v[136:139], a[108:111], v[88:91], v[136:139]\n"
      "s_add_u32 s52, s42, s35\n"
      "s_addc_u32 s53, s43, 0\n"
      "v_mfma_f32_16x16x32_bf16 v[136:139], a[112:115], v[92:95], v[136:139]\n"
      "s_add_u32 m0, s17, 0x12000\n"
      "v_mfma_f32_16x16x32_bf16 v[136:139], a[116:119], v[108:111], v[136:139]\n"
      "ds_write_b64 v244, v[208:209] offset:0\n"
      "v_mfma_f32_16x16x32_bf16 v[136:139], a[120:123], v[112:115], v[136:139]\n"
      "global_load_lds_dwordx4 v236, s[52:53] offset:0 nt\n"
      "v_mfma_f32_16x16x32_bf16 v[136:139], a[124:127], v[116:119], v[136:139]\n"
      "v_mfma_f32_16x16x32_bf16 v[132:135], a[128:131], v[36:39], v[8:11]\n"
      "v_mfma_f32_16x16x32_bf16 v[132:135], a[132:135], v[40:43], v[132:135]\n"
      "v_mfma_f32_16x16x32_bf16 v[132:135], a[136:139], v[44:47], v[132:135]\n"
      "v_mfma_f32_16x16x32_bf16 v[132:135], a[140:143], v[60:63], v[132:135]\n"
      "v_cvt_pk_bf16_f32 v210, v136, v137\n"
      "v_mfma_f32_16x16x32_bf16 v[132:135], a[144:147], v[64:67], v[132:135]\n"
      "v_cvt_pk_bf16_f32 v211, v138, v139\n"
      "v_mfma_f32_16x16x32_bf16 v[132:135], a[148:151], v[68:71], v[132:135]\n"
      "v_mfma_f32_16x16x32_bf16 v[132:135], a[152:155], v[84:87], v[132:135]\n"
      "s_waitcnt lgkmcnt(3)\n"
      "v_mfma_f32_16x16x32_bf16 v[132:135], a[156:159], v[88:91], v[132:135]\n"
      "global_store_dwordx4 v252, v[220:223], s[54:55] offset:0 sc1\n"
      "v_mfma_f32_16x16x32_bf16 v[132:135], a[160:163], v[92:95], v[132:135]\n"
      "s_add_u32 m0, s17, 0x13f00\n"
      "v_mfma_f32_16x16x32_bf16 v[132:135], a[164:167], v[108:111], v[132:135]\n"
      "ds_write_b64 v244, v[210:211] offset:8\n"
      "v_mfma_f32_16x16x32_bf16 v[132:135], a[168:171], v[112:115], v[132:135]\n"
      "global_load_lds_dwordx4 v236, s[52:53] offset:256 nt\n"
      "v_mfma_f32_16x16x32_bf16 v[132:135], a[172:175], v[116:119], v[132:135]\n"
      "v_mfma_f32_16x16x32_bf16 v[136:139], a[176:179], v[36:39], v[12:15]\n"
      "v_mfma_f32_16x16x32_bf16 v[136:139], a[180:183], v[40:43], v[136:139]\n"
      "v_mfma_f32_16x16x32_bf16 v[136:139], a[184:187], v[44:47], v[136:139]\n"
      "v_mfma_f32_16x16x32_bf16 v[136:139], a[188:191], v[60:63], v[136:139]\n"
      "v_cvt_pk_bf16_f32 v212, v132, v133\n"
      "v_mfma_f32_16x16x32_bf16 v[136:139], a[192:195], v[64:67], v[136:139]\n"
      "v_cvt_pk_bf16_f32 v213, v134, v135\n"
      "v_mfma_f32_16x16x32_bf16 v[136:139], a[196:199], v[68:71], v[136:139]\n"
      "v_mfma_f32_16x16x32_bf16 v[136:139], a[200:203], v[84:87], v[136:139]\n"
      "v_mfma_f32_16x16x32_bf16 v[136:139], a[204:207], v[88:91], v[136:139]\n"
      "s_waitcnt lgkmcnt(3)\n"
      "v_mfma_f32_16x16x32_bf16 v[136:139], a[208:211], v[92:95], v[136:139]\n"
      "global_store_dwordx4 v253, v[224:227], s[54:55] offset:0 sc1\n"
      "v_mfma_f32_16x16x32_bf16 v[136:139], a[212:215], v[108:111], v[136:139]\n"
      "ds_write_b64 v245, v[212:213] offset:0\n"
      "v_mfma_f32_16x16x32_bf16 v[136:139], a[216:219], v[112:115], v[136:139]\n"
      "s_add_u32 m0, s17, 0x15e00\n"
      "v_mfma_f32_16x16x32_bf16 v[136:139], a[220:223], v[116:119], v[136:139]\n"
      "global_load_lds_dwordx4 v236, s[52:53] offset:512 nt\n"
      "v_mfma_f32_16x16x32_bf16 v[132:135], a[224:227], v[36:39], v[16:19]\n"
      "v_mfma_f32_16x16x32_bf16 v[132:135], a[228:231], v[40:43], v[132:135]\n"
      "v_mfma_f32_16x16x32_bf16 v[132:135], a[232:235], v[44:47], v[132:135]\n"
      "v_mfma_f32_16x16x32_bf16 v[132:135], a[236:239], v[60:63], v[132:135]\n"
      "v_cvt_pk_bf16_f32 v214, v136, v137\n"
      "v_mfma_f32_16x16x32_bf16 v[132:135], a[0:3], v[64:67], v[132:135]\n"
      "v_cvt_pk_bf16_f32 v215, v138, v139\n"
      "v_mfma_f32_16x16x32_bf16 v[132:135], a[4:7], v[68:71], v[132:135]\n"
      "v_mfma_f32_16x16x32_bf16 v[132:135], a[8:11], v[84:87], v[132:135]\n"
      "v_mfma_f32_16x16x32_bf16 v[132:135], a[12:15], v[88:91], v[132:135]\n"
      "v_mfma_f32_16x16x32_bf16 v[132:135], a[16:19], v[92:95], v[132:135]\n"
      "v_mfma_f32_16x16x32_bf16 v[132:135], a[20:23], v[108:111], v[132:135]\n"
      "ds_write_b64 v245, v[214:215] offset:8\n"
      "v_mfma_f32_16x16x32_bf16 v[132:135], v[140:143], v[112:115], v[132:135]\n"
      "v_mfma_f32_16x16x32_bf16 v[132:135], v[156:159], v[116:119], v[132:135]\n"
      "s_waitcnt lgkmcnt(4)\n"
      "s_barrier\n"
      "v_mfma_f32_16x16x32_bf16 v[136:139], v[160:163], v[36:39], v[20:23]\n"
      "ds_read_b128 v[36:39], v232 offset:256\n"
      "v_mfma_f32_16x16x32_bf16 v[136:139], v[164:167], v[40:43], v[136:139]\n"
      "ds_read_b128 v[40:43], v233 offset:256\n"
      "v_mfma_f32_16x16x32_bf16 v[136:139], v[180:183], v[44:47], v[136:139]\n"
      "ds_read_b128 v[44:47], v234 offset:256\n"
      "v_mfma_f32_16x16x32_bf16 v[136:139], v[184:187], v[60:63], v[136:139]\n"
      "ds_read_b128 v[60:63], v235 offset:256\n"
      "v_cvt_pk_bf16_f32 v208, v132, v133\n"
      "v_mfma_f32_16x16x32_bf16 v[136:139], v[188:191], v[64:67], v[136:139]\n"
      "ds_read_b128 v[64:67], v232 offset:8448\n"
      "v_cvt_pk_bf16_f32 v209, v134, v135\n"
      "v_mfma_f32_16x16x32_bf16 v[136:139], v[204:207], v[68:71], v[136:139]\n"
      "ds_read_b128 v[68:71], v233 offset:8448\n"
      "v_mfma_f32_16x16x32_bf16 v[136:139], a[24:27], v[84:87], v[136:139]\n"
      "ds_read_b128 v[84:87], v234 offset:8448\n"
      "v_mfma_f32_16x16x32_bf16 v[136:139], a[28:31], v[88:91], v[136:139]\n"
      "ds_read_b128 v[88:91], v235 offset:8448\n"
      "v_mfma_f32_16x16x32_bf16 v[136:139], a[32:35], v[92:95], v[136:139]\n"
      "ds_read_b128 v[92:95], v232 offset:16640\n"
      "v_mfma_f32_16x16x32_bf16 v[136:139], a[36:39], v[108:111], v[136:139]\n"
      "ds_read_b128 v[108:111], v233 offset:16640\n"
      "v_mfma_f32_16x16x32_bf16 v[136:139], a[40:43], v[112:115], v[136:139]\n"
      "ds_read_b128 v[112:115], v234 offset:16640\n"
      "v_mfma_f32_16x16x32_bf16 v[136:139], a[44:47], v[116:119], v[136:139]\n"
      "ds_read_b128 v[116:119], v235 offset:16640\n"
      "s_waitcnt lgkmcnt(8)\n"
      "v_mfma_f32_16x16x32_bf16 v[132:135], a[240:243], v[36:39], v[0:3]\n"
      "ds_write_b64 v244, v[208:209] offset:128\n"
      "v_mfma_f32_16x16x32_bf16 v[132:135], a[244:247], v[40:43], v[132:135]\n"
      "v_mfma_f32_16x16x32_bf16 v[132:135], a[248:251], v[44:47], v[132:135]\n"
      "v_mfma_f32_16x16x32_bf16 v[132:135], a[252:255], v[60:63], v[132:135]\n"
      "v_cvt_pk_bf16_f32 v210, v136, v137\n"
      "s_waitcnt lgkmcnt(5)\n"
      "v_mfma_f32_16x16x32_bf16 v[132:135], a[48:51], v[64:67], v[132:135]\n"
      "v_cvt_pk_bf16_f32 v211, v138, v139\n"
      "v_mfma_f32_16x16x32_bf16 v[132:135], a[52:55], v[68:71], v[132:135]\n"
      "v_mfma_f32_16x16x32_bf16 v[132:135], a[56:59], v[84:87], v[132:135]\n"
      "ds_read_b128 v[216:219], v246 offset:24576\n"
      "v_mfma_f32_16x16x32_bf16 v[132:135], a[60:63], v[88:91], v[132:135]\n"
      "ds_read_b128 v[220:223], v246 offset:25600\n"
      "s_waitcnt lgkmcnt(3)\n"
      "v_mfma_f32_16x16x32_bf16 v[132:135], a[64:67], v[92:95], v[132:135]\n"
      "ds_read_b128 v[224:227], v246 offset:26624\n"
      "v_mfma_f32_16x16x32_bf16 v[132:135], a[68:71], v[108:111], v[132:135]\n"
      "ds_write_b64 v244, v[210:211] offset:136\n"
      "v_mfma_f32_16x16x32_bf16 v[132:135], a[72:75], v[112:115], v[132:135]\n"
      "v_mfma_f32_16x16x32_bf16 v[132:135], a[76:79], v[116:119], v[132:135]\n"
      "v_mfma_f32_16x16x32_bf16 v[136:139], a[80:83], v[36:39], v[4:7]\n"
      "v_mfma_f32_16x16x32_bf16 v[136:139], a[84:87], v[40:43], v[136:139]\n"
      "v_mfma_f32_16x16x32_bf16 v[136:139], a[88:91], v[44:47], v[136:139]\n"
      "v_mfma_f32_16x16x32_bf16 v[136:139], a[92:95], v[60:63], v[136:139]\n"
      "v_cvt_pk_bf16_f32 v208, v132, v133\n"
      "v_mfma_f32_16x16x32_bf16 v[136:139], a[96:99], v[64:67], v[136:139]\n"
      "v_cvt_pk_bf16_f32 v209, v134, v135\n"
      "v_mfma_f32_16x16x32_bf16 v[136:139], a[100:103], v[68:71], v[136:139]\n"
      "s_waitcnt lgkmcnt(3)\n"
      "v_mfma_f32_16x16x32_bf16 v[136:139], a[104:107], v[84:87], v[136:139]\n"
      "global_store_dwordx4 v247, v[216:219], s[54:55] offset:768 sc1\n"
      "v_mfma_f32_16x16x32_bf16 v[136:139], a[108:111], v[88:91], v[136:139]\n"
      "s_add_u32 m0, s17, 0x13000\n"
      "v_mfma_f32_16x16x32_bf16 v[136:139], a[112:115], v[92:95], v[136:139]\n"
      "global_load_lds_dwordx4 v237, s[52:53] offset:0 nt\n"
      "v_mfma_f32_16x16x32_bf16 v[136:139], a[116:119], v[108:111], v[136:139]\n"
      "ds_write_b64 v244, v[208:209] offset:12288\n"
      "v_mfma_f32_16x16x32_bf16 v[136:139], a[120:123], v[112:115], v[136:139]\n"
      "v_mfma_f32_16x16x32_bf16 v[136:139], a[124:127], v[116:119], v[136:139]\n"
      "v_mfma_f32_16x16x32_bf16 v[132:135], a[128:131], v[36:39], v[8:11]\n"
      "v_mfma_f32_16x16x32_bf16 v[132:135], a[132:135], v[40:43], v[132:135]\n"
      "v_mfma_f32_16x16x32_bf16 v[132:135], a[136:139], v[44:47], v[132:135]\n"
      "v_mfma_f32_16x16x32_bf16 v[132:135], a[140:143], v[60:63], v[132:135]\n"
      "v_cvt_pk_bf16_f32 v210, v136, v137\n"
      "v_mfma_f32_16x16x32_bf16 v[132:135], a[144:147], v[64:67], v[132:135]\n"
      "v_cvt_pk_bf16_f32 v211, v138, v139\n"
      "v_mfma_f32_16x16x32_bf16 v[132:135], a[148:151], v[68:71], v[132:135]\n"
      "v_mfma_f32_16x16x32_bf16 v[132:135], a[152:155], v[84:87], v[132:135]\n"
      "s_waitcnt lgkmcnt(3)\n"
      "s_waitcnt vmcnt(18)\n"
      "s_mov_b32 s87, 1\n"
      "s_or_b32 s56, s51, s76\n"
      "s_cmp_lg_u32 s56, 0\n"
      "s_cbranch_scc1 LG1\n"
      "v_cmp_ne_u64 s[58:59], s[80:81], v[24:25]\n"
      "v_cmp_ne_u64 s[56:57], s[80:81], v[26:27]\n"
      "s_mov_b32 s87, 0\n"
      "s_nop 3\n"
      "s_or_b64 s[56:57], s[56:57], s[58:59]\n"
      "s_cbranch_scc1 LG1\n"
      "v_mov_b32 v28, 0\n"
      "v_mov_b32 v30, s80\n"
      "v_mov_b32 v31, s81\n"
      "s_mov_b32 s87, 1\n"
      "s_mov_b64 exec, 1\n"
      "global_store_dwordx2 v28, v[30:31], s[28:29] sc1\n"
      "s_mov_b64 exec, -1\n"
      "s_nop 4\n"
      "LG1:\n"
      "v_mfma_f32_16x16x32_bf16 v[132:135], a[156:159], v[88:91], v[132:135]\n"
      "global_store_dwordx4 v252, v[220:223], s[54:55] offset:768 sc1\n"
      "v_mfma_f32_16x16x32_bf16 v[132:135], a[160:163], v[92:95], v[132:135]\n"
      "s_add_u32 m0, s17, 0x14f00\n"
      "v_mfma_f32_16x16x32_bf16 v[132:135], a[164:167], v[108:111], v[132:135]\n"
      "ds_write_b64 v244, v[210:211] offset:12296\n"
      "v_mfma_f32_16x16x32_bf16 v[132:135], a[168:171], v[112:115], v[132:135]\n"
      "global_load_lds_dwordx4 v237, s[52:53] offset:256 nt\n"
      "v_mfma_f32_16x16x32_bf16 v[132:135], a[172:175], v[116:119], v[132:135]\n"
      "v_mfma_f32_16x16x32_bf16 v[136:139], a[176:179], v[36:39], v[12:15]\n"
      "v_mfma_f32_16x16x32_bf16 v[136:139], a[180:183], v[40:43], v[136:139]\n"
      "v_mfma_f32_16x16x32_bf16 v[136:139], a[184:187], v[44:47], v[136:139]\n"
      "v_mfma_f32_16x16x32_bf16 v[136:139], a[188:191], v[60:63], v[136:139]\n"
      "v_cvt_pk_bf16_f32 v212, v132, v133\n"
      "v_mfma_f32_16x16x32_bf16 v[136:139], a[192:195], v[64:67], v[136:139]\n"
      "v_cvt_pk_bf16_f32 v213, v134, v135\n"
      "v_mfma_f32_16x16x32_bf16 v[136:139], a[196:199], v[68:71], v[136:139]\n"
      "v_mfma_f32_16x16x32_bf16 v[136:139], a[200:203], v[84:87], v[136:139]\n"
      "v_mfma_f32_16x16x32_bf16 v[136:139], a[204:207], v[88:91], v[136:139]\n"
      "s_waitcnt lgkmcnt(3)\n"
      "v_mfma_f32_16x16x32_bf16 v[136:139], a[208:211], v[92:95], v[136:139]\n"
      "global_store_dwordx4 v253, v[224:227], s[54:55] offset:768 sc1\n"
      "v_mfma_f32_16x16x32_bf16 v[136:139], a[212:215], v[108:111], v[136:139]\n"
      "ds_write_b64 v245, v[212:213] offset:12288\n"
      "v_mfma_f32_16x16x32_bf16 v[136:139], a[216:219], v[112:115], v[136:139]\n"
      "s_add_u32 m0, s17, 0x16e00\n"
      "v_mfma_f32_16x16x32_bf16 v[136:139], a[220:223], v[116:119], v[136:139]\n"
      "global_load_lds_dwordx4 v237, s[52:53] offset:512 nt\n"
      "v_mfma_f32_16x16x32_bf16 v[132:135], a[224:227], v[36:39], v[16:19]\n"
      "v_mfma_f32_16x16x32_bf16 v[132:135], a[228:231], v[40:43], v[132:135]\n"
      "v_mfma_f32_16x16x32_bf16 v[132:135], a[232:235], v[44:47], v[132:135]\n"
      "v_mfma_f32_16x16x32_bf16 v[132:135], a[236:239], v[60:63], v[132:135]\n"
      "s_memrealtime s[84:85]\n"
      "v_mfma_f32_16x16x32_bf16 v[132:135], a[0:3], v[64:67], v[132:135]\n"
      "v_cvt_pk_bf16_f32 v214, v136, v137\n"
      "v_mfma_f32_16x16x32_bf16 v[132:135], a[4:7], v[68:71], v[132:135]\n"
      "v_cvt_pk_bf16_f32 v215, v138, v139\n"
      "v_mfma_f32_16x16x32_bf16 v[132:135], a[8:11], v[84:87], v[132:135]\n"
      "v_mfma_f32_16x16x32_bf16 v[132:135], a[12:15], v[88:91], v[132:135]\n"
      "v_mfma_f32_16x16x32_bf16 v[132:135], a[16:19], v[92:95], v[132:135]\n"
      "v_mfma_f32_16x16x32_bf16 v[132:135], a[20:23], v[108:111], v[132:135]\n"
      "v_mfma_f32_16x16x32_bf16 v[132:135], v[140:143], v[112:115], v[132:135]\n"
      "s_waitcnt lgkmcnt(0)\n"
      "s_sub_u32 s48, s84, s48\n"
      "v_mfma_f32_16x16x32_bf16 v[132:135], v[156:159], v[116:119], v[132:135]\n"
      "s_barrier\n"
      "ds_write_b64 v245, v[214:215] offset:12296\n"
      "v_mfma_f32_16x16x32_bf16 v[136:139], v[160:163], v[36:39], v[20:23]\n"
      "ds_read_b128 v[36:39], v232 offset:24576\n"
      "s_lshl_b32 s79, s91, 1\n"
      "v_mfma_f32_16x16x32_bf16 v[136:139], v[164:167], v[40:43], v[136:139]\n"
      "ds_read_b128 v[40:43], v233 offset:24576\n"
      "v_mad_i32_i24 v252, v251, s79, v252\n"
      "v_mfma_f32_16x16x32_bf16 v[136:139], v[180:183], v[44:47], v[136:139]\n"
      "ds_read_b128 v[44:47], v234 offset:24576\n"
      "s_mul_i32 s79, s79, s100\n"
      "v_mfma_f32_16x16x32_bf16 v[136:139], v[184:187], v[60:63], v[136:139]\n"
      "ds_read_b128 v[60:63], v235 offset:24576\n"
      "v_cvt_pk_bf16_f32 v208, v132, v133\n"
      "v_mfma_f32_16x16x32_bf16 v[136:139], v[188:191], v[64:67], v[136:139]\n"
      "ds_read_b128 v[64:67], v232 offset:32768\n"
      "v_cvt_pk_bf16_f32 v209, v134, v135\n"
      "v_mfma_f32_16x16x32_bf16 v[136:139], v[204:207], v[68:71], v[136:139]\n"
      "ds_read_b128 v[68:71], v233 offset:32768\n"
      "v_add_u32 v247, s79, v247\n"
      "v_mfma_f32_16x16x32_bf16 v[136:139], a[24:27], v[84:87], v[136:139]\n"
      "ds_read_b128 v[84:87], v234 offset:32768\n"
      "s_mul_i32 s79, s79, s101\n"
      "v_mfma_f32_16x16x32_bf16 v[136:139], a[28:31], v[88:91], v[136:139]\n"
      "ds_read_b128 v[88:91], v235 offset:32768\n"
      "v_add_u32 v253, s79, v253\n"
      "v_mfma_f32_16x16x32_bf16 v[136:139], a[32:35], v[92:95], v[136:139]\n"
      "ds_read_b128 v[92:95], v232 offset:40960\n"
      "v_mfma_f32_16x16x32_bf16 v[136:139], a[36:39], v[108:111], v[136:139]\n"
      "ds_read_b128 v[108:111], v233 offset:40960\n"
      "v_mfma_f32_16x16x32_bf16 v[136:139], a[40:43], v[112:115], v[136:139]\n"
      "ds_read_b128 v[112:115], v234 offset:40960\n"
      "v_mfma_f32_16x16x32_bf16 v[136:139], a[44:47], v[116:119], v[136:139]\n"
      "ds_read_b128 v[116:119], v235 offset:40960\n"
      "s_waitcnt lgkmcnt(8)\n"
      "v_mfma_f32_16x16x32_bf16 v[132:135], a[240:243], v[36:39], v[0:3]\n"
      "ds_write_b64 v244, v[208:209] offset:12416\n"
      "v_mfma_f32_16x16x32_bf16 v[132:135], a[244:247], v[40:43], v[132:135]\n"
      "s_add_u32 s54, s30, s34\n"
      "s_addc_u32 s55, s31, 0\n"
      "v_mfma_f32_16x16x32_bf16 v[132:135], a[248:251], v[44:47], v[132:135]\n"
      "s_mul_i32 s48, s48, 853\n"
      "v_mfma_f32_16x16x32_bf16 v[132:135], a[252:255], v[60:63], v[132:135]\n"
      "s_lshr_b32 s48, s48, 8\n"
      "v_cvt_pk_bf16_f32 v210, v136, v137\n"
      "s_waitcnt lgkmcnt(5)\n"
      "v_mfma_f32_16x16x32_bf16 v[132:135], a[48:51], v[64:67], v[132:135]\n"
      "s_add_u32 s48, s48, s84\n"
      "v_cvt_pk_bf16_f32 v211, v138, v139\n"
      "v_mfma_f32_16x16x32_bf16 v[132:135], a[52:55], v[68:71], v[132:135]\n"
      "s_xor_b32 s49, s48, s80\n"
      "s_mul_i32 s78, s93, s19\n"
      "v_mfma_f32_16x16x32_bf16 v[132:135], a[56:59], v[84:87], v[132:135]\n"
      "v_mov_b32 v230, s49\n"
      "v_add_u32 v236, s78, v236\n"
      "v_mfma_f32_16x16x32_bf16 v[132:135], a[60:63], v[88:91], v[132:135]\n"
      "s_mul_i32 s49, s51, 0x1100\n"
      "s_mul_i32 s78, s78, s99\n"
      "s_waitcnt lgkmcnt(1)\n"
      "v_mfma_f32_16x16x32_bf16 v[132:135], a[64:67], v[92:95], v[132:135]\n"
      "s_and_b32 s49, s49, 0xf000\n"
      "v_add_u32 v237, s78, v237\n"
      "v_mfma_f32_16x16x32_bf16 v[132:135], a[68:71], v[108:111], v[132:135]\n"
      "s_lshr_b32 s49, s49, 0\n"
      "ds_write_b64 v244, v[210:211] offset:12424\n"
      "v_mfma_f32_16x16x32_bf16 v[132:135], a[72:75], v[112:115], v[132:135]\n"
      "s_add_u32 s49, s49, 1114112\n"
      "v_mfma_f32_16x16x32_bf16 v[132:135], a[76:79], v[116:119], v[132:135]\n"
      "v_mov_b32 v229, s49\n"
      "v_mfma_f32_16x16x32_bf16 v[136:139], a[80:83], v[36:39], v[4:7]\n"
      "s_and_b32 s49, s51, 0x78\n"
      "v_mfma_f32_16x16x32_bf16 v[136:139], a[84:87], v[40:43], v[136:139]\n"
      "s_xor_b32 s49, s49, 0x40\n"
      "v_mfma_f32_16x16x32_bf16 v[136:139], a[88:91], v[44:47], v[136:139]\n"
      "s_or_b32 s49, s49, s76\n"
      "v_mfma_f32_16x16x32_bf16 v[136:139], a[92:95], v[60:63], v[136:139]\n"
      "v_cvt_pk_bf16_f32 v208, v132, v133\n"
      "v_mfma_f32_16x16x32_bf16 v[136:139], a[96:99], v[64:67], v[136:139]\n"
      "s_cmp_lg_u32 s49, 0\n"
      "s_cbranch_scc0 LPS\n"
      "LPR:\n"
      "v_mfma_f32_16x16x32_bf16 v[136:139], a[100:103], v[68:71], v[136:139]\n"
      "v_cvt_pk_bf16_f32 v209, v134, v135\n"
      "v_mfma_f32_16x16x32_bf16 v[136:139], a[104:107], v[84:87], v[136:139]\n"
      "ds_read_b128 v[216:219], v246 offset:0\n"
      "v_mfma_f32_16x16x32_bf16 v[136:139], a[108:111], v[88:91], v[136:139]\n"
      "ds_read_b128 v[220:223], v246 offset:1024\n"
      "v_mfma_f32_16x16x32_bf16 v[136:139], a[112:115], v[92:95], v[136:139]\n"
      "ds_read_b128 v[224:227], v246 offset:2048\n"
      "v_mfma_f32_16x16x32_bf16 v[136:139], a[116:119], v[108:111], v[136:139]\n"
      "s_waitcnt lgkmcnt(2)\n"
      "v_mfma_f32_16x16x32_bf16 v[136:139], a[120:123], v[112:115], v[136:139]\n"
      "ds_write_b64 v244, v[208:209] offset:24576\n"
      "v_mfma_f32_16x16x32_bf16 v[136:139], a[124:127], v[116:119], v[136:139]\n"
      "global_store_dwordx4 v247, v[216:219], s[54:55] offset:0 sc1\n"
      "v_mfma_f32_16x16x32_bf16 v[132:135], a[128:131], v[36:39], v[8:11]\n"
      "s_add_u32 s52, s20, s36\n"
      "s_addc_u32 s53, s21, 0\n"
      "v_mfma_f32_16x16x32_bf16 v[132:135], a[132:135], v[40:43], v[132:135]\n"
      "s_add_u32 m0, s17, 0x0\n"
      "v_mfma_f32_16x16x32_bf16 v[132:135], a[136:139], v[44:47], v[132:135]\n"
      "global_load_lds_dwordx4 v236, s[52:53] offset:0 nt\n"
      "v_mfma_f32_16x16x32_bf16 v[132:135], a[140:143], v[60:63], v[132:135]\n"
      "v_cvt_pk_bf16_f32 v210, v136, v137\n"
      "v_mfma_f32_16x16x32_bf16 v[132:135], a[144:147], v[64:67], v[132:135]\n"
      "v_cvt_pk_bf16_f32 v211, v138, v139\n"
      "v_mfma_f32_16x16x32_bf16 v[132:135], a[148:151], v[68:71], v[132:135]\n"
      "v_mfma_f32_16x16x32_bf16 v[132:135], a[152:155], v[84:87], v[132:135]\n"
      "s_waitcnt lgkmcnt(2)\n"
      "v_mfma_f32_16x16x32_bf16 v[132:135], a[156:159], v[88:91], v[132:135]\n"
      "global_store_dwordx4 v252, v[220:223], s[54:55] offset:0 sc1\n"
      "v_mfma_f32_16x16x32_bf16 v[132:135], a[160:163], v[92:95], v[132:135]\n"
      "v_mfma_f32_16x16x32_bf16 v[132:135], a[164:167], v[108:111], v[132:135]\n"
      "ds_write_b64 v244, v[210:211] offset:24584\n"
      "v_mfma_f32_16x16x32_bf16 v[132:135], a[168:171], v[112:115], v[132:135]\n"
      "v_mfma_f32_16x16x32_bf16 v[132:135], a[172:175], v[116:119], v[132:135]\n"
      "v_mfma_f32_16x16x32_bf16 v[136:139], a[176:179], v[36:39], v[12:15]\n"
      "s_add_u32 m0, s17, 0x1f00\n"
      "v_mfma_f32_16x16x32_bf16 v[136:139], a[180:183], v[40:43], v[136:139]\n"
      "global_load_lds_dwordx4 v236, s[52:53] offset:256 nt\n"
      "v_mfma_f32_16x16x32_bf16 v[136:139], a[184:187], v[44:47], v[136:139]\n"
      "v_mfma_f32_16x16x32_bf16 v[136:139], a[188:191], v[60:63], v[136:139]\n"
      "v_cvt_pk_bf16_f32 v212, v132, v133\n"
      "v_mfma_f32_16x16x32_bf16 v[136:139], a[192:195], v[64:67], v[136:139]\n"
      "v_cvt_pk_bf16_f32 v213, v134, v135\n"
      "v_mfma_f32_16x16x32_bf16 v[136:139], a[196:199], v[68:71], v[136:139]\n"
      "v_mfma_f32_16x16x32_bf16 v[136:139], a[200:203], v[84:87], v[136:139]\n"
      "v_mfma_f32_16x16x32_bf16 v[136:139], a[204:207], v[88:91], v[136:139]\n"
      "s_waitcnt lgkmcnt(2)\n"
      "v_mfma_f32_16x16x32_bf16 v[136:139], a[208:211], v[92:95], v[136:139]\n"
      "global_store_dwordx4 v253, v[224:227], s[54:55] offset:0 sc1\n"
      "v_mfma_f32_16x16x32_bf16 v[136:139], a[212:215], v[108:111], v[136:139]\n"
      "ds_write_b64 v245, v[212:213] offset:24576\n"
      "v_mfma_f32_16x16x32_bf16 v[136:139], a[216:219], v[112:115], v[136:139]\n"
      "v_mfma_f32_16x16x32_bf16 v[136:139], a[220:223], v[116:119], v[136:139]\n"
      "s_add_u32 m0, s17, 0x3e00\n"
      "v_mfma_f32_16x16x32_bf16 v[132:135], a[224:227], v[36:39], v[16:19]\n"
      "global_load_lds_dwordx4 v236, s[52:53] offset:512 nt\n"
      "v_mfma_f32_16x16x32_bf16 v[132:135], a[228:231], v[40:43], v[132:135]\n"
      "v_mfma_f32_16x16x32_bf16 v[132:135], a[232:235], v[44:47], v[132:135]\n"
      "v_mfma_f32_16x16x32_bf16 v[132:135], a[236:239], v[60:63], v[132:135]\n"
      "v_cvt_pk_bf16_f32 v214, v136, v137\n"
      "v_mfma_f32_16x16x32_bf16 v[132:135], a[0:3], v[64:67], v[132:135]\n"
      "v_cvt_pk_bf16_f32 v215, v138, v139\n"
      "v_mfma_f32_16x16x32_bf16 v[132:135], a[4:7], v[68:71], v[132:135]\n"
      "v_mfma_f32_16x16x32_bf16 v[132:135], a[8:11], v[84:87], v[132:135]\n"
      "v_mfma_f32_16x16x32_bf16 v[132:135], a[12:15], v[88:91], v[132:135]\n"
      "v_mfma_f32_16x16x32_bf16 v[132:135], a[16:19], v[92:95], v[132:135]\n"
      "v_mfma_f32_16x16x32_bf16 v[132:135], a[20:23], v[108:111], v[132:135]\n"
      "ds_write_b64 v245, v[214:215] offset:24584\n"
      "v_mfma_f32_16x16x32_bf16 v[132:135], v[140:143], v[112:115], v[132:135]\n"
      "v_mfma_f32_16x16x32_bf16 v[132:135], v[156:159], v[116:119], v[132:135]\n"
      "s_barrier\n"
      "v_mfma_f32_16x16x32_bf16 v[136:139], v[160:163], v[36:39], v[20:23]\n"
      "ds_read_b128 v[36:39], v232 offset:24832\n"
      "v_mfma_f32_16x16x32_bf16 v[136:139], v[164:167], v[40:43], v[136:139]\n"
      "ds_read_b128 v[40:43], v233 offset:24832\n"
      "v_mfma_f32_16x16x32_bf16 v[136:139], v[180:183], v[44:47], v[136:139]\n"
      "ds_read_b128 v[44:47], v234 offset:24832\n"
      "v_mfma_f32_16x16x32_bf16 v[136:139], v[184:187], v[60:63], v[136:139]\n"
      "ds_read_b128 v[60:63], v235 offset:24832\n"
      "v_cvt_pk_bf16_f32 v208, v132, v133\n"
      "v_mfma_f32_16x16x32_bf16 v[136:139], v[188:191], v[64:67], v[136:139]\n"
      "ds_read_b128 v[64:67], v232 offset:33024\n"
      "v_cvt_pk_bf16_f32 v209, v134, v135\n"
      "v_mfma_f32_16x16x32_bf16 v[136:139], v[204:207], v[68:71], v[136:139]\n"
      "ds_read_b128 v[68:71], v233 offset:33024\n"
      "v_mfma_f32_16x16x32_bf16 v[136:139], a[24:27], v[84:87], v[136:139]\n"
      "ds_read_b128 v[84:87], v234 offset:33024\n"
      "v_mfma_f32_16x16x32_bf16 v[136:139], a[28:31], v[88:91], v[136:139]\n"
      "ds_read_b128 v[88:91], v235 offset:33024\n"
      "v_mfma_f32_16x16x32_bf16 v[136:139], a[32:35], v[92:95], v[136:139]\n"
      "LPD10:\n"
      "s_cbranch_execz LPD13\n"
      "ds_read_b128 v[92:95], v232 offset:41216\n"
      "v_mfma_f32_16x16x32_bf16 v[136:139], a[36:39], v[108:111], v[136:139]\n"
      "ds_read_b128 v[108:111], v233 offset:41216\n"
      "ds_write_b64 v244, v[208:209] offset:24704\n"
      "v_mfma_f32_16x16x32_bf16 v[136:139], a[40:43], v[112:115], v[136:139]\n"
      "ds_read_b128 v[112:115], v234 offset:41216\n"
      "v_mfma_f32_16x16x32_bf16 v[136:139], a[44:47], v[116:119], v[136:139]\n"
      "ds_read_b128 v[116:119], v235 offset:41216\n"
      "s_waitcnt lgkmcnt(9)\n"
      "v_mfma_f32_16x16x32_bf16 v[132:135], a[240:243], v[36:39], v[0:3]\n"
      "v_add_u32 v232, 0xc000, v232\n"
      "v_mfma_f32_16x16x32_bf16 v[132:135], a[244:247], v[40:43], v[132:135]\n"
      "v_add_u32 v233, 0xc000, v233\n"
      "v_mfma_f32_16x16x32_bf16 v[132:135], a[248:251], v[44:47], v[132:135]\n"
      "v_add_u32 v234, 0xc000, v234\n"
      "v_mfma_f32_16x16x32_bf16 v[132:135], a[252:255], v[60:63], v[132:135]\n"
      "v_add_u32 v235, 0xc000, v235\n"
      "s_waitcnt lgkmcnt(5)\n"
      "v_mfma_f32_16x16x32_bf16 v[132:135], a[48:51], v[64:67], v[132:135]\n"
      "v_cvt_pk_bf16_f32 v210, v136, v137\n"
      "v_mfma_f32_16x16x32_bf16 v[132:135], a[52:55], v[68:71], v[132:135]\n"
      "v_cvt_pk_bf16_f32 v211, v138, v139\n"
      "v_mfma_f32_16x16x32_bf16 v[132:135], a[56:59], v[84:87], v[132:135]\n"
      "ds_read_b128 v[216:219], v246 offset:12288\n"
      "v_mfma_f32_16x16x32_bf16 v[132:135], a[60:63], v[88:91], v[132:135]\n"
      "ds_read_b128 v[220:223], v246 offset:13312\n"
      "s_waitcnt lgkmcnt(2)\n"
      "v_mfma_f32_16x16x32_bf16 v[132:135], a[64:67], v[92:95], v[132:135]\n"
      "ds_read_b128 v[224:227], v246 offset:14336\n"
      "v_mfma_f32_16x16x32_bf16 v[132:135], a[68:71], v[108:111], v[132:135]\n"
      "v_lshlrev_b32 v0, 16, v72\n"
      "v_mfma_f32_16x16x32_bf16 v[132:135], a[72:75], v[112:115], v[132:135]\n"
      "ds_write_b64 v244, v[210:211] offset:24712\n"
      "v_mfma_f32_16x16x32_bf16 v[132:135], a[76:79], v[116:119], v[132:135]\n"
      "v_and_b32 v1, 0xffff0000, v72\n"
      "v_mfma_f32_16x16x32_bf16 v[136:139], a[80:83], v[36:39], v[4:7]\n"
      "v_lshlrev_b32 v2, 16, v73\n"
      "v_mfma_f32_16x16x32_bf16 v[136:139], a[84:87], v[40:43], v[136:139]\n"
      "v_and_b32 v3, 0xffff0000, v73\n"
      "v_mfma_f32_16x16x32_bf16 v[136:139], a[88:91], v[44:47], v[136:139]\n"
      "v_mfma_f32_16x16x32_bf16 v[136:139], a[92:95], v[60:63], v[136:139]\n"
      "v_cvt_pk_bf16_f32 v208, v132, v133\n"
      "v_mfma_f32_16x16x32_bf16 v[136:139], a[96:99], v[64:67], v[136:139]\n"
      "v_cvt_pk_bf16_f32 v209, v134, v135\n"
      "v_mfma_f32_16x16x32_bf16 v[136:139], a[100:103], v[68:71], v[136:139]\n"
      "s_waitcnt lgkmcnt(3)\n"
      "v_mfma_f32_16x16x32_bf16 v[136:139], a[104:107], v[84:87], v[136:139]\n"
      "global_store_dwordx4 v247, v[216:219], s[54:55] offset:768 sc1\n"
      "v_mfma_f32_16x16x32_bf16 v[136:139], a[108:111], v[88:91], v[136:139]\n"
      "s_add_u32 m0, s17, 0x1000\n"
      "v_mfma_f32_16x16x32_bf16 v[136:139], a[112:115], v[92:95], v[136:139]\n"
      "global_load_lds_dwordx4 v237, s[52:53] offset:0 nt\n"
      "v_mfma_f32_16x16x32_bf16 v[136:139], a[116:119], v[108:111], v[136:139]\n"
      "ds_write_b64 v244, v[208:209] offset:0\n"
      "v_mfma_f32_16x16x32_bf16 v[136:139], a[120:123], v[112:115], v[136:139]\n"
      "v_lshlrev_b32 v4, 16, v74\n"
      "v_mfma_f32_16x16x32_bf16 v[136:139], a[124:127], v[116:119], v[136:139]\n"
      "v_and_b32 v5, 0xffff0000, v74\n"
      "v_mfma_f32_16x16x32_bf16 v[132:135], a[128:131], v[36:39], v[8:11]\n"
      "v_lshlrev_b32 v6, 16, v75\n"
      "v_mfma_f32_16x16x32_bf16 v[132:135], a[132:135], v[40:43], v[132:135]\n"
      "v_and_b32 v7, 0xffff0000, v75\n"
      "v_mfma_f32_16x16x32_bf16 v[132:135], a[136:139], v[44:47], v[132:135]\n"
      "v_mfma_f32_16x16x32_bf16 v[132:135], a[140:143], v[60:63], v[132:135]\n"
      "v_cvt_pk_bf16_f32 v210, v136, v137\n"
      "v_mfma_f32_16x16x32_bf16 v[132:135], a[144:147], v[64:67], v[132:135]\n"
      "v_cvt_pk_bf16_f32 v211, v138, v139\n"
      "v_mfma_f32_16x16x32_bf16 v[132:135], a[148:151], v[68:71], v[132:135]\n"
      "v_lshlrev_b32 v8, 16, v76\n"
      "v_mfma_f32_16x16x32_bf16 v[132:135], a[152:155], v[84:87], v[132:135]\n"
      "s_waitcnt lgkmcnt(3)\n"
      "v_mfma_f32_16x16x32_bf16 v[132:135], a[156:159], v[88:91], v[132:135]\n"
      "global_store_dwordx4 v252, v[220:223], s[54:55] offset:768 sc1\n"
      "v_mfma_f32_16x16x32_bf16 v[132:135], a[160:163], v[92:95], v[132:135]\n"
      "s_add_u32 m0, s17, 0x2f00\n"
      "v_mfma_f32_16x16x32_bf16 v[132:135], a[164:167], v[108:111], v[132:135]\n"
      "ds_write_b64 v244, v[210:211] offset:8\n"
      "v_mfma_f32_16x16x32_bf16 v[132:135], a[168:171], v[112:115], v[132:135]\n"
      "global_load_lds_dwordx4 v237, s[52:53] offset:256 nt\n"
      "v_mfma_f32_16x16x32_bf16 v[132:135], a[172:175], v[116:119], v[132:135]\n"
      "v_and_b32 v9, 0xffff0000, v76\n"
      "v_mfma_f32_16x16x32_bf16 v[136:139], a[176:179], v[36:39], v[12:15]\n"
      "v_lshlrev_b32 v10, 16, v77\n"
      "v_mfma_f32_16x16x32_bf16 v[136:139], a[180:183], v[40:43], v[136:139]\n"
      "v_and_b32 v11, 0xffff0000, v77\n"
      "v_mfma_f32_16x16x32_bf16 v[136:139], a[184:187], v[44:47], v[136:139]\n"
      "v_mfma_f32_16x16x32_bf16 v[136:139], a[188:191], v[60:63], v[136:139]\n"
      "v_cvt_pk_bf16_f32 v212, v132, v133\n"
      "v_mfma_f32_16x16x32_bf16 v[136:139], a[192:195], v[64:67], v[136:139]\n"
      "v_cvt_pk_bf16_f32 v213, v134, v135\n"
      "v_mfma_f32_16x16x32_bf16 v[136:139], a[196:199], v[68:71], v[136:139]\n"
      "v_lshlrev_b32 v12, 16, v78\n"
      "v_mfma_f32_16x16x32_bf16 v[136:139], a[200:203], v[84:87], v[136:139]\n"
      "v_and_b32 v13, 0xffff0000, v78\n"
      "v_mfma_f32_16x16x32_bf16 v[136:139], a[204:207], v[88:91], v[136:139]\n"
      "s_waitcnt lgkmcnt(3)\n"
      "v_mfma_f32_16x16x32_bf16 v[136:139], a[208:211], v[92:95], v[136:139]\n"
      "global_store_dwordx4 v253, v[224:227], s[54:55] offset:768 sc1\n"
      "v_mfma_f32_16x16x32_bf16 v[136:139], a[212:215], v[108:111], v[136:139]\n"
      "ds_write_b64 v245, v[212:213] offset:0\n"
      "v_mfma_f32_16x16x32_bf16 v[136:139], a[216:219], v[112:115], v[136:139]\n"
      "s_add_u32 m0, s17, 0x4e00\n"
      "v_mfma_f32_16x16x32_bf16 v[136:139], a[220:223], v[116:119], v[136:139]\n"
      "global_load_lds_dwordx4 v237, s[52:53] offset:512 nt\n"
      "v_mfma_f32_16x16x32_bf16 v[132:135], a[224:227], v[36:39], v[16:19]\n"
      "v_lshlrev_b32 v14, 16, v79\n"
      "v_mfma_f32_16x16x32_bf16 v[132:135], a[228:231], v[40:43], v[132:135]\n"
      "v_and_b32 v15, 0xffff0000, v79\n"
      "v_mfma_f32_16x16x32_bf16 v[132:135], a[232:235], v[44:47], v[132:135]\n"
      "v_mfma_f32_16x16x32_bf16 v[132:135], a[236:239], v[60:63], v[132:135]\n"
      "v_cvt_pk_bf16_f32 v214, v136, v137\n"
      "v_mfma_f32_16x16x32_bf16 v[132:135], a[0:3], v[64:67], v[132:135]\n"
      "v_cvt_pk_bf16_f32 v215, v138, v139\n"
      "v_mfma_f32_16x16x32_bf16 v[132:135], a[4:7], v[68:71], v[132:135]\n"
      "v_lshlrev_b32 v16, 16, v80\n"
      "v_mfma_f32_16x16x32_bf16 v[132:135], a[8:11], v[84:87], v[132:135]\n"
      "v_and_b32 v17, 0xffff0000, v80\n"
      "v_mfma_f32_16x16x32_bf16 v[132:135], a[12:15], v[88:91], v[132:135]\n"
      "v_lshlrev_b32 v18, 16, v81\n"
      "v_mfma_f32_16x16x32_bf16 v[132:135], a[16:19], v[92:95], v[132:135]\n"
      "v_and_b32 v19, 0xffff0000, v81\n"
      "v_mfma_f32_16x16x32_bf16 v[132:135], a[20:23], v[108:111], v[132:135]\n"
      "ds_write_b64 v245, v[214:215] offset:8\n"
      "v_mfma_f32_16x16x32_bf16 v[132:135], v[140:143], v[112:115], v[132:135]\n"
      "v_mfma_f32_16x16x32_bf16 v[132:135], v[156:159], v[116:119], v[132:135]\n"
      "s_waitcnt vmcnt(24) lgkmcnt(4)\n"
      "s_barrier\n"
      "v_mfma_f32_16x16x32_bf16 v[136:139], v[160:163], v[36:39], v[20:23]\n"
      "ds_read_b128 v[36:39], v232 offset:0\n"
      "v_mfma_f32_16x16x32_bf16 v[136:139], v[164:167], v[40:43], v[136:139]\n"
      "ds_read_b128 v[40:43], v233 offset:0\n"
      "v_mfma_f32_16x16x32_bf16 v[136:139], v[180:183], v[44:47], v[136:139]\n"
      "ds_read_b128 v[44:47], v234 offset:0\n"
      "v_mfma_f32_16x16x32_bf16 v[136:139], v[184:187], v[60:63], v[136:139]\n"
      "ds_read_b128 v[60:63], v235 offset:0\n"
      "v_cvt_pk_bf16_f32 v208, v132, v133\n"
      "v_mfma_f32_16x16x32_bf16 v[136:139], v[188:191], v[64:67], v[136:139]\n"
      "ds_read_b128 v[64:67], v232 offset:8192\n"
      "v_cvt_pk_bf16_f32 v209, v134, v135\n"
      "v_mfma_f32_16x16x32_bf16 v[136:139], v[204:207], v[68:71], v[136:139]\n"
      "ds_read_b128 v[68:71], v233 offset:8192\n"
      "v_mfma_f32_16x16x32_bf16 v[136:139], a[24:27], v[84:87], v[136:139]\n"
      "ds_read_b128 v[84:87], v234 offset:8192\n"
      "v_mfma_f32_16x16x32_bf16 v[136:139], a[28:31], v[88:91], v[136:139]\n"
      "ds_read_b128 v[88:91], v235 offset:8192\n"
      "v_mfma_f32_16x16x32_bf16 v[136:139], a[32:35], v[92:95], v[136:139]\n"
      "ds_read_b128 v[92:95], v232 offset:16384\n"
      "v_mfma_f32_16x16x32_bf16 v[136:139], a[36:39], v[108:111], v[136:139]\n"
      "ds_read_b128 v[108:111], v233 offset:16384\n"
      "v_mfma_f32_16x16x32_bf16 v[136:139], a[40:43], v[112:115], v[136:139]\n"
      "ds_read_b128 v[112:115], v234 offset:16384\n"
      "v_mfma_f32_16x16x32_bf16 v[136:139], a[44:47], v[116:119], v[136:139]\n"
      "ds_read_b128 v[116:119], v235 offset:16384\n"
      "s_waitcnt lgkmcnt(8)\n"
      "v_mfma_f32_16x16x32_bf16 v[132:135], a[240:243], v[36:39], v[0:3]\n"
      "ds_write_b64 v244, v[208:209] offset:128\n"
      "v_mfma_f32_16x16x32_bf16 v[132:135], a[244:247], v[40:43], v[132:135]\n"
      "v_lshlrev_b32 v20, 16, v82\n"
      "v_mfma_f32_16x16x32_bf16 v[132:135], a[248:251], v[44:47], v[132:135]\n"
      "v_and_b32 v21, 0xffff0000, v82\n"
      "v_mfma_f32_16x16x32_bf16 v[132:135], a[252:255], v[60:63], v[132:135]\n"
      "v_lshlrev_b32 v22, 16, v83\n"
      "s_waitcnt lgkmcnt(5)\n"
      "v_mfma_f32_16x16x32_bf16 v[132:135], a[48:51], v[64:67], v[132:135]\n"
      "v_and_b32 v23, 0xffff0000, v83\n"
      "v_mfma_f32_16x16x32_bf16 v[132:135], a[52:55], v[68:71], v[132:135]\n"
      "v_cvt_pk_bf16_f32 v210, v136, v137\n"
      "v_mfma_f32_16x16x32_bf16 v[132:135], a[56:59], v[84:87], v[132:135]\n"
      "v_cvt_pk_bf16_f32 v211, v138, v139\n"
      "v_mfma_f32_16x16x32_bf16 v[132:135], a[60:63], v[88:91], v[132:135]\n"
      "s_add_u32 s54, s44, s34\n"
      "s_addc_u32 s55, s45, 0\n"
      "s_waitcnt lgkmcnt(1)\n"
      "v_mfma_f32_16x16x32_bf16 v[132:135], a[64:67], v[92:95], v[132:135]\n"
      "ds_read_b128 v[216:219], v246 offset:24576\n"
      "v_mfma_f32_16x16x32_bf16 v[132:135], a[68:71], v[108:111], v[132:135]\n"
      "ds_read_b128 v[220:223], v246 offset:25600\n"
      "v_mfma_f32_16x16x32_bf16 v[132:135], a[72:75], v[112:115], v[132:135]\n"
      "ds_read_b128 v[224:227], v246 offset:26624\n"
      "v_mfma_f32_16x16x32_bf16 v[132:135], a[76:79], v[116:119], v[132:135]\n"
      "ds_write_b64 v244, v[210:211] offset:136\n"
      "v_mfma_f32_16x16x32_bf16 v[136:139], a[80:83], v[36:39], v[4:7]\n"
      "v_mfma_f32_16x16x32_bf16 v[136:139], a[84:87], v[40:43], v[136:139]\n"
      "v_mfma_f32_16x16x32_bf16 v[136:139], a[88:91], v[44:47], v[136:139]\n"
      "v_mfma_f32_16x16x32_bf16 v[136:139], a[92:95], v[60:63], v[136:139]\n"
      "v_cvt_pk_bf16_f32 v208, v132, v133\n"
      "v_mfma_f32_16x16x32_bf16 v[136:139], a[96:99], v[64:67], v[136:139]\n"
      "v_cvt_pk_bf16_f32 v209, v134, v135\n"
      "v_mfma_f32_16x16x32_bf16 v[136:139], a[100:103], v[68:71], v[136:139]\n"
      "s_waitcnt lgkmcnt(3)\n"
      "v_mfma_f32_16x16x32_bf16 v[136:139], a[104:107], v[84:87], v[136:139]\n"
      "global_store_dwordx4 v247, v[216:219], s[54:55] offset:0 sc1\n"
      "v_mfma_f32_16x16x32_bf16 v[136:139], a[108:111], v[88:91], v[136:139]\n"
      "s_add_u32 s52, s42, s36\n"
      "s_addc_u32 s53, s43, 0\n"
      "v_mfma_f32_16x16x32_bf16 v[136:139], a[112:115], v[92:95], v[136:139]\n"
      "s_add_u32 m0, s17, 0x6000\n"
      "v_mfma_f32_16x16x32_bf16 v[136:139], a[116:119], v[108:111], v[136:139]\n"
      "ds_write_b64 v244, v[208:209] offset:12288\n"
      "v_mfma_f32_16x16x32_bf16 v[136:139], a[120:123], v[112:115], v[136:139]\n"
      "global_load_lds_dwordx4 v236, s[52:53] offset:0 nt\n"
      "v_mfma_f32_16x16x32_bf16 v[136:139], a[124:127], v[116:119], v[136:139]\n"
      "v_mfma_f32_16x16x32_bf16 v[132:135], a[128:131], v[36:39], v[8:11]\n"
      "v_mfma_f32_16x16x32_bf16 v[132:135], a[132:135], v[40:43], v[132:135]\n"
      "v_mfma_f32_16x16x32_bf16 v[132:135], a[136:139], v[44:47], v[132:135]\n"
      "v_mfma_f32_16x16x32_bf16 v[132:135], a[140:143], v[60:63], v[132:135]\n"
      "v_cvt_pk_bf16_f32 v210, v136, v137\n"
      "v_mfma_f32_16x16x32_bf16 v[132:135], a[144:147], v[64:67], v[132:135]\n"
      "v_cvt_pk_bf16_f32 v211, v138, v139\n"
      "v_mfma_f32_16x16x32_bf16 v[132:135], a[148:151], v[68:71], v[132:135]\n"
      "v_mfma_f32_16x16x32_bf16 v[132:135], a[152:155], v[84:87], v[132:135]\n"
      "s_waitcnt lgkmcnt(3)\n"
      "v_mfma_f32_16x16x32_bf16 v[132:135], a[156:159], v[88:91], v[132:135]\n"
      "global_store_dwordx4 v252, v[220:223], s[54:55] offset:0 sc1\n"
      "v_mfma_f32_16x16x32_bf16 v[132:135], a[160:163], v[92:95], v[132:135]\n"
      "s_add_u32 m0, s17, 0x7f00\n"
      "v_mfma_f32_16x16x32_bf16 v[132:135], a[164:167], v[108:111], v[132:135]\n"
      "ds_write_b64 v244, v[210:211] offset:12296\n"
      "v_mfma_f32_16x16x32_bf16 v[132:135], a[168:171], v[112:115], v[132:135]\n"
      "global_load_lds_dwordx4 v236, s[52:53] offset:256 nt\n"
      "v_mfma_f32_16x16x32_bf16 v[132:135], a[172:175], v[116:119], v[132:135]\n"
      "v_mfma_f32_16x16x32_bf16 v[136:139], a[176:179], v[36:39], v[12:15]\n"
      "v_mfma_f32_16x16x32_bf16 v[136:139], a[180:183], v[40:43], v[136:139]\n"
      "v_mfma_f32_16x16x32_bf16 v[136:139], a[184:187], v[44:47], v[136:139]\n"
      "v_mfma_f32_16x16x32_bf16 v[136:139], a[188:191], v[60:63], v[136:139]\n"
      "v_cvt_pk_bf16_f32 v212, v132, v133\n"
      "v_mfma_f32_16x16x32_bf16 v[136:139], a[192:195], v[64:67], v[136:139]\n"
      "v_cvt_pk_bf16_f32 v213, v134, v135\n"
      "v_mfma_f32_16x16x32_bf16 v[136:139], a[196:199], v[68:71], v[136:139]\n"
      "v_mfma_f32_16x16x32_bf16 v[136:139], a[200:203], v[84:87], v[136:139]\n"
      "v_mfma_f32_16x16x32_bf16 v[136:139], a[204:207], v[88:91], v[136:139]\n"
      "s_waitcnt lgkmcnt(3)\n"
      "v_mfma_f32_16x16x32_bf16 v[136:139], a[208:211], v[92:95], v[136:139]\n"
      "global_store_dwordx4 v253, v[224:227], s[54:55] offset:0 sc1\n"
      "v_mfma_f32_16x16x32_bf16 v[136:139], a[212:215], v[108:111], v[136:139]\n"
      "ds_write_b64 v245, v[212:213] offset:12288\n"
      "v_mfma_f32_16x16x32_bf16 v[136:139], a[216:219], v[112:115], v[136:139]\n"
      "s_add_u32 m0, s17, 0x9e00\n"
      "v_mfma_f32_16x16x32_bf16 v[136:139], a[220:223], v[116:119], v[136:139]\n"
      "global_load_lds_dwordx4 v236, s[52:53] offset:512 nt\n"
      "v_mfma_f32_16x16x32_bf16 v[132:135], a[224:227], v[36:39], v[16:19]\n"
      "v_mfma_f32_16x16x32_bf16 v[132:135], a[228:231], v[40:43], v[132:135]\n"
      "v_mfma_f32_16x16x32_bf16 v[132:135], a[232:235], v[44:47], v[132:135]\n"
      "v_mfma_f32_16x16x32_bf16 v[132:135], a[236:239], v[60:63], v[132:135]\n"
      "v_cvt_pk_bf16_f32 v214, v136, v137\n"
      "v_mfma_f32_16x16x32_bf16 v[132:135], a[0:3], v[64:67], v[132:135]\n"
      "v_cvt_pk_bf16_f32 v215, v138, v139\n"
      "v_mfma_f32_16x16x32_bf16 v[132:135], a[4:7], v[68:71], v[132:135]\n"
      "v_mfma_f32_16x16x32_bf16 v[132:135], a[8:11], v[84:87], v[132:135]\n"
      "v_mfma_f32_16x16x32_bf16 v[132:135], a[12:15], v[88:91], v[132:135]\n"
      "v_mfma_f32_16x16x32_bf16 v[132:135], a[16:19], v[92:95], v[132:135]\n"
      "v_mfma_f32_16x16x32_bf16 v[132:135], a[20:23], v[108:111], v[132:135]\n"
      "ds_write_b64 v245, v[214:215] offset:12296\n"
      "v_mfma_f32_16x16x32_bf16 v[132:135], v[140:143], v[112:115], v[132:135]\n"
      "v_mfma_f32_16x16x32_bf16 v[132:135], v[156:159], v[116:119], v[132:135]\n"
      "s_waitcnt lgkmcnt(4)\n"
      "s_barrier\n"
      "v_mfma_f32_16x16x32_bf16 v[136:139], v[160:163], v[36:39], v[20:23]\n"
      "ds_read_b128 v[36:39], v232 offset:256\n"
      "v_mfma_f32_16x16x32_bf16 v[136:139], v[164:167], v[40:43], v[136:139]\n"
      "ds_read_b128 v[40:43], v233 offset:256\n"
      "v_mfma_f32_16x16x32_bf16 v[136:139], v[180:183], v[44:47], v[136:139]\n"
      "ds_read_b128 v[44:47], v234 offset:256\n"
      "v_mfma_f32_16x16x32_bf16 v[136:139], v[184:187], v[60:63], v[136:139]\n"
      "ds_read_b128 v[60:63], v235 offset:256\n"
      "v_cvt_pk_bf16_f32 v208, v132, v133\n"
      "v_mfma_f32_16x16x32_bf16 v[136:139], v[188:191], v[64:67], v[136:139]\n"
      "ds_read_b128 v[64:67], v232 offset:8448\n"
      "v_cvt_pk_bf16_f32 v209, v134, v135\n"
      "v_mfma_f32_16x16x32_bf16 v[136:139], v[204:207], v[68:71], v[136:139]\n"
      "ds_read_b128 v[68:71], v233 offset:8448\n"
      "v_mfma_f32_16x16x32_bf16 v[136:139], a[24:27], v[84:87], v[136:139]\n"
      "ds_read_b128 v[84:87], v234 offset:8448\n"
      "v_mfma_f32_16x16x32_bf16 v[136:139], a[28:31], v[88:91], v[136:139]\n"
      "ds_read_b128 v[88:91], v235 offset:8448\n"
      "v_mfma_f32_16x16x32_bf16 v[136:139], a[32:35], v[92:95], v[136:139]\n"
      "ds_read_b128 v[92:95], v232 offset:16640\n"
      "v_mfma_f32_16x16x32_bf16 v[136:139], a[36:39], v[108:111], v[136:139]\n"
      "ds_read_b128 v[108:111], v233 offset:16640\n"
      "v_mfma_f32_16x16x32_bf16 v[136:139], a[40:43], v[112:115], v[136:139]\n"
      "ds_read_b128 v[112:115], v234 offset:16640\n"
      "v_mfma_f32_16x16x32_bf16 v[136:139], a[44:47], v[116:119], v[136:139]\n"
      "ds_read_b128 v[116:119], v235 offset:16640\n"
      "s_waitcnt lgkmcnt(8)\n"
      "v_mfma_f32_16x16x32_bf16 v[132:135], a[240:243], v[36:39], v[0:3]\n"
      "ds_write_b64 v244, v[208:209] offset:12416\n"
      "v_mfma_f32_16x16x32_bf16 v[132:135], a[244:247], v[40:43], v[132:135]\n"
      "v_mfma_f32_16x16x32_bf16 v[132:135], a[248:251], v[44:47], v[132:135]\n"
      "v_mfma_f32_16x16x32_bf16 v[132:135], a[252:255], v[60:63], v[132:135]\n"
      "v_cvt_pk_bf16_f32 v210, v136, v137\n"
      "s_waitcnt lgkmcnt(5)\n"
      "v_mfma_f32_16x16x32_bf16 v[132:135], a[48:51], v[64:67], v[132:135]\n"
      "v_lshlrev_b32 v229, 8, v239\n"
      "v_mfma_f32_16x16x32_bf16 v[132:135], a[52:55], v[68:71], v[132:135]\n"
      "v_add_u32 v229, 1114112, v229\n"
      "v_mfma_f32_16x16x32_bf16 v[132:135], a[56:59], v[84:87], v[132:135]\n"
      "v_cvt_pk_bf16_f32 v211, v138, v139\n"
      "v_mfma_f32_16x16x32_bf16 v[132:135], a[60:63], v[88:91], v[132:135]\n"
      "s_mov_b64 exec, 0xffff\n"
      "global_load_dword v228, v229, s[28:29] sc1\n"
      "s_mov_b64 exec, -1\n"
      "s_nop 1\n"
      "s_waitcnt lgkmcnt(1)\n"
      "v_mfma_f32_16x16x32_bf16 v[132:135], a[64:67], v[92:95], v[132:135]\n"
      "ds_read_b128 v[216:219], v246 offset:0\n"
      "v_mfma_f32_16x16x32_bf16 v[132:135], a[68:71], v[108:111], v[132:135]\n"
      "ds_read_b128 v[220:223], v246 offset:1024\n"
      "v_mfma_f32_16x16x32_bf16 v[132:135], a[72:75], v[112:115], v[132:135]\n"
      "ds_read_b128 v[224:227], v246 offset:2048\n"
      "v_mfma_f32_16x16x32_bf16 v[132:135], a[76:79], v[116:119], v[132:135]\n"
      "ds_write_b64 v244, v[210:211] offset:12424\n"
      "v_mfma_f32_16x16x32_bf16 v[136:139], a[80:83], v[36:39], v[4:7]\n"
      "v_mfma_f32_16x16x32_bf16 v[136:139], a[84:87], v[40:43], v[136:139]\n"
      "v_mfma_f32_16x16x32_bf16 v[136:139], a[88:91], v[44:47], v[136:139]\n"
      "v_mfma_f32_16x16x32_bf16 v[136:139], a[92:95], v[60:63], v[136:139]\n"
      "v_cvt_pk_bf16_f32 v208, v132, v133\n"
      "v_mfma_f32_16x16x32_bf16 v[136:139], a[96:99], v[64:67], v[136:139]\n"
      "v_cvt_pk_bf16_f32 v209, v134, v135\n"
      "v_mfma_f32_16x16x32_bf16 v[136:139], a[100:103], v[68:71], v[136:139]\n"
      "s_waitcnt lgkmcnt(3)\n"
      "v_mfma_f32_16x16x32_bf16 v[136:139], a[104:107], v[84:87], v[136:139]\n"
      "global_store_dwordx4 v247, v[216:219], s[54:55] offset:768 sc1\n"
      "v_mfma_f32_16x16x32_bf16 v[136:139], a[108:111], v[88:91], v[136:139]\n"
      "s_add_u32 m0, s17, 0x7000\n"
      "v_mfma_f32_16x16x32_bf16 v[136:139], a[112:115], v[92:95], v[136:139]\n"
      "global_load_lds_dwordx4 v237, s[52:53] offset:0 nt\n"
      "v_mfma_f32_16x16x32_bf16 v[136:139], a[116:119], v[108:111], v[136:139]\n"
      "ds_write_b64 v244, v[208:209] offset:24576\n"
      "v_mfma_f32_16x16x32_bf16 v[136:139], a[120:123], v[112:115], v[136:139]\n"
      "v_mfma_f32_16x16x32_bf16 v[136:139], a[124:127], v[116:119], v[136:139]\n"
      "v_mfma_f32_16x16x32_bf16 v[132:135], a[128:131], v[36:39], v[8:11]\n"
      "v_mfma_f32_16x16x32_bf16 v[132:135], a[132:135], v[40:43], v[132:135]\n"
      "v_mfma_f32_16x16x32_bf16 v[132:135], a[136:139], v[44:47], v[132:135]\n"
      "v_mfma_f32_16x16x32_bf16 v[132:135], a[140:143], v[60:63], v[132:135]\n"
      "v_cvt_pk_bf16_f32 v210, v136, v137\n"
      "v_mfma_f32_16x16x32_bf16 v[132:135], a[144:147], v[64:67], v[132:135]\n"
      "v_cvt_pk_bf16_f32 v211, v138, v139\n"
      "v_mfma_f32_16x16x32_bf16 v[132:135], a[148:151], v[68:71], v[132:135]\n"
      "v_mfma_f32_16x16x32_bf16 v[132:135], a[152:155], v[84:87], v[132:135]\n"
      "s_waitcnt lgkmcnt(3)\n"
      "v_mfma_f32_16x16x32_bf16 v[132:135], a[156:159], v[88:91], v[132:135]\n"
      "global_store_dwordx4 v252, v[220:223], s[54:55] offset:768 sc1\n"
      "v_mfma_f32_16x16x32_bf16 v[132:135], a[160:163], v[92:95], v[132:135]\n"
      "s_add_u32 m0, s17, 0x8f00\n"
      "v_mfma_f32_16x16x32_bf16 v[132:135], a[164:167], v[108:111], v[132:135]\n"
      "ds_write_b64 v244, v[210:211] offset:24584\n"
      "v_mfma_f32_16x16x32_bf16 v[132:135], a[168:171], v[112:115], v[132:135]\n"
      "global_load_lds_dwordx4 v237, s[52:53] offset:256 nt\n"
      "v_mfma_f32_16x16x32_bf16 v[132:135], a[172:175], v[116:119], v[132:135]\n"
      "v_mfma_f32_16x16x32_bf16 v[136:139], a[176:179], v[36:39], v[12:15]\n"
      "v_mfma_f32_16x16x32_bf16 v[136:139], a[180:183], v[40:43], v[136:139]\n"
      "v_mfma_f32_16x16x32_bf16 v[136:139], a[184:187], v[44:47], v[136:139]\n"
      "v_mfma_f32_16x16x32_bf16 v[136:139], a[188:191], v[60:63], v[136:139]\n"
      "v_cvt_pk_bf16_f32 v212, v132, v133\n"
      "v_mfma_f32_16x16x32_bf16 v[136:139], a[192:195], v[64:67], v[136:139]\n"
      "v_cvt_pk_bf16_f32 v213, v134, v135\n"
      "v_mfma_f32_16x16x32_bf16 v[136:139], a[196:199], v[68:71], v[136:139]\n"
      "v_mfma_f32_16x16x32_bf16 v[136:139], a[200:203], v[84:87], v[136:139]\n"
      "v_mfma_f32_16x16x32_bf16 v[136:139], a[204:207], v[88:91], v[136:139]\n"
      "s_waitcnt lgkmcnt(3)\n"
      "v_mfma_f32_16x16x32_bf16 v[136:139], a[208:211], v[92:95], v[136:139]\n"
      "global_store_dwordx4 v253, v[224:227], s[54:55] offset:768 sc1\n"
      "v_mfma_f32_16x16x32_bf16 v[136:139], a[212:215], v[108:111], v[136:139]\n"
      "ds_write_b64 v245, v[212:213] offset:24576\n"
      "v_mfma_f32_16x16x32_bf16 v[136:139], a[216:219], v[112:115], v[136:139]\n"
      "s_add_u32 m0, s17, 0xae00\n"
      "v_mfma_f32_16x16x32_bf16 v[136:139], a[220:223], v[116:119], v[136:139]\n"
      "global_load_lds_dwordx4 v237, s[52:53] offset:512 nt\n"
      "v_mfma_f32_16x16x32_bf16 v[132:135], a[224:227], v[36:39], v[16:19]\n"
      "v_mfma_f32_16x16x32_bf16 v[132:135], a[228:231], v[40:43], v[132:135]\n"
      "v_mfma_f32_16x16x32_bf16 v[132:135], a[232:235], v[44:47], v[132:135]\n"
      "v_mfma_f32_16x16x32_bf16 v[132:135], a[236:239], v[60:63], v[132:135]\n"
      "v_cvt_pk_bf16_f32 v214, v136, v137\n"
      "v_mfma_f32_16x16x32_bf16 v[132:135], a[0:3], v[64:67], v[132:135]\n"
      "v_cvt_pk_bf16_f32 v215, v138, v139\n"
      "v_mfma_f32_16x16x32_bf16 v[132:135], a[4:7], v[68:71], v[132:135]\n"
      "v_mfma_f32_16x16x32_bf16 v[132:135], a[8:11], v[84:87], v[132:135]\n"
      "v_mfma_f32_16x16x32_bf16 v[132:135], a[12:15], v[88:91], v[132:135]\n"
      "v_mfma_f32_16x16x32_bf16 v[132:135], a[16:19], v[92:95], v[132:135]\n"
      "v_mfma_f32_16x16x32_bf16 v[132:135], a[20:23], v[108:111], v[132:135]\n"
      "ds_write_b64 v245, v[214:215] offset:24584\n"
      "v_mfma_f32_16x16x32_bf16 v[132:135], v[140:143], v[112:115], v[132:135]\n"
      "s_lshl_b32 s79, s92, 1\n"
      "v_mfma_f32_16x16x32_bf16 v[132:135], v[156:159], v[116:119], v[132:135]\n"
      "s_waitcnt vmcnt(25) lgkmcnt(4)\n"
      "s_barrier\n"
      "v_mad_i32_i24 v252, v251, s79, v252\n"
      "v_mfma_f32_16x16x32_bf16 v[136:139], v[160:163], v[36:39], v[20:23]\n"
      "ds_read_b128 v[36:39], v232 offset:24576\n"
      "s_mul_i32 s79, s79, s100\n"
      "v_mfma_f32_16x16x32_bf16 v[136:139], v[164:167], v[40:43], v[136:139]\n"
      "ds_read_b128 v[40:43], v233 offset:24576\n"
      "v_add_u32 v247, s79, v247\n"
      "v_mfma_f32_16x16x32_bf16 v[136:139], v[180:183], v[44:47], v[136:139]\n"
      "ds_read_b128 v[44:47], v234 offset:24576\n"
      "s_mul_i32 s79, s79, s101\n"
      "v_mfma_f32_16x16x32_bf16 v[136:139], v[184:187], v[60:63], v[136:139]\n"
      "ds_read_b128 v[60:63], v235 offset:24576\n"
      "v_cvt_pk_bf16_f32 v208, v132, v133\n"
      "v_mfma_f32_16x16x32_bf16 v[136:139], v[188:191], v[64:67], v[136:139]\n"
      "ds_read_b128 v[64:67], v232 offset:32768\n"
      "v_cvt_pk_bf16_f32 v209, v134, v135\n"
      "v_mfma_f32_16x16x32_bf16 v[136:139], v[204:207], v[68:71], v[136:139]\n"
      "ds_read_b128 v[68:71], v233 offset:32768\n"
      "v_add_u32 v253, s79, v253\n"
      "v_mfma_f32_16x16x32_bf16 v[136:139], a[24:27], v[84:87], v[136:139]\n"
      "ds_read_b128 v[84:87], v234 offset:32768\n"
      "v_mfma_f32_16x16x32_bf16 v[136:139], a[28:31], v[88:91], v[136:139]\n"
      "ds_read_b128 v[88:91], v235 offset:32768\n"
      "v_mfma_f32_16x16x32_bf16 v[136:139], a[32:35], v[92:95], v[136:139]\n"
      "ds_read_b128 v[92:95], v232 offset:40960\n"
      "v_mfma_f32_16x16x32_bf16 v[136:139], a[36:39], v[108:111], v[136:139]\n"
      "ds_read_b128 v[108:111], v233 offset:40960\n"
      "v_mfma_f32_16x16x32_bf16 v[136:139], a[40:43], v[112:115], v[136:139]\n"
      "ds_read_b128 v[112:115], v234 offset:40960\n"
      "v_mfma_f32_16x16x32_bf16 v[136:139], a[44:47], v[116:119], v[136:139]\n"
      "ds_read_b128 v[116:119], v235 offset:40960\n"
      "s_waitcnt lgkmcnt(8)\n"
      "v_mfma_f32_16x16x32_bf16 v[132:135], a[240:243], v[36:39], v[0:3]\n"
      "ds_write_b64 v244, v[208:209] offset:24704\n"
      "v_mfma_f32_16x16x32_bf16 v[132:135], a[244:247], v[40:43], v[132:135]\n"
      "s_add_u32 s54, s30, s35\n"
      "s_addc_u32 s55, s31, 0\n"
      "v_mfma_f32_16x16x32_bf16 v[132:135], a[248:251], v[44:47], v[132:135]\n"
      "v_mfma_f32_16x16x32_bf16 v[132:135], a[252:255], v[60:63], v[132:135]\n"
      "v_cvt_pk_bf16_f32 v210, v136, v137\n"
      "s_waitcnt lgkmcnt(5)\n"
      "v_mfma_f32_16x16x32_bf16 v[132:135], a[48:51], v[64:67], v[132:135]\n"
      "v_cvt_pk_bf16_f32 v211, v138, v139\n"
      "v_mfma_f32_16x16x32_bf16 v[132:135], a[52:55], v[68:71], v[132:135]\n"
      "s_mul_i32 s78, s94, s19\n"
      "v_mfma_f32_16x16x32_bf16 v[132:135], a[56:59], v[84:87], v[132:135]\n"
      "ds_read_b128 v[216:219], v246 offset:12288\n"
      "v_add_u32 v236, s78, v236\n"
      "v_mfma_f32_16x16x32_bf16 v[132:135], a[60:63], v[88:91], v[132:135]\n"
      "ds_read_b128 v[220:223], v246 offset:13312\n"
      "s_waitcnt lgkmcnt(3)\n"
      "v_mfma_f32_16x16x32_bf16 v[132:135], a[64:67], v[92:95], v[132:135]\n"
      "ds_read_b128 v[224:227], v246 offset:14336\n"
      "v_mfma_f32_16x16x32_bf16 v[132:135], a[68:71], v[108:111], v[132:135]\n"
      "ds_write_b64 v244, v[210:211] offset:24712\n"
      "v_mfma_f32_16x16x32_bf16 v[132:135], a[72:75], v[112:115], v[132:135]\n"
      "s_mul_i32 s78, s78, s99\n"
      "v_mfma_f32_16x16x32_bf16 v[132:135], a[76:79], v[116:119], v[132:135]\n"
      "v_add_u32 v237, s78, v237\n"
      "v_mfma_f32_16x16x32_bf16 v[136:139], a[80:83], v[36:39], v[4:7]\n"
      "v_mfma_f32_16x16x32_bf16 v[136:139], a[84:87], v[40:43], v[136:139]\n"
      "v_mfma_f32_16x16x32_bf16 v[136:139], a[88:91], v[44:47], v[136:139]\n"
      "v_mfma_f32_16x16x32_bf16 v[136:139], a[92:95], v[60:63], v[136:139]\n"
      "v_cvt_pk_bf16_f32 v208, v132, v133\n"
      "v_mfma_f32_16x16x32_bf16 v[136:139], a[96:99], v[64:67], v[136:139]\n"
      "v_cvt_pk_bf16_f32 v209, v134, v135\n"
      "v_mfma_f32_16x16x32_bf16 v[136:139], a[100:103], v[68:71], v[136:139]\n"
      "s_waitcnt lgkmcnt(3)\n"
      "v_mfma_f32_16x16x32_bf16 v[136:139], a[104:107], v[84:87], v[136:139]\n"
      "global_store_dwordx4 v247, v[216:219], s[54:55] offset:0 sc1\n"
      "v_mfma_f32_16x16x32_bf16 v[136:139], a[108:111], v[88:91], v[136:139]\n"
      "s_add_u32 s52, s20, s37\n"
      "s_addc_u32 s53, s21, 0\n"
      "v_mfma_f32_16x16x32_bf16 v[136:139], a[112:115], v[92:95], v[136:139]\n"
      "s_add_u32 m0, s17, 0xc000\n"
      "v_mfma_f32_16x16x32_bf16 v[136:139], a[116:119], v[108:111], v[136:139]\n"
      "ds_write_b64 v244, v[208:209] offset:0\n"
      "v_mfma_f32_16x16x32_bf16 v[136:139], a[120:123], v[112:115], v[136:139]\n"
      "global_load_lds_dwordx4 v236, s[52:53] offset:0 nt\n"
      "v_mfma_f32_16x16x32_bf16 v[136:139], a[124:127], v[116:119], v[136:139]\n"
      "v_mfma_f32_16x16x32_bf16 v[132:135], a[128:131], v[36:39], v[8:11]\n"
      "v_mfma_f32_16x16x32_bf16 v[132:135], a[132:135], v[40:43], v[132:135]\n"
      "v_mfma_f32_16x16x32_bf16 v[132:135], a[136:139], v[44:47], v[132:135]\n"
      "v_mfma_f32_16x16x32_bf16 v[132:135], a[140:143], v[60:63], v[132:135]\n"
      "v_cvt_pk_bf16_f32 v210, v136, v137\n"
      "v_mfma_f32_16x16x32_bf16 v[132:135], a[144:147], v[64:67], v[132:135]\n"
      "v_cvt_pk_bf16_f32 v211, v138, v139\n"
      "v_mfma_f32_16x16x32_bf16 v[132:135], a[148:151], v[68:71], v[132:135]\n"
      "v_mfma_f32_16x16x32_bf16 v[132:135], a[152:155], v[84:87], v[132:135]\n"
      "s_waitcnt lgkmcnt(3)\n"
      "v_mfma_f32_16x16x32_bf16 v[132:135], a[156:159], v[88:91], v[132:135]\n"
      "global_store_dwordx4 v252, v[220:223], s[54:55] offset:0 sc1\n"
      "v_mfma_f32_16x16x32_bf16 v[132:135], a[160:163], v[92:95], v[132:135]\n"
      "s_add_u32 m0, s17, 0xdf00\n"
      "v_mfma_f32_16x16x32_bf16 v[132:135], a[164:167], v[108:111], v[132:135]\n"
      "ds_write_b64 v244, v[210:211] offset:8\n"
      "v_mfma_f32_16x16x32_bf16 v[132:135], a[168:171], v[112:115], v[132:135]\n"
      "global_load_lds_dwordx4 v236, s[52:53] offset:256 nt\n"
      "v_mfma_f32_16x16x32_bf16 v[132:135], a[172:175], v[116:119], v[132:135]\n"
      "v_mfma_f32_16x16x32_bf16 v[136:139], a[176:179], v[36:39], v[12:15]\n"
      "v_mfma_f32_16x16x32_bf16 v[136:139], a[180:183], v[40:43], v[136:139]\n"
      "v_mfma_f32_16x16x32_bf16 v[136:139], a[184:187], v[44:47], v[136:139]\n"
      "v_mfma_f32_16x16x32_bf16 v[136:139], a[188:191], v[60:63], v[136:139]\n"
      "v_cvt_pk_bf16_f32 v212, v132, v133\n"
      "v_mfma_f32_16x16x32_bf16 v[136:139], a[192:195], v[64:67], v[136:139]\n"
      "v_cvt_pk_bf16_f32 v213, v134, v135\n"
      "v_mfma_f32_16x16x32_bf16 v[136:139], a[196:199], v[68:71], v[136:139]\n"
      "v_mfma_f32_16x16x32_bf16 v[136:139], a[200:203], v[84:87], v[136:139]\n"
      "v_mfma_f32_16x16x32_bf16 v[136:139], a[204:207], v[88:91], v[136:139]\n"
      "s_waitcnt lgkmcnt(3)\n"
      "v_mfma_f32_16x16x32_bf16 v[136:139], a[208:211], v[92:95], v[136:139]\n"
      "global_store_dwordx4 v253, v[224:227], s[54:55] offset:0 sc1\n"
      "v_mfma_f32_16x16x32_bf16 v[136:139], a[212:215], v[108:111], v[136:139]\n"
      "ds_write_b64 v245, v[212:213] offset:0\n"
      "v_mfma_f32_16x16x32_bf16 v[136:139], a[216:219], v[112:115], v[136:139]\n"
      "s_add_u32 m0, s17, 0xfe00\n"
      "v_mfma_f32_16x16x32_bf16 v[136:139], a[220:223], v[116:119], v[136:139]\n"
      "global_load_lds_dwordx4 v236, s[52:53] offset:512 nt\n"
      "v_mfma_f32_16x16x32_bf16 v[132:135], a[224:227], v[36:39], v[16:19]\n"
      "v_mfma_f32_16x16x32_bf16 v[132:135], a[228:231], v[40:43], v[132:135]\n"
      "v_mfma_f32_16x16x32_bf16 v[132:135], a[232:235], v[44:47], v[132:135]\n"
      "v_mfma_f32_16x16x32_bf16 v[132:135], a[236:239], v[60:63], v[132:135]\n"
      "v_cvt_pk_bf16_f32 v214, v136, v137\n"
      "v_mfma_f32_16x16x32_bf16 v[132:135], a[0:3], v[64:67], v[132:135]\n"
      "v_cvt_pk_bf16_f32 v215, v138, v139\n"
      "v_mfma_f32_16x16x32_bf16 v[132:135], a[4:7], v[68:71], v[132:135]\n"
      "v_mfma_f32_16x16x32_bf16 v[132:135], a[8:11], v[84:87], v[132:135]\n"
      "v_mfma_f32_16x16x32_bf16 v[132:135], a[12:15], v[88:91], v[132:135]\n"
      "v_mfma_f32_16x16x32_bf16 v[132:135], a[16:19], v[92:95], v[132:135]\n"
      "v_mfma_f32_16x16x32_bf16 v[132:135], a[20:23], v[108:111], v[132:135]\n"
      "ds_write_b64 v245, v[214:215] offset:8\n"
      "v_mfma_f32_16x16x32_bf16 v[132:135], v[140:143], v[112:115], v[132:135]\n"
      "v_mfma_f32_16x16x32_bf16 v[132:135], v[156:159], v[116:119], v[132:135]\n"
      "s_waitcnt lgkmcnt(4)\n"
      "s_barrier\n"
      "v_mfma_f32_16x16x32_bf16 v[136:139], v[160:163], v[36:39], v[20:23]\n"
      "ds_read_b128 v[36:39], v232 offset:24832\n"
      "v_mfma_f32_16x16x32_bf16 v[136:139], v[164:167], v[40:43], v[136:139]\n"
      "ds_read_b128 v[40:43], v233 offset:24832\n"
      "v_mfma_f32_16x16x32_bf16 v[136:139], v[180:183], v[44:47], v[136:139]\n"
      "ds_read_b128 v[44:47], v234 offset:24832\n"
      "v_mfma_f32_16x16x32_bf16 v[136:139], v[184:187], v[60:63], v[136:139]\n"
      "ds_read_b128 v[60:63], v235 offset:24832\n"
      "v_cvt_pk_bf16_f32 v208, v132, v133\n"
      "v_mfma_f32_16x16x32_bf16 v[136:139], v[188:191], v[64:67], v[136:139]\n"
      "ds_read_b128 v[64:67], v232 offset:33024\n"
      "v_cvt_pk_bf16_f32 v209, v134, v135\n"
      "v_mfma_f32_16x16x32_bf16 v[136:139], v[204:207], v[68:71], v[136:139]\n"
      "ds_read_b128 v[68:71], v233 offset:33024\n"
      "v_mfma_f32_16x16x32_bf16 v[136:139], a[24:27], v[84:87], v[136:139]\n"
      "ds_read_b128 v[84:87], v234 offset:33024\n"
      "v_mfma_f32_16x16x32_bf16 v[136:139], a[28:31], v[88:91], v[136:139]\n"
      "ds_read_b128 v[88:91], v235 offset:33024\n"
      "v_mfma_f32_16x16x32_bf16 v[136:139], a[32:35], v[92:95], v[136:139]\n"
      "ds_read_b128 v[92:95], v232 offset:41216\n"
      "v_mfma_f32_16x16x32_bf16 v[136:139], a[36:39], v[108:111], v[136:139]\n"
      "ds_read_b128 v[108:111], v233 offset:41216\n"
      "LPD11:\n"
      "s_cbranch_execz LPD14\n"
      "ds_write_b64 v244, v[208:209] offset:128\n"
      "v_mfma_f32_16x16x32_bf16 v[136:139], a[40:43], v[112:115], v[136:139]\n"
      "ds_read_b128 v[112:115], v234 offset:41216\n"
      "v_mfma_f32_16x16x32_bf16 v[136:139], a[44:47], v[116:119], v[136:139]\n"
      "ds_read_b128 v[116:119], v235 offset:41216\n"
      "s_waitcnt lgkmcnt(9)\n"
      "v_mfma_f32_16x16x32_bf16 v[132:135], a[240:243], v[36:39], v[0:3]\n"
      "v_add_u32 v232, 0xffff4000, v232\n"
      "v_mfma_f32_16x16x32_bf16 v[132:135], a[244:247], v[40:43], v[132:135]\n"
      "v_add_u32 v233, 0xffff4000, v233\n"
      "v_mfma_f32_16x16x32_bf16 v[132:135], a[248:251], v[44:47], v[132:135]\n"
      "v_add_u32 v234, 0xffff4000, v234\n"
      "v_mfma_f32_16x16x32_bf16 v[132:135], a[252:255], v[60:63], v[132:135]\n"
      "v_add_u32 v235, 0xffff4000, v235\n"
      "s_waitcnt lgkmcnt(5)\n"
      "v_mfma_f32_16x16x32_bf16 v[132:135], a[48:51], v[64:67], v[132:135]\n"
      "v_cvt_pk_bf16_f32 v210, v136, v137\n"
      "v_mfma_f32_16x16x32_bf16 v[132:135], a[52:55], v[68:71], v[132:135]\n"
      "v_cvt_pk_bf16_f32 v211, v138, v139\n"
      "v_mfma_f32_16x16x32_bf16 v[132:135], a[56:59], v[84:87], v[132:135]\n"
      "ds_read_b128 v[216:219], v246 offset:24576\n"
      "v_mfma_f32_16x16x32_bf16 v[132:135], a[60:63], v[88:91], v[132:135]\n"
      "ds_read_b128 v[220:223], v246 offset:25600\n"
      "s_waitcnt lgkmcnt(2)\n"
      "v_mfma_f32_16x16x32_bf16 v[132:135], a[64:67], v[92:95], v[132:135]\n"
      "ds_read_b128 v[224:227], v246 offset:26624\n"
      "v_mfma_f32_16x16x32_bf16 v[132:135], a[68:71], v[108:111], v[132:135]\n"
      "v_lshlrev_b32 v0, 16, v96\n"
      "v_mfma_f32_16x16x32_bf16 v[132:135], a[72:75], v[112:115], v[132:135]\n"
      "ds_write_b64 v244, v[210:211] offset:136\n"
      "v_mfma_f32_16x16x32_bf16 v[132:135], a[76:79], v[116:119], v[132:135]\n"
      "v_and_b32 v1, 0xffff0000, v96\n"
      "v_mfma_f32_16x16x32_bf16 v[136:139], a[80:83], v[36:39], v[4:7]\n"
      "v_lshlrev_b32 v2, 16, v97\n"
      "v_mfma_f32_16x16x32_bf16 v[136:139], a[84:87], v[40:43], v[136:139]\n"
      "v_and_b32 v3, 0xffff0000, v97\n"
      "v_mfma_f32_16x16x32_bf16 v[136:139], a[88:91], v[44:47], v[136:139]\n"
      "s_waitcnt vmcnt(12)\n"
      "s_sub_u32 s49, s48, 1000\n"
      "v_mfma_f32_16x16x32_bf16 v[136:139], a[92:95], v[60:63], v[136:139]\n"
      "v_mov_b32 v230, 1100\n"
      "v_cvt_pk_bf16_f32 v209, v134, v135\n"
      "v_mfma_f32_16x16x32_bf16 v[136:139], a[96:99], v[64:67], v[136:139]\n"
      "v_xor_b32 v228, s80, v228\n"
      "v_mfma_f32_16x16x32_bf16 v[136:139], a[100:103], v[68:71], v[136:139]\n"
      "v_subrev_u32 v228, s49, v228\n"
      "v_mfma_f32_16x16x32_bf16 v[136:139], a[104:107], v[84:87], v[136:139]\n"
      "v_mov_b32 v231, 2000\n"
      "v_mfma_f32_16x16x32_bf16 v[136:139], a[108:111], v[88:91], v[136:139]\n"
      "v_cmp_gt_u32 s[58:59], v231, v228\n"
      "v_mfma_f32_16x16x32_bf16 v[136:139], a[112:115], v[92:95], v[136:139]\n"
      "v_cndmask_b32 v228, v230, v228, s[58:59]\n"
      "v_mfma_f32_16x16x32_bf16 v[136:139], a[116:119], v[108:111], v[136:139]\n"
      "v_cvt_pk_bf16_f32 v208, v132, v133\n"
      "v_mfma_f32_16x16x32_bf16 v[136:139], a[120:123], v[112:115], v[136:139]\n"
      "v_add_u32_dpp v228, v228, v228 row_shr:1 row_mask:0xf bank_mask:0xf bound_ctrl:0\n"
      "v_mfma_f32_16x16x32_bf16 v[136:139], a[124:127], v[116:119], v[136:139]\n"
      "s_waitcnt lgkmcnt(3)\n"
      "v_mfma_f32_16x16x32_bf16 v[132:135], a[128:131], v[36:39], v[8:11]\n"
      "v_add_u32_dpp v228, v228, v228 row_shr:2 row_mask:0xf bank_mask:0xf bound_ctrl:0\n"
      "v_mfma_f32_16x16x32_bf16 v[132:135], a[132:135], v[40:43], v[132:135]\n"
      "global_store_dwordx4 v247, v[216:219], s[54:55] offset:768 sc1\n"
      "v_mfma_f32_16x16x32_bf16 v[132:135], a[136:139], v[44:47], v[132:135]\n"
      "v_add_u32_dpp v228, v228, v228 row_shr:4 row_mask:0xf bank_mask:0xf bound_ctrl:0\n"
      "v_mfma_f32_16x16x32_bf16 v[132:135], a[140:143], v[60:63], v[132:135]\n"
      "ds_write_b64 v244, v[208:209] offset:12288\n"
      "v_mfma_f32_16x16x32_bf16 v[132:135], a[144:147], v[64:67], v[132:135]\n"
      "v_add_u32_dpp v228, v228, v228 row_shr:8 row_mask:0xf bank_mask:0xf bound_ctrl:0\n"
      "v_mfma_f32_16x16x32_bf16 v[132:135], a[148:151], v[68:71], v[132:135]\n"
      "v_cvt_pk_bf16_f32 v210, v136, v137\n"
      "v_mfma_f32_16x16x32_bf16 v[132:135], a[152:155], v[84:87], v[132:135]\n"
      "v_readlane_b32 s62, v228, 15\n"
      "v_mfma_f32_16x16x32_bf16 v[132:135], a[156:159], v[88:91], v[132:135]\n"
      "v_cvt_pk_bf16_f32 v211, v138, v139\n"
      "v_mfma_f32_16x16x32_bf16 v[132:135], a[160:163], v[92:95], v[132:135]\n"
      "s_waitcnt lgkmcnt(3)\n"
      "v_mfma_f32_16x16x32_bf16 v[132:135], a[164:167], v[108:111], v[132:135]\n"
      "s_lshr_b32 s62, s62, 4\n"
      "v_mfma_f32_16x16x32_bf16 v[132:135], a[168:171], v[112:115], v[132:135]\n"
      "s_add_u32 s62, s62, 20\n"
      "v_mfma_f32_16x16x32_bf16 v[132:135], a[172:175], v[116:119], v[132:135]\n"
      "s_add_u32 s49, s49, s62\n"
      "v_mfma_f32_16x16x32_bf16 v[136:139], a[176:179], v[36:39], v[12:15]\n"
      "s_add_u32 s85, s83, 3475\n"
      "ds_write_b64 v244, v[210:211] offset:12296\n"
      "v_mfma_f32_16x16x32_bf16 v[136:139], a[180:183], v[40:43], v[136:139]\n"
      "s_sub_u32 s85, s85, s49\n"
      "v_mfma_f32_16x16x32_bf16 v[136:139], a[184:187], v[44:47], v[136:139]\n"
      "s_max_i32 s85, s85, 0\n"
      "v_mfma_f32_16x16x32_bf16 v[136:139], a[188:191], v[60:63], v[136:139]\n"
      "s_add_u32 s49, s49, s85\n"
      "v_mfma_f32_16x16x32_bf16 v[136:139], a[192:195], v[64:67], v[136:139]\n"
      "s_add_u32 s62, s62, s85\n"
      "v_mfma_f32_16x16x32_bf16 v[136:139], a[196:199], v[68:71], v[136:139]\n"
      "v_cvt_pk_bf16_f32 v212, v132, v133\n"
      "v_mfma_f32_16x16x32_bf16 v[136:139], a[200:203], v[84:87], v[136:139]\n"
      "v_cvt_pk_bf16_f32 v213, v134, v135\n"
      "v_mfma_f32_16x16x32_bf16 v[136:139], a[204:207], v[88:91], v[136:139]\n"
      "global_store_dwordx4 v252, v[220:223], s[54:55] offset:768 sc1\n"
      "v_mfma_f32_16x16x32_bf16 v[136:139], a[208:211], v[92:95], v[136:139]\n"
      "s_waitcnt lgkmcnt(3)\n"
      "v_mfma_f32_16x16x32_bf16 v[136:139], a[212:215], v[108:111], v[136:139]\n"
      "global_store_dwordx4 v253, v[224:227], s[54:55] offset:768 sc1\n"
      "v_mfma_f32_16x16x32_bf16 v[136:139], a[216:219], v[112:115], v[136:139]\n"
      "s_add_u32 m0, s17, 0xd000\n"
      "v_mfma_f32_16x16x32_bf16 v[136:139], a[220:223], v[116:119], v[136:139]\n"
      "ds_write_b64 v245, v[212:213] offset:12288\n"
      "v_mfma_f32_16x16x32_bf16 v[132:135], a[224:227], v[36:39], v[16:19]\n"
      "global_load_lds_dwordx4 v237, s[52:53] offset:0 nt\n"
      "v_mfma_f32_16x16x32_bf16 v[132:135], a[228:231], v[40:43], v[132:135]\n"
      "v_lshlrev_b32 v4, 16, v98\n"
      "v_mfma_f32_16x16x32_bf16 v[132:135], a[232:235], v[44:47], v[132:135]\n"
      "v_and_b32 v5, 0xffff0000, v98\n"
      "v_mfma_f32_16x16x32_bf16 v[132:135], a[236:239], v[60:63], v[132:135]\n"
      "s_memrealtime s[84:85]\n"
      "v_mfma_f32_16x16x32_bf16 v[132:135], a[0:3], v[64:67], v[132:135]\n"
      "v_cvt_pk_bf16_f32 v214, v136, v137\n"
      "v_mfma_f32_16x16x32_bf16 v[132:135], a[4:7], v[68:71], v[132:135]\n"
      "v_cvt_pk_bf16_f32 v215, v138, v139\n"
      "v_mfma_f32_16x16x32_bf16 v[132:135], a[8:11], v[84:87], v[132:135]\n"
      "v_lshlrev_b32 v6, 16, v99\n"
      "v_mfma_f32_16x16x32_bf16 v[132:135], a[12:15], v[88:91], v[132:135]\n"
      "v_and_b32 v7, 0xffff0000, v99\n"
      "v_mfma_f32_16x16x32_bf16 v[132:135], a[16:19], v[92:95], v[132:135]\n"
      "v_lshlrev_b32 v8, 16, v100\n"
      "v_mfma_f32_16x16x32_bf16 v[132:135], a[20:23], v[108:111], v[132:135]\n"
      "v_and_b32 v9, 0xffff0000, v100\n"
      "v_mfma_f32_16x16x32_bf16 v[132:135], v[140:143], v[112:115], v[132:135]\n"
      "s_waitcnt lgkmcnt(0)\n"
      "s_sub_u32 s86, s49, s84\n"
      "s_mov_b32 s83, s84\n"
      "v_mfma_f32_16x16x32_bf16 v[132:135], v[156:159], v[116:119], v[132:135]\n"
      "s_cmp_gt_u32 s62, 1080\n"
      "s_cselect_b64 vcc, -1, 0\n"
      "s_barrier\n"
      "v_mfma_f32_16x16x32_bf16 v[136:139], v[160:163], v[36:39], v[20:23]\n"
      "ds_read_b128 v[36:39], v232 offset:0\n"
      "ds_write_b64 v245, v[214:215] offset:12296\n"
      "v_mfma_f32_16x16x32_bf16 v[136:139], v[164:167], v[40:43], v[136:139]\n"
      "ds_read_b128 v[40:43], v233 offset:0\n"
      "v_mfma_f32_16x16x32_bf16 v[136:139], v[180:183], v[44:47], v[136:139]\n"
      "ds_read_b128 v[44:47], v234 offset:0\n"
      "v_mfma_f32_16x16x32_bf16 v[136:139], v[184:187], v[60:63], v[136:139]\n"
      "ds_read_b128 v[60:63], v235 offset:0\n"
      "v_cvt_pk_bf16_f32 v208, v132, v133\n"
      "v_mfma_f32_16x16x32_bf16 v[136:139], v[188:191], v[64:67], v[136:139]\n"
      "ds_read_b128 v[64:67], v232 offset:8192\n"
      "v_cvt_pk_bf16_f32 v209, v134, v135\n"
      "v_mfma_f32_16x16x32_bf16 v[136:139], v[204:207], v[68:71], v[136:139]\n"
      "ds_read_b128 v[68:71], v233 offset:8192\n"
      "v_mfma_f32_16x16x32_bf16 v[136:139], a[24:27], v[84:87], v[136:139]\n"
      "ds_read_b128 v[84:87], v234 offset:8192\n"
      "v_mfma_f32_16x16x32_bf16 v[136:139], a[28:31], v[88:91], v[136:139]\n"
      "ds_read_b128 v[88:91], v235 offset:8192\n"
      "v_mfma_f32_16x16x32_bf16 v[136:139], a[32:35], v[92:95], v[136:139]\n"
      "ds_read_b128 v[92:95], v232 offset:16384\n"
      "v_mfma_f32_16x16x32_bf16 v[136:139], a[36:39], v[108:111], v[136:139]\n"
      "ds_read_b128 v[108:111], v233 offset:16384\n"
      "v_mfma_f32_16x16x32_bf16 v[136:139], a[40:43], v[112:115], v[136:139]\n"
      "ds_read_b128 v[112:115], v234 offset:16384\n"
      "v_mfma_f32_16x16x32_bf16 v[136:139], a[44:47], v[116:119], v[136:139]\n"
      "ds_read_b128 v[116:119], v235 offset:16384\n"
      "s_waitcnt lgkmcnt(8)\n"
      "v_mfma_f32_16x16x32_bf16 v[132:135], a[240:243], v[36:39], v[0:3]\n"
      "ds_write_b64 v244, v[208:209] offset:12416\n"
      "s_cbranch_vccnz LT0\n"
      "LS0:\n"
      "v_mfma_f32_16x16x32_bf16 v[132:135], a[244:247], v[40:43], v[132:135]\n"
      "s_add_u32 m0, s17, 0xef00\n"
      "v_mfma_f32_16x16x32_bf16 v[132:135], a[248:251], v[44:47], v[132:135]\n"
      "s_mul_i32 s86, s86, 146\n"
      "v_mfma_f32_16x16x32_bf16 v[132:135], a[252:255], v[60:63], v[132:135]\n"
      "global_load_lds_dwordx4 v237, s[52:53] offset:256 nt\n"
      "v_cvt_pk_bf16_f32 v210, v136, v137\n"
      "s_waitcnt lgkmcnt(5)\n"
      "v_mfma_f32_16x16x32_bf16 v[132:135], a[48:51], v[64:67], v[132:135]\n"
      "s_ashr_i32 s86, s86, 10\n"
      "v_cvt_pk_bf16_f32 v211, v138, v139\n"
      "v_mfma_f32_16x16x32_bf16 v[132:135], a[52:55], v[68:71], v[132:135]\n"
      "v_lshlrev_b32 v10, 16, v101\n"
      "v_mfma_f32_16x16x32_bf16 v[132:135], a[56:59], v[84:87], v[132:135]\n"
      "v_and_b32 v11, 0xffff0000, v101\n"
      "v_mfma_f32_16x16x32_bf16 v[132:135], a[60:63], v[88:91], v[132:135]\n"
      "v_lshlrev_b32 v12, 16, v102\n"
      "s_waitcnt lgkmcnt(1)\n"
      "v_mfma_f32_16x16x32_bf16 v[132:135], a[64:67], v[92:95], v[132:135]\n"
      "v_and_b32 v13, 0xffff0000, v102\n"
      "v_mfma_f32_16x16x32_bf16 v[132:135], a[68:71], v[108:111], v[132:135]\n"
      "v_lshlrev_b32 v14, 16, v103\n"
      "ds_write_b64 v244, v[210:211] offset:12424\n"
      "v_mfma_f32_16x16x32_bf16 v[132:135], a[72:75], v[112:115], v[132:135]\n"
      "v_and_b32 v15, 0xffff0000, v103\n"
      "v_mfma_f32_16x16x32_bf16 v[132:135], a[76:79], v[116:119], v[132:135]\n"
      "v_lshlrev_b32 v16, 16, v104\n"
      "v_mfma_f32_16x16x32_bf16 v[136:139], a[80:83], v[36:39], v[4:7]\n"
      "v_and_b32 v17, 0xffff0000, v104\n"
      "v_mfma_f32_16x16x32_bf16 v[136:139], a[84:87], v[40:43], v[136:139]\n"
      "s_add_u32 m0, s17, 0x10e00\n"
      "v_mfma_f32_16x16x32_bf16 v[136:139], a[88:91], v[44:47], v[136:139]\n"
      "global_load_lds_dwordx4 v237, s[52:53] offset:512 nt\n"
      "v_mfma_f32_16x16x32_bf16 v[136:139], a[92:95], v[60:63], v[136:139]\n"
      "v_lshlrev_b32 v18, 16, v105\n"
      "v_cvt_pk_bf16_f32 v209, v134, v135\n"
      "v_mfma_f32_16x16x32_bf16 v[136:139], a[96:99], v[64:67], v[136:139]\n"
      "v_and_b32 v19, 0xffff0000, v105\n"
      "v_mfma_f32_16x16x32_bf16 v[136:139], a[100:103], v[68:71], v[136:139]\n"
      "v_lshlrev_b32 v20, 16, v106\n"
      "v_mfma_f32_16x16x32_bf16 v[136:139], a[104:107], v[84:87], v[136:139]\n"
      "v_and_b32 v21, 0xffff0000, v106\n"
      "v_mfma_f32_16x16x32_bf16 v[136:139], a[108:111], v[88:91], v[136:139]\n"
      "v_lshlrev_b32 v22, 16, v107\n"
      "v_mfma_f32_16x16x32_bf16 v[136:139], a[112:115], v[92:95], v[136:139]\n"
      "v_and_b32 v23, 0xffff0000, v107\n"
      "v_mfma_f32_16x16x32_bf16 v[136:139], a[116:119], v[108:111], v[136:139]\n"
      "v_cvt_pk_bf16_f32 v208, v132, v133\n"
      "v_mfma_f32_16x16x32_bf16 v[136:139], a[120:123], v[112:115], v[136:139]\n"
      "s_add_u32 s54, s44, s35\n"
      "s_addc_u32 s55, s45, 0\n"
      "v_mfma_f32_16x16x32_bf16 v[136:139], a[124:127], v[116:119], v[136:139]\n"
      "ds_read_b128 v[216:219], v246 offset:0\n"
      "v_mfma_f32_16x16x32_bf16 v[132:135], a[128:131], v[36:39], v[8:11]\n"
      "ds_read_b128 v[220:223], v246 offset:1024\n"
      "s_cbranch_vccnz LT1\n"
      "LS1:\n"
      "v_mfma_f32_16x16x32_bf16 v[132:135], a[132:135], v[40:43], v[132:135]\n"
      "ds_read_b128 v[224:227], v246 offset:2048\n"
      "v_mfma_f32_16x16x32_bf16 v[132:135], a[136:139], v[44:47], v[132:135]\n"
      "ds_write_b64 v244, v[208:209] offset:24576\n"
      "v_mfma_f32_16x16x32_bf16 v[132:135], a[140:143], v[60:63], v[132:135]\n"
      "v_cvt_pk_bf16_f32 v210, v136, v137\n"
      "v_mfma_f32_16x16x32_bf16 v[132:135], a[144:147], v[64:67], v[132:135]\n"
      "v_cvt_pk_bf16_f32 v211, v138, v139\n"
      "v_mfma_f32_16x16x32_bf16 v[132:135], a[148:151], v[68:71], v[132:135]\n"
      "s_waitcnt lgkmcnt(3)\n"
      "v_mfma_f32_16x16x32_bf16 v[132:135], a[152:155], v[84:87], v[132:135]\n"
      "global_store_dwordx4 v247, v[216:219], s[54:55] offset:0 sc1\n"
      "v_mfma_f32_16x16x32_bf16 v[132:135], a[156:159], v[88:91], v[132:135]\n"
      "s_waitcnt lgkmcnt(2)\n"
      "v_mfma_f32_16x16x32_bf16 v[132:135], a[160:163], v[92:95], v[132:135]\n"
      "global_store_dwordx4 v252, v[220:223], s[54:55] offset:0 sc1\n"
      "v_mfma_f32_16x16x32_bf16 v[132:135], a[164:167], v[108:111], v[132:135]\n"
      "ds_write_b64 v244, v[210:211] offset:24584\n"
      "v_mfma_f32_16x16x32_bf16 v[132:135], a[168:171], v[112:115], v[132:135]\n"
      "s_add_u32 s52, s42, s37\n"
      "s_addc_u32 s53, s43, 0\n"
      "v_mfma_f32_16x16x32_bf16 v[132:135], a[172:175], v[116:119], v[132:135]\n"
      "s_add_u32 m0, s17, 0x12000\n"
      "v_mfma_f32_16x16x32_bf16 v[136:139], a[176:179], v[36:39], v[12:15]\n"
      "global_load_lds_dwordx4 v236, s[52:53] offset:0 nt\n"
      "v_mfma_f32_16x16x32_bf16 v[136:139], a[180:183], v[40:43], v[136:139]\n"
      "v_mfma_f32_16x16x32_bf16 v[136:139], a[184:187], v[44:47], v[136:139]\n"
      "v_mfma_f32_16x16x32_bf16 v[136:139], a[188:191], v[60:63], v[136:139]\n"
      "v_cvt_pk_bf16_f32 v212, v132, v133\n"
      "v_mfma_f32_16x16x32_bf16 v[136:139], a[192:195], v[64:67], v[136:139]\n"
      "v_cvt_pk_bf16_f32 v213, v134, v135\n"
      "v_mfma_f32_16x16x32_bf16 v[136:139], a[196:199], v[68:71], v[136:139]\n"
      "v_mfma_f32_16x16x32_bf16 v[136:139], a[200:203], v[84:87], v[136:139]\n"
      "v_mfma_f32_16x16x32_bf16 v[136:139], a[204:207], v[88:91], v[136:139]\n"
      "s_waitcnt lgkmcnt(2)\n"
      "v_mfma_f32_16x16x32_bf16 v[136:139], a[208:211], v[92:95], v[136:139]\n"
      "global_store_dwordx4 v253, v[224:227], s[54:55] offset:0 sc1\n"
      "v_mfma_f32_16x16x32_bf16 v[136:139], a[212:215], v[108:111], v[136:139]\n"
      "ds_write_b64 v245, v[212:213] offset:24576\n"
      "v_mfma_f32_16x16x32_bf16 v[136:139], a[216:219], v[112:115], v[136:139]\n"
      "s_add_u32 m0, s17, 0x13f00\n"
      "v_mfma_f32_16x16x32_bf16 v[136:139], a[220:223], v[116:119], v[136:139]\n"
      "global_load_lds_dwordx4 v236, s[52:53] offset:256 nt\n"
      "v_mfma_f32_16x16x32_bf16 v[132:135], a[224:227], v[36:39], v[16:19]\n"
      "s_cbranch_vccnz LT2\n"
      "LS2:\n"
      "v_mfma_f32_16x16x32_bf16 v[132:135], a[228:231], v[40:43], v[132:135]\n"
      "v_mfma_f32_16x16x32_bf16 v[132:135], a[232:235], v[44:47], v[132:135]\n"
      "v_mfma_f32_16x16x32_bf16 v[132:135], a[236:239], v[60:63], v[132:135]\n"
      "v_cvt_pk_bf16_f32 v214, v136, v137\n"
      "v_mfma_f32_16x16x32_bf16 v[132:135], a[0:3], v[64:67], v[132:135]\n"
      "v_cvt_pk_bf16_f32 v215, v138, v139\n"
      "v_mfma_f32_16x16x32_bf16 v[132:135], a[4:7], v[68:71], v[132:135]\n"
      "v_mfma_f32_16x16x32_bf16 v[132:135], a[8:11], v[84:87], v[132:135]\n"
      "v_mfma_f32_16x16x32_bf16 v[132:135], a[12:15], v[88:91], v[132:135]\n"
      "v_mfma_f32_16x16x32_bf16 v[132:135], a[16:19], v[92:95], v[132:135]\n"
      "v_mfma_f32_16x16x32_bf16 v[132:135], a[20:23], v[108:111], v[132:135]\n"
      "ds_write_b64 v245, v[214:215] offset:24584\n"
      "v_mfma_f32_16x16x32_bf16 v[132:135], v[140:143], v[112:115], v[132:135]\n"
      "s_add_u32 m0, s17, 0x15e00\n"
      "v_mfma_f32_16x16x32_bf16 v[132:135], v[156:159], v[116:119], v[132:135]\n"
      "s_barrier\n"
      "v_mfma_f32_16x16x32_bf16 v[136:139], v[160:163], v[36:39], v[20:23]\n"
      "ds_read_b128 v[36:39], v232 offset:256\n"
      "v_mfma_f32_16x16x32_bf16 v[136:139], v[164:167], v[40:43], v[136:139]\n"
      "ds_read_b128 v[40:43], v233 offset:256\n"
      "v_mfma_f32_16x16x32_bf16 v[136:139], v[180:183], v[44:47], v[136:139]\n"
      "ds_read_b128 v[44:47], v234 offset:256\n"
      "v_mfma_f32_16x16x32_bf16 v[136:139], v[184:187], v[60:63], v[136:139]\n"
      "ds_read_b128 v[60:63], v235 offset:256\n"
      "v_cvt_pk_bf16_f32 v208, v132, v133\n"
      "v_mfma_f32_16x16x32_bf16 v[136:139], v[188:191], v[64:67], v[136:139]\n"
      "ds_read_b128 v[64:67], v232 offset:8448\n"
      "v_cvt_pk_bf16_f32 v209, v134, v135\n"
      "v_mfma_f32_16x16x32_bf16 v[136:139], v[204:207], v[68:71], v[136:139]\n"
      "ds_read_b128 v[68:71], v233 offset:8448\n"
      "v_mfma_f32_16x16x32_bf16 v[136:139], a[24:27], v[84:87], v[136:139]\n"
      "ds_read_b128 v[84:87], v234 offset:8448\n"
      "v_mfma_f32_16x16x32_bf16 v[136:139], a[28:31], v[88:91], v[136:139]\n"
      "ds_read_b128 v[88:91], v235 offset:8448\n"
      "v_mfma_f32_16x16x32_bf16 v[136:139], a[32:35], v[92:95], v[136:139]\n"
      "ds_read_b128 v[92:95], v232 offset:16640\n"
      "v_mfma_f32_16x16x32_bf16 v[136:139], a[36:39], v[108:111], v[136:139]\n"
      "ds_read_b128 v[108:111], v233 offset:16640\n"
      "v_mfma_f32_16x16x32_bf16 v[136:139], a[40:43], v[112:115], v[136:139]\n"
      "ds_read_b128 v[112:115], v234 offset:16640\n"
      "v_mfma_f32_16x16x32_bf16 v[136:139], a[44:47], v[116:119], v[136:139]\n"
      "ds_read_b128 v[116:119], v235 offset:16640\n"
      "s_waitcnt lgkmcnt(8)\n"
      "v_mfma_f32_16x16x32_bf16 v[132:135], a[240:243], v[36:39], v[0:3]\n"
      "ds_write_b64 v244, v[208:209] offset:24704\n"
      "v_mfma_f32_16x16x32_bf16 v[132:135], a[244:247], v[40:43], v[132:135]\n"
      "global_load_lds_dwordx4 v236, s[52:53] offset:512 nt\n"
      "v_mfma_f32_16x16x32_bf16 v[132:135], a[248:251], v[44:47], v[132:135]\n"
      "s_cbranch_vccnz LT3\n"
      "LS3:\n"
      "v_mfma_f32_16x16x32_bf16 v[132:135], a[252:255], v[60:63], v[132:135]\n"
      "v_cvt_pk_bf16_f32 v210, v136, v137\n"
      "s_waitcnt lgkmcnt(5)\n"
      "v_mfma_f32_16x16x32_bf16 v[132:135], a[48:51], v[64:67], v[132:135]\n"
      "v_cvt_pk_bf16_f32 v211, v138, v139\n"
      "v_mfma_f32_16x16x32_bf16 v[132:135], a[52:55], v[68:71], v[132:135]\n"
      "v_mfma_f32_16x16x32_bf16 v[132:135], a[56:59], v[84:87], v[132:135]\n"
      "ds_read_b128 v[216:219], v246 offset:12288\n"
      "v_mfma_f32_16x16x32_bf16 v[132:135], a[60:63], v[88:91], v[132:135]\n"
      "ds_read_b128 v[220:223], v246 offset:13312\n"
      "s_waitcnt lgkmcnt(3)\n"
      "v_mfma_f32_16x16x32_bf16 v[132:135], a[64:67], v[92:95], v[132:135]\n"
      "ds_read_b128 v[224:227], v246 offset:14336\n"
      "v_mfma_f32_16x16x32_bf16 v[132:135], a[68:71], v[108:111], v[132:135]\n"
      "ds_write_b64 v244, v[210:211] offset:24712\n"
      "v_mfma_f32_16x16x32_bf16 v[132:135], a[72:75], v[112:115], v[132:135]\n"
      "v_mfma_f32_16x16x32_bf16 v[132:135], a[76:79], v[116:119], v[132:135]\n"
      "v_mfma_f32_16x16x32_bf16 v[136:139], a[80:83], v[36:39], v[4:7]\n"
      "v_mfma_f32_16x16x32_bf16 v[136:139], a[84:87], v[40:43], v[136:139]\n"
      "v_mfma_f32_16x16x32_bf16 v[136:139], a[88:91], v[44:47], v[136:139]\n"
      "v_mfma_f32_16x16x32_bf16 v[136:139], a[92:95], v[60:63], v[136:139]\n"
      "v_cvt_pk_bf16_f32 v208, v132, v133\n"
      "v_mfma_f32_16x16x32_bf16 v[136:139], a[96:99], v[64:67], v[136:139]\n"
      "v_cvt_pk_bf16_f32 v209, v134, v135\n"
      "v_mfma_f32_16x16x32_bf16 v[136:139], a[100:103], v[68:71], v[136:139]\n"
      "s_waitcnt lgkmcnt(3)\n"
      "v_mfma_f32_16x16x32_bf16 v[136:139], a[104:107], v[84:87], v[136:139]\n"
      "global_store_dwordx4 v247, v[216:219], s[54:55] offset:768 sc1\n"
      "v_mfma_f32_16x16x32_bf16 v[136:139], a[108:111], v[88:91], v[136:139]\n"
      "s_add_u32 m0, s17, 0x13000\n"
      "v_mfma_f32_16x16x32_bf16 v[136:139], a[112:115], v[92:95], v[136:139]\n"
      "global_load_lds_dwordx4 v237, s[52:53] offset:0 nt\n"
      "v_mfma_f32_16x16x32_bf16 v[136:139], a[116:119], v[108:111], v[136:139]\n"
      "ds_write_b64 v244, v[208:209] offset:0\n"
      "v_mfma_f32_16x16x32_bf16 v[136:139], a[120:123], v[112:115], v[136:139]\n"
      "v_mfma_f32_16x16x32_bf16 v[136:139], a[124:127], v[116:119], v[136:139]\n"
      "v_mfma_f32_16x16x32_bf16 v[132:135], a[128:131], v[36:39], v[8:11]\n"
      "s_cbranch_vccnz LT4\n"
      "LS4:\n"
      "v_mfma_f32_16x16x32_bf16 v[132:135], a[132:135], v[40:43], v[132:135]\n"
      "v_mfma_f32_16x16x32_bf16 v[132:135], a[136:139], v[44:47], v[132:135]\n"
      "v_mfma_f32_16x16x32_bf16 v[132:135], a[140:143], v[60:63], v[132:135]\n"
      "v_cvt_pk_bf16_f32 v210, v136, v137\n"
      "v_mfma_f32_16x16x32_bf16 v[132:135], a[144:147], v[64:67], v[132:135]\n"
      "v_cvt_pk_bf16_f32 v211, v138, v139\n"
      "v_mfma_f32_16x16x32_bf16 v[132:135], a[148:151], v[68:71], v[132:135]\n"
      "v_mfma_f32_16x16x32_bf16 v[132:135], a[152:155], v[84:87], v[132:135]\n"
      "s_waitcnt lgkmcnt(3)\n"
      "v_mfma_f32_16x16x32_bf16 v[132:135], a[156:159], v[88:91], v[132:135]\n"
      "global_store_dwordx4 v252, v[220:223], s[54:55] offset:768 sc1\n"
      "v_mfma_f32_16x16x32_bf16 v[132:135], a[160:163], v[92:95], v[132:135]\n"
      "s_add_u32 m0, s17, 0x14f00\n"
      "v_mfma_f32_16x16x32_bf16 v[132:135], a[164:167], v[108:111], v[132:135]\n"
      "ds_write_b64 v244, v[210:211] offset:8\n"
      "v_mfma_f32_16x16x32_bf16 v[132:135], a[168:171], v[112:115], v[132:135]\n"
      "global_load_lds_dwordx4 v237, s[52:53] offset:256 nt\n"
      "v_mfma_f32_16x16x32_bf16 v[132:135], a[172:175], v[116:119], v[132:135]\n"
      "v_mfma_f32_16x16x32_bf16 v[136:139], a[176:179], v[36:39], v[12:15]\n"
      "v_mfma_f32_16x16x32_bf16 v[136:139], a[180:183], v[40:43], v[136:139]\n"
      "v_mfma_f32_16x16x32_bf16 v[136:139], a[184:187], v[44:47], v[136:139]\n"
      "v_mfma_f32_16x16x32_bf16 v[136:139], a[188:191], v[60:63], v[136:139]\n"
      "v_cvt_pk_bf16_f32 v212, v132, v133\n"
      "v_mfma_f32_16x16x32_bf16 v[136:139], a[192:195], v[64:67], v[136:139]\n"
      "v_cvt_pk_bf16_f32 v213, v134, v135\n"
      "v_mfma_f32_16x16x32_bf16 v[136:139], a[196:199], v[68:71], v[136:139]\n"
      "v_mfma_f32_16x16x32_bf16 v[136:139], a[200:203], v[84:87], v[136:139]\n"
      "v_mfma_f32_16x16x32_bf16 v[136:139], a[204:207], v[88:91], v[136:139]\n"
      "s_waitcnt lgkmcnt(3)\n"
      "v_mfma_f32_16x16x32_bf16 v[136:139], a[208:211], v[92:95], v[136:139]\n"
      "global_store_dwordx4 v253, v[224:227], s[54:55] offset:768 sc1\n"
      "v_mfma_f32_16x16x32_bf16 v[136:139], a[212:215], v[108:111], v[136:139]\n"
      "ds_write_b64 v245, v[212:213] offset:0\n"
      "v_mfma_f32_16x16x32_bf16 v[136:139], a[216:219], v[112:115], v[136:139]\n"
      "s_add_u32 m0, s17, 0x16e00\n"
      "v_mfma_f32_16x16x32_bf16 v[136:139], a[220:223], v[116:119], v[136:139]\n"
      "global_load_lds_dwordx4 v237, s[52:53] offset:512 nt\n"
      "v_mfma_f32_16x16x32_bf16 v[132:135], a[224:227], v[36:39], v[16:19]\n"
      "s_cbranch_vccnz LT5\n"
      "LS5:\n"
      "v_mfma_f32_16x16x32_bf16 v[132:135], a[228:231], v[40:43], v[132:135]\n"
      "v_mfma_f32_16x16x32_bf16 v[132:135], a[232:235], v[44:47], v[132:135]\n"
      "v_mfma_f32_16x16x32_bf16 v[132:135], a[236:239], v[60:63], v[132:135]\n"
      "s_memrealtime s[84:85]\n"
      "v_mfma_f32_16x16x32_bf16 v[132:135], a[0:3], v[64:67], v[132:135]\n"
      "v_cvt_pk_bf16_f32 v214, v136, v137\n"
      "v_mfma_f32_16x16x32_bf16 v[132:135], a[4:7], v[68:71], v[132:135]\n"
      "v_cvt_pk_bf16_f32 v215, v138, v139\n"
      "v_mfma_f32_16x16x32_bf16 v[132:135], a[8:11], v[84:87], v[132:135]\n"
      "v_mfma_f32_16x16x32_bf16 v[132:135], a[12:15], v[88:91], v[132:135]\n"
      "s_add_u32 s83, s83, s86\n"
      "v_mfma_f32_16x16x32_bf16 v[132:135], a[16:19], v[92:95], v[132:135]\n"
      "v_mfma_f32_16x16x32_bf16 v[132:135], a[20:23], v[108:111], v[132:135]\n"
      "v_mfma_f32_16x16x32_bf16 v[132:135], v[140:143], v[112:115], v[132:135]\n"
      "s_waitcnt lgkmcnt(0)\n"
      "s_sub_u32 s84, s84, s83\n"
      "v_mfma_f32_16x16x32_bf16 v[132:135], v[156:159], v[116:119], v[132:135]\n"
      "s_cmp_lt_i32 s84, 0\n"
      "s_cselect_b64 vcc, -1, 0\n"
      "s_waitcnt vmcnt(24)\n"
      "s_barrier\n"
      "v_mfma_f32_16x16x32_bf16 v[136:139], v[160:163], v[36:39], v[20:23]\n"
      "ds_read_b128 v[36:39], v232 offset:24576\n"
      "ds_write_b64 v245, v[214:215] offset:8\n"
      "v_mfma_f32_16x16x32_bf16 v[136:139], v[164:167], v[40:43], v[136:139]\n"
      "ds_read_b128 v[40:43], v233 offset:24576\n"
      "s_lshl_b32 s79, s93, 1\n"
      "v_mfma_f32_16x16x32_bf16 v[136:139], v[180:183], v[44:47], v[136:139]\n"
      "ds_read_b128 v[44:47], v234 offset:24576\n"
      "v_mad_i32_i24 v252, v251, s79, v252\n"
      "v_mfma_f32_16x16x32_bf16 v[136:139], v[184:187], v[60:63], v[136:139]\n"
      "ds_read_b128 v[60:63], v235 offset:24576\n"
      "v_cvt_pk_bf16_f32 v208, v132, v133\n"
      "v_mfma_f32_16x16x32_bf16 v[136:139], v[188:191], v[64:67], v[136:139]\n"
      "ds_read_b128 v[64:67], v232 offset:32768\n"
      "v_cvt_pk_bf16_f32 v209, v134, v135\n"
      "v_mfma_f32_16x16x32_bf16 v[136:139], v[204:207], v[68:71], v[136:139]\n"
      "ds_read_b128 v[68:71], v233 offset:32768\n"
      "s_mul_i32 s79, s79, s100\n"
      "v_mfma_f32_16x16x32_bf16 v[136:139], a[24:27], v[84:87], v[136:139]\n"
      "ds_read_b128 v[84:87], v234 offset:32768\n"
      "v_add_u32 v247, s79, v247\n"
      "v_mfma_f32_16x16x32_bf16 v[136:139], a[28:31], v[88:91], v[136:139]\n"
      "ds_read_b128 v[88:91], v235 offset:32768\n"
      "s_mul_i32 s79, s79, s101\n"
      "v_mfma_f32_16x16x32_bf16 v[136:139], a[32:35], v[92:95], v[136:139]\n"
      "ds_read_b128 v[92:95], v232 offset:40960\n"
      "v_add_u32 v253, s79, v253\n"
      "v_mfma_f32_16x16x32_bf16 v[136:139], a[36:39], v[108:111], v[136:139]\n"
      "ds_read_b128 v[108:111], v233 offset:40960\n"
      "v_mfma_f32_16x16x32_bf16 v[136:139], a[40:43], v[112:115], v[136:139]\n"
      "ds_read_b128 v[112:115], v234 offset:40960\n"
      "v_mfma_f32_16x16x32_bf16 v[136:139], a[44:47], v[116:119], v[136:139]\n"
      "ds_read_b128 v[116:119], v235 offset:40960\n"
      "s_waitcnt lgkmcnt(8)\n"
      "v_mfma_f32_16x16x32_bf16 v[132:135], a[240:243], v[36:39], v[0:3]\n"
      "ds_write_b64 v244, v[208:209] offset:128\n"
      "v_mfma_f32_16x16x32_bf16 v[132:135], a[244:247], v[40:43], v[132:135]\n"
      "s_add_u32 s54, s30, s36\n"
      "s_addc_u32 s55, s31, 0\n"
      "v_mfma_f32_16x16x32_bf16 v[132:135], a[248:251], v[44:47], v[132:135]\n"
      "s_cbranch_vccnz LT6\n"
      "LS6:\n"
      "v_mfma_f32_16x16x32_bf16 v[132:135], a[252:255], v[60:63], v[132:135]\n"
      "v_cvt_pk_bf16_f32 v210, v136, v137\n"
      "s_waitcnt lgkmcnt(5)\n"
      "v_mfma_f32_16x16x32_bf16 v[132:135], a[48:51], v[64:67], v[132:135]\n"
      "v_cvt_pk_bf16_f32 v211, v138, v139\n"
      "v_mfma_f32_16x16x32_bf16 v[132:135], a[52:55], v[68:71], v[132:135]\n"
      "s_mul_i32 s78, s95, s19\n"
      "v_mfma_f32_16x16x32_bf16 v[132:135], a[56:59], v[84:87], v[132:135]\n"
      "ds_read_b128 v[216:219], v246 offset:24576\n"
      "v_add_u32 v236, s78, v236\n"
      "v_mfma_f32_16x16x32_bf16 v[132:135], a[60:63], v[88:91], v[132:135]\n"
      "ds_read_b128 v[220:223], v246 offset:25600\n"
      "s_waitcnt lgkmcnt(3)\n"
      "v_mfma_f32_16x16x32_bf16 v[132:135], a[64:67], v[92:95], v[132:135]\n"
      "ds_read_b128 v[224:227], v246 offset:26624\n"
      "v_mfma_f32_16x16x32_bf16 v[132:135], a[68:71], v[108:111], v[132:135]\n"
      "ds_write_b64 v244, v[210:211] offset:136\n"
      "v_mfma_f32_16x16x32_bf16 v[132:135], a[72:75], v[112:115], v[132:135]\n"
      "s_mul_i32 s78, s78, s99\n"
      "v_mfma_f32_16x16x32_bf16 v[132:135], a[76:79], v[116:119], v[132:135]\n"
      "v_add_u32 v237, s78, v237\n"
      "v_mfma_f32_16x16x32_bf16 v[136:139], a[80:83], v[36:39], v[4:7]\n"
      "v_mfma_f32_16x16x32_bf16 v[136:139], a[84:87], v[40:43], v[136:139]\n"
      "v_mfma_f32_16x16x32_bf16 v[136:139], a[88:91], v[44:47], v[136:139]\n"
      "v_mfma_f32_16x16x32_bf16 v[136:139], a[92:95], v[60:63], v[136:139]\n"
      "v_cvt_pk_bf16_f32 v208, v132, v133\n"
      "v_mfma_f32_16x16x32_bf16 v[136:139], a[96:99], v[64:67], v[136:139]\n"
      "v_cvt_pk_bf16_f32 v209, v134, v135\n"
      "v_mfma_f32_16x16x32_bf16 v[136:139], a[100:103], v[68:71], v[136:139]\n"
      "s_waitcnt lgkmcnt(3)\n"
      "v_mfma_f32_16x16x32_bf16 v[136:139], a[104:107], v[84:87], v[136:139]\n"
      "global_store_dwordx4 v247, v[216:219], s[54:55] offset:0 sc1\n"
      "v_mfma_f32_16x16x32_bf16 v[136:139], a[108:111], v[88:91], v[136:139]\n"
      "s_add_u32 s52, s20, s38\n"
      "s_addc_u32 s53, s21, 0\n"
      "v_mfma_f32_16x16x32_bf16 v[136:139], a[112:115], v[92:95], v[136:139]\n"
      "s_add_u32 m0, s17, 0x0\n"
      "v_mfma_f32_16x16x32_bf16 v[136:139], a[116:119], v[108:111], v[136:139]\n"
      "ds_write_b64 v244, v[208:209] offset:12288\n"
      "v_mfma_f32_16x16x32_bf16 v[136:139], a[120:123], v[112:115], v[136:139]\n"
      "global_load_lds_dwordx4 v236, s[52:53] offset:0 nt\n"
      "v_mfma_f32_16x16x32_bf16 v[136:139], a[124:127], v[116:119], v[136:139]\n"
      "v_mfma_f32_16x16x32_bf16 v[132:135], a[128:131], v[36:39], v[8:11]\n"
      "s_cbranch_vccnz LT7\n"
      "LS7:\n"
      "v_mfma_f32_16x16x32_bf16 v[132:135], a[132:135], v[40:43], v[132:135]\n"
      "v_mfma_f32_16x16x32_bf16 v[132:135], a[136:139], v[44:47], v[132:135]\n"
      "v_mfma_f32_16x16x32_bf16 v[132:135], a[140:143], v[60:63], v[132:135]\n"
      "v_cvt_pk_bf16_f32 v210, v136, v137\n"
      "v_mfma_f32_16x16x32_bf16 v[132:135], a[144:147], v[64:67], v[132:135]\n"
      "v_cvt_pk_bf16_f32 v211, v138, v139\n"
      "v_mfma_f32_16x16x32_bf16 v[132:135], a[148:151], v[68:71], v[132:135]\n"
      "v_mfma_f32_16x16x32_bf16 v[132:135], a[152:155], v[84:87], v[132:135]\n"
      "s_waitcnt lgkmcnt(3)\n"
      "v_mfma_f32_16x16x32_bf16 v[132:135], a[156:159], v[88:91], v[132:135]\n"
      "global_store_dwordx4 v252, v[220:223], s[54:55] offset:0 sc1\n"
      "v_mfma_f32_16x16x32_bf16 v[132:135], a[160:163], v[92:95], v[132:135]\n"
      "s_add_u32 m0, s17, 0x1f00\n"
      "v_mfma_f32_16x16x32_bf16 v[132:135], a[164:167], v[108:111], v[132:135]\n"
      "ds_write_b64 v244, v[210:211] offset:12296\n"
      "v_mfma_f32_16x16x32_bf16 v[132:135], a[168:171], v[112:115], v[132:135]\n"
      "global_load_lds_dwordx4 v236, s[52:53] offset:256 nt\n"
      "v_mfma_f32_16x16x32_bf16 v[132:135], a[172:175], v[116:119], v[132:135]\n"
      "v_mfma_f32_16x16x32_bf16 v[136:139], a[176:179], v[36:39], v[12:15]\n"
      "v_mfma_f32_16x16x32_bf16 v[136:139], a[180:183], v[40:43], v[136:139]\n"
      "v_mfma_f32_16x16x32_bf16 v[136:139], a[184:187], v[44:47], v[136:139]\n"
      "v_mfma_f32_16x16x32_bf16 v[136:139], a[188:191], v[60:63], v[136:139]\n"
      "v_cvt_pk_bf16_f32 v212, v132, v133\n"
      "v_mfma_f32_16x16x32_bf16 v[136:139], a[192:195], v[64:67], v[136:139]\n"
      "v_cvt_pk_bf16_f32 v213, v134, v135\n"
      "v_mfma_f32_16x16x32_bf16 v[136:139], a[196:199], v[68:71], v[136:139]\n"
      "v_mfma_f32_16x16x32_bf16 v[136:139], a[200:203], v[84:87], v[136:139]\n"
      "v_mfma_f32_16x16x32_bf16 v[136:139], a[204:207], v[88:91], v[136:139]\n"
      "s_waitcnt lgkmcnt(3)\n"
      "v_mfma_f32_16x16x32_bf16 v[136:139], a[208:211], v[92:95], v[136:139]\n"
      "global_store_dwordx4 v253, v[224:227], s[54:55] offset:0 sc1\n"
      "v_mfma_f32_16x16x32_bf16 v[136:139], a[212:215], v[108:111], v[136:139]\n"
      "ds_write_b64 v245, v[212:213] offset:12288\n"
      "v_mfma_f32_16x16x32_bf16 v[136:139], a[216:219], v[112:115], v[136:139]\n"
      "s_add_u32 m0, s17, 0x3e00\n"
      "v_mfma_f32_16x16x32_bf16 v[136:139], a[220:223], v[116:119], v[136:139]\n"
      "global_load_lds_dwordx4 v236, s[52:53] offset:512 nt\n"
      "v_mfma_f32_16x16x32_bf16 v[132:135], a[224:227], v[36:39], v[16:19]\n"
      "s_cbranch_vccnz LT8\n"
      "LS8:\n"
      "v_mfma_f32_16x16x32_bf16 v[132:135], a[228:231], v[40:43], v[132:135]\n"
      "v_mfma_f32_16x16x32_bf16 v[132:135], a[232:235], v[44:47], v[132:135]\n"
      "v_mfma_f32_16x16x32_bf16 v[132:135], a[236:239], v[60:63], v[132:135]\n"
      "v_cvt_pk_bf16_f32 v214, v136, v137\n"
      "v_mfma_f32_16x16x32_bf16 v[132:135], a[0:3], v[64:67], v[132:135]\n"
      "v_cvt_pk_bf16_f32 v215, v138, v139\n"
      "v_mfma_f32_16x16x32_bf16 v[132:135], a[4:7], v[68:71], v[132:135]\n"
      "v_mfma_f32_16x16x32_bf16 v[132:135], a[8:11], v[84:87], v[132:135]\n"
      "v_mfma_f32_16x16x32_bf16 v[132:135], a[12:15], v[88:91], v[132:135]\n"
      "v_mfma_f32_16x16x32_bf16 v[132:135], a[16:19], v[92:95], v[132:135]\n"
      "v_mfma_f32_16x16x32_bf16 v[132:135], a[20:23], v[108:111], v[132:135]\n"
      "ds_write_b64 v245, v[214:215] offset:12296\n"
      "v_mfma_f32_16x16x32_bf16 v[132:135], v[140:143], v[112:115], v[132:135]\n"
      "v_mfma_f32_16x16x32_bf16 v[132:135], v[156:159], v[116:119], v[132:135]\n"
      "s_waitcnt lgkmcnt(4)\n"
      "s_barrier\n"
      "v_mfma_f32_16x16x32_bf16 v[136:139], v[160:163], v[36:39], v[20:23]\n"
      "ds_read_b128 v[36:39], v232 offset:24832\n"
      "LPD12:\n"
      "s_cbranch_execz LPD15\n"
      "v_mfma_f32_16x16x32_bf16 v[136:139], v[164:167], v[40:43], v[136:139]\n"
      "ds_read_b128 v[40:43], v233 offset:24832\n"
      "v_mfma_f32_16x16x32_bf16 v[136:139], v[180:183], v[44:47], v[136:139]\n"
      "ds_read_b128 v[44:47], v234 offset:24832\n"
      "v_mfma_f32_16x16x32_bf16 v[136:139], v[184:187], v[60:63], v[136:139]\n"
      "ds_read_b128 v[60:63], v235 offset:24832\n"
      "v_cvt_pk_bf16_f32 v208, v132, v133\n"
      "v_mfma_f32_16x16x32_bf16 v[136:139], v[188:191], v[64:67], v[136:139]\n"
      "ds_read_b128 v[64:67], v232 offset:33024\n"
      "v_cvt_pk_bf16_f32 v209, v134, v135\n"
      "v_mfma_f32_16x16x32_bf16 v[136:139], v[204:207], v[68:71], v[136:139]\n"
      "ds_read_b128 v[68:71], v233 offset:33024\n"
      "v_mfma_f32_16x16x32_bf16 v[136:139], a[24:27], v[84:87], v[136:139]\n"
      "ds_read_b128 v[84:87], v234 offset:33024\n"
      "v_mfma_f32_16x16x32_bf16 v[136:139], a[28:31], v[88:91], v[136:139]\n"
      "ds_read_b128 v[88:91], v235 offset:33024\n"
      "v_mfma_f32_16x16x32_bf16 v[136:139], a[32:35], v[92:95], v[136:139]\n"
      "ds_read_b128 v[92:95], v232 offset:41216\n"
      "v_mfma_f32_16x16x32_bf16 v[136:139], a[36:39], v[108:111], v[136:139]\n"
      "ds_read_b128 v[108:111], v233 offset:41216\n"
      "ds_write_b64 v244, v[208:209] offset:12416\n"
      "v_mfma_f32_16x16x32_bf16 v[136:139], a[40:43], v[112:115], v[136:139]\n"
      "ds_read_b128 v[112:115], v234 offset:41216\n"
      "v_mfma_f32_16x16x32_bf16 v[136:139], a[44:47], v[116:119], v[136:139]\n"
      "ds_read_b128 v[116:119], v235 offset:41216\n"
      "s_waitcnt lgkmcnt(9)\n"
      "v_mfma_f32_16x16x32_bf16 v[132:135], a[240:243], v[36:39], v[0:3]\n"
      "v_add_u32 v232, 0xc000, v232\n"
      "s_cbranch_vccnz LT9\n"
      "LS9:\n"
      "v_mfma_f32_16x16x32_bf16 v[132:135], a[244:247], v[40:43], v[132:135]\n"
      "v_add_u32 v233, 0xc000, v233\n"
      "v_mfma_f32_16x16x32_bf16 v[132:135], a[248:251], v[44:47], v[132:135]\n"
      "v_add_u32 v234, 0xc000, v234\n"
      "v_mfma_f32_16x16x32_bf16 v[132:135], a[252:255], v[60:63], v[132:135]\n"
      "v_add_u32 v235, 0xc000, v235\n"
      "s_waitcnt lgkmcnt(5)\n"
      "v_mfma_f32_16x16x32_bf16 v[132:135], a[48:51], v[64:67], v[132:135]\n"
      "v_cvt_pk_bf16_f32 v210, v136, v137\n"
      "v_mfma_f32_16x16x32_bf16 v[132:135], a[52:55], v[68:71], v[132:135]\n"
      "v_cvt_pk_bf16_f32 v211, v138, v139\n"
      "v_mfma_f32_16x16x32_bf16 v[132:135], a[56:59], v[84:87], v[132:135]\n"
      "ds_read_b128 v[216:219], v246 offset:0\n"
      "v_mfma_f32_16x16x32_bf16 v[132:135], a[60:63], v[88:91], v[132:135]\n"
      "ds_read_b128 v[220:223], v246 offset:1024\n"
      "s_waitcnt lgkmcnt(2)\n"
      "v_mfma_f32_16x16x32_bf16 v[132:135], a[64:67], v[92:95], v[132:135]\n"
      "ds_read_b128 v[224:227], v246 offset:2048\n"
      "v_mfma_f32_16x16x32_bf16 v[132:135], a[68:71], v[108:111], v[132:135]\n"
      "v_lshlrev_b32 v0, 16, v120\n"
      "v_mfma_f32_16x16x32_bf16 v[132:135], a[72:75], v[112:115], v[132:135]\n"
      "ds_write_b64 v244, v[210:211] offset:12424\n"
      "v_mfma_f32_16x16x32_bf16 v[132:135], a[76:79], v[116:119], v[132:135]\n"
      "v_and_b32 v1, 0xffff0000, v120\n"
      "v_mfma_f32_16x16x32_bf16 v[136:139], a[80:83], v[36:39], v[4:7]\n"
      "v_lshlrev_b32 v2, 16, v121\n"
      "v_mfma_f32_16x16x32_bf16 v[136:139], a[84:87], v[40:43], v[136:139]\n"
      "v_and_b32 v3, 0xffff0000, v121\n"
      "v_mfma_f32_16x16x32_bf16 v[136:139], a[88:91], v[44:47], v[136:139]\n"
      "v_mfma_f32_16x16x32_bf16 v[136:139], a[92:95], v[60:63], v[136:139]\n"
      "v_cvt_pk_bf16_f32 v208, v132, v133\n"
      "v_mfma_f32_16x16x32_bf16 v[136:139], a[96:99], v[64:67], v[136:139]\n"
      "v_cvt_pk_bf16_f32 v209, v134, v135\n"
      "v_mfma_f32_16x16x32_bf16 v[136:139], a[100:103], v[68:71], v[136:139]\n"
      "s_waitcnt lgkmcnt(3)\n"
      "v_mfma_f32_16x16x32_bf16 v[136:139], a[104:107], v[84:87], v[136:139]\n"
      "global_store_dwordx4 v247, v[216:219], s[54:55] offset:768 sc1\n"
      "v_mfma_f32_16x16x32_bf16 v[136:139], a[108:111], v[88:91], v[136:139]\n"
      "s_add_u32 m0, s17, 0x1000\n"
      "v_mfma_f32_16x16x32_bf16 v[136:139], a[112:115], v[92:95], v[136:139]\n"
      "global_load_lds_dwordx4 v237, s[52:53] offset:0 nt\n"
      "v_mfma_f32_16x16x32_bf16 v[136:139], a[116:119], v[108:111], v[136:139]\n"
      "ds_write_b64 v244, v[208:209] offset:24576\n"
      "v_mfma_f32_16x16x32_bf16 v[136:139], a[120:123], v[112:115], v[136:139]\n"
      "v_lshlrev_b32 v4, 16, v122\n"
      "v_mfma_f32_16x16x32_bf16 v[136:139], a[124:127], v[116:119], v[136:139]\n"
      "v_and_b32 v5, 0xffff0000, v122\n"
      "v_mfma_f32_16x16x32_bf16 v[132:135], a[128:131], v[36:39], v[8:11]\n"
      "v_lshlrev_b32 v6, 16, v123\n"
      "v_mfma_f32_16x16x32_bf16 v[132:135], a[132:135], v[40:43], v[132:135]\n"
      "v_and_b32 v7, 0xffff0000, v123\n"
      "v_mfma_f32_16x16x32_bf16 v[132:135], a[136:139], v[44:47], v[132:135]\n"
      "s_cbranch_vccnz LT10\n"
      "LS10:\n"
      "v_mfma_f32_16x16x32_bf16 v[132:135], a[140:143], v[60:63], v[132:135]\n"
      "v_cvt_pk_bf16_f32 v210, v136, v137\n"
      "v_mfma_f32_16x16x32_bf16 v[132:135], a[144:147], v[64:67], v[132:135]\n"
      "v_cvt_pk_bf16_f32 v211, v138, v139\n"
      "v_mfma_f32_16x16x32_bf16 v[132:135], a[148:151], v[68:71], v[132:135]\n"
      "v_lshlrev_b32 v8, 16, v124\n"
      "v_mfma_f32_16x16x32_bf16 v[132:135], a[152:155], v[84:87], v[132:135]\n"
      "s_waitcnt lgkmcnt(3)\n"
      "v_mfma_f32_16x16x32_bf16 v[132:135], a[156:159], v[88:91], v[132:135]\n"
      "global_store_dwordx4 v252, v[220:223], s[54:55] offset:768 sc1\n"
      "v_mfma_f32_16x16x32_bf16 v[132:135], a[160:163], v[92:95], v[132:135]\n"
      "s_add_u32 m0, s17, 0x2f00\n"
      "v_mfma_f32_16x16x32_bf16 v[132:135], a[164:167], v[108:111], v[132:135]\n"
      "ds_write_b64 v244, v[210:211] offset:24584\n"
      "v_mfma_f32_16x16x32_bf16 v[132:135], a[168:171], v[112:115], v[132:135]\n"
      "global_load_lds_dwordx4 v237, s[52:53] offset:256 nt\n"
      "v_mfma_f32_16x16x32_bf16 v[132:135], a[172:175], v[116:119], v[132:135]\n"
      "v_and_b32 v9, 0xffff0000, v124\n"
      "v_mfma_f32_16x16x32_bf16 v[136:139], a[176:179], v[36:39], v[12:15]\n"
      "v_lshlrev_b32 v10, 16, v125\n"
      "v_mfma_f32_16x16x32_bf16 v[136:139], a[180:183], v[40:43], v[136:139]\n"
      "v_and_b32 v11, 0xffff0000, v125\n"
      "v_mfma_f32_16x16x32_bf16 v[136:139], a[184:187], v[44:47], v[136:139]\n"
      "v_mfma_f32_16x16x32_bf16 v[136:139], a[188:191], v[60:63], v[136:139]\n"
      "v_cvt_pk_bf16_f32 v212, v132, v133\n"
      "v_mfma_f32_16x16x32_bf16 v[136:139], a[192:195], v[64:67], v[136:139]\n"
      "v_cvt_pk_bf16_f32 v213, v134, v135\n"
      "v_mfma_f32_16x16x32_bf16 v[136:139], a[196:199], v[68:71], v[136:139]\n"
      "v_lshlrev_b32 v12, 16, v126\n"
      "v_mfma_f32_16x16x32_bf16 v[136:139], a[200:203], v[84:87], v[136:139]\n"
      "v_and_b32 v13, 0xffff0000, v126\n"
      "v_mfma_f32_16x16x32_bf16 v[136:139], a[204:207], v[88:91], v[136:139]\n"
      "s_waitcnt lgkmcnt(3)\n"
      "v_mfma_f32_16x16x32_bf16 v[136:139], a[208:211], v[92:95], v[136:139]\n"
      "global_store_dwordx4 v253, v[224:227], s[54:55] offset:768 sc1\n"
      "v_mfma_f32_16x16x32_bf16 v[136:139], a[212:215], v[108:111], v[136:139]\n"
      "ds_write_b64 v245, v[212:213] offset:24576\n"
      "v_mfma_f32_16x16x32_bf16 v[136:139], a[216:219], v[112:115], v[136:139]\n"
      "s_add_u32 m0, s17, 0x4e00\n"
      "v_mfma_f32_16x16x32_bf16 v[136:139], a[220:223], v[116:119], v[136:139]\n"
      "global_load_lds_dwordx4 v237, s[52:53] offset:512 nt\n"
      "v_mfma_f32_16x16x32_bf16 v[132:135], a[224:227], v[36:39], v[16:19]\n"
      "v_lshlrev_b32 v14, 16, v127\n"
      "v_mfma_f32_16x16x32_bf16 v[132:135], a[228:231], v[40:43], v[132:135]\n"
      "v_and_b32 v15, 0xffff0000, v127\n"
      "v_mfma_f32_16x16x32_bf16 v[132:135], a[232:235], v[44:47], v[132:135]\n"
      "s_cbranch_vccnz LT11\n"
      "LS11:\n"
      "v_mfma_f32_16x16x32_bf16 v[132:135], a[236:239], v[60:63], v[132:135]\n"
      "s_memrealtime s[84:85]\n"
      "v_mfma_f32_16x16x32_bf16 v[132:135], a[0:3], v[64:67], v[132:135]\n"
      "v_cvt_pk_bf16_f32 v214, v136, v137\n"
      "v_mfma_f32_16x16x32_bf16 v[132:135], a[4:7], v[68:71], v[132:135]\n"
      "v_cvt_pk_bf16_f32 v215, v138, v139\n"
      "v_mfma_f32_16x16x32_bf16 v[132:135], a[8:11], v[84:87], v[132:135]\n"
      "v_lshlrev_b32 v16, 16, v128\n"
      "v_mfma_f32_16x16x32_bf16 v[132:135], a[12:15], v[88:91], v[132:135]\n"
      "s_add_u32 s83, s83, s86\n"
      "v_mfma_f32_16x16x32_bf16 v[132:135], a[16:19], v[92:95], v[132:135]\n"
      "v_and_b32 v17, 0xffff0000, v128\n"
      "v_mfma_f32_16x16x32_bf16 v[132:135], a[20:23], v[108:111], v[132:135]\n"
      "v_lshlrev_b32 v18, 16, v129\n"
      "v_mfma_f32_16x16x32_bf16 v[132:135], v[140:143], v[112:115], v[132:135]\n"
      "s_waitcnt lgkmcnt(0)\n"
      "s_sub_u32 s84, s84, s83\n"
      "v_mfma_f32_16x16x32_bf16 v[132:135], v[156:159], v[116:119], v[132:135]\n"
      "s_cmp_lt_i32 s84, 0\n"
      "s_cselect_b64 vcc, -1, 0\n"
      "s_waitcnt vmcnt(24)\n"
      "s_barrier\n"
      "v_mfma_f32_16x16x32_bf16 v[136:139], v[160:163], v[36:39], v[20:23]\n"
      "ds_read_b128 v[36:39], v232 offset:0\n"
      "ds_write_b64 v245, v[214:215] offset:24584\n"
      "v_mfma_f32_16x16x32_bf16 v[136:139], v[164:167], v[40:43], v[136:139]\n"
      "ds_read_b128 v[40:43], v233 offset:0\n"
      "v_mfma_f32_16x16x32_bf16 v[136:139], v[180:183], v[44:47], v[136:139]\n"
      "ds_read_b128 v[44:47], v234 offset:0\n"
      "v_mfma_f32_16x16x32_bf16 v[136:139], v[184:187], v[60:63], v[136:139]\n"
      "ds_read_b128 v[60:63], v235 offset:0\n"
      "v_cvt_pk_bf16_f32 v208, v132, v133\n"
      "v_mfma_f32_16x16x32_bf16 v[136:139], v[188:191], v[64:67], v[136:139]\n"
      "ds_read_b128 v[64:67], v232 offset:8192\n"
      "v_cvt_pk_bf16_f32 v209, v134, v135\n"
      "v_mfma_f32_16x16x32_bf16 v[136:139], v[204:207], v[68:71], v[136:139]\n"
      "ds_read_b128 v[68:71], v233 offset:8192\n"
      "v_mfma_f32_16x16x32_bf16 v[136:139], a[24:27], v[84:87], v[136:139]\n"
      "ds_read_b128 v[84:87], v234 offset:8192\n"
      "v_mfma_f32_16x16x32_bf16 v[136:139], a[28:31], v[88:91], v[136:139]\n"
      "ds_read_b128 v[88:91], v235 offset:8192\n"
      "v_mfma_f32_16x16x32_bf16 v[136:139], a[32:35], v[92:95], v[136:139]\n"
      "ds_read_b128 v[92:95], v232 offset:16384\n"
      "v_mfma_f32_16x16x32_bf16 v[136:139], a[36:39], v[108:111], v[136:139]\n"
      "ds_read_b128 v[108:111], v233 offset:16384\n"
      "v_mfma_f32_16x16x32_bf16 v[136:139], a[40:43], v[112:115], v[136:139]\n"
      "ds_read_b128 v[112:115], v234 offset:16384\n"
      "v_mfma_f32_16x16x32_bf16 v[136:139], a[44:47], v[116:119], v[136:139]\n"
      "ds_read_b128 v[116:119], v235 offset:16384\n"
      "s_waitcnt lgkmcnt(8)\n"
      "v_mfma_f32_16x16x32_bf16 v[132:135], a[240:243], v[36:39], v[0:3]\n"
      "ds_write_b64 v244, v[208:209] offset:24704\n"
      "s_cbranch_vccnz LT12\n"
      "LS12:\n"
      "v_mfma_f32_16x16x32_bf16 v[132:135], a[244:247], v[40:43], v[132:135]\n"
      "v_and_b32 v19, 0xffff0000, v129\n"
      "v_mfma_f32_16x16x32_bf16 v[132:135], a[248:251], v[44:47], v[132:135]\n"
      "v_lshlrev_b32 v20, 16, v130\n"
      "v_mfma_f32_16x16x32_bf16 v[132:135], a[252:255], v[60:63], v[132:135]\n"
      "v_and_b32 v21, 0xffff0000, v130\n"
      "s_waitcnt lgkmcnt(5)\n"
      "v_mfma_f32_16x16x32_bf16 v[132:135], a[48:51], v[64:67], v[132:135]\n"
      "v_lshlrev_b32 v22, 16, v131\n"
      "v_mfma_f32_16x16x32_bf16 v[132:135], a[52:55], v[68:71], v[132:135]\n"
      "v_and_b32 v23, 0xffff0000, v131\n"
      "v_mfma_f32_16x16x32_bf16 v[132:135], a[56:59], v[84:87], v[132:135]\n"
      "v_cvt_pk_bf16_f32 v210, v136, v137\n"
      "v_mfma_f32_16x16x32_bf16 v[132:135], a[60:63], v[88:91], v[132:135]\n"
      "v_cvt_pk_bf16_f32 v211, v138, v139\n"
      "s_waitcnt lgkmcnt(1)\n"
      "v_mfma_f32_16x16x32_bf16 v[132:135], a[64:67], v[92:95], v[132:135]\n"
      "s_add_u32 s54, s44, s36\n"
      "s_addc_u32 s55, s45, 0\n"
      "v_mfma_f32_16x16x32_bf16 v[132:135], a[68:71], v[108:111], v[132:135]\n"
      "ds_read_b128 v[216:219], v246 offset:12288\n"
      "v_mfma_f32_16x16x32_bf16 v[132:135], a[72:75], v[112:115], v[132:135]\n"
      "ds_read_b128 v[220:223], v246 offset:13312\n"
      "v_mfma_f32_16x16x32_bf16 v[132:135], a[76:79], v[116:119], v[132:135]\n"
      "ds_read_b128 v[224:227], v246 offset:14336\n"
      "v_mfma_f32_16x16x32_bf16 v[136:139], a[80:83], v[36:39], v[4:7]\n"
      "ds_write_b64 v244, v[210:211] offset:24712\n"
      "v_mfma_f32_16x16x32_bf16 v[136:139], a[84:87], v[40:43], v[136:139]\n"
      "v_mfma_f32_16x16x32_bf16 v[136:139], a[88:91], v[44:47], v[136:139]\n"
      "v_mfma_f32_16x16x32_bf16 v[136:139], a[92:95], v[60:63], v[136:139]\n"
      "v_cvt_pk_bf16_f32 v208, v132, v133\n"
      "v_mfma_f32_16x16x32_bf16 v[136:139], a[96:99], v[64:67], v[136:139]\n"
      "v_cvt_pk_bf16_f32 v209, v134, v135\n"
      "v_mfma_f32_16x16x32_bf16 v[136:139], a[100:103], v[68:71], v[136:139]\n"
      "s_waitcnt lgkmcnt(3)\n"
      "v_mfma_f32_16x16x32_bf16 v[136:139], a[104:107], v[84:87], v[136:139]\n"
      "global_store_dwordx4 v247, v[216:219], s[54:55] offset:0 sc1\n"
      "v_mfma_f32_16x16x32_bf16 v[136:139], a[108:111], v[88:91], v[136:139]\n"
      "s_add_u32 s52, s42, s38\n"
      "s_addc_u32 s53, s43, 0\n"
      "v_mfma_f32_16x16x32_bf16 v[136:139], a[112:115], v[92:95], v[136:139]\n"
      "s_add_u32 m0, s17, 0x6000\n"
      "v_mfma_f32_16x16x32_bf16 v[136:139], a[116:119], v[108:111], v[136:139]\n"
      "ds_write_b64 v244, v[208:209] offset:0\n"
      "v_mfma_f32_16x16x32_bf16 v[136:139], a[120:123], v[112:115], v[136:139]\n"
      "global_load_lds_dwordx4 v236, s[52:53] offset:0 nt\n"
      "v_mfma_f32_16x16x32_bf16 v[136:139], a[124:127], v[116:119], v[136:139]\n"
      "v_mfma_f32_16x16x32_bf16 v[132:135], a[128:131], v[36:39], v[8:11]\n"
      "s_cbranch_vccnz LT13\n"
      "LS13:\n"
      "v_mfma_f32_16x16x32_bf16 v[132:135], a[132:135], v[40:43], v[132:135]\n"
      "v_mfma_f32_16x16x32_bf16 v[132:135], a[136:139], v[44:47], v[132:135]\n"
      "v_mfma_f32_16x16x32_bf16 v[132:135], a[140:143], v[60:63], v[132:135]\n"
      "v_cvt_pk_bf16_f32 v210, v136, v137\n"
      "v_mfma_f32_16x16x32_bf16 v[132:135], a[144:147], v[64:67], v[132:135]\n"
      "v_cvt_pk_bf16_f32 v211, v138, v139\n"
      "v_mfma_f32_16x16x32_bf16 v[132:135], a[148:151], v[68:71], v[132:135]\n"
      "v_mfma_f32_16x16x32_bf16 v[132:135], a[152:155], v[84:87], v[132:135]\n"
      "s_waitcnt lgkmcnt(3)\n"
      "v_mfma_f32_16x16x32_bf16 v[132:135], a[156:159], v[88:91], v[132:135]\n"
      "global_store_dwordx4 v252, v[220:223], s[54:55] offset:0 sc1\n"
      "v_mfma_f32_16x16x32_bf16 v[132:135], a[160:163], v[92:95], v[132:135]\n"
      "s_add_u32 m0, s17, 0x7f00\n"
      "v_mfma_f32_16x16x32_bf16 v[132:135], a[164:167], v[108:111], v[132:135]\n"
      "ds_write_b64 v244, v[210:211] offset:8\n"
      "v_mfma_f32_16x16x32_bf16 v[132:135], a[168:171], v[112:115], v[132:135]\n"
      "global_load_lds_dwordx4 v236, s[52:53] offset:256 nt\n"
      "v_mfma_f32_16x16x32_bf16 v[132:135], a[172:175], v[116:119], v[132:135]\n"
      "v_mfma_f32_16x16x32_bf16 v[136:139], a[176:179], v[36:39], v[12:15]\n"
      "v_mfma_f32_16x16x32_bf16 v[136:139], a[180:183], v[40:43], v[136:139]\n"
      "v_mfma_f32_16x16x32_bf16 v[136:139], a[184:187], v[44:47], v[136:139]\n"
      "v_mfma_f32_16x16x32_bf16 v[136:139], a[188:191], v[60:63], v[136:139]\n"
      "v_cvt_pk_bf16_f32 v212, v132, v133\n"
      "v_mfma_f32_16x16x32_bf16 v[136:139], a[192:195], v[64:67], v[136:139]\n"
      "v_cvt_pk_bf16_f32 v213, v134, v135\n"
      "v_mfma_f32_16x16x32_bf16 v[136:139], a[196:199], v[68:71], v[136:139]\n"
      "v_mfma_f32_16x16x32_bf16 v[136:139], a[200:203], v[84:87], v[136:139]\n"
      "v_mfma_f32_16x16x32_bf16 v[136:139], a[204:207], v[88:91], v[136:139]\n"
      "s_waitcnt lgkmcnt(3)\n"
      "v_mfma_f32_16x16x32_bf16 v[136:139], a[208:211], v[92:95], v[136:139]\n"
      "global_store_dwordx4 v253, v[224:227], s[54:55] offset:0 sc1\n"
      "v_mfma_f32_16x16x32_bf16 v[136:139], a[212:215], v[108:111], v[136:139]\n"
      "ds_write_b64 v245, v[212:213] offset:0\n"
      "v_mfma_f32_16x16x32_bf16 v[136:139], a[216:219], v[112:115], v[136:139]\n"
      "s_add_u32 m0, s17, 0x9e00\n"
      "v_mfma_f32_16x16x32_bf16 v[136:139], a[220:223], v[116:119], v[136:139]\n"
      "global_load_lds_dwordx4 v236, s[52:53] offset:512 nt\n"
      "v_mfma_f32_16x16x32_bf16 v[132:135], a[224:227], v[36:39], v[16:19]\n"
      "s_cbranch_vccnz LT14\n"
      "LS14:\n"
      "v_mfma_f32_16x16x32_bf16 v[132:135], a[228:231], v[40:43], v[132:135]\n"
      "v_mfma_f32_16x16x32_bf16 v[132:135], a[232:235], v[44:47], v[132:135]\n"
      "v_mfma_f32_16x16x32_bf16 v[132:135], a[236:239], v[60:63], v[132:135]\n"
      "v_cvt_pk_bf16_f32 v214, v136, v137\n"
      "v_mfma_f32_16x16x32_bf16 v[132:135], a[0:3], v[64:67], v[132:135]\n"
      "v_cvt_pk_bf16_f32 v215, v138, v139\n"
      "v_mfma_f32_16x16x32_bf16 v[132:135], a[4:7], v[68:71], v[132:135]\n"
      "v_mfma_f32_16x16x32_bf16 v[132:135], a[8:11], v[84:87], v[132:135]\n"
      "v_mfma_f32_16x16x32_bf16 v[132:135], a[12:15], v[88:91], v[132:135]\n"
      "v_mfma_f32_16x16x32_bf16 v[132:135], a[16:19], v[92:95], v[132:135]\n"
      "v_mfma_f32_16x16x32_bf16 v[132:135], a[20:23], v[108:111], v[132:135]\n"
      "ds_write_b64 v245, v[214:215] offset:8\n"
      "v_mfma_f32_16x16x32_bf16 v[132:135], v[140:143], v[112:115], v[132:135]\n"
      "v_mfma_f32_16x16x32_bf16 v[132:135], v[156:159], v[116:119], v[132:135]\n"
      "s_waitcnt lgkmcnt(4)\n"
      "s_barrier\n"
      "v_mfma_f32_16x16x32_bf16 v[136:139], v[160:163], v[36:39], v[20:23]\n"
      "ds_read_b128 v[36:39], v232 offset:256\n"
      "v_mfma_f32_16x16x32_bf16 v[136:139], v[164:167], v[40:43], v[136:139]\n"
      "ds_read_b128 v[40:43], v233 offset:256\n"
      "v_mfma_f32_16x16x32_bf16 v[136:139], v[180:183], v[44:47], v[136:139]\n"
      "ds_read_b128 v[44:47], v234 offset:256\n"
      "v_mfma_f32_16x16x32_bf16 v[136:139], v[184:187], v[60:63], v[136:139]\n"
      "ds_read_b128 v[60:63], v235 offset:256\n"
      "v_cvt_pk_bf16_f32 v208, v132, v133\n"
      "v_mfma_f32_16x16x32_bf16 v[136:139], v[188:191], v[64:67], v[136:139]\n"
      "ds_read_b128 v[64:67], v232 offset:8448\n"
      "v_cvt_pk_bf16_f32 v209, v134, v135\n"
      "v_mfma_f32_16x16x32_bf16 v[136:139], v[204:207], v[68:71], v[136:139]\n"
      "ds_read_b128 v[68:71], v233 offset:8448\n"
      "v_mfma_f32_16x16x32_bf16 v[136:139], a[24:27], v[84:87], v[136:139]\n"
      "ds_read_b128 v[84:87], v234 offset:8448\n"
      "v_mfma_f32_16x16x32_bf16 v[136:139], a[28:31], v[88:91], v[136:139]\n"
      "ds_read_b128 v[88:91], v235 offset:8448\n"
      "v_mfma_f32_16x16x32_bf16 v[136:139], a[32:35], v[92:95], v[136:139]\n"
      "ds_read_b128 v[92:95], v232 offset:16640\n"
      "v_mfma_f32_16x16x32_bf16 v[136:139], a[36:39], v[108:111], v[136:139]\n"
      "ds_read_b128 v[108:111], v233 offset:16640\n"
      "v_mfma_f32_16x16x32_bf16 v[136:139], a[40:43], v[112:115], v[136:139]\n"
      "ds_read_b128 v[112:115], v234 offset:16640\n"
      "v_mfma_f32_16x16x32_bf16 v[136:139], a[44:47], v[116:119], v[136:139]\n"
      "ds_read_b128 v[116:119], v235 offset:16640\n"
      "s_waitcnt lgkmcnt(8)\n"
      "v_mfma_f32_16x16x32_bf16 v[132:135], a[240:243], v[36:39], v[0:3]\n"
      "ds_write_b64 v244, v[208:209] offset:128\n"
      "v_mfma_f32_16x16x32_bf16 v[132:135], a[244:247], v[40:43], v[132:135]\n"
      "s_cbranch_vccnz LT15\n"
      "LS15:\n"
      "v_mfma_f32_16x16x32_bf16 v[132:135], a[248:251], v[44:47], v[132:135]\n"
      "v_mfma_f32_16x16x32_bf16 v[132:135], a[252:255], v[60:63], v[132:135]\n"
      "v_cvt_pk_bf16_f32 v210, v136, v137\n"
      "s_waitcnt lgkmcnt(5)\n"
      "v_mfma_f32_16x16x32_bf16 v[132:135], a[48:51], v[64:67], v[132:135]\n"
      "v_cvt_pk_bf16_f32 v211, v138, v139\n"
      "v_mfma_f32_16x16x32_bf16 v[132:135], a[52:55], v[68:71], v[132:135]\n"
      "v_mfma_f32_16x16x32_bf16 v[132:135], a[56:59], v[84:87], v[132:135]\n"
      "ds_read_b128 v[216:219], v246 offset:24576\n"
      "v_mfma_f32_16x16x32_bf16 v[132:135], a[60:63], v[88:91], v[132:135]\n"
      "ds_read_b128 v[220:223], v246 offset:25600\n"
      "s_waitcnt lgkmcnt(3)\n"
      "v_mfma_f32_16x16x32_bf16 v[132:135], a[64:67], v[92:95], v[132:135]\n"
      "ds_read_b128 v[224:227], v246 offset:26624\n"
      "v_mfma_f32_16x16x32_bf16 v[132:135], a[68:71], v[108:111], v[132:135]\n"
      "ds_write_b64 v244, v[210:211] offset:136\n"
      "v_mfma_f32_16x16x32_bf16 v[132:135], a[72:75], v[112:115], v[132:135]\n"
      "v_mfma_f32_16x16x32_bf16 v[132:135], a[76:79], v[116:119], v[132:135]\n"
      "v_mfma_f32_16x16x32_bf16 v[136:139], a[80:83], v[36:39], v[4:7]\n"
      "v_mfma_f32_16x16x32_bf16 v[136:139], a[84:87], v[40:43], v[136:139]\n"
      "v_mfma_f32_16x16x32_bf16 v[136:139], a[88:91], v[44:47], v[136:139]\n"
      "v_mfma_f32_16x16x32_bf16 v[136:139], a[92:95], v[60:63], v[136:139]\n"
      "v_cvt_pk_bf16_f32 v208, v132, v133\n"
      "v_mfma_f32_16x16x32_bf16 v[136:139], a[96:99], v[64:67], v[136:139]\n"
      "v_cvt_pk_bf16_f32 v209, v134, v135\n"
      "v_mfma_f32_16x16x32_bf16 v[136:139], a[100:103], v[68:71], v[136:139]\n"
      "s_waitcnt lgkmcnt(3)\n"
      "v_mfma_f32_16x16x32_bf16 v[136:139], a[104:107], v[84:87], v[136:139]\n"
      "global_store_dwordx4 v247, v[216:219], s[54:55] offset:768 sc1\n"
      "v_mfma_f32_16x16x32_bf16 v[136:139], a[108:111], v[88:91], v[136:139]\n"
      "s_add_u32 m0, s17, 0x7000\n"
      "v_mfma_f32_16x16x32_bf16 v[136:139], a[112:115], v[92:95], v[136:139]\n"
      "global_load_lds_dwordx4 v237, s[52:53] offset:0 nt\n"
      "v_mfma_f32_16x16x32_bf16 v[136:139], a[116:119], v[108:111], v[136:139]\n"
      "ds_write_b64 v244, v[208:209] offset:12288\n"
      "v_mfma_f32_16x16x32_bf16 v[136:139], a[120:123], v[112:115], v[136:139]\n"
      "v_mfma_f32_16x16x32_bf16 v[136:139], a[124:127], v[116:119], v[136:139]\n"
      "v_mfma_f32_16x16x32_bf16 v[132:135], a[128:131], v[36:39], v[8:11]\n"
      "s_cbranch_vccnz LT16\n"
      "LS16:\n"
      "v_mfma_f32_16x16x32_bf16 v[132:135], a[132:135], v[40:43], v[132:135]\n"
      "v_mfma_f32_16x16x32_bf16 v[132:135], a[136:139], v[44:47], v[132:135]\n"
      "v_mfma_f32_16x16x32_bf16 v[132:135], a[140:143], v[60:63], v[132:135]\n"
      "v_cvt_pk_bf16_f32 v210, v136, v137\n"
      "v_mfma_f32_16x16x32_bf16 v[132:135], a[144:147], v[64:67], v[132:135]\n"
      "v_cvt_pk_bf16_f32 v211, v138, v139\n"
      "v_mfma_f32_16x16x32_bf16 v[132:135], a[148:151], v[68:71], v[132:135]\n"
      "v_mfma_f32_16x16x32_bf16 v[132:135], a[152:155], v[84:87], v[132:135]\n"
      "s_waitcnt lgkmcnt(3)\n"
      "v_mfma_f32_16x16x32_bf16 v[132:135], a[156:159], v[88:91], v[132:135]\n"
      "global_store_dwordx4 v252, v[220:223], s[54:55] offset:768 sc1\n"
      "v_mfma_f32_16x16x32_bf16 v[132:135], a[160:163], v[92:95], v[132:135]\n"
      "s_add_u32 m0, s17, 0x8f00\n"
      "v_mfma_f32_16x16x32_bf16 v[132:135], a[164:167], v[108:111], v[132:135]\n"
      "ds_write_b64 v244, v[210:211] offset:12296\n"
      "v_mfma_f32_16x16x32_bf16 v[132:135], a[168:171], v[112:115], v[132:135]\n"
      "global_load_lds_dwordx4 v237, s[52:53] offset:256 nt\n"
      "v_mfma_f32_16x16x32_bf16 v[132:135], a[172:175], v[116:119], v[132:135]\n"
      "v_mfma_f32_16x16x32_bf16 v[136:139], a[176:179], v[36:39], v[12:15]\n"
      "v_mfma_f32_16x16x32_bf16 v[136:139], a[180:183], v[40:43], v[136:139]\n"
      "v_mfma_f32_16x16x32_bf16 v[136:139], a[184:187], v[44:47], v[136:139]\n"
      "v_mfma_f32_16x16x32_bf16 v[136:139], a[188:191], v[60:63], v[136:139]\n"
      "v_cvt_pk_bf16_f32 v212, v132, v133\n"
      "v_mfma_f32_16x16x32_bf16 v[136:139], a[192:195], v[64:67], v[136:139]\n"
      "v_cvt_pk_bf16_f32 v213, v134, v135\n"
      "v_mfma_f32_16x16x32_bf16 v[136:139], a[196:199], v[68:71], v[136:139]\n"
      "v_mfma_f32_16x16x32_bf16 v[136:139], a[200:203], v[84:87], v[136:139]\n"
      "v_mfma_f32_16x16x32_bf16 v[136:139], a[204:207], v[88:91], v[136:139]\n"
      "s_waitcnt lgkmcnt(3)\n"
      "v_mfma_f32_16x16x32_bf16 v[136:139], a[208:211], v[92:95], v[136:139]\n"
      "global_store_dwordx4 v253, v[224:227], s[54:55] offset:768 sc1\n"
      "v_mfma_f32_16x16x32_bf16 v[136:139], a[212:215], v[108:111], v[136:139]\n"
      "ds_write_b64 v245, v[212:213] offset:12288\n"
      "v_mfma_f32_16x16x32_bf16 v[136:139], a[216:219], v[112:115], v[136:139]\n"
      "s_add_u32 m0, s17, 0xae00\n"
      "v_mfma_f32_16x16x32_bf16 v[136:139], a[220:223], v[116:119], v[136:139]\n"
      "global_load_lds_dwordx4 v237, s[52:53] offset:512 nt\n"
      "v_mfma_f32_16x16x32_bf16 v[132:135], a[224:227], v[36:39], v[16:19]\n"
      "s_cbranch_vccnz LT17\n"
      "LS17:\n"
      "v_mfma_f32_16x16x32_bf16 v[132:135], a[228:231], v[40:43], v[132:135]\n"
      "v_mfma_f32_16x16x32_bf16 v[132:135], a[232:235], v[44:47], v[132:135]\n"
      "v_mfma_f32_16x16x32_bf16 v[132:135], a[236:239], v[60:63], v[132:135]\n"
      "s_memrealtime s[84:85]\n"
      "v_mfma_f32_16x16x32_bf16 v[132:135], a[0:3], v[64:67], v[132:135]\n"
      "v_cvt_pk_bf16_f32 v214, v136, v137\n"
      "v_mfma_f32_16x16x32_bf16 v[132:135], a[4:7], v[68:71], v[132:135]\n"
      "v_cvt_pk_bf16_f32 v215, v138, v139\n"
      "v_mfma_f32_16x16x32_bf16 v[132:135], a[8:11], v[84:87], v[132:135]\n"
      "v_mfma_f32_16x16x32_bf16 v[132:135], a[12:15], v[88:91], v[132:135]\n"
      "s_add_u32 s83, s83, s86\n"
      "v_mfma_f32_16x16x32_bf16 v[132:135], a[16:19], v[92:95], v[132:135]\n"
      "v_mfma_f32_16x16x32_bf16 v[132:135], a[20:23], v[108:111], v[132:135]\n"
      "v_mfma_f32_16x16x32_bf16 v[132:135], v[140:143], v[112:115], v[132:135]\n"
      "s_waitcnt lgkmcnt(0)\n"
      "s_sub_u32 s84, s84, s83\n"
      "v_mfma_f32_16x16x32_bf16 v[132:135], v[156:159], v[116:119], v[132:135]\n"
      "s_cmp_lt_i32 s84, 0\n"
      "s_cselect_b64 vcc, -1, 0\n"
      "s_waitcnt vmcnt(24)\n"
      "s_barrier\n"
      "v_mfma_f32_16x16x32_bf16 v[136:139], v[160:163], v[36:39], v[20:23]\n"
      "ds_read_b128 v[36:39], v232 offset:24576\n"
      "ds_write_b64 v245, v[214:215] offset:12296\n"
      "v_mfma_f32_16x16x32_bf16 v[136:139], v[164:167], v[40:43], v[136:139]\n"
      "ds_read_b128 v[40:43], v233 offset:24576\n"
      "s_lshl_b32 s79, s94, 1\n"
      "v_mfma_f32_16x16x32_bf16 v[136:139], v[180:183], v[44:47], v[136:139]\n"
      "ds_read_b128 v[44:47], v234 offset:24576\n"
      "v_mad_i32_i24 v252, v251, s79, v252\n"
      "v_mfma_f32_16x16x32_bf16 v[136:139], v[184:187], v[60:63], v[136:139]\n"
      "ds_read_b128 v[60:63], v235 offset:24576\n"
      "v_cvt_pk_bf16_f32 v208, v132, v133\n"
      "v_mfma_f32_16x16x32_bf16 v[136:139], v[188:191], v[64:67], v[136:139]\n"
      "ds_read_b128 v[64:67], v232 offset:32768\n"
      "v_cvt_pk_bf16_f32 v209, v134, v135\n"
      "v_mfma_f32_16x16x32_bf16 v[136:139], v[204:207], v[68:71], v[136:139]\n"
      "ds_read_b128 v[68:71], v233 offset:32768\n"
      "s_mul_i32 s79, s79, s100\n"
      "v_mfma_f32_16x16x32_bf16 v[136:139], a[24:27], v[84:87], v[136:139]\n"
      "ds_read_b128 v[84:87], v234 offset:32768\n"
      "v_add_u32 v247, s79, v247\n"
      "v_mfma_f32_16x16x32_bf16 v[136:139], a[28:31], v[88:91], v[136:139]\n"
      "ds_read_b128 v[88:91], v235 offset:32768\n"
      "s_mul_i32 s79, s79, s101\n"
      "v_mfma_f32_16x16x32_bf16 v[136:139], a[32:35], v[92:95], v[136:139]\n"
      "ds_read_b128 v[92:95], v232 offset:40960\n"
      "v_add_u32 v253, s79, v253\n"
      "v_mfma_f32_16x16x32_bf16 v[136:139], a[36:39], v[108:111], v[136:139]\n"
      "ds_read_b128 v[108:111], v233 offset:40960\n"
      "v_mfma_f32_16x16x32_bf16 v[136:139], a[40:43], v[112:115], v[136:139]\n"
      "ds_read_b128 v[112:115], v234 offset:40960\n"
      "v_mfma_f32_16x16x32_bf16 v[136:139], a[44:47], v[116:119], v[136:139]\n"
      "ds_read_b128 v[116:119], v235 offset:40960\n"
      "s_waitcnt lgkmcnt(8)\n"
      "v_mfma_f32_16x16x32_bf16 v[132:135], a[240:243], v[36:39], v[0:3]\n"
      "ds_write_b64 v244, v[208:209] offset:12416\n"
      "v_mfma_f32_16x16x32_bf16 v[132:135], a[244:247], v[40:43], v[132:135]\n"
      "s_add_u32 s54, s30, s37\n"
      "s_addc_u32 s55, s31, 0\n"
      "v_mfma_f32_16x16x32_bf16 v[132:135], a[248:251], v[44:47], v[132:135]\n"
      "s_cbranch_vccnz LT18\n"
      "LS18:\n"
      "v_mfma_f32_16x16x32_bf16 v[132:135], a[252:255], v[60:63], v[132:135]\n"
      "v_cvt_pk_bf16_f32 v210, v136, v137\n"
      "s_waitcnt lgkmcnt(5)\n"
      "v_mfma_f32_16x16x32_bf16 v[132:135], a[48:51], v[64:67], v[132:135]\n"
      "v_cvt_pk_bf16_f32 v211, v138, v139\n"
      "v_mfma_f32_16x16x32_bf16 v[132:135], a[52:55], v[68:71], v[132:135]\n"
      "s_mul_i32 s78, s96, s19\n"
      "v_mfma_f32_16x16x32_bf16 v[132:135], a[56:59], v[84:87], v[132:135]\n"
      "ds_read_b128 v[216:219], v246 offset:0\n"
      "v_add_u32 v236, s78, v236\n"
      "v_mfma_f32_16x16x32_bf16 v[132:135], a[60:63], v[88:91], v[132:135]\n"
      "ds_read_b128 v[220:223], v246 offset:1024\n"
      "s_waitcnt lgkmcnt(3)\n"
      "v_mfma_f32_16x16x32_bf16 v[132:135], a[64:67], v[92:95], v[132:135]\n"
      "ds_read_b128 v[224:227], v246 offset:2048\n"
      "v_mfma_f32_16x16x32_bf16 v[132:135], a[68:71], v[108:111], v[132:135]\n"
      "ds_write_b64 v244, v[210:211] offset:12424\n"
      "v_mfma_f32_16x16x32_bf16 v[132:135], a[72:75], v[112:115], v[132:135]\n"
      "s_mul_i32 s78, s78, s99\n"
      "v_mfma_f32_16x16x32_bf16 v[132:135], a[76:79], v[116:119], v[132:135]\n"
      "v_add_u32 v237, s78, v237\n"
      "v_mfma_f32_16x16x32_bf16 v[136:139], a[80:83], v[36:39], v[4:7]\n"
      "v_mfma_f32_16x16x32_bf16 v[136:139], a[84:87], v[40:43], v[136:139]\n"
      "v_mfma_f32_16x16x32_bf16 v[136:139], a[88:91], v[44:47], v[136:139]\n"
      "v_mfma_f32_16x16x32_bf16 v[136:139], a[92:95], v[60:63], v[136:139]\n"
      "v_cvt_pk_bf16_f32 v208, v132, v133\n"
      "v_mfma_f32_16x16x32_bf16 v[136:139], a[96:99], v[64:67], v[136:139]\n"
      "v_cvt_pk_bf16_f32 v209, v134, v135\n"
      "v_mfma_f32_16x16x32_bf16 v[136:139], a[100:103], v[68:71], v[136:139]\n"
      "s_waitcnt lgkmcnt(3)\n"
      "v_mfma_f32_16x16x32_bf16 v[136:139], a[104:107], v[84:87], v[136:139]\n"
      "global_store_dwordx4 v247, v[216:219], s[54:55] offset:0 sc1\n"
      "v_mfma_f32_16x16x32_bf16 v[136:139], a[108:111], v[88:91], v[136:139]\n"
      "s_add_u32 s52, s20, s39\n"
      "s_addc_u32 s53, s21, 0\n"
      "v_mfma_f32_16x16x32_bf16 v[136:139], a[112:115], v[92:95], v[136:139]\n"
      "s_add_u32 m0, s17, 0xc000\n"
      "v_mfma_f32_16x16x32_bf16 v[136:139], a[116:119], v[108:111], v[136:139]\n"
      "ds_write_b64 v244, v[208:209] offset:24576\n"
      "v_mfma_f32_16x16x32_bf16 v[136:139], a[120:123], v[112:115], v[136:139]\n"
      "global_load_lds_dwordx4 v236, s[52:53] offset:0 nt\n"
      "v_mfma_f32_16x16x32_bf16 v[136:139], a[124:127], v[116:119], v[136:139]\n"
      "v_mfma_f32_16x16x32_bf16 v[132:135], a[128:131], v[36:39], v[8:11]\n"
      "s_cbranch_vccnz LT19\n"
      "LS19:\n"
      "v_mfma_f32_16x16x32_bf16 v[132:135], a[132:135], v[40:43], v[132:135]\n"
      "v_mfma_f32_16x16x32_bf16 v[132:135], a[136:139], v[44:47], v[132:135]\n"
      "v_mfma_f32_16x16x32_bf16 v[132:135], a[140:143], v[60:63], v[132:135]\n"
      "v_cvt_pk_bf16_f32 v210, v136, v137\n"
      "v_mfma_f32_16x16x32_bf16 v[132:135], a[144:147], v[64:67], v[132:135]\n"
      "v_cvt_pk_bf16_f32 v211, v138, v139\n"
      "v_mfma_f32_16x16x32_bf16 v[132:135], a[148:151], v[68:71], v[132:135]\n"
      "v_mfma_f32_16x16x32_bf16 v[132:135], a[152:155], v[84:87], v[132:135]\n"
      "s_waitcnt lgkmcnt(3)\n"
      "v_mfma_f32_16x16x32_bf16 v[132:135], a[156:159], v[88:91], v[132:135]\n"
      "global_store_dwordx4 v252, v[220:223], s[54:55] offset:0 sc1\n"
      "v_mfma_f32_16x16x32_bf16 v[132:135], a[160:163], v[92:95], v[132:135]\n"
      "s_add_u32 m0, s17, 0xdf00\n"
      "v_mfma_f32_16x16x32_bf16 v[132:135], a[164:167], v[108:111], v[132:135]\n"
      "ds_write_b64 v244, v[210:211] offset:24584\n"
      "v_mfma_f32_16x16x32_bf16 v[132:135], a[168:171], v[112:115], v[132:135]\n"
      "global_load_lds_dwordx4 v236, s[52:53] offset:256 nt\n"
      "v_mfma_f32_16x16x32_bf16 v[132:135], a[172:175], v[116:119], v[132:135]\n"
      "v_mfma_f32_16x16x32_bf16 v[136:139], a[176:179], v[36:39], v[12:15]\n"
      "v_mfma_f32_16x16x32_bf16 v[136:139], a[180:183], v[40:43], v[136:139]\n"
      "v_mfma_f32_16x16x32_bf16 v[136:139], a[184:187], v[44:47], v[136:139]\n"
      "v_mfma_f32_16x16x32_bf16 v[136:139], a[188:191], v[60:63], v[136:139]\n"
      "v_cvt_pk_bf16_f32 v212, v132, v133\n"
      "v_mfma_f32_16x16x32_bf16 v[136:139], a[192:195], v[64:67], v[136:139]\n"
      "v_cvt_pk_bf16_f32 v213, v134, v135\n"
      "v_mfma_f32_16x16x32_bf16 v[136:139], a[196:199], v[68:71], v[136:139]\n"
      "v_mfma_f32_16x16x32_bf16 v[136:139], a[200:203], v[84:87], v[136:139]\n"
      "v_mfma_f32_16x16x32_bf16 v[136:139], a[204:207], v[88:91], v[136:139]\n"
      "s_waitcnt lgkmcnt(3)\n"
      "v_mfma_f32_16x16x32_bf16 v[136:139], a[208:211], v[92:95], v[136:139]\n"
      "global_store_dwordx4 v253, v[224:227], s[54:55] offset:0 sc1\n"
      "v_mfma_f32_16x16x32_bf16 v[136:139], a[212:215], v[108:111], v[136:139]\n"
      "ds_write_b64 v245, v[212:213] offset:24576\n"
      "v_mfma_f32_16x16x32_bf16 v[136:139], a[216:219], v[112:115], v[136:139]\n"
      "s_add_u32 m0, s17, 0xfe00\n"
      "v_mfma_f32_16x16x32_bf16 v[136:139], a[220:223], v[116:119], v[136:139]\n"
      "global_load_lds_dwordx4 v236, s[52:53] offset:512 nt\n"
      "v_mfma_f32_16x16x32_bf16 v[132:135], a[224:227], v[36:39], v[16:19]\n"
      "s_cbranch_vccnz LT20\n"
      "LS20:\n"
      "v_mfma_f32_16x16x32_bf16 v[132:135], a[228:231], v[40:43], v[132:135]\n"
      "v_mfma_f32_16x16x32_bf16 v[132:135], a[232:235], v[44:47], v[132:135]\n"
      "v_mfma_f32_16x16x32_bf16 v[132:135], a[236:239], v[60:63], v[132:135]\n"
      "v_cvt_pk_bf16_f32 v214, v136, v137\n"
      "v_mfma_f32_16x16x32_bf16 v[132:135], a[0:3], v[64:67], v[132:135]\n"
      "v_cvt_pk_bf16_f32 v215, v138, v139\n"
      "v_mfma_f32_16x16x32_bf16 v[132:135], a[4:7], v[68:71], v[132:135]\n"
      "v_mfma_f32_16x16x32_bf16 v[132:135], a[8:11], v[84:87], v[132:135]\n"
      "v_mfma_f32_16x16x32_bf16 v[132:135], a[12:15], v[88:91], v[132:135]\n"
      "v_mfma_f32_16x16x32_bf16 v[132:135], a[16:19], v[92:95], v[132:135]\n"
      "v_mfma_f32_16x16x32_bf16 v[132:135], a[20:23], v[108:111], v[132:135]\n"
      "ds_write_b64 v245, v[214:215] offset:24584\n"
      "LPD13:\n"
      "s_cbranch_execz LPD16\n"
      "v_mfma_f32_16x16x32_bf16 v[132:135], v[140:143], v[112:115], v[132:135]\n"
      "v_mfma_f32_16x16x32_bf16 v[132:135], v[156:159], v[116:119], v[132:135]\n"
      "s_waitcnt lgkmcnt(4)\n"
      "s_barrier\n"
      "v_mfma_f32_16x16x32_bf16 v[136:139], v[160:163], v[36:39], v[20:23]\n"
      "ds_read_b128 v[36:39], v232 offset:24832\n"
      "v_mfma_f32_16x16x32_bf16 v[136:139], v[164:167], v[40:43], v[136:139]\n"
      "ds_read_b128 v[40:43], v233 offset:24832\n"
      "v_mfma_f32_16x16x32_bf16 v[136:139], v[180:183], v[44:47], v[136:139]\n"
      "ds_read_b128 v[44:47], v234 offset:24832\n"
      "v_mfma_f32_16x16x32_bf16 v[136:139], v[184:187], v[60:63], v[136:139]\n"
      "ds_read_b128 v[60:63], v235 offset:24832\n"
      "v_cvt_pk_bf16_f32 v208, v132, v133\n"
      "v_mfma_f32_16x16x32_bf16 v[136:139], v[188:191], v[64:67], v[136:139]\n"
      "ds_read_b128 v[64:67], v232 offset:33024\n"
      "v_cvt_pk_bf16_f32 v209, v134, v135\n"
      "v_mfma_f32_16x16x32_bf16 v[136:139], v[204:207], v[68:71], v[136:139]\n"
      "ds_read_b128 v[68:71], v233 offset:33024\n"
      "v_mfma_f32_16x16x32_bf16 v[136:139], a[24:27], v[84:87], v[136:139]\n"
      "ds_read_b128 v[84:87], v234 offset:33024\n"
      "v_mfma_f32_16x16x32_bf16 v[136:139], a[28:31], v[88:91], v[136:139]\n"
      "ds_read_b128 v[88:91], v235 offset:33024\n"
      "v_mfma_f32_16x16x32_bf16 v[136:139], a[32:35], v[92:95], v[136:139]\n"
      "ds_read_b128 v[92:95], v232 offset:41216\n"
      "v_mfma_f32_16x16x32_bf16 v[136:139], a[36:39], v[108:111], v[136:139]\n"
      "ds_read_b128 v[108:111], v233 offset:41216\n"
      "ds_write_b64 v244, v[208:209] offset:24704\n"
      "v_mfma_f32_16x16x32_bf16 v[136:139], a[40:43], v[112:115], v[136:139]\n"
      "ds_read_b128 v[112:115], v234 offset:41216\n"
      "v_mfma_f32_16x16x32_bf16 v[136:139], a[44:47], v[116:119], v[136:139]\n"
      "ds_read_b128 v[116:119], v235 offset:41216\n"
      "s_waitcnt lgkmcnt(9)\n"
      "v_mfma_f32_16x16x32_bf16 v[132:135], a[240:243], v[36:39], v[0:3]\n"
      "v_add_u32 v232, 0xffff4000, v232\n"
      "s_cbranch_vccnz LT21\n"
      "LS21:\n"
      "v_mfma_f32_16x16x32_bf16 v[132:135], a[244:247], v[40:43], v[132:135]\n"
      "v_add_u32 v233, 0xffff4000, v233\n"
      "v_mfma_f32_16x16x32_bf16 v[132:135], a[248:251], v[44:47], v[132:135]\n"
      "v_add_u32 v234, 0xffff4000, v234\n"
      "v_mfma_f32_16x16x32_bf16 v[132:135], a[252:255], v[60:63], v[132:135]\n"
      "v_add_u32 v235, 0xffff4000, v235\n"
      "s_waitcnt lgkmcnt(5)\n"
      "v_mfma_f32_16x16x32_bf16 v[132:135], a[48:51], v[64:67], v[132:135]\n"
      "v_cvt_pk_bf16_f32 v210, v136, v137\n"
      "v_mfma_f32_16x16x32_bf16 v[132:135], a[52:55], v[68:71], v[132:135]\n"
      "v_cvt_pk_bf16_f32 v211, v138, v139\n"
      "v_mfma_f32_16x16x32_bf16 v[132:135], a[56:59], v[84:87], v[132:135]\n"
      "ds_read_b128 v[216:219], v246 offset:12288\n"
      "v_mfma_f32_16x16x32_bf16 v[132:135], a[60:63], v[88:91], v[132:135]\n"
      "ds_read_b128 v[220:223], v246 offset:13312\n"
      "s_waitcnt lgkmcnt(2)\n"
      "v_mfma_f32_16x16x32_bf16 v[132:135], a[64:67], v[92:95], v[132:135]\n"
      "ds_read_b128 v[224:227], v246 offset:14336\n"
      "v_mfma_f32_16x16x32_bf16 v[132:135], a[68:71], v[108:111], v[132:135]\n"
      "v_lshlrev_b32 v0, 16, v144\n"
      "v_mfma_f32_16x16x32_bf16 v[132:135], a[72:75], v[112:115], v[132:135]\n"
      "ds_write_b64 v244, v[210:211] offset:24712\n"
      "v_mfma_f32_16x16x32_bf16 v[132:135], a[76:79], v[116:119], v[132:135]\n"
      "v_and_b32 v1, 0xffff0000, v144\n"
      "v_mfma_f32_16x16x32_bf16 v[136:139], a[80:83], v[36:39], v[4:7]\n"
      "v_lshlrev_b32 v2, 16, v145\n"
      "v_mfma_f32_16x16x32_bf16 v[136:139], a[84:87], v[40:43], v[136:139]\n"
      "v_and_b32 v3, 0xffff0000, v145\n"
      "v_mfma_f32_16x16x32_bf16 v[136:139], a[88:91], v[44:47], v[136:139]\n"
      "v_mfma_f32_16x16x32_bf16 v[136:139], a[92:95], v[60:63], v[136:139]\n"
      "v_cvt_pk_bf16_f32 v208, v132, v133\n"
      "v_mfma_f32_16x16x32_bf16 v[136:139], a[96:99], v[64:67], v[136:139]\n"
      "v_cvt_pk_bf16_f32 v209, v134, v135\n"
      "v_mfma_f32_16x16x32_bf16 v[136:139], a[100:103], v[68:71], v[136:139]\n"
      "s_waitcnt lgkmcnt(3)\n"
      "v_mfma_f32_16x16x32_bf16 v[136:139], a[104:107], v[84:87], v[136:139]\n"
      "global_store_dwordx4 v247, v[216:219], s[54:55] offset:768 sc1\n"
      "v_mfma_f32_16x16x32_bf16 v[136:139], a[108:111], v[88:91], v[136:139]\n"
      "s_add_u32 m0, s17, 0xd000\n"
      "v_mfma_f32_16x16x32_bf16 v[136:139], a[112:115], v[92:95], v[136:139]\n"
      "global_load_lds_dwordx4 v237, s[52:53] offset:0 nt\n"
      "v_mfma_f32_16x16x32_bf16 v[136:139], a[116:119], v[108:111], v[136:139]\n"
      "ds_write_b64 v244, v[208:209] offset:0\n"
      "v_mfma_f32_16x16x32_bf16 v[136:139], a[120:123], v[112:115], v[136:139]\n"
      "v_lshlrev_b32 v4, 16, v146\n"
      "v_mfma_f32_16x16x32_bf16 v[136:139], a[124:127], v[116:119], v[136:139]\n"
      "v_and_b32 v5, 0xffff0000, v146\n"
      "v_mfma_f32_16x16x32_bf16 v[132:135], a[128:131], v[36:39], v[8:11]\n"
      "v_lshlrev_b32 v6, 16, v147\n"
      "v_mfma_f32_16x16x32_bf16 v[132:135], a[132:135], v[40:43], v[132:135]\n"
      "v_and_b32 v7, 0xffff0000, v147\n"
      "v_mfma_f32_16x16x32_bf16 v[132:135], a[136:139], v[44:47], v[132:135]\n"
      "s_cbranch_vccnz LT22\n"
      "LS22:\n"
      "v_mfma_f32_16x16x32_bf16 v[132:135], a[140:143], v[60:63], v[132:135]\n"
      "v_cvt_pk_bf16_f32 v210, v136, v137\n"
      "v_mfma_f32_16x16x32_bf16 v[132:135], a[144:147], v[64:67], v[132:135]\n"
      "v_cvt_pk_bf16_f32 v211, v138, v139\n"
      "v_mfma_f32_16x16x32_bf16 v[132:135], a[148:151], v[68:71], v[132:135]\n"
      "v_lshlrev_b32 v8, 16, v148\n"
      "v_mfma_f32_16x16x32_bf16 v[132:135], a[152:155], v[84:87], v[132:135]\n"
      "s_waitcnt lgkmcnt(3)\n"
      "v_mfma_f32_16x16x32_bf16 v[132:135], a[156:159], v[88:91], v[132:135]\n"
      "global_store_dwordx4 v252, v[220:223], s[54:55] offset:768 sc1\n"
      "v_mfma_f32_16x16x32_bf16 v[132:135], a[160:163], v[92:95], v[132:135]\n"
      "s_add_u32 m0, s17, 0xef00\n"
      "v_mfma_f32_16x16x32_bf16 v[132:135], a[164:167], v[108:111], v[132:135]\n"
      "ds_write_b64 v244, v[210:211] offset:8\n"
      "v_mfma_f32_16x16x32_bf16 v[132:135], a[168:171], v[112:115], v[132:135]\n"
      "global_load_lds_dwordx4 v237, s[52:53] offset:256 nt\n"
      "v_mfma_f32_16x16x32_bf16 v[132:135], a[172:175], v[116:119], v[132:135]\n"
      "v_and_b32 v9, 0xffff0000, v148\n"
      "v_mfma_f32_16x16x32_bf16 v[136:139], a[176:179], v[36:39], v[12:15]\n"
      "v_lshlrev_b32 v10, 16, v149\n"
      "v_mfma_f32_16x16x32_bf16 v[136:139], a[180:183], v[40:43], v[136:139]\n"
      "v_and_b32 v11, 0xffff0000, v149\n"
      "v_mfma_f32_16x16x32_bf16 v[136:139], a[184:187], v[44:47], v[136:139]\n"
      "v_mfma_f32_16x16x32_bf16 v[136:139], a[188:191], v[60:63], v[136:139]\n"
      "v_cvt_pk_bf16_f32 v212, v132, v133\n"
      "v_mfma_f32_16x16x32_bf16 v[136:139], a[192:195], v[64:67], v[136:139]\n"
      "v_cvt_pk_bf16_f32 v213, v134, v135\n"
      "v_mfma_f32_16x16x32_bf16 v[136:139], a[196:199], v[68:71], v[136:139]\n"
      "v_lshlrev_b32 v12, 16, v150\n"
      "v_mfma_f32_16x16x32_bf16 v[136:139], a[200:203], v[84:87], v[136:139]\n"
      "v_and_b32 v13, 0xffff0000, v150\n"
      "v_mfma_f32_16x16x32_bf16 v[136:139], a[204:207], v[88:91], v[136:139]\n"
      "s_waitcnt lgkmcnt(3)\n"
      "v_mfma_f32_16x16x32_bf16 v[136:139], a[208:211], v[92:95], v[136:139]\n"
      "global_store_dwordx4 v253, v[224:227], s[54:55] offset:768 sc1\n"
      "v_mfma_f32_16x16x32_bf16 v[136:139], a[212:215], v[108:111], v[136:139]\n"
      "ds_write_b64 v245, v[212:213] offset:0\n"
      "v_mfma_f32_16x16x32_bf16 v[136:139], a[216:219], v[112:115], v[136:139]\n"
      "s_add_u32 m0, s17, 0x10e00\n"
      "v_mfma_f32_16x16x32_bf16 v[136:139], a[220:223], v[116:119], v[136:139]\n"
      "global_load_lds_dwordx4 v237, s[52:53] offset:512 nt\n"
      "v_mfma_f32_16x16x32_bf16 v[132:135], a[224:227], v[36:39], v[16:19]\n"
      "v_lshlrev_b32 v14, 16, v151\n"
      "v_mfma_f32_16x16x32_bf16 v[132:135], a[228:231], v[40:43], v[132:135]\n"
      "v_and_b32 v15, 0xffff0000, v151\n"
      "v_mfma_f32_16x16x32_bf16 v[132:135], a[232:235], v[44:47], v[132:135]\n"
      "s_cbranch_vccnz LT23\n"
      "LS23:\n"
      "v_mfma_f32_16x16x32_bf16 v[132:135], a[236:239], v[60:63], v[132:135]\n"
      "s_memrealtime s[84:85]\n"
      "v_mfma_f32_16x16x32_bf16 v[132:135], a[0:3], v[64:67], v[132:135]\n"
      "v_cvt_pk_bf16_f32 v214, v136, v137\n"
      "v_mfma_f32_16x16x32_bf16 v[132:135], a[4:7], v[68:71], v[132:135]\n"
      "v_cvt_pk_bf16_f32 v215, v138, v139\n"
      "v_mfma_f32_16x16x32_bf16 v[132:135], a[8:11], v[84:87], v[132:135]\n"
      "v_lshlrev_b32 v16, 16, v152\n"
      "v_mfma_f32_16x16x32_bf16 v[132:135], a[12:15], v[88:91], v[132:135]\n"
      "s_add_u32 s83, s83, s86\n"
      "v_mfma_f32_16x16x32_bf16 v[132:135], a[16:19], v[92:95], v[132:135]\n"
      "v_and_b32 v17, 0xffff0000, v152\n"
      "v_mfma_f32_16x16x32_bf16 v[132:135], a[20:23], v[108:111], v[132:135]\n"
      "v_lshlrev_b32 v18, 16, v153\n"
      "v_mfma_f32_16x16x32_bf16 v[132:135], v[140:143], v[112:115], v[132:135]\n"
      "s_waitcnt lgkmcnt(0)\n"
      "s_sub_u32 s84, s84, s83\n"
      "v_mfma_f32_16x16x32_bf16 v[132:135], v[156:159], v[116:119], v[132:135]\n"
      "s_cmp_lt_i32 s84, 0\n"
      "s_cselect_b64 vcc, -1, 0\n"
      "s_waitcnt vmcnt(24)\n"
      "s_barrier\n"
      "v_mfma_f32_16x16x32_bf16 v[136:139], v[160:163], v[36:39], v[20:23]\n"
      "ds_read_b128 v[36:39], v232 offset:0\n"
      "ds_write_b64 v245, v[214:215] offset:8\n"
      "v_mfma_f32_16x16x32_bf16 v[136:139], v[164:167], v[40:43], v[136:139]\n"
      "ds_read_b128 v[40:43], v233 offset:0\n"
      "v_mfma_f32_16x16x32_bf16 v[136:139], v[180:183], v[44:47], v[136:139]\n"
      "ds_read_b128 v[44:47], v234 offset:0\n"
      "v_mfma_f32_16x16x32_bf16 v[136:139], v[184:187], v[60:63], v[136:139]\n"
      "ds_read_b128 v[60:63], v235 offset:0\n"
      "v_cvt_pk_bf16_f32 v208, v132, v133\n"
      "v_mfma_f32_16x16x32_bf16 v[136:139], v[188:191], v[64:67], v[136:139]\n"
      "ds_read_b128 v[64:67], v232 offset:8192\n"
      "v_cvt_pk_bf16_f32 v209, v134, v135\n"
      "v_mfma_f32_16x16x32_bf16 v[136:139], v[204:207], v[68:71], v[136:139]\n"
      "ds_read_b128 v[68:71], v233 offset:8192\n"
      "v_mfma_f32_16x16x32_bf16 v[136:139], a[24:27], v[84:87], v[136:139]\n"
      "ds_read_b128 v[84:87], v234 offset:8192\n"
      "v_mfma_f32_16x16x32_bf16 v[136:139], a[28:31], v[88:91], v[136:139]\n"
      "ds_read_b128 v[88:91], v235 offset:8192\n"
      "v_mfma_f32_16x16x32_bf16 v[136:139], a[32:35], v[92:95], v[136:139]\n"
      "ds_read_b128 v[92:95], v232 offset:16384\n"
      "v_mfma_f32_16x16x32_bf16 v[136:139], a[36:39], v[108:111], v[136:139]\n"
      "ds_read_b128 v[108:111], v233 offset:16384\n"
      "v_mfma_f32_16x16x32_bf16 v[136:139], a[40:43], v[112:115], v[136:139]\n"
      "ds_read_b128 v[112:115], v234 offset:16384\n"
      "v_mfma_f32_16x16x32_bf16 v[136:139], a[44:47], v[116:119], v[136:139]\n"
      "ds_read_b128 v[116:119], v235 offset:16384\n"
      "s_waitcnt lgkmcnt(8)\n"
      "v_mfma_f32_16x16x32_bf16 v[132:135], a[240:243], v[36:39], v[0:3]\n"
      "ds_write_b64 v244, v[208:209] offset:128\n"
      "s_cbranch_vccnz LT24\n"
      "LS24:\n"
      "v_mfma_f32_16x16x32_bf16 v[132:135], a[244:247], v[40:43], v[132:135]\n"
      "v_and_b32 v19, 0xffff0000, v153\n"
      "v_mfma_f32_16x16x32_bf16 v[132:135], a[248:251], v[44:47], v[132:135]\n"
      "v_lshlrev_b32 v20, 16, v154\n"
      "v_mfma_f32_16x16x32_bf16 v[132:135], a[252:255], v[60:63], v[132:135]\n"
      "v_and_b32 v21, 0xffff0000, v154\n"
      "s_waitcnt lgkmcnt(5)\n"
      "v_mfma_f32_16x16x32_bf16 v[132:135], a[48:51], v[64:67], v[132:135]\n"
      "v_lshlrev_b32 v22, 16, v155\n"
      "v_mfma_f32_16x16x32_bf16 v[132:135], a[52:55], v[68:71], v[132:135]\n"
      "v_and_b32 v23, 0xffff0000, v155\n"
      "v_mfma_f32_16x16x32_bf16 v[132:135], a[56:59], v[84:87], v[132:135]\n"
      "v_cvt_pk_bf16_f32 v210, v136, v137\n"
      "v_mfma_f32_16x16x32_bf16 v[132:135], a[60:63], v[88:91], v[132:135]\n"
      "v_cvt_pk_bf16_f32 v211, v138, v139\n"
      "s_waitcnt lgkmcnt(1)\n"
      "v_mfma_f32_16x16x32_bf16 v[132:135], a[64:67], v[92:95], v[132:135]\n"
      "s_add_u32 s54, s44, s37\n"
      "s_addc_u32 s55, s45, 0\n"
      "v_mfma_f32_16x16x32_bf16 v[132:135], a[68:71], v[108:111], v[132:135]\n"
      "ds_read_b128 v[216:219], v246 offset:24576\n"
      "v_mfma_f32_16x16x32_bf16 v[132:135], a[72:75], v[112:115], v[132:135]\n"
      "ds_read_b128 v[220:223], v246 offset:25600\n"
      "v_mfma_f32_16x16x32_bf16 v[132:135], a[76:79], v[116:119], v[132:135]\n"
      "ds_read_b128 v[224:227], v246 offset:26624\n"
      "v_mfma_f32_16x16x32_bf16 v[136:139], a[80:83], v[36:39], v[4:7]\n"
      "ds_write_b64 v244, v[210:211] offset:136\n"
      "v_mfma_f32_16x16x32_bf16 v[136:139], a[84:87], v[40:43], v[136:139]\n"
      "v_mfma_f32_16x16x32_bf16 v[136:139], a[88:91], v[44:47], v[136:139]\n"
      "v_mfma_f32_16x16x32_bf16 v[136:139], a[92:95], v[60:63], v[136:139]\n"
      "v_cvt_pk_bf16_f32 v208, v132, v133\n"
      "v_mfma_f32_16x16x32_bf16 v[136:139], a[96:99], v[64:67], v[136:139]\n"
      "v_cvt_pk_bf16_f32 v209, v134, v135\n"
      "v_mfma_f32_16x16x32_bf16 v[136:139], a[100:103], v[68:71], v[136:139]\n"
      "s_waitcnt lgkmcnt(3)\n"
      "v_mfma_f32_16x16x32_bf16 v[136:139], a[104:107], v[84:87], v[136:139]\n"
      "global_store_dwordx4 v247, v[216:219], s[54:55] offset:0 sc1\n"
      "v_mfma_f32_16x16x32_bf16 v[136:139], a[108:111], v[88:91], v[136:139]\n"
      "s_add_u32 s52, s42, s39\n"
      "s_addc_u32 s53, s43, 0\n"
      "v_mfma_f32_16x16x32_bf16 v[136:139], a[112:115], v[92:95], v[136:139]\n"
      "s_add_u32 m0, s17, 0x12000\n"
      "v_mfma_f32_16x16x32_bf16 v[136:139], a[116:119], v[108:111], v[136:139]\n"
      "ds_write_b64 v244, v[208:209] offset:12288\n"
      "v_mfma_f32_16x16x32_bf16 v[136:139], a[120:123], v[112:115], v[136:139]\n"
      "global_load_lds_dwordx4 v236, s[52:53] offset:0 nt\n"
      "v_mfma_f32_16x16x32_bf16 v[136:139], a[124:127], v[116:119], v[136:139]\n"
      "v_mfma_f32_16x16x32_bf16 v[132:135], a[128:131], v[36:39], v[8:11]\n"
      "s_cbranch_vccnz LT25\n"
      "LS25:\n"
      "v_mfma_f32_16x16x32_bf16 v[132:135], a[132:135], v[40:43], v[132:135]\n"
      "v_mfma_f32_16x16x32_bf16 v[132:135], a[136:139], v[44:47], v[132:135]\n"
      "v_mfma_f32_16x16x32_bf16 v[132:135], a[140:143], v[60:63], v[132:135]\n"
      "v_cvt_pk_bf16_f32 v210, v136, v137\n"
      "v_mfma_f32_16x16x32_bf16 v[132:135], a[144:147], v[64:67], v[132:135]\n"
      "v_cvt_pk_bf16_f32 v211, v138, v139\n"
      "v_mfma_f32_16x16x32_bf16 v[132:135], a[148:151], v[68:71], v[132:135]\n"
      "v_mfma_f32_16x16x32_bf16 v[132:135], a[152:155], v[84:87], v[132:135]\n"
      "s_waitcnt lgkmcnt(3)\n"
      "v_mfma_f32_16x16x32_bf16 v[132:135], a[156:159], v[88:91], v[132:135]\n"
      "global_store_dwordx4 v252, v[220:223], s[54:55] offset:0 sc1\n"
      "v_mfma_f32_16x16x32_bf16 v[132:135], a[160:163], v[92:95], v[132:135]\n"
      "s_add_u32 m0, s17, 0x13f00\n"
      "v_mfma_f32_16x16x32_bf16 v[132:135], a[164:167], v[108:111], v[132:135]\n"
      "ds_write_b64 v244, v[210:211] offset:12296\n"
      "v_mfma_f32_16x16x32_bf16 v[132:135], a[168:171], v[112:115], v[132:135]\n"
      "global_load_lds_dwordx4 v236, s[52:53] offset:256 nt\n"
      "v_mfma_f32_16x16x32_bf16 v[132:135], a[172:175], v[116:119], v[132:135]\n"
      "v_mfma_f32_16x16x32_bf16 v[136:139], a[176:179], v[36:39], v[12:15]\n"
      "v_mfma_f32_16x16x32_bf16 v[136:139], a[180:183], v[40:43], v[136:139]\n"
      "v_mfma_f32_16x16x32_bf16 v[136:139], a[184:187], v[44:47], v[136:139]\n"
      "v_mfma_f32_16x16x32_bf16 v[136:139], a[188:191], v[60:63], v[136:139]\n"
      "v_cvt_pk_bf16_f32 v212, v132, v133\n"
      "v_mfma_f32_16x16x32_bf16 v[136:139], a[192:195], v[64:67], v[136:139]\n"
      "v_cvt_pk_bf16_f32 v213, v134, v135\n"
      "v_mfma_f32_16x16x32_bf16 v[136:139], a[196:199], v[68:71], v[136:139]\n"
      "v_mfma_f32_16x16x32_bf16 v[136:139], a[200:203], v[84:87], v[136:139]\n"
      "v_mfma_f32_16x16x32_bf16 v[136:139], a[204:207], v[88:91], v[136:139]\n"
      "s_waitcnt lgkmcnt(3)\n"
      "v_mfma_f32_16x16x32_bf16 v[136:139], a[208:211], v[92:95], v[136:139]\n"
      "global_store_dwordx4 v253, v[224:227], s[54:55] offset:0 sc1\n"
      "v_mfma_f32_16x16x32_bf16 v[136:139], a[212:215], v[108:111], v[136:139]\n"
      "ds_write_b64 v245, v[212:213] offset:12288\n"
      "v_mfma_f32_16x16x32_bf16 v[136:139], a[216:219], v[112:115], v[136:139]\n"
      "s_add_u32 m0, s17, 0x15e00\n"
      "v_mfma_f32_16x16x32_bf16 v[136:139], a[220:223], v[116:119], v[136:139]\n"
      "global_load_lds_dwordx4 v236, s[52:53] offset:512 nt\n"
      "v_mfma_f32_16x16x32_bf16 v[132:135], a[224:227], v[36:39], v[16:19]\n"
      "s_cbranch_vccnz LT26\n"
      "LS26:\n"
      "v_mfma_f32_16x16x32_bf16 v[132:135], a[228:231], v[40:43], v[132:135]\n"
      "v_mfma_f32_16x16x32_bf16 v[132:135], a[232:235], v[44:47], v[132:135]\n"
      "v_mfma_f32_16x16x32_bf16 v[132:135], a[236:239], v[60:63], v[132:135]\n"
      "v_cvt_pk_bf16_f32 v214, v136, v137\n"
      "v_mfma_f32_16x16x32_bf16 v[132:135], a[0:3], v[64:67], v[132:135]\n"
      "v_cvt_pk_bf16_f32 v215, v138, v139\n"
      "v_mfma_f32_16x16x32_bf16 v[132:135], a[4:7], v[68:71], v[132:135]\n"
      "v_mfma_f32_16x16x32_bf16 v[132:135], a[8:11], v[84:87], v[132:135]\n"
      "v_mfma_f32_16x16x32_bf16 v[132:135], a[12:15], v[88:91], v[132:135]\n"
      "v_mfma_f32_16x16x32_bf16 v[132:135], a[16:19], v[92:95], v[132:135]\n"
      "v_mfma_f32_16x16x32_bf16 v[132:135], a[20:23], v[108:111], v[132:135]\n"
      "ds_write_b64 v245, v[214:215] offset:12296\n"
      "v_mfma_f32_16x16x32_bf16 v[132:135], v[140:143], v[112:115], v[132:135]\n"
      "v_mfma_f32_16x16x32_bf16 v[132:135], v[156:159], v[116:119], v[132:135]\n"
      "s_waitcnt lgkmcnt(4)\n"
      "s_barrier\n"
      "v_mfma_f32_16x16x32_bf16 v[136:139], v[160:163], v[36:39], v[20:23]\n"
      "ds_read_b128 v[36:39], v232 offset:256\n"
      "v_mfma_f32_16x16x32_bf16 v[136:139], v[164:167], v[40:43], v[136:139]\n"
      "ds_read_b128 v[40:43], v233 offset:256\n"
      "v_mfma_f32_16x16x32_bf16 v[136:139], v[180:183], v[44:47], v[136:139]\n"
      "ds_read_b128 v[44:47], v234 offset:256\n"
      "v_mfma_f32_16x16x32_bf16 v[136:139], v[184:187], v[60:63], v[136:139]\n"
      "ds_read_b128 v[60:63], v235 offset:256\n"
      "v_cvt_pk_bf16_f32 v208, v132, v133\n"
      "v_mfma_f32_16x16x32_bf16 v[136:139], v[188:191], v[64:67], v[136:139]\n"
      "ds_read_b128 v[64:67], v232 offset:8448\n"
      "v_cvt_pk_bf16_f32 v209, v134, v135\n"
      "v_mfma_f32_16x16x32_bf16 v[136:139], v[204:207], v[68:71], v[136:139]\n"
      "ds_read_b128 v[68:71], v233 offset:8448\n"
      "v_mfma_f32_16x16x32_bf16 v[136:139], a[24:27], v[84:87], v[136:139]\n"
      "ds_read_b128 v[84:87], v234 offset:8448\n"
      "v_mfma_f32_16x16x32_bf16 v[136:139], a[28:31], v[88:91], v[136:139]\n"
      "ds_read_b128 v[88:91], v235 offset:8448\n"
      "v_mfma_f32_16x16x32_bf16 v[136:139], a[32:35], v[92:95], v[136:139]\n"
      "ds_read_b128 v[92:95], v232 offset:16640\n"
      "v_mfma_f32_16x16x32_bf16 v[136:139], a[36:39], v[108:111], v[136:139]\n"
      "ds_read_b128 v[108:111], v233 offset:16640\n"
      "v_mfma_f32_16x16x32_bf16 v[136:139], a[40:43], v[112:115], v[136:139]\n"
      "ds_read_b128 v[112:115], v234 offset:16640\n"
      "v_mfma_f32_16x16x32_bf16 v[136:139], a[44:47], v[116:119], v[136:139]\n"
      "ds_read_b128 v[116:119], v235 offset:16640\n"
      "s_waitcnt lgkmcnt(8)\n"
      "v_mfma_f32_16x16x32_bf16 v[132:135], a[240:243], v[36:39], v[0:3]\n"
      "ds_write_b64 v244, v[208:209] offset:12416\n"
      "v_mfma_f32_16x16x32_bf16 v[132:135], a[244:247], v[40:43], v[132:135]\n"
      "s_cbranch_vccnz LT27\n"
      "LS27:\n"
      "v_mfma_f32_16x16x32_bf16 v[132:135], a[248:251], v[44:47], v[132:135]\n"
      "v_mfma_f32_16x16x32_bf16 v[132:135], a[252:255], v[60:63], v[132:135]\n"
      "v_cvt_pk_bf16_f32 v210, v136, v137\n"
      "s_waitcnt lgkmcnt(5)\n"
      "v_mfma_f32_16x16x32_bf16 v[132:135], a[48:51], v[64:67], v[132:135]\n"
      "v_cvt_pk_bf16_f32 v211, v138, v139\n"
      "v_mfma_f32_16x16x32_bf16 v[132:135], a[52:55], v[68:71], v[132:135]\n"
      "v_mfma_f32_16x16x32_bf16 v[132:135], a[56:59], v[84:87], v[132:135]\n"
      "ds_read_b128 v[216:219], v246 offset:0\n"
      "v_mfma_f32_16x16x32_bf16 v[132:135], a[60:63], v[88:91], v[132:135]\n"
      "ds_read_b128 v[220:223], v246 offset:1024\n"
      "s_waitcnt lgkmcnt(3)\n"
      "v_mfma_f32_16x16x32_bf16 v[132:135], a[64:67], v[92:95], v[132:135]\n"
      "ds_read_b128 v[224:227], v246 offset:2048\n"
      "v_mfma_f32_16x16x32_bf16 v[132:135], a[68:71], v[108:111], v[132:135]\n"
      "ds_write_b64 v244, v[210:211] offset:12424\n"
      "v_mfma_f32_16x16x32_bf16 v[132:135], a[72:75], v[112:115], v[132:135]\n"
      "v_mfma_f32_16x16x32_bf16 v[132:135], a[76:79], v[116:119], v[132:135]\n"
      "v_mfma_f32_16x16x32_bf16 v[136:139], a[80:83], v[36:39], v[4:7]\n"
      "v_mfma_f32_16x16x32_bf16 v[136:139], a[84:87], v[40:43], v[136:139]\n"
      "v_mfma_f32_16x16x32_bf16 v[136:139], a[88:91], v[44:47], v[136:139]\n"
      "v_mfma_f32_16x16x32_bf16 v[136:139], a[92:95], v[60:63], v[136:139]\n"
      "v_cvt_pk_bf16_f32 v208, v132, v133\n"
      "v_mfma_f32_16x16x32_bf16 v[136:139], a[96:99], v[64:67], v[136:139]\n"
      "v_cvt_pk_bf16_f32 v209, v134, v135\n"
      "v_mfma_f32_16x16x32_bf16 v[136:139], a[100:103], v[68:71], v[136:139]\n"
      "s_waitcnt lgkmcnt(3)\n"
      "v_mfma_f32_16x16x32_bf16 v[136:139], a[104:107], v[84:87], v[136:139]\n"
      "global_store_dwordx4 v247, v[216:219], s[54:55] offset:768 sc1\n"
      "v_mfma_f32_16x16x32_bf16 v[136:139], a[108:111], v[88:91], v[136:139]\n"
      "s_add_u32 m0, s17, 0x13000\n"
      "v_mfma_f32_16x16x32_bf16 v[136:139], a[112:115], v[92:95], v[136:139]\n"
      "global_load_lds_dwordx4 v237, s[52:53] offset:0 nt\n"
      "v_mfma_f32_16x16x32_bf16 v[136:139], a[116:119], v[108:111], v[136:139]\n"
      "ds_write_b64 v244, v[208:209] offset:24576\n"
      "v_mfma_f32_16x16x32_bf16 v[136:139], a[120:123], v[112:115], v[136:139]\n"
      "v_mfma_f32_16x16x32_bf16 v[136:139], a[124:127], v[116:119], v[136:139]\n"
      "v_mfma_f32_16x16x32_bf16 v[132:135], a[128:131], v[36:39], v[8:11]\n"
      "s_cbranch_vccnz LT28\n"
      "LS28:\n"
      "v_mfma_f32_16x16x32_bf16 v[132:135], a[132:135], v[40:43], v[132:135]\n"
      "v_mfma_f32_16x16x32_bf16 v[132:135], a[136:139], v[44:47], v[132:135]\n"
      "v_mfma_f32_16x16x32_bf16 v[132:135], a[140:143], v[60:63], v[132:135]\n"
      "v_cvt_pk_bf16_f32 v210, v136, v137\n"
      "v_mfma_f32_16x16x32_bf16 v[132:135], a[144:147], v[64:67], v[132:135]\n"
      "v_cvt_pk_bf16_f32 v211, v138, v139\n"
      "v_mfma_f32_16x16x32_bf16 v[132:135], a[148:151], v[68:71], v[132:135]\n"
      "v_mfma_f32_16x16x32_bf16 v[132:135], a[152:155], v[84:87], v[132:135]\n"
      "s_waitcnt lgkmcnt(3)\n"
      "v_mfma_f32_16x16x32_bf16 v[132:135], a[156:159], v[88:91], v[132:135]\n"
      "global_store_dwordx4 v252, v[220:223], s[54:55] offset:768 sc1\n"
      "v_mfma_f32_16x16x32_bf16 v[132:135], a[160:163], v[92:95], v[132:135]\n"
      "s_add_u32 m0, s17, 0x14f00\n"
      "v_mfma_f32_16x16x32_bf16 v[132:135], a[164:167], v[108:111], v[132:135]\n"
      "ds_write_b64 v244, v[210:211] offset:24584\n"
      "v_mfma_f32_16x16x32_bf16 v[132:135], a[168:171], v[112:115], v[132:135]\n"
      "global_load_lds_dwordx4 v237, s[52:53] offset:256 nt\n"
      "v_mfma_f32_16x16x32_bf16 v[132:135], a[172:175], v[116:119], v[132:135]\n"
      "v_mfma_f32_16x16x32_bf16 v[136:139], a[176:179], v[36:39], v[12:15]\n"
      "v_mfma_f32_16x16x32_bf16 v[136:139], a[180:183], v[40:43], v[136:139]\n"
      "v_mfma_f32_16x16x32_bf16 v[136:139], a[184:187], v[44:47], v[136:139]\n"
      "v_mfma_f32_16x16x32_bf16 v[136:139], a[188:191], v[60:63], v[136:139]\n"
      "v_cvt_pk_bf16_f32 v212, v132, v133\n"
      "v_mfma_f32_16x16x32_bf16 v[136:139], a[192:195], v[64:67], v[136:139]\n"
      "v_cvt_pk_bf16_f32 v213, v134, v135\n"
      "v_mfma_f32_16x16x32_bf16 v[136:139], a[196:199], v[68:71], v[136:139]\n"
      "v_mfma_f32_16x16x32_bf16 v[136:139], a[200:203], v[84:87], v[136:139]\n"
      "v_mfma_f32_16x16x32_bf16 v[136:139], a[204:207], v[88:91], v[136:139]\n"
      "s_waitcnt lgkmcnt(3)\n"
      "v_mfma_f32_16x16x32_bf16 v[136:139], a[208:211], v[92:95], v[136:139]\n"
      "global_store_dwordx4 v253, v[224:227], s[54:55] offset:768 sc1\n"
      "v_mfma_f32_16x16x32_bf16 v[136:139], a[212:215], v[108:111], v[136:139]\n"
      "ds_write_b64 v245, v[212:213] offset:24576\n"
      "v_mfma_f32_16x16x32_bf16 v[136:139], a[216:219], v[112:115], v[136:139]\n"
      "s_add_u32 m0, s17, 0x16e00\n"
      "v_mfma_f32_16x16x32_bf16 v[136:139], a[220:223], v[116:119], v[136:139]\n"
      "global_load_lds_dwordx4 v237, s[52:53] offset:512 nt\n"
      "v_mfma_f32_16x16x32_bf16 v[132:135], a[224:227], v[36:39], v[16:19]\n"
      "s_cbranch_vccnz LT29\n"
      "LS29:\n"
      "v_mfma_f32_16x16x32_bf16 v[132:135], a[228:231], v[40:43], v[132:135]\n"
      "v_mfma_f32_16x16x32_bf16 v[132:135], a[232:235], v[44:47], v[132:135]\n"
      "v_mfma_f32_16x16x32_bf16 v[132:135], a[236:239], v[60:63], v[132:135]\n"
      "s_memrealtime s[84:85]\n"
      "v_mfma_f32_16x16x32_bf16 v[132:135], a[0:3], v[64:67], v[132:135]\n"
      "v_cvt_pk_bf16_f32 v214, v136, v137\n"
      "v_mfma_f32_16x16x32_bf16 v[132:135], a[4:7], v[68:71], v[132:135]\n"
      "v_cvt_pk_bf16_f32 v215, v138, v139\n"
      "v_mfma_f32_16x16x32_bf16 v[132:135], a[8:11], v[84:87], v[132:135]\n"
      "v_mfma_f32_16x16x32_bf16 v[132:135], a[12:15], v[88:91], v[132:135]\n"
      "s_add_u32 s83, s83, s86\n"
      "v_mfma_f32_16x16x32_bf16 v[132:135], a[16:19], v[92:95], v[132:135]\n"
      "v_mfma_f32_16x16x32_bf16 v[132:135], a[20:23], v[108:111], v[132:135]\n"
      "v_mfma_f32_16x16x32_bf16 v[132:135], v[140:143], v[112:115], v[132:135]\n"
      "s_waitcnt lgkmcnt(0)\n"
      "s_sub_u32 s84, s84, s83\n"
      "v_mfma_f32_16x16x32_bf16 v[132:135], v[156:159], v[116:119], v[132:135]\n"
      "s_cmp_lt_i32 s84, 0\n"
      "s_cselect_b64 vcc, -1, 0\n"
      "s_waitcnt vmcnt(24)\n"
      "s_barrier\n"
      "v_mfma_f32_16x16x32_bf16 v[136:139], v[160:163], v[36:39], v[20:23]\n"
      "ds_read_b128 v[36:39], v232 offset:24576\n"
      "ds_write_b64 v245, v[214:215] offset:24584\n"
      "v_mfma_f32_16x16x32_bf16 v[136:139], v[164:167], v[40:43], v[136:139]\n"
      "ds_read_b128 v[40:43], v233 offset:24576\n"
      "v_mfma_f32_16x16x32_bf16 v[136:139], v[180:183], v[44:47], v[136:139]\n"
      "ds_read_b128 v[44:47], v234 offset:24576\n"
      "v_mfma_f32_16x16x32_bf16 v[136:139], v[184:187], v[60:63], v[136:139]\n"
      "ds_read_b128 v[60:63], v235 offset:24576\n"
      "v_cvt_pk_bf16_f32 v208, v132, v133\n"
      "v_mfma_f32_16x16x32_bf16 v[136:139], v[188:191], v[64:67], v[136:139]\n"
      "ds_read_b128 v[64:67], v232 offset:32768\n"
      "v_cvt_pk_bf16_f32 v209, v134, v135\n"
      "v_mfma_f32_16x16x32_bf16 v[136:139], v[204:207], v[68:71], v[136:139]\n"
      "ds_read_b128 v[68:71], v233 offset:32768\n"
      "v_mfma_f32_16x16x32_bf16 v[136:139], a[24:27], v[84:87], v[136:139]\n"
      "ds_read_b128 v[84:87], v234 offset:32768\n"
      "v_mfma_f32_16x16x32_bf16 v[136:139], a[28:31], v[88:91], v[136:139]\n"
      "ds_read_b128 v[88:91], v235 offset:32768\n"
      "v_mfma_f32_16x16x32_bf16 v[136:139], a[32:35], v[92:95], v[136:139]\n"
      "ds_read_b128 v[92:95], v232 offset:40960\n"
      "v_mfma_f32_16x16x32_bf16 v[136:139], a[36:39], v[108:111], v[136:139]\n"
      "ds_read_b128 v[108:111], v233 offset:40960\n"
      "v_mfma_f32_16x16x32_bf16 v[136:139], a[40:43], v[112:115], v[136:139]\n"
      "ds_read_b128 v[112:115], v234 offset:40960\n"
      "v_mfma_f32_16x16x32_bf16 v[136:139], a[44:47], v[116:119], v[136:139]\n"
      "ds_read_b128 v[116:119], v235 offset:40960\n"
      "s_waitcnt lgkmcnt(8)\n"
      "v_mfma_f32_16x16x32_bf16 v[132:135], a[240:243], v[36:39], v[0:3]\n"
      "ds_write_b64 v244, v[208:209] offset:24704\n"
      "v_mfma_f32_16x16x32_bf16 v[132:135], a[244:247], v[40:43], v[132:135]\n"
      "s_cbranch_vccnz LT30\n"
      "LS30:\n"
      "v_mfma_f32_16x16x32_bf16 v[132:135], a[248:251], v[44:47], v[132:135]\n"
      "v_mfma_f32_16x16x32_bf16 v[132:135], a[252:255], v[60:63], v[132:135]\n"
      "v_cvt_pk_bf16_f32 v210, v136, v137\n"
      "s_lshl_b32 s79, s95, 1\n"
      "s_waitcnt lgkmcnt(5)\n"
      "v_mfma_f32_16x16x32_bf16 v[132:135], a[48:51], v[64:67], v[132:135]\n"
      "v_cvt_pk_bf16_f32 v211, v138, v139\n"
      "s_mul_i32 s78, s97, s19\n"
      "v_mfma_f32_16x16x32_bf16 v[132:135], a[52:55], v[68:71], v[132:135]\n"
      "v_mad_i32_i24 v252, v251, s79, v252\n"
      "v_add_u32 v236, s78, v236\n"
      "v_mfma_f32_16x16x32_bf16 v[132:135], a[56:59], v[84:87], v[132:135]\n"
      "ds_read_b128 v[24:27], v246 offset:12288\n"
      "s_mul_i32 s79, s79, s100\n"
      "v_mfma_f32_16x16x32_bf16 v[132:135], a[60:63], v[88:91], v[132:135]\n"
      "ds_read_b128 v[28:31], v246 offset:13312\n"
      "v_add_u32 v247, s79, v247\n"
      "s_waitcnt lgkmcnt(3)\n"
      "v_mfma_f32_16x16x32_bf16 v[132:135], a[64:67], v[92:95], v[132:135]\n"
      "ds_read_b128 v[32:35], v246 offset:14336\n"
      "s_mul_i32 s79, s79, s101\n"
      "v_mfma_f32_16x16x32_bf16 v[132:135], a[68:71], v[108:111], v[132:135]\n"
      "ds_write_b64 v244, v[210:211] offset:24712\n"
      "s_mul_i32 s78, s78, s99\n"
      "v_mfma_f32_16x16x32_bf16 v[132:135], a[72:75], v[112:115], v[132:135]\n"
      "v_add_u32 v253, s79, v253\n"
      "v_add_u32 v237, s78, v237\n"
      "v_mfma_f32_16x16x32_bf16 v[132:135], a[76:79], v[116:119], v[132:135]\n"
      "s_add_u32 s54, s30, s38\n"
      "s_addc_u32 s55, s31, 0\n"
      "v_mfma_f32_16x16x32_bf16 v[136:139], a[80:83], v[36:39], v[4:7]\n"
      "s_waitcnt lgkmcnt(3)\n"
      "v_mfma_f32_16x16x32_bf16 v[136:139], a[84:87], v[40:43], v[136:139]\n"
      "global_store_dwordx4 v247, v[24:27], s[54:55] offset:0 sc1\n"
      "v_mfma_f32_16x16x32_bf16 v[136:139], a[88:91], v[44:47], v[136:139]\n"
      "v_mfma_f32_16x16x32_bf16 v[136:139], a[92:95], v[60:63], v[136:139]\n"
      "v_cvt_pk_bf16_f32 v208, v132, v133\n"
      "v_mfma_f32_16x16x32_bf16 v[136:139], a[96:99], v[64:67], v[136:139]\n"
      "v_cvt_pk_bf16_f32 v209, v134, v135\n"
      "v_mfma_f32_16x16x32_bf16 v[136:139], a[100:103], v[68:71], v[136:139]\n"
      "v_mfma_f32_16x16x32_bf16 v[136:139], a[104:107], v[84:87], v[136:139]\n"
      "v_mfma_f32_16x16x32_bf16 v[136:139], a[108:111], v[88:91], v[136:139]\n"
      "s_add_u32 s52, s20, s40\n"
      "s_addc_u32 s53, s21, 0\n"
      "v_mfma_f32_16x16x32_bf16 v[136:139], a[112:115], v[92:95], v[136:139]\n"
      "s_add_u32 m0, s17, 0x0\n"
      "v_mfma_f32_16x16x32_bf16 v[136:139], a[116:119], v[108:111], v[136:139]\n"
      "ds_write_b64 v244, v[208:209] offset:0\n"
      "v_mfma_f32_16x16x32_bf16 v[136:139], a[120:123], v[112:115], v[136:139]\n"
      "global_load_lds_dwordx4 v236, s[52:53] offset:0 nt\n"
      "v_mfma_f32_16x16x32_bf16 v[136:139], a[124:127], v[116:119], v[136:139]\n"
      "v_mfma_f32_16x16x32_bf16 v[132:135], a[128:131], v[36:39], v[8:11]\n"
      "s_cbranch_vccnz LT31\n"
      "LS31:\n"
      "v_mfma_f32_16x16x32_bf16 v[132:135], a[132:135], v[40:43], v[132:135]\n"
      "v_mfma_f32_16x16x32_bf16 v[132:135], a[136:139], v[44:47], v[132:135]\n"
      "v_mfma_f32_16x16x32_bf16 v[132:135], a[140:143], v[60:63], v[132:135]\n"
      "v_cvt_pk_bf16_f32 v210, v136, v137\n"
      "v_mfma_f32_16x16x32_bf16 v[132:135], a[144:147], v[64:67], v[132:135]\n"
      "v_cvt_pk_bf16_f32 v211, v138, v139\n"
      "v_mfma_f32_16x16x32_bf16 v[132:135], a[148:151], v[68:71], v[132:135]\n"
      "v_mfma_f32_16x16x32_bf16 v[132:135], a[152:155], v[84:87], v[132:135]\n"
      "s_waitcnt lgkmcnt(3)\n"
      "v_mfma_f32_16x16x32_bf16 v[132:135], a[156:159], v[88:91], v[132:135]\n"
      "global_store_dwordx4 v252, v[28:31], s[54:55] offset:0 sc1\n"
      "v_mfma_f32_16x16x32_bf16 v[132:135], a[160:163], v[92:95], v[132:135]\n"
      "s_add_u32 m0, s17, 0x1f00\n"
      "v_mfma_f32_16x16x32_bf16 v[132:135], a[164:167], v[108:111], v[132:135]\n"
      "ds_write_b64 v244, v[210:211] offset:8\n"
      "v_mfma_f32_16x16x32_bf16 v[132:135], a[168:171], v[112:115], v[132:135]\n"
      "global_load_lds_dwordx4 v236, s[52:53] offset:256 nt\n"
      "v_mfma_f32_16x16x32_bf16 v[132:135], a[172:175], v[116:119], v[132:135]\n"
      "v_mfma_f32_16x16x32_bf16 v[136:139], a[176:179], v[36:39], v[12:15]\n"
      "v_mfma_f32_16x16x32_bf16 v[136:139], a[180:183], v[40:43], v[136:139]\n"
      "v_mfma_f32_16x16x32_bf16 v[136:139], a[184:187], v[44:47], v[136:139]\n"
      "v_mfma_f32_16x16x32_bf16 v[136:139], a[188:191], v[60:63], v[136:139]\n"
      "v_cvt_pk_bf16_f32 v212, v132, v133\n"
      "v_mfma_f32_16x16x32_bf16 v[136:139], a[192:195], v[64:67], v[136:139]\n"
      "v_cvt_pk_bf16_f32 v213, v134, v135\n"
      "v_mfma_f32_16x16x32_bf16 v[136:139], a[196:199], v[68:71], v[136:139]\n"
      "v_mfma_f32_16x16x32_bf16 v[136:139], a[200:203], v[84:87], v[136:139]\n"
      "v_mfma_f32_16x16x32_bf16 v[136:139], a[204:207], v[88:91], v[136:139]\n"
      "s_waitcnt lgkmcnt(3)\n"
      "v_mfma_f32_16x16x32_bf16 v[136:139], a[208:211], v[92:95], v[136:139]\n"
      "global_store_dwordx4 v253, v[32:35], s[54:55] offset:0 sc1\n"
      "v_mfma_f32_16x16x32_bf16 v[136:139], a[212:215], v[108:111], v[136:139]\n"
      "ds_write_b64 v245, v[212:213] offset:0\n"
      "v_mfma_f32_16x16x32_bf16 v[136:139], a[216:219], v[112:115], v[136:139]\n"
      "s_add_u32 m0, s17, 0x3e00\n"
      "v_mfma_f32_16x16x32_bf16 v[136:139], a[220:223], v[116:119], v[136:139]\n"
      "global_load_lds_dwordx4 v236, s[52:53] offset:512 nt\n"
      "v_mfma_f32_16x16x32_bf16 v[132:135], a[224:227], v[36:39], v[16:19]\n"
      "s_cbranch_vccnz LT32\n"
      "LS32:\n"
      "v_mfma_f32_16x16x32_bf16 v[132:135], a[228:231], v[40:43], v[132:135]\n"
      "v_mfma_f32_16x16x32_bf16 v[132:135], a[232:235], v[44:47], v[132:135]\n"
      "v_mfma_f32_16x16x32_bf16 v[132:135], a[236:239], v[60:63], v[132:135]\n"
      "v_cvt_pk_bf16_f32 v214, v136, v137\n"
      "v_mfma_f32_16x16x32_bf16 v[132:135], a[0:3], v[64:67], v[132:135]\n"
      "v_cvt_pk_bf16_f32 v215, v138, v139\n"
      "v_mfma_f32_16x16x32_bf16 v[132:135], a[4:7], v[68:71], v[132:135]\n"
      "v_mfma_f32_16x16x32_bf16 v[132:135], a[8:11], v[84:87], v[132:135]\n"
      "LPD14:\n"
      "s_cbranch_execz LRET\n"
      "v_mfma_f32_16x16x32_bf16 v[132:135], a[12:15], v[88:91], v[132:135]\n"
      "v_mfma_f32_16x16x32_bf16 v[132:135], a[16:19], v[92:95], v[132:135]\n"
      "v_mfma_f32_16x16x32_bf16 v[132:135], a[20:23], v[108:111], v[132:135]\n"
      "ds_write_b64 v245, v[214:215] offset:8\n"
      "v_mfma_f32_16x16x32_bf16 v[132:135], v[140:143], v[112:115], v[132:135]\n"
      "v_mfma_f32_16x16x32_bf16 v[132:135], v[156:159], v[116:119], v[132:135]\n"
      "s_waitcnt lgkmcnt(4)\n"
      "s_barrier\n"
      "v_mfma_f32_16x16x32_bf16 v[136:139], v[160:163], v[36:39], v[20:23]\n"
      "ds_read_b128 v[36:39], v232 offset:24832\n"
      "v_mfma_f32_16x16x32_bf16 v[136:139], v[164:167], v[40:43], v[136:139]\n"
      "ds_read_b128 v[40:43], v233 offset:24832\n"
      "v_mfma_f32_16x16x32_bf16 v[136:139], v[180:183], v[44:47], v[136:139]\n"
      "ds_read_b128 v[44:47], v234 offset:24832\n"
      "v_mfma_f32_16x16x32_bf16 v[136:139], v[184:187], v[60:63], v[136:139]\n"
      "ds_read_b128 v[60:63], v235 offset:24832\n"
      "v_cvt_pk_bf16_f32 v208, v132, v133\n"
      "v_mfma_f32_16x16x32_bf16 v[136:139], v[188:191], v[64:67], v[136:139]\n"
      "ds_read_b128 v[64:67], v232 offset:33024\n"
      "v_cvt_pk_bf16_f32 v209, v134, v135\n"
      "v_mfma_f32_16x16x32_bf16 v[136:139], v[204:207], v[68:71], v[136:139]\n"
      "ds_read_b128 v[68:71], v233 offset:33024\n"
      "v_mfma_f32_16x16x32_bf16 v[136:139], a[24:27], v[84:87], v[136:139]\n"
      "ds_read_b128 v[84:87], v234 offset:33024\n"
      "v_mfma_f32_16x16x32_bf16 v[136:139], a[28:31], v[88:91], v[136:139]\n"
      "ds_read_b128 v[88:91], v235 offset:33024\n"
      "v_mfma_f32_16x16x32_bf16 v[136:139], a[32:35], v[92:95], v[136:139]\n"
      "ds_read_b128 v[92:95], v232 offset:41216\n"
      "v_mfma_f32_16x16x32_bf16 v[136:139], a[36:39], v[108:111], v[136:139]\n"
      "ds_read_b128 v[108:111], v233 offset:41216\n"
      "ds_write_b64 v244, v[208:209] offset:128\n"
      "v_mfma_f32_16x16x32_bf16 v[136:139], a[40:43], v[112:115], v[136:139]\n"
      "ds_read_b128 v[112:115], v234 offset:41216\n"
      "v_mfma_f32_16x16x32_bf16 v[136:139], a[44:47], v[116:119], v[136:139]\n"
      "ds_read_b128 v[116:119], v235 offset:41216\n"
      "s_waitcnt lgkmcnt(9)\n"
      "v_mfma_f32_16x16x32_bf16 v[132:135], a[240:243], v[36:39], v[0:3]\n"
      "v_add_u32 v232, 0xc000, v232\n"
      "s_cbranch_vccnz LT33\n"
      "LS33:\n"
      "v_mfma_f32_16x16x32_bf16 v[132:135], a[244:247], v[40:43], v[132:135]\n"
      "v_add_u32 v233, 0xc000, v233\n"
      "v_mfma_f32_16x16x32_bf16 v[132:135], a[248:251], v[44:47], v[132:135]\n"
      "v_add_u32 v234, 0xc000, v234\n"
      "v_mfma_f32_16x16x32_bf16 v[132:135], a[252:255], v[60:63], v[132:135]\n"
      "v_add_u32 v235, 0xc000, v235\n"
      "s_waitcnt lgkmcnt(5)\n"
      "v_mfma_f32_16x16x32_bf16 v[132:135], a[48:51], v[64:67], v[132:135]\n"
      "v_cvt_pk_bf16_f32 v210, v136, v137\n"
      "v_mfma_f32_16x16x32_bf16 v[132:135], a[52:55], v[68:71], v[132:135]\n"
      "v_cvt_pk_bf16_f32 v211, v138, v139\n"
      "v_mfma_f32_16x16x32_bf16 v[132:135], a[56:59], v[84:87], v[132:135]\n"
      "ds_read_b128 v[48:51], v246 offset:24576\n"
      "v_mfma_f32_16x16x32_bf16 v[132:135], a[60:63], v[88:91], v[132:135]\n"
      "ds_read_b128 v[52:55], v246 offset:25600\n"
      "s_waitcnt lgkmcnt(2)\n"
      "v_mfma_f32_16x16x32_bf16 v[132:135], a[64:67], v[92:95], v[132:135]\n"
      "ds_read_b128 v[56:59], v246 offset:26624\n"
      "v_mfma_f32_16x16x32_bf16 v[132:135], a[68:71], v[108:111], v[132:135]\n"
      "v_lshlrev_b32 v0, 16, v168\n"
      "v_mfma_f32_16x16x32_bf16 v[132:135], a[72:75], v[112:115], v[132:135]\n"
      "ds_write_b64 v244, v[210:211] offset:136\n"
      "v_mfma_f32_16x16x32_bf16 v[132:135], a[76:79], v[116:119], v[132:135]\n"
      "v_and_b32 v1, 0xffff0000, v168\n"
      "v_mfma_f32_16x16x32_bf16 v[136:139], a[80:83], v[36:39], v[4:7]\n"
      "v_lshlrev_b32 v2, 16, v169\n"
      "v_mfma_f32_16x16x32_bf16 v[136:139], a[84:87], v[40:43], v[136:139]\n"
      "v_and_b32 v3, 0xffff0000, v169\n"
      "v_mfma_f32_16x16x32_bf16 v[136:139], a[88:91], v[44:47], v[136:139]\n"
      "v_mfma_f32_16x16x32_bf16 v[136:139], a[92:95], v[60:63], v[136:139]\n"
      "v_cvt_pk_bf16_f32 v208, v132, v133\n"
      "v_mfma_f32_16x16x32_bf16 v[136:139], a[96:99], v[64:67], v[136:139]\n"
      "v_cvt_pk_bf16_f32 v209, v134, v135\n"
      "v_mfma_f32_16x16x32_bf16 v[136:139], a[100:103], v[68:71], v[136:139]\n"
      "s_waitcnt lgkmcnt(3)\n"
      "v_mfma_f32_16x16x32_bf16 v[136:139], a[104:107], v[84:87], v[136:139]\n"
      "global_store_dwordx4 v247, v[48:51], s[54:55] offset:768 sc1\n"
      "v_mfma_f32_16x16x32_bf16 v[136:139], a[108:111], v[88:91], v[136:139]\n"
      "s_add_u32 m0, s17, 0x1000\n"
      "v_mfma_f32_16x16x32_bf16 v[136:139], a[112:115], v[92:95], v[136:139]\n"
      "global_load_lds_dwordx4 v237, s[52:53] offset:0 nt\n"
      "v_mfma_f32_16x16x32_bf16 v[136:139], a[116:119], v[108:111], v[136:139]\n"
      "ds_write_b64 v244, v[208:209] offset:12288\n"
      "v_mfma_f32_16x16x32_bf16 v[136:139], a[120:123], v[112:115], v[136:139]\n"
      "v_lshlrev_b32 v4, 16, v170\n"
      "v_mfma_f32_16x16x32_bf16 v[136:139], a[124:127], v[116:119], v[136:139]\n"
      "v_and_b32 v5, 0xffff0000, v170\n"
      "v_mfma_f32_16x16x32_bf16 v[132:135], a[128:131], v[36:39], v[8:11]\n"
      "v_lshlrev_b32 v6, 16, v171\n"
      "v_mfma_f32_16x16x32_bf16 v[132:135], a[132:135], v[40:43], v[132:135]\n"
      "v_and_b32 v7, 0xffff0000, v171\n"
      "v_mfma_f32_16x16x32_bf16 v[132:135], a[136:139], v[44:47], v[132:135]\n"
      "s_cbranch_vccnz LT34\n"
      "LS34:\n"
      "v_mfma_f32_16x16x32_bf16 v[132:135], a[140:143], v[60:63], v[132:135]\n"
      "v_cvt_pk_bf16_f32 v210, v136, v137\n"
      "v_mfma_f32_16x16x32_bf16 v[132:135], a[144:147], v[64:67], v[132:135]\n"
      "v_cvt_pk_bf16_f32 v211, v138, v139\n"
      "v_mfma_f32_16x16x32_bf16 v[132:135], a[148:151], v[68:71], v[132:135]\n"
      "v_lshlrev_b32 v8, 16, v172\n"
      "v_mfma_f32_16x16x32_bf16 v[132:135], a[152:155], v[84:87], v[132:135]\n"
      "s_waitcnt lgkmcnt(3)\n"
      "v_mfma_f32_16x16x32_bf16 v[132:135], a[156:159], v[88:91], v[132:135]\n"
      "global_store_dwordx4 v252, v[52:55], s[54:55] offset:768 sc1\n"
      "v_mfma_f32_16x16x32_bf16 v[132:135], a[160:163], v[92:95], v[132:135]\n"
      "s_add_u32 m0, s17, 0x2f00\n"
      "v_mfma_f32_16x16x32_bf16 v[132:135], a[164:167], v[108:111], v[132:135]\n"
      "ds_write_b64 v244, v[210:211] offset:12296\n"
      "v_mfma_f32_16x16x32_bf16 v[132:135], a[168:171], v[112:115], v[132:135]\n"
      "global_load_lds_dwordx4 v237, s[52:53] offset:256 nt\n"
      "v_mfma_f32_16x16x32_bf16 v[132:135], a[172:175], v[116:119], v[132:135]\n"
      "v_and_b32 v9, 0xffff0000, v172\n"
      "v_mfma_f32_16x16x32_bf16 v[136:139], a[176:179], v[36:39], v[12:15]\n"
      "v_lshlrev_b32 v10, 16, v173\n"
      "v_mfma_f32_16x16x32_bf16 v[136:139], a[180:183], v[40:43], v[136:139]\n"
      "v_and_b32 v11, 0xffff0000, v173\n"
      "v_mfma_f32_16x16x32_bf16 v[136:139], a[184:187], v[44:47], v[136:139]\n"
      "v_mfma_f32_16x16x32_bf16 v[136:139], a[188:191], v[60:63], v[136:139]\n"
      "v_cvt_pk_bf16_f32 v212, v132, v133\n"
      "v_mfma_f32_16x16x32_bf16 v[136:139], a[192:195], v[64:67], v[136:139]\n"
      "v_cvt_pk_bf16_f32 v213, v134, v135\n"
      "v_mfma_f32_16x16x32_bf16 v[136:139], a[196:199], v[68:71], v[136:139]\n"
      "v_lshlrev_b32 v12, 16, v174\n"
      "v_mfma_f32_16x16x32_bf16 v[136:139], a[200:203], v[84:87], v[136:139]\n"
      "v_and_b32 v13, 0xffff0000, v174\n"
      "v_mfma_f32_16x16x32_bf16 v[136:139], a[204:207], v[88:91], v[136:139]\n"
      "s_waitcnt lgkmcnt(3)\n"
      "v_mfma_f32_16x16x32_bf16 v[136:139], a[208:211], v[92:95], v[136:139]\n"
      "global_store_dwordx4 v253, v[56:59], s[54:55] offset:768 sc1\n"
      "v_mfma_f32_16x16x32_bf16 v[136:139], a[212:215], v[108:111], v[136:139]\n"
      "ds_write_b64 v245, v[212:213] offset:12288\n"
      "v_mfma_f32_16x16x32_bf16 v[136:139], a[216:219], v[112:115], v[136:139]\n"
      "s_add_u32 m0, s17, 0x4e00\n"
      "v_mfma_f32_16x16x32_bf16 v[136:139], a[220:223], v[116:119], v[136:139]\n"
      "global_load_lds_dwordx4 v237, s[52:53] offset:512 nt\n"
      "v_mfma_f32_16x16x32_bf16 v[132:135], a[224:227], v[36:39], v[16:19]\n"
      "v_lshlrev_b32 v14, 16, v175\n"
      "v_mfma_f32_16x16x32_bf16 v[132:135], a[228:231], v[40:43], v[132:135]\n"
      "v_and_b32 v15, 0xffff0000, v175\n"
      "v_mfma_f32_16x16x32_bf16 v[132:135], a[232:235], v[44:47], v[132:135]\n"
      "s_cbranch_vccnz LT35\n"
      "LS35:\n"
      "v_mfma_f32_16x16x32_bf16 v[132:135], a[236:239], v[60:63], v[132:135]\n"
      "s_memrealtime s[84:85]\n"
      "v_mfma_f32_16x16x32_bf16 v[132:135], a[0:3], v[64:67], v[132:135]\n"
      "v_cvt_pk_bf16_f32 v214, v136, v137\n"
      "v_mfma_f32_16x16x32_bf16 v[132:135], a[4:7], v[68:71], v[132:135]\n"
      "v_cvt_pk_bf16_f32 v215, v138, v139\n"
      "v_mfma_f32_16x16x32_bf16 v[132:135], a[8:11], v[84:87], v[132:135]\n"
      "v_lshlrev_b32 v16, 16, v176\n"
      "v_mfma_f32_16x16x32_bf16 v[132:135], a[12:15], v[88:91], v[132:135]\n"
      "s_add_u32 s83, s83, s86\n"
      "v_mfma_f32_16x16x32_bf16 v[132:135], a[16:19], v[92:95], v[132:135]\n"
      "v_and_b32 v17, 0xffff0000, v176\n"
      "v_mfma_f32_16x16x32_bf16 v[132:135], a[20:23], v[108:111], v[132:135]\n"
      "v_lshlrev_b32 v18, 16, v177\n"
      "v_mfma_f32_16x16x32_bf16 v[132:135], v[140:143], v[112:115], v[132:135]\n"
      "s_waitcnt lgkmcnt(0)\n"
      "s_sub_u32 s84, s84, s83\n"
      "v_mfma_f32_16x16x32_bf16 v[132:135], v[156:159], v[116:119], v[132:135]\n"
      "s_cmp_lt_i32 s84, 0\n"
      "s_cselect_b64 vcc, -1, 0\n"
      "s_waitcnt vmcnt(24)\n"
      "s_barrier\n"
      "v_mfma_f32_16x16x32_bf16 v[136:139], v[160:163], v[36:39], v[20:23]\n"
      "ds_read_b128 v[36:39], v232 offset:0\n"
      "ds_write_b64 v245, v[214:215] offset:12296\n"
      "v_mfma_f32_16x16x32_bf16 v[136:139], v[164:167], v[40:43], v[136:139]\n"
      "ds_read_b128 v[40:43], v233 offset:0\n"
      "v_mfma_f32_16x16x32_bf16 v[136:139], v[180:183], v[44:47], v[136:139]\n"
      "ds_read_b128 v[44:47], v234 offset:0\n"
      "v_mfma_f32_16x16x32_bf16 v[136:139], v[184:187], v[60:63], v[136:139]\n"
      "ds_read_b128 v[60:63], v235 offset:0\n"
      "v_cvt_pk_bf16_f32 v208, v132, v133\n"
      "v_mfma_f32_16x16x32_bf16 v[136:139], v[188:191], v[64:67], v[136:139]\n"
      "ds_read_b128 v[64:67], v232 offset:8192\n"
      "v_cvt_pk_bf16_f32 v209, v134, v135\n"
      "v_mfma_f32_16x16x32_bf16 v[136:139], v[204:207], v[68:71], v[136:139]\n"
      "ds_read_b128 v[68:71], v233 offset:8192\n"
      "v_mfma_f32_16x16x32_bf16 v[136:139], a[24:27], v[84:87], v[136:139]\n"
      "ds_read_b128 v[84:87], v234 offset:8192\n"
      "v_mfma_f32_16x16x32_bf16 v[136:139], a[28:31], v[88:91], v[136:139]\n"
      "ds_read_b128 v[88:91], v235 offset:8192\n"
      "v_mfma_f32_16x16x32_bf16 v[136:139], a[32:35], v[92:95], v[136:139]\n"
      "ds_read_b128 v[92:95], v232 offset:16384\n"
      "v_mfma_f32_16x16x32_bf16 v[136:139], a[36:39], v[108:111], v[136:139]\n"
      "ds_read_b128 v[108:111], v233 offset:16384\n"
      "v_mfma_f32_16x16x32_bf16 v[136:139], a[40:43], v[112:115], v[136:139]\n"
      "ds_read_b128 v[112:115], v234 offset:16384\n"
      "v_mfma_f32_16x16x32_bf16 v[136:139], a[44:47], v[116:119], v[136:139]\n"
      "ds_read_b128 v[116:119], v235 offset:16384\n"
      "s_waitcnt lgkmcnt(8)\n"
      "v_mfma_f32_16x16x32_bf16 v[132:135], a[240:243], v[36:39], v[0:3]\n"
      "ds_write_b64 v244, v[208:209] offset:12416\n"
      "s_cbranch_vccnz LT36\n"
      "LS36:\n"
      "v_mfma_f32_16x16x32_bf16 v[132:135], a[244:247], v[40:43], v[132:135]\n"
      "v_and_b32 v19, 0xffff0000, v177\n"
      "v_mfma_f32_16x16x32_bf16 v[132:135], a[248:251], v[44:47], v[132:135]\n"
      "v_lshlrev_b32 v20, 16, v178\n"
      "v_mfma_f32_16x16x32_bf16 v[132:135], a[252:255], v[60:63], v[132:135]\n"
      "v_and_b32 v21, 0xffff0000, v178\n"
      "s_waitcnt lgkmcnt(5)\n"
      "v_mfma_f32_16x16x32_bf16 v[132:135], a[48:51], v[64:67], v[132:135]\n"
      "v_lshlrev_b32 v22, 16, v179\n"
      "v_mfma_f32_16x16x32_bf16 v[132:135], a[52:55], v[68:71], v[132:135]\n"
      "v_and_b32 v23, 0xffff0000, v179\n"
      "v_mfma_f32_16x16x32_bf16 v[132:135], a[56:59], v[84:87], v[132:135]\n"
      "v_cvt_pk_bf16_f32 v210, v136, v137\n"
      "v_mfma_f32_16x16x32_bf16 v[132:135], a[60:63], v[88:91], v[132:135]\n"
      "v_cvt_pk_bf16_f32 v211, v138, v139\n"
      "s_waitcnt lgkmcnt(1)\n"
      "v_mfma_f32_16x16x32_bf16 v[132:135], a[64:67], v[92:95], v[132:135]\n"
      "ds_read_b128 v[72:75], v246 offset:0\n"
      "v_mfma_f32_16x16x32_bf16 v[132:135], a[68:71], v[108:111], v[132:135]\n"
      "ds_read_b128 v[76:79], v246 offset:1024\n"
      "v_mfma_f32_16x16x32_bf16 v[132:135], a[72:75], v[112:115], v[132:135]\n"
      "ds_read_b128 v[80:83], v246 offset:2048\n"
      "v_mfma_f32_16x16x32_bf16 v[132:135], a[76:79], v[116:119], v[132:135]\n"
      "v_mfma_f32_16x16x32_bf16 v[136:139], a[80:83], v[36:39], v[4:7]\n"
      "ds_write_b64 v244, v[210:211] offset:12424\n"
      "v_mfma_f32_16x16x32_bf16 v[136:139], a[84:87], v[40:43], v[136:139]\n"
      "v_mfma_f32_16x16x32_bf16 v[136:139], a[88:91], v[44:47], v[136:139]\n"
      "v_mfma_f32_16x16x32_bf16 v[136:139], a[92:95], v[60:63], v[136:139]\n"
      "v_cvt_pk_bf16_f32 v208, v132, v133\n"
      "s_add_u32 s54, s44, s38\n"
      "s_addc_u32 s55, s45, 0\n"
      "v_mfma_f32_16x16x32_bf16 v[136:139], a[96:99], v[64:67], v[136:139]\n"
      "v_cvt_pk_bf16_f32 v209, v134, v135\n"
      "v_mfma_f32_16x16x32_bf16 v[136:139], a[100:103], v[68:71], v[136:139]\n"
      "s_waitcnt lgkmcnt(3)\n"
      "v_mfma_f32_16x16x32_bf16 v[136:139], a[104:107], v[84:87], v[136:139]\n"
      "global_store_dwordx4 v247, v[72:75], s[54:55] offset:0 sc1\n"
      "v_mfma_f32_16x16x32_bf16 v[136:139], a[108:111], v[88:91], v[136:139]\n"
      "s_add_u32 s52, s42, s40\n"
      "s_addc_u32 s53, s43, 0\n"
      "v_mfma_f32_16x16x32_bf16 v[136:139], a[112:115], v[92:95], v[136:139]\n"
      "s_add_u32 m0, s17, 0x6000\n"
      "v_mfma_f32_16x16x32_bf16 v[136:139], a[116:119], v[108:111], v[136:139]\n"
      "ds_write_b64 v244, v[208:209] offset:24576\n"
      "v_mfma_f32_16x16x32_bf16 v[136:139], a[120:123], v[112:115], v[136:139]\n"
      "global_load_lds_dwordx4 v236, s[52:53] offset:0 nt\n"
      "v_mfma_f32_16x16x32_bf16 v[136:139], a[124:127], v[116:119], v[136:139]\n"
      "v_mfma_f32_16x16x32_bf16 v[132:135], a[128:131], v[36:39], v[8:11]\n"
      "s_cbranch_vccnz LT37\n"
      "LS37:\n"
      "v_mfma_f32_16x16x32_bf16 v[132:135], a[132:135], v[40:43], v[132:135]\n"
      "v_mfma_f32_16x16x32_bf16 v[132:135], a[136:139], v[44:47], v[132:135]\n"
      "v_mfma_f32_16x16x32_bf16 v[132:135], a[140:143], v[60:63], v[132:135]\n"
      "v_cvt_pk_bf16_f32 v210, v136, v137\n"
      "v_mfma_f32_16x16x32_bf16 v[132:135], a[144:147], v[64:67], v[132:135]\n"
      "v_cvt_pk_bf16_f32 v211, v138, v139\n"
      "v_mfma_f32_16x16x32_bf16 v[132:135], a[148:151], v[68:71], v[132:135]\n"
      "v_mfma_f32_16x16x32_bf16 v[132:135], a[152:155], v[84:87], v[132:135]\n"
      "s_waitcnt lgkmcnt(3)\n"
      "v_mfma_f32_16x16x32_bf16 v[132:135], a[156:159], v[88:91], v[132:135]\n"
      "global_store_dwordx4 v252, v[76:79], s[54:55] offset:0 sc1\n"
      "v_mfma_f32_16x16x32_bf16 v[132:135], a[160:163], v[92:95], v[132:135]\n"
      "s_add_u32 m0, s17, 0x7f00\n"
      "v_mfma_f32_16x16x32_bf16 v[132:135], a[164:167], v[108:111], v[132:135]\n"
      "ds_write_b64 v244, v[210:211] offset:24584\n"
      "v_mfma_f32_16x16x32_bf16 v[132:135], a[168:171], v[112:115], v[132:135]\n"
      "global_load_lds_dwordx4 v236, s[52:53] offset:256 nt\n"
      "v_mfma_f32_16x16x32_bf16 v[132:135], a[172:175], v[116:119], v[132:135]\n"
      "v_mfma_f32_16x16x32_bf16 v[136:139], a[176:179], v[36:39], v[12:15]\n"
      "v_mfma_f32_16x16x32_bf16 v[136:139], a[180:183], v[40:43], v[136:139]\n"
      "v_mfma_f32_16x16x32_bf16 v[136:139], a[184:187], v[44:47], v[136:139]\n"
      "v_mfma_f32_16x16x32_bf16 v[136:139], a[188:191], v[60:63], v[136:139]\n"
      "v_cvt_pk_bf16_f32 v212, v132, v133\n"
      "v_mfma_f32_16x16x32_bf16 v[136:139], a[192:195], v[64:67], v[136:139]\n"
      "v_cvt_pk_bf16_f32 v213, v134, v135\n"
      "v_mfma_f32_16x16x32_bf16 v[136:139], a[196:199], v[68:71], v[136:139]\n"
      "v_mfma_f32_16x16x32_bf16 v[136:139], a[200:203], v[84:87], v[136:139]\n"
      "v_mfma_f32_16x16x32_bf16 v[136:139], a[204:207], v[88:91], v[136:139]\n"
      "s_waitcnt lgkmcnt(3)\n"
      "v_mfma_f32_16x16x32_bf16 v[136:139], a[208:211], v[92:95], v[136:139]\n"
      "global_store_dwordx4 v253, v[80:83], s[54:55] offset:0 sc1\n"
      "v_mfma_f32_16x16x32_bf16 v[136:139], a[212:215], v[108:111], v[136:139]\n"
      "ds_write_b64 v245, v[212:213] offset:24576\n"
      "v_mfma_f32_16x16x32_bf16 v[136:139], a[216:219], v[112:115], v[136:139]\n"
      "s_add_u32 m0, s17, 0x9e00\n"
      "v_mfma_f32_16x16x32_bf16 v[136:139], a[220:223], v[116:119], v[136:139]\n"
      "global_load_lds_dwordx4 v236, s[52:53] offset:512 nt\n"
      "v_mfma_f32_16x16x32_bf16 v[132:135], a[224:227], v[36:39], v[16:19]\n"
      "s_cbranch_vccnz LT38\n"
      "LS38:\n"
      "v_mfma_f32_16x16x32_bf16 v[132:135], a[228:231], v[40:43], v[132:135]\n"
      "v_mfma_f32_16x16x32_bf16 v[132:135], a[232:235], v[44:47], v[132:135]\n"
      "v_mfma_f32_16x16x32_bf16 v[132:135], a[236:239], v[60:63], v[132:135]\n"
      "v_cvt_pk_bf16_f32 v214, v136, v137\n"
      "v_mfma_f32_16x16x32_bf16 v[132:135], a[0:3], v[64:67], v[132:135]\n"
      "v_cvt_pk_bf16_f32 v215, v138, v139\n"
      "v_mfma_f32_16x16x32_bf16 v[132:135], a[4:7], v[68:71], v[132:135]\n"
      "v_mfma_f32_16x16x32_bf16 v[132:135], a[8:11], v[84:87], v[132:135]\n"
      "v_mfma_f32_16x16x32_bf16 v[132:135], a[12:15], v[88:91], v[132:135]\n"
      "v_mfma_f32_16x16x32_bf16 v[132:135], a[16:19], v[92:95], v[132:135]\n"
      "v_mfma_f32_16x16x32_bf16 v[132:135], a[20:23], v[108:111], v[132:135]\n"
      "ds_write_b64 v245, v[214:215] offset:24584\n"
      "v_mfma_f32_16x16x32_bf16 v[132:135], v[140:143], v[112:115], v[132:135]\n"
      "v_mfma_f32_16x16x32_bf16 v[132:135], v[156:159], v[116:119], v[132:135]\n"
      "s_waitcnt lgkmcnt(4)\n"
      "s_barrier\n"
      "v_mfma_f32_16x16x32_bf16 v[136:139], v[160:163], v[36:39], v[20:23]\n"
      "ds_read_b128 v[36:39], v232 offset:256\n"
      "v_mfma_f32_16x16x32_bf16 v[136:139], v[164:167], v[40:43], v[136:139]\n"
      "ds_read_b128 v[40:43], v233 offset:256\n"
      "v_mfma_f32_16x16x32_bf16 v[136:139], v[180:183], v[44:47], v[136:139]\n"
      "ds_read_b128 v[44:47], v234 offset:256\n"
      "v_mfma_f32_16x16x32_bf16 v[136:139], v[184:187], v[60:63], v[136:139]\n"
      "ds_read_b128 v[60:63], v235 offset:256\n"
      "v_cvt_pk_bf16_f32 v208, v132, v133\n"
      "v_mfma_f32_16x16x32_bf16 v[136:139], v[188:191], v[64:67], v[136:139]\n"
      "ds_read_b128 v[64:67], v232 offset:8448\n"
      "v_cvt_pk_bf16_f32 v209, v134, v135\n"
      "v_mfma_f32_16x16x32_bf16 v[136:139], v[204:207], v[68:71], v[136:139]\n"
      "ds_read_b128 v[68:71], v233 offset:8448\n"
      "v_mfma_f32_16x16x32_bf16 v[136:139], a[24:27], v[84:87], v[136:139]\n"
      "ds_read_b128 v[84:87], v234 offset:8448\n"
      "v_mfma_f32_16x16x32_bf16 v[136:139], a[28:31], v[88:91], v[136:139]\n"
      "ds_read_b128 v[88:91], v235 offset:8448\n"
      "v_mfma_f32_16x16x32_bf16 v[136:139], a[32:35], v[92:95], v[136:139]\n"
      "ds_read_b128 v[92:95], v232 offset:16640\n"
      "v_mfma_f32_16x16x32_bf16 v[136:139], a[36:39], v[108:111], v[136:139]\n"
      "ds_read_b128 v[108:111], v233 offset:16640\n"
      "v_mfma_f32_16x16x32_bf16 v[136:139], a[40:43], v[112:115], v[136:139]\n"
      "ds_read_b128 v[112:115], v234 offset:16640\n"
      "v_mfma_f32_16x16x32_bf16 v[136:139], a[44:47], v[116:119], v[136:139]\n"
      "ds_read_b128 v[116:119], v235 offset:16640\n"
      "s_waitcnt lgkmcnt(8)\n"
      "v_mfma_f32_16x16x32_bf16 v[132:135], a[240:243], v[36:39], v[0:3]\n"
      "ds_write_b64 v244, v[208:209] offset:24704\n"
      "v_mfma_f32_16x16x32_bf16 v[132:135], a[244:247], v[40:43], v[132:135]\n"
      "s_cbranch_vccnz LT39\n"
      "LS39:\n"
      "v_mfma_f32_16x16x32_bf16 v[132:135], a[248:251], v[44:47], v[132:135]\n"
      "v_mfma_f32_16x16x32_bf16 v[132:135], a[252:255], v[60:63], v[132:135]\n"
      "v_cvt_pk_bf16_f32 v210, v136, v137\n"
      "s_waitcnt lgkmcnt(5)\n"
      "v_mfma_f32_16x16x32_bf16 v[132:135], a[48:51], v[64:67], v[132:135]\n"
      "v_cvt_pk_bf16_f32 v211, v138, v139\n"
      "v_mfma_f32_16x16x32_bf16 v[132:135], a[52:55], v[68:71], v[132:135]\n"
      "v_mfma_f32_16x16x32_bf16 v[132:135], a[56:59], v[84:87], v[132:135]\n"
      "ds_read_b128 v[96:99], v246 offset:12288\n"
      "v_mfma_f32_16x16x32_bf16 v[132:135], a[60:63], v[88:91], v[132:135]\n"
      "ds_read_b128 v[100:103], v246 offset:13312\n"
      "s_waitcnt lgkmcnt(3)\n"
      "v_mfma_f32_16x16x32_bf16 v[132:135], a[64:67], v[92:95], v[132:135]\n"
      "ds_read_b128 v[104:107], v246 offset:14336\n"
      "v_mfma_f32_16x16x32_bf16 v[132:135], a[68:71], v[108:111], v[132:135]\n"
      "ds_write_b64 v244, v[210:211] offset:24712\n"
      "v_mfma_f32_16x16x32_bf16 v[132:135], a[72:75], v[112:115], v[132:135]\n"
      "v_mfma_f32_16x16x32_bf16 v[132:135], a[76:79], v[116:119], v[132:135]\n"
      "v_mfma_f32_16x16x32_bf16 v[136:139], a[80:83], v[36:39], v[4:7]\n"
      "v_mfma_f32_16x16x32_bf16 v[136:139], a[84:87], v[40:43], v[136:139]\n"
      "v_mfma_f32_16x16x32_bf16 v[136:139], a[88:91], v[44:47], v[136:139]\n"
      "v_mfma_f32_16x16x32_bf16 v[136:139], a[92:95], v[60:63], v[136:139]\n"
      "v_cvt_pk_bf16_f32 v208, v132, v133\n"
      "v_mfma_f32_16x16x32_bf16 v[136:139], a[96:99], v[64:67], v[136:139]\n"
      "v_cvt_pk_bf16_f32 v209, v134, v135\n"
      "v_mfma_f32_16x16x32_bf16 v[136:139], a[100:103], v[68:71], v[136:139]\n"
      "v_mfma_f32_16x16x32_bf16 v[136:139], a[104:107], v[84:87], v[136:139]\n"
      "v_mfma_f32_16x16x32_bf16 v[136:139], a[108:111], v[88:91], v[136:139]\n"
      "s_add_u32 m0, s17, 0x7000\n"
      "v_mfma_f32_16x16x32_bf16 v[136:139], a[112:115], v[92:95], v[136:139]\n"
      "global_load_lds_dwordx4 v237, s[52:53] offset:0 nt\n"
      "v_mfma_f32_16x16x32_bf16 v[136:139], a[116:119], v[108:111], v[136:139]\n"
      "ds_write_b64 v244, v[208:209] offset:0\n"
      "v_mfma_f32_16x16x32_bf16 v[136:139], a[120:123], v[112:115], v[136:139]\n"
      "v_mfma_f32_16x16x32_bf16 v[136:139], a[124:127], v[116:119], v[136:139]\n"
      "v_mfma_f32_16x16x32_bf16 v[132:135], a[128:131], v[36:39], v[8:11]\n"
      "s_cbranch_vccnz LT40\n"
      "LS40:\n"
      "v_mfma_f32_16x16x32_bf16 v[132:135], a[132:135], v[40:43], v[132:135]\n"
      "v_mfma_f32_16x16x32_bf16 v[132:135], a[136:139], v[44:47], v[132:135]\n"
      "v_mfma_f32_16x16x32_bf16 v[132:135], a[140:143], v[60:63], v[132:135]\n"
      "v_cvt_pk_bf16_f32 v210, v136, v137\n"
      "v_mfma_f32_16x16x32_bf16 v[132:135], a[144:147], v[64:67], v[132:135]\n"
      "v_cvt_pk_bf16_f32 v211, v138, v139\n"
      "v_mfma_f32_16x16x32_bf16 v[132:135], a[148:151], v[68:71], v[132:135]\n"
      "v_mfma_f32_16x16x32_bf16 v[132:135], a[152:155], v[84:87], v[132:135]\n"
      "s_add_u32 m0, s17, 0x8f00\n"
      "v_mfma_f32_16x16x32_bf16 v[132:135], a[156:159], v[88:91], v[132:135]\n"
      "global_load_lds_dwordx4 v237, s[52:53] offset:256 nt\n"
      "v_mfma_f32_16x16x32_bf16 v[132:135], a[160:163], v[92:95], v[132:135]\n"
      "v_mfma_f32_16x16x32_bf16 v[132:135], a[164:167], v[108:111], v[132:135]\n"
      "ds_write_b64 v244, v[210:211] offset:8\n"
      "v_mfma_f32_16x16x32_bf16 v[132:135], a[168:171], v[112:115], v[132:135]\n"
      "v_mfma_f32_16x16x32_bf16 v[132:135], a[172:175], v[116:119], v[132:135]\n"
      "v_mfma_f32_16x16x32_bf16 v[136:139], a[176:179], v[36:39], v[12:15]\n"
      "v_mfma_f32_16x16x32_bf16 v[136:139], a[180:183], v[40:43], v[136:139]\n"
      "v_mfma_f32_16x16x32_bf16 v[136:139], a[184:187], v[44:47], v[136:139]\n"
      "v_mfma_f32_16x16x32_bf16 v[136:139], a[188:191], v[60:63], v[136:139]\n"
      "v_cvt_pk_bf16_f32 v212, v132, v133\n"
      "v_mfma_f32_16x16x32_bf16 v[136:139], a[192:195], v[64:67], v[136:139]\n"
      "v_cvt_pk_bf16_f32 v213, v134, v135\n"
      "v_mfma_f32_16x16x32_bf16 v[136:139], a[196:199], v[68:71], v[136:139]\n"
      "s_add_u32 m0, s17, 0xae00\n"
      "v_mfma_f32_16x16x32_bf16 v[136:139], a[200:203], v[84:87], v[136:139]\n"
      "global_load_lds_dwordx4 v237, s[52:53] offset:512 nt\n"
      "v_mfma_f32_16x16x32_bf16 v[136:139], a[204:207], v[88:91], v[136:139]\n"
      "v_mfma_f32_16x16x32_bf16 v[136:139], a[208:211], v[92:95], v[136:139]\n"
      "v_mfma_f32_16x16x32_bf16 v[136:139], a[212:215], v[108:111], v[136:139]\n"
      "ds_write_b64 v245, v[212:213] offset:0\n"
      "v_mfma_f32_16x16x32_bf16 v[136:139], a[216:219], v[112:115], v[136:139]\n"
      "v_mfma_f32_16x16x32_bf16 v[136:139], a[220:223], v[116:119], v[136:139]\n"
      "v_mfma_f32_16x16x32_bf16 v[132:135], a[224:227], v[36:39], v[16:19]\n"
      "s_cbranch_vccnz LT41\n"
      "LS41:\n"
      "v_mfma_f32_16x16x32_bf16 v[132:135], a[228:231], v[40:43], v[132:135]\n"
      "v_mfma_f32_16x16x32_bf16 v[132:135], a[232:235], v[44:47], v[132:135]\n"
      "v_mfma_f32_16x16x32_bf16 v[132:135], a[236:239], v[60:63], v[132:135]\n"
      "v_cvt_pk_bf16_f32 v214, v136, v137\n"
      "v_mfma_f32_16x16x32_bf16 v[132:135], a[0:3], v[64:67], v[132:135]\n"
      "v_cvt_pk_bf16_f32 v215, v138, v139\n"
      "v_mfma_f32_16x16x32_bf16 v[132:135], a[4:7], v[68:71], v[132:135]\n"
      "v_mfma_f32_16x16x32_bf16 v[132:135], a[8:11], v[84:87], v[132:135]\n"
      "v_mfma_f32_16x16x32_bf16 v[132:135], a[12:15], v[88:91], v[132:135]\n"
      "v_mfma_f32_16x16x32_bf16 v[132:135], a[16:19], v[92:95], v[132:135]\n"
      "v_mfma_f32_16x16x32_bf16 v[132:135], a[20:23], v[108:111], v[132:135]\n"
      "ds_write_b64 v245, v[214:215] offset:8\n"
      "v_mfma_f32_16x16x32_bf16 v[132:135], v[140:143], v[112:115], v[132:135]\n"
      "v_mfma_f32_16x16x32_bf16 v[132:135], v[156:159], v[116:119], v[132:135]\n"
      "s_waitcnt vmcnt(21) lgkmcnt(4)\n"
      "s_barrier\n"
      "v_mfma_f32_16x16x32_bf16 v[136:139], v[160:163], v[36:39], v[20:23]\n"
      "ds_read_b128 v[36:39], v232 offset:24576\n"
      "v_mfma_f32_16x16x32_bf16 v[136:139], v[164:167], v[40:43], v[136:139]\n"
      "ds_read_b128 v[40:43], v233 offset:24576\n"
      "v_mfma_f32_16x16x32_bf16 v[136:139], v[180:183], v[44:47], v[136:139]\n"
      "ds_read_b128 v[44:47], v234 offset:24576\n"
      "v_mfma_f32_16x16x32_bf16 v[136:139], v[184:187], v[60:63], v[136:139]\n"
      "ds_read_b128 v[60:63], v235 offset:24576\n"
      "v_cvt_pk_bf16_f32 v208, v132, v133\n"
      "v_mfma_f32_16x16x32_bf16 v[136:139], v[188:191], v[64:67], v[136:139]\n"
      "ds_read_b128 v[64:67], v232 offset:32768\n"
      "v_cvt_pk_bf16_f32 v209, v134, v135\n"
      "v_mfma_f32_16x16x32_bf16 v[136:139], v[204:207], v[68:71], v[136:139]\n"
      "ds_read_b128 v[68:71], v233 offset:32768\n"
      "v_mfma_f32_16x16x32_bf16 v[136:139], a[24:27], v[84:87], v[136:139]\n"
      "ds_read_b128 v[84:87], v234 offset:32768\n"
      "v_mfma_f32_16x16x32_bf16 v[136:139], a[28:31], v[88:91], v[136:139]\n"
      "ds_read_b128 v[88:91], v235 offset:32768\n"
      "v_mfma_f32_16x16x32_bf16 v[136:139], a[32:35], v[92:95], v[136:139]\n"
      "ds_read_b128 v[92:95], v232 offset:40960\n"
      "v_mfma_f32_16x16x32_bf16 v[136:139], a[36:39], v[108:111], v[136:139]\n"
      "ds_read_b128 v[108:111], v233 offset:40960\n"
      "v_mfma_f32_16x16x32_bf16 v[136:139], a[40:43], v[112:115], v[136:139]\n"
      "ds_read_b128 v[112:115], v234 offset:40960\n"
      "v_mfma_f32_16x16x32_bf16 v[136:139], a[44:47], v[116:119], v[136:139]\n"
      "ds_read_b128 v[116:119], v235 offset:40960\n"
      "s_waitcnt lgkmcnt(8)\n"
      "v_mfma_f32_16x16x32_bf16 v[132:135], a[240:243], v[36:39], v[0:3]\n"
      "ds_write_b64 v244, v[208:209] offset:128\n"
      "v_mfma_f32_16x16x32_bf16 v[132:135], a[244:247], v[40:43], v[132:135]\n"
      "v_mfma_f32_16x16x32_bf16 v[132:135], a[248:251], v[44:47], v[132:135]\n"
      "v_mfma_f32_16x16x32_bf16 v[132:135], a[252:255], v[60:63], v[132:135]\n"
      "v_cvt_pk_bf16_f32 v210, v136, v137\n"
      "s_waitcnt lgkmcnt(5)\n"
      "v_mfma_f32_16x16x32_bf16 v[132:135], a[48:51], v[64:67], v[132:135]\n"
      "v_cvt_pk_bf16_f32 v211, v138, v139\n"
      "v_mfma_f32_16x16x32_bf16 v[132:135], a[52:55], v[68:71], v[132:135]\n"
      "v_mfma_f32_16x16x32_bf16 v[132:135], a[56:59], v[84:87], v[132:135]\n"
      "ds_read_b128 v[120:123], v246 offset:24576\n"
      "v_mfma_f32_16x16x32_bf16 v[132:135], a[60:63], v[88:91], v[132:135]\n"
      "ds_read_b128 v[124:127], v246 offset:25600\n"
      "s_waitcnt lgkmcnt(3)\n"
      "v_mfma_f32_16x16x32_bf16 v[132:135], a[64:67], v[92:95], v[132:135]\n"
      "ds_read_b128 v[128:131], v246 offset:26624\n"
      "v_mfma_f32_16x16x32_bf16 v[132:135], a[68:71], v[108:111], v[132:135]\n"
      "ds_write_b64 v244, v[210:211] offset:136\n"
      "v_mfma_f32_16x16x32_bf16 v[132:135], a[72:75], v[112:115], v[132:135]\n"
      "v_mfma_f32_16x16x32_bf16 v[132:135], a[76:79], v[116:119], v[132:135]\n"
      "v_mfma_f32_16x16x32_bf16 v[136:139], a[80:83], v[36:39], v[4:7]\n"
      "v_mfma_f32_16x16x32_bf16 v[136:139], a[84:87], v[40:43], v[136:139]\n"
      "v_mfma_f32_16x16x32_bf16 v[136:139], a[88:91], v[44:47], v[136:139]\n"
      "v_mfma_f32_16x16x32_bf16 v[136:139], a[92:95], v[60:63], v[136:139]\n"
      "v_cvt_pk_bf16_f32 v208, v132, v133\n"
      "v_mfma_f32_16x16x32_bf16 v[136:139], a[96:99], v[64:67], v[136:139]\n"
      "v_cvt_pk_bf16_f32 v209, v134, v135\n"
      "v_mfma_f32_16x16x32_bf16 v[136:139], a[100:103], v[68:71], v[136:139]\n"
      "v_mfma_f32_16x16x32_bf16 v[136:139], a[104:107], v[84:87], v[136:139]\n"
      "v_mfma_f32_16x16x32_bf16 v[136:139], a[108:111], v[88:91], v[136:139]\n"
      "v_mfma_f32_16x16x32_bf16 v[136:139], a[112:115], v[92:95], v[136:139]\n"
      "v_mfma_f32_16x16x32_bf16 v[136:139], a[116:119], v[108:111], v[136:139]\n"
      "ds_write_b64 v244, v[208:209] offset:12288\n"
      "v_mfma_f32_16x16x32_bf16 v[136:139], a[120:123], v[112:115], v[136:139]\n"
      "v_mfma_f32_16x16x32_bf16 v[136:139], a[124:127], v[116:119], v[136:139]\n"
      "v_mfma_f32_16x16x32_bf16 v[132:135], a[128:131], v[36:39], v[8:11]\n"
      "v_mfma_f32_16x16x32_bf16 v[132:135], a[132:135], v[40:43], v[132:135]\n"
      "v_mfma_f32_16x16x32_bf16 v[132:135], a[136:139], v[44:47], v[132:135]\n"
      "v_mfma_f32_16x16x32_bf16 v[132:135], a[140:143], v[60:63], v[132:135]\n"
      "v_cvt_pk_bf16_f32 v210, v136, v137\n"
      "v_mfma_f32_16x16x32_bf16 v[132:135], a[144:147], v[64:67], v[132:135]\n"
      "v_cvt_pk_bf16_f32 v211, v138, v139\n"
      "v_mfma_f32_16x16x32_bf16 v[132:135], a[148:151], v[68:71], v[132:135]\n"
      "v_mfma_f32_16x16x32_bf16 v[132:135], a[152:155], v[84:87], v[132:135]\n"
      "v_mfma_f32_16x16x32_bf16 v[132:135], a[156:159], v[88:91], v[132:135]\n"
      "v_mfma_f32_16x16x32_bf16 v[132:135], a[160:163], v[92:95], v[132:135]\n"
      "v_mfma_f32_16x16x32_bf16 v[132:135], a[164:167], v[108:111], v[132:135]\n"
      "ds_write_b64 v244, v[210:211] offset:12296\n"
      "v_mfma_f32_16x16x32_bf16 v[132:135], a[168:171], v[112:115], v[132:135]\n"
      "v_mfma_f32_16x16x32_bf16 v[132:135], a[172:175], v[116:119], v[132:135]\n"
      "v_mfma_f32_16x16x32_bf16 v[136:139], a[176:179], v[36:39], v[12:15]\n"
      "v_mfma_f32_16x16x32_bf16 v[136:139], a[180:183], v[40:43], v[136:139]\n"
      "v_mfma_f32_16x16x32_bf16 v[136:139], a[184:187], v[44:47], v[136:139]\n"
      "v_mfma_f32_16x16x32_bf16 v[136:139], a[188:191], v[60:63], v[136:139]\n"
      "v_cvt_pk_bf16_f32 v212, v132, v133\n"
      "v_mfma_f32_16x16x32_bf16 v[136:139], a[192:195], v[64:67], v[136:139]\n"
      "v_cvt_pk_bf16_f32 v213, v134, v135\n"
      "v_mfma_f32_16x16x32_bf16 v[136:139], a[196:199], v[68:71], v[136:139]\n"
      "v_mfma_f32_16x16x32_bf16 v[136:139], a[200:203], v[84:87], v[136:139]\n"
      "v_mfma_f32_16x16x32_bf16 v[136:139], a[204:207], v[88:91], v[136:139]\n"
      "v_mfma_f32_16x16x32_bf16 v[136:139], a[208:211], v[92:95], v[136:139]\n"
      "v_mfma_f32_16x16x32_bf16 v[136:139], a[212:215], v[108:111], v[136:139]\n"
      "ds_write_b64 v245, v[212:213] offset:12288\n"
      "v_mfma_f32_16x16x32_bf16 v[136:139], a[216:219], v[112:115], v[136:139]\n"
      "v_mfma_f32_16x16x32_bf16 v[136:139], a[220:223], v[116:119], v[136:139]\n"
      "v_mfma_f32_16x16x32_bf16 v[132:135], a[224:227], v[36:39], v[16:19]\n"
      "v_mfma_f32_16x16x32_bf16 v[132:135], a[228:231], v[40:43], v[132:135]\n"
      "v_mfma_f32_16x16x32_bf16 v[132:135], a[232:235], v[44:47], v[132:135]\n"
      "v_mfma_f32_16x16x32_bf16 v[132:135], a[236:239], v[60:63], v[132:135]\n"
      "v_cvt_pk_bf16_f32 v214, v136, v137\n"
      "v_mfma_f32_16x16x32_bf16 v[132:135], a[0:3], v[64:67], v[132:135]\n"
      "v_cvt_pk_bf16_f32 v215, v138, v139\n"
      "v_mfma_f32_16x16x32_bf16 v[132:135], a[4:7], v[68:71], v[132:135]\n"
      "v_mfma_f32_16x16x32_bf16 v[132:135], a[8:11], v[84:87], v[132:135]\n"
      "v_mfma_f32_16x16x32_bf16 v[132:135], a[12:15], v[88:91], v[132:135]\n"
      "v_mfma_f32_16x16x32_bf16 v[132:135], a[16:19], v[92:95], v[132:135]\n"
      "v_mfma_f32_16x16x32_bf16 v[132:135], a[20:23], v[108:111], v[132:135]\n"
      "ds_write_b64 v245, v[214:215] offset:12296\n"
      "v_mfma_f32_16x16x32_bf16 v[132:135], v[140:143], v[112:115], v[132:135]\n"
      "v_mfma_f32_16x16x32_bf16 v[132:135], v[156:159], v[116:119], v[132:135]\n"
      "s_waitcnt lgkmcnt(4)\n"
      "s_barrier\n"
      "v_mfma_f32_16x16x32_bf16 v[136:139], v[160:163], v[36:39], v[20:23]\n"
      "ds_read_b128 v[36:39], v232 offset:24832\n"
      "v_mfma_f32_16x16x32_bf16 v[136:139], v[164:167], v[40:43], v[136:139]\n"
      "ds_read_b128 v[40:43], v233 offset:24832\n"
      "v_mfma_f32_16x16x32_bf16 v[136:139], v[180:183], v[44:47], v[136:139]\n"
      "ds_read_b128 v[44:47], v234 offset:24832\n"
      "v_mfma_f32_16x16x32_bf16 v[136:139], v[184:187], v[60:63], v[136:139]\n"
      "ds_read_b128 v[60:63], v235 offset:24832\n"
      "v_cvt_pk_bf16_f32 v208, v132, v133\n"
      "v_mfma_f32_16x16x32_bf16 v[136:139], v[188:191], v[64:67], v[136:139]\n"
      "ds_read_b128 v[64:67], v232 offset:33024\n"
      "v_cvt_pk_bf16_f32 v209, v134, v135\n"
      "v_mfma_f32_16x16x32_bf16 v[136:139], v[204:207], v[68:71], v[136:139]\n"
      "ds_read_b128 v[68:71], v233 offset:33024\n"
      "v_mfma_f32_16x16x32_bf16 v[136:139], a[24:27], v[84:87], v[136:139]\n"
      "LPD15:\n"
      "s_cbranch_execz LRET\n"
      "ds_read_b128 v[84:87], v234 offset:33024\n"
      "v_mfma_f32_16x16x32_bf16 v[136:139], a[28:31], v[88:91], v[136:139]\n"
      "ds_read_b128 v[88:91], v235 offset:33024\n"
      "v_mfma_f32_16x16x32_bf16 v[136:139], a[32:35], v[92:95], v[136:139]\n"
      "ds_read_b128 v[92:95], v232 offset:41216\n"
      "v_mfma_f32_16x16x32_bf16 v[136:139], a[36:39], v[108:111], v[136:139]\n"
      "ds_read_b128 v[108:111], v233 offset:41216\n"
      "ds_write_b64 v244, v[208:209] offset:12416\n"
      "v_mfma_f32_16x16x32_bf16 v[136:139], a[40:43], v[112:115], v[136:139]\n"
      "ds_read_b128 v[112:115], v234 offset:41216\n"
      "v_mfma_f32_16x16x32_bf16 v[136:139], a[44:47], v[116:119], v[136:139]\n"
      "ds_read_b128 v[116:119], v235 offset:41216\n"
      "s_waitcnt lgkmcnt(9)\n"
      "v_mfma_f32_16x16x32_bf16 v[132:135], a[240:243], v[36:39], v[0:3]\n"
      "v_add_u32 v232, 0xffff4000, v232\n"
      "v_mfma_f32_16x16x32_bf16 v[132:135], a[244:247], v[40:43], v[132:135]\n"
      "v_add_u32 v233, 0xffff4000, v233\n"
      "v_mfma_f32_16x16x32_bf16 v[132:135], a[248:251], v[44:47], v[132:135]\n"
      "v_add_u32 v234, 0xffff4000, v234\n"
      "v_mfma_f32_16x16x32_bf16 v[132:135], a[252:255], v[60:63], v[132:135]\n"
      "v_add_u32 v235, 0xffff4000, v235\n"
      "s_waitcnt lgkmcnt(5)\n"
      "v_mfma_f32_16x16x32_bf16 v[132:135], a[48:51], v[64:67], v[132:135]\n"
      "v_cvt_pk_bf16_f32 v210, v136, v137\n"
      "v_mfma_f32_16x16x32_bf16 v[132:135], a[52:55], v[68:71], v[132:135]\n"
      "v_cvt_pk_bf16_f32 v211, v138, v139\n"
      "v_mfma_f32_16x16x32_bf16 v[132:135], a[56:59], v[84:87], v[132:135]\n"
      "ds_read_b128 v[144:147], v246 offset:0\n"
      "v_mfma_f32_16x16x32_bf16 v[132:135], a[60:63], v[88:91], v[132:135]\n"
      "ds_read_b128 v[148:151], v246 offset:1024\n"
      "s_waitcnt lgkmcnt(2)\n"
      "v_mfma_f32_16x16x32_bf16 v[132:135], a[64:67], v[92:95], v[132:135]\n"
      "ds_read_b128 v[152:155], v246 offset:2048\n"
      "v_mfma_f32_16x16x32_bf16 v[132:135], a[68:71], v[108:111], v[132:135]\n"
      "v_lshlrev_b32 v0, 16, v192\n"
      "v_mfma_f32_16x16x32_bf16 v[132:135], a[72:75], v[112:115], v[132:135]\n"
      "ds_write_b64 v244, v[210:211] offset:12424\n"
      "v_mfma_f32_16x16x32_bf16 v[132:135], a[76:79], v[116:119], v[132:135]\n"
      "v_and_b32 v1, 0xffff0000, v192\n"
      "v_mfma_f32_16x16x32_bf16 v[136:139], a[80:83], v[36:39], v[4:7]\n"
      "v_lshlrev_b32 v2, 16, v193\n"
      "v_mfma_f32_16x16x32_bf16 v[136:139], a[84:87], v[40:43], v[136:139]\n"
      "v_and_b32 v3, 0xffff0000, v193\n"
      "v_mfma_f32_16x16x32_bf16 v[136:139], a[88:91], v[44:47], v[136:139]\n"
      "v_mfma_f32_16x16x32_bf16 v[136:139], a[92:95], v[60:63], v[136:139]\n"
      "v_cvt_pk_bf16_f32 v208, v132, v133\n"
      "v_mfma_f32_16x16x32_bf16 v[136:139], a[96:99], v[64:67], v[136:139]\n"
      "v_cvt_pk_bf16_f32 v209, v134, v135\n"
      "v_mfma_f32_16x16x32_bf16 v[136:139], a[100:103], v[68:71], v[136:139]\n"
      "v_lshlrev_b32 v4, 16, v194\n"
      "v_mfma_f32_16x16x32_bf16 v[136:139], a[104:107], v[84:87], v[136:139]\n"
      "v_and_b32 v5, 0xffff0000, v194\n"
      "v_mfma_f32_16x16x32_bf16 v[136:139], a[108:111], v[88:91], v[136:139]\n"
      "v_lshlrev_b32 v6, 16, v195\n"
      "v_mfma_f32_16x16x32_bf16 v[136:139], a[112:115], v[92:95], v[136:139]\n"
      "v_and_b32 v7, 0xffff0000, v195\n"
      "v_mfma_f32_16x16x32_bf16 v[136:139], a[116:119], v[108:111], v[136:139]\n"
      "ds_write_b64 v244, v[208:209] offset:24576\n"
      "v_mfma_f32_16x16x32_bf16 v[136:139], a[120:123], v[112:115], v[136:139]\n"
      "v_mfma_f32_16x16x32_bf16 v[136:139], a[124:127], v[116:119], v[136:139]\n"
      "v_mfma_f32_16x16x32_bf16 v[132:135], a[128:131], v[36:39], v[8:11]\n"
      "v_mfma_f32_16x16x32_bf16 v[132:135], a[132:135], v[40:43], v[132:135]\n"
      "v_mfma_f32_16x16x32_bf16 v[132:135], a[136:139], v[44:47], v[132:135]\n"
      "v_mfma_f32_16x16x32_bf16 v[132:135], a[140:143], v[60:63], v[132:135]\n"
      "v_cvt_pk_bf16_f32 v210, v136, v137\n"
      "v_mfma_f32_16x16x32_bf16 v[132:135], a[144:147], v[64:67], v[132:135]\n"
      "v_cvt_pk_bf16_f32 v211, v138, v139\n"
      "v_mfma_f32_16x16x32_bf16 v[132:135], a[148:151], v[68:71], v[132:135]\n"
      "v_lshlrev_b32 v8, 16, v196\n"
      "v_mfma_f32_16x16x32_bf16 v[132:135], a[152:155], v[84:87], v[132:135]\n"
      "v_and_b32 v9, 0xffff0000, v196\n"
      "v_mfma_f32_16x16x32_bf16 v[132:135], a[156:159], v[88:91], v[132:135]\n"
      "v_lshlrev_b32 v10, 16, v197\n"
      "v_mfma_f32_16x16x32_bf16 v[132:135], a[160:163], v[92:95], v[132:135]\n"
      "v_and_b32 v11, 0xffff0000, v197\n"
      "v_mfma_f32_16x16x32_bf16 v[132:135], a[164:167], v[108:111], v[132:135]\n"
      "ds_write_b64 v244, v[210:211] offset:24584\n"
      "v_mfma_f32_16x16x32_bf16 v[132:135], a[168:171], v[112:115], v[132:135]\n"
      "v_mfma_f32_16x16x32_bf16 v[132:135], a[172:175], v[116:119], v[132:135]\n"
      "v_mfma_f32_16x16x32_bf16 v[136:139], a[176:179], v[36:39], v[12:15]\n"
      "v_mfma_f32_16x16x32_bf16 v[136:139], a[180:183], v[40:43], v[136:139]\n"
      "v_mfma_f32_16x16x32_bf16 v[136:139], a[184:187], v[44:47], v[136:139]\n"
      "v_mfma_f32_16x16x32_bf16 v[136:139], a[188:191], v[60:63], v[136:139]\n"
      "v_cvt_pk_bf16_f32 v212, v132, v133\n"
      "v_mfma_f32_16x16x32_bf16 v[136:139], a[192:195], v[64:67], v[136:139]\n"
      "v_cvt_pk_bf16_f32 v213, v134, v135\n"
      "v_mfma_f32_16x16x32_bf16 v[136:139], a[196:199], v[68:71], v[136:139]\n"
      "v_lshlrev_b32 v12, 16, v198\n"
      "v_mfma_f32_16x16x32_bf16 v[136:139], a[200:203], v[84:87], v[136:139]\n"
      "v_and_b32 v13, 0xffff0000, v198\n"
      "v_mfma_f32_16x16x32_bf16 v[136:139], a[204:207], v[88:91], v[136:139]\n"
      "v_lshlrev_b32 v14, 16, v199\n"
      "v_mfma_f32_16x16x32_bf16 v[136:139], a[208:211], v[92:95], v[136:139]\n"
      "v_and_b32 v15, 0xffff0000, v199\n"
      "v_mfma_f32_16x16x32_bf16 v[136:139], a[212:215], v[108:111], v[136:139]\n"
      "ds_write_b64 v245, v[212:213] offset:24576\n"
      "v_mfma_f32_16x16x32_bf16 v[136:139], a[216:219], v[112:115], v[136:139]\n"
      "v_mfma_f32_16x16x32_bf16 v[136:139], a[220:223], v[116:119], v[136:139]\n"
      "v_mfma_f32_16x16x32_bf16 v[132:135], a[224:227], v[36:39], v[16:19]\n"
      "v_mfma_f32_16x16x32_bf16 v[132:135], a[228:231], v[40:43], v[132:135]\n"
      "v_mfma_f32_16x16x32_bf16 v[132:135], a[232:235], v[44:47], v[132:135]\n"
      "v_mfma_f32_16x16x32_bf16 v[132:135], a[236:239], v[60:63], v[132:135]\n"
      "v_cvt_pk_bf16_f32 v214, v136, v137\n"
      "v_mfma_f32_16x16x32_bf16 v[132:135], a[0:3], v[64:67], v[132:135]\n"
      "v_cvt_pk_bf16_f32 v215, v138, v139\n"
      "v_mfma_f32_16x16x32_bf16 v[132:135], a[4:7], v[68:71], v[132:135]\n"
      "v_lshlrev_b32 v16, 16, v200\n"
      "v_mfma_f32_16x16x32_bf16 v[132:135], a[8:11], v[84:87], v[132:135]\n"
      "v_and_b32 v17, 0xffff0000, v200\n"
      "v_mfma_f32_16x16x32_bf16 v[132:135], a[12:15], v[88:91], v[132:135]\n"
      "v_lshlrev_b32 v18, 16, v201\n"
      "v_mfma_f32_16x16x32_bf16 v[132:135], a[16:19], v[92:95], v[132:135]\n"
      "v_and_b32 v19, 0xffff0000, v201\n"
      "v_mfma_f32_16x16x32_bf16 v[132:135], a[20:23], v[108:111], v[132:135]\n"
      "ds_write_b64 v245, v[214:215] offset:24584\n"
      "v_mfma_f32_16x16x32_bf16 v[132:135], v[140:143], v[112:115], v[132:135]\n"
      "v_mfma_f32_16x16x32_bf16 v[132:135], v[156:159], v[116:119], v[132:135]\n"
      "s_waitcnt vmcnt(9) lgkmcnt(4)\n"
      "s_barrier\n"
      "v_mfma_f32_16x16x32_bf16 v[136:139], v[160:163], v[36:39], v[20:23]\n"
      "ds_read_b128 v[36:39], v232 offset:0\n"
      "v_mfma_f32_16x16x32_bf16 v[136:139], v[164:167], v[40:43], v[136:139]\n"
      "ds_read_b128 v[40:43], v233 offset:0\n"
      "v_mfma_f32_16x16x32_bf16 v[136:139], v[180:183], v[44:47], v[136:139]\n"
      "ds_read_b128 v[44:47], v234 offset:0\n"
      "v_mfma_f32_16x16x32_bf16 v[136:139], v[184:187], v[60:63], v[136:139]\n"
      "ds_read_b128 v[60:63], v235 offset:0\n"
      "v_cvt_pk_bf16_f32 v208, v132, v133\n"
      "v_mfma_f32_16x16x32_bf16 v[136:139], v[188:191], v[64:67], v[136:139]\n"
      "ds_read_b128 v[64:67], v232 offset:8192\n"
      "v_cvt_pk_bf16_f32 v209, v134, v135\n"
      "v_mfma_f32_16x16x32_bf16 v[136:139], v[204:207], v[68:71], v[136:139]\n"
      "ds_read_b128 v[68:71], v233 offset:8192\n"
      "v_mfma_f32_16x16x32_bf16 v[136:139], a[24:27], v[84:87], v[136:139]\n"
      "ds_read_b128 v[84:87], v234 offset:8192\n"
      "v_mfma_f32_16x16x32_bf16 v[136:139], a[28:31], v[88:91], v[136:139]\n"
      "ds_read_b128 v[88:91], v235 offset:8192\n"
      "v_mfma_f32_16x16x32_bf16 v[136:139], a[32:35], v[92:95], v[136:139]\n"
      "ds_read_b128 v[92:95], v232 offset:16384\n"
      "v_mfma_f32_16x16x32_bf16 v[136:139], a[36:39], v[108:111], v[136:139]\n"
      "ds_read_b128 v[108:111], v233 offset:16384\n"
      "v_mfma_f32_16x16x32_bf16 v[136:139], a[40:43], v[112:115], v[136:139]\n"
      "ds_read_b128 v[112:115], v234 offset:16384\n"
      "v_mfma_f32_16x16x32_bf16 v[136:139], a[44:47], v[116:119], v[136:139]\n"
      "ds_read_b128 v[116:119], v235 offset:16384\n"
      "s_waitcnt lgkmcnt(8)\n"
      "v_mfma_f32_16x16x32_bf16 v[132:135], a[240:243], v[36:39], v[0:3]\n"
      "ds_write_b64 v244, v[208:209] offset:24704\n"
      "v_mfma_f32_16x16x32_bf16 v[132:135], a[244:247], v[40:43], v[132:135]\n"
      "v_lshlrev_b32 v20, 16, v202\n"
      "v_mfma_f32_16x16x32_bf16 v[132:135], a[248:251], v[44:47], v[132:135]\n"
      "v_and_b32 v21, 0xffff0000, v202\n"
      "v_mfma_f32_16x16x32_bf16 v[132:135], a[252:255], v[60:63], v[132:135]\n"
      "v_lshlrev_b32 v22, 16, v203\n"
      "global_store_dwordx4 v247, v[96:99], s[54:55] offset:768 sc1\n"
      "s_waitcnt lgkmcnt(5)\n"
      "v_mfma_f32_16x16x32_bf16 v[132:135], a[48:51], v[64:67], v[132:135]\n"
      "v_and_b32 v23, 0xffff0000, v203\n"
      "v_mfma_f32_16x16x32_bf16 v[132:135], a[52:55], v[68:71], v[132:135]\n"
      "v_cvt_pk_bf16_f32 v210, v136, v137\n"
      "v_mfma_f32_16x16x32_bf16 v[132:135], a[56:59], v[84:87], v[132:135]\n"
      "v_cvt_pk_bf16_f32 v211, v138, v139\n"
      "v_mfma_f32_16x16x32_bf16 v[132:135], a[60:63], v[88:91], v[132:135]\n"
      "ds_read_b128 v[24:27], v246 offset:12288\n"
      "s_waitcnt lgkmcnt(2)\n"
      "v_mfma_f32_16x16x32_bf16 v[132:135], a[64:67], v[92:95], v[132:135]\n"
      "ds_read_b128 v[28:31], v246 offset:13312\n"
      "v_mfma_f32_16x16x32_bf16 v[132:135], a[68:71], v[108:111], v[132:135]\n"
      "ds_read_b128 v[32:35], v246 offset:14336\n"
      "v_mfma_f32_16x16x32_bf16 v[132:135], a[72:75], v[112:115], v[132:135]\n"
      "v_mfma_f32_16x16x32_bf16 v[132:135], a[76:79], v[116:119], v[132:135]\n"
      "ds_write_b64 v244, v[210:211] offset:24712\n"
      "v_mfma_f32_16x16x32_bf16 v[136:139], a[80:83], v[36:39], v[4:7]\n"
      "v_mfma_f32_16x16x32_bf16 v[136:139], a[84:87], v[40:43], v[136:139]\n"
      "v_mfma_f32_16x16x32_bf16 v[136:139], a[88:91], v[44:47], v[136:139]\n"
      "v_mfma_f32_16x16x32_bf16 v[136:139], a[92:95], v[60:63], v[136:139]\n"
      "v_cvt_pk_bf16_f32 v208, v132, v133\n"
      "global_store_dwordx4 v252, v[100:103], s[54:55] offset:768 sc1\n"
      "v_mfma_f32_16x16x32_bf16 v[136:139], a[96:99], v[64:67], v[136:139]\n"
      "v_cvt_pk_bf16_f32 v209, v134, v135\n"
      "v_mfma_f32_16x16x32_bf16 v[136:139], a[100:103], v[68:71], v[136:139]\n"
      "v_mfma_f32_16x16x32_bf16 v[136:139], a[104:107], v[84:87], v[136:139]\n"
      "v_mfma_f32_16x16x32_bf16 v[136:139], a[108:111], v[88:91], v[136:139]\n"
      "v_mfma_f32_16x16x32_bf16 v[136:139], a[112:115], v[92:95], v[136:139]\n"
      "v_mfma_f32_16x16x32_bf16 v[136:139], a[116:119], v[108:111], v[136:139]\n"
      "ds_write_b64 v244, v[208:209] offset:0\n"
      "v_mfma_f32_16x16x32_bf16 v[136:139], a[120:123], v[112:115], v[136:139]\n"
      "v_mfma_f32_16x16x32_bf16 v[136:139], a[124:127], v[116:119], v[136:139]\n"
      "v_mfma_f32_16x16x32_bf16 v[132:135], a[128:131], v[36:39], v[8:11]\n"
      "v_mfma_f32_16x16x32_bf16 v[132:135], a[132:135], v[40:43], v[132:135]\n"
      "v_mfma_f32_16x16x32_bf16 v[132:135], a[136:139], v[44:47], v[132:135]\n"
      "v_mfma_f32_16x16x32_bf16 v[132:135], a[140:143], v[60:63], v[132:135]\n"
      "v_cvt_pk_bf16_f32 v210, v136, v137\n"
      "v_mfma_f32_16x16x32_bf16 v[132:135], a[144:147], v[64:67], v[132:135]\n"
      "v_cvt_pk_bf16_f32 v211, v138, v139\n"
      "v_mfma_f32_16x16x32_bf16 v[132:135], a[148:151], v[68:71], v[132:135]\n"
      "global_store_dwordx4 v253, v[104:107], s[54:55] offset:768 sc1\n"
      "v_mfma_f32_16x16x32_bf16 v[132:135], a[152:155], v[84:87], v[132:135]\n"
      "v_mfma_f32_16x16x32_bf16 v[132:135], a[156:159], v[88:91], v[132:135]\n"
      "s_lshl_b32 s79, s96, 1\n"
      "v_mfma_f32_16x16x32_bf16 v[132:135], a[160:163], v[92:95], v[132:135]\n"
      "v_mad_i32_i24 v252, v251, s79, v252\n"
      "v_mfma_f32_16x16x32_bf16 v[132:135], a[164:167], v[108:111], v[132:135]\n"
      "ds_write_b64 v244, v[210:211] offset:8\n"
      "v_mfma_f32_16x16x32_bf16 v[132:135], a[168:171], v[112:115], v[132:135]\n"
      "s_mul_i32 s79, s79, s100\n"
      "v_mfma_f32_16x16x32_bf16 v[132:135], a[172:175], v[116:119], v[132:135]\n"
      "v_add_u32 v247, s79, v247\n"
      "v_mfma_f32_16x16x32_bf16 v[136:139], a[176:179], v[36:39], v[12:15]\n"
      "s_mul_i32 s79, s79, s101\n"
      "v_mfma_f32_16x16x32_bf16 v[136:139], a[180:183], v[40:43], v[136:139]\n"
      "v_add_u32 v253, s79, v253\n"
      "v_mfma_f32_16x16x32_bf16 v[136:139], a[184:187], v[44:47], v[136:139]\n"
      "s_add_u32 s54, s30, s39\n"
      "s_addc_u32 s55, s31, 0\n"
      "v_mfma_f32_16x16x32_bf16 v[136:139], a[188:191], v[60:63], v[136:139]\n"
      "v_cvt_pk_bf16_f32 v212, v132, v133\n"
      "v_mfma_f32_16x16x32_bf16 v[136:139], a[192:195], v[64:67], v[136:139]\n"
      "v_cvt_pk_bf16_f32 v213, v134, v135\n"
      "v_mfma_f32_16x16x32_bf16 v[136:139], a[196:199], v[68:71], v[136:139]\n"
      "global_store_dwordx4 v247, v[120:123], s[54:55] offset:0 sc1\n"
      "v_mfma_f32_16x16x32_bf16 v[136:139], a[200:203], v[84:87], v[136:139]\n"
      "v_mfma_f32_16x16x32_bf16 v[136:139], a[204:207], v[88:91], v[136:139]\n"
      "v_mfma_f32_16x16x32_bf16 v[136:139], a[208:211], v[92:95], v[136:139]\n"
      "v_mfma_f32_16x16x32_bf16 v[136:139], a[212:215], v[108:111], v[136:139]\n"
      "ds_write_b64 v245, v[212:213] offset:0\n"
      "v_mfma_f32_16x16x32_bf16 v[136:139], a[216:219], v[112:115], v[136:139]\n"
      "v_mfma_f32_16x16x32_bf16 v[136:139], a[220:223], v[116:119], v[136:139]\n"
      "v_mfma_f32_16x16x32_bf16 v[132:135], a[224:227], v[36:39], v[16:19]\n"
      "v_mfma_f32_16x16x32_bf16 v[132:135], a[228:231], v[40:43], v[132:135]\n"
      "v_mfma_f32_16x16x32_bf16 v[132:135], a[232:235], v[44:47], v[132:135]\n"
      "v_mfma_f32_16x16x32_bf16 v[132:135], a[236:239], v[60:63], v[132:135]\n"
      "v_cvt_pk_bf16_f32 v214, v136, v137\n"
      "v_mfma_f32_16x16x32_bf16 v[132:135], a[0:3], v[64:67], v[132:135]\n"
      "v_cvt_pk_bf16_f32 v215, v138, v139\n"
      "v_mfma_f32_16x16x32_bf16 v[132:135], a[4:7], v[68:71], v[132:135]\n"
      "v_mfma_f32_16x16x32_bf16 v[132:135], a[8:11], v[84:87], v[132:135]\n"
      "global_store_dwordx4 v252, v[124:127], s[54:55] offset:0 sc1\n"
      "v_mfma_f32_16x16x32_bf16 v[132:135], a[12:15], v[88:91], v[132:135]\n"
      "v_mfma_f32_16x16x32_bf16 v[132:135], a[16:19], v[92:95], v[132:135]\n"
      "v_mfma_f32_16x16x32_bf16 v[132:135], a[20:23], v[108:111], v[132:135]\n"
      "ds_write_b64 v245, v[214:215] offset:8\n"
      "v_mfma_f32_16x16x32_bf16 v[132:135], v[140:143], v[112:115], v[132:135]\n"
      "v_mfma_f32_16x16x32_bf16 v[132:135], v[156:159], v[116:119], v[132:135]\n"
      "s_waitcnt lgkmcnt(4)\n"
      "s_barrier\n"
      "v_mfma_f32_16x16x32_bf16 v[136:139], v[160:163], v[36:39], v[20:23]\n"
      "ds_read_b128 v[36:39], v232 offset:256\n"
      "v_mfma_f32_16x16x32_bf16 v[136:139], v[164:167], v[40:43], v[136:139]\n"
      "ds_read_b128 v[40:43], v233 offset:256\n"
      "v_mfma_f32_16x16x32_bf16 v[136:139], v[180:183], v[44:47], v[136:139]\n"
      "ds_read_b128 v[44:47], v234 offset:256\n"
      "v_mfma_f32_16x16x32_bf16 v[136:139], v[184:187], v[60:63], v[136:139]\n"
      "ds_read_b128 v[60:63], v235 offset:256\n"
      "v_cvt_pk_bf16_f32 v208, v132, v133\n"
      "v_mfma_f32_16x16x32_bf16 v[136:139], v[188:191], v[64:67], v[136:139]\n"
      "ds_read_b128 v[64:67], v232 offset:8448\n"
      "v_cvt_pk_bf16_f32 v209, v134, v135\n"
      "v_mfma_f32_16x16x32_bf16 v[136:139], v[204:207], v[68:71], v[136:139]\n"
      "ds_read_b128 v[68:71], v233 offset:8448\n"
      "v_mfma_f32_16x16x32_bf16 v[136:139], a[24:27], v[84:87], v[136:139]\n"
      "ds_read_b128 v[84:87], v234 offset:8448\n"
      "v_mfma_f32_16x16x32_bf16 v[136:139], a[28:31], v[88:91], v[136:139]\n"
      "ds_read_b128 v[88:91], v235 offset:8448\n"
      "v_mfma_f32_16x16x32_bf16 v[136:139], a[32:35], v[92:95], v[136:139]\n"
      "ds_read_b128 v[92:95], v232 offset:16640\n"
      "v_mfma_f32_16x16x32_bf16 v[136:139], a[36:39], v[108:111], v[136:139]\n"
      "ds_read_b128 v[108:111], v233 offset:16640\n"
      "v_mfma_f32_16x16x32_bf16 v[136:139], a[40:43], v[112:115], v[136:139]\n"
      "ds_read_b128 v[112:115], v234 offset:16640\n"
      "v_mfma_f32_16x16x32_bf16 v[136:139], a[44:47], v[116:119], v[136:139]\n"
      "ds_read_b128 v[116:119], v235 offset:16640\n"
      "s_waitcnt lgkmcnt(8)\n"
      "v_mfma_f32_16x16x32_bf16 v[132:135], a[240:243], v[36:39], v[0:3]\n"
      "ds_write_b64 v244, v[208:209] offset:128\n"
      "v_mfma_f32_16x16x32_bf16 v[132:135], a[244:247], v[40:43], v[132:135]\n"
      "v_mfma_f32_16x16x32_bf16 v[132:135], a[248:251], v[44:47], v[132:135]\n"
      "v_mfma_f32_16x16x32_bf16 v[132:135], a[252:255], v[60:63], v[132:135]\n"
      "v_cvt_pk_bf16_f32 v210, v136, v137\n"
      "global_store_dwordx4 v253, v[128:131], s[54:55] offset:0 sc1\n"
      "s_waitcnt lgkmcnt(5)\n"
      "v_mfma_f32_16x16x32_bf16 v[132:135], a[48:51], v[64:67], v[132:135]\n"
      "v_cvt_pk_bf16_f32 v211, v138, v139\n"
      "v_mfma_f32_16x16x32_bf16 v[132:135], a[52:55], v[68:71], v[132:135]\n"
      "v_mfma_f32_16x16x32_bf16 v[132:135], a[56:59], v[84:87], v[132:135]\n"
      "ds_read_b128 v[48:51], v246 offset:24576\n"
      "v_mfma_f32_16x16x32_bf16 v[132:135], a[60:63], v[88:91], v[132:135]\n"
      "ds_read_b128 v[52:55], v246 offset:25600\n"
      "s_waitcnt lgkmcnt(3)\n"
      "v_mfma_f32_16x16x32_bf16 v[132:135], a[64:67], v[92:95], v[132:135]\n"
      "ds_read_b128 v[56:59], v246 offset:26624\n"
      "v_mfma_f32_16x16x32_bf16 v[132:135], a[68:71], v[108:111], v[132:135]\n"
      "ds_write_b64 v244, v[210:211] offset:136\n"
      "v_mfma_f32_16x16x32_bf16 v[132:135], a[72:75], v[112:115], v[132:135]\n"
      "v_mfma_f32_16x16x32_bf16 v[132:135], a[76:79], v[116:119], v[132:135]\n"
      "v_mfma_f32_16x16x32_bf16 v[136:139], a[80:83], v[36:39], v[4:7]\n"
      "v_mfma_f32_16x16x32_bf16 v[136:139], a[84:87], v[40:43], v[136:139]\n"
      "v_mfma_f32_16x16x32_bf16 v[136:139], a[88:91], v[44:47], v[136:139]\n"
      "v_mfma_f32_16x16x32_bf16 v[136:139], a[92:95], v[60:63], v[136:139]\n"
      "v_cvt_pk_bf16_f32 v208, v132, v133\n"
      "global_store_dwordx4 v247, v[144:147], s[54:55] offset:768 sc1\n"
      "v_mfma_f32_16x16x32_bf16 v[136:139], a[96:99], v[64:67], v[136:139]\n"
      "v_cvt_pk_bf16_f32 v209, v134, v135\n"
      "v_mfma_f32_16x16x32_bf16 v[136:139], a[100:103], v[68:71], v[136:139]\n"
      "v_mfma_f32_16x16x32_bf16 v[136:139], a[104:107], v[84:87], v[136:139]\n"
      "v_mfma_f32_16x16x32_bf16 v[136:139], a[108:111], v[88:91], v[136:139]\n"
      "v_mfma_f32_16x16x32_bf16 v[136:139], a[112:115], v[92:95], v[136:139]\n"
      "v_mfma_f32_16x16x32_bf16 v[136:139], a[116:119], v[108:111], v[136:139]\n"
      "ds_write_b64 v244, v[208:209] offset:12288\n"
      "v_mfma_f32_16x16x32_bf16 v[136:139], a[120:123], v[112:115], v[136:139]\n"
      "v_mfma_f32_16x16x32_bf16 v[136:139], a[124:127], v[116:119], v[136:139]\n"
      "v_mfma_f32_16x16x32_bf16 v[132:135], a[128:131], v[36:39], v[8:11]\n"
      "v_mfma_f32_16x16x32_bf16 v[132:135], a[132:135], v[40:43], v[132:135]\n"
      "v_mfma_f32_16x16x32_bf16 v[132:135], a[136:139], v[44:47], v[132:135]\n"
      "v_mfma_f32_16x16x32_bf16 v[132:135], a[140:143], v[60:63], v[132:135]\n"
      "v_cvt_pk_bf16_f32 v210, v136, v137\n"
      "v_mfma_f32_16x16x32_bf16 v[132:135], a[144:147], v[64:67], v[132:135]\n"
      "v_cvt_pk_bf16_f32 v211, v138, v139\n"
      "v_mfma_f32_16x16x32_bf16 v[132:135], a[148:151], v[68:71], v[132:135]\n"
      "global_store_dwordx4 v252, v[148:151], s[54:55] offset:768 sc1\n"
      "v_mfma_f32_16x16x32_bf16 v[132:135], a[152:155], v[84:87], v[132:135]\n"
      "v_mfma_f32_16x16x32_bf16 v[132:135], a[156:159], v[88:91], v[132:135]\n"
      "v_mfma_f32_16x16x32_bf16 v[132:135], a[160:163], v[92:95], v[132:135]\n"
      "v_mfma_f32_16x16x32_bf16 v[132:135], a[164:167], v[108:111], v[132:135]\n"
      "ds_write_b64 v244, v[210:211] offset:12296\n"
      "v_mfma_f32_16x16x32_bf16 v[132:135], a[168:171], v[112:115], v[132:135]\n"
      "v_mfma_f32_16x16x32_bf16 v[132:135], a[172:175], v[116:119], v[132:135]\n"
      "v_mfma_f32_16x16x32_bf16 v[136:139], a[176:179], v[36:39], v[12:15]\n"
      "v_mfma_f32_16x16x32_bf16 v[136:139], a[180:183], v[40:43], v[136:139]\n"
      "v_mfma_f32_16x16x32_bf16 v[136:139], a[184:187], v[44:47], v[136:139]\n"
      "v_mfma_f32_16x16x32_bf16 v[136:139], a[188:191], v[60:63], v[136:139]\n"
      "v_cvt_pk_bf16_f32 v212, v132, v133\n"
      "v_mfma_f32_16x16x32_bf16 v[136:139], a[192:195], v[64:67], v[136:139]\n"
      "v_cvt_pk_bf16_f32 v213, v134, v135\n"
      "v_mfma_f32_16x16x32_bf16 v[136:139], a[196:199], v[68:71], v[136:139]\n"
      "v_mfma_f32_16x16x32_bf16 v[136:139], a[200:203], v[84:87], v[136:139]\n"
      "global_store_dwordx4 v253, v[152:155], s[54:55] offset:768 sc1\n"
      "v_mfma_f32_16x16x32_bf16 v[136:139], a[204:207], v[88:91], v[136:139]\n"
      "v_mfma_f32_16x16x32_bf16 v[136:139], a[208:211], v[92:95], v[136:139]\n"
      "v_mfma_f32_16x16x32_bf16 v[136:139], a[212:215], v[108:111], v[136:139]\n"
      "ds_write_b64 v245, v[212:213] offset:12288\n"
      "v_mfma_f32_16x16x32_bf16 v[136:139], a[216:219], v[112:115], v[136:139]\n"
      "v_mfma_f32_16x16x32_bf16 v[136:139], a[220:223], v[116:119], v[136:139]\n"
      "v_mfma_f32_16x16x32_bf16 v[132:135], a[224:227], v[36:39], v[16:19]\n"
      "v_mfma_f32_16x16x32_bf16 v[132:135], a[228:231], v[40:43], v[132:135]\n"
      "v_mfma_f32_16x16x32_bf16 v[132:135], a[232:235], v[44:47], v[132:135]\n"
      "v_mfma_f32_16x16x32_bf16 v[132:135], a[236:239], v[60:63], v[132:135]\n"
      "v_cvt_pk_bf16_f32 v214, v136, v137\n"
      "s_add_u32 s54, s44, s39\n"
      "s_addc_u32 s55, s45, 0\n"
      "v_mfma_f32_16x16x32_bf16 v[132:135], a[0:3], v[64:67], v[132:135]\n"
      "v_cvt_pk_bf16_f32 v215, v138, v139\n"
      "v_mfma_f32_16x16x32_bf16 v[132:135], a[4:7], v[68:71], v[132:135]\n"
      "v_mfma_f32_16x16x32_bf16 v[132:135], a[8:11], v[84:87], v[132:135]\n"
      "global_store_dwordx4 v247, v[24:27], s[54:55] offset:0 sc1\n"
      "v_mfma_f32_16x16x32_bf16 v[132:135], a[12:15], v[88:91], v[132:135]\n"
      "v_mfma_f32_16x16x32_bf16 v[132:135], a[16:19], v[92:95], v[132:135]\n"
      "v_mfma_f32_16x16x32_bf16 v[132:135], a[20:23], v[108:111], v[132:135]\n"
      "ds_write_b64 v245, v[214:215] offset:12296\n"
      "v_mfma_f32_16x16x32_bf16 v[132:135], v[140:143], v[112:115], v[132:135]\n"
      "v_mfma_f32_16x16x32_bf16 v[132:135], v[156:159], v[116:119], v[132:135]\n"
      "s_waitcnt vmcnt(10) lgkmcnt(4)\n"
      "s_barrier\n"
      "v_mfma_f32_16x16x32_bf16 v[136:139], v[160:163], v[36:39], v[20:23]\n"
      "ds_read_b128 v[36:39], v232 offset:24576\n"
      "v_mfma_f32_16x16x32_bf16 v[136:139], v[164:167], v[40:43], v[136:139]\n"
      "ds_read_b128 v[40:43], v233 offset:24576\n"
      "v_mfma_f32_16x16x32_bf16 v[136:139], v[180:183], v[44:47], v[136:139]\n"
      "ds_read_b128 v[44:47], v234 offset:24576\n"
      "v_mfma_f32_16x16x32_bf16 v[136:139], v[184:187], v[60:63], v[136:139]\n"
      "ds_read_b128 v[60:63], v235 offset:24576\n"
      "v_cvt_pk_bf16_f32 v208, v132, v133\n"
      "v_mfma_f32_16x16x32_bf16 v[136:139], v[188:191], v[64:67], v[136:139]\n"
      "ds_read_b128 v[64:67], v232 offset:32768\n"
      "v_cvt_pk_bf16_f32 v209, v134, v135\n"
      "v_mfma_f32_16x16x32_bf16 v[136:139], v[204:207], v[68:71], v[136:139]\n"
      "ds_read_b128 v[68:71], v233 offset:32768\n"
      "v_mfma_f32_16x16x32_bf16 v[136:139], a[24:27], v[84:87], v[136:139]\n"
      "ds_read_b128 v[84:87], v234 offset:32768\n"
      "v_mfma_f32_16x16x32_bf16 v[136:139], a[28:31], v[88:91], v[136:139]\n"
      "ds_read_b128 v[88:91], v235 offset:32768\n"
      "v_mfma_f32_16x16x32_bf16 v[136:139], a[32:35], v[92:95], v[136:139]\n"
      "ds_read_b128 v[92:95], v232 offset:40960\n"
      "v_mfma_f32_16x16x32_bf16 v[136:139], a[36:39], v[108:111], v[136:139]\n"
      "ds_read_b128 v[108:111], v233 offset:40960\n"
      "v_mfma_f32_16x16x32_bf16 v[136:139], a[40:43], v[112:115], v[136:139]\n"
      "ds_read_b128 v[112:115], v234 offset:40960\n"
      "v_mfma_f32_16x16x32_bf16 v[136:139], a[44:47], v[116:119], v[136:139]\n"
      "ds_read_b128 v[116:119], v235 offset:40960\n"
      "s_waitcnt lgkmcnt(8)\n"
      "v_mfma_f32_16x16x32_bf16 v[132:135], a[240:243], v[36:39], v[0:3]\n"
      "ds_write_b64 v244, v[208:209] offset:12416\n"
      "v_mfma_f32_16x16x32_bf16 v[132:135], a[244:247], v[40:43], v[132:135]\n"
      "v_mfma_f32_16x16x32_bf16 v[132:135], a[248:251], v[44:47], v[132:135]\n"
      "v_mfma_f32_16x16x32_bf16 v[132:135], a[252:255], v[60:63], v[132:135]\n"
      "v_cvt_pk_bf16_f32 v210, v136, v137\n"
      "global_store_dwordx4 v252, v[28:31], s[54:55] offset:0 sc1\n"
      "s_waitcnt lgkmcnt(5)\n"
      "v_mfma_f32_16x16x32_bf16 v[132:135], a[48:51], v[64:67], v[132:135]\n"
      "v_cvt_pk_bf16_f32 v211, v138, v139\n"
      "v_mfma_f32_16x16x32_bf16 v[132:135], a[52:55], v[68:71], v[132:135]\n"
      "v_mfma_f32_16x16x32_bf16 v[132:135], a[56:59], v[84:87], v[132:135]\n"
      "ds_read_b128 v[72:75], v246 offset:0\n"
      "v_mfma_f32_16x16x32_bf16 v[132:135], a[60:63], v[88:91], v[132:135]\n"
      "ds_read_b128 v[76:79], v246 offset:1024\n"
      "s_waitcnt lgkmcnt(3)\n"
      "v_mfma_f32_16x16x32_bf16 v[132:135], a[64:67], v[92:95], v[132:135]\n"
      "ds_read_b128 v[80:83], v246 offset:2048\n"
      "v_mfma_f32_16x16x32_bf16 v[132:135], a[68:71], v[108:111], v[132:135]\n"
      "ds_write_b64 v244, v[210:211] offset:12424\n"
      "v_mfma_f32_16x16x32_bf16 v[132:135], a[72:75], v[112:115], v[132:135]\n"
      "v_mfma_f32_16x16x32_bf16 v[132:135], a[76:79], v[116:119], v[132:135]\n"
      "v_mfma_f32_16x16x32_bf16 v[136:139], a[80:83], v[36:39], v[4:7]\n"
      "v_mfma_f32_16x16x32_bf16 v[136:139], a[84:87], v[40:43], v[136:139]\n"
      "v_mfma_f32_16x16x32_bf16 v[136:139], a[88:91], v[44:47], v[136:139]\n"
      "v_mfma_f32_16x16x32_bf16 v[136:139], a[92:95], v[60:63], v[136:139]\n"
      "v_cvt_pk_bf16_f32 v208, v132, v133\n"
      "global_store_dwordx4 v253, v[32:35], s[54:55] offset:0 sc1\n"
      "v_mfma_f32_16x16x32_bf16 v[136:139], a[96:99], v[64:67], v[136:139]\n"
      "v_cvt_pk_bf16_f32 v209, v134, v135\n"
      "v_mfma_f32_16x16x32_bf16 v[136:139], a[100:103], v[68:71], v[136:139]\n"
      "v_mfma_f32_16x16x32_bf16 v[136:139], a[104:107], v[84:87], v[136:139]\n"
      "v_mfma_f32_16x16x32_bf16 v[136:139], a[108:111], v[88:91], v[136:139]\n"
      "v_mfma_f32_16x16x32_bf16 v[136:139], a[112:115], v[92:95], v[136:139]\n"
      "v_mfma_f32_16x16x32_bf16 v[136:139], a[116:119], v[108:111], v[136:139]\n"
      "ds_write_b64 v244, v[208:209] offset:24576\n"
      "v_mfma_f32_16x16x32_bf16 v[136:139], a[120:123], v[112:115], v[136:139]\n"
      "v_mfma_f32_16x16x32_bf16 v[136:139], a[124:127], v[116:119], v[136:139]\n"
      "v_mfma_f32_16x16x32_bf16 v[132:135], a[128:131], v[36:39], v[8:11]\n"
      "v_mfma_f32_16x16x32_bf16 v[132:135], a[132:135], v[40:43], v[132:135]\n"
      "v_mfma_f32_16x16x32_bf16 v[132:135], a[136:139], v[44:47], v[132:135]\n"
      "v_mfma_f32_16x16x32_bf16 v[132:135], a[140:143], v[60:63], v[132:135]\n"
      "v_cvt_pk_bf16_f32 v210, v136, v137\n"
      "v_mfma_f32_16x16x32_bf16 v[132:135], a[144:147], v[64:67], v[132:135]\n"
      "v_cvt_pk_bf16_f32 v211, v138, v139\n"
      "v_mfma_f32_16x16x32_bf16 v[132:135], a[148:151], v[68:71], v[132:135]\n"
      "global_store_dwordx4 v247, v[48:51], s[54:55] offset:768 sc1\n"
      "v_mfma_f32_16x16x32_bf16 v[132:135], a[152:155], v[84:87], v[132:135]\n"
      "v_mfma_f32_16x16x32_bf16 v[132:135], a[156:159], v[88:91], v[132:135]\n"
      "v_mfma_f32_16x16x32_bf16 v[132:135], a[160:163], v[92:95], v[132:135]\n"
      "v_mfma_f32_16x16x32_bf16 v[132:135], a[164:167], v[108:111], v[132:135]\n"
      "ds_write_b64 v244, v[210:211] offset:24584\n"
      "v_mfma_f32_16x16x32_bf16 v[132:135], a[168:171], v[112:115], v[132:135]\n"
      "v_mfma_f32_16x16x32_bf16 v[132:135], a[172:175], v[116:119], v[132:135]\n"
      "v_mfma_f32_16x16x32_bf16 v[136:139], a[176:179], v[36:39], v[12:15]\n"
      "v_mfma_f32_16x16x32_bf16 v[136:139], a[180:183], v[40:43], v[136:139]\n"
      "v_mfma_f32_16x16x32_bf16 v[136:139], a[184:187], v[44:47], v[136:139]\n"
      "v_mfma_f32_16x16x32_bf16 v[136:139], a[188:191], v[60:63], v[136:139]\n"
      "v_cvt_pk_bf16_f32 v212, v132, v133\n"
      "v_mfma_f32_16x16x32_bf16 v[136:139], a[192:195], v[64:67], v[136:139]\n"
      "v_cvt_pk_bf16_f32 v213, v134, v135\n"
      "v_mfma_f32_16x16x32_bf16 v[136:139], a[196:199], v[68:71], v[136:139]\n"
      "v_mfma_f32_16x16x32_bf16 v[136:139], a[200:203], v[84:87], v[136:139]\n"
      "global_store_dwordx4 v252, v[52:55], s[54:55] offset:768 sc1\n"
      "v_mfma_f32_16x16x32_bf16 v[136:139], a[204:207], v[88:91], v[136:139]\n"
      "v_mfma_f32_16x16x32_bf16 v[136:139], a[208:211], v[92:95], v[136:139]\n"
      "v_mfma_f32_16x16x32_bf16 v[136:139], a[212:215], v[108:111], v[136:139]\n"
      "ds_write_b64 v245, v[212:213] offset:24576\n"
      "v_mfma_f32_16x16x32_bf16 v[136:139], a[216:219], v[112:115], v[136:139]\n"
      "v_mfma_f32_16x16x32_bf16 v[136:139], a[220:223], v[116:119], v[136:139]\n"
      "v_mfma_f32_16x16x32_bf16 v[132:135], a[224:227], v[36:39], v[16:19]\n"
      "v_mfma_f32_16x16x32_bf16 v[132:135], a[228:231], v[40:43], v[132:135]\n"
      "v_mfma_f32_16x16x32_bf16 v[132:135], a[232:235], v[44:47], v[132:135]\n"
      "v_mfma_f32_16x16x32_bf16 v[132:135], a[236:239], v[60:63], v[132:135]\n"
      "v_cvt_pk_bf16_f32 v214, v136, v137\n"
      "v_mfma_f32_16x16x32_bf16 v[132:135], a[0:3], v[64:67], v[132:135]\n"
      "v_cvt_pk_bf16_f32 v215, v138, v139\n"
      "v_mfma_f32_16x16x32_bf16 v[132:135], a[4:7], v[68:71], v[132:135]\n"
      "v_mfma_f32_16x16x32_bf16 v[132:135], a[8:11], v[84:87], v[132:135]\n"
      "global_store_dwordx4 v253, v[56:59], s[54:55] offset:768 sc1\n"
      "v_mfma_f32_16x16x32_bf16 v[132:135], a[12:15], v[88:91], v[132:135]\n"
      "v_mfma_f32_16x16x32_bf16 v[132:135], a[16:19], v[92:95], v[132:135]\n"
      "v_mfma_f32_16x16x32_bf16 v[132:135], a[20:23], v[108:111], v[132:135]\n"
      "ds_write_b64 v245, v[214:215] offset:24584\n"
      "v_mfma_f32_16x16x32_bf16 v[132:135], v[140:143], v[112:115], v[132:135]\n"
      "v_mfma_f32_16x16x32_bf16 v[132:135], v[156:159], v[116:119], v[132:135]\n"
      "s_waitcnt lgkmcnt(4)\n"
      "s_barrier\n"
      "v_mfma_f32_16x16x32_bf16 v[136:139], v[160:163], v[36:39], v[20:23]\n"
      "ds_read_b128 v[36:39], v232 offset:24832\n"
      "s_lshl_b32 s79, s97, 1\n"
      "v_mfma_f32_16x16x32_bf16 v[136:139], v[164:167], v[40:43], v[136:139]\n"
      "ds_read_b128 v[40:43], v233 offset:24832\n"
      "v_mad_i32_i24 v252, v251, s79, v252\n"
      "v_mfma_f32_16x16x32_bf16 v[136:139], v[180:183], v[44:47], v[136:139]\n"
      "ds_read_b128 v[44:47], v234 offset:24832\n"
      "s_mul_i32 s79, s79, s100\n"
      "v_mfma_f32_16x16x32_bf16 v[136:139], v[184:187], v[60:63], v[136:139]\n"
      "ds_read_b128 v[60:63], v235 offset:24832\n"
      "v_cvt_pk_bf16_f32 v208, v132, v133\n"
      "v_add_u32 v247, s79, v247\n"
      "v_mfma_f32_16x16x32_bf16 v[136:139], v[188:191], v[64:67], v[136:139]\n"
      "ds_read_b128 v[64:67], v232 offset:33024\n"
      "v_cvt_pk_bf16_f32 v209, v134, v135\n"
      "v_mfma_f32_16x16x32_bf16 v[136:139], v[204:207], v[68:71], v[136:139]\n"
      "ds_read_b128 v[68:71], v233 offset:33024\n"
      "s_mul_i32 s79, s79, s101\n"
      "v_mfma_f32_16x16x32_bf16 v[136:139], a[24:27], v[84:87], v[136:139]\n"
      "ds_read_b128 v[84:87], v234 offset:33024\n"
      "v_add_u32 v253, s79, v253\n"
      "v_mfma_f32_16x16x32_bf16 v[136:139], a[28:31], v[88:91], v[136:139]\n"
      "ds_read_b128 v[88:91], v235 offset:33024\n"
      "s_add_u32 s54, s30, s40\n"
      "s_addc_u32 s55, s31, 0\n"
      "v_mfma_f32_16x16x32_bf16 v[136:139], a[32:35], v[92:95], v[136:139]\n"
      "ds_read_b128 v[92:95], v232 offset:41216\n"
      "v_mfma_f32_16x16x32_bf16 v[136:139], a[36:39], v[108:111], v[136:139]\n"
      "ds_read_b128 v[108:111], v233 offset:41216\n"
      "global_store_dwordx4 v247, v[72:75], s[54:55] offset:0 sc1\n"
      "v_mfma_f32_16x16x32_bf16 v[136:139], a[40:43], v[112:115], v[136:139]\n"
      "ds_read_b128 v[112:115], v234 offset:41216\n"
      "v_mfma_f32_16x16x32_bf16 v[136:139], a[44:47], v[116:119], v[136:139]\n"
      "ds_read_b128 v[116:119], v235 offset:41216\n"
      "s_waitcnt lgkmcnt(8)\n"
      "v_mfma_f32_16x16x32_bf16 v[132:135], a[240:243], v[36:39], v[0:3]\n"
      "ds_write_b64 v244, v[208:209] offset:24704\n"
      "v_mfma_f32_16x16x32_bf16 v[132:135], a[244:247], v[40:43], v[132:135]\n"
      "v_mfma_f32_16x16x32_bf16 v[132:135], a[248:251], v[44:47], v[132:135]\n"
      "v_mfma_f32_16x16x32_bf16 v[132:135], a[252:255], v[60:63], v[132:135]\n"
      "v_cvt_pk_bf16_f32 v210, v136, v137\n"
      "s_waitcnt lgkmcnt(5)\n"
      "v_mfma_f32_16x16x32_bf16 v[132:135], a[48:51], v[64:67], v[132:135]\n"
      "v_cvt_pk_bf16_f32 v211, v138, v139\n"
      "v_mfma_f32_16x16x32_bf16 v[132:135], a[52:55], v[68:71], v[132:135]\n"
      "v_mfma_f32_16x16x32_bf16 v[132:135], a[56:59], v[84:87], v[132:135]\n"
      "ds_read_b128 v[96:99], v246 offset:12288\n"
      "v_mfma_f32_16x16x32_bf16 v[132:135], a[60:63], v[88:91], v[132:135]\n"
      "ds_read_b128 v[100:103], v246 offset:13312\n"
      "s_waitcnt lgkmcnt(3)\n"
      "v_mfma_f32_16x16x32_bf16 v[132:135], a[64:67], v[92:95], v[132:135]\n"
      "ds_read_b128 v[104:107], v246 offset:14336\n"
      "v_mfma_f32_16x16x32_bf16 v[132:135], a[68:71], v[108:111], v[132:135]\n"
      "ds_write_b64 v244, v[210:211] offset:24712\n"
      "v_mfma_f32_16x16x32_bf16 v[132:135], a[72:75], v[112:115], v[132:135]\n"
      "v_mfma_f32_16x16x32_bf16 v[132:135], a[76:79], v[116:119], v[132:135]\n"
      "v_mfma_f32_16x16x32_bf16 v[136:139], a[80:83], v[36:39], v[4:7]\n"
      "v_mfma_f32_16x16x32_bf16 v[136:139], a[84:87], v[40:43], v[136:139]\n"
      "LPD16:\n"
      "s_cbranch_execz LRET\n"
      "global_store_dwordx4 v252, v[76:79], s[54:55] offset:0 sc1\n"
      "v_mfma_f32_16x16x32_bf16 v[136:139], a[88:91], v[44:47], v[136:139]\n"
      "v_mfma_f32_16x16x32_bf16 v[136:139], a[92:95], v[60:63], v[136:139]\n"
      "v_cvt_pk_bf16_f32 v208, v132, v133\n"
      "v_mfma_f32_16x16x32_bf16 v[136:139], a[96:99], v[64:67], v[136:139]\n"
      "v_cvt_pk_bf16_f32 v209, v134, v135\n"
      "v_mfma_f32_16x16x32_bf16 v[136:139], a[100:103], v[68:71], v[136:139]\n"
      "v_mfma_f32_16x16x32_bf16 v[136:139], a[104:107], v[84:87], v[136:139]\n"
      "v_mfma_f32_16x16x32_bf16 v[136:139], a[108:111], v[88:91], v[136:139]\n"
      "v_mfma_f32_16x16x32_bf16 v[136:139], a[112:115], v[92:95], v[136:139]\n"
      "v_mfma_f32_16x16x32_bf16 v[136:139], a[116:119], v[108:111], v[136:139]\n"
      "ds_write_b64 v244, v[208:209] offset:0\n"
      "v_mfma_f32_16x16x32_bf16 v[136:139], a[120:123], v[112:115], v[136:139]\n"
      "v_mfma_f32_16x16x32_bf16 v[136:139], a[124:127], v[116:119], v[136:139]\n"
      "v_mfma_f32_16x16x32_bf16 v[132:135], a[128:131], v[36:39], v[8:11]\n"
      "global_store_dwordx4 v253, v[80:83], s[54:55] offset:0 sc1\n"
      "v_mfma_f32_16x16x32_bf16 v[132:135], a[132:135], v[40:43], v[132:135]\n"
      "v_mfma_f32_16x16x32_bf16 v[132:135], a[136:139], v[44:47], v[132:135]\n"
      "v_mfma_f32_16x16x32_bf16 v[132:135], a[140:143], v[60:63], v[132:135]\n"
      "v_cvt_pk_bf16_f32 v210, v136, v137\n"
      "v_mfma_f32_16x16x32_bf16 v[132:135], a[144:147], v[64:67], v[132:135]\n"
      "v_cvt_pk_bf16_f32 v211, v138, v139\n"
      "v_mfma_f32_16x16x32_bf16 v[132:135], a[148:151], v[68:71], v[132:135]\n"
      "v_mfma_f32_16x16x32_bf16 v[132:135], a[152:155], v[84:87], v[132:135]\n"
      "v_mfma_f32_16x16x32_bf16 v[132:135], a[156:159], v[88:91], v[132:135]\n"
      "v_mfma_f32_16x16x32_bf16 v[132:135], a[160:163], v[92:95], v[132:135]\n"
      "v_mfma_f32_16x16x32_bf16 v[132:135], a[164:167], v[108:111], v[132:135]\n"
      "ds_write_b64 v244, v[210:211] offset:8\n"
      "v_mfma_f32_16x16x32_bf16 v[132:135], a[168:171], v[112:115], v[132:135]\n"
      "s_waitcnt lgkmcnt(5)\n"
      "v_mfma_f32_16x16x32_bf16 v[132:135], a[172:175], v[116:119], v[132:135]\n"
      "global_store_dwordx4 v247, v[96:99], s[54:55] offset:768 sc1\n"
      "v_mfma_f32_16x16x32_bf16 v[136:139], a[176:179], v[36:39], v[12:15]\n"
      "v_mfma_f32_16x16x32_bf16 v[136:139], a[180:183], v[40:43], v[136:139]\n"
      "v_mfma_f32_16x16x32_bf16 v[136:139], a[184:187], v[44:47], v[136:139]\n"
      "v_mfma_f32_16x16x32_bf16 v[136:139], a[188:191], v[60:63], v[136:139]\n"
      "v_cvt_pk_bf16_f32 v212, v132, v133\n"
      "v_mfma_f32_16x16x32_bf16 v[136:139], a[192:195], v[64:67], v[136:139]\n"
      "v_cvt_pk_bf16_f32 v213, v134, v135\n"
      "v_mfma_f32_16x16x32_bf16 v[136:139], a[196:199], v[68:71], v[136:139]\n"
      "v_mfma_f32_16x16x32_bf16 v[136:139], a[200:203], v[84:87], v[136:139]\n"
      "v_mfma_f32_16x16x32_bf16 v[136:139], a[204:207], v[88:91], v[136:139]\n"
      "v_mfma_f32_16x16x32_bf16 v[136:139], a[208:211], v[92:95], v[136:139]\n"
      "s_waitcnt lgkmcnt(4)\n"
      "v_mfma_f32_16x16x32_bf16 v[136:139], a[212:215], v[108:111], v[136:139]\n"
      "ds_write_b64 v245, v[212:213] offset:0\n"
      "v_mfma_f32_16x16x32_bf16 v[136:139], a[216:219], v[112:115], v[136:139]\n"
      "global_store_dwordx4 v252, v[100:103], s[54:55] offset:768 sc1\n"
      "v_mfma_f32_16x16x32_bf16 v[136:139], a[220:223], v[116:119], v[136:139]\n"
      "v_mfma_f32_16x16x32_bf16 v[132:135], a[224:227], v[36:39], v[16:19]\n"
      "v_mfma_f32_16x16x32_bf16 v[132:135], a[228:231], v[40:43], v[132:135]\n"
      "v_mfma_f32_16x16x32_bf16 v[132:135], a[232:235], v[44:47], v[132:135]\n"
      "v_mfma_f32_16x16x32_bf16 v[132:135], a[236:239], v[60:63], v[132:135]\n"
      "v_cvt_pk_bf16_f32 v214, v136, v137\n"
      "v_mfma_f32_16x16x32_bf16 v[132:135], a[0:3], v[64:67], v[132:135]\n"
      "v_cvt_pk_bf16_f32 v215, v138, v139\n"
      "v_mfma_f32_16x16x32_bf16 v[132:135], a[4:7], v[68:71], v[132:135]\n"
      "v_mfma_f32_16x16x32_bf16 v[132:135], a[8:11], v[84:87], v[132:135]\n"
      "v_mfma_f32_16x16x32_bf16 v[132:135], a[12:15], v[88:91], v[132:135]\n"
      "s_waitcnt lgkmcnt(4)\n"
      "v_mfma_f32_16x16x32_bf16 v[132:135], a[16:19], v[92:95], v[132:135]\n"
      "global_store_dwordx4 v253, v[104:107], s[54:55] offset:768 sc1\n"
      "v_mfma_f32_16x16x32_bf16 v[132:135], a[20:23], v[108:111], v[132:135]\n"
      "ds_write_b64 v245, v[214:215] offset:8\n"
      "v_mfma_f32_16x16x32_bf16 v[132:135], v[140:143], v[112:115], v[132:135]\n"
      "v_mfma_f32_16x16x32_bf16 v[132:135], v[156:159], v[116:119], v[132:135]\n"
      "s_waitcnt lgkmcnt(4)\n"
      "s_barrier\n"
      "v_mfma_f32_16x16x32_bf16 v[136:139], v[160:163], v[36:39], v[20:23]\n"
      "v_mfma_f32_16x16x32_bf16 v[136:139], v[164:167], v[40:43], v[136:139]\n"
      "v_mfma_f32_16x16x32_bf16 v[136:139], v[180:183], v[44:47], v[136:139]\n"
      "v_mfma_f32_16x16x32_bf16 v[136:139], v[184:187], v[60:63], v[136:139]\n"
      "v_cvt_pk_bf16_f32 v208, v132, v133\n"
      "v_mfma_f32_16x16x32_bf16 v[136:139], v[188:191], v[64:67], v[136:139]\n"
      "v_cvt_pk_bf16_f32 v209, v134, v135\n"
      "v_mfma_f32_16x16x32_bf16 v[136:139], v[204:207], v[68:71], v[136:139]\n"
      "v_mfma_f32_16x16x32_bf16 v[136:139], a[24:27], v[84:87], v[136:139]\n"
      "v_mfma_f32_16x16x32_bf16 v[136:139], a[28:31], v[88:91], v[136:139]\n"
      "v_mfma_f32_16x16x32_bf16 v[136:139], a[32:35], v[92:95], v[136:139]\n"
      "v_mfma_f32_16x16x32_bf16 v[136:139], a[36:39], v[108:111], v[136:139]\n"
      "ds_write_b64 v244, v[208:209] offset:128\n"
      "v_mfma_f32_16x16x32_bf16 v[136:139], a[40:43], v[112:115], v[136:139]\n"
      "v_mfma_f32_16x16x32_bf16 v[136:139], a[44:47], v[116:119], v[136:139]\n"
      "ds_read_b128 v[120:123], v246 offset:24576\n"
      "ds_read_b128 v[124:127], v246 offset:25600\n"
      "ds_read_b128 v[128:131], v246 offset:26624\n"
      "s_add_u32 s54, s44, s40\n"
      "s_addc_u32 s55, s45, 0\n"
      "s_nop 3\n"
      "v_cvt_pk_bf16_f32 v210, v136, v137\n"
      "v_cvt_pk_bf16_f32 v211, v138, v139\n"
      "ds_write_b128 v244, v[208:211] offset:128\n"
      "s_waitcnt lgkmcnt(3)\n"
      "global_store_dwordx4 v247, v[120:123], s[54:55] offset:0 sc1\n"
      "s_waitcnt lgkmcnt(2)\n"
      "global_store_dwordx4 v252, v[124:127], s[54:55] offset:0 sc1\n"
      "s_waitcnt lgkmcnt(1)\n"
      "global_store_dwordx4 v253, v[128:131], s[54:55] offset:0 sc1\n"
      "s_waitcnt lgkmcnt(0)\n"
      "s_barrier\n"
      "ds_read_b128 v[144:147], v246 offset:0\n"
      "ds_read_b128 v[148:151], v246 offset:1024\n"
      "ds_read_b128 v[152:155], v246 offset:2048\n"
      "s_waitcnt lgkmcnt(2)\n"
      "global_store_dwordx4 v247, v[144:147], s[54:55] offset:768 sc1\n"
      "s_waitcnt lgkmcnt(1)\n"
      "global_store_dwordx4 v252, v[148:151], s[54:55] offset:768 sc1\n"
      "s_waitcnt lgkmcnt(0)\n"
      "global_store_dwordx4 v253, v[152:155], s[54:55] offset:768 sc1\n"
      "s_cmp_lg_u32 s87, 0\n"
      "s_cbranch_scc1 LE1\n"
      "v_min_u32 v0, 0x370, v239\n"
      "v_add_u32 v0, 0x2480, v0\n"
      "s_mov_b32 s82, 0x40000\n"
      "LE0:\n"
      "global_load_dwordx4 v[4:7], v0, s[28:29] sc1\n"
      "s_sub_u32 s82, s82, 1\n"
      "s_cbranch_scc1 LE1\n"
      "s_waitcnt vmcnt(0)\n"
      "v_cmp_ne_u64 vcc, s[80:81], v[4:5]\n"
      "v_cmp_ne_u64 s[56:57], s[80:81], v[6:7]\n"
      "s_nop 3\n"
      "s_or_b64 vcc, vcc, s[56:57]\n"
      "s_cbranch_scc1 LE0\n"
      "v_mov_b32 v0, 0\n"
      "v_mov_b32 v2, s80\n"
      "v_mov_b32 v3, s81\n"
      "s_mov_b64 exec, 1\n"
      "global_store_dwordx2 v0, v[2:3], s[28:29] sc1\n"
      "s_mov_b64 exec, -1\n"
      "LE1:\n"
      "s_waitcnt vmcnt(0)\n"
      "s_branch LCEND\n"
      "LPS:\n"
      "s_mov_b64 exec, 1\n"
      "global_store_dword v229, v230, s[28:29] sc1\n"
      "s_mov_b64 exec, -1\n"
      "s_branch LPR\n"
      "LT0:\n"
      "s_nop 15\n"
      "s_nop 3\n"
      "s_branch LS0\n"
      "LT1:\n"
      "s_nop 15\n"
      "s_nop 3\n"
      "s_branch LS1\n"
      "LT2:\n"
      "s_nop 15\n"
      "s_nop 3\n"
      "s_branch LS2\n"
      "LT3:\n"
      "s_nop 15\n"
      "s_nop 3\n"
      "s_branch LS3\n"
      "LT4:\n"
      "s_nop 15\n"
      "s_nop 3\n"
      "s_branch LS4\n"
      "LT5:\n"
      "s_nop 15\n"
      "s_nop 3\n"
      "s_branch LS5\n"
      "LT6:\n"
      "s_nop 15\n"
      "s_nop 3\n"
      "s_branch LS6\n"
      "LT7:\n"
      "s_nop 15\n"
      "s_nop 3\n"
      "s_branch LS7\n"
      "LT8:\n"
      "s_nop 15\n"
      "s_nop 3\n"
      "s_branch LS8\n"
      "LT9:\n"
      "s_nop 15\n"
      "s_nop 3\n"
      "s_branch LS9\n"
      "LT10:\n"
      "s_nop 15\n"
      "s_nop 3\n"
      "s_branch LS10\n"
      "LT11:\n"
      "s_nop 15\n"
      "s_nop 3\n"
      "s_branch LS11\n"
      "LT12:\n"
      "s_nop 15\n"
      "s_nop 3\n"
      "s_branch LS12\n"
      "LT13:\n"
      "s_nop 15\n"
      "s_nop 3\n"
      "s_branch LS13\n"
      "LT14:\n"
      "s_nop 15\n"
      "s_nop 3\n"
      "s_branch LS14\n"
      "LT15:\n"
      "s_nop 15\n"
      "s_nop 3\n"
      "s_branch LS15\n"
      "LT16:\n"
      "s_nop 15\n"
      "s_nop 3\n"
      "s_branch LS16\n"
      "LT17:\n"
      "s_nop 15\n"
      "s_nop 3\n"
      "s_branch LS17\n"
      "LT18:\n"
      "s_nop 15\n"
      "s_nop 3\n"
      "s_branch LS18\n"
      "LT19:\n"
      "s_nop 15\n"
      "s_nop 3\n"
      "s_branch LS19\n"
      "LT20:\n"
      "s_nop 15\n"
      "s_nop 3\n"
      "s_branch LS20\n"
      "LT21:\n"
      "s_nop 15\n"
      "s_nop 3\n"
      "s_branch LS21\n"
      "LT22:\n"
      "s_nop 15\n"
      "s_nop 3\n"
      "s_branch LS22\n"
      "LT23:\n"
      "s_nop 15\n"
      "s_nop 3\n"
      "s_branch LS23\n"
      "LT24:\n"
      "s_nop 15\n"
      "s_nop 3\n"
      "s_branch LS24\n"
      "LT25:\n"
      "s_nop 15\n"
      "s_nop 3\n"
      "s_branch LS25\n"
      "LT26:\n"
      "s_nop 15\n"
      "s_nop 3\n"
      "s_branch LS26\n"
      "LT27:\n"
      "s_nop 15\n"
      "s_nop 3\n"
      "s_branch LS27\n"
      "LT28:\n"
      "s_nop 15\n"
      "s_nop 3\n"
      "s_branch LS28\n"
      "LT29:\n"
      "s_nop 15\n"
      "s_nop 3\n"
      "s_branch LS29\n"
      "LT30:\n"
      "s_nop 15\n"
      "s_nop 3\n"
      "s_branch LS30\n"
      "LT31:\n"
      "s_nop 15\n"
      "s_nop 3\n"
      "s_branch LS31\n"
      "LT32:\n"
      "s_nop 15\n"
      "s_nop 3\n"
      "s_branch LS32\n"
      "LT33:\n"
      "s_nop 15\n"
      "s_nop 3\n"
      "s_branch LS33\n"
      "LT34:\n"
      "s_nop 15\n"
      "s_nop 3\n"
      "s_branch LS34\n"
      "LT35:\n"
      "s_nop 15\n"
      "s_nop 3\n"
      "s_branch LS35\n"
      "LT36:\n"
      "s_nop 15\n"
      "s_nop 3\n"
      "s_branch LS36\n"
      "LT37:\n"
      "s_nop 15\n"
      "s_nop 3\n"
      "s_branch LS37\n"
      "LT38:\n"
      "s_nop 15\n"
      "s_nop 3\n"
      "s_branch LS38\n"
      "LT39:\n"
      "s_nop 15\n"
      "s_nop 3\n"
      "s_branch LS39\n"
      "LT40:\n"
      "s_nop 15\n"
      "s_nop 3\n"
      "s_branch LS40\n"
      "LT41:\n"
      "s_nop 15\n"
      "s_nop 3\n"
      "s_branch LS41\n"
      "LCEND:\n"

      :
      : "{v232}"(cB0), "{v233}"(cB0 ^ 64), "{v234}"(cB0 ^ 128), "{v235}"(cB0 ^ 192), "{v238}"(pvo),
        "{v239}"(lo16), "{v240}"(wro[0]), "{v241}"(wro[1]), "{v242}"(wro[2]), "{v243}"(tra), "{v248}"(fla), "{v249}"(c256),
        "{v250}"(pk), "{v105}"(two), "{v106}"(tcode), "{v[252:253]}"(ep),
        "{s[20:21]}"(cf), "{s[22:23]}"(pc), "{s[24:25]}"(wb), "{s[30:31]}"(out), "{s[28:29]}"(ws), "{s18}"(mP), "{s17}"(mX),
        "{s32}"(xo[0]), "{s33}"(xo[1]), "{s34}"(xo[2]), "{s35}"(xo[3]), "{s36}"(xo[4]), "{s37}"(xo[5]), "{s38}"(xo[6]),
        "{s39}"(xo[7]), "{s40}"(xo[8]), "{s[42:43]}"(cf + 294912), "{s[44:45]}"(out + 294912), "{s[46:47]}"(tab), "{s50}"(var), "{s51}"(b),
        "{s[64:65]}"(w2w), "{s74}"(mW), "{s76}"(wid), "{s77}"(jfl), "{v251}"(sgn),
        "{s89}"(dj[0]), "{s90}"(dj[1]), "{s91}"(dj[2]), "{s92}"(dj[3]), "{s93}"(dj[4]), "{s94}"(dj[5]), "{s95}"(dj[6]),
        "{s96}"(dj[7]), "{s97}"(dj[8]), "{s98}"(d0), "{s99}"(wid ? -1 : 1), "{s100}"(wid < 3 ? 1 : -1), "{s101}"(wid == 2 ? -1 : 1),
        "{v102}"(jt), "{v103}"(jrw), "{v104}"(jrr), "{v[110:111]}"(cd), "{v[112:113]}"(cr), "{v107}"(sg1), "{v108}"(scr), "{s[72:73]}"(jo),
        "{v[4:7]}"(ja0), "{v[8:11]}"(ja1), "{v[12:15]}"(ja2), "{v[16:19]}"(ja3), "{v[20:23]}"(ja4), "{v[24:27]}"(ja5),
        "{s83}"(t_start + (0) + var * ((0) - (0))), "{s86}"(158 + var * ((158) - (158)))
      : "memory", "scc", "vcc", "v0", "v1", "v2", "v3", "v28", "v29", "v30", "v31", "v32", "v33", "v34", "v35", "v36", "v37", "v38", "v39", "v40", "v41", "v42", "v43", "v44", "v45", "v46", "v47", "v48", "v49", "v50", "v51", "v52", "v53", "v54", "v55", "v56", "v57", "v58", "v59", "v60", "v61", "v62", "v63", "v64", "v65", "v66", "v67", "v68", "v69", "v70", "v71", "v72", "v73", "v74", "v75", "v76", "v77", "v78", "v79", "v80", "v81", "v82", "v83", "v84", "v85", "v86", "v87", "v88", "v89", "v90", "v91", "v92", "v93", "v94", "v95", "v96", "v97", "v98", "v99", "v100", "v101", "v109", "v114", "v115", "v116", "v117", "v118", "v119", "v120", "v121", "v122", "v123", "v124", "v125", "v126", "v127", "v128", "v129", "v130", "v131", "v132", "v133", "v134", "v135", "v136", "v137", "v138", "v139", "v140", "v141", "v142", "v143", "v144", "v145", "v146", "v147", "v148", "v149", "v150", "v151", "v152", "v153", "v154", "v155", "v156", "v157", "v158", "v159", "v160", "v161", "v162", "v163", "v164", "v165", "v166", "v167", "v168", "v169", "v170", "v171", "v172", "v173", "v174", "v175", "v176", "v177", "v178", "v179", "v180", "v181", "v182", "v183", "v184", "v185", "v186", "v187", "v188", "v189", "v190", "v191", "v192", "v193", "v194", "v195", "v196", "v197", "v198", "v199", "v200", "v201", "v202", "v203", "v204", "v205", "v206", "v207", "v208", "v209", "v210", "v211", "v212", "v213", "v214", "v215", "v216", "v217", "v218", "v219", "v220", "v221", "v222", "v223", "v224", "v225", "v226", "v227", "v228", "v229", "v230", "v231", "v236", "v237", "v244", "v245", "v246", "v247", "v254", "v255", "a0", "a1", "a2", "a3", "a4", "a5", "a6", "a7", "a8", "a9", "a10", "a11", "a12", "a13", "a14", "a15", "a16", "a17", "a18", "a19", "a20", "a21", "a22", "a23", "a24", "a25", "a26", "a27", "a28", "a29", "a30", "a31", "a32", "a33", "a34", "a35", "a36", "a37", "a38", "a39", "a40", "a41", "a42", "a43", "a44", "a45", "a46", "a47", "a48", "a49", "a50", "a51", "a52", "a53", "a54", "a55", "a56", "a57", "a58", "a59", "a60", "a61", "a62", "a63", "a64", "a65", "a66", "a67", "a68", "a69", "a70", "a71", "a72", "a73", "a74", "a75", "a76", "a77", "a78", "a79", "a80", "a81", "a82", "a83", "a84", "a85", "a86", "a87", "a88", "a89", "a90", "a91", "a92", "a93", "a94", "a95", "a96", "a97", "a98", "a99", "a100", "a101", "a102", "a103", "a104", "a105", "a106", "a107", "a108", "a109", "a110", "a111", "a112", "a113", "a114", "a115", "a116", "a117", "a118", "a119", "a120", "a121", "a122", "a123", "a124", "a125", "a126", "a127", "a128", "a129", "a130", "a131", "a132", "a133", "a134", "a135", "a136", "a137", "a138", "a139", "a140", "a141", "a142", "a143", "a144", "a145", "a146", "a147", "a148", "a149", "a150", "a151", "a152", "a153", "a154", "a155", "a156", "a157", "a158", "a159", "a160", "a161", "a162", "a163", "a164", "a165", "a166", "a167", "a168", "a169", "a170", "a171", "a172", "a173", "a174", "a175", "a176", "a177", "a178", "a179", "a180", "a181", "a182", "a183", "a184", "a185", "a186", "a187", "a188", "a189", "a190", "a191", "a192", "a193", "a194", "a195", "a196", "a197", "a198", "a199", "a200", "a201", "a202", "a203", "a204", "a205", "a206", "a207", "a208", "a209", "a210", "a211", "a212", "a213", "a214", "a215", "a216", "a217", "a218", "a219", "a220", "a221", "a222", "a223", "a224", "a225", "a226", "a227", "a228", "a229", "a230", "a231", "a232", "a233", "a234", "a235", "a236", "a237", "a238", "a239", "a240", "a241", "a242", "a243", "a244", "a245", "a246", "a247", "a248", "a249", "a250", "a251", "a252", "a253", "a254", "a255", "s48", "s49", "s66", "s67", "s68", "s69", "s70", "s71", "s52", "s53", "s54", "s55", "s56", "s57", "s58", "s59", "s60", "s61", "s62", "s63", "s78", "s79", "s80", "s81", "s82", "s84", "s85", "s87", "s19");
}

extern "C" void kernel_launch(void* const* d_in, const int* in_sizes, int n_in,
                              void* d_out, int out_size, void* d_ws, size_t ws_size,
                              hipStream_t stream) {
  (void)hipFuncSetAttribute((const void*)fpn_kernel, hipFuncAttributeMaxDynamicSharedMemorySize, 159808);
  fpn_kernel<<<256, 256, 159808, stream>>>((const char*)d_in[0], (const char*)d_in[1], (const char*)d_in[2],
                                                (const char*)d_in[3], (char*)d_ws, (char*)d_out);
}
```

```cpp
#include <hip/hip_runtime.h>
#include <hip/hip_bf16.h>

typedef __attribute__((ext_vector_type(4))) unsigned u32x4;
typedef __attribute__((ext_vector_type(2))) unsigned u32x2;
#define NSTAMPS 0

__device__ __forceinline__ unsigned col0(unsigned i) { return i + (i >= 5 ? 6 : 0) + (i >= 10 ? 5 : 0); }

static __device__ const unsigned __attribute__((aligned(128))) lane_tab[2048] = {
  98304, 98368, 98304, 0, 99088, 99152, 98320, 16, 99872, 99936, 98336, 32, 100656, 100720, 98352, 48,
  101440, 101376, 98368, 64, 102224, 102160, 98384, 80, 103008, 102944, 98400, 96, 103792, 103728, 98416, 112,
  104448, 104512, 98432, 128, 105232, 105296, 98448, 144, 106016, 106080, 98464, 160, 106800, 106864, 98480, 176,
  107584, 107520, 98496, 192, 108368, 108304, 98512, 208, 109152, 109088, 98528, 224, 109936, 109872, 98544, 240,
  98320, 98384, 98560, 256, 99072, 99136, 98576, 272, 99888, 99952, 98592, 288, 100640, 100704, 98608, 304,
  101456, 101392, 98624, 320, 102208, 102144, 98640, 336, 103024, 102960, 98656, 352, 103776, 103712, 98672, 368,
  104464, 104528, 98688, 384, 105216, 105280, 98704, 400, 106032, 106096, 98720, 416, 106784, 106848, 98736, 432,
  107600, 107536, 98752, 448, 108352, 108288, 98768, 464, 109168, 109104, 98784, 480, 109920, 109856, 98800, 496,
  98336, 98400, 98816, 512, 99120, 99184, 98832, 528, 99840, 99904, 98848, 544, 100624, 100688, 98864, 560,
  101472, 101408, 98880, 576, 102256, 102192, 98896, 592, 102976, 102912, 98912, 608, 103760, 103696, 98928, 624,
  104480, 104544, 98944, 640, 105264, 105328, 98960, 656, 105984, 106048, 98976, 672, 106768, 106832, 98992, 688,
  107616, 107552, 99008, 704, 108400, 108336, 99024, 720, 109120, 109056, 99040, 736, 109904, 109840, 99056, 752,
  98352, 98416, 99072, 1552, 99104, 99168, 99088, 1536, 99856, 99920, 99104, 1584, 100608, 100672, 99120, 1568,
  101488, 101424, 99136, 1616, 102240, 102176, 99152, 1600, 102992, 102928, 99168, 1648, 103744, 103680, 99184, 1632,
  104496, 104560, 99200, 1680, 105248, 105312, 99216, 1664, 106000, 106064, 99232, 1712, 106752, 106816, 99248, 1696,
  107632, 107568, 99264, 1744, 108384, 108320, 99280, 1728, 109136, 109072, 99296, 1776, 109888, 109824, 99312, 1760,
  98496, 98560, 101376, 6208, 99280, 99344, 101392, 6224, 100064, 100128, 101408, 6240, 100848, 100912, 101424, 6256,
  101504, 101696, 101440, 6144, 102288, 102480, 101456, 6160, 103072, 103264, 101472, 6176, 103856, 104048, 101488, 6192,
  104640, 104704, 101504, 6336, 105424, 105488, 101520, 6352, 106208, 106272, 101536, 6368, 106992, 107056, 101552, 6384,
  107648, 107840, 101568, 6272, 108432, 108624, 101584, 6288, 109216, 109408, 101600, 6304, 110000, 110192, 101616, 6320,
  98512, 98576, 101632, 6464, 99264, 99328, 101648, 6480, 100080, 100144, 101664, 6496, 100832, 100896, 101680, 6512,
  101520, 101712, 101696, 6400, 102272, 102464, 101712, 6416, 103088, 103280, 101728, 6432, 103840, 104032, 101744, 6448,
  104656, 104720, 101760, 6592, 105408, 105472, 101776, 6608, 106224, 106288, 101792, 6624, 106976, 107040, 101808, 6640,
  107664, 107856, 101824, 6528, 108416, 108608, 101840, 6544, 109232, 109424, 101856, 6560, 109984, 110176, 101872, 6576,
  98528, 98592, 101888, 6720, 99312, 99376, 101904, 6736, 100032, 100096, 101920, 6752, 100816, 100880, 101936, 6768,
  101536, 101728, 101952, 6656, 102320, 102512, 101968, 6672, 103040, 103232, 101984, 6688, 103824, 104016, 102000, 6704,
  104672, 104736, 102016, 6848, 105456, 105520, 102032, 6864, 106176, 106240, 102048, 6880, 106960, 107024, 102064, 6896,
  107680, 107872, 102080, 6784, 108464, 108656, 102096, 6800, 109184, 109376, 102112, 6816, 109968, 110160, 102128, 6832,
  98544, 98608, 102144, 16976, 99296, 99360, 102160, 16960, 100048, 100112, 102176, 17008, 100800, 100864, 102192, 16992,
  101552, 101744, 102208, 16912, 102304, 102496, 102224, 16896, 103056, 103248, 102240, 16944, 103808, 104000, 102256, 16928,
  104688, 104752, 102272, 17104, 105440, 105504, 102288, 17088, 106192, 106256, 102304, 17136, 106944, 107008, 102320, 17120,
  107696, 107888, 102336, 17040, 108448, 108640, 102352, 17024, 109200, 109392, 102368, 17072, 109952, 110144, 102384, 17056,
  1808, 3616, 0, 21632, 1792, 3632, 16, 21648, 1840, 3584, 32, 21664, 1824, 3600, 48, 21680,
  1872, 3680, 64, 21696, 1856, 3696, 80, 21712, 1904, 3648, 96, 21728, 1888, 3664, 112, 21744,
  1936, 3744, 128, 21504, 1920, 3760, 144, 21520, 1968, 3712, 160, 21536, 1952, 3728, 176, 21552,
  2000, 3808, 192, 21568, 1984, 3824, 208, 21584, 2032, 3776, 224, 21600, 2016, 3792, 240, 21616,
  2064, 4656, 768, 22400, 2048, 4640, 784, 22416, 2096, 4624, 800, 22432, 2080, 4608, 816, 22448,
  2128, 4720, 832, 22464, 2112, 4704, 848, 22480, 2160, 4688, 864, 22496, 2144, 4672, 880, 22512,
  2192, 4784, 896, 22272, 2176, 4768, 912, 22288, 2224, 4752, 928, 22304, 2208, 4736, 944, 22320,
  2256, 4848, 960, 22336, 2240, 4832, 976, 22352, 2288, 4816, 992, 22368, 2272, 4800, 1008, 22384,
  3104, 4912, 1552, 23184, 3120, 4896, 1536, 23168, 3072, 4880, 1584, 23216, 3088, 4864, 1568, 23200,
  3168, 4976, 1616, 23248, 3184, 4960, 1600, 23232, 3136, 4944, 1648, 23280, 3152, 4928, 1632, 23264,
  3232, 5040, 1680, 23056, 3248, 5024, 1664, 23040, 3200, 5008, 1712, 23088, 3216, 4992, 1696, 23072,
  3296, 5104, 1744, 23120, 3312, 5088, 1728, 23104, 3264, 5072, 1776, 23152, 3280, 5056, 1760, 23136,
  3360, 5168, 2320, 23952, 3376, 5152, 2304, 23936, 3328, 5136, 2352, 23984, 3344, 5120, 2336, 23968,
  3424, 5232, 2384, 24016, 3440, 5216, 2368, 24000, 3392, 5200, 2416, 24048, 3408, 5184, 2400, 24032,
  3488, 5296, 2448, 23824, 3504, 5280, 2432, 23808, 3456, 5264, 2480, 23856, 3472, 5248, 2464, 23840,
  3552, 5360, 2512, 23888, 3568, 5344, 2496, 23872, 3520, 5328, 2544, 23920, 3536, 5312, 2528, 23904,
  17232, 19040, 3104, 32416, 17216, 19056, 3120, 32432, 17264, 19008, 3072, 32384, 17248, 19024, 3088, 32400,
  17168, 18976, 3168, 32480, 17152, 18992, 3184, 32496, 17200, 18944, 3136, 32448, 17184, 18960, 3152, 32464,
  17360, 19168, 3232, 32288, 17344, 19184, 3248, 32304, 17392, 19136, 3200, 32256, 17376, 19152, 3216, 32272,
  17296, 19104, 3296, 32352, 17280, 19120, 3312, 32368, 17328, 19072, 3264, 32320, 17312, 19088, 3280, 32336,
  17488, 20080, 3872, 33184, 17472, 20064, 3888, 33200, 17520, 20048, 3840, 33152, 17504, 20032, 3856, 33168,
  17424, 20016, 3936, 33248, 17408, 20000, 3952, 33264, 17456, 19984, 3904, 33216, 17440, 19968, 3920, 33232,
  17616, 20208, 4000, 33056, 17600, 20192, 4016, 33072, 17648, 20176, 3968, 33024, 17632, 20160, 3984, 33040,
  17552, 20144, 4064, 33120, 17536, 20128, 4080, 33136, 17584, 20112, 4032, 33088, 17568, 20096, 4048, 33104,
  18528, 20336, 4656, 33968, 18544, 20320, 4640, 33952, 18496, 20304, 4624, 33936, 18512, 20288, 4608, 33920,
  18464, 20272, 4720, 34032, 18480, 20256, 4704, 34016, 18432, 20240, 4688, 34000, 18448, 20224, 4672, 33984,
  18656, 20464, 4784, 33840, 18672, 20448, 4768, 33824, 18624, 20432, 4752, 33808, 18640, 20416, 4736, 33792,
  18592, 20400, 4848, 33904, 18608, 20384, 4832, 33888, 18560, 20368, 4816, 33872, 18576, 20352, 4800, 33856,
  18784, 20592, 5424, 34736, 18800, 20576, 5408, 34720, 18752, 20560, 5392, 34704, 18768, 20544, 5376, 34688,
  18720, 20528, 5488, 34800, 18736, 20512, 5472, 34784, 18688, 20496, 5456, 34768, 18704, 20480, 5440, 34752,
  18912, 20720, 5552, 34608, 18928, 20704, 5536, 34592, 18880, 20688, 5520, 34576, 18896, 20672, 5504, 34560,
  18848, 20656, 5616, 34672, 18864, 20640, 5600, 34656, 18816, 20624, 5584, 34640, 18832, 20608, 5568, 34624,
  98688, 98752, 104448, 21504, 99472, 99536, 104464, 21520, 100256, 100320, 104480, 21536, 101040, 101104, 104496, 21552,
  101824, 101760, 104512, 21568, 102608, 102544, 104528, 21584, 103392, 103328, 104544, 21600, 104176, 104112, 104560, 21616,
  104832, 104896, 104576, 21632, 105616, 105680, 104592, 21648, 106400, 106464, 104608, 21664, 107184, 107248, 104624, 21680,
  107968, 107904, 104640, 21696, 108752, 108688, 104656, 21712, 109536, 109472, 104672, 21728, 110320, 110256, 104688, 21744,
  98704, 98768, 104704, 21760, 99456, 99520, 104720, 21776, 100272, 100336, 104736, 21792, 101024, 101088, 104752, 21808,
  101840, 101776, 104768, 21824, 102592, 102528, 104784, 21840, 103408, 103344, 104800, 21856, 104160, 104096, 104816, 21872,
  104848, 104912, 104832, 21888, 105600, 105664, 104848, 21904, 106416, 106480, 104864, 21920, 107168, 107232, 104880, 21936,
  107984, 107920, 104896, 21952, 108736, 108672, 104912, 21968, 109552, 109488, 104928, 21984, 110304, 110240, 104944, 22000,
  98720, 98784, 104960, 22016, 99504, 99568, 104976, 22032, 100224, 100288, 104992, 22048, 101008, 101072, 105008, 22064,
  101856, 101792, 105024, 22080, 102640, 102576, 105040, 22096, 103360, 103296, 105056, 22112, 104144, 104080, 105072, 22128,
  104864, 104928, 105088, 22144, 105648, 105712, 105104, 22160, 106368, 106432, 105120, 22176, 107152, 107216, 105136, 22192,
  108000, 107936, 105152, 22208, 108784, 108720, 105168, 22224, 109504, 109440, 105184, 22240, 110288, 110224, 105200, 22256,
  98736, 98800, 105216, 23056, 99488, 99552, 105232, 23040, 100240, 100304, 105248, 23088, 100992, 101056, 105264, 23072,
  101872, 101808, 105280, 23120, 102624, 102560, 105296, 23104, 103376, 103312, 105312, 23152, 104128, 104064, 105328, 23136,
  104880, 104944, 105344, 23184, 105632, 105696, 105360, 23168, 106384, 106448, 105376, 23216, 107136, 107200, 105392, 23200,
  108016, 107952, 105408, 23248, 108768, 108704, 105424, 23232, 109520, 109456, 105440, 23280, 110272, 110208, 105456, 23264,
  98880, 98944, 107520, 35392, 99664, 99728, 107536, 35408, 100448, 100512, 107552, 35424, 101232, 101296, 107568, 35440,
  101888, 102080, 107584, 35328, 102672, 102864, 107600, 35344, 103456, 103648, 107616, 35360, 104240, 104432, 107632, 35376,
  105024, 105088, 107648, 35520, 105808, 105872, 107664, 35536, 106592, 106656, 107680, 35552, 107376, 107440, 107696, 35568,
  108032, 108224, 107712, 35456, 108816, 109008, 107728, 35472, 109600, 109792, 107744, 35488, 110384, 110576, 107760, 35504,
  98896, 98960, 107776, 35648, 99648, 99712, 107792, 35664, 100464, 100528, 107808, 35680, 101216, 101280, 107824, 35696,
  101904, 102096, 107840, 35584, 102656, 102848, 107856, 35600, 103472, 103664, 107872, 35616, 104224, 104416, 107888, 35632,
  105040, 105104, 107904, 35776, 105792, 105856, 107920, 35792, 106608, 106672, 107936, 35808, 107360, 107424, 107952, 35824,
  108048, 108240, 107968, 35712, 108800, 108992, 107984, 35728, 109616, 109808, 108000, 35744, 110368, 110560, 108016, 35760,
  98912, 98976, 108032, 35904, 99696, 99760, 108048, 35920, 100416, 100480, 108064, 35936, 101200, 101264, 108080, 35952,
  101920, 102112, 108096, 35840, 102704, 102896, 108112, 35856, 103424, 103616, 108128, 35872, 104208, 104400, 108144, 35888,
  105056, 105120, 108160, 36032, 105840, 105904, 108176, 36048, 106560, 106624, 108192, 36064, 107344, 107408, 108208, 36080,
  108064, 108256, 108224, 35968, 108848, 109040, 108240, 35984, 109568, 109760, 108256, 36000, 110352, 110544, 108272, 36016,
  98928, 98992, 108288, 36944, 99680, 99744, 108304, 36928, 100432, 100496, 108320, 36976, 101184, 101248, 108336, 36960,
  101936, 102128, 108352, 36880, 102688, 102880, 108368, 36864, 103440, 103632, 108384, 36912, 104192, 104384, 108400, 36896,
  105072, 105136, 108416, 37072, 105824, 105888, 108432, 37056, 106576, 106640, 108448, 37104, 107328, 107392, 108464, 37088,
  108080, 108272, 108480, 37008, 108832, 109024, 108496, 36992, 109584, 109776, 108512, 37040, 110336, 110528, 108528, 37024,
  23312, 32800, 6208, 35520, 23296, 32816, 6224, 35536, 23344, 32768, 6240, 35552, 23328, 32784, 6256, 35568,
  23376, 32864, 6144, 35456, 23360, 32880, 6160, 35472, 23408, 32832, 6176, 35488, 23392, 32848, 6192, 35504,
  23440, 32928, 6336, 35392, 23424, 32944, 6352, 35408, 23472, 32896, 6368, 35424, 23456, 32912, 6384, 35440,
  23504, 32992, 6272, 35328, 23488, 33008, 6288, 35344, 23536, 32960, 6304, 35360, 23520, 32976, 6320, 35376,
  23568, 33840, 6976, 36288, 23552, 33824, 6992, 36304, 23600, 33808, 7008, 36320, 23584, 33792, 7024, 36336,
  23632, 33904, 6912, 36224, 23616, 33888, 6928, 36240, 23664, 33872, 6944, 36256, 23648, 33856, 6960, 36272,
  23696, 33968, 7104, 36160, 23680, 33952, 7120, 36176, 23728, 33936, 7136, 36192, 23712, 33920, 7152, 36208,
  23760, 34032, 7040, 36096, 23744, 34016, 7056, 36112, 23792, 34000, 7072, 36128, 23776, 33984, 7088, 36144,
  32288, 34096, 16976, 37072, 32304, 34080, 16960, 37056, 32256, 34064, 17008, 37104, 32272, 34048, 16992, 37088,
  32352, 34160, 16912, 37008, 32368, 34144, 16896, 36992, 32320, 34128, 16944, 37040, 32336, 34112, 16928, 37024,
  32416, 34224, 17104, 36944, 32432, 34208, 17088, 36928, 32384, 34192, 17136, 36976, 32400, 34176, 17120, 36960,
  32480, 34288, 17040, 36880, 32496, 34272, 17024, 36864, 32448, 34256, 17072, 36912, 32464, 34240, 17056, 36896,
  32544, 34352, 17744, 37840, 32560, 34336, 17728, 37824, 32512, 34320, 17776, 37872, 32528, 34304, 17760, 37856,
  32608, 34416, 17680, 37776, 32624, 34400, 17664, 37760, 32576, 34384, 17712, 37808, 32592, 34368, 17696, 37792,
  32672, 34480, 17872, 37712, 32688, 34464, 17856, 37696, 32640, 34448, 17904, 37744, 32656, 34432, 17888, 37728,
  32736, 34544, 17808, 37648, 32752, 34528, 17792, 37632, 32704, 34512, 17840, 37680, 32720, 34496, 17824, 37664,
  37200, 39008, 18528, 38624, 37184, 39024, 18544, 38640, 37232, 38976, 18496, 38592, 37216, 38992, 18512, 38608,
  37136, 38944, 18464, 38560, 37120, 38960, 18480, 38576, 37168, 38912, 18432, 38528, 37152, 38928, 18448, 38544,
  37328, 39136, 18656, 38496, 37312, 39152, 18672, 38512, 37360, 39104, 18624, 38464, 37344, 39120, 18640, 38480,
  37264, 39072, 18592, 38432, 37248, 39088, 18608, 38448, 37296, 39040, 18560, 38400, 37280, 39056, 18576, 38416,
  37456, 40048, 19296, 39392, 37440, 40032, 19312, 39408, 37488, 40016, 19264, 39360, 37472, 40000, 19280, 39376,
  37392, 39984, 19232, 39328, 37376, 39968, 19248, 39344, 37424, 39952, 19200, 39296, 37408, 39936, 19216, 39312,
  37584, 40176, 19424, 39264, 37568, 40160, 19440, 39280, 37616, 40144, 19392, 39232, 37600, 40128, 19408, 39248,
  37520, 40112, 19360, 39200, 37504, 40096, 19376, 39216, 37552, 40080, 19328, 39168, 37536, 40064, 19344, 39184,
  38496, 40304, 20080, 40176, 38512, 40288, 20064, 40160, 38464, 40272, 20048, 40144, 38480, 40256, 20032, 40128,
  38432, 40240, 20016, 40112, 38448, 40224, 20000, 40096, 38400, 40208, 19984, 40080, 38416, 40192, 19968, 40064,
  38624, 40432, 20208, 40048, 38640, 40416, 20192, 40032, 38592, 40400, 20176, 40016, 38608, 40384, 20160, 40000,
  38560, 40368, 20144, 39984, 38576, 40352, 20128, 39968, 38528, 40336, 20112, 39952, 38544, 40320, 20096, 39936,
  38752, 40560, 20848, 40944, 38768, 40544, 20832, 40928, 38720, 40528, 20816, 40912, 38736, 40512, 20800, 40896,
  38688, 40496, 20784, 40880, 38704, 40480, 20768, 40864, 38656, 40464, 20752, 40848, 38672, 40448, 20736, 40832,
  38880, 40688, 20976, 40816, 38896, 40672, 20960, 40800, 38848, 40656, 20944, 40784, 38864, 40640, 20928, 40768,
  38816, 40624, 20912, 40752, 38832, 40608, 20896, 40736, 38784, 40592, 20880, 40720, 38800, 40576, 20864, 40704};

__global__ __attribute__((aligned(4096))) __launch_bounds__(256, 1) void fpn_kernel(const char* __restrict__ cf, const char* __restrict__ pc,
                                                     const char* __restrict__ w1, const char* __restrict__ w2,
                                                     char* __restrict__ ws, char* __restrict__ out) {
  unsigned lane = threadIdx.x & 63;
  unsigned wid = __builtin_amdgcn_readfirstlane(threadIdx.x >> 6);
  unsigned b = blockIdx.x;
  unsigned long long t64;
  asm volatile("s_memrealtime %0" : "={s[88:89]}"(t64), "+s"(b), "+s"(wid));
  const unsigned t_start = (unsigned)t64;
  const unsigned pr = (b >> 3) * 4 + (b & 3);
  unsigned xo[9], pg[9];
#pragma unroll
  for (int j = 0; j < 9; ++j) {
    xo[j] = ((3 * pr) >> 1) * 589824u + (pr & 1) * (147456u + (j >= 3 && j < 6 ? 294912u : 0)) + j * 49152u + (j >= 6 ? 294912u : 0);
    pg[j] = xo[j] >> 21;
  }
  int dj[9];
#pragma unroll
  for (int j = 1; j < 9; ++j) dj[j] = (int)(pg[j] - pg[j - 1]) * (j <= 4 ? -12288 : 12288);
  const int d0 = (int)(pg[4] - pg[0]) * 12288;
  dj[0] = d0 - (int)(pg[8] - pg[4]) * 12288;
  const unsigned scr = 159744 + wid * 4;
  const int sgn = (4 * wid + (lane >> 4)) < 10 ? 1 : -1;
  const unsigned mP = 73728 + wid * 1024;
  unsigned pvo = __umul24(col0(4 * wid + (lane >> 4)), 768) + ((((lane & 15) ^ (lane >> 4) ^ (4 * wid)) & 15) << 4)
                 + pr * (9 * 24576);
  int sg1;
  u32x2 cd, cr, po;
  po[0] = xo[4] + lane * 1024 + (b & 4) * 128;
  po[1] = po[0] + 8192;
  const unsigned mJ = 110592 + wid * 6144;
  const unsigned var = (b + 112) >> 8;
  u32x4 ja0, ja1, ja2, ja3, ja4, ja5;
  u32x2 ep;
  unsigned two = min((((b >> 3) - 18) * 4 + wid) * 42 + min(lane, 41u), 2303u) * 128;
  unsigned tcode = ((((b >> 3) - 18) * 4 + wid) * 11 + min(lane, 10u)) * 128;
  const unsigned jb = b < 144 ? b : 0;
  const unsigned G32 = (jb * 171) >> 11, jcb = jb - 12 * G32;
  {
    const unsigned jw = __umul24(lane >> 2, 768) + (lane & 3) * 16 + 64 * G32 + wid * (96 * 768);
    const unsigned ja = __umul24(8 * ((lane & 15) >> 2) + (lane & 3), 768) + 16 * (lane >> 4) + jcb * (32 * 768) + wid * 192;
    const unsigned cla = 1048576 + pr * 128;
    asm volatile(
      "s_nop 4\n"
      "s_cmp_lg_u32 s76, 0\n"
      "s_cbranch_scc1 LQ0\n"
      "s_mul_i32 s62, s88, 0x1080\n"
      "s_and_b32 s62, s62, 0x4180\n"
      "s_add_u32 s56, s30, s62\n"
      "s_addc_u32 s57, s31, 0\n"
      "s_cmp_lg_u32 s76, 0\n"
      "s_mov_b64 exec, 0x1\n"
      "global_load_dword v116, v118, s[56:57] nt\n"
      "global_load_dword v117, v119, s[56:57] nt\n"
      "s_mov_b64 exec, -1\n"
      "LQ0:\n"
      "v_mov_b32 v0, 0\n"
      "s_barrier\n"
      "global_load_dwordx2 v[252:253], v0, s[28:29] sc1\n"
      "s_cbranch_scc1 LQ1\n"
      ".p2align 5\n"
      "s_waitcnt vmcnt(2)\n"
      "s_memtime s[60:61]\n"
      "s_waitcnt vmcnt(1)\n"
      "s_memtime s[62:63]\n"
      "s_waitcnt vmcnt(0) lgkmcnt(0)\n"
      "s_sub_u32 s62, s62, s60\n"
      "s_bfe_u32 s63, s51, 0x10002\n"
      "s_cmp_lt_u32 s62, 30\n"
      "s_cselect_b32 s62, 1, 0\n"
      "v_mov_b32 v116, s62\n"
      "ds_write_b32 v108, v116 offset:16\n"
      "s_xor_b32 s62, s62, s63\n"
      "v_add_co_u32 v110, vcc, 0x7f4a7c15, v252\n"
      "v_mov_b32 v111, 0x9e3779b9\n"
      "v_addc_co_u32 v111, vcc, v111, v253, vcc\n"
      "v_and_or_b32 v110, v110, -2, s62\n"
      "s_mov_b64 exec, 1\n"
      "global_atomic_swap_x2 v[112:113], v114, v[110:111], s[28:29] sc0 sc1\n"
      "s_mov_b64 exec, -1\n"
      "LQ1:\n"
      "s_cmp_lg_u32 s50, 0\n"
      "s_cbranch_scc0 LU1\n"
      "s_getpc_b64 s[56:57]\n"
      "LC0_1:\n"
      "v_min_u32 v106, LCEND-LC0_1-4, v106\n"
      "global_load_dword v106, v106, s[56:57]\n"
      "global_load_dword v105, v105, s[26:27]\n"
      "v_and_b32 v117, -16, v108\n"
      "s_waitcnt lgkmcnt(0)\n"
      "s_barrier\n"
      "ds_read_b32 v116, v117 offset:16\n"
      "s_waitcnt lgkmcnt(0)\n"
      "v_readfirstlane_b32 s62, v116\n"
      "s_nop 3\n"
      "s_mul_i32 s63, s62, 0x3000\n"
      "s_sub_u32 s82, s98, s63\n"
      "s_lshl_b32 s62, s62, 1\n"
      "s_sub_u32 s62, 1, s62\n"
      "v_mul_i32_i24 v107, s62, v251\n"
      "v_mad_i32_i24 v238, v107, s82, v238\n"
      "s_add_u32 s56, s22, 0x0\n"
      "s_addc_u32 s57, s23, 0\n"
      "s_add_u32 m0, s18, 0x0\n"
      "s_nop 0\n"
      "global_load_lds_dwordx4 v238, s[56:57] offset:0 \n"
      "v_mad_i32_i24 v238, v107, s90, v238\n"
      "s_add_u32 s56, s22, 0x6000\n"
      "s_addc_u32 s57, s23, 0\n"
      "s_add_u32 m0, s18, 0x1000\n"
      "s_nop 0\n"
      "global_load_lds_dwordx4 v238, s[56:57] offset:0 \n"
      "v_mad_i32_i24 v238, v107, s91, v238\n"
      "s_add_u32 s56, s22, 0xc000\n"
      "s_addc_u32 s57, s23, 0\n"
      "s_add_u32 m0, s18, 0x2000\n"
      "s_nop 0\n"
      "global_load_lds_dwordx4 v238, s[56:57] offset:0 \n"
      "v_mad_i32_i24 v238, v107, s92, v238\n"
      "s_add_u32 s56, s22, 0x12000\n"
      "s_addc_u32 s57, s23, 0\n"
      "s_add_u32 m0, s18, 0x3000\n"
      "s_nop 0\n"
      "global_load_lds_dwordx4 v238, s[56:57] offset:0 \n"
      "v_mad_i32_i24 v238, v107, s93, v238\n"
      "s_add_u32 s56, s22, 0x18000\n"
      "s_addc_u32 s57, s23, 0\n"
      "s_add_u32 m0, s18, 0x4000\n"
      "s_nop 0\n"
      "global_load_lds_dwordx4 v238, s[56:57] offset:0 \n"
      "v_mad_i32_i24 v238, v107, s94, v238\n"
      "s_add_u32 s56, s22, 0x1e000\n"
      "s_addc_u32 s57, s23, 0\n"
      "s_add_u32 m0, s18, 0x5000\n"
      "s_nop 0\n"
      "global_load_lds_dwordx4 v238, s[56:57] offset:0 \n"
      "v_mad_i32_i24 v238, v107, s95, v238\n"
      "s_add_u32 s56, s22, 0x24000\n"
      "s_addc_u32 s57, s23, 0\n"
      "s_add_u32 m0, s18, 0x6000\n"
      "s_nop 0\n"
      "global_load_lds_dwordx4 v238, s[56:57] offset:0 \n"
      "v_mad_i32_i24 v238, v107, s96, v238\n"
      "s_add_u32 s56, s22, 0x2a000\n"
      "s_addc_u32 s57, s23, 0\n"
      "s_add_u32 m0, s18, 0x7000\n"
      "s_nop 0\n"
      "global_load_lds_dwordx4 v238, s[56:57] offset:0 \n"
      "v_mad_i32_i24 v238, v107, s97, v238\n"
      "s_add_u32 s56, s22, 0x30000\n"
      "s_addc_u32 s57, s23, 0\n"
      "s_add_u32 m0, s18, 0x8000\n"
      "s_nop 0\n"
      "global_load_lds_dwordx4 v238, s[56:57] offset:0 \n"
      "s_branch LUE\n"
      "LU1:\n"
      "s_add_u32 s56, s26, 0x0\n"
      "s_addc_u32 s57, s27, 0\n"
      "s_add_u32 m0, s75, 0x0\n"
      "s_nop 0\n"
      "global_load_lds_dwordx4 v100, s[56:57]\n"
      "s_add_u32 s56, s26, 0x3000\n"
      "s_addc_u32 s57, s27, 0\n"
      "s_add_u32 m0, s75, 0x400\n"
      "s_nop 0\n"
      "global_load_lds_dwordx4 v100, s[56:57]\n"
      "s_add_u32 s56, s26, 0x6000\n"
      "s_addc_u32 s57, s27, 0\n"
      "s_add_u32 m0, s75, 0x800\n"
      "s_nop 0\n"
      "global_load_lds_dwordx4 v100, s[56:57]\n"
      "s_add_u32 s56, s26, 0x9000\n"
      "s_addc_u32 s57, s27, 0\n"
      "s_add_u32 m0, s75, 0xc00\n"
      "s_nop 0\n"
      "global_load_lds_dwordx4 v100, s[56:57]\n"
      "s_add_u32 s56, s26, 0xc000\n"
      "s_addc_u32 s57, s27, 0\n"
      "s_add_u32 m0, s75, 0x1000\n"
      "s_nop 0\n"
      "global_load_lds_dwordx4 v100, s[56:57]\n"
      "s_add_u32 s56, s26, 0xf000\n"
      "s_addc_u32 s57, s27, 0\n"
      "s_add_u32 m0, s75, 0x1400\n"
      "s_nop 0\n"
      "global_load_lds_dwordx4 v100, s[56:57]\n"
      "global_load_dwordx4 v[4:7], v101, s[24:25] offset:0\n"
      "global_load_dwordx4 v[8:11], v101, s[24:25] offset:3072\n"
      "global_load_dwordx4 v[12:15], v101, s[24:25] offset:64\n"
      "global_load_dwordx4 v[16:19], v101, s[24:25] offset:3136\n"
      "global_load_dwordx4 v[20:23], v101, s[24:25] offset:128\n"
      "global_load_dwordx4 v[24:27], v101, s[24:25] offset:3200\n"
      "LUE:\n"

      : "={v[4:7]}"(ja0), "={v[8:11]}"(ja1), "={v[12:15]}"(ja2), "={v[16:19]}"(ja3), "={v[20:23]}"(ja4), "={v[24:27]}"(ja5),
        "={v[252:253]}"(ep), "+{v105}"(two), "+{v106}"(tcode), "={v[110:111]}"(cd), "={v[112:113]}"(cr), "+{v[118:119]}"(po),
        "+{v238}"(pvo), "={v107}"(sg1)
      : "{v114}"(cla), "{s[88:89]}"(t64), "{v100}"(jw), "{v101}"(ja), "{s[20:21]}"(cf), "{s[24:25]}"(w1), "{s[26:27]}"(w2), "{s[28:29]}"(ws), "{s[30:31]}"(out),
        "{s75}"(mJ), "{s51}"(b), "{s50}"(var), "{s76}"(wid), "{s[22:23]}"(pc), "{s18}"(mP), "{v108}"(scr), "{v251}"(sgn),
        "{s90}"(dj[1]), "{s91}"(dj[2]), "{s92}"(dj[3]), "{s93}"(dj[4]), "{s94}"(dj[5]), "{s95}"(dj[6]), "{s96}"(dj[7]), "{s97}"(dj[8]), "{s98}"(d0)
      : "memory", "scc", "vcc", "v0", "v116", "v117", "s56", "s57", "s60", "s61", "s62", "s63", "s82");
  }
  asm volatile("" : "+v"(lane));
  asm volatile("" : "+s"(wid));
  unsigned wro[3];
#pragma unroll
  for (int i = 0; i < 3; ++i) { const unsigned x = lane + 64 * i, q = __umul24(x, 171) >> 11; wro[i] = __umul24(q, 576) + x * 16; }
  const unsigned mW = wid * 6144;
  const char* w2w = w2 + wid * 192;
  const unsigned lo16 = lane * 16;
  const char* wb = ws + (65536 + wid * 73728);
  const unsigned c = lane & 15, g = lane >> 4;
  const unsigned cB0 = 73728 + c * 256 + ((g ^ c) << 4);
  const unsigned r8 = 8 * g + (c >> 2), c3 = (c & 3) * 16;
  const unsigned tra = mW + __umul24(r8, 192) + c3;
  const unsigned mX = wid * 1024;
  const unsigned fla = 4096 + wid * 288 + min(lo16, 272u);
  const unsigned c256 = c * 256 - 73728;
  const unsigned jfl = (b < 144 ? 4096 : 8192) + 8 * b;
  char* jo = ws + (65536 + (24 * G32 + jcb) * 1024);
  const unsigned jt = mJ + r8 * 64 + c3;
  const unsigned jrr = 135168 + lo16;
  const unsigned jrw = jrr + wid * 4096;
  const unsigned pk = 151552 + mX + lo16;
  const char* tab = (const char*)lane_tab + ((wid >> 1) * 4096 + (wid & 1) * 1024);
  asm volatile(
      "s_nop 4\n"
      "LBODY:\n"
      "s_branch LB0\n"
      "LRET:\n"
      "s_setpc_b64 s[56:57]\n"
      "LB0:\n"
      "s_cmp_lg_u32 s50, 0\n"
      "s_cbranch_scc1 LV1\n"
      "s_getpc_b64 s[56:57]\n"
      "LTR_0:\n"
      "s_add_u32 s56, s56, LTE_0-LTR_0\n"
      "s_addc_u32 s57, s57, 0\n"
      "s_mov_b64 exec, 0\n"
      "s_cmp_eq_u32 s76, 3\n"
      "s_cbranch_scc1 LPD2\n"
      "s_cmp_eq_u32 s76, 2\n"
      "s_cbranch_scc1 LPD1\n"
      "s_cmp_eq_u32 s76, 1\n"
      "s_cbranch_scc1 LPD0\n"
      "LTE_0:\n"
      "s_mov_b64 exec, -1\n"
      "s_waitcnt vmcnt(12)\n"
      "v_readfirstlane_b32 s80, v252\n"
      "v_readfirstlane_b32 s81, v253\n"
      "s_nop 3\n"
      "s_add_u32 s80, s80, 0x7f4a7c15\n"
      "s_addc_u32 s81, s81, 0x9e3779b9\n"
      "v_mov_b32 v252, s80\n"
      "v_mov_b32 v253, s81\n"
      "v_xor_b32 v116, v112, v110\n"
      "v_xor_b32 v117, v113, v111\n"
      "v_and_or_b32 v116, v116, -2, v117\n"
      "v_cmp_eq_u32 vcc, 0, v116\n"
      "v_cndmask_b32 v116, v110, v112, vcc\n"
      "s_mov_b64 exec, 1\n"
      "ds_write_b32 v108, v116\n"
      "s_mov_b64 exec, -1\n"
      "v_and_b32 v108, -16, v108\n"
      "s_waitcnt lgkmcnt(0)\n"
      "s_barrier\n"
      "ds_read_b32 v116, v108\n"
      "s_waitcnt lgkmcnt(0)\n"
      "v_readfirstlane_b32 s62, v116\n"
      "s_lshr_b32 s63, s51, 2\n"
      "s_nop 2\n"
      "s_xor_b32 s62, s62, s63\n"
      "s_and_b32 s62, s62, 1\n"
      "s_mul_i32 s63, s62, 0x3000\n"
      "s_sub_u32 s98, s98, s63\n"
      "s_lshl_b32 s62, s62, 1\n"
      "s_sub_u32 s62, 1, s62\n"
      "v_mul_i32_i24 v251, s62, v251\n"
      "s_lshl_b32 s19, s62, 1\n"
      "s_mul_i32 s100, s100, s62\n"
      "v_mad_i32_i24 v238, v251, s98, v238\n"
      "s_add_u32 s56, s22, 0x0\n"
      "s_addc_u32 s57, s23, 0\n"
      "s_add_u32 m0, s18, 0x0\n"
      "s_nop 0\n"
      "global_load_lds_dwordx4 v238, s[56:57] offset:0 \n"
      "v_mad_i32_i24 v238, v251, s90, v238\n"
      "s_add_u32 s56, s22, 0x6000\n"
      "s_addc_u32 s57, s23, 0\n"
      "s_add_u32 m0, s18, 0x1000\n"
      "s_nop 0\n"
      "global_load_lds_dwordx4 v238, s[56:57] offset:0 \n"
      "v_mad_i32_i24 v238, v251, s91, v238\n"
      "s_add_u32 s56, s22, 0xc000\n"
      "s_addc_u32 s57, s23, 0\n"
      "s_add_u32 m0, s18, 0x2000\n"
      "s_nop 0\n"
      "global_load_lds_dwordx4 v238, s[56:57] offset:0 \n"
      "v_mad_i32_i24 v238, v251, s92, v238\n"
      "s_add_u32 s56, s22, 0x12000\n"
      "s_addc_u32 s57, s23, 0\n"
      "s_add_u32 m0, s18, 0x3000\n"
      "s_nop 0\n"
      "global_load_lds_dwordx4 v238, s[56:57] offset:0 \n"
      "v_mad_i32_i24 v238, v251, s93, v238\n"
      "s_add_u32 s56, s22, 0x18000\n"
      "s_addc_u32 s57, s23, 0\n"
      "s_add_u32 m0, s18, 0x4000\n"
      "s_nop 0\n"
      "global_load_lds_dwordx4 v238, s[56:57] offset:0 \n"
      "v_mad_i32_i24 v238, v251, s94, v238\n"
      "s_add_u32 s56, s22, 0x1e000\n"
      "s_addc_u32 s57, s23, 0\n"
      "s_add_u32 m0, s18, 0x5000\n"
      "s_nop 0\n"
      "global_load_lds_dwordx4 v238, s[56:57] offset:0 \n"
      "v_mad_i32_i24 v238, v251, s95, v238\n"
      "s_add_u32 s56, s22, 0x24000\n"
      "s_addc_u32 s57, s23, 0\n"
      "s_add_u32 m0, s18, 0x6000\n"
      "s_nop 0\n"
      "global_load_lds_dwordx4 v238, s[56:57] offset:0 \n"
      "v_mad_i32_i24 v238, v251, s96, v238\n"
      "s_add_u32 s56, s22, 0x2a000\n"
      "s_addc_u32 s57, s23, 0\n"
      "s_add_u32 m0, s18, 0x7000\n"
      "s_nop 0\n"
      "global_load_lds_dwordx4 v238, s[56:57] offset:0 \n"
      "v_mad_i32_i24 v238, v251, s97, v238\n"
      "s_add_u32 s56, s22, 0x30000\n"
      "s_addc_u32 s57, s23, 0\n"
      "s_add_u32 m0, s18, 0x8000\n"
      "s_nop 0\n"
      "global_load_lds_dwordx4 v238, s[56:57] offset:0 \n"
      "s_waitcnt vmcnt(9)\n"
      "ds_read_b64_tr_b16 v[28:29], v102 offset:0\n"
      "ds_read_b64_tr_b16 v[30:31], v102 offset:256\n"
      "ds_read_b64_tr_b16 v[32:33], v102 offset:8\n"
      "ds_read_b64_tr_b16 v[34:35], v102 offset:264\n"
      "ds_read_b64_tr_b16 v[36:37], v102 offset:2048\n"
      "ds_read_b64_tr_b16 v[38:39], v102 offset:2304\n"
      "ds_read_b64_tr_b16 v[40:41], v102 offset:2056\n"
      "ds_read_b64_tr_b16 v[42:43], v102 offset:2312\n"
      "ds_read_b64_tr_b16 v[44:45], v102 offset:4096\n"
      "ds_read_b64_tr_b16 v[46:47], v102 offset:4352\n"
      "ds_read_b64_tr_b16 v[48:49], v102 offset:4104\n"
      "ds_read_b64_tr_b16 v[50:51], v102 offset:4360\n"
      "s_waitcnt lgkmcnt(0)\n"
      "v_mfma_f32_16x16x32_bf16 v[52:55], v[4:7], v[28:31], 0\n"
      "v_mfma_f32_16x16x32_bf16 v[56:59], v[8:11], v[28:31], 0\n"
      "v_mfma_f32_16x16x32_bf16 v[60:63], v[4:7], v[32:35], 0\n"
      "v_mfma_f32_16x16x32_bf16 v[64:67], v[8:11], v[32:35], 0\n"
      "v_mfma_f32_16x16x32_bf16 v[52:55], v[12:15], v[36:39], v[52:55]\n"
      "v_mfma_f32_16x16x32_bf16 v[56:59], v[16:19], v[36:39], v[56:59]\n"
      "v_mfma_f32_16x16x32_bf16 v[60:63], v[12:15], v[40:43], v[60:63]\n"
      "v_mfma_f32_16x16x32_bf16 v[64:67], v[16:19], v[40:43], v[64:67]\n"
      "v_mfma_f32_16x16x32_bf16 v[52:55], v[20:23], v[44:47], v[52:55]\n"
      "v_mfma_f32_16x16x32_bf16 v[56:59], v[24:27], v[44:47], v[56:59]\n"
      "v_mfma_f32_16x16x32_bf16 v[60:63], v[20:23], v[48:51], v[60:63]\n"
      "v_mfma_f32_16x16x32_bf16 v[64:67], v[24:27], v[48:51], v[64:67]\n"
      "s_nop 7\n"
      "ds_write_b128 v103, v[52:55] offset:0\n"
      "ds_write_b128 v103, v[56:59] offset:1024\n"
      "ds_write_b128 v103, v[60:63] offset:2048\n"
      "ds_write_b128 v103, v[64:67] offset:3072\n"
      "s_waitcnt lgkmcnt(0)\n"
      "s_barrier\n"
      "s_cmp_lg_u32 s76, 1\n"
      "s_cbranch_scc1 LJ1\n"
      "ds_read_b128 v[4:7], v104 offset:0\n"
      "ds_read_b128 v[8:11], v104 offset:1024\n"
      "ds_read_b128 v[12:15], v104 offset:2048\n"
      "ds_read_b128 v[16:19], v104 offset:3072\n"
      "ds_read_b128 v[20:23], v104 offset:4096\n"
      "ds_read_b128 v[24:27], v104 offset:5120\n"
      "ds_read_b128 v[28:31], v104 offset:6144\n"
      "ds_read_b128 v[32:35], v104 offset:7168\n"
      "ds_read_b128 v[36:39], v104 offset:8192\n"
      "ds_read_b128 v[40:43], v104 offset:9216\n"
      "ds_read_b128 v[44:47], v104 offset:10240\n"
      "ds_read_b128 v[48:51], v104 offset:11264\n"
      "ds_read_b128 v[52:55], v104 offset:12288\n"
      "ds_read_b128 v[56:59], v104 offset:13312\n"
      "ds_read_b128 v[60:63], v104 offset:14336\n"
      "ds_read_b128 v[64:67], v104 offset:15360\n"
      "s_waitcnt lgkmcnt(8)\n"
      "v_add_f32 v4, v4, v20\n"
      "v_add_f32 v5, v5, v21\n"
      "v_add_f32 v6, v6, v22\n"
      "v_add_f32 v7, v7, v23\n"
      "v_add_f32 v8, v8, v24\n"
      "v_add_f32 v9, v9, v25\n"
      "v_add_f32 v10, v10, v26\n"
      "v_add_f32 v11, v11, v27\n"
      "v_add_f32 v12, v12, v28\n"
      "v_add_f32 v13, v13, v29\n"
      "v_add_f32 v14, v14, v30\n"
      "v_add_f32 v15, v15, v31\n"
      "v_add_f32 v16, v16, v32\n"
      "v_add_f32 v17, v17, v33\n"
      "v_add_f32 v18, v18, v34\n"
      "v_add_f32 v19, v19, v35\n"
      "s_waitcnt lgkmcnt(4)\n"
      "v_add_f32 v4, v4, v36\n"
      "v_add_f32 v5, v5, v37\n"
      "v_add_f32 v6, v6, v38\n"
      "v_add_f32 v7, v7, v39\n"
      "v_add_f32 v8, v8, v40\n"
      "v_add_f32 v9, v9, v41\n"
      "v_add_f32 v10, v10, v42\n"
      "v_add_f32 v11, v11, v43\n"
      "v_add_f32 v12, v12, v44\n"
      "v_add_f32 v13, v13, v45\n"
      "v_add_f32 v14, v14, v46\n"
      "v_add_f32 v15, v15, v47\n"
      "v_add_f32 v16, v16, v48\n"
      "v_add_f32 v17, v17, v49\n"
      "v_add_f32 v18, v18, v50\n"
      "v_add_f32 v19, v19, v51\n"
      "s_waitcnt lgkmcnt(0)\n"
      "v_add_f32 v4, v4, v52\n"
      "v_add_f32 v5, v5, v53\n"
      "v_add_f32 v6, v6, v54\n"
      "v_add_f32 v7, v7, v55\n"
      "v_add_f32 v8, v8, v56\n"
      "v_add_f32 v9, v9, v57\n"
      "v_add_f32 v10, v10, v58\n"
      "v_add_f32 v11, v11, v59\n"
      "v_add_f32 v12, v12, v60\n"
      "v_add_f32 v13, v13, v61\n"
      "v_add_f32 v14, v14, v62\n"
      "v_add_f32 v15, v15, v63\n"
      "v_add_f32 v16, v16, v64\n"
      "v_add_f32 v17, v17, v65\n"
      "v_add_f32 v18, v18, v66\n"
      "v_add_f32 v19, v19, v67\n"
      "v_cvt_pk_bf16_f32 v68, v4, v5\n"
      "v_cvt_pk_bf16_f32 v69, v6, v7\n"
      "v_cvt_pk_bf16_f32 v70, v8, v9\n"
      "v_cvt_pk_bf16_f32 v71, v10, v11\n"
      "v_cvt_pk_bf16_f32 v72, v12, v13\n"
      "v_cvt_pk_bf16_f32 v73, v14, v15\n"
      "v_cvt_pk_bf16_f32 v74, v16, v17\n"
      "v_cvt_pk_bf16_f32 v75, v18, v19\n"
      "global_store_dwordx4 v239, v[68:71], s[72:73] sc1\n"
      "s_add_u32 s72, s72, 0x3000\n"
      "s_addc_u32 s73, s73, 0\n"
      "global_store_dwordx4 v239, v[72:75], s[72:73] sc1\n"
      "LJ1:\n"
      "s_add_u32 s78, s64, 0x0\n"
      "s_addc_u32 s79, s65, 0\n"
      "s_add_u32 m0, s74, 0x0\n"
      "s_nop 0\n"
      "global_load_lds_dwordx4 v240, s[78:79]\n"
      "s_add_u32 m0, s74, 0x400\n"
      "s_nop 0\n"
      "global_load_lds_dwordx4 v241, s[78:79]\n"
      "s_add_u32 m0, s74, 0x800\n"
      "s_nop 0\n"
      "global_load_lds_dwordx4 v242, s[78:79]\n"
      "s_add_u32 s78, s64, 0x3000\n"
      "s_addc_u32 s79, s65, 0\n"
      "s_add_u32 m0, s74, 0xc00\n"
      "s_nop 0\n"
      "global_load_lds_dwordx4 v240, s[78:79]\n"
      "s_add_u32 m0, s74, 0x1000\n"
      "s_nop 0\n"
      "global_load_lds_dwordx4 v241, s[78:79]\n"
      "s_add_u32 m0, s74, 0x1400\n"
      "s_nop 0\n"
      "global_load_lds_dwordx4 v242, s[78:79]\n"
      "s_add_u32 s78, s64, 0x6000\n"
      "s_addc_u32 s79, s65, 0\n"
      "s_add_u32 m0, s74, 0x6000\n"
      "s_nop 0\n"
      "global_load_lds_dwordx4 v240, s[78:79]\n"
      "s_add_u32 m0, s74, 0x6400\n"
      "s_nop 0\n"
      "global_load_lds_dwordx4 v241, s[78:79]\n"
      "s_add_u32 m0, s74, 0x6800\n"
      "s_nop 0\n"
      "global_load_lds_dwordx4 v242, s[78:79]\n"
      "s_add_u32 s78, s64, 0x9000\n"
      "s_addc_u32 s79, s65, 0\n"
      "s_add_u32 m0, s74, 0x6c00\n"
      "s_nop 0\n"
      "global_load_lds_dwordx4 v240, s[78:79]\n"
      "s_add_u32 m0, s74, 0x7000\n"
      "s_nop 0\n"
      "global_load_lds_dwordx4 v241, s[78:79]\n"
      "s_add_u32 m0, s74, 0x7400\n"
      "s_nop 0\n"
      "global_load_lds_dwordx4 v242, s[78:79]\n"
      "s_add_u32 s78, s64, 0xc000\n"
      "s_addc_u32 s79, s65, 0\n"
      "s_add_u32 m0, s74, 0xc000\n"
      "s_nop 0\n"
      "global_load_lds_dwordx4 v240, s[78:79]\n"
      "s_add_u32 m0, s74, 0xc400\n"
      "s_nop 0\n"
      "global_load_lds_dwordx4 v241, s[78:79]\n"
      "s_add_u32 m0, s74, 0xc800\n"
      "s_nop 0\n"
      "global_load_lds_dwordx4 v242, s[78:79]\n"
      "s_add_u32 s78, s64, 0xf000\n"
      "s_addc_u32 s79, s65, 0\n"
      "s_add_u32 m0, s74, 0xcc00\n"
      "s_nop 0\n"
      "global_load_lds_dwordx4 v240, s[78:79]\n"
      "s_add_u32 m0, s74, 0xd000\n"
      "s_nop 0\n"
      "global_load_lds_dwordx4 v241, s[78:79]\n"
      "s_add_u32 m0, s74, 0xd400\n"
      "s_nop 0\n"
      "global_load_lds_dwordx4 v242, s[78:79]\n"
      "s_add_u32 m0, s17, 0x25000\n"
      "s_nop 0\n"
      "global_load_lds_dwordx4 v239, s[46:47] offset:0\n"
      "s_add_u32 m0, s17, 0x25800\n"
      "s_nop 0\n"
      "global_load_lds_dwordx4 v239, s[46:47] offset:2048\n"
      "s_waitcnt vmcnt(14)\n"
      "ds_read_b64_tr_b16 a[0:1], v243 offset:0\n"
      "ds_read_b64_tr_b16 a[2:3], v243 offset:768\n"
      "ds_read_b64_tr_b16 a[4:5], v243 offset:8\n"
      "ds_read_b64_tr_b16 a[6:7], v243 offset:776\n"
      "ds_read_b64_tr_b16 a[8:9], v243 offset:64\n"
      "ds_read_b64_tr_b16 a[10:11], v243 offset:832\n"
      "ds_read_b64_tr_b16 a[12:13], v243 offset:72\n"
      "ds_read_b64_tr_b16 a[14:15], v243 offset:840\n"
      "ds_read_b64_tr_b16 a[16:17], v243 offset:128\n"
      "ds_read_b64_tr_b16 a[18:19], v243 offset:896\n"
      "ds_read_b64_tr_b16 a[20:21], v243 offset:136\n"
      "ds_read_b64_tr_b16 a[22:23], v243 offset:904\n"
      "s_barrier\n"
      "s_cmp_lg_u32 s76, 1\n"
      "s_cbranch_scc1 LA_0\n"
      "v_mov_b32 v244, s77\n"
      "s_mov_b64 exec, 1\n"
      "global_store_dwordx2 v244, v[252:253], s[28:29] sc1\n"
      "s_mov_b64 exec, -1\n"
      "s_nop 4\n"
      "LA_0:\n"
      "ds_read_b128 v[216:219], v232 offset:0\n"
      "ds_read_b128 v[220:223], v232 offset:4096\n"
      "ds_read_b128 v[224:227], v232 offset:8192\n"
      "s_waitcnt lgkmcnt(2)\n"
      "v_mfma_f32_16x16x32_bf16 v[0:3], a[0:3], v[216:219], 0\n"
      "s_add_u32 s78, s64, 0x12000\n"
      "s_addc_u32 s79, s65, 0\n"
      "s_add_u32 m0, s74, 0x0\n"
      "v_mfma_f32_16x16x32_bf16 v[4:7], a[4:7], v[216:219], 0\n"
      "global_load_lds_dwordx4 v240, s[78:79]\n"
      "v_mfma_f32_16x16x32_bf16 v[8:11], a[8:11], v[216:219], 0\n"
      "ds_read_b128 v[228:231], v232 offset:12288\n"
      "v_mfma_f32_16x16x32_bf16 v[12:15], a[12:15], v[216:219], 0\n"
      "v_mfma_f32_16x16x32_bf16 v[16:19], a[16:19], v[216:219], 0\n"
      "s_add_u32 m0, s74, 0x400\n"
      "v_mfma_f32_16x16x32_bf16 v[20:23], a[20:23], v[216:219], 0\n"
      "global_load_lds_dwordx4 v241, s[78:79]\n"
      "s_waitcnt lgkmcnt(2)\n"
      "v_mfma_f32_16x16x32_bf16 v[24:27], a[0:3], v[220:223], 0\n"
      "s_waitcnt vmcnt(10)\n"
      "v_mfma_f32_16x16x32_bf16 v[28:31], a[4:7], v[220:223], 0\n"
      "ds_read_b64_tr_b16 a[24:25], v243 offset:24576\n"
      "v_mfma_f32_16x16x32_bf16 v[32:35], a[8:11], v[220:223], 0\n"
      "ds_read_b128 v[216:219], v232 offset:16384\n"
      "s_add_u32 m0, s74, 0x800\n"
      "v_mfma_f32_16x16x32_bf16 v[36:39], a[12:15], v[220:223], 0\n"
      "ds_read_b64_tr_b16 a[26:27], v243 offset:25344\n"
      "global_load_lds_dwordx4 v242, s[78:79]\n"
      "v_mfma_f32_16x16x32_bf16 v[40:43], a[16:19], v[220:223], 0\n"
      "LPD0:\n"
      "s_cbranch_execz LPD3\n"
      "ds_read_b64_tr_b16 a[28:29], v243 offset:24584\n"
      "v_mfma_f32_16x16x32_bf16 v[44:47], a[20:23], v[220:223], 0\n"
      "s_waitcnt lgkmcnt(5)\n"
      "v_mfma_f32_16x16x32_bf16 v[48:51], a[0:3], v[224:227], 0\n"
      "ds_read_b64_tr_b16 a[30:31], v243 offset:25352\n"
      "s_add_u32 s78, s64, 0x15000\n"
      "s_addc_u32 s79, s65, 0\n"
      "s_add_u32 m0, s74, 0xc00\n"
      "v_mfma_f32_16x16x32_bf16 v[52:55], a[4:7], v[224:227], 0\n"
      "ds_read_b64_tr_b16 a[32:33], v243 offset:24640\n"
      "global_load_lds_dwordx4 v240, s[78:79]\n"
      "v_mfma_f32_16x16x32_bf16 v[56:59], a[8:11], v[224:227], 0\n"
      "ds_read_b128 v[220:223], v232 offset:20480\n"
      "v_mfma_f32_16x16x32_bf16 v[60:63], a[12:15], v[224:227], 0\n"
      "ds_read_b64_tr_b16 a[34:35], v243 offset:25408\n"
      "v_mfma_f32_16x16x32_bf16 v[64:67], a[16:19], v[224:227], 0\n"
      "ds_read_b64_tr_b16 a[36:37], v243 offset:24648\n"
      "s_add_u32 m0, s74, 0x1000\n"
      "v_mfma_f32_16x16x32_bf16 v[68:71], a[20:23], v[224:227], 0\n"
      "global_load_lds_dwordx4 v241, s[78:79]\n"
      "s_waitcnt lgkmcnt(9)\n"
      "v_mfma_f32_16x16x32_bf16 v[72:75], a[0:3], v[228:231], 0\n"
      "ds_read_b64_tr_b16 a[38:39], v243 offset:25416\n"
      "v_mfma_f32_16x16x32_bf16 v[76:79], a[4:7], v[228:231], 0\n"
      "ds_read_b64_tr_b16 a[40:41], v243 offset:24704\n"
      "v_mfma_f32_16x16x32_bf16 v[80:83], a[8:11], v[228:231], 0\n"
      "ds_read_b128 v[224:227], v232 offset:24576\n"
      "s_add_u32 m0, s74, 0x1400\n"
      "v_mfma_f32_16x16x32_bf16 v[84:87], a[12:15], v[228:231], 0\n"
      "ds_read_b64_tr_b16 a[42:43], v243 offset:25472\n"
      "global_load_lds_dwordx4 v242, s[78:79]\n"
      "v_mfma_f32_16x16x32_bf16 v[88:91], a[16:19], v[228:231], 0\n"
      "ds_read_b64_tr_b16 a[44:45], v243 offset:24712\n"
      "v_mfma_f32_16x16x32_bf16 v[92:95], a[20:23], v[228:231], 0\n"
      "s_waitcnt lgkmcnt(12)\n"
      "v_mfma_f32_16x16x32_bf16 v[96:99], a[0:3], v[216:219], 0\n"
      "ds_read_b64_tr_b16 a[46:47], v243 offset:25480\n"
      "v_mfma_f32_16x16x32_bf16 v[100:103], a[4:7], v[216:219], 0\n"
      "s_waitcnt vmcnt(8)\n"
      "v_mfma_f32_16x16x32_bf16 v[104:107], a[8:11], v[216:219], 0\n"
      "ds_read_b128 v[228:231], v232 offset:28672\n"
      "v_mfma_f32_16x16x32_bf16 v[108:111], a[12:15], v[216:219], 0\n"
      "ds_read_b64_tr_b16 a[48:49], v243 offset:49152\n"
      "v_mfma_f32_16x16x32_bf16 v[112:115], a[16:19], v[216:219], 0\n"
      "ds_read_b64_tr_b16 a[50:51], v243 offset:49920\n"
      "v_mfma_f32_16x16x32_bf16 v[116:119], a[20:23], v[216:219], 0\n"
      "s_waitcnt lgkmcnt(11)\n"
      "v_mfma_f32_16x16x32_bf16 v[120:123], a[0:3], v[220:223], 0\n"
      "ds_read_b64_tr_b16 a[52:53], v243 offset:49160\n"
      "v_mfma_f32_16x16x32_bf16 v[124:127], a[4:7], v[220:223], 0\n"
      "ds_read_b64_tr_b16 a[54:55], v243 offset:49928\n"
      "v_mfma_f32_16x16x32_bf16 v[128:131], a[8:11], v[220:223], 0\n"
      "ds_read_b128 v[216:219], v232 offset:32768\n"
      "v_add_u32 v232, 0x9000, v232\n"
      "v_mfma_f32_16x16x32_bf16 v[132:135], a[12:15], v[220:223], 0\n"
      "ds_read_b64_tr_b16 a[56:57], v243 offset:49216\n"
      "v_mfma_f32_16x16x32_bf16 v[136:139], a[16:19], v[220:223], 0\n"
      "ds_read_b64_tr_b16 a[58:59], v243 offset:49984\n"
      "s_waitcnt lgkmcnt(8)\n"
      "s_add_u32 s78, s64, 0x18000\n"
      "s_addc_u32 s79, s65, 0\n"
      "s_add_u32 m0, s74, 0x6000\n"
      "v_mfma_f32_16x16x32_bf16 v[140:143], a[20:23], v[220:223], 0\n"
      "global_load_lds_dwordx4 v240, s[78:79]\n"
      "v_mfma_f32_16x16x32_bf16 v[144:147], a[0:3], v[224:227], 0\n"
      "ds_read_b64_tr_b16 a[60:61], v243 offset:49224\n"
      "v_mfma_f32_16x16x32_bf16 v[148:151], a[4:7], v[224:227], 0\n"
      "ds_read_b64_tr_b16 a[62:63], v243 offset:49992\n"
      "v_mfma_f32_16x16x32_bf16 v[152:155], a[8:11], v[224:227], 0\n"
      "ds_read_b128 v[220:223], v233 offset:0\n"
      "s_add_u32 m0, s74, 0x6400\n"
      "v_mfma_f32_16x16x32_bf16 v[156:159], a[12:15], v[224:227], 0\n"
      "ds_read_b64_tr_b16 a[64:65], v243 offset:49280\n"
      "global_load_lds_dwordx4 v241, s[78:79]\n"
      "v_mfma_f32_16x16x32_bf16 v[160:163], a[16:19], v[224:227], 0\n"
      "ds_read_b64_tr_b16 a[66:67], v243 offset:50048\n"
      "v_mfma_f32_16x16x32_bf16 v[164:167], a[20:23], v[224:227], 0\n"
      "s_waitcnt lgkmcnt(12)\n"
      "v_mfma_f32_16x16x32_bf16 v[168:171], a[0:3], v[228:231], 0\n"
      "ds_read_b64_tr_b16 a[68:69], v243 offset:49288\n"
      "s_add_u32 m0, s74, 0x6800\n"
      "v_mfma_f32_16x16x32_bf16 v[172:175], a[4:7], v[228:231], 0\n"
      "ds_read_b64_tr_b16 a[70:71], v243 offset:50056\n"
      "global_load_lds_dwordx4 v242, s[78:79]\n"
      "v_mfma_f32_16x16x32_bf16 v[176:179], a[8:11], v[228:231], 0\n"
      "ds_read_b128 v[224:227], v233 offset:4096\n"
      "v_mfma_f32_16x16x32_bf16 v[180:183], a[12:15], v[228:231], 0\n"
      "v_mfma_f32_16x16x32_bf16 v[184:187], a[16:19], v[228:231], 0\n"
      "s_add_u32 s78, s64, 0x1b000\n"
      "s_addc_u32 s79, s65, 0\n"
      "s_add_u32 m0, s74, 0x6c00\n"
      "v_mfma_f32_16x16x32_bf16 v[188:191], a[20:23], v[228:231], 0\n"
      "global_load_lds_dwordx4 v240, s[78:79]\n"
      "s_waitcnt lgkmcnt(10)\n"
      "v_mfma_f32_16x16x32_bf16 v[192:195], a[0:3], v[216:219], 0\n"
      "v_mfma_f32_16x16x32_bf16 v[196:199], a[4:7], v[216:219], 0\n"
      "v_mfma_f32_16x16x32_bf16 v[200:203], a[8:11], v[216:219], 0\n"
      "ds_read_b128 v[228:231], v233 offset:8192\n"
      "s_add_u32 m0, s74, 0x7000\n"
      "v_mfma_f32_16x16x32_bf16 v[204:207], a[12:15], v[216:219], 0\n"
      "global_load_lds_dwordx4 v241, s[78:79]\n"
      "v_mfma_f32_16x16x32_bf16 v[208:211], a[16:19], v[216:219], 0\n"
      "v_mfma_f32_16x16x32_bf16 v[212:215], a[20:23], v[216:219], 0\n"
      "s_waitcnt lgkmcnt(6)\n"
      "v_mfma_f32_16x16x32_bf16 v[0:3], a[24:27], v[220:223], v[0:3]\n"
      "s_add_u32 m0, s74, 0x7400\n"
      "v_mfma_f32_16x16x32_bf16 v[4:7], a[28:31], v[220:223], v[4:7]\n"
      "global_load_lds_dwordx4 v242, s[78:79]\n"
      "v_mfma_f32_16x16x32_bf16 v[8:11], a[32:35], v[220:223], v[8:11]\n"
      "ds_read_b128 v[216:219], v233 offset:12288\n"
      "s_waitcnt vmcnt(6)\n"
      "v_mfma_f32_16x16x32_bf16 v[12:15], a[36:39], v[220:223], v[12:15]\n"
      "ds_read_b64_tr_b16 a[72:73], v243 offset:0\n"
      "v_mfma_f32_16x16x32_bf16 v[16:19], a[40:43], v[220:223], v[16:19]\n"
      "ds_read_b64_tr_b16 a[74:75], v243 offset:768\n"
      "v_mad_i32_i24 v238, v251, s89, v238\n"
      "s_add_u32 s56, s22, 0x0\n"
      "s_addc_u32 s57, s23, 0\n"
      "s_add_u32 m0, s18, 0x8f00\n"
      "v_mfma_f32_16x16x32_bf16 v[20:23], a[44:47], v[220:223], v[20:23]\n"
      "global_load_lds_dwordx4 v238, s[56:57] offset:256 \n"
      "s_waitcnt lgkmcnt(4)\n"
      "v_mfma_f32_16x16x32_bf16 v[24:27], a[24:27], v[224:227], v[24:27]\n"
      "ds_read_b64_tr_b16 a[76:77], v243 offset:8\n"
      "v_mfma_f32_16x16x32_bf16 v[28:31], a[28:31], v[224:227], v[28:31]\n"
      "ds_read_b64_tr_b16 a[78:79], v243 offset:776\n"
      "v_mad_i32_i24 v238, v251, s90, v238\n"
      "s_add_u32 s56, s22, 0x6000\n"
      "s_addc_u32 s57, s23, 0\n"
      "s_add_u32 m0, s18, 0x9f00\n"
      "v_mfma_f32_16x16x32_bf16 v[32:35], a[32:35], v[224:227], v[32:35]\n"
      "ds_read_b128 v[220:223], v233 offset:16384\n"
      "global_load_lds_dwordx4 v238, s[56:57] offset:256 \n"
      "v_mfma_f32_16x16x32_bf16 v[36:39], a[36:39], v[224:227], v[36:39]\n"
      "ds_read_b64_tr_b16 a[80:81], v243 offset:64\n"
      "v_mfma_f32_16x16x32_bf16 v[40:43], a[40:43], v[224:227], v[40:43]\n"
      "ds_read_b64_tr_b16 a[82:83], v243 offset:832\n"
      "v_mad_i32_i24 v238, v251, s91, v238\n"
      "s_add_u32 s56, s22, 0xc000\n"
      "s_addc_u32 s57, s23, 0\n"
      "s_add_u32 m0, s18, 0xaf00\n"
      "v_mfma_f32_16x16x32_bf16 v[44:47], a[44:47], v[224:227], v[44:47]\n"
      "global_load_lds_dwordx4 v238, s[56:57] offset:256 \n"
      "s_waitcnt lgkmcnt(8)\n"
      "v_mfma_f32_16x16x32_bf16 v[48:51], a[24:27], v[228:231], v[48:51]\n"
      "ds_read_b64_tr_b16 a[84:85], v243 offset:72\n"
      "v_mfma_f32_16x16x32_bf16 v[52:55], a[28:31], v[228:231], v[52:55]\n"
      "ds_read_b64_tr_b16 a[86:87], v243 offset:840\n"
      "v_mad_i32_i24 v238, v251, s92, v238\n"
      "s_add_u32 s56, s22, 0x12000\n"
      "s_addc_u32 s57, s23, 0\n"
      "s_add_u32 m0, s18, 0xbf00\n"
      "v_mfma_f32_16x16x32_bf16 v[56:59], a[32:35], v[228:231], v[56:59]\n"
      "ds_read_b128 v[224:227], v233 offset:20480\n"
      "global_load_lds_dwordx4 v238, s[56:57] offset:256 \n"
      "v_mfma_f32_16x16x32_bf16 v[60:63], a[36:39], v[228:231], v[60:63]\n"
      "ds_read_b64_tr_b16 a[88:89], v243 offset:128\n"
      "v_mfma_f32_16x16x32_bf16 v[64:67], a[40:43], v[228:231], v[64:67]\n"
      "ds_read_b64_tr_b16 a[90:91], v243 offset:896\n"
      "v_mad_i32_i24 v238, v251, s93, v238\n"
      "s_add_u32 s56, s22, 0x18000\n"
      "s_addc_u32 s57, s23, 0\n"
      "s_add_u32 m0, s18, 0xcf00\n"
      "v_mfma_f32_16x16x32_bf16 v[68:71], a[44:47], v[228:231], v[68:71]\n"
      "global_load_lds_dwordx4 v238, s[56:57] offset:256 \n"
      "s_waitcnt lgkmcnt(12)\n"
      "v_mfma_f32_16x16x32_bf16 v[72:75], a[24:27], v[216:219], v[72:75]\n"
      "ds_read_b64_tr_b16 a[92:93], v243 offset:136\n"
      "v_mfma_f32_16x16x32_bf16 v[76:79], a[28:31], v[216:219], v[76:79]\n"
      "ds_read_b64_tr_b16 a[94:95], v243 offset:904\n"
      "v_mad_i32_i24 v238, v251, s94, v238\n"
      "s_add_u32 s56, s22, 0x1e000\n"
      "s_addc_u32 s57, s23, 0\n"
      "s_add_u32 m0, s18, 0xdf00\n"
      "v_mfma_f32_16x16x32_bf16 v[80:83], a[32:35], v[216:219], v[80:83]\n"
      "ds_read_b128 v[228:231], v233 offset:24576\n"
      "global_load_lds_dwordx4 v238, s[56:57] offset:256 \n"
      "v_mfma_f32_16x16x32_bf16 v[84:87], a[36:39], v[216:219], v[84:87]\n"
      "v_mfma_f32_16x16x32_bf16 v[88:91], a[40:43], v[216:219], v[88:91]\n"
      "v_mad_i32_i24 v238, v251, s95, v238\n"
      "s_add_u32 s56, s22, 0x24000\n"
      "s_addc_u32 s57, s23, 0\n"
      "s_add_u32 m0, s18, 0xef00\n"
      "v_mfma_f32_16x16x32_bf16 v[92:95], a[44:47], v[216:219], v[92:95]\n"
      "global_load_lds_dwordx4 v238, s[56:57] offset:256 \n"
      "s_waitcnt lgkmcnt(10)\n"
      "v_mfma_f32_16x16x32_bf16 v[96:99], a[24:27], v[220:223], v[96:99]\n"
      "v_mfma_f32_16x16x32_bf16 v[100:103], a[28:31], v[220:223], v[100:103]\n"
      "v_mad_i32_i24 v238, v251, s96, v238\n"
      "s_add_u32 s56, s22, 0x2a000\n"
      "s_addc_u32 s57, s23, 0\n"
      "s_add_u32 m0, s18, 0xff00\n"
      "v_mfma_f32_16x16x32_bf16 v[104:107], a[32:35], v[220:223], v[104:107]\n"
      "ds_read_b128 v[216:219], v233 offset:28672\n"
      "global_load_lds_dwordx4 v238, s[56:57] offset:256 \n"
      "v_mfma_f32_16x16x32_bf16 v[108:111], a[36:39], v[220:223], v[108:111]\n"
      "v_mfma_f32_16x16x32_bf16 v[112:115], a[40:43], v[220:223], v[112:115]\n"
      "v_mad_i32_i24 v238, v251, s97, v238\n"
      "s_add_u32 s56, s22, 0x30000\n"
      "s_addc_u32 s57, s23, 0\n"
      "s_add_u32 m0, s18, 0x10f00\n"
      "v_mfma_f32_16x16x32_bf16 v[116:119], a[44:47], v[220:223], v[116:119]\n"
      "global_load_lds_dwordx4 v238, s[56:57] offset:256 \n"
      "s_waitcnt lgkmcnt(6)\n"
      "v_mfma_f32_16x16x32_bf16 v[120:123], a[24:27], v[224:227], v[120:123]\n"
      "v_mfma_f32_16x16x32_bf16 v[124:127], a[28:31], v[224:227], v[124:127]\n"
      "v_mfma_f32_16x16x32_bf16 v[128:131], a[32:35], v[224:227], v[128:131]\n"
      "ds_read_b128 v[220:223], v233 offset:32768\n"
      "v_add_u32 v233, 0x9000, v233\n"
      "s_add_u32 s78, s64, 0x1e000\n"
      "s_addc_u32 s79, s65, 0\n"
      "s_add_u32 m0, s74, 0xc000\n"
      "v_mfma_f32_16x16x32_bf16 v[132:135], a[36:39], v[224:227], v[132:135]\n"
      "global_load_lds_dwordx4 v240, s[78:79]\n"
      "v_mfma_f32_16x16x32_bf16 v[136:139], a[40:43], v[224:227], v[136:139]\n"
      "v_mfma_f32_16x16x32_bf16 v[140:143], a[44:47], v[224:227], v[140:143]\n"
      "s_waitcnt lgkmcnt(2)\n"
      "v_mfma_f32_16x16x32_bf16 v[144:147], a[24:27], v[228:231], v[144:147]\n"
      "s_waitcnt vmcnt(10)\n"
      "s_add_u32 m0, s74, 0xc400\n"
      "v_mfma_f32_16x16x32_bf16 v[148:151], a[28:31], v[228:231], v[148:151]\n"
      "ds_read_b64_tr_b16 a[96:97], v243 offset:24576\n"
      "global_load_lds_dwordx4 v241, s[78:79]\n"
      "v_mfma_f32_16x16x32_bf16 v[152:155], a[32:35], v[228:231], v[152:155]\n"
      "ds_read_b128 v[224:227], v234 offset:0\n"
      "v_mfma_f32_16x16x32_bf16 v[156:159], a[36:39], v[228:231], v[156:159]\n"
      "ds_read_b64_tr_b16 a[98:99], v243 offset:25344\n"
      "v_mfma_f32_16x16x32_bf16 v[160:163], a[40:43], v[228:231], v[160:163]\n"
      "ds_read_b64_tr_b16 a[100:101], v243 offset:24584\n"
      "s_add_u32 m0, s74, 0xc800\n"
      "v_mfma_f32_16x16x32_bf16 v[164:167], a[44:47], v[228:231], v[164:167]\n"
      "global_load_lds_dwordx4 v242, s[78:79]\n"
      "s_waitcnt lgkmcnt(5)\n"
      "v_mfma_f32_16x16x32_bf16 v[168:171], a[24:27], v[216:219], v[168:171]\n"
      "ds_read_b64_tr_b16 a[102:103], v243 offset:25352\n"
      "v_mfma_f32_16x16x32_bf16 v[172:175], a[28:31], v[216:219], v[172:175]\n"
      "ds_read_b64_tr_b16 a[104:105], v243 offset:24640\n"
      "v_mfma_f32_16x16x32_bf16 v[176:179], a[32:35], v[216:219], v[176:179]\n"
      "ds_read_b128 v[228:231], v234 offset:4096\n"
      "s_add_u32 s78, s64, 0x21000\n"
      "s_addc_u32 s79, s65, 0\n"
      "s_add_u32 m0, s74, 0xcc00\n"
      "v_mfma_f32_16x16x32_bf16 v[180:183], a[36:39], v[216:219], v[180:183]\n"
      "ds_read_b64_tr_b16 a[106:107], v243 offset:25408\n"
      "global_load_lds_dwordx4 v240, s[78:79]\n"
      "v_mfma_f32_16x16x32_bf16 v[184:187], a[40:43], v[216:219], v[184:187]\n"
      "ds_read_b64_tr_b16 a[108:109], v243 offset:24648\n"
      "v_mfma_f32_16x16x32_bf16 v[188:191], a[44:47], v[216:219], v[188:191]\n"
      "s_waitcnt lgkmcnt(9)\n"
      "v_mfma_f32_16x16x32_bf16 v[192:195], a[24:27], v[220:223], v[192:195]\n"
      "ds_read_b64_tr_b16 a[110:111], v243 offset:25416\n"
      "s_add_u32 m0, s74, 0xd000\n"
      "v_mfma_f32_16x16x32_bf16 v[196:199], a[28:31], v[220:223], v[196:199]\n"
      "ds_read_b64_tr_b16 a[112:113], v243 offset:24704\n"
      "global_load_lds_dwordx4 v241, s[78:79]\n"
      "v_mfma_f32_16x16x32_bf16 v[200:203], a[32:35], v[220:223], v[200:203]\n"
      "ds_read_b128 v[216:219], v234 offset:8192\n"
      "v_mfma_f32_16x16x32_bf16 v[204:207], a[36:39], v[220:223], v[204:207]\n"
      "ds_read_b64_tr_b16 a[114:115], v243 offset:25472\n"
      "v_mfma_f32_16x16x32_bf16 v[208:211], a[40:43], v[220:223], v[208:211]\n"
      "ds_read_b64_tr_b16 a[116:117], v243 offset:24712\n"
      "s_add_u32 m0, s74, 0xd400\n"
      "v_mfma_f32_16x16x32_bf16 v[212:215], a[44:47], v[220:223], v[212:215]\n"
      "global_load_lds_dwordx4 v242, s[78:79]\n"
      "s_waitcnt lgkmcnt(12)\n"
      "v_mfma_f32_16x16x32_bf16 v[0:3], a[48:51], v[224:227], v[0:3]\n"
      "ds_read_b64_tr_b16 a[118:119], v243 offset:25480\n"
      "v_mfma_f32_16x16x32_bf16 v[4:7], a[52:55], v[224:227], v[4:7]\n"
      "v_mfma_f32_16x16x32_bf16 v[8:11], a[56:59], v[224:227], v[8:11]\n"
      "ds_read_b128 v[220:223], v234 offset:12288\n"
      "s_add_u32 s78, s64, 0x24000\n"
      "s_addc_u32 s79, s65, 0\n"
      "s_add_u32 m0, s74, 0x0\n"
      "v_mfma_f32_16x16x32_bf16 v[12:15], a[60:63], v[224:227], v[12:15]\n"
      "global_load_lds_dwordx4 v240, s[78:79]\n"
      "v_mfma_f32_16x16x32_bf16 v[16:19], a[64:67], v[224:227], v[16:19]\n"
      "v_mfma_f32_16x16x32_bf16 v[20:23], a[68:71], v[224:227], v[20:23]\n"
      "s_waitcnt lgkmcnt(9)\n"
      "v_mfma_f32_16x16x32_bf16 v[24:27], a[48:51], v[228:231], v[24:27]\n"
      "s_add_u32 m0, s74, 0x400\n"
      "v_mfma_f32_16x16x32_bf16 v[28:31], a[52:55], v[228:231], v[28:31]\n"
      "global_load_lds_dwordx4 v241, s[78:79]\n"
      "v_mfma_f32_16x16x32_bf16 v[32:35], a[56:59], v[228:231], v[32:35]\n"
      "ds_read_b128 v[224:227], v234 offset:16384\n"
      "v_mfma_f32_16x16x32_bf16 v[36:39], a[60:63], v[228:231], v[36:39]\n"
      "v_mfma_f32_16x16x32_bf16 v[40:43], a[64:67], v[228:231], v[40:43]\n"
      "s_add_u32 m0, s74, 0x800\n"
      "v_mfma_f32_16x16x32_bf16 v[44:47], a[68:71], v[228:231], v[44:47]\n"
      "global_load_lds_dwordx4 v242, s[78:79]\n"
      "s_waitcnt lgkmcnt(5)\n"
      "v_mfma_f32_16x16x32_bf16 v[48:51], a[48:51], v[216:219], v[48:51]\n"
      "v_mfma_f32_16x16x32_bf16 v[52:55], a[52:55], v[216:219], v[52:55]\n"
      "v_mfma_f32_16x16x32_bf16 v[56:59], a[56:59], v[216:219], v[56:59]\n"
      "ds_read_b128 v[228:231], v234 offset:20480\n"
      "s_add_u32 s78, s64, 0x27000\n"
      "s_addc_u32 s79, s65, 0\n"
      "s_add_u32 m0, s74, 0xc00\n"
      "v_mfma_f32_16x16x32_bf16 v[60:63], a[60:63], v[216:219], v[60:63]\n"
      "global_load_lds_dwordx4 v240, s[78:79]\n"
      "v_mfma_f32_16x16x32_bf16 v[64:67], a[64:67], v[216:219], v[64:67]\n"
      "v_mfma_f32_16x16x32_bf16 v[68:71], a[68:71], v[216:219], v[68:71]\n"
      "s_waitcnt lgkmcnt(2)\n"
      "v_mfma_f32_16x16x32_bf16 v[72:75], a[48:51], v[220:223], v[72:75]\n"
      "s_add_u32 m0, s74, 0x1000\n"
      "v_mfma_f32_16x16x32_bf16 v[76:79], a[52:55], v[220:223], v[76:79]\n"
      "global_load_lds_dwordx4 v241, s[78:79]\n"
      "v_mfma_f32_16x16x32_bf16 v[80:83], a[56:59], v[220:223], v[80:83]\n"
      "ds_read_b128 v[216:219], v234 offset:24576\n"
      "v_mfma_f32_16x16x32_bf16 v[84:87], a[60:63], v[220:223], v[84:87]\n"
      "v_mfma_f32_16x16x32_bf16 v[88:91], a[64:67], v[220:223], v[88:91]\n"
      "s_add_u32 m0, s74, 0x1400\n"
      "v_mfma_f32_16x16x32_bf16 v[92:95], a[68:71], v[220:223], v[92:95]\n"
      "global_load_lds_dwordx4 v242, s[78:79]\n"
      "s_waitcnt lgkmcnt(2)\n"
      "v_mfma_f32_16x16x32_bf16 v[96:99], a[48:51], v[224:227], v[96:99]\n"
      "v_mfma_f32_16x16x32_bf16 v[100:103], a[52:55], v[224:227], v[100:103]\n"
      "v_mfma_f32_16x16x32_bf16 v[104:107], a[56:59], v[224:227], v[104:107]\n"
      "ds_read_b128 v[220:223], v234 offset:28672\n"
      "s_add_u32 s78, s64, 0x2a000\n"
      "s_addc_u32 s79, s65, 0\n"
      "s_add_u32 m0, s74, 0x6000\n"
      "v_mfma_f32_16x16x32_bf16 v[108:111], a[60:63], v[224:227], v[108:111]\n"
      "global_load_lds_dwordx4 v240, s[78:79]\n"
      "v_mfma_f32_16x16x32_bf16 v[112:115], a[64:67], v[224:227], v[112:115]\n"
      "v_mfma_f32_16x16x32_bf16 v[116:119], a[68:71], v[224:227], v[116:119]\n"
      "s_waitcnt lgkmcnt(2)\n"
      "v_mfma_f32_16x16x32_bf16 v[120:123], a[48:51], v[228:231], v[120:123]\n"
      "s_add_u32 m0, s74, 0x6400\n"
      "v_mfma_f32_16x16x32_bf16 v[124:127], a[52:55], v[228:231], v[124:127]\n"
      "global_load_lds_dwordx4 v241, s[78:79]\n"
      "v_mfma_f32_16x16x32_bf16 v[128:131], a[56:59], v[228:231], v[128:131]\n"
      "ds_read_b128 v[224:227], v234 offset:32768\n"
      "v_add_u32 v234, 0x9000, v234\n"
      "v_mfma_f32_16x16x32_bf16 v[132:135], a[60:63], v[228:231], v[132:135]\n"
      "v_mfma_f32_16x16x32_bf16 v[136:139], a[64:67], v[228:231], v[136:139]\n"
      "s_waitcnt vmcnt(8)\n"
      "s_add_u32 m0, s74, 0x6800\n"
      "v_mfma_f32_16x16x32_bf16 v[140:143], a[68:71], v[228:231], v[140:143]\n"
      "global_load_lds_dwordx4 v242, s[78:79]\n"
      "s_waitcnt lgkmcnt(2)\n"
      "v_mfma_f32_16x16x32_bf16 v[144:147], a[48:51], v[216:219], v[144:147]\n"
      "ds_read_b64_tr_b16 a[120:121], v243 offset:49152\n"
      "v_mfma_f32_16x16x32_bf16 v[148:151], a[52:55], v[216:219], v[148:151]\n"
      "ds_read_b64_tr_b16 a[122:123], v243 offset:49920\n"
      "v_mfma_f32_16x16x32_bf16 v[152:155], a[56:59], v[216:219], v[152:155]\n"
      "ds_read_b128 v[228:231], v235 offset:0\n"
      "s_add_u32 s78, s64, 0x2d000\n"
      "s_addc_u32 s79, s65, 0\n"
      "s_add_u32 m0, s74, 0x6c00\n"
      "v_mfma_f32_16x16x32_bf16 v[156:159], a[60:63], v[216:219], v[156:159]\n"
      "ds_read_b64_tr_b16 a[124:125], v243 offset:49160\n"
      "global_load_lds_dwordx4 v240, s[78:79]\n"
      "v_mfma_f32_16x16x32_bf16 v[160:163], a[64:67], v[216:219], v[160:163]\n"
      "ds_read_b64_tr_b16 a[126:127], v243 offset:49928\n"
      "v_mfma_f32_16x16x32_bf16 v[164:167], a[68:71], v[216:219], v[164:167]\n"
      "s_waitcnt lgkmcnt(6)\n"
      "v_mfma_f32_16x16x32_bf16 v[168:171], a[48:51], v[220:223], v[168:171]\n"
      "ds_read_b64_tr_b16 a[128:129], v243 offset:49216\n"
      "s_add_u32 m0, s74, 0x7000\n"
      "v_mfma_f32_16x16x32_bf16 v[172:175], a[52:55], v[220:223], v[172:175]\n"
      "ds_read_b64_tr_b16 a[130:131], v243 offset:49984\n"
      "global_load_lds_dwordx4 v241, s[78:79]\n"
      "v_mfma_f32_16x16x32_bf16 v[176:179], a[56:59], v[220:223], v[176:179]\n"
      "ds_read_b128 v[216:219], v235 offset:4096\n"
      "v_mfma_f32_16x16x32_bf16 v[180:183], a[60:63], v[220:223], v[180:183]\n"
      "ds_read_b64_tr_b16 a[132:133], v243 offset:49224\n"
      "v_mfma_f32_16x16x32_bf16 v[184:187], a[64:67], v[220:223], v[184:187]\n"
      "ds_read_b64_tr_b16 a[134:135], v243 offset:49992\n"
      "s_add_u32 m0, s74, 0x7400\n"
      "v_mfma_f32_16x16x32_bf16 v[188:191], a[68:71], v[220:223], v[188:191]\n"
      "global_load_lds_dwordx4 v242, s[78:79]\n"
      "s_waitcnt lgkmcnt(10)\n"
      "v_mfma_f32_16x16x32_bf16 v[192:195], a[48:51], v[224:227], v[192:195]\n"
      "ds_read_b64_tr_b16 a[136:137], v243 offset:49280\n"
      "v_mfma_f32_16x16x32_bf16 v[196:199], a[52:55], v[224:227], v[196:199]\n"
      "ds_read_b64_tr_b16 a[138:139], v243 offset:50048\n"
      "v_mfma_f32_16x16x32_bf16 v[200:203], a[56:59], v[224:227], v[200:203]\n"
      "ds_read_b128 v[220:223], v235 offset:8192\n"
      "global_load_dwordx4 v[244:247], v248, s[28:29] sc1\n"
      "v_mfma_f32_16x16x32_bf16 v[204:207], a[60:63], v[224:227], v[204:207]\n"
      "ds_read_b64_tr_b16 a[140:141], v243 offset:49288\n"
      "v_mfma_f32_16x16x32_bf16 v[208:211], a[64:67], v[224:227], v[208:211]\n"
      "ds_read_b64_tr_b16 a[142:143], v243 offset:50056\n"
      "v_mfma_f32_16x16x32_bf16 v[212:215], a[68:71], v[224:227], v[212:215]\n"
      "s_waitcnt lgkmcnt(12)\n"
      "v_mfma_f32_16x16x32_bf16 v[0:3], a[72:75], v[228:231], v[0:3]\n"
      "v_mfma_f32_16x16x32_bf16 v[4:7], a[76:79], v[228:231], v[4:7]\n"
      "v_mfma_f32_16x16x32_bf16 v[8:11], a[80:83], v[228:231], v[8:11]\n"
      "ds_read_b128 v[224:227], v235 offset:12288\n"
      "v_mfma_f32_16x16x32_bf16 v[12:15], a[84:87], v[228:231], v[12:15]\n"
      "v_mfma_f32_16x16x32_bf16 v[16:19], a[88:91], v[228:231], v[16:19]\n"
      "s_waitcnt vmcnt(7)\n"
      "v_mfma_f32_16x16x32_bf16 v[20:23], a[92:95], v[228:231], v[20:23]\n"
      "s_waitcnt lgkmcnt(8)\n"
      "v_mfma_f32_16x16x32_bf16 v[24:27], a[72:75], v[216:219], v[24:27]\n"
      "ds_read_b64_tr_b16 a[144:145], v243 offset:0\n"
      "v_mfma_f32_16x16x32_bf16 v[28:31], a[76:79], v[216:219], v[28:31]\n"
      "ds_read_b64_tr_b16 a[146:147], v243 offset:768\n"
      "v_mfma_f32_16x16x32_bf16 v[32:35], a[80:83], v[216:219], v[32:35]\n"
      "ds_read_b128 v[228:231], v235 offset:16384\n"
      "s_waitcnt lgkmcnt(4)\n"
      "s_add_u32 s78, s64, 0x30000\n"
      "s_addc_u32 s79, s65, 0\n"
      "s_add_u32 m0, s74, 0xc000\n"
      "v_mfma_f32_16x16x32_bf16 v[36:39], a[84:87], v[216:219], v[36:39]\n"
      "ds_read_b64_tr_b16 a[148:149], v243 offset:8\n"
      "global_load_lds_dwordx4 v240, s[78:79]\n"
      "v_mfma_f32_16x16x32_bf16 v[40:43], a[88:91], v[216:219], v[40:43]\n"
      "ds_read_b64_tr_b16 a[150:151], v243 offset:776\n"
      "v_mfma_f32_16x16x32_bf16 v[44:47], a[92:95], v[216:219], v[44:47]\n"
      "v_mfma_f32_16x16x32_bf16 v[48:51], a[72:75], v[220:223], v[48:51]\n"
      "ds_read_b64_tr_b16 a[152:153], v243 offset:64\n"
      "s_add_u32 m0, s74, 0xc400\n"
      "v_mfma_f32_16x16x32_bf16 v[52:55], a[76:79], v[220:223], v[52:55]\n"
      "ds_read_b64_tr_b16 a[154:155], v243 offset:832\n"
      "global_load_lds_dwordx4 v241, s[78:79]\n"
      "v_mfma_f32_16x16x32_bf16 v[56:59], a[80:83], v[220:223], v[56:59]\n"
      "ds_read_b128 v[216:219], v235 offset:20480\n"
      "v_mfma_f32_16x16x32_bf16 v[60:63], a[84:87], v[220:223], v[60:63]\n"
      "ds_read_b64_tr_b16 a[156:157], v243 offset:72\n"
      "v_mfma_f32_16x16x32_bf16 v[64:67], a[88:91], v[220:223], v[64:67]\n"
      "ds_read_b64_tr_b16 a[158:159], v243 offset:840\n"
      "s_add_u32 m0, s74, 0xc800\n"
      "v_mfma_f32_16x16x32_bf16 v[68:71], a[92:95], v[220:223], v[68:71]\n"
      "global_load_lds_dwordx4 v242, s[78:79]\n"
      "s_waitcnt lgkmcnt(10)\n"
      "v_mfma_f32_16x16x32_bf16 v[72:75], a[72:75], v[224:227], v[72:75]\n"
      "ds_read_b64_tr_b16 a[160:161], v243 offset:128\n"
      "v_mfma_f32_16x16x32_bf16 v[76:79], a[76:79], v[224:227], v[76:79]\n"
      "ds_read_b64_tr_b16 a[162:163], v243 offset:896\n"
      "v_mfma_f32_16x16x32_bf16 v[80:83], a[80:83], v[224:227], v[80:83]\n"
      "ds_read_b128 v[220:223], v235 offset:24576\n"
      "s_add_u32 s78, s64, 0x33000\n"
      "s_addc_u32 s79, s65, 0\n"
      "s_add_u32 m0, s74, 0xcc00\n"
      "v_mfma_f32_16x16x32_bf16 v[84:87], a[84:87], v[224:227], v[84:87]\n"
      "ds_read_b64_tr_b16 a[164:165], v243 offset:136\n"
      "global_load_lds_dwordx4 v240, s[78:79]\n"
      "v_mfma_f32_16x16x32_bf16 v[88:91], a[88:91], v[224:227], v[88:91]\n"
      "ds_read_b64_tr_b16 a[166:167], v243 offset:904\n"
      "v_mfma_f32_16x16x32_bf16 v[92:95], a[92:95], v[224:227], v[92:95]\n"
      "s_waitcnt lgkmcnt(12)\n"
      "v_mfma_f32_16x16x32_bf16 v[96:99], a[72:75], v[228:231], v[96:99]\n"
      "s_add_u32 m0, s74, 0xd000\n"
      "v_mfma_f32_16x16x32_bf16 v[100:103], a[76:79], v[228:231], v[100:103]\n"
      "global_load_lds_dwordx4 v241, s[78:79]\n"
      "v_mfma_f32_16x16x32_bf16 v[104:107], a[80:83], v[228:231], v[104:107]\n"
      "ds_read_b128 v[224:227], v235 offset:28672\n"
      "v_mfma_f32_16x16x32_bf16 v[108:111], a[84:87], v[228:231], v[108:111]\n"
      "v_mfma_f32_16x16x32_bf16 v[112:115], a[88:91], v[228:231], v[112:115]\n"
      "s_waitcnt vmcnt(6)\n"
      "s_add_u32 m0, s74, 0xd400\n"
      "v_mfma_f32_16x16x32_bf16 v[116:119], a[92:95], v[228:231], v[116:119]\n"
      "global_load_lds_dwordx4 v242, s[78:79]\n"
      "s_waitcnt lgkmcnt(8)\n"
      "v_mfma_f32_16x16x32_bf16 v[120:123], a[72:75], v[216:219], v[120:123]\n"
      "ds_read_b64_tr_b16 a[168:169], v243 offset:24576\n"
      "v_mfma_f32_16x16x32_bf16 v[124:127], a[76:79], v[216:219], v[124:127]\n"
      "ds_read_b64_tr_b16 a[170:171], v243 offset:25344\n"
      "v_mfma_f32_16x16x32_bf16 v[128:131], a[80:83], v[216:219], v[128:131]\n"
      "ds_read_b128 v[228:231], v235 offset:32768\n"
      "v_add_u32 v235, 0x9000, v235\n"
      "s_waitcnt lgkmcnt(4)\n"
      "s_add_u32 s78, s64, 0x36000\n"
      "s_addc_u32 s79, s65, 0\n"
      "s_add_u32 m0, s74, 0x0\n"
      "v_mfma_f32_16x16x32_bf16 v[132:135], a[84:87], v[216:219], v[132:135]\n"
      "ds_read_b64_tr_b16 a[172:173], v243 offset:24584\n"
      "global_load_lds_dwordx4 v240, s[78:79]\n"
      "v_mfma_f32_16x16x32_bf16 v[136:139], a[88:91], v[216:219], v[136:139]\n"
      "ds_read_b64_tr_b16 a[174:175], v243 offset:25352\n"
      "v_mfma_f32_16x16x32_bf16 v[140:143], a[92:95], v[216:219], v[140:143]\n"
      "v_mfma_f32_16x16x32_bf16 v[144:147], a[72:75], v[220:223], v[144:147]\n"
      "ds_read_b64_tr_b16 a[176:177], v243 offset:24640\n"
      "s_waitcnt vmcnt(7)\n"
      "v_cmp_ne_u64 vcc, s[80:81], v[244:245]\n"
      "v_cmp_ne_u64 s[56:57], s[80:81], v[246:247]\n"
      "s_nop 3\n"
      "s_or_b64 vcc, vcc, s[56:57]\n"
      "s_cbranch_scc0 LP1_0\n"
      "s_mov_b32 s82, 0x40000\n"
      "LP0_0:\n"
      "s_sleep 2\n"
      "global_load_dwordx4 v[244:247], v248, s[28:29] sc1\n"
      "s_sub_u32 s82, s82, 1\n"
      "s_cbranch_scc1 LP2_0\n"
      "s_waitcnt vmcnt(0)\n"
      "v_cmp_ne_u64 vcc, s[80:81], v[244:245]\n"
      "v_cmp_ne_u64 s[56:57], s[80:81], v[246:247]\n"
      "s_nop 3\n"
      "s_or_b64 vcc, vcc, s[56:57]\n"
      "s_cbranch_scc1 LP0_0\n"
      "LP2_0:\n"
      "s_waitcnt vmcnt(0)\n"
      "LP1_0:\n"
      "s_nop 0\n"
      "v_mfma_f32_16x16x32_bf16 v[148:151], a[76:79], v[220:223], v[148:151]\n"
      "s_barrier\n"
      "s_add_u32 m0, s74, 0x400\n"
      "v_mfma_f32_16x16x32_bf16 v[152:155], a[80:83], v[220:223], v[152:155]\n"
      "ds_read_b128 v[216:219], v232 offset:0\n"
      "global_load_lds_dwordx4 v241, s[78:79]\n"
      "v_mfma_f32_16x16x32_bf16 v[156:159], a[84:87], v[220:223], v[156:159]\n"
      "ds_read_b64_tr_b16 a[178:179], v243 offset:25408\n"
      "v_mfma_f32_16x16x32_bf16 v[160:163], a[88:91], v[220:223], v[160:163]\n"
      "ds_read_b64_tr_b16 a[180:181], v243 offset:24648\n"
      "v_mfma_f32_16x16x32_bf16 v[164:167], a[92:95], v[220:223], v[164:167]\n"
      "s_add_u32 m0, s74, 0x800\n"
      "s_waitcnt lgkmcnt(9)\n"
      "v_mfma_f32_16x16x32_bf16 v[168:171], a[72:75], v[224:227], v[168:171]\n"
      "ds_read_b64_tr_b16 a[182:183], v243 offset:25416\n"
      "global_load_lds_dwordx4 v242, s[78:79]\n"
      "v_mfma_f32_16x16x32_bf16 v[172:175], a[76:79], v[224:227], v[172:175]\n"
      "ds_read_b64_tr_b16 a[184:185], v243 offset:24704\n"
      "v_mfma_f32_16x16x32_bf16 v[176:179], a[80:83], v[224:227], v[176:179]\n"
      "ds_read_b128 v[220:223], v232 offset:4096\n"
      "v_mfma_f32_16x16x32_bf16 v[180:183], a[84:87], v[224:227], v[180:183]\n"
      "ds_read_b64_tr_b16 a[186:187], v243 offset:25472\n"
      "s_add_u32 s78, s64, 0x39000\n"
      "s_addc_u32 s79, s65, 0\n"
      "s_add_u32 m0, s74, 0xc00\n"
      "v_mfma_f32_16x16x32_bf16 v[184:187], a[88:91], v[224:227], v[184:187]\n"
      "ds_read_b64_tr_b16 a[188:189], v243 offset:24712\n"
      "global_load_lds_dwordx4 v240, s[78:79]\n"
      "v_mfma_f32_16x16x32_bf16 v[188:191], a[92:95], v[224:227], v[188:191]\n"
      "s_waitcnt lgkmcnt(11)\n"
      "v_mfma_f32_16x16x32_bf16 v[192:195], a[72:75], v[228:231], v[192:195]\n"
      "ds_read_b64_tr_b16 a[190:191], v243 offset:25480\n"
      "v_mfma_f32_16x16x32_bf16 v[196:199], a[76:79], v[228:231], v[196:199]\n"
      "s_add_u32 m0, s74, 0x1000\n"
      "v_mfma_f32_16x16x32_bf16 v[200:203], a[80:83], v[228:231], v[200:203]\n"
      "ds_read_b128 v[224:227], v232 offset:8192\n"
      "global_load_lds_dwordx4 v241, s[78:79]\n"
      "v_mfma_f32_16x16x32_bf16 v[204:207], a[84:87], v[228:231], v[204:207]\n"
      "v_mfma_f32_16x16x32_bf16 v[208:211], a[88:91], v[228:231], v[208:211]\n"
      "v_mfma_f32_16x16x32_bf16 v[212:215], a[92:95], v[228:231], v[212:215]\n"
      "s_add_u32 m0, s74, 0x1400\n"
      "s_waitcnt lgkmcnt(9)\n"
      "v_mfma_f32_16x16x32_bf16 v[0:3], a[96:99], v[216:219], v[0:3]\n"
      "global_load_lds_dwordx4 v242, s[78:79]\n"
      "v_mfma_f32_16x16x32_bf16 v[4:7], a[100:103], v[216:219], v[4:7]\n"
      "s_barrier\n"
      "v_mfma_f32_16x16x32_bf16 v[8:11], a[104:107], v[216:219], v[8:11]\n"
      "ds_read_b128 v[228:231], v232 offset:12288\n"
      "v_mfma_f32_16x16x32_bf16 v[12:15], a[108:111], v[216:219], v[12:15]\n"
      "s_add_u32 s58, s24, 0x0\n"
      "s_addc_u32 s59, s25, 0\n"
      "global_load_dwordx4 a[240:243], v239, s[58:59] offset:0 sc1\n"
      "v_mfma_f32_16x16x32_bf16 v[16:19], a[112:115], v[216:219], v[16:19]\n"
      "v_mfma_f32_16x16x32_bf16 v[20:23], a[116:119], v[216:219], v[20:23]\n"
      "s_waitcnt lgkmcnt(5)\n"
      "v_mfma_f32_16x16x32_bf16 v[24:27], a[96:99], v[220:223], v[24:27]\n"
      "v_mfma_f32_16x16x32_bf16 v[28:31], a[100:103], v[220:223], v[28:31]\n"
      "s_waitcnt lgkmcnt(2)\n"
      "s_add_u32 s78, s64, 0x3c000\n"
      "s_addc_u32 s79, s65, 0\n"
      "s_add_u32 m0, s74, 0x6000\n"
      "v_mfma_f32_16x16x32_bf16 v[32:35], a[104:107], v[220:223], v[32:35]\n"
      "ds_read_b128 v[216:219], v232 offset:16384\n"
      "LPD1:\n"
      "s_cbranch_execz LPD8\n"
      "global_load_lds_dwordx4 v240, s[78:79]\n"
      "v_mfma_f32_16x16x32_bf16 v[36:39], a[108:111], v[220:223], v[36:39]\n"
      "v_mfma_f32_16x16x32_bf16 v[40:43], a[112:115], v[220:223], v[40:43]\n"
      "s_waitcnt vmcnt(8)\n"
      "v_mfma_f32_16x16x32_bf16 v[44:47], a[116:119], v[220:223], v[44:47]\n"
      "s_add_u32 m0, s74, 0x6400\n"
      "s_waitcnt lgkmcnt(2)\n"
      "v_mfma_f32_16x16x32_bf16 v[48:51], a[96:99], v[224:227], v[48:51]\n"
      "ds_read_b64_tr_b16 a[192:193], v243 offset:49152\n"
      "global_load_lds_dwordx4 v241, s[78:79]\n"
      "v_mfma_f32_16x16x32_bf16 v[52:55], a[100:103], v[224:227], v[52:55]\n"
      "ds_read_b64_tr_b16 a[194:195], v243 offset:49920\n"
      "v_mfma_f32_16x16x32_bf16 v[56:59], a[104:107], v[224:227], v[56:59]\n"
      "ds_read_b128 v[220:223], v232 offset:20480\n"
      "v_mfma_f32_16x16x32_bf16 v[60:63], a[108:111], v[224:227], v[60:63]\n"
      "ds_read_b64_tr_b16 a[196:197], v243 offset:49160\n"
      "s_add_u32 m0, s74, 0x6800\n"
      "v_mfma_f32_16x16x32_bf16 v[64:67], a[112:115], v[224:227], v[64:67]\n"
      "ds_read_b64_tr_b16 a[198:199], v243 offset:49928\n"
      "global_load_lds_dwordx4 v242, s[78:79]\n"
      "v_mfma_f32_16x16x32_bf16 v[68:71], a[116:119], v[224:227], v[68:71]\n"
      "s_waitcnt lgkmcnt(6)\n"
      "v_mfma_f32_16x16x32_bf16 v[72:75], a[96:99], v[228:231], v[72:75]\n"
      "ds_read_b64_tr_b16 a[200:201], v243 offset:49216\n"
      "v_mfma_f32_16x16x32_bf16 v[76:79], a[100:103], v[228:231], v[76:79]\n"
      "ds_read_b64_tr_b16 a[202:203], v243 offset:49984\n"
      "s_add_u32 s78, s64, 0x3f000\n"
      "s_addc_u32 s79, s65, 0\n"
      "s_add_u32 m0, s74, 0x6c00\n"
      "v_mfma_f32_16x16x32_bf16 v[80:83], a[104:107], v[228:231], v[80:83]\n"
      "ds_read_b128 v[224:227], v232 offset:24576\n"
      "global_load_lds_dwordx4 v240, s[78:79]\n"
      "v_mfma_f32_16x16x32_bf16 v[84:87], a[108:111], v[228:231], v[84:87]\n"
      "ds_read_b64_tr_b16 a[204:205], v243 offset:49224\n"
      "v_mfma_f32_16x16x32_bf16 v[88:91], a[112:115], v[228:231], v[88:91]\n"
      "ds_read_b64_tr_b16 a[206:207], v243 offset:49992\n"
      "v_mfma_f32_16x16x32_bf16 v[92:95], a[116:119], v[228:231], v[92:95]\n"
      "s_add_u32 m0, s74, 0x7000\n"
      "s_waitcnt lgkmcnt(10)\n"
      "v_mfma_f32_16x16x32_bf16 v[96:99], a[96:99], v[216:219], v[96:99]\n"
      "ds_read_b64_tr_b16 a[208:209], v243 offset:49280\n"
      "global_load_lds_dwordx4 v241, s[78:79]\n"
      "v_mfma_f32_16x16x32_bf16 v[100:103], a[100:103], v[216:219], v[100:103]\n"
      "ds_read_b64_tr_b16 a[210:211], v243 offset:50048\n"
      "v_mfma_f32_16x16x32_bf16 v[104:107], a[104:107], v[216:219], v[104:107]\n"
      "ds_read_b128 v[228:231], v232 offset:28672\n"
      "v_mfma_f32_16x16x32_bf16 v[108:111], a[108:111], v[216:219], v[108:111]\n"
      "ds_read_b64_tr_b16 a[212:213], v243 offset:49288\n"
      "s_add_u32 m0, s74, 0x7400\n"
      "v_mfma_f32_16x16x32_bf16 v[112:115], a[112:115], v[216:219], v[112:115]\n"
      "ds_read_b64_tr_b16 a[214:215], v243 offset:50056\n"
      "global_load_lds_dwordx4 v242, s[78:79]\n"
      "v_mfma_f32_16x16x32_bf16 v[116:119], a[116:119], v[216:219], v[116:119]\n"
      "s_waitcnt lgkmcnt(12)\n"
      "v_mfma_f32_16x16x32_bf16 v[120:123], a[96:99], v[220:223], v[120:123]\n"
      "v_mfma_f32_16x16x32_bf16 v[124:127], a[100:103], v[220:223], v[124:127]\n"
      "global_load_dwordx4 a[244:247], v239, s[58:59] offset:1024 sc1\n"
      "v_mfma_f32_16x16x32_bf16 v[128:131], a[104:107], v[220:223], v[128:131]\n"
      "ds_read_b128 v[216:219], v232 offset:32768\n"
      "v_add_u32 v232, 0xffff7000, v232\n"
      "v_mfma_f32_16x16x32_bf16 v[132:135], a[108:111], v[220:223], v[132:135]\n"
      "v_mfma_f32_16x16x32_bf16 v[136:139], a[112:115], v[220:223], v[136:139]\n"
      "v_mfma_f32_16x16x32_bf16 v[140:143], a[116:119], v[220:223], v[140:143]\n"
      "global_load_dwordx4 a[248:251], v239, s[58:59] offset:2048 sc1\n"
      "s_waitcnt lgkmcnt(8)\n"
      "v_mfma_f32_16x16x32_bf16 v[144:147], a[96:99], v[224:227], v[144:147]\n"
      "s_waitcnt vmcnt(9)\n"
      "v_mfma_f32_16x16x32_bf16 v[148:151], a[100:103], v[224:227], v[148:151]\n"
      "ds_read_b64_tr_b16 a[216:217], v243 offset:0\n"
      "v_mfma_f32_16x16x32_bf16 v[152:155], a[104:107], v[224:227], v[152:155]\n"
      "ds_read_b128 v[220:223], v233 offset:0\n"
      "v_mfma_f32_16x16x32_bf16 v[156:159], a[108:111], v[224:227], v[156:159]\n"
      "ds_read_b64_tr_b16 a[218:219], v243 offset:768\n"
      "s_waitcnt lgkmcnt(4)\n"
      "s_add_u32 s78, s64, 0x42000\n"
      "s_addc_u32 s79, s65, 0\n"
      "s_add_u32 m0, s74, 0xc000\n"
      "v_mfma_f32_16x16x32_bf16 v[160:163], a[112:115], v[224:227], v[160:163]\n"
      "ds_read_b64_tr_b16 a[220:221], v243 offset:8\n"
      "global_load_lds_dwordx4 v240, s[78:79]\n"
      "v_mfma_f32_16x16x32_bf16 v[164:167], a[116:119], v[224:227], v[164:167]\n"
      "v_mfma_f32_16x16x32_bf16 v[168:171], a[96:99], v[228:231], v[168:171]\n"
      "ds_read_b64_tr_b16 a[222:223], v243 offset:776\n"
      "v_mfma_f32_16x16x32_bf16 v[172:175], a[100:103], v[228:231], v[172:175]\n"
      "ds_read_b64_tr_b16 a[224:225], v243 offset:64\n"
      "s_add_u32 m0, s74, 0xc400\n"
      "v_mfma_f32_16x16x32_bf16 v[176:179], a[104:107], v[228:231], v[176:179]\n"
      "ds_read_b128 v[224:227], v233 offset:4096\n"
      "global_load_lds_dwordx4 v241, s[78:79]\n"
      "v_mfma_f32_16x16x32_bf16 v[180:183], a[108:111], v[228:231], v[180:183]\n"
      "ds_read_b64_tr_b16 a[226:227], v243 offset:832\n"
      "v_mfma_f32_16x16x32_bf16 v[184:187], a[112:115], v[228:231], v[184:187]\n"
      "ds_read_b64_tr_b16 a[228:229], v243 offset:72\n"
      "v_mfma_f32_16x16x32_bf16 v[188:191], a[116:119], v[228:231], v[188:191]\n"
      "s_add_u32 m0, s74, 0xc800\n"
      "s_waitcnt lgkmcnt(9)\n"
      "v_mfma_f32_16x16x32_bf16 v[192:195], a[96:99], v[216:219], v[192:195]\n"
      "ds_read_b64_tr_b16 a[230:231], v243 offset:840\n"
      "global_load_lds_dwordx4 v242, s[78:79]\n"
      "v_mfma_f32_16x16x32_bf16 v[196:199], a[100:103], v[216:219], v[196:199]\n"
      "ds_read_b64_tr_b16 a[232:233], v243 offset:128\n"
      "v_mfma_f32_16x16x32_bf16 v[200:203], a[104:107], v[216:219], v[200:203]\n"
      "ds_read_b128 v[228:231], v233 offset:8192\n"
      "v_mfma_f32_16x16x32_bf16 v[204:207], a[108:111], v[216:219], v[204:207]\n"
      "ds_read_b64_tr_b16 a[234:235], v243 offset:896\n"
      "s_add_u32 s78, s64, 0x45000\n"
      "s_addc_u32 s79, s65, 0\n"
      "s_add_u32 m0, s74, 0xcc00\n"
      "v_mfma_f32_16x16x32_bf16 v[208:211], a[112:115], v[216:219], v[208:211]\n"
      "ds_read_b64_tr_b16 a[236:237], v243 offset:136\n"
      "global_load_lds_dwordx4 v240, s[78:79]\n"
      "v_mfma_f32_16x16x32_bf16 v[212:215], a[116:119], v[216:219], v[212:215]\n"
      "s_waitcnt lgkmcnt(12)\n"
      "v_mfma_f32_16x16x32_bf16 v[0:3], a[120:123], v[220:223], v[0:3]\n"
      "ds_read_b64_tr_b16 a[238:239], v243 offset:904\n"
      "v_mfma_f32_16x16x32_bf16 v[4:7], a[124:127], v[220:223], v[4:7]\n"
      "s_add_u32 m0, s74, 0xd000\n"
      "v_mfma_f32_16x16x32_bf16 v[8:11], a[128:131], v[220:223], v[8:11]\n"
      "ds_read_b128 v[216:219], v233 offset:12288\n"
      "global_load_lds_dwordx4 v241, s[78:79]\n"
      "v_mfma_f32_16x16x32_bf16 v[12:15], a[132:135], v[220:223], v[12:15]\n"
      "v_mfma_f32_16x16x32_bf16 v[16:19], a[136:139], v[220:223], v[16:19]\n"
      "v_mfma_f32_16x16x32_bf16 v[20:23], a[140:143], v[220:223], v[20:23]\n"
      "s_add_u32 m0, s74, 0xd400\n"
      "s_waitcnt lgkmcnt(9)\n"
      "v_mfma_f32_16x16x32_bf16 v[24:27], a[120:123], v[224:227], v[24:27]\n"
      "global_load_lds_dwordx4 v242, s[78:79]\n"
      "v_mfma_f32_16x16x32_bf16 v[28:31], a[124:127], v[224:227], v[28:31]\n"
      "v_mfma_f32_16x16x32_bf16 v[32:35], a[128:131], v[224:227], v[32:35]\n"
      "ds_read_b128 v[220:223], v233 offset:16384\n"
      "v_mfma_f32_16x16x32_bf16 v[36:39], a[132:135], v[224:227], v[36:39]\n"
      "s_waitcnt vmcnt(8)\n"
      "v_mad_i32_i24 v238, v251, s89, v238\n"
      "s_add_u32 s56, s22, 0x0\n"
      "s_addc_u32 s57, s23, 0\n"
      "s_add_u32 m0, s18, 0xfffffe00\n"
      "v_mfma_f32_16x16x32_bf16 v[40:43], a[136:139], v[224:227], v[40:43]\n"
      "ds_read_b64_tr_b16 a[0:1], v243 offset:24576\n"
      "global_load_lds_dwordx4 v238, s[56:57] offset:512 \n"
      "v_mfma_f32_16x16x32_bf16 v[44:47], a[140:143], v[224:227], v[44:47]\n"
      "s_waitcnt lgkmcnt(6)\n"
      "v_mfma_f32_16x16x32_bf16 v[48:51], a[120:123], v[228:231], v[48:51]\n"
      "ds_read_b64_tr_b16 a[2:3], v243 offset:25344\n"
      "v_mad_i32_i24 v238, v251, s90, v238\n"
      "s_add_u32 s56, s22, 0x6000\n"
      "s_addc_u32 s57, s23, 0\n"
      "s_add_u32 m0, s18, 0xe00\n"
      "v_mfma_f32_16x16x32_bf16 v[52:55], a[124:127], v[228:231], v[52:55]\n"
      "ds_read_b64_tr_b16 a[4:5], v243 offset:24584\n"
      "global_load_lds_dwordx4 v238, s[56:57] offset:512 \n"
      "v_mfma_f32_16x16x32_bf16 v[56:59], a[128:131], v[228:231], v[56:59]\n"
      "ds_read_b128 v[224:227], v233 offset:20480\n"
      "v_mfma_f32_16x16x32_bf16 v[60:63], a[132:135], v[228:231], v[60:63]\n"
      "ds_read_b64_tr_b16 a[6:7], v243 offset:25352\n"
      "v_mad_i32_i24 v238, v251, s91, v238\n"
      "s_add_u32 s56, s22, 0xc000\n"
      "s_addc_u32 s57, s23, 0\n"
      "s_add_u32 m0, s18, 0x1e00\n"
      "v_mfma_f32_16x16x32_bf16 v[64:67], a[136:139], v[228:231], v[64:67]\n"
      "ds_read_b64_tr_b16 a[8:9], v243 offset:24640\n"
      "global_load_lds_dwordx4 v238, s[56:57] offset:512 \n"
      "v_mfma_f32_16x16x32_bf16 v[68:71], a[140:143], v[228:231], v[68:71]\n"
      "s_waitcnt lgkmcnt(7)\n"
      "v_mfma_f32_16x16x32_bf16 v[72:75], a[120:123], v[216:219], v[72:75]\n"
      "ds_read_b64_tr_b16 a[10:11], v243 offset:25408\n"
      "v_mad_i32_i24 v238, v251, s92, v238\n"
      "s_add_u32 s56, s22, 0x12000\n"
      "s_addc_u32 s57, s23, 0\n"
      "s_add_u32 m0, s18, 0x2e00\n"
      "v_mfma_f32_16x16x32_bf16 v[76:79], a[124:127], v[216:219], v[76:79]\n"
      "ds_read_b64_tr_b16 a[12:13], v243 offset:24648\n"
      "global_load_lds_dwordx4 v238, s[56:57] offset:512 \n"
      "v_mfma_f32_16x16x32_bf16 v[80:83], a[128:131], v[216:219], v[80:83]\n"
      "ds_read_b128 v[228:231], v233 offset:24576\n"
      "v_mfma_f32_16x16x32_bf16 v[84:87], a[132:135], v[216:219], v[84:87]\n"
      "ds_read_b64_tr_b16 a[14:15], v243 offset:25416\n"
      "v_mad_i32_i24 v238, v251, s93, v238\n"
      "s_add_u32 s56, s22, 0x18000\n"
      "s_addc_u32 s57, s23, 0\n"
      "s_add_u32 m0, s18, 0x3e00\n"
      "v_mfma_f32_16x16x32_bf16 v[88:91], a[136:139], v[216:219], v[88:91]\n"
      "ds_read_b64_tr_b16 a[16:17], v243 offset:24704\n"
      "global_load_lds_dwordx4 v238, s[56:57] offset:512 \n"
      "v_mfma_f32_16x16x32_bf16 v[92:95], a[140:143], v[216:219], v[92:95]\n"
      "s_waitcnt lgkmcnt(11)\n"
      "v_mfma_f32_16x16x32_bf16 v[96:99], a[120:123], v[220:223], v[96:99]\n"
      "ds_read_b64_tr_b16 a[18:19], v243 offset:25472\n"
      "v_mad_i32_i24 v238, v251, s94, v238\n"
      "s_add_u32 s56, s22, 0x1e000\n"
      "s_addc_u32 s57, s23, 0\n"
      "s_add_u32 m0, s18, 0x4e00\n"
      "v_mfma_f32_16x16x32_bf16 v[100:103], a[124:127], v[220:223], v[100:103]\n"
      "ds_read_b64_tr_b16 a[20:21], v243 offset:24712\n"
      "global_load_lds_dwordx4 v238, s[56:57] offset:512 \n"
      "v_mfma_f32_16x16x32_bf16 v[104:107], a[128:131], v[220:223], v[104:107]\n"
      "ds_read_b128 v[216:219], v233 offset:28672\n"
      "v_mfma_f32_16x16x32_bf16 v[108:111], a[132:135], v[220:223], v[108:111]\n"
      "ds_read_b64_tr_b16 a[22:23], v243 offset:25480\n"
      "v_mad_i32_i24 v238, v251, s95, v238\n"
      "s_add_u32 s56, s22, 0x24000\n"
      "s_addc_u32 s57, s23, 0\n"
      "s_add_u32 m0, s18, 0x5e00\n"
      "v_mfma_f32_16x16x32_bf16 v[112:115], a[136:139], v[220:223], v[112:115]\n"
      "global_load_lds_dwordx4 v238, s[56:57] offset:512 \n"
      "v_mfma_f32_16x16x32_bf16 v[116:119], a[140:143], v[220:223], v[116:119]\n"
      "s_waitcnt lgkmcnt(11)\n"
      "v_mfma_f32_16x16x32_bf16 v[120:123], a[120:123], v[224:227], v[120:123]\n"
      "v_mad_i32_i24 v238, v251, s96, v238\n"
      "s_add_u32 s56, s22, 0x2a000\n"
      "s_addc_u32 s57, s23, 0\n"
      "s_add_u32 m0, s18, 0x6e00\n"
      "v_mfma_f32_16x16x32_bf16 v[124:127], a[124:127], v[224:227], v[124:127]\n"
      "global_load_lds_dwordx4 v238, s[56:57] offset:512 \n"
      "v_mfma_f32_16x16x32_bf16 v[128:131], a[128:131], v[224:227], v[128:131]\n"
      "ds_read_b128 v[220:223], v233 offset:32768\n"
      "v_add_u32 v233, 0xffff7000, v233\n"
      "v_mfma_f32_16x16x32_bf16 v[132:135], a[132:135], v[224:227], v[132:135]\n"
      "v_mad_i32_i24 v238, v251, s97, v238\n"
      "s_add_u32 s56, s22, 0x30000\n"
      "s_addc_u32 s57, s23, 0\n"
      "s_add_u32 m0, s18, 0x7e00\n"
      "v_mfma_f32_16x16x32_bf16 v[136:139], a[136:139], v[224:227], v[136:139]\n"
      "global_load_lds_dwordx4 v238, s[56:57] offset:512 \n"
      "v_mfma_f32_16x16x32_bf16 v[140:143], a[140:143], v[224:227], v[140:143]\n"
      "s_waitcnt lgkmcnt(7)\n"
      "v_mfma_f32_16x16x32_bf16 v[144:147], a[120:123], v[228:231], v[144:147]\n"
      "v_mfma_f32_16x16x32_bf16 v[148:151], a[124:127], v[228:231], v[148:151]\n"
      "global_load_dwordx4 a[252:255], v239, s[58:59] offset:3072 sc1\n"
      "v_mfma_f32_16x16x32_bf16 v[152:155], a[128:131], v[228:231], v[152:155]\n"
      "ds_read_b128 v[224:227], v234 offset:0\n"
      "v_mfma_f32_16x16x32_bf16 v[156:159], a[132:135], v[228:231], v[156:159]\n"
      "v_mfma_f32_16x16x32_bf16 v[160:163], a[136:139], v[228:231], v[160:163]\n"
      "v_mfma_f32_16x16x32_bf16 v[164:167], a[140:143], v[228:231], v[164:167]\n"
      "s_add_u32 s58, s24, 0x1000\n"
      "s_addc_u32 s59, s25, 0\n"
      "global_load_dwordx4 a[48:51], v239, s[58:59] offset:0 sc1\n"
      "s_waitcnt lgkmcnt(3)\n"
      "v_mfma_f32_16x16x32_bf16 v[168:171], a[120:123], v[216:219], v[168:171]\n"
      "s_waitcnt vmcnt(11)\n"
      "v_mfma_f32_16x16x32_bf16 v[172:175], a[124:127], v[216:219], v[172:175]\n"
      "ds_read_b64_tr_b16 a[24:25], v243 offset:49152\n"
      "v_mfma_f32_16x16x32_bf16 v[176:179], a[128:131], v[216:219], v[176:179]\n"
      "ds_read_b128 v[228:231], v234 offset:4096\n"
      "v_mfma_f32_16x16x32_bf16 v[180:183], a[132:135], v[216:219], v[180:183]\n"
      "ds_read_b64_tr_b16 a[26:27], v243 offset:49920\n"
      "global_load_dwordx4 a[52:55], v239, s[58:59] offset:1024 sc1\n"
      "v_mfma_f32_16x16x32_bf16 v[184:187], a[136:139], v[216:219], v[184:187]\n"
      "ds_read_b64_tr_b16 a[28:29], v243 offset:49160\n"
      "v_mfma_f32_16x16x32_bf16 v[188:191], a[140:143], v[216:219], v[188:191]\n"
      "s_waitcnt lgkmcnt(5)\n"
      "v_mfma_f32_16x16x32_bf16 v[192:195], a[120:123], v[220:223], v[192:195]\n"
      "ds_read_b64_tr_b16 a[30:31], v243 offset:49928\n"
      "v_mfma_f32_16x16x32_bf16 v[196:199], a[124:127], v[220:223], v[196:199]\n"
      "ds_read_b64_tr_b16 a[32:33], v243 offset:49216\n"
      "global_load_dwordx4 a[56:59], v239, s[58:59] offset:2048 sc1\n"
      "v_mfma_f32_16x16x32_bf16 v[200:203], a[128:131], v[220:223], v[200:203]\n"
      "ds_read_b128 v[216:219], v234 offset:8192\n"
      "v_mfma_f32_16x16x32_bf16 v[204:207], a[132:135], v[220:223], v[204:207]\n"
      "ds_read_b64_tr_b16 a[34:35], v243 offset:49984\n"
      "v_mfma_f32_16x16x32_bf16 v[208:211], a[136:139], v[220:223], v[208:211]\n"
      "ds_read_b64_tr_b16 a[36:37], v243 offset:49224\n"
      "v_mfma_f32_16x16x32_bf16 v[212:215], a[140:143], v[220:223], v[212:215]\n"
      "global_load_dwordx4 a[60:63], v239, s[58:59] offset:3072 sc1\n"
      "s_waitcnt lgkmcnt(9)\n"
      "v_mfma_f32_16x16x32_bf16 v[0:3], a[144:147], v[224:227], v[0:3]\n"
      "ds_read_b64_tr_b16 a[38:39], v243 offset:49992\n"
      "v_mfma_f32_16x16x32_bf16 v[4:7], a[148:151], v[224:227], v[4:7]\n"
      "ds_read_b64_tr_b16 a[40:41], v243 offset:49280\n"
      "v_mfma_f32_16x16x32_bf16 v[8:11], a[152:155], v[224:227], v[8:11]\n"
      "ds_read_b128 v[220:223], v234 offset:12288\n"
      "v_mfma_f32_16x16x32_bf16 v[12:15], a[156:159], v[224:227], v[12:15]\n"
      "ds_read_b64_tr_b16 a[42:43], v243 offset:50048\n"
      "s_add_u32 s58, s24, 0x2000\n"
      "s_addc_u32 s59, s25, 0\n"
      "global_load_dwordx4 a[64:67], v239, s[58:59] offset:0 sc1\n"
      "v_mfma_f32_16x16x32_bf16 v[16:19], a[160:163], v[224:227], v[16:19]\n"
      "ds_read_b64_tr_b16 a[44:45], v243 offset:49288\n"
      "v_mfma_f32_16x16x32_bf16 v[20:23], a[164:167], v[224:227], v[20:23]\n"
      "s_waitcnt lgkmcnt(12)\n"
      "v_mfma_f32_16x16x32_bf16 v[24:27], a[144:147], v[228:231], v[24:27]\n"
      "ds_read_b64_tr_b16 a[46:47], v243 offset:50056\n"
      "v_mfma_f32_16x16x32_bf16 v[28:31], a[148:151], v[228:231], v[28:31]\n"
      "global_load_dwordx4 a[68:71], v239, s[58:59] offset:1024 sc1\n"
      "v_mfma_f32_16x16x32_bf16 v[32:35], a[152:155], v[228:231], v[32:35]\n"
      "ds_read_b128 v[224:227], v234 offset:16384\n"
      "v_mfma_f32_16x16x32_bf16 v[36:39], a[156:159], v[228:231], v[36:39]\n"
      "v_mfma_f32_16x16x32_bf16 v[40:43], a[160:163], v[228:231], v[40:43]\n"
      "v_mfma_f32_16x16x32_bf16 v[44:47], a[164:167], v[228:231], v[44:47]\n"
      "global_load_dwordx4 a[72:75], v239, s[58:59] offset:2048 sc1\n"
      "s_waitcnt lgkmcnt(9)\n"
      "v_mfma_f32_16x16x32_bf16 v[48:51], a[144:147], v[216:219], v[48:51]\n"
      "v_mfma_f32_16x16x32_bf16 v[52:55], a[148:151], v[216:219], v[52:55]\n"
      "v_mfma_f32_16x16x32_bf16 v[56:59], a[152:155], v[216:219], v[56:59]\n"
      "ds_read_b128 v[228:231], v234 offset:20480\n"
      "v_mfma_f32_16x16x32_bf16 v[60:63], a[156:159], v[216:219], v[60:63]\n"
      "global_load_dwordx4 a[76:79], v239, s[58:59] offset:3072 sc1\n"
      "v_mfma_f32_16x16x32_bf16 v[64:67], a[160:163], v[216:219], v[64:67]\n"
      "v_mfma_f32_16x16x32_bf16 v[68:71], a[164:167], v[216:219], v[68:71]\n"
      "s_waitcnt lgkmcnt(5)\n"
      "v_mfma_f32_16x16x32_bf16 v[72:75], a[144:147], v[220:223], v[72:75]\n"
      "v_mfma_f32_16x16x32_bf16 v[76:79], a[148:151], v[220:223], v[76:79]\n"
      "s_add_u32 s58, s24, 0x3000\n"
      "s_addc_u32 s59, s25, 0\n"
      "global_load_dwordx4 a[80:83], v239, s[58:59] offset:0 sc1\n"
      "v_mfma_f32_16x16x32_bf16 v[80:83], a[152:155], v[220:223], v[80:83]\n"
      "ds_read_b128 v[216:219], v234 offset:24576\n"
      "v_mfma_f32_16x16x32_bf16 v[84:87], a[156:159], v[220:223], v[84:87]\n"
      "v_mfma_f32_16x16x32_bf16 v[88:91], a[160:163], v[220:223], v[88:91]\n"
      "v_mfma_f32_16x16x32_bf16 v[92:95], a[164:167], v[220:223], v[92:95]\n"
      "global_load_dwordx4 a[84:87], v239, s[58:59] offset:1024 sc1\n"
      "s_waitcnt lgkmcnt(2)\n"
      "v_mfma_f32_16x16x32_bf16 v[96:99], a[144:147], v[224:227], v[96:99]\n"
      "v_mfma_f32_16x16x32_bf16 v[100:103], a[148:151], v[224:227], v[100:103]\n"
      "v_mfma_f32_16x16x32_bf16 v[104:107], a[152:155], v[224:227], v[104:107]\n"
      "ds_read_b128 v[220:223], v234 offset:28672\n"
      "v_mfma_f32_16x16x32_bf16 v[108:111], a[156:159], v[224:227], v[108:111]\n"
      "global_load_dwordx4 a[88:91], v239, s[58:59] offset:2048 sc1\n"
      "v_mfma_f32_16x16x32_bf16 v[112:115], a[160:163], v[224:227], v[112:115]\n"
      "v_mfma_f32_16x16x32_bf16 v[116:119], a[164:167], v[224:227], v[116:119]\n"
      "s_waitcnt lgkmcnt(2)\n"
      "v_mfma_f32_16x16x32_bf16 v[120:123], a[144:147], v[228:231], v[120:123]\n"
      "v_mfma_f32_16x16x32_bf16 v[124:127], a[148:151], v[228:231], v[124:127]\n"
      "global_load_dwordx4 a[92:95], v239, s[58:59] offset:3072 sc1\n"
      "v_mfma_f32_16x16x32_bf16 v[128:131], a[152:155], v[228:231], v[128:131]\n"
      "ds_read_b128 v[224:227], v234 offset:32768\n"
      "v_add_u32 v234, 0xffff7000, v234\n"
      "v_mfma_f32_16x16x32_bf16 v[132:135], a[156:159], v[228:231], v[132:135]\n"
      "v_mfma_f32_16x16x32_bf16 v[136:139], a[160:163], v[228:231], v[136:139]\n"
      "v_mfma_f32_16x16x32_bf16 v[140:143], a[164:167], v[228:231], v[140:143]\n"
      "s_add_u32 s58, s24, 0x4000\n"
      "s_addc_u32 s59, s25, 0\n"
      "global_load_dwordx4 a[96:99], v239, s[58:59] offset:0 sc1\n"
      "s_waitcnt lgkmcnt(2)\n"
      "v_mfma_f32_16x16x32_bf16 v[144:147], a[144:147], v[216:219], v[144:147]\n"
      "ds_read_b64 v[236:237], v250 offset:4104\n"
      "v_mfma_f32_16x16x32_bf16 v[148:151], a[148:151], v[216:219], v[148:151]\n"
      "v_mfma_f32_16x16x32_bf16 v[152:155], a[152:155], v[216:219], v[152:155]\n"
      "ds_read_b128 v[228:231], v235 offset:0\n"
      "v_mfma_f32_16x16x32_bf16 v[156:159], a[156:159], v[216:219], v[156:159]\n"
      "global_load_dwordx4 a[100:103], v239, s[58:59] offset:1024 sc1\n"
      "v_mfma_f32_16x16x32_bf16 v[160:163], a[160:163], v[216:219], v[160:163]\n"
      "v_mfma_f32_16x16x32_bf16 v[164:167], a[164:167], v[216:219], v[164:167]\n"
      "s_waitcnt lgkmcnt(3)\n"
      "v_mfma_f32_16x16x32_bf16 v[168:171], a[144:147], v[220:223], v[168:171]\n"
      "v_mfma_f32_16x16x32_bf16 v[172:175], a[148:151], v[220:223], v[172:175]\n"
      "global_load_dwordx4 a[104:107], v239, s[58:59] offset:2048 sc1\n"
      "v_mfma_f32_16x16x32_bf16 v[176:179], a[152:155], v[220:223], v[176:179]\n"
      "ds_read_b128 v[216:219], v235 offset:4096\n"
      "v_mfma_f32_16x16x32_bf16 v[180:183], a[156:159], v[220:223], v[180:183]\n"
      "v_mfma_f32_16x16x32_bf16 v[184:187], a[160:163], v[220:223], v[184:187]\n"
      "v_mfma_f32_16x16x32_bf16 v[188:191], a[164:167], v[220:223], v[188:191]\n"
      "global_load_dwordx4 a[108:111], v239, s[58:59] offset:3072 sc1\n"
      "s_waitcnt lgkmcnt(3)\n"
      "v_mfma_f32_16x16x32_bf16 v[192:195], a[144:147], v[224:227], v[192:195]\n"
      "v_mfma_f32_16x16x32_bf16 v[196:199], a[148:151], v[224:227], v[196:199]\n"
      "v_mfma_f32_16x16x32_bf16 v[200:203], a[152:155], v[224:227], v[200:203]\n"
      "ds_read_b128 v[220:223], v235 offset:8192\n"
      "v_mfma_f32_16x16x32_bf16 v[204:207], a[156:159], v[224:227], v[204:207]\n"
      "s_add_u32 s58, s24, 0x5000\n"
      "s_addc_u32 s59, s25, 0\n"
      "global_load_dwordx4 a[112:115], v239, s[58:59] offset:0 sc1\n"
      "v_mfma_f32_16x16x32_bf16 v[208:211], a[160:163], v[224:227], v[208:211]\n"
      "v_mfma_f32_16x16x32_bf16 v[212:215], a[164:167], v[224:227], v[212:215]\n"
      "s_waitcnt lgkmcnt(2)\n"
      "v_mfma_f32_16x16x32_bf16 v[0:3], a[168:171], v[228:231], v[0:3]\n"
      "v_mfma_f32_16x16x32_bf16 v[4:7], a[172:175], v[228:231], v[4:7]\n"
      "global_load_dwordx4 a[116:119], v239, s[58:59] offset:1024 sc1\n"
      "v_mfma_f32_16x16x32_bf16 v[8:11], a[176:179], v[228:231], v[8:11]\n"
      "ds_read_b128 v[224:227], v235 offset:12288\n"
      "v_mfma_f32_16x16x32_bf16 v[12:15], a[180:183], v[228:231], v[12:15]\n"
      "v_mfma_f32_16x16x32_bf16 v[16:19], a[184:187], v[228:231], v[16:19]\n"
      "v_mfma_f32_16x16x32_bf16 v[20:23], a[188:191], v[228:231], v[20:23]\n"
      "global_load_dwordx4 a[120:123], v239, s[58:59] offset:2048 sc1\n"
      "s_waitcnt lgkmcnt(2)\n"
      "v_mfma_f32_16x16x32_bf16 v[24:27], a[168:171], v[216:219], v[24:27]\n"
      "v_mfma_f32_16x16x32_bf16 v[28:31], a[172:175], v[216:219], v[28:31]\n"
      "v_mfma_f32_16x16x32_bf16 v[32:35], a[176:179], v[216:219], v[32:35]\n"
      "ds_read_b128 v[228:231], v235 offset:16384\n"
      "v_mfma_f32_16x16x32_bf16 v[36:39], a[180:183], v[216:219], v[36:39]\n"
      "global_load_dwordx4 a[124:127], v239, s[58:59] offset:3072 sc1\n"
      "v_mfma_f32_16x16x32_bf16 v[40:43], a[184:187], v[216:219], v[40:43]\n"
      "v_mfma_f32_16x16x32_bf16 v[44:47], a[188:191], v[216:219], v[44:47]\n"
      "s_waitcnt lgkmcnt(2)\n"
      "v_mfma_f32_16x16x32_bf16 v[48:51], a[168:171], v[220:223], v[48:51]\n"
      "v_mfma_f32_16x16x32_bf16 v[52:55], a[172:175], v[220:223], v[52:55]\n"
      "s_add_u32 s58, s24, 0x6000\n"
      "s_addc_u32 s59, s25, 0\n"
      "global_load_dwordx4 a[128:131], v239, s[58:59] offset:0 sc1\n"
      "v_mfma_f32_16x16x32_bf16 v[56:59], a[176:179], v[220:223], v[56:59]\n"
      "ds_read_b128 v[216:219], v235 offset:20480\n"
      "v_mfma_f32_16x16x32_bf16 v[60:63], a[180:183], v[220:223], v[60:63]\n"
      "v_mfma_f32_16x16x32_bf16 v[64:67], a[184:187], v[220:223], v[64:67]\n"
      "v_mfma_f32_16x16x32_bf16 v[68:71], a[188:191], v[220:223], v[68:71]\n"
      "global_load_dwordx4 a[132:135], v239, s[58:59] offset:1024 sc1\n"
      "s_waitcnt lgkmcnt(2)\n"
      "v_mfma_f32_16x16x32_bf16 v[72:75], a[168:171], v[224:227], v[72:75]\n"
      "v_mfma_f32_16x16x32_bf16 v[76:79], a[172:175], v[224:227], v[76:79]\n"
      "v_mfma_f32_16x16x32_bf16 v[80:83], a[176:179], v[224:227], v[80:83]\n"
      "ds_read_b128 v[220:223], v235 offset:24576\n"
      "v_mfma_f32_16x16x32_bf16 v[84:87], a[180:183], v[224:227], v[84:87]\n"
      "global_load_dwordx4 a[136:139], v239, s[58:59] offset:2048 sc1\n"
      "v_mfma_f32_16x16x32_bf16 v[88:91], a[184:187], v[224:227], v[88:91]\n"
      "v_mfma_f32_16x16x32_bf16 v[92:95], a[188:191], v[224:227], v[92:95]\n"
      "s_waitcnt lgkmcnt(2)\n"
      "v_mfma_f32_16x16x32_bf16 v[96:99], a[168:171], v[228:231], v[96:99]\n"
      "v_mfma_f32_16x16x32_bf16 v[100:103], a[172:175], v[228:231], v[100:103]\n"
      "global_load_dwordx4 a[140:143], v239, s[58:59] offset:3072 sc1\n"
      "v_mfma_f32_16x16x32_bf16 v[104:107], a[176:179], v[228:231], v[104:107]\n"
      "ds_read_b128 v[224:227], v235 offset:28672\n"
      "v_mfma_f32_16x16x32_bf16 v[108:111], a[180:183], v[228:231], v[108:111]\n"
      "v_mfma_f32_16x16x32_bf16 v[112:115], a[184:187], v[228:231], v[112:115]\n"
      "v_mfma_f32_16x16x32_bf16 v[116:119], a[188:191], v[228:231], v[116:119]\n"
      "s_add_u32 s58, s24, 0x7000\n"
      "s_addc_u32 s59, s25, 0\n"
      "global_load_dwordx4 a[144:147], v239, s[58:59] offset:0 sc1\n"
      "s_waitcnt lgkmcnt(2)\n"
      "v_mfma_f32_16x16x32_bf16 v[120:123], a[168:171], v[216:219], v[120:123]\n"
      "v_mfma_f32_16x16x32_bf16 v[124:127], a[172:175], v[216:219], v[124:127]\n"
      "v_mfma_f32_16x16x32_bf16 v[128:131], a[176:179], v[216:219], v[128:131]\n"
      "ds_read_b128 v[228:231], v235 offset:32768\n"
      "v_add_u32 v235, 0xffff7000, v235\n"
      "v_mfma_f32_16x16x32_bf16 v[132:135], a[180:183], v[216:219], v[132:135]\n"
      "global_load_dwordx4 a[148:151], v239, s[58:59] offset:1024 sc1\n"
      "v_mfma_f32_16x16x32_bf16 v[136:139], a[184:187], v[216:219], v[136:139]\n"
      "v_mfma_f32_16x16x32_bf16 v[140:143], a[188:191], v[216:219], v[140:143]\n"
      "s_waitcnt lgkmcnt(2)\n"
      "v_mfma_f32_16x16x32_bf16 v[144:147], a[168:171], v[220:223], v[144:147]\n"
      "v_mfma_f32_16x16x32_bf16 v[148:151], a[172:175], v[220:223], v[148:151]\n"
      "s_waitcnt vmcnt(27)\n"
      "s_barrier\n"
      "global_load_dwordx4 a[152:155], v239, s[58:59] offset:2048 sc1\n"
      "v_mfma_f32_16x16x32_bf16 v[152:155], a[176:179], v[220:223], v[152:155]\n"
      "ds_read_b128 v[216:219], v232 offset:0\n"
      "v_mfma_f32_16x16x32_bf16 v[156:159], a[180:183], v[220:223], v[156:159]\n"
      "v_mfma_f32_16x16x32_bf16 v[160:163], a[184:187], v[220:223], v[160:163]\n"
      "v_mfma_f32_16x16x32_bf16 v[164:167], a[188:191], v[220:223], v[164:167]\n"
      "global_load_dwordx4 a[156:159], v239, s[58:59] offset:3072 sc1\n"
      "s_waitcnt lgkmcnt(2)\n"
      "v_mfma_f32_16x16x32_bf16 v[168:171], a[168:171], v[224:227], v[168:171]\n"
      "v_mfma_f32_16x16x32_bf16 v[172:175], a[172:175], v[224:227], v[172:175]\n"
      "v_mfma_f32_16x16x32_bf16 v[176:179], a[176:179], v[224:227], v[176:179]\n"
      "ds_read_b128 v[220:223], v232 offset:4096\n"
      "v_mfma_f32_16x16x32_bf16 v[180:183], a[180:183], v[224:227], v[180:183]\n"
      "s_add_u32 s58, s24, 0x8000\n"
      "s_addc_u32 s59, s25, 0\n"
      "global_load_dwordx4 a[160:163], v239, s[58:59] offset:0 sc1\n"
      "v_mfma_f32_16x16x32_bf16 v[184:187], a[184:187], v[224:227], v[184:187]\n"
      "v_mfma_f32_16x16x32_bf16 v[188:191], a[188:191], v[224:227], v[188:191]\n"
      "s_waitcnt lgkmcnt(2)\n"
      "v_mfma_f32_16x16x32_bf16 v[192:195], a[168:171], v[228:231], v[192:195]\n"
      "v_mfma_f32_16x16x32_bf16 v[196:199], a[172:175], v[228:231], v[196:199]\n"
      "global_load_dwordx4 a[164:167], v239, s[58:59] offset:1024 sc1\n"
      "v_mfma_f32_16x16x32_bf16 v[200:203], a[176:179], v[228:231], v[200:203]\n"
      "ds_read_b128 v[224:227], v232 offset:8192\n"
      "v_mfma_f32_16x16x32_bf16 v[204:207], a[180:183], v[228:231], v[204:207]\n"
      "v_mfma_f32_16x16x32_bf16 v[208:211], a[184:187], v[228:231], v[208:211]\n"
      "v_mfma_f32_16x16x32_bf16 v[212:215], a[188:191], v[228:231], v[212:215]\n"
      "s_waitcnt lgkmcnt(2)\n"
      "v_mfma_f32_16x16x32_bf16 v[0:3], a[192:195], v[216:219], v[0:3]\n"
      "global_load_dwordx4 a[168:171], v239, s[58:59] offset:2048 sc1\n"
      "v_mfma_f32_16x16x32_bf16 v[4:7], a[196:199], v[216:219], v[4:7]\n"
      "v_mfma_f32_16x16x32_bf16 v[8:11], a[200:203], v[216:219], v[8:11]\n"
      "ds_read_b128 v[228:231], v232 offset:12288\n"
      "v_mfma_f32_16x16x32_bf16 v[12:15], a[204:207], v[216:219], v[12:15]\n"
      "v_mfma_f32_16x16x32_bf16 v[16:19], a[208:211], v[216:219], v[16:19]\n"
      "s_mul_i32 s78, s98, s19\n"
      "v_mfma_f32_16x16x32_bf16 v[20:23], a[212:215], v[216:219], v[20:23]\n"
      "v_add_u32 v236, s78, v236\n"
      "s_waitcnt lgkmcnt(2)\n"
      "v_mfma_f32_16x16x32_bf16 v[24:27], a[192:195], v[220:223], v[24:27]\n"
      "s_mul_i32 s78, s78, s99\n"
      "v_mfma_f32_16x16x32_bf16 v[28:31], a[196:199], v[220:223], v[28:31]\n"
      "v_add_u32 v237, s78, v237\n"
      "v_mfma_f32_16x16x32_bf16 v[32:35], a[200:203], v[220:223], v[32:35]\n"
      "ds_read_b128 v[216:219], v232 offset:16384\n"
      "s_add_u32 s52, s20, s32\n"
      "s_addc_u32 s53, s21, 0\n"
      "s_add_u32 m0, s17, 0x0\n"
      "v_mfma_f32_16x16x32_bf16 v[36:39], a[204:207], v[220:223], v[36:39]\n"
      "global_load_lds_dwordx4 v236, s[52:53] offset:0 \n"
      "v_mfma_f32_16x16x32_bf16 v[40:43], a[208:211], v[220:223], v[40:43]\n"
      "v_mfma_f32_16x16x32_bf16 v[44:47], a[212:215], v[220:223], v[44:47]\n"
      "s_add_u32 m0, s17, 0x1f00\n"
      "s_waitcnt lgkmcnt(2)\n"
      "v_mfma_f32_16x16x32_bf16 v[48:51], a[192:195], v[224:227], v[48:51]\n"
      "global_load_lds_dwordx4 v236, s[52:53] offset:256 \n"
      "v_mfma_f32_16x16x32_bf16 v[52:55], a[196:199], v[224:227], v[52:55]\n"
      "v_mfma_f32_16x16x32_bf16 v[56:59], a[200:203], v[224:227], v[56:59]\n"
      "ds_read_b128 v[220:223], v232 offset:20480\n"
      "s_add_u32 m0, s17, 0x3e00\n"
      "v_mfma_f32_16x16x32_bf16 v[60:63], a[204:207], v[224:227], v[60:63]\n"
      "global_load_lds_dwordx4 v236, s[52:53] offset:512 \n"
      "v_mfma_f32_16x16x32_bf16 v[64:67], a[208:211], v[224:227], v[64:67]\n"
      "v_mfma_f32_16x16x32_bf16 v[68:71], a[212:215], v[224:227], v[68:71]\n"
      "s_add_u32 m0, s17, 0x1000\n"
      "s_waitcnt lgkmcnt(2)\n"
      "v_mfma_f32_16x16x32_bf16 v[72:75], a[192:195], v[228:231], v[72:75]\n"
      "global_load_lds_dwordx4 v237, s[52:53] offset:0 \n"
      "v_mfma_f32_16x16x32_bf16 v[76:79], a[196:199], v[228:231], v[76:79]\n"
      "v_mfma_f32_16x16x32_bf16 v[80:83], a[200:203], v[228:231], v[80:83]\n"
      "ds_read_b128 v[224:227], v232 offset:24576\n"
      "s_add_u32 m0, s17, 0x2f00\n"
      "v_mfma_f32_16x16x32_bf16 v[84:87], a[204:207], v[228:231], v[84:87]\n"
      "global_load_lds_dwordx4 v237, s[52:53] offset:256 \n"
      "v_mfma_f32_16x16x32_bf16 v[88:91], a[208:211], v[228:231], v[88:91]\n"
      "v_mfma_f32_16x16x32_bf16 v[92:95], a[212:215], v[228:231], v[92:95]\n"
      "s_add_u32 m0, s17, 0x4e00\n"
      "s_waitcnt lgkmcnt(2)\n"
      "v_mfma_f32_16x16x32_bf16 v[96:99], a[192:195], v[216:219], v[96:99]\n"
      "global_load_lds_dwordx4 v237, s[52:53] offset:512 \n"
      "v_mfma_f32_16x16x32_bf16 v[100:103], a[196:199], v[216:219], v[100:103]\n"
      "v_mfma_f32_16x16x32_bf16 v[104:107], a[200:203], v[216:219], v[104:107]\n"
      "ds_read_b128 v[228:231], v232 offset:28672\n"
      "v_mfma_f32_16x16x32_bf16 v[108:111], a[204:207], v[216:219], v[108:111]\n"
      "v_mfma_f32_16x16x32_bf16 v[112:115], a[208:211], v[216:219], v[112:115]\n"
      "v_mfma_f32_16x16x32_bf16 v[116:119], a[212:215], v[216:219], v[116:119]\n"
      "s_waitcnt lgkmcnt(2)\n"
      "v_mfma_f32_16x16x32_bf16 v[120:123], a[192:195], v[220:223], v[120:123]\n"
      "v_mfma_f32_16x16x32_bf16 v[124:127], a[196:199], v[220:223], v[124:127]\n"
      "v_mfma_f32_16x16x32_bf16 v[128:131], a[200:203], v[220:223], v[128:131]\n"
      "ds_read_b128 v[216:219], v232 offset:32768\n"
      "v_add_u32 v232, v249, v232\n"
      "v_mfma_f32_16x16x32_bf16 v[132:135], a[204:207], v[220:223], v[132:135]\n"
      "v_mfma_f32_16x16x32_bf16 v[136:139], a[208:211], v[220:223], v[136:139]\n"
      "v_mfma_f32_16x16x32_bf16 v[140:143], a[212:215], v[220:223], v[140:143]\n"
      "s_waitcnt lgkmcnt(2)\n"
      "v_mfma_f32_16x16x32_bf16 v[144:147], a[192:195], v[224:227], v[144:147]\n"
      "v_mfma_f32_16x16x32_bf16 v[148:151], a[196:199], v[224:227], v[148:151]\n"
      "v_mfma_f32_16x16x32_bf16 v[152:155], a[200:203], v[224:227], v[152:155]\n"
      "ds_read_b128 v[220:223], v233 offset:0\n"
      "v_mfma_f32_16x16x32_bf16 v[156:159], a[204:207], v[224:227], v[156:159]\n"
      "v_mfma_f32_16x16x32_bf16 v[160:163], a[208:211], v[224:227], v[160:163]\n"
      "v_mfma_f32_16x16x32_bf16 v[164:167], a[212:215], v[224:227], v[164:167]\n"
      "s_waitcnt lgkmcnt(2)\n"
      "v_mfma_f32_16x16x32_bf16 v[168:171], a[192:195], v[228:231], v[168:171]\n"
      "v_mfma_f32_16x16x32_bf16 v[172:175], a[196:199], v[228:231], v[172:175]\n"
      "v_mfma_f32_16x16x32_bf16 v[176:179], a[200:203], v[228:231], v[176:179]\n"
      "ds_read_b128 v[224:227], v233 offset:4096\n"
      "v_mfma_f32_16x16x32_bf16 v[180:183], a[204:207], v[228:231], v[180:183]\n"
      "v_mfma_f32_16x16x32_bf16 v[184:187], a[208:211], v[228:231], v[184:187]\n"
      "v_mfma_f32_16x16x32_bf16 v[188:191], a[212:215], v[228:231], v[188:191]\n"
      "s_waitcnt lgkmcnt(2)\n"
      "v_mfma_f32_16x16x32_bf16 v[192:195], a[192:195], v[216:219], v[192:195]\n"
      "v_mfma_f32_16x16x32_bf16 v[196:199], a[196:199], v[216:219], v[196:199]\n"
      "v_mfma_f32_16x16x32_bf16 v[200:203], a[200:203], v[216:219], v[200:203]\n"
      "ds_read_b128 v[228:231], v233 offset:8192\n"
      "v_mfma_f32_16x16x32_bf16 v[204:207], a[204:207], v[216:219], v[204:207]\n"
      "v_mfma_f32_16x16x32_bf16 v[208:211], a[208:211], v[216:219], v[208:211]\n"
      "v_mfma_f32_16x16x32_bf16 v[212:215], a[212:215], v[216:219], v[212:215]\n"
      "s_waitcnt lgkmcnt(2)\n"
      "v_mfma_f32_16x16x32_bf16 v[0:3], a[216:219], v[220:223], v[0:3]\n"
      "LPD2:\n"
      "s_cbranch_execz LPD7\n"
      "v_mfma_f32_16x16x32_bf16 v[4:7], a[220:223], v[220:223], v[4:7]\n"
      "v_mfma_f32_16x16x32_bf16 v[8:11], a[224:227], v[220:223], v[8:11]\n"
      "ds_read_b128 v[216:219], v233 offset:12288\n"
      "v_mfma_f32_16x16x32_bf16 v[12:15], a[228:231], v[220:223], v[12:15]\n"
      "v_mfma_f32_16x16x32_bf16 v[16:19], a[232:235], v[220:223], v[16:19]\n"
      "s_add_u32 s52, s42, s32\n"
      "s_addc_u32 s53, s43, 0\n"
      "s_add_u32 m0, s17, 0x6000\n"
      "v_mfma_f32_16x16x32_bf16 v[20:23], a[236:239], v[220:223], v[20:23]\n"
      "global_load_lds_dwordx4 v236, s[52:53] offset:0 \n"
      "s_waitcnt lgkmcnt(2)\n"
      "v_mfma_f32_16x16x32_bf16 v[24:27], a[216:219], v[224:227], v[24:27]\n"
      "v_mfma_f32_16x16x32_bf16 v[28:31], a[220:223], v[224:227], v[28:31]\n"
      "s_add_u32 m0, s17, 0x7f00\n"
      "v_mfma_f32_16x16x32_bf16 v[32:35], a[224:227], v[224:227], v[32:35]\n"
      "ds_read_b128 v[220:223], v233 offset:16384\n"
      "global_load_lds_dwordx4 v236, s[52:53] offset:256 \n"
      "v_mfma_f32_16x16x32_bf16 v[36:39], a[228:231], v[224:227], v[36:39]\n"
      "v_mfma_f32_16x16x32_bf16 v[40:43], a[232:235], v[224:227], v[40:43]\n"
      "s_add_u32 m0, s17, 0x9e00\n"
      "v_mfma_f32_16x16x32_bf16 v[44:47], a[236:239], v[224:227], v[44:47]\n"
      "global_load_lds_dwordx4 v236, s[52:53] offset:512 \n"
      "s_waitcnt lgkmcnt(2)\n"
      "v_mfma_f32_16x16x32_bf16 v[48:51], a[216:219], v[228:231], v[48:51]\n"
      "v_mfma_f32_16x16x32_bf16 v[52:55], a[220:223], v[228:231], v[52:55]\n"
      "s_add_u32 m0, s17, 0x7000\n"
      "v_mfma_f32_16x16x32_bf16 v[56:59], a[224:227], v[228:231], v[56:59]\n"
      "ds_read_b128 v[224:227], v233 offset:20480\n"
      "global_load_lds_dwordx4 v237, s[52:53] offset:0 \n"
      "v_mfma_f32_16x16x32_bf16 v[60:63], a[228:231], v[228:231], v[60:63]\n"
      "v_mfma_f32_16x16x32_bf16 v[64:67], a[232:235], v[228:231], v[64:67]\n"
      "s_add_u32 m0, s17, 0x8f00\n"
      "v_mfma_f32_16x16x32_bf16 v[68:71], a[236:239], v[228:231], v[68:71]\n"
      "global_load_lds_dwordx4 v237, s[52:53] offset:256 \n"
      "s_waitcnt lgkmcnt(2)\n"
      "v_mfma_f32_16x16x32_bf16 v[72:75], a[216:219], v[216:219], v[72:75]\n"
      "v_mfma_f32_16x16x32_bf16 v[76:79], a[220:223], v[216:219], v[76:79]\n"
      "s_add_u32 m0, s17, 0xae00\n"
      "v_mfma_f32_16x16x32_bf16 v[80:83], a[224:227], v[216:219], v[80:83]\n"
      "ds_read_b128 v[228:231], v233 offset:24576\n"
      "global_load_lds_dwordx4 v237, s[52:53] offset:512 \n"
      "v_mfma_f32_16x16x32_bf16 v[84:87], a[228:231], v[216:219], v[84:87]\n"
      "v_mfma_f32_16x16x32_bf16 v[88:91], a[232:235], v[216:219], v[88:91]\n"
      "v_mfma_f32_16x16x32_bf16 v[92:95], a[236:239], v[216:219], v[92:95]\n"
      "s_waitcnt lgkmcnt(2)\n"
      "v_mfma_f32_16x16x32_bf16 v[96:99], a[216:219], v[220:223], v[96:99]\n"
      "v_mfma_f32_16x16x32_bf16 v[100:103], a[220:223], v[220:223], v[100:103]\n"
      "v_mfma_f32_16x16x32_bf16 v[104:107], a[224:227], v[220:223], v[104:107]\n"
      "ds_read_b128 v[216:219], v233 offset:28672\n"
      "v_mfma_f32_16x16x32_bf16 v[108:111], a[228:231], v[220:223], v[108:111]\n"
      "v_mfma_f32_16x16x32_bf16 v[112:115], a[232:235], v[220:223], v[112:115]\n"
      "v_mfma_f32_16x16x32_bf16 v[116:119], a[236:239], v[220:223], v[116:119]\n"
      "s_waitcnt lgkmcnt(2)\n"
      "v_mfma_f32_16x16x32_bf16 v[120:123], a[216:219], v[224:227], v[120:123]\n"
      "v_mfma_f32_16x16x32_bf16 v[124:127], a[220:223], v[224:227], v[124:127]\n"
      "v_mfma_f32_16x16x32_bf16 v[128:131], a[224:227], v[224:227], v[128:131]\n"
      "ds_read_b128 v[220:223], v233 offset:32768\n"
      "v_add_u32 v233, v249, v233\n"
      "v_mfma_f32_16x16x32_bf16 v[132:135], a[228:231], v[224:227], v[132:135]\n"
      "v_mfma_f32_16x16x32_bf16 v[136:139], a[232:235], v[224:227], v[136:139]\n"
      "v_mfma_f32_16x16x32_bf16 v[140:143], a[236:239], v[224:227], v[140:143]\n"
      "s_waitcnt lgkmcnt(2)\n"
      "v_mfma_f32_16x16x32_bf16 v[144:147], a[216:219], v[228:231], v[144:147]\n"
      "v_mfma_f32_16x16x32_bf16 v[148:151], a[220:223], v[228:231], v[148:151]\n"
      "v_mfma_f32_16x16x32_bf16 v[152:155], a[224:227], v[228:231], v[152:155]\n"
      "ds_read_b128 v[224:227], v234 offset:0\n"
      "v_mfma_f32_16x16x32_bf16 v[156:159], a[228:231], v[228:231], v[156:159]\n"
      "v_mfma_f32_16x16x32_bf16 v[160:163], a[232:235], v[228:231], v[160:163]\n"
      "v_mfma_f32_16x16x32_bf16 v[164:167], a[236:239], v[228:231], v[164:167]\n"
      "s_waitcnt lgkmcnt(2)\n"
      "v_mfma_f32_16x16x32_bf16 v[168:171], a[216:219], v[216:219], v[168:171]\n"
      "v_mfma_f32_16x16x32_bf16 v[172:175], a[220:223], v[216:219], v[172:175]\n"
      "v_mfma_f32_16x16x32_bf16 v[176:179], a[224:227], v[216:219], v[176:179]\n"
      "ds_read_b128 v[228:231], v234 offset:4096\n"
      "v_mfma_f32_16x16x32_bf16 v[180:183], a[228:231], v[216:219], v[180:183]\n"
      "v_mfma_f32_16x16x32_bf16 v[184:187], a[232:235], v[216:219], v[184:187]\n"
      "v_mfma_f32_16x16x32_bf16 v[188:191], a[236:239], v[216:219], v[188:191]\n"
      "s_waitcnt lgkmcnt(2)\n"
      "v_mfma_f32_16x16x32_bf16 v[192:195], a[216:219], v[220:223], v[192:195]\n"
      "v_mfma_f32_16x16x32_bf16 v[196:199], a[220:223], v[220:223], v[196:199]\n"
      "v_mfma_f32_16x16x32_bf16 v[200:203], a[224:227], v[220:223], v[200:203]\n"
      "ds_read_b128 v[216:219], v234 offset:8192\n"
      "v_mfma_f32_16x16x32_bf16 v[204:207], a[228:231], v[220:223], v[204:207]\n"
      "v_mfma_f32_16x16x32_bf16 v[208:211], a[232:235], v[220:223], v[208:211]\n"
      "v_mfma_f32_16x16x32_bf16 v[212:215], a[236:239], v[220:223], v[212:215]\n"
      "s_waitcnt lgkmcnt(2)\n"
      "v_mfma_f32_16x16x32_bf16 v[0:3], a[0:3], v[224:227], v[0:3]\n"
      "ds_read_b128 v[244:247], v250\n"
      "v_mfma_f32_16x16x32_bf16 v[4:7], a[4:7], v[224:227], v[4:7]\n"
      "ds_read_b64 v[252:253], v250 offset:4096\n"
      "v_mfma_f32_16x16x32_bf16 v[8:11], a[8:11], v[224:227], v[8:11]\n"
      "ds_read_b128 v[220:223], v234 offset:12288\n"
      "v_mfma_f32_16x16x32_bf16 v[12:15], a[12:15], v[224:227], v[12:15]\n"
      "v_mfma_f32_16x16x32_bf16 v[16:19], a[16:19], v[224:227], v[16:19]\n"
      "v_mfma_f32_16x16x32_bf16 v[20:23], a[20:23], v[224:227], v[20:23]\n"
      "s_waitcnt lgkmcnt(4)\n"
      "v_mfma_f32_16x16x32_bf16 v[24:27], a[0:3], v[228:231], v[24:27]\n"
      "v_mfma_f32_16x16x32_bf16 v[28:31], a[4:7], v[228:231], v[28:31]\n"
      "v_mfma_f32_16x16x32_bf16 v[32:35], a[8:11], v[228:231], v[32:35]\n"
      "ds_read_b128 v[224:227], v234 offset:16384\n"
      "v_mfma_f32_16x16x32_bf16 v[36:39], a[12:15], v[228:231], v[36:39]\n"
      "global_load_dwordx4 a[172:175], v239, s[58:59] offset:3072 sc1\n"
      "v_mfma_f32_16x16x32_bf16 v[40:43], a[16:19], v[228:231], v[40:43]\n"
      "v_mfma_f32_16x16x32_bf16 v[44:47], a[20:23], v[228:231], v[44:47]\n"
      "s_waitcnt lgkmcnt(4)\n"
      "v_mfma_f32_16x16x32_bf16 v[48:51], a[0:3], v[216:219], v[48:51]\n"
      "v_mfma_f32_16x16x32_bf16 v[52:55], a[4:7], v[216:219], v[52:55]\n"
      "s_add_u32 s58, s24, 0x9000\n"
      "s_addc_u32 s59, s25, 0\n"
      "global_load_dwordx4 a[176:179], v239, s[58:59] offset:0 sc1\n"
      "v_mfma_f32_16x16x32_bf16 v[56:59], a[8:11], v[216:219], v[56:59]\n"
      "ds_read_b128 v[228:231], v234 offset:20480\n"
      "v_mfma_f32_16x16x32_bf16 v[60:63], a[12:15], v[216:219], v[60:63]\n"
      "v_mfma_f32_16x16x32_bf16 v[64:67], a[16:19], v[216:219], v[64:67]\n"
      "v_mfma_f32_16x16x32_bf16 v[68:71], a[20:23], v[216:219], v[68:71]\n"
      "global_load_dwordx4 a[180:183], v239, s[58:59] offset:1024 sc1\n"
      "s_waitcnt lgkmcnt(2)\n"
      "v_mfma_f32_16x16x32_bf16 v[72:75], a[0:3], v[220:223], v[72:75]\n"
      "v_mfma_f32_16x16x32_bf16 v[76:79], a[4:7], v[220:223], v[76:79]\n"
      "v_mfma_f32_16x16x32_bf16 v[80:83], a[8:11], v[220:223], v[80:83]\n"
      "ds_read_b128 v[216:219], v234 offset:24576\n"
      "v_mfma_f32_16x16x32_bf16 v[84:87], a[12:15], v[220:223], v[84:87]\n"
      "global_load_dwordx4 a[184:187], v239, s[58:59] offset:2048 sc1\n"
      "v_mfma_f32_16x16x32_bf16 v[88:91], a[16:19], v[220:223], v[88:91]\n"
      "v_mfma_f32_16x16x32_bf16 v[92:95], a[20:23], v[220:223], v[92:95]\n"
      "s_waitcnt lgkmcnt(2)\n"
      "v_mfma_f32_16x16x32_bf16 v[96:99], a[0:3], v[224:227], v[96:99]\n"
      "v_mfma_f32_16x16x32_bf16 v[100:103], a[4:7], v[224:227], v[100:103]\n"
      "global_load_dwordx4 a[188:191], v239, s[58:59] offset:3072 sc1\n"
      "v_mfma_f32_16x16x32_bf16 v[104:107], a[8:11], v[224:227], v[104:107]\n"
      "ds_read_b128 v[220:223], v234 offset:28672\n"
      "v_mfma_f32_16x16x32_bf16 v[108:111], a[12:15], v[224:227], v[108:111]\n"
      "v_mfma_f32_16x16x32_bf16 v[112:115], a[16:19], v[224:227], v[112:115]\n"
      "v_mfma_f32_16x16x32_bf16 v[116:119], a[20:23], v[224:227], v[116:119]\n"
      "s_add_u32 s58, s24, 0xa000\n"
      "s_addc_u32 s59, s25, 0\n"
      "global_load_dwordx4 a[192:195], v239, s[58:59] offset:0 sc1\n"
      "s_waitcnt lgkmcnt(2)\n"
      "v_mfma_f32_16x16x32_bf16 v[120:123], a[0:3], v[228:231], v[120:123]\n"
      "v_mfma_f32_16x16x32_bf16 v[124:127], a[4:7], v[228:231], v[124:127]\n"
      "v_mfma_f32_16x16x32_bf16 v[128:131], a[8:11], v[228:231], v[128:131]\n"
      "ds_read_b128 v[224:227], v234 offset:32768\n"
      "v_add_u32 v234, v249, v234\n"
      "v_mfma_f32_16x16x32_bf16 v[132:135], a[12:15], v[228:231], v[132:135]\n"
      "global_load_dwordx4 a[196:199], v239, s[58:59] offset:1024 sc1\n"
      "v_mfma_f32_16x16x32_bf16 v[136:139], a[16:19], v[228:231], v[136:139]\n"
      "v_mfma_f32_16x16x32_bf16 v[140:143], a[20:23], v[228:231], v[140:143]\n"
      "s_waitcnt lgkmcnt(2)\n"
      "v_mfma_f32_16x16x32_bf16 v[144:147], a[0:3], v[216:219], v[144:147]\n"
      "v_mfma_f32_16x16x32_bf16 v[148:151], a[4:7], v[216:219], v[148:151]\n"
      "global_load_dwordx4 a[200:203], v239, s[58:59] offset:2048 sc1\n"
      "v_mfma_f32_16x16x32_bf16 v[152:155], a[8:11], v[216:219], v[152:155]\n"
      "ds_read_b128 v[228:231], v235 offset:4096\n"
      "v_mfma_f32_16x16x32_bf16 v[156:159], a[12:15], v[216:219], v[156:159]\n"
      "v_mfma_f32_16x16x32_bf16 v[160:163], a[16:19], v[216:219], v[160:163]\n"
      "v_mfma_f32_16x16x32_bf16 v[164:167], a[20:23], v[216:219], v[164:167]\n"
      "global_load_dwordx4 a[204:207], v239, s[58:59] offset:3072 sc1\n"
      "s_waitcnt lgkmcnt(2)\n"
      "v_mfma_f32_16x16x32_bf16 v[168:171], a[0:3], v[220:223], v[168:171]\n"
      "v_mfma_f32_16x16x32_bf16 v[172:175], a[4:7], v[220:223], v[172:175]\n"
      "v_mfma_f32_16x16x32_bf16 v[176:179], a[8:11], v[220:223], v[176:179]\n"
      "ds_read_b128 v[216:219], v235 offset:8192\n"
      "v_mfma_f32_16x16x32_bf16 v[180:183], a[12:15], v[220:223], v[180:183]\n"
      "s_add_u32 s58, s24, 0xb000\n"
      "s_addc_u32 s59, s25, 0\n"
      "global_load_dwordx4 a[208:211], v239, s[58:59] offset:0 sc1\n"
      "v_mfma_f32_16x16x32_bf16 v[184:187], a[16:19], v[220:223], v[184:187]\n"
      "v_mfma_f32_16x16x32_bf16 v[188:191], a[20:23], v[220:223], v[188:191]\n"
      "s_waitcnt lgkmcnt(2)\n"
      "v_mfma_f32_16x16x32_bf16 v[192:195], a[0:3], v[224:227], v[192:195]\n"
      "v_mfma_f32_16x16x32_bf16 v[196:199], a[4:7], v[224:227], v[196:199]\n"
      "global_load_dwordx4 a[212:215], v239, s[58:59] offset:1024 sc1\n"
      "v_mfma_f32_16x16x32_bf16 v[200:203], a[8:11], v[224:227], v[200:203]\n"
      "ds_read_b128 v[220:223], v235 offset:12288\n"
      "v_mfma_f32_16x16x32_bf16 v[204:207], a[12:15], v[224:227], v[204:207]\n"
      "v_mfma_f32_16x16x32_bf16 v[208:211], a[16:19], v[224:227], v[208:211]\n"
      "v_mfma_f32_16x16x32_bf16 v[212:215], a[20:23], v[224:227], v[212:215]\n"
      "global_load_dwordx4 a[216:219], v239, s[58:59] offset:2048 sc1\n"
      "s_waitcnt lgkmcnt(2)\n"
      "v_mfma_f32_16x16x32_bf16 v[24:27], a[24:27], v[228:231], v[24:27]\n"
      "v_mfma_f32_16x16x32_bf16 v[28:31], a[28:31], v[228:231], v[28:31]\n"
      "v_mfma_f32_16x16x32_bf16 v[32:35], a[32:35], v[228:231], v[32:35]\n"
      "ds_read_b128 v[224:227], v235 offset:16384\n"
      "v_mfma_f32_16x16x32_bf16 v[36:39], a[36:39], v[228:231], v[36:39]\n"
      "global_load_dwordx4 a[220:223], v239, s[58:59] offset:3072 sc1\n"
      "v_mfma_f32_16x16x32_bf16 v[40:43], a[40:43], v[228:231], v[40:43]\n"
      "v_mfma_f32_16x16x32_bf16 v[44:47], a[44:47], v[228:231], v[44:47]\n"
      "s_waitcnt lgkmcnt(2)\n"
      "v_mfma_f32_16x16x32_bf16 v[48:51], a[24:27], v[216:219], v[48:51]\n"
      "v_cvt_pk_bf16_f32 v24, v24, v25\n"
      "v_cvt_pk_bf16_f32 v25, v26, v27\n"
      "v_mfma_f32_16x16x32_bf16 v[52:55], a[28:31], v[216:219], v[52:55]\n"
      "v_cvt_pk_bf16_f32 v26, v28, v29\n"
      "v_cvt_pk_bf16_f32 v27, v30, v31\n"
      "s_add_u32 s58, s24, 0xc000\n"
      "s_addc_u32 s59, s25, 0\n"
      "global_load_dwordx4 a[224:227], v239, s[58:59] offset:0 sc1\n"
      "v_mfma_f32_16x16x32_bf16 v[56:59], a[32:35], v[216:219], v[56:59]\n"
      "ds_read_b128 v[228:231], v235 offset:20480\n"
      "v_cvt_pk_bf16_f32 v28, v32, v33\n"
      "v_cvt_pk_bf16_f32 v29, v34, v35\n"
      "v_mfma_f32_16x16x32_bf16 v[60:63], a[36:39], v[216:219], v[60:63]\n"
      "v_cvt_pk_bf16_f32 v30, v36, v37\n"
      "v_cvt_pk_bf16_f32 v31, v38, v39\n"
      "v_mfma_f32_16x16x32_bf16 v[64:67], a[40:43], v[216:219], v[64:67]\n"
      "v_cvt_pk_bf16_f32 v32, v40, v41\n"
      "v_cvt_pk_bf16_f32 v33, v42, v43\n"
      "v_mfma_f32_16x16x32_bf16 v[68:71], a[44:47], v[216:219], v[68:71]\n"
      "v_cvt_pk_bf16_f32 v34, v44, v45\n"
      "v_cvt_pk_bf16_f32 v35, v46, v47\n"
      "global_load_dwordx4 a[228:231], v239, s[58:59] offset:1024 sc1\n"
      "s_waitcnt lgkmcnt(2)\n"
      "v_mfma_f32_16x16x32_bf16 v[72:75], a[24:27], v[220:223], v[72:75]\n"
      "v_cvt_pk_bf16_f32 v48, v48, v49\n"
      "v_cvt_pk_bf16_f32 v49, v50, v51\n"
      "v_mfma_f32_16x16x32_bf16 v[76:79], a[28:31], v[220:223], v[76:79]\n"
      "v_cvt_pk_bf16_f32 v50, v52, v53\n"
      "v_cvt_pk_bf16_f32 v51, v54, v55\n"
      "v_mfma_f32_16x16x32_bf16 v[80:83], a[32:35], v[220:223], v[80:83]\n"
      "ds_read_b128 v[216:219], v235 offset:24576\n"
      "v_cvt_pk_bf16_f32 v52, v56, v57\n"
      "v_cvt_pk_bf16_f32 v53, v58, v59\n"
      "v_mfma_f32_16x16x32_bf16 v[84:87], a[36:39], v[220:223], v[84:87]\n"
      "v_cvt_pk_bf16_f32 v54, v60, v61\n"
      "v_cvt_pk_bf16_f32 v55, v62, v63\n"
      "global_load_dwordx4 a[232:235], v239, s[58:59] offset:2048 sc1\n"
      "v_mfma_f32_16x16x32_bf16 v[88:91], a[40:43], v[220:223], v[88:91]\n"
      "v_cvt_pk_bf16_f32 v56, v64, v65\n"
      "v_cvt_pk_bf16_f32 v57, v66, v67\n"
      "v_mfma_f32_16x16x32_bf16 v[92:95], a[44:47], v[220:223], v[92:95]\n"
      "v_cvt_pk_bf16_f32 v58, v68, v69\n"
      "v_cvt_pk_bf16_f32 v59, v70, v71\n"
      "s_waitcnt lgkmcnt(2)\n"
      "v_mfma_f32_16x16x32_bf16 v[96:99], a[24:27], v[224:227], v[96:99]\n"
      "v_cvt_pk_bf16_f32 v72, v72, v73\n"
      "v_cvt_pk_bf16_f32 v73, v74, v75\n"
      "v_mfma_f32_16x16x32_bf16 v[100:103], a[28:31], v[224:227], v[100:103]\n"
      "v_cvt_pk_bf16_f32 v74, v76, v77\n"
      "v_cvt_pk_bf16_f32 v75, v78, v79\n"
      "global_load_dwordx4 a[236:239], v239, s[58:59] offset:3072 sc1\n"
      "v_mfma_f32_16x16x32_bf16 v[104:107], a[32:35], v[224:227], v[104:107]\n"
      "ds_read_b128 v[220:223], v235 offset:28672\n"
      "v_cvt_pk_bf16_f32 v76, v80, v81\n"
      "v_cvt_pk_bf16_f32 v77, v82, v83\n"
      "v_mfma_f32_16x16x32_bf16 v[108:111], a[36:39], v[224:227], v[108:111]\n"
      "v_cvt_pk_bf16_f32 v78, v84, v85\n"
      "v_cvt_pk_bf16_f32 v79, v86, v87\n"
      "v_mfma_f32_16x16x32_bf16 v[112:115], a[40:43], v[224:227], v[112:115]\n"
      "v_cvt_pk_bf16_f32 v80, v88, v89\n"
      "v_cvt_pk_bf16_f32 v81, v90, v91\n"
      "v_mfma_f32_16x16x32_bf16 v[116:119], a[44:47], v[224:227], v[116:119]\n"
      "v_cvt_pk_bf16_f32 v82, v92, v93\n"
      "v_cvt_pk_bf16_f32 v83, v94, v95\n"
      "s_add_u32 s58, s24, 0xd000\n"
      "s_addc_u32 s59, s25, 0\n"
      "global_load_dwordx4 a[0:3], v239, s[58:59] offset:0 sc1\n"
      "s_waitcnt lgkmcnt(2)\n"
      "v_mfma_f32_16x16x32_bf16 v[120:123], a[24:27], v[228:231], v[120:123]\n"
      "v_cvt_pk_bf16_f32 v96, v96, v97\n"
      "v_cvt_pk_bf16_f32 v97, v98, v99\n"
      "v_mfma_f32_16x16x32_bf16 v[124:127], a[28:31], v[228:231], v[124:127]\n"
      "v_cvt_pk_bf16_f32 v98, v100, v101\n"
      "v_cvt_pk_bf16_f32 v99, v102, v103\n"
      "v_mfma_f32_16x16x32_bf16 v[128:131], a[32:35], v[228:231], v[128:131]\n"
      "ds_read_b128 v[224:227], v235 offset:32768\n"
      "v_cvt_pk_bf16_f32 v100, v104, v105\n"
      "v_cvt_pk_bf16_f32 v101, v106, v107\n"
      "v_mfma_f32_16x16x32_bf16 v[132:135], a[36:39], v[228:231], v[132:135]\n"
      "v_cvt_pk_bf16_f32 v102, v108, v109\n"
      "v_cvt_pk_bf16_f32 v103, v110, v111\n"
      "global_load_dwordx4 a[4:7], v239, s[58:59] offset:1024 sc1\n"
      "v_mfma_f32_16x16x32_bf16 v[136:139], a[40:43], v[228:231], v[136:139]\n"
      "v_cvt_pk_bf16_f32 v104, v112, v113\n"
      "v_cvt_pk_bf16_f32 v105, v114, v115\n"
      "v_mfma_f32_16x16x32_bf16 v[140:143], a[44:47], v[228:231], v[140:143]\n"
      "v_cvt_pk_bf16_f32 v106, v116, v117\n"
      "v_cvt_pk_bf16_f32 v107, v118, v119\n"
      "s_waitcnt lgkmcnt(2)\n"
      "v_mfma_f32_16x16x32_bf16 v[144:147], a[24:27], v[216:219], v[144:147]\n"
      "v_cvt_pk_bf16_f32 v120, v120, v121\n"
      "v_cvt_pk_bf16_f32 v121, v122, v123\n"
      "v_mfma_f32_16x16x32_bf16 v[148:151], a[28:31], v[216:219], v[148:151]\n"
      "v_cvt_pk_bf16_f32 v122, v124, v125\n"
      "v_cvt_pk_bf16_f32 v123, v126, v127\n"
      "global_load_dwordx4 a[8:11], v239, s[58:59] offset:2048 sc1\n"
      "v_mfma_f32_16x16x32_bf16 v[152:155], a[32:35], v[216:219], v[152:155]\n"
      "ds_read_b128 v[228:231], v235 offset:0\n"
      "v_add_u32 v235, v249, v235\n"
      "s_waitcnt vmcnt(26)\n"
      "s_barrier\n"
      "v_cvt_pk_bf16_f32 v124, v128, v129\n"
      "v_cvt_pk_bf16_f32 v125, v130, v131\n"
      "v_mfma_f32_16x16x32_bf16 v[156:159], a[36:39], v[216:219], v[156:159]\n"
      "ds_read_b128 v[36:39], v232 offset:0\n"
      "v_cvt_pk_bf16_f32 v126, v132, v133\n"
      "v_cvt_pk_bf16_f32 v127, v134, v135\n"
      "v_mfma_f32_16x16x32_bf16 v[160:163], a[40:43], v[216:219], v[160:163]\n"
      "v_cvt_pk_bf16_f32 v128, v136, v137\n"
      "v_cvt_pk_bf16_f32 v129, v138, v139\n"
      "v_mfma_f32_16x16x32_bf16 v[164:167], a[44:47], v[216:219], v[164:167]\n"
      "ds_read_b128 v[40:43], v233 offset:0\n"
      "v_cvt_pk_bf16_f32 v130, v140, v141\n"
      "v_cvt_pk_bf16_f32 v131, v142, v143\n"
      "global_load_dwordx4 a[12:15], v239, s[58:59] offset:3072 sc1\n"
      "s_waitcnt lgkmcnt(4)\n"
      "v_mfma_f32_16x16x32_bf16 v[168:171], a[24:27], v[220:223], v[168:171]\n"
      "v_cvt_pk_bf16_f32 v144, v144, v145\n"
      "v_cvt_pk_bf16_f32 v145, v146, v147\n"
      "v_mfma_f32_16x16x32_bf16 v[172:175], a[28:31], v[220:223], v[172:175]\n"
      "ds_read_b128 v[44:47], v234 offset:0\n"
      "v_cvt_pk_bf16_f32 v146, v148, v149\n"
      "v_cvt_pk_bf16_f32 v147, v150, v151\n"
      "v_mfma_f32_16x16x32_bf16 v[176:179], a[32:35], v[220:223], v[176:179]\n"
      "v_cvt_pk_bf16_f32 v148, v152, v153\n"
      "v_cvt_pk_bf16_f32 v149, v154, v155\n"
      "v_mfma_f32_16x16x32_bf16 v[180:183], a[36:39], v[220:223], v[180:183]\n"
      "ds_read_b128 v[60:63], v235 offset:0\n"
      "v_cvt_pk_bf16_f32 v150, v156, v157\n"
      "v_cvt_pk_bf16_f32 v151, v158, v159\n"
      "s_add_u32 s58, s24, 0xe000\n"
      "s_addc_u32 s59, s25, 0\n"
      "global_load_dwordx4 a[16:19], v239, s[58:59] offset:0 sc1\n"
      "v_mfma_f32_16x16x32_bf16 v[184:187], a[40:43], v[220:223], v[184:187]\n"
      "v_cvt_pk_bf16_f32 v152, v160, v161\n"
      "v_cvt_pk_bf16_f32 v153, v162, v163\n"
      "v_mfma_f32_16x16x32_bf16 v[188:191], a[44:47], v[220:223], v[188:191]\n"
      "ds_read_b128 v[64:67], v232 offset:8192\n"
      "v_cvt_pk_bf16_f32 v154, v164, v165\n"
      "v_cvt_pk_bf16_f32 v155, v166, v167\n"
      "s_waitcnt lgkmcnt(6)\n"
      "v_mfma_f32_16x16x32_bf16 v[192:195], a[24:27], v[224:227], v[192:195]\n"
      "v_cvt_pk_bf16_f32 v168, v168, v169\n"
      "v_cvt_pk_bf16_f32 v169, v170, v171\n"
      "v_mfma_f32_16x16x32_bf16 v[196:199], a[28:31], v[224:227], v[196:199]\n"
      "ds_read_b128 v[68:71], v233 offset:8192\n"
      "v_cvt_pk_bf16_f32 v170, v172, v173\n"
      "v_cvt_pk_bf16_f32 v171, v174, v175\n"
      "global_load_dwordx4 a[20:23], v239, s[58:59] offset:1024 sc1\n"
      "v_mfma_f32_16x16x32_bf16 v[200:203], a[32:35], v[224:227], v[200:203]\n"
      "v_cvt_pk_bf16_f32 v172, v176, v177\n"
      "v_cvt_pk_bf16_f32 v173, v178, v179\n"
      "v_mfma_f32_16x16x32_bf16 v[204:207], a[36:39], v[224:227], v[204:207]\n"
      "ds_read_b128 v[84:87], v234 offset:8192\n"
      "v_cvt_pk_bf16_f32 v174, v180, v181\n"
      "v_cvt_pk_bf16_f32 v175, v182, v183\n"
      "v_mfma_f32_16x16x32_bf16 v[208:211], a[40:43], v[224:227], v[208:211]\n"
      "v_cvt_pk_bf16_f32 v176, v184, v185\n"
      "v_cvt_pk_bf16_f32 v177, v186, v187\n"
      "v_mfma_f32_16x16x32_bf16 v[212:215], a[44:47], v[224:227], v[212:215]\n"
      "ds_read_b128 v[88:91], v235 offset:8192\n"
      "v_cvt_pk_bf16_f32 v178, v188, v189\n"
      "v_cvt_pk_bf16_f32 v179, v190, v191\n"
      "global_load_dwordx4 v[140:143], v239, s[58:59] offset:2048 sc1\n"
      "s_waitcnt lgkmcnt(8)\n"
      "v_mfma_f32_16x16x32_bf16 v[0:3], a[24:27], v[228:231], v[0:3]\n"
      "v_cvt_pk_bf16_f32 v192, v192, v193\n"
      "v_cvt_pk_bf16_f32 v193, v194, v195\n"
      "v_mfma_f32_16x16x32_bf16 v[4:7], a[28:31], v[228:231], v[4:7]\n"
      "ds_read_b128 v[92:95], v232 offset:16384\n"
      "v_cvt_pk_bf16_f32 v194, v196, v197\n"
      "v_cvt_pk_bf16_f32 v195, v198, v199\n"
      "v_mfma_f32_16x16x32_bf16 v[8:11], a[32:35], v[228:231], v[8:11]\n"
      "v_cvt_pk_bf16_f32 v196, v200, v201\n"
      "v_cvt_pk_bf16_f32 v197, v202, v203\n"
      "v_mfma_f32_16x16x32_bf16 v[12:15], a[36:39], v[228:231], v[12:15]\n"
      "ds_read_b128 v[108:111], v233 offset:16384\n"
      "v_cvt_pk_bf16_f32 v198, v204, v205\n"
      "v_cvt_pk_bf16_f32 v199, v206, v207\n"
      "global_load_dwordx4 v[156:159], v239, s[58:59] offset:3072 sc1\n"
      "v_mfma_f32_16x16x32_bf16 v[16:19], a[40:43], v[228:231], v[16:19]\n"
      "ds_read_b128 v[112:115], v234 offset:16384\n"
      "v_cvt_pk_bf16_f32 v200, v208, v209\n"
      "v_cvt_pk_bf16_f32 v201, v210, v211\n"
      "v_mfma_f32_16x16x32_bf16 v[20:23], a[44:47], v[228:231], v[20:23]\n"
      "ds_read_b128 v[116:119], v235 offset:16384\n"
      "v_cvt_pk_bf16_f32 v202, v212, v213\n"
      "v_cvt_pk_bf16_f32 v203, v214, v215\n"
      "s_waitcnt lgkmcnt(8)\n"
      "v_mfma_f32_16x16x32_bf16 v[132:135], a[240:243], v[36:39], v[0:3]\n"
      "s_mov_b64 vcc, 0\n"
      "v_mfma_f32_16x16x32_bf16 v[132:135], a[244:247], v[40:43], v[132:135]\n"
      "s_add_u32 s58, s24, 0xf000\n"
      "s_addc_u32 s59, s25, 0\n"
      "global_load_dwordx4 v[160:163], v239, s[58:59] offset:0 sc1\n"
      "v_mfma_f32_16x16x32_bf16 v[132:135], a[248:251], v[44:47], v[132:135]\n"
      "v_mfma_f32_16x16x32_bf16 v[132:135], a[252:255], v[60:63], v[132:135]\n"
      "s_waitcnt lgkmcnt(4)\n"
      "v_mfma_f32_16x16x32_bf16 v[132:135], a[48:51], v[64:67], v[132:135]\n"
      "global_load_dwordx4 v[164:167], v239, s[58:59] offset:1024 sc1\n"
      "v_mfma_f32_16x16x32_bf16 v[132:135], a[52:55], v[68:71], v[132:135]\n"
      "v_mfma_f32_16x16x32_bf16 v[132:135], a[56:59], v[84:87], v[132:135]\n"
      "v_mfma_f32_16x16x32_bf16 v[132:135], a[60:63], v[88:91], v[132:135]\n"
      "global_load_dwordx4 v[180:183], v239, s[58:59] offset:2048 sc1\n"
      "s_waitcnt lgkmcnt(0)\n"
      "v_mfma_f32_16x16x32_bf16 v[132:135], a[64:67], v[92:95], v[132:135]\n"
      "v_mfma_f32_16x16x32_bf16 v[132:135], a[68:71], v[108:111], v[132:135]\n"
      "v_mfma_f32_16x16x32_bf16 v[132:135], a[72:75], v[112:115], v[132:135]\n"
      "global_load_dwordx4 v[184:187], v239, s[58:59] offset:3072 sc1\n"
      "v_mfma_f32_16x16x32_bf16 v[132:135], a[76:79], v[116:119], v[132:135]\n"
      "v_mfma_f32_16x16x32_bf16 v[136:139], a[80:83], v[36:39], v[4:7]\n"
      "v_mfma_f32_16x16x32_bf16 v[136:139], a[84:87], v[40:43], v[136:139]\n"
      "s_add_u32 s58, s24, 0x10000\n"
      "s_addc_u32 s59, s25, 0\n"
      "global_load_dwordx4 v[188:191], v239, s[58:59] offset:0 sc1\n"
      "v_mfma_f32_16x16x32_bf16 v[136:139], a[88:91], v[44:47], v[136:139]\n"
      "v_mfma_f32_16x16x32_bf16 v[136:139], a[92:95], v[60:63], v[136:139]\n"
      "v_cvt_pk_bf16_f32 v208, v132, v133\n"
      "v_mfma_f32_16x16x32_bf16 v[136:139], a[96:99], v[64:67], v[136:139]\n"
      "global_load_dwordx4 v[204:207], v239, s[58:59] offset:1024 sc1\n"
      "v_mfma_f32_16x16x32_bf16 v[136:139], a[100:103], v[68:71], v[136:139]\n"
      "v_cvt_pk_bf16_f32 v209, v134, v135\n"
      "v_mfma_f32_16x16x32_bf16 v[136:139], a[104:107], v[84:87], v[136:139]\n"
      "v_mfma_f32_16x16x32_bf16 v[136:139], a[108:111], v[88:91], v[136:139]\n"
      "global_load_dwordx4 a[24:27], v239, s[58:59] offset:2048 sc1\n"
      "v_mfma_f32_16x16x32_bf16 v[136:139], a[112:115], v[92:95], v[136:139]\n"
      "v_mfma_f32_16x16x32_bf16 v[136:139], a[116:119], v[108:111], v[136:139]\n"
      "v_mfma_f32_16x16x32_bf16 v[136:139], a[120:123], v[112:115], v[136:139]\n"
      "global_load_dwordx4 a[28:31], v239, s[58:59] offset:3072 sc1\n"
      "v_mfma_f32_16x16x32_bf16 v[136:139], a[124:127], v[116:119], v[136:139]\n"
      "v_mfma_f32_16x16x32_bf16 v[132:135], a[128:131], v[36:39], v[8:11]\n"
      "v_mfma_f32_16x16x32_bf16 v[132:135], a[132:135], v[40:43], v[132:135]\n"
      "s_add_u32 s58, s24, 0x11000\n"
      "s_addc_u32 s59, s25, 0\n"
      "global_load_dwordx4 a[32:35], v239, s[58:59] offset:0 sc1\n"
      "v_mfma_f32_16x16x32_bf16 v[132:135], a[136:139], v[44:47], v[132:135]\n"
      "v_mfma_f32_16x16x32_bf16 v[132:135], a[140:143], v[60:63], v[132:135]\n"
      "v_cvt_pk_bf16_f32 v210, v136, v137\n"
      "v_mfma_f32_16x16x32_bf16 v[132:135], a[144:147], v[64:67], v[132:135]\n"
      "global_load_dwordx4 a[36:39], v239, s[58:59] offset:1024 sc1\n"
      "v_mfma_f32_16x16x32_bf16 v[132:135], a[148:151], v[68:71], v[132:135]\n"
      "v_cvt_pk_bf16_f32 v211, v138, v139\n"
      "v_mfma_f32_16x16x32_bf16 v[132:135], a[152:155], v[84:87], v[132:135]\n"
      "v_mfma_f32_16x16x32_bf16 v[132:135], a[156:159], v[88:91], v[132:135]\n"
      "global_load_dwordx4 a[40:43], v239, s[58:59] offset:2048 sc1\n"
      "s_waitcnt vmcnt(35)\n"
      "v_mfma_f32_16x16x32_bf16 v[132:135], a[160:163], v[92:95], v[132:135]\n"
      "v_mfma_f32_16x16x32_bf16 v[132:135], a[164:167], v[108:111], v[132:135]\n"
      "v_mfma_f32_16x16x32_bf16 v[132:135], a[168:171], v[112:115], v[132:135]\n"
      "global_load_dwordx4 a[44:47], v239, s[58:59] offset:3072 sc1\n"
      "v_mfma_f32_16x16x32_bf16 v[132:135], a[172:175], v[116:119], v[132:135]\n"
      "ds_write_b128 v244, v[208:211] offset:12288\n"
      "s_waitcnt vmcnt(32)\n"
      "v_mfma_f32_16x16x32_bf16 v[136:139], a[176:179], v[36:39], v[12:15]\n"
      "v_mfma_f32_16x16x32_bf16 v[136:139], a[180:183], v[40:43], v[136:139]\n"
      "s_mul_i32 s78, s90, s19\n"
      "v_mfma_f32_16x16x32_bf16 v[136:139], a[184:187], v[44:47], v[136:139]\n"
      "v_add_u32 v236, s78, v236\n"
      "v_mfma_f32_16x16x32_bf16 v[136:139], a[188:191], v[60:63], v[136:139]\n"
      "s_mul_i32 s78, s78, s99\n"
      "s_waitcnt vmcnt(28)\n"
      "v_mfma_f32_16x16x32_bf16 v[136:139], a[192:195], v[64:67], v[136:139]\n"
      "v_add_u32 v237, s78, v237\n"
      "v_mfma_f32_16x16x32_bf16 v[136:139], a[196:199], v[68:71], v[136:139]\n"
      "s_add_u32 s52, s20, s33\n"
      "s_addc_u32 s53, s21, 0\n"
      "s_add_u32 m0, s17, 0xc000\n"
      "v_mfma_f32_16x16x32_bf16 v[136:139], a[200:203], v[84:87], v[136:139]\n"
      "global_load_lds_dwordx4 v236, s[52:53] offset:0 \n"
      "v_mfma_f32_16x16x32_bf16 v[136:139], a[204:207], v[88:91], v[136:139]\n"
      "v_cvt_pk_bf16_f32 v212, v132, v133\n"
      "s_waitcnt vmcnt(25)\n"
      "v_mfma_f32_16x16x32_bf16 v[136:139], a[208:211], v[92:95], v[136:139]\n"
      "v_cvt_pk_bf16_f32 v213, v134, v135\n"
      "v_mfma_f32_16x16x32_bf16 v[136:139], a[212:215], v[108:111], v[136:139]\n"
      "v_mfma_f32_16x16x32_bf16 v[136:139], a[216:219], v[112:115], v[136:139]\n"
      "v_mfma_f32_16x16x32_bf16 v[136:139], a[220:223], v[116:119], v[136:139]\n"
      "s_add_u32 m0, s17, 0xdf00\n"
      "s_waitcnt vmcnt(21)\n"
      "v_mfma_f32_16x16x32_bf16 v[132:135], a[224:227], v[36:39], v[16:19]\n"
      "global_load_lds_dwordx4 v236, s[52:53] offset:256 \n"
      "v_mfma_f32_16x16x32_bf16 v[132:135], a[228:231], v[40:43], v[132:135]\n"
      "v_mfma_f32_16x16x32_bf16 v[132:135], a[232:235], v[44:47], v[132:135]\n"
      "v_mfma_f32_16x16x32_bf16 v[132:135], a[236:239], v[60:63], v[132:135]\n"
      "v_cvt_pk_bf16_f32 v214, v136, v137\n"
      "s_waitcnt vmcnt(18)\n"
      "v_mfma_f32_16x16x32_bf16 v[132:135], a[0:3], v[64:67], v[132:135]\n"
      "v_cvt_pk_bf16_f32 v215, v138, v139\n"
      "v_mfma_f32_16x16x32_bf16 v[132:135], a[4:7], v[68:71], v[132:135]\n"
      "s_add_u32 m0, s17, 0xfe00\n"
      "v_mfma_f32_16x16x32_bf16 v[132:135], a[8:11], v[84:87], v[132:135]\n"
      "global_load_lds_dwordx4 v236, s[52:53] offset:512 \n"
      "v_mfma_f32_16x16x32_bf16 v[132:135], a[12:15], v[88:91], v[132:135]\n"
      "s_waitcnt vmcnt(15)\n"
      "v_mfma_f32_16x16x32_bf16 v[132:135], a[16:19], v[92:95], v[132:135]\n"
      "v_mfma_f32_16x16x32_bf16 v[132:135], a[20:23], v[108:111], v[132:135]\n"
      "ds_write_b128 v245, v[212:215] offset:12288\n"
      "v_mfma_f32_16x16x32_bf16 v[132:135], v[140:143], v[112:115], v[132:135]\n"
      "v_mfma_f32_16x16x32_bf16 v[132:135], v[156:159], v[116:119], v[132:135]\n"
      "s_barrier\n"
      "s_add_u32 m0, s17, 0xd000\n"
      "s_waitcnt vmcnt(11)\n"
      "v_mfma_f32_16x16x32_bf16 v[136:139], v[160:163], v[36:39], v[20:23]\n"
      "ds_read_b128 v[36:39], v232 offset:256\n"
      "global_load_lds_dwordx4 v237, s[52:53] offset:0 \n"
      "v_mfma_f32_16x16x32_bf16 v[136:139], v[164:167], v[40:43], v[136:139]\n"
      "ds_read_b128 v[40:43], v233 offset:256\n"
      "v_mfma_f32_16x16x32_bf16 v[136:139], v[180:183], v[44:47], v[136:139]\n"
      "ds_read_b128 v[44:47], v234 offset:256\n"
      "v_mfma_f32_16x16x32_bf16 v[136:139], v[184:187], v[60:63], v[136:139]\n"
      "ds_read_b128 v[60:63], v235 offset:256\n"
      "v_cvt_pk_bf16_f32 v208, v132, v133\n"
      "s_waitcnt vmcnt(8)\n"
      "v_mfma_f32_16x16x32_bf16 v[136:139], v[188:191], v[64:67], v[136:139]\n"
      "ds_read_b128 v[64:67], v232 offset:8448\n"
      "v_cvt_pk_bf16_f32 v209, v134, v135\n"
      "v_mfma_f32_16x16x32_bf16 v[136:139], v[204:207], v[68:71], v[136:139]\n"
      "ds_read_b128 v[68:71], v233 offset:8448\n"
      "s_add_u32 m0, s17, 0xef00\n"
      "v_mfma_f32_16x16x32_bf16 v[136:139], a[24:27], v[84:87], v[136:139]\n"
      "ds_read_b128 v[84:87], v234 offset:8448\n"
      "global_load_lds_dwordx4 v237, s[52:53] offset:256 \n"
      "v_mfma_f32_16x16x32_bf16 v[136:139], a[28:31], v[88:91], v[136:139]\n"
      "ds_read_b128 v[88:91], v235 offset:8448\n"
      "s_waitcnt vmcnt(5)\n"
      "v_mfma_f32_16x16x32_bf16 v[136:139], a[32:35], v[92:95], v[136:139]\n"
      "ds_read_b128 v[92:95], v232 offset:16640\n"
      "v_mfma_f32_16x16x32_bf16 v[136:139], a[36:39], v[108:111], v[136:139]\n"
      "ds_read_b128 v[108:111], v233 offset:16640\n"
      "v_mfma_f32_16x16x32_bf16 v[136:139], a[40:43], v[112:115], v[136:139]\n"
      "ds_read_b128 v[112:115], v234 offset:16640\n"
      "v_mfma_f32_16x16x32_bf16 v[136:139], a[44:47], v[116:119], v[136:139]\n"
      "ds_read_b128 v[116:119], v235 offset:16640\n"
      "s_add_u32 m0, s17, 0x10e00\n"
      "s_waitcnt lgkmcnt(8)\n"
      "v_mfma_f32_16x16x32_bf16 v[132:135], a[240:243], v[36:39], v[0:3]\n"
      "global_load_lds_dwordx4 v237, s[52:53] offset:512 \n"
      "v_mfma_f32_16x16x32_bf16 v[132:135], a[244:247], v[40:43], v[132:135]\n"
      "v_mfma_f32_16x16x32_bf16 v[132:135], a[248:251], v[44:47], v[132:135]\n"
      "v_mfma_f32_16x16x32_bf16 v[132:135], a[252:255], v[60:63], v[132:135]\n"
      "v_cvt_pk_bf16_f32 v210, v136, v137\n"
      "s_waitcnt lgkmcnt(4)\n"
      "v_mfma_f32_16x16x32_bf16 v[132:135], a[48:51], v[64:67], v[132:135]\n"
      "v_cvt_pk_bf16_f32 v211, v138, v139\n"
      "v_mfma_f32_16x16x32_bf16 v[132:135], a[52:55], v[68:71], v[132:135]\n"
      "v_mfma_f32_16x16x32_bf16 v[132:135], a[56:59], v[84:87], v[132:135]\n"
      "s_add_u32 s52, s42, s33\n"
      "s_addc_u32 s53, s43, 0\n"
      "s_add_u32 m0, s17, 0x12000\n"
      "v_mfma_f32_16x16x32_bf16 v[132:135], a[60:63], v[88:91], v[132:135]\n"
      "global_load_lds_dwordx4 v236, s[52:53] offset:0 \n"
      "s_waitcnt lgkmcnt(0)\n"
      "v_mfma_f32_16x16x32_bf16 v[132:135], a[64:67], v[92:95], v[132:135]\n"
      "v_mfma_f32_16x16x32_bf16 v[132:135], a[68:71], v[108:111], v[132:135]\n"
      "ds_write_b128 v244, v[208:211] offset:12416\n"
      "v_mfma_f32_16x16x32_bf16 v[132:135], a[72:75], v[112:115], v[132:135]\n"
      "LPD3:\n"
      "s_cbranch_execz LPD9\n"
      "v_mfma_f32_16x16x32_bf16 v[132:135], a[76:79], v[116:119], v[132:135]\n"
      "v_mfma_f32_16x16x32_bf16 v[136:139], a[80:83], v[36:39], v[4:7]\n"
      "s_add_u32 m0, s17, 0x13f00\n"
      "v_mfma_f32_16x16x32_bf16 v[136:139], a[84:87], v[40:43], v[136:139]\n"
      "global_load_lds_dwordx4 v236, s[52:53] offset:256 \n"
      "v_mfma_f32_16x16x32_bf16 v[136:139], a[88:91], v[44:47], v[136:139]\n"
      "v_mfma_f32_16x16x32_bf16 v[136:139], a[92:95], v[60:63], v[136:139]\n"
      "v_cvt_pk_bf16_f32 v208, v132, v133\n"
      "v_mfma_f32_16x16x32_bf16 v[136:139], a[96:99], v[64:67], v[136:139]\n"
      "v_cvt_pk_bf16_f32 v209, v134, v135\n"
      "v_mfma_f32_16x16x32_bf16 v[136:139], a[100:103], v[68:71], v[136:139]\n"
      "v_mfma_f32_16x16x32_bf16 v[136:139], a[104:107], v[84:87], v[136:139]\n"
      "s_add_u32 m0, s17, 0x15e00\n"
      "v_mfma_f32_16x16x32_bf16 v[136:139], a[108:111], v[88:91], v[136:139]\n"
      "global_load_lds_dwordx4 v236, s[52:53] offset:512 \n"
      "v_mfma_f32_16x16x32_bf16 v[136:139], a[112:115], v[92:95], v[136:139]\n"
      "v_mfma_f32_16x16x32_bf16 v[136:139], a[116:119], v[108:111], v[136:139]\n"
      "v_mfma_f32_16x16x32_bf16 v[136:139], a[120:123], v[112:115], v[136:139]\n"
      "v_mfma_f32_16x16x32_bf16 v[136:139], a[124:127], v[116:119], v[136:139]\n"
      "v_mfma_f32_16x16x32_bf16 v[132:135], a[128:131], v[36:39], v[8:11]\n"
      "s_add_u32 m0, s17, 0x13000\n"
      "v_mfma_f32_16x16x32_bf16 v[132:135], a[132:135], v[40:43], v[132:135]\n"
      "global_load_lds_dwordx4 v237, s[52:53] offset:0 \n"
      "v_mfma_f32_16x16x32_bf16 v[132:135], a[136:139], v[44:47], v[132:135]\n"
      "v_mfma_f32_16x16x32_bf16 v[132:135], a[140:143], v[60:63], v[132:135]\n"
      "v_cvt_pk_bf16_f32 v210, v136, v137\n"
      "v_mfma_f32_16x16x32_bf16 v[132:135], a[144:147], v[64:67], v[132:135]\n"
      "v_cvt_pk_bf16_f32 v211, v138, v139\n"
      "v_mfma_f32_16x16x32_bf16 v[132:135], a[148:151], v[68:71], v[132:135]\n"
      "v_mfma_f32_16x16x32_bf16 v[132:135], a[152:155], v[84:87], v[132:135]\n"
      "s_add_u32 m0, s17, 0x14f00\n"
      "v_mfma_f32_16x16x32_bf16 v[132:135], a[156:159], v[88:91], v[132:135]\n"
      "global_load_lds_dwordx4 v237, s[52:53] offset:256 \n"
      "v_mfma_f32_16x16x32_bf16 v[132:135], a[160:163], v[92:95], v[132:135]\n"
      "v_mfma_f32_16x16x32_bf16 v[132:135], a[164:167], v[108:111], v[132:135]\n"
      "ds_write_b128 v244, v[208:211] offset:24576\n"
      "v_mfma_f32_16x16x32_bf16 v[132:135], a[168:171], v[112:115], v[132:135]\n"
      "v_mfma_f32_16x16x32_bf16 v[132:135], a[172:175], v[116:119], v[132:135]\n"
      "v_mfma_f32_16x16x32_bf16 v[136:139], a[176:179], v[36:39], v[12:15]\n"
      "s_add_u32 m0, s17, 0x16e00\n"
      "v_mfma_f32_16x16x32_bf16 v[136:139], a[180:183], v[40:43], v[136:139]\n"
      "global_load_lds_dwordx4 v237, s[52:53] offset:512 \n"
      "v_mfma_f32_16x16x32_bf16 v[136:139], a[184:187], v[44:47], v[136:139]\n"
      "v_mfma_f32_16x16x32_bf16 v[136:139], a[188:191], v[60:63], v[136:139]\n"
      "v_cvt_pk_bf16_f32 v212, v132, v133\n"
      "v_mfma_f32_16x16x32_bf16 v[136:139], a[192:195], v[64:67], v[136:139]\n"
      "v_cvt_pk_bf16_f32 v213, v134, v135\n"
      "v_mfma_f32_16x16x32_bf16 v[136:139], a[196:199], v[68:71], v[136:139]\n"
      "v_mfma_f32_16x16x32_bf16 v[136:139], a[200:203], v[84:87], v[136:139]\n"
      "v_mfma_f32_16x16x32_bf16 v[136:139], a[204:207], v[88:91], v[136:139]\n"
      "v_mfma_f32_16x16x32_bf16 v[136:139], a[208:211], v[92:95], v[136:139]\n"
      "v_mfma_f32_16x16x32_bf16 v[136:139], a[212:215], v[108:111], v[136:139]\n"
      "v_mfma_f32_16x16x32_bf16 v[136:139], a[216:219], v[112:115], v[136:139]\n"
      "v_mfma_f32_16x16x32_bf16 v[136:139], a[220:223], v[116:119], v[136:139]\n"
      "v_mfma_f32_16x16x32_bf16 v[132:135], a[224:227], v[36:39], v[16:19]\n"
      "v_mfma_f32_16x16x32_bf16 v[132:135], a[228:231], v[40:43], v[132:135]\n"
      "v_mfma_f32_16x16x32_bf16 v[132:135], a[232:235], v[44:47], v[132:135]\n"
      "v_mfma_f32_16x16x32_bf16 v[132:135], a[236:239], v[60:63], v[132:135]\n"
      "v_cvt_pk_bf16_f32 v214, v136, v137\n"
      "v_mfma_f32_16x16x32_bf16 v[132:135], a[0:3], v[64:67], v[132:135]\n"
      "v_cvt_pk_bf16_f32 v215, v138, v139\n"
      "v_mfma_f32_16x16x32_bf16 v[132:135], a[4:7], v[68:71], v[132:135]\n"
      "v_mfma_f32_16x16x32_bf16 v[132:135], a[8:11], v[84:87], v[132:135]\n"
      "v_mfma_f32_16x16x32_bf16 v[132:135], a[12:15], v[88:91], v[132:135]\n"
      "v_mfma_f32_16x16x32_bf16 v[132:135], a[16:19], v[92:95], v[132:135]\n"
      "v_mfma_f32_16x16x32_bf16 v[132:135], a[20:23], v[108:111], v[132:135]\n"
      "ds_write_b128 v245, v[212:215] offset:24576\n"
      "v_mfma_f32_16x16x32_bf16 v[132:135], v[140:143], v[112:115], v[132:135]\n"
      "s_lshl_b32 s79, s98, 1\n"
      "v_mfma_f32_16x16x32_bf16 v[132:135], v[156:159], v[116:119], v[132:135]\n"
      "s_waitcnt lgkmcnt(2)\n"
      "s_barrier\n"
      "v_mad_i32_i24 v252, v251, s79, v252\n"
      "v_mfma_f32_16x16x32_bf16 v[136:139], v[160:163], v[36:39], v[20:23]\n"
      "ds_read_b128 v[36:39], v232 offset:24576\n"
      "s_mul_i32 s79, s79, s100\n"
      "v_mfma_f32_16x16x32_bf16 v[136:139], v[164:167], v[40:43], v[136:139]\n"
      "ds_read_b128 v[40:43], v233 offset:24576\n"
      "v_add_u32 v247, s79, v247\n"
      "v_mfma_f32_16x16x32_bf16 v[136:139], v[180:183], v[44:47], v[136:139]\n"
      "ds_read_b128 v[44:47], v234 offset:24576\n"
      "s_mul_i32 s79, s79, s101\n"
      "v_mfma_f32_16x16x32_bf16 v[136:139], v[184:187], v[60:63], v[136:139]\n"
      "ds_read_b128 v[60:63], v235 offset:24576\n"
      "v_cvt_pk_bf16_f32 v208, v132, v133\n"
      "v_mfma_f32_16x16x32_bf16 v[136:139], v[188:191], v[64:67], v[136:139]\n"
      "ds_read_b128 v[64:67], v232 offset:32768\n"
      "v_cvt_pk_bf16_f32 v209, v134, v135\n"
      "v_mfma_f32_16x16x32_bf16 v[136:139], v[204:207], v[68:71], v[136:139]\n"
      "ds_read_b128 v[68:71], v233 offset:32768\n"
      "v_add_u32 v253, s79, v253\n"
      "v_mfma_f32_16x16x32_bf16 v[136:139], a[24:27], v[84:87], v[136:139]\n"
      "ds_read_b128 v[84:87], v234 offset:32768\n"
      "v_mfma_f32_16x16x32_bf16 v[136:139], a[28:31], v[88:91], v[136:139]\n"
      "ds_read_b128 v[88:91], v235 offset:32768\n"
      "v_mfma_f32_16x16x32_bf16 v[136:139], a[32:35], v[92:95], v[136:139]\n"
      "ds_read_b128 v[92:95], v232 offset:40960\n"
      "v_mfma_f32_16x16x32_bf16 v[136:139], a[36:39], v[108:111], v[136:139]\n"
      "ds_read_b128 v[108:111], v233 offset:40960\n"
      "v_mfma_f32_16x16x32_bf16 v[136:139], a[40:43], v[112:115], v[136:139]\n"
      "ds_read_b128 v[112:115], v234 offset:40960\n"
      "v_mfma_f32_16x16x32_bf16 v[136:139], a[44:47], v[116:119], v[136:139]\n"
      "ds_read_b128 v[116:119], v235 offset:40960\n"
      "s_branch LJOIN\n"
      "LF_1:\n"
      "s_mul_i32 s62, s62, 0x3000\n"
      "s_sub_u32 s62, s98, s62\n"
      "s_sub_u32 s62, s62, s89\n"
      "v_mad_i32_i24 v238, v251, s62, v238\n"
      "v_mad_i32_i24 v238, v251, s98, v238\n"
      "s_add_u32 s56, s22, 0x0\n"
      "s_addc_u32 s57, s23, 0\n"
      "s_add_u32 m0, s18, 0x0\n"
      "s_nop 0\n"
      "global_load_lds_dwordx4 v238, s[56:57] offset:0 \n"
      "v_mad_i32_i24 v238, v251, s90, v238\n"
      "s_add_u32 s56, s22, 0x6000\n"
      "s_addc_u32 s57, s23, 0\n"
      "s_add_u32 m0, s18, 0x1000\n"
      "s_nop 0\n"
      "global_load_lds_dwordx4 v238, s[56:57] offset:0 \n"
      "v_mad_i32_i24 v238, v251, s91, v238\n"
      "s_add_u32 s56, s22, 0xc000\n"
      "s_addc_u32 s57, s23, 0\n"
      "s_add_u32 m0, s18, 0x2000\n"
      "s_nop 0\n"
      "global_load_lds_dwordx4 v238, s[56:57] offset:0 \n"
      "v_mad_i32_i24 v238, v251, s92, v238\n"
      "s_add_u32 s56, s22, 0x12000\n"
      "s_addc_u32 s57, s23, 0\n"
      "s_add_u32 m0, s18, 0x3000\n"
      "s_nop 0\n"
      "global_load_lds_dwordx4 v238, s[56:57] offset:0 \n"
      "v_mad_i32_i24 v238, v251, s93, v238\n"
      "s_add_u32 s56, s22, 0x18000\n"
      "s_addc_u32 s57, s23, 0\n"
      "s_add_u32 m0, s18, 0x4000\n"
      "s_nop 0\n"
      "global_load_lds_dwordx4 v238, s[56:57] offset:0 \n"
      "v_mad_i32_i24 v238, v251, s94, v238\n"
      "s_add_u32 s56, s22, 0x1e000\n"
      "s_addc_u32 s57, s23, 0\n"
      "s_add_u32 m0, s18, 0x5000\n"
      "s_nop 0\n"
      "global_load_lds_dwordx4 v238, s[56:57] offset:0 \n"
      "v_mad_i32_i24 v238, v251, s95, v238\n"
      "s_add_u32 s56, s22, 0x24000\n"
      "s_addc_u32 s57, s23, 0\n"
      "s_add_u32 m0, s18, 0x6000\n"
      "s_nop 0\n"
      "global_load_lds_dwordx4 v238, s[56:57] offset:0 \n"
      "v_mad_i32_i24 v238, v251, s96, v238\n"
      "s_add_u32 s56, s22, 0x2a000\n"
      "s_addc_u32 s57, s23, 0\n"
      "s_add_u32 m0, s18, 0x7000\n"
      "s_nop 0\n"
      "global_load_lds_dwordx4 v238, s[56:57] offset:0 \n"
      "v_mad_i32_i24 v238, v251, s97, v238\n"
      "s_add_u32 s56, s22, 0x30000\n"
      "s_addc_u32 s57, s23, 0\n"
      "s_add_u32 m0, s18, 0x8000\n"
      "s_nop 0\n"
      "global_load_lds_dwordx4 v238, s[56:57] offset:0 \n"
      "s_branch LR_1\n"
      "LV1:\n"
      "s_add_u32 s78, s64, 0x0\n"
      "s_addc_u32 s79, s65, 0\n"
      "s_add_u32 m0, s74, 0x0\n"
      "s_nop 0\n"
      "global_load_lds_dwordx4 v240, s[78:79]\n"
      "s_add_u32 m0, s74, 0x400\n"
      "s_nop 0\n"
      "global_load_lds_dwordx4 v241, s[78:79]\n"
      "s_add_u32 m0, s74, 0x800\n"
      "s_nop 0\n"
      "global_load_lds_dwordx4 v242, s[78:79]\n"
      "s_add_u32 s78, s64, 0x3000\n"
      "s_addc_u32 s79, s65, 0\n"
      "s_add_u32 m0, s74, 0xc00\n"
      "s_nop 0\n"
      "global_load_lds_dwordx4 v240, s[78:79]\n"
      "s_add_u32 m0, s74, 0x1000\n"
      "s_nop 0\n"
      "global_load_lds_dwordx4 v241, s[78:79]\n"
      "s_add_u32 m0, s74, 0x1400\n"
      "s_nop 0\n"
      "global_load_lds_dwordx4 v242, s[78:79]\n"
      "s_getpc_b64 s[56:57]\n"
      "LTR_1:\n"
      "s_add_u32 s56, s56, LTE_1-LTR_1\n"
      "s_addc_u32 s57, s57, 0\n"
      "s_mov_b64 exec, 0\n"
      "s_cmp_eq_u32 s76, 3\n"
      "s_cbranch_scc1 LPD6\n"
      "s_cmp_eq_u32 s76, 2\n"
      "s_cbranch_scc1 LPD5\n"
      "s_cmp_eq_u32 s76, 1\n"
      "s_cbranch_scc1 LPD4\n"
      "LTE_1:\n"
      "s_mov_b64 exec, -1\n"
      "s_waitcnt vmcnt(17)\n"
      "v_readfirstlane_b32 s80, v252\n"
      "v_readfirstlane_b32 s81, v253\n"
      "s_nop 3\n"
      "s_add_u32 s80, s80, 0x7f4a7c15\n"
      "s_addc_u32 s81, s81, 0x9e3779b9\n"
      "v_mov_b32 v252, s80\n"
      "v_mov_b32 v253, s81\n"
      "v_xor_b32 v116, v112, v110\n"
      "v_xor_b32 v117, v113, v111\n"
      "v_and_or_b32 v116, v116, -2, v117\n"
      "v_cmp_eq_u32 vcc, 0, v116\n"
      "v_cndmask_b32 v116, v110, v112, vcc\n"
      "s_mov_b64 exec, 1\n"
      "ds_write_b32 v108, v116\n"
      "s_mov_b64 exec, -1\n"
      "v_and_b32 v108, -16, v108\n"
      "s_waitcnt lgkmcnt(0)\n"
      "s_barrier\n"
      "ds_read_b32 v116, v108\n"
      "s_waitcnt lgkmcnt(0)\n"
      "v_readfirstlane_b32 s62, v116\n"
      "s_lshr_b32 s63, s51, 2\n"
      "s_nop 2\n"
      "s_xor_b32 s62, s62, s63\n"
      "s_and_b32 s62, s62, 1\n"
      "s_mul_i32 s63, s62, 0x3000\n"
      "s_sub_u32 s98, s98, s63\n"
      "s_lshl_b32 s62, s62, 1\n"
      "s_sub_u32 s62, 1, s62\n"
      "v_mul_i32_i24 v251, s62, v251\n"
      "s_lshl_b32 s19, s62, 1\n"
      "s_mul_i32 s100, s100, s62\n"
      "v_cmp_ne_u32 vcc, v107, v251\n"
      "s_nop 3\n"
      "s_cbranch_vccnz LF_1\n"
      "LR_1:\n"
      "s_nop 0\n"
      "s_add_u32 s78, s64, 0x6000\n"
      "s_addc_u32 s79, s65, 0\n"
      "s_add_u32 m0, s74, 0x6000\n"
      "s_nop 0\n"
      "global_load_lds_dwordx4 v240, s[78:79]\n"
      "s_add_u32 m0, s74, 0x6400\n"
      "s_nop 0\n"
      "global_load_lds_dwordx4 v241, s[78:79]\n"
      "s_add_u32 m0, s74, 0x6800\n"
      "s_nop 0\n"
      "global_load_lds_dwordx4 v242, s[78:79]\n"
      "s_add_u32 s78, s64, 0x9000\n"
      "s_addc_u32 s79, s65, 0\n"
      "s_add_u32 m0, s74, 0x6c00\n"
      "s_nop 0\n"
      "global_load_lds_dwordx4 v240, s[78:79]\n"
      "s_add_u32 m0, s74, 0x7000\n"
      "s_nop 0\n"
      "global_load_lds_dwordx4 v241, s[78:79]\n"
      "s_add_u32 m0, s74, 0x7400\n"
      "s_nop 0\n"
      "global_load_lds_dwordx4 v242, s[78:79]\n"
      "s_add_u32 s78, s64, 0xc000\n"
      "s_addc_u32 s79, s65, 0\n"
      "s_add_u32 m0, s74, 0xc000\n"
      "s_nop 0\n"
      "global_load_lds_dwordx4 v240, s[78:79]\n"
      "s_add_u32 m0, s74, 0xc400\n"
      "s_nop 0\n"
      "global_load_lds_dwordx4 v241, s[78:79]\n"
      "s_add_u32 m0, s74, 0xc800\n"
      "s_nop 0\n"
      "global_load_lds_dwordx4 v242, s[78:79]\n"
      "s_add_u32 s78, s64, 0xf000\n"
      "s_addc_u32 s79, s65, 0\n"
      "s_add_u32 m0, s74, 0xcc00\n"
      "s_nop 0\n"
      "global_load_lds_dwordx4 v240, s[78:79]\n"
      "s_add_u32 m0, s74, 0xd000\n"
      "s_nop 0\n"
      "global_load_lds_dwordx4 v241, s[78:79]\n"
      "s_add_u32 m0, s74, 0xd400\n"
      "s_nop 0\n"
      "global_load_lds_dwordx4 v242, s[78:79]\n"
      "s_add_u32 m0, s17, 0x25000\n"
      "s_nop 0\n"
      "global_load_lds_dwordx4 v239, s[46:47] offset:0\n"
      "s_add_u32 m0, s17, 0x25800\n"
      "s_nop 0\n"
      "global_load_lds_dwordx4 v239, s[46:47] offset:2048\n"
      "s_waitcnt vmcnt(14)\n"
      "ds_read_b64_tr_b16 a[0:1], v243 offset:0\n"
      "ds_read_b64_tr_b16 a[2:3], v243 offset:768\n"
      "ds_read_b64_tr_b16 a[4:5], v243 offset:8\n"
      "ds_read_b64_tr_b16 a[6:7], v243 offset:776\n"
      "ds_read_b64_tr_b16 a[8:9], v243 offset:64\n"
      "ds_read_b64_tr_b16 a[10:11], v243 offset:832\n"
      "ds_read_b64_tr_b16 a[12:13], v243 offset:72\n"
      "ds_read_b64_tr_b16 a[14:15], v243 offset:840\n"
      "ds_read_b64_tr_b16 a[16:17], v243 offset:128\n"
      "ds_read_b64_tr_b16 a[18:19], v243 offset:896\n"
      "ds_read_b64_tr_b16 a[20:21], v243 offset:136\n"
      "ds_read_b64_tr_b16 a[22:23], v243 offset:904\n"
      "s_waitcnt vmcnt(13)\n"
      "s_barrier\n"
      "ds_read_b128 v[216:219], v232 offset:0\n"
      "ds_read_b128 v[220:223], v232 offset:4096\n"
      "ds_read_b128 v[224:227], v232 offset:8192\n"
      "s_waitcnt lgkmcnt(2)\n"
      "v_mfma_f32_16x16x32_bf16 v[0:3], a[0:3], v[216:219], 0\n"
      "s_add_u32 s78, s64, 0x12000\n"
      "s_addc_u32 s79, s65, 0\n"
      "s_add_u32 m0, s74, 0x0\n"
      "v_mfma_f32_16x16x32_bf16 v[4:7], a[4:7], v[216:219], 0\n"
      "global_load_lds_dwordx4 v240, s[78:79]\n"
      "v_mfma_f32_16x16x32_bf16 v[8:11], a[8:11], v[216:219], 0\n"
      "ds_read_b128 v[228:231], v232 offset:12288\n"
      "v_mfma_f32_16x16x32_bf16 v[12:15], a[12:15], v[216:219], 0\n"
      "v_mfma_f32_16x16x32_bf16 v[16:19], a[16:19], v[216:219], 0\n"
      "s_add_u32 m0, s74, 0x400\n"
      "v_mfma_f32_16x16x32_bf16 v[20:23], a[20:23], v[216:219], 0\n"
      "global_load_lds_dwordx4 v241, s[78:79]\n"
      "s_waitcnt lgkmcnt(2)\n"
      "v_mfma_f32_16x16x32_bf16 v[24:27], a[0:3], v[220:223], 0\n"
      "s_waitcnt vmcnt(10)\n"
      "s_cmp_lg_u32 s76, 1\n"
      "s_cbranch_scc1 LA_1\n"
      "v_mov_b32 v244, s77\n"
      "s_mov_b64 exec, 1\n"
      "global_store_dwordx2 v244, v[252:253], s[28:29] sc1\n"
      "s_mov_b64 exec, -1\n"
      "s_nop 4\n"
      "LA_1:\n"
      "v_mfma_f32_16x16x32_bf16 v[28:31], a[4:7], v[220:223], 0\n"
      "ds_read_b64_tr_b16 a[24:25], v243 offset:24576\n"
      "v_mfma_f32_16x16x32_bf16 v[32:35], a[8:11], v[220:223], 0\n"
      "ds_read_b128 v[216:219], v232 offset:16384\n"
      "s_add_u32 m0, s74, 0x800\n"
      "v_mfma_f32_16x16x32_bf16 v[36:39], a[12:15], v[220:223], 0\n"
      "ds_read_b64_tr_b16 a[26:27], v243 offset:25344\n"
      "global_load_lds_dwordx4 v242, s[78:79]\n"
      "v_mfma_f32_16x16x32_bf16 v[40:43], a[16:19], v[220:223], 0\n"
      "ds_read_b64_tr_b16 a[28:29], v243 offset:24584\n"
      "v_mfma_f32_16x16x32_bf16 v[44:47], a[20:23], v[220:223], 0\n"
      "s_waitcnt lgkmcnt(5)\n"
      "v_mfma_f32_16x16x32_bf16 v[48:51], a[0:3], v[224:227], 0\n"
      "ds_read_b64_tr_b16 a[30:31], v243 offset:25352\n"
      "s_add_u32 s78, s64, 0x15000\n"
      "s_addc_u32 s79, s65, 0\n"
      "s_add_u32 m0, s74, 0xc00\n"
      "v_mfma_f32_16x16x32_bf16 v[52:55], a[4:7], v[224:227], 0\n"
      "ds_read_b64_tr_b16 a[32:33], v243 offset:24640\n"
      "global_load_lds_dwordx4 v240, s[78:79]\n"
      "v_mfma_f32_16x16x32_bf16 v[56:59], a[8:11], v[224:227], 0\n"
      "ds_read_b128 v[220:223], v232 offset:20480\n"
      "v_mfma_f32_16x16x32_bf16 v[60:63], a[12:15], v[224:227], 0\n"
      "ds_read_b64_tr_b16 a[34:35], v243 offset:25408\n"
      "v_mfma_f32_16x16x32_bf16 v[64:67], a[16:19], v[224:227], 0\n"
      "ds_read_b64_tr_b16 a[36:37], v243 offset:24648\n"
      "s_add_u32 m0, s74, 0x1000\n"
      "v_mfma_f32_16x16x32_bf16 v[68:71], a[20:23], v[224:227], 0\n"
      "global_load_lds_dwordx4 v241, s[78:79]\n"
      "s_waitcnt lgkmcnt(9)\n"
      "v_mfma_f32_16x16x32_bf16 v[72:75], a[0:3], v[228:231], 0\n"
      "ds_read_b64_tr_b16 a[38:39], v243 offset:25416\n"
      "v_mfma_f32_16x16x32_bf16 v[76:79], a[4:7], v[228:231], 0\n"
      "ds_read_b64_tr_b16 a[40:41], v243 offset:24704\n"
      "v_mfma_f32_16x16x32_bf16 v[80:83], a[8:11], v[228:231], 0\n"
      "ds_read_b128 v[224:227], v232 offset:24576\n"
      "s_add_u32 m0, s74, 0x1400\n"
      "v_mfma_f32_16x16x32_bf16 v[84:87], a[12:15], v[228:231], 0\n"
      "ds_read_b64_tr_b16 a[42:43], v243 offset:25472\n"
      "global_load_lds_dwordx4 v242, s[78:79]\n"
      "v_mfma_f32_16x16x32_bf16 v[88:91], a[16:19], v[228:231], 0\n"
      "ds_read_b64_tr_b16 a[44:45], v243 offset:24712\n"
      "v_mfma_f32_16x16x32_bf16 v[92:95], a[20:23], v[228:231], 0\n"
      "s_waitcnt lgkmcnt(12)\n"
      "v_mfma_f32_16x16x32_bf16 v[96:99], a[0:3], v[216:219], 0\n"
      "ds_read_b64_tr_b16 a[46:47], v243 offset:25480\n"
      "v_mfma_f32_16x16x32_bf16 v[100:103], a[4:7], v[216:219], 0\n"
      "s_waitcnt vmcnt(8)\n"
      "v_mfma_f32_16x16x32_bf16 v[104:107], a[8:11], v[216:219], 0\n"
      "ds_read_b128 v[228:231], v232 offset:28672\n"
      "v_mfma_f32_16x16x32_bf16 v[108:111], a[12:15], v[216:219], 0\n"
      "ds_read_b64_tr_b16 a[48:49], v243 offset:49152\n"
      "v_mfma_f32_16x16x32_bf16 v[112:115], a[16:19], v[216:219], 0\n"
      "ds_read_b64_tr_b16 a[50:51], v243 offset:49920\n"
      "v_mfma_f32_16x16x32_bf16 v[116:119], a[20:23], v[216:219], 0\n"
      "s_waitcnt lgkmcnt(11)\n"
      "v_mfma_f32_16x16x32_bf16 v[120:123], a[0:3], v[220:223], 0\n"
      "ds_read_b64_tr_b16 a[52:53], v243 offset:49160\n"
      "v_mfma_f32_16x16x32_bf16 v[124:127], a[4:7], v[220:223], 0\n"
      "ds_read_b64_tr_b16 a[54:55], v243 offset:49928\n"
      "v_mfma_f32_16x16x32_bf16 v[128:131], a[8:11], v[220:223], 0\n"
      "ds_read_b128 v[216:219], v232 offset:32768\n"
      "v_add_u32 v232, 0x9000, v232\n"
      "v_mfma_f32_16x16x32_bf16 v[132:135], a[12:15], v[220:223], 0\n"
      "ds_read_b64_tr_b16 a[56:57], v243 offset:49216\n"
      "v_mfma_f32_16x16x32_bf16 v[136:139], a[16:19], v[220:223], 0\n"
      "ds_read_b64_tr_b16 a[58:59], v243 offset:49984\n"
      "s_waitcnt lgkmcnt(8)\n"
      "s_add_u32 s78, s64, 0x18000\n"
      "s_addc_u32 s79, s65, 0\n"
      "s_add_u32 m0, s74, 0x6000\n"
      "v_mfma_f32_16x16x32_bf16 v[140:143], a[20:23], v[220:223], 0\n"
      "global_load_lds_dwordx4 v240, s[78:79]\n"
      "v_mfma_f32_16x16x32_bf16 v[144:147], a[0:3], v[224:227], 0\n"
      "ds_read_b64_tr_b16 a[60:61], v243 offset:49224\n"
      "v_mfma_f32_16x16x32_bf16 v[148:151], a[4:7], v[224:227], 0\n"
      "ds_read_b64_tr_b16 a[62:63], v243 offset:49992\n"
      "v_mfma_f32_16x16x32_bf16 v[152:155], a[8:11], v[224:227], 0\n"
      "ds_read_b128 v[220:223], v233 offset:0\n"
      "s_add_u32 m0, s74, 0x6400\n"
      "v_mfma_f32_16x16x32_bf16 v[156:159], a[12:15], v[224:227], 0\n"
      "ds_read_b64_tr_b16 a[64:65], v243 offset:49280\n"
      "global_load_lds_dwordx4 v241, s[78:79]\n"
      "v_mfma_f32_16x16x32_bf16 v[160:163], a[16:19], v[224:227], 0\n"
      "ds_read_b64_tr_b16 a[66:67], v243 offset:50048\n"
      "v_mfma_f32_16x16x32_bf16 v[164:167], a[20:23], v[224:227], 0\n"
      "s_waitcnt lgkmcnt(12)\n"
      "v_mfma_f32_16x16x32_bf16 v[168:171], a[0:3], v[228:231], 0\n"
      "ds_read_b64_tr_b16 a[68:69], v243 offset:49288\n"
      "s_add_u32 m0, s74, 0x6800\n"
      "v_mfma_f32_16x16x32_bf16 v[172:175], a[4:7], v[228:231], 0\n"
      "ds_read_b64_tr_b16 a[70:71], v243 offset:50056\n"
      "global_load_lds_dwordx4 v242, s[78:79]\n"
      "v_mfma_f32_16x16x32_bf16 v[176:179], a[8:11], v[228:231], 0\n"
      "ds_read_b128 v[224:227], v233 offset:4096\n"
      "v_mfma_f32_16x16x32_bf16 v[180:183], a[12:15], v[228:231], 0\n"
      "v_mfma_f32_16x16x32_bf16 v[184:187], a[16:19], v[228:231], 0\n"
      "s_add_u32 s78, s64, 0x1b000\n"
      "s_addc_u32 s79, s65, 0\n"
      "s_add_u32 m0, s74, 0x6c00\n"
      "v_mfma_f32_16x16x32_bf16 v[188:191], a[20:23], v[228:231], 0\n"
      "global_load_lds_dwordx4 v240, s[78:79]\n"
      "s_waitcnt lgkmcnt(10)\n"
      "v_mfma_f32_16x16x32_bf16 v[192:195], a[0:3], v[216:219], 0\n"
      "v_mfma_f32_16x16x32_bf16 v[196:199], a[4:7], v[216:219], 0\n"
      "v_mfma_f32_16x16x32_bf16 v[200:203], a[8:11], v[216:219], 0\n"
      "ds_read_b128 v[228:231], v233 offset:8192\n"
      "s_add_u32 m0, s74, 0x7000\n"
      "v_mfma_f32_16x16x32_bf16 v[204:207], a[12:15], v[216:219], 0\n"
      "global_load_lds_dwordx4 v241, s[78:79]\n"
      "v_mfma_f32_16x16x32_bf16 v[208:211], a[16:19], v[216:219], 0\n"
      "v_mfma_f32_16x16x32_bf16 v[212:215], a[20:23], v[216:219], 0\n"
      "s_waitcnt lgkmcnt(6)\n"
      "v_mfma_f32_16x16x32_bf16 v[0:3], a[24:27], v[220:223], v[0:3]\n"
      "s_add_u32 m0, s74, 0x7400\n"
      "v_mfma_f32_16x16x32_bf16 v[4:7], a[28:31], v[220:223], v[4:7]\n"
      "global_load_lds_dwordx4 v242, s[78:79]\n"
      "v_mfma_f32_16x16x32_bf16 v[8:11], a[32:35], v[220:223], v[8:11]\n"
      "ds_read_b128 v[216:219], v233 offset:12288\n"
      "s_waitcnt vmcnt(6)\n"
      "v_mfma_f32_16x16x32_bf16 v[12:15], a[36:39], v[220:223], v[12:15]\n"
      "ds_read_b64_tr_b16 a[72:73], v243 offset:0\n"
      "v_mfma_f32_16x16x32_bf16 v[16:19], a[40:43], v[220:223], v[16:19]\n"
      "ds_read_b64_tr_b16 a[74:75], v243 offset:768\n"
      "v_mad_i32_i24 v238, v251, s89, v238\n"
      "s_add_u32 s56, s22, 0x0\n"
      "s_addc_u32 s57, s23, 0\n"
      "s_add_u32 m0, s18, 0x8f00\n"
      "v_mfma_f32_16x16x32_bf16 v[20:23], a[44:47], v[220:223], v[20:23]\n"
      "global_load_lds_dwordx4 v238, s[56:57] offset:256 \n"
      "s_waitcnt lgkmcnt(4)\n"
      "v_mfma_f32_16x16x32_bf16 v[24:27], a[24:27], v[224:227], v[24:27]\n"
      "ds_read_b64_tr_b16 a[76:77], v243 offset:8\n"
      "v_mfma_f32_16x16x32_bf16 v[28:31], a[28:31], v[224:227], v[28:31]\n"
      "ds_read_b64_tr_b16 a[78:79], v243 offset:776\n"
      "v_mad_i32_i24 v238, v251, s90, v238\n"
      "s_add_u32 s56, s22, 0x6000\n"
      "s_addc_u32 s57, s23, 0\n"
      "s_add_u32 m0, s18, 0x9f00\n"
      "v_mfma_f32_16x16x32_bf16 v[32:35], a[32:35], v[224:227], v[32:35]\n"
      "ds_read_b128 v[220:223], v233 offset:16384\n"
      "global_load_lds_dwordx4 v238, s[56:57] offset:256 \n"
      "v_mfma_f32_16x16x32_bf16 v[36:39], a[36:39], v[224:227], v[36:39]\n"
      "ds_read_b64_tr_b16 a[80:81], v243 offset:64\n"
      "v_mfma_f32_16x16x32_bf16 v[40:43], a[40:43], v[224:227], v[40:43]\n"
      "ds_read_b64_tr_b16 a[82:83], v243 offset:832\n"
      "v_mad_i32_i24 v238, v251, s91, v238\n"
      "s_add_u32 s56, s22, 0xc000\n"
      "s_addc_u32 s57, s23, 0\n"
      "s_add_u32 m0, s18, 0xaf00\n"
      "v_mfma_f32_16x16x32_bf16 v[44:47], a[44:47], v[224:227], v[44:47]\n"
      "global_load_lds_dwordx4 v238, s[56:57] offset:256 \n"
      "s_waitcnt lgkmcnt(8)\n"
      "v_mfma_f32_16x16x32_bf16 v[48:51], a[24:27], v[228:231], v[48:51]\n"
      "ds_read_b64_tr_b16 a[84:85], v243 offset:72\n"
      "v_mfma_f32_16x16x32_bf16 v[52:55], a[28:31], v[228:231], v[52:55]\n"
      "ds_read_b64_tr_b16 a[86:87], v243 offset:840\n"
      "v_mad_i32_i24 v238, v251, s92, v238\n"
      "s_add_u32 s56, s22, 0x12000\n"
      "s_addc_u32 s57, s23, 0\n"
      "s_add_u32 m0, s18, 0xbf00\n"
      "v_mfma_f32_16x16x32_bf16 v[56:59], a[32:35], v[228:231], v[56:59]\n"
      "ds_read_b128 v[224:227], v233 offset:20480\n"
      "global_load_lds_dwordx4 v238, s[56:57] offset:256 \n"
      "v_mfma_f32_16x16x32_bf16 v[60:63], a[36:39], v[228:231], v[60:63]\n"
      "ds_read_b64_tr_b16 a[88:89], v243 offset:128\n"
      "v_mfma_f32_16x16x32_bf16 v[64:67], a[40:43], v[228:231], v[64:67]\n"
      "ds_read_b64_tr_b16 a[90:91], v243 offset:896\n"
      "v_mad_i32_i24 v238, v251, s93, v238\n"
      "s_add_u32 s56, s22, 0x18000\n"
      "s_addc_u32 s57, s23, 0\n"
      "s_add_u32 m0, s18, 0xcf00\n"
      "v_mfma_f32_16x16x32_bf16 v[68:71], a[44:47], v[228:231], v[68:71]\n"
      "global_load_lds_dwordx4 v238, s[56:57] offset:256 \n"
      "s_waitcnt lgkmcnt(12)\n"
      "v_mfma_f32_16x16x32_bf16 v[72:75], a[24:27], v[216:219], v[72:75]\n"
      "ds_read_b64_tr_b16 a[92:93], v243 offset:136\n"
      "v_mfma_f32_16x16x32_bf16 v[76:79], a[28:31], v[216:219], v[76:79]\n"
      "ds_read_b64_tr_b16 a[94:95], v243 offset:904\n"
      "v_mad_i32_i24 v238, v251, s94, v238\n"
      "s_add_u32 s56, s22, 0x1e000\n"
      "s_addc_u32 s57, s23, 0\n"
      "s_add_u32 m0, s18, 0xdf00\n"
      "v_mfma_f32_16x16x32_bf16 v[80:83], a[32:35], v[216:219], v[80:83]\n"
      "ds_read_b128 v[228:231], v233 offset:24576\n"
      "global_load_lds_dwordx4 v238, s[56:57] offset:256 \n"
      "v_mfma_f32_16x16x32_bf16 v[84:87], a[36:39], v[216:219], v[84:87]\n"
      "v_mfma_f32_16x16x32_bf16 v[88:91], a[40:43], v[216:219], v[88:91]\n"
      "v_mad_i32_i24 v238, v251, s95, v238\n"
      "s_add_u32 s56, s22, 0x24000\n"
      "s_addc_u32 s57, s23, 0\n"
      "s_add_u32 m0, s18, 0xef00\n"
      "v_mfma_f32_16x16x32_bf16 v[92:95], a[44:47], v[216:219], v[92:95]\n"
      "global_load_lds_dwordx4 v238, s[56:57] offset:256 \n"
      "s_waitcnt lgkmcnt(10)\n"
      "v_mfma_f32_16x16x32_bf16 v[96:99], a[24:27], v[220:223], v[96:99]\n"
      "v_mfma_f32_16x16x32_bf16 v[100:103], a[28:31], v[220:223], v[100:103]\n"
      "v_mad_i32_i24 v238, v251, s96, v238\n"
      "s_add_u32 s56, s22, 0x2a000\n"
      "s_addc_u32 s57, s23, 0\n"
      "s_add_u32 m0, s18, 0xff00\n"
      "v_mfma_f32_16x16x32_bf16 v[104:107], a[32:35], v[220:223], v[104:107]\n"
      "ds_read_b128 v[216:219], v233 offset:28672\n"
      "global_load_lds_dwordx4 v238, s[56:57] offset:256 \n"
      "v_mfma_f32_16x16x32_bf16 v[108:111], a[36:39], v[220:223], v[108:111]\n"
      "v_mfma_f32_16x16x32_bf16 v[112:115], a[40:43], v[220:223], v[112:115]\n"
      "v_mad_i32_i24 v238, v251, s97, v238\n"
      "s_add_u32 s56, s22, 0x30000\n"
      "s_addc_u32 s57, s23, 0\n"
      "s_add_u32 m0, s18, 0x10f00\n"
      "v_mfma_f32_16x16x32_bf16 v[116:119], a[44:47], v[220:223], v[116:119]\n"
      "global_load_lds_dwordx4 v238, s[56:57] offset:256 \n"
      "s_waitcnt lgkmcnt(6)\n"
      "v_mfma_f32_16x16x32_bf16 v[120:123], a[24:27], v[224:227], v[120:123]\n"
      "v_mfma_f32_16x16x32_bf16 v[124:127], a[28:31], v[224:227], v[124:127]\n"
      "v_mfma_f32_16x16x32_bf16 v[128:131], a[32:35], v[224:227], v[128:131]\n"
      "ds_read_b128 v[220:223], v233 offset:32768\n"
      "v_add_u32 v233, 0x9000, v233\n"
      "s_add_u32 s78, s64, 0x1e000\n"
      "s_addc_u32 s79, s65, 0\n"
      "s_add_u32 m0, s74, 0xc000\n"
      "v_mfma_f32_16x16x32_bf16 v[132:135], a[36:39], v[224:227], v[132:135]\n"
      "global_load_lds_dwordx4 v240, s[78:79]\n"
      "v_mfma_f32_16x16x32_bf16 v[136:139], a[40:43], v[224:227], v[136:139]\n"
      "v_mfma_f32_16x16x32_bf16 v[140:143], a[44:47], v[224:227], v[140:143]\n"
      "s_waitcnt lgkmcnt(2)\n"
      "v_mfma_f32_16x16x32_bf16 v[144:147], a[24:27], v[228:231], v[144:147]\n"
      "s_waitcnt vmcnt(10)\n"
      "s_add_u32 m0, s74, 0xc400\n"
      "v_mfma_f32_16x16x32_bf16 v[148:151], a[28:31], v[228:231], v[148:151]\n"
      "ds_read_b64_tr_b16 a[96:97], v243 offset:24576\n"
      "global_load_lds_dwordx4 v241, s[78:79]\n"
      "v_mfma_f32_16x16x32_bf16 v[152:155], a[32:35], v[228:231], v[152:155]\n"
      "ds_read_b128 v[224:227], v234 offset:0\n"
      "v_mfma_f32_16x16x32_bf16 v[156:159], a[36:39], v[228:231], v[156:159]\n"
      "ds_read_b64_tr_b16 a[98:99], v243 offset:25344\n"
      "v_mfma_f32_16x16x32_bf16 v[160:163], a[40:43], v[228:231], v[160:163]\n"
      "ds_read_b64_tr_b16 a[100:101], v243 offset:24584\n"
      "s_add_u32 m0, s74, 0xc800\n"
      "v_mfma_f32_16x16x32_bf16 v[164:167], a[44:47], v[228:231], v[164:167]\n"
      "global_load_lds_dwordx4 v242, s[78:79]\n"
      "s_waitcnt lgkmcnt(5)\n"
      "v_mfma_f32_16x16x32_bf16 v[168:171], a[24:27], v[216:219], v[168:171]\n"
      "ds_read_b64_tr_b16 a[102:103], v243 offset:25352\n"
      "v_mfma_f32_16x16x32_bf16 v[172:175], a[28:31], v[216:219], v[172:175]\n"
      "ds_read_b64_tr_b16 a[104:105], v243 offset:24640\n"
      "v_mfma_f32_16x16x32_bf16 v[176:179], a[32:35], v[216:219], v[176:179]\n"
      "ds_read_b128 v[228:231], v234 offset:4096\n"
      "s_add_u32 s78, s64, 0x21000\n"
      "s_addc_u32 s79, s65, 0\n"
      "s_add_u32 m0, s74, 0xcc00\n"
      "v_mfma_f32_16x16x32_bf16 v[180:183], a[36:39], v[216:219], v[180:183]\n"
      "ds_read_b64_tr_b16 a[106:107], v243 offset:25408\n"
      "global_load_lds_dwordx4 v240, s[78:79]\n"
      "v_mfma_f32_16x16x32_bf16 v[184:187], a[40:43], v[216:219], v[184:187]\n"
      "ds_read_b64_tr_b16 a[108:109], v243 offset:24648\n"
      "v_mfma_f32_16x16x32_bf16 v[188:191], a[44:47], v[216:219], v[188:191]\n"
      "s_waitcnt lgkmcnt(9)\n"
      "v_mfma_f32_16x16x32_bf16 v[192:195], a[24:27], v[220:223], v[192:195]\n"
      "ds_read_b64_tr_b16 a[110:111], v243 offset:25416\n"
      "s_add_u32 m0, s74, 0xd000\n"
      "LPD4:\n"
      "s_cbranch_execz LPD9\n"
      "v_mfma_f32_16x16x32_bf16 v[196:199], a[28:31], v[220:223], v[196:199]\n"
      "ds_read_b64_tr_b16 a[112:113], v243 offset:24704\n"
      "global_load_lds_dwordx4 v241, s[78:79]\n"
      "v_mfma_f32_16x16x32_bf16 v[200:203], a[32:35], v[220:223], v[200:203]\n"
      "ds_read_b128 v[216:219], v234 offset:8192\n"
      "v_mfma_f32_16x16x32_bf16 v[204:207], a[36:39], v[220:223], v[204:207]\n"
      "ds_read_b64_tr_b16 a[114:115], v243 offset:25472\n"
      "v_mfma_f32_16x16x32_bf16 v[208:211], a[40:43], v[220:223], v[208:211]\n"
      "ds_read_b64_tr_b16 a[116:117], v243 offset:24712\n"
      "s_add_u32 m0, s74, 0xd400\n"
      "v_mfma_f32_16x16x32_bf16 v[212:215], a[44:47], v[220:223], v[212:215]\n"
      "global_load_lds_dwordx4 v242, s[78:79]\n"
      "s_waitcnt lgkmcnt(12)\n"
      "v_mfma_f32_16x16x32_bf16 v[0:3], a[48:51], v[224:227], v[0:3]\n"
      "ds_read_b64_tr_b16 a[118:119], v243 offset:25480\n"
      "v_mfma_f32_16x16x32_bf16 v[4:7], a[52:55], v[224:227], v[4:7]\n"
      "v_mfma_f32_16x16x32_bf16 v[8:11], a[56:59], v[224:227], v[8:11]\n"
      "ds_read_b128 v[220:223], v234 offset:12288\n"
      "s_add_u32 s78, s64, 0x24000\n"
      "s_addc_u32 s79, s65, 0\n"
      "s_add_u32 m0, s74, 0x0\n"
      "v_mfma_f32_16x16x32_bf16 v[12:15], a[60:63], v[224:227], v[12:15]\n"
      "global_load_lds_dwordx4 v240, s[78:79]\n"
      "v_mfma_f32_16x16x32_bf16 v[16:19], a[64:67], v[224:227], v[16:19]\n"
      "v_mfma_f32_16x16x32_bf16 v[20:23], a[68:71], v[224:227], v[20:23]\n"
      "s_waitcnt lgkmcnt(9)\n"
      "v_mfma_f32_16x16x32_bf16 v[24:27], a[48:51], v[228:231], v[24:27]\n"
      "s_add_u32 m0, s74, 0x400\n"
      "v_mfma_f32_16x16x32_bf16 v[28:31], a[52:55], v[228:231], v[28:31]\n"
      "global_load_lds_dwordx4 v241, s[78:79]\n"
      "v_mfma_f32_16x16x32_bf16 v[32:35], a[56:59], v[228:231], v[32:35]\n"
      "ds_read_b128 v[224:227], v234 offset:16384\n"
      "v_mfma_f32_16x16x32_bf16 v[36:39], a[60:63], v[228:231], v[36:39]\n"
      "v_mfma_f32_16x16x32_bf16 v[40:43], a[64:67], v[228:231], v[40:43]\n"
      "s_add_u32 m0, s74, 0x800\n"
      "v_mfma_f32_16x16x32_bf16 v[44:47], a[68:71], v[228:231], v[44:47]\n"
      "global_load_lds_dwordx4 v242, s[78:79]\n"
      "s_waitcnt lgkmcnt(5)\n"
      "v_mfma_f32_16x16x32_bf16 v[48:51], a[48:51], v[216:219], v[48:51]\n"
      "v_mfma_f32_16x16x32_bf16 v[52:55], a[52:55], v[216:219], v[52:55]\n"
      "v_mfma_f32_16x16x32_bf16 v[56:59], a[56:59], v[216:219], v[56:59]\n"
      "ds_read_b128 v[228:231], v234 offset:20480\n"
      "s_add_u32 s78, s64, 0x27000\n"
      "s_addc_u32 s79, s65, 0\n"
      "s_add_u32 m0, s74, 0xc00\n"
      "v_mfma_f32_16x16x32_bf16 v[60:63], a[60:63], v[216:219], v[60:63]\n"
      "global_load_lds_dwordx4 v240, s[78:79]\n"
      "v_mfma_f32_16x16x32_bf16 v[64:67], a[64:67], v[216:219], v[64:67]\n"
      "v_mfma_f32_16x16x32_bf16 v[68:71], a[68:71], v[216:219], v[68:71]\n"
      "s_waitcnt lgkmcnt(2)\n"
      "v_mfma_f32_16x16x32_bf16 v[72:75], a[48:51], v[220:223], v[72:75]\n"
      "s_add_u32 m0, s74, 0x1000\n"
      "v_mfma_f32_16x16x32_bf16 v[76:79], a[52:55], v[220:223], v[76:79]\n"
      "global_load_lds_dwordx4 v241, s[78:79]\n"
      "v_mfma_f32_16x16x32_bf16 v[80:83], a[56:59], v[220:223], v[80:83]\n"
      "ds_read_b128 v[216:219], v234 offset:24576\n"
      "v_mfma_f32_16x16x32_bf16 v[84:87], a[60:63], v[220:223], v[84:87]\n"
      "v_mfma_f32_16x16x32_bf16 v[88:91], a[64:67], v[220:223], v[88:91]\n"
      "s_add_u32 m0, s74, 0x1400\n"
      "v_mfma_f32_16x16x32_bf16 v[92:95], a[68:71], v[220:223], v[92:95]\n"
      "global_load_lds_dwordx4 v242, s[78:79]\n"
      "s_waitcnt lgkmcnt(2)\n"
      "v_mfma_f32_16x16x32_bf16 v[96:99], a[48:51], v[224:227], v[96:99]\n"
      "v_mfma_f32_16x16x32_bf16 v[100:103], a[52:55], v[224:227], v[100:103]\n"
      "v_mfma_f32_16x16x32_bf16 v[104:107], a[56:59], v[224:227], v[104:107]\n"
      "ds_read_b128 v[220:223], v234 offset:28672\n"
      "s_add_u32 s78, s64, 0x2a000\n"
      "s_addc_u32 s79, s65, 0\n"
      "s_add_u32 m0, s74, 0x6000\n"
      "v_mfma_f32_16x16x32_bf16 v[108:111], a[60:63], v[224:227], v[108:111]\n"
      "global_load_lds_dwordx4 v240, s[78:79]\n"
      "v_mfma_f32_16x16x32_bf16 v[112:115], a[64:67], v[224:227], v[112:115]\n"
      "v_mfma_f32_16x16x32_bf16 v[116:119], a[68:71], v[224:227], v[116:119]\n"
      "s_waitcnt lgkmcnt(2)\n"
      "v_mfma_f32_16x16x32_bf16 v[120:123], a[48:51], v[228:231], v[120:123]\n"
      "s_add_u32 m0, s74, 0x6400\n"
      "v_mfma_f32_16x16x32_bf16 v[124:127], a[52:55], v[228:231], v[124:127]\n"
      "global_load_lds_dwordx4 v241, s[78:79]\n"
      "v_mfma_f32_16x16x32_bf16 v[128:131], a[56:59], v[228:231], v[128:131]\n"
      "ds_read_b128 v[224:227], v234 offset:32768\n"
      "v_add_u32 v234, 0x9000, v234\n"
      "v_mfma_f32_16x16x32_bf16 v[132:135], a[60:63], v[228:231], v[132:135]\n"
      "v_mfma_f32_16x16x32_bf16 v[136:139], a[64:67], v[228:231], v[136:139]\n"
      "s_waitcnt vmcnt(8)\n"
      "s_add_u32 m0, s74, 0x6800\n"
      "v_mfma_f32_16x16x32_bf16 v[140:143], a[68:71], v[228:231], v[140:143]\n"
      "global_load_lds_dwordx4 v242, s[78:79]\n"
      "s_waitcnt lgkmcnt(2)\n"
      "v_mfma_f32_16x16x32_bf16 v[144:147], a[48:51], v[216:219], v[144:147]\n"
      "ds_read_b64_tr_b16 a[120:121], v243 offset:49152\n"
      "v_mfma_f32_16x16x32_bf16 v[148:151], a[52:55], v[216:219], v[148:151]\n"
      "ds_read_b64_tr_b16 a[122:123], v243 offset:49920\n"
      "v_mfma_f32_16x16x32_bf16 v[152:155], a[56:59], v[216:219], v[152:155]\n"
      "ds_read_b128 v[228:231], v235 offset:0\n"
      "s_add_u32 s78, s64, 0x2d000\n"
      "s_addc_u32 s79, s65, 0\n"
      "s_add_u32 m0, s74, 0x6c00\n"
      "v_mfma_f32_16x16x32_bf16 v[156:159], a[60:63], v[216:219], v[156:159]\n"
      "ds_read_b64_tr_b16 a[124:125], v243 offset:49160\n"
      "global_load_lds_dwordx4 v240, s[78:79]\n"
      "v_mfma_f32_16x16x32_bf16 v[160:163], a[64:67], v[216:219], v[160:163]\n"
      "ds_read_b64_tr_b16 a[126:127], v243 offset:49928\n"
      "v_mfma_f32_16x16x32_bf16 v[164:167], a[68:71], v[216:219], v[164:167]\n"
      "s_waitcnt lgkmcnt(6)\n"
      "v_mfma_f32_16x16x32_bf16 v[168:171], a[48:51], v[220:223], v[168:171]\n"
      "ds_read_b64_tr_b16 a[128:129], v243 offset:49216\n"
      "s_add_u32 m0, s74, 0x7000\n"
      "v_mfma_f32_16x16x32_bf16 v[172:175], a[52:55], v[220:223], v[172:175]\n"
      "ds_read_b64_tr_b16 a[130:131], v243 offset:49984\n"
      "global_load_lds_dwordx4 v241, s[78:79]\n"
      "v_mfma_f32_16x16x32_bf16 v[176:179], a[56:59], v[220:223], v[176:179]\n"
      "ds_read_b128 v[216:219], v235 offset:4096\n"
      "v_mfma_f32_16x16x32_bf16 v[180:183], a[60:63], v[220:223], v[180:183]\n"
      "ds_read_b64_tr_b16 a[132:133], v243 offset:49224\n"
      "v_mfma_f32_16x16x32_bf16 v[184:187], a[64:67], v[220:223], v[184:187]\n"
      "ds_read_b64_tr_b16 a[134:135], v243 offset:49992\n"
      "s_add_u32 m0, s74, 0x7400\n"
      "v_mfma_f32_16x16x32_bf16 v[188:191], a[68:71], v[220:223], v[188:191]\n"
      "global_load_lds_dwordx4 v242, s[78:79]\n"
      "s_waitcnt lgkmcnt(10)\n"
      "v_mfma_f32_16x16x32_bf16 v[192:195], a[48:51], v[224:227], v[192:195]\n"
      "ds_read_b64_tr_b16 a[136:137], v243 offset:49280\n"
      "v_mfma_f32_16x16x32_bf16 v[196:199], a[52:55], v[224:227], v[196:199]\n"
      "ds_read_b64_tr_b16 a[138:139], v243 offset:50048\n"
      "v_mfma_f32_16x16x32_bf16 v[200:203], a[56:59], v[224:227], v[200:203]\n"
      "ds_read_b128 v[220:223], v235 offset:8192\n"
      "global_load_dwordx4 v[244:247], v248, s[28:29] sc1\n"
      "v_mfma_f32_16x16x32_bf16 v[204:207], a[60:63], v[224:227], v[204:207]\n"
      "ds_read_b64_tr_b16 a[140:141], v243 offset:49288\n"
      "v_mfma_f32_16x16x32_bf16 v[208:211], a[64:67], v[224:227], v[208:211]\n"
      "ds_read_b64_tr_b16 a[142:143], v243 offset:50056\n"
      "v_mfma_f32_16x16x32_bf16 v[212:215], a[68:71], v[224:227], v[212:215]\n"
      "s_waitcnt lgkmcnt(12)\n"
      "v_mfma_f32_16x16x32_bf16 v[0:3], a[72:75], v[228:231], v[0:3]\n"
      "v_mfma_f32_16x16x32_bf16 v[4:7], a[76:79], v[228:231], v[4:7]\n"
      "v_mfma_f32_16x16x32_bf16 v[8:11], a[80:83], v[228:231], v[8:11]\n"
      "ds_read_b128 v[224:227], v235 offset:12288\n"
      "v_mfma_f32_16x16x32_bf16 v[12:15], a[84:87], v[228:231], v[12:15]\n"
      "v_mfma_f32_16x16x32_bf16 v[16:19], a[88:91], v[228:231], v[16:19]\n"
      "s_waitcnt vmcnt(7)\n"
      "v_mfma_f32_16x16x32_bf16 v[20:23], a[92:95], v[228:231], v[20:23]\n"
      "s_waitcnt lgkmcnt(8)\n"
      "v_mfma_f32_16x16x32_bf16 v[24:27], a[72:75], v[216:219], v[24:27]\n"
      "ds_read_b64_tr_b16 a[144:145], v243 offset:0\n"
      "v_mfma_f32_16x16x32_bf16 v[28:31], a[76:79], v[216:219], v[28:31]\n"
      "ds_read_b64_tr_b16 a[146:147], v243 offset:768\n"
      "v_mfma_f32_16x16x32_bf16 v[32:35], a[80:83], v[216:219], v[32:35]\n"
      "ds_read_b128 v[228:231], v235 offset:16384\n"
      "s_waitcnt lgkmcnt(4)\n"
      "s_add_u32 s78, s64, 0x30000\n"
      "s_addc_u32 s79, s65, 0\n"
      "s_add_u32 m0, s74, 0xc000\n"
      "v_mfma_f32_16x16x32_bf16 v[36:39], a[84:87], v[216:219], v[36:39]\n"
      "ds_read_b64_tr_b16 a[148:149], v243 offset:8\n"
      "global_load_lds_dwordx4 v240, s[78:79]\n"
      "v_mfma_f32_16x16x32_bf16 v[40:43], a[88:91], v[216:219], v[40:43]\n"
      "ds_read_b64_tr_b16 a[150:151], v243 offset:776\n"
      "v_mfma_f32_16x16x32_bf16 v[44:47], a[92:95], v[216:219], v[44:47]\n"
      "v_mfma_f32_16x16x32_bf16 v[48:51], a[72:75], v[220:223], v[48:51]\n"
      "ds_read_b64_tr_b16 a[152:153], v243 offset:64\n"
      "s_add_u32 m0, s74, 0xc400\n"
      "v_mfma_f32_16x16x32_bf16 v[52:55], a[76:79], v[220:223], v[52:55]\n"
      "ds_read_b64_tr_b16 a[154:155], v243 offset:832\n"
      "global_load_lds_dwordx4 v241, s[78:79]\n"
      "v_mfma_f32_16x16x32_bf16 v[56:59], a[80:83], v[220:223], v[56:59]\n"
      "ds_read_b128 v[216:219], v235 offset:20480\n"
      "v_mfma_f32_16x16x32_bf16 v[60:63], a[84:87], v[220:223], v[60:63]\n"
      "ds_read_b64_tr_b16 a[156:157], v243 offset:72\n"
      "v_mfma_f32_16x16x32_bf16 v[64:67], a[88:91], v[220:223], v[64:67]\n"
      "ds_read_b64_tr_b16 a[158:159], v243 offset:840\n"
      "s_add_u32 m0, s74, 0xc800\n"
      "v_mfma_f32_16x16x32_bf16 v[68:71], a[92:95], v[220:223], v[68:71]\n"
      "global_load_lds_dwordx4 v242, s[78:79]\n"
      "s_waitcnt lgkmcnt(10)\n"
      "v_mfma_f32_16x16x32_bf16 v[72:75], a[72:75], v[224:227], v[72:75]\n"
      "ds_read_b64_tr_b16 a[160:161], v243 offset:128\n"
      "v_mfma_f32_16x16x32_bf16 v[76:79], a[76:79], v[224:227], v[76:79]\n"
      "ds_read_b64_tr_b16 a[162:163], v243 offset:896\n"
      "v_mfma_f32_16x16x32_bf16 v[80:83], a[80:83], v[224:227], v[80:83]\n"
      "ds_read_b128 v[220:223], v235 offset:24576\n"
      "s_add_u32 s78, s64, 0x33000\n"
      "s_addc_u32 s79, s65, 0\n"
      "s_add_u32 m0, s74, 0xcc00\n"
      "v_mfma_f32_16x16x32_bf16 v[84:87], a[84:87], v[224:227], v[84:87]\n"
      "ds_read_b64_tr_b16 a[164:165], v243 offset:136\n"
      "global_load_lds_dwordx4 v240, s[78:79]\n"
      "v_mfma_f32_16x16x32_bf16 v[88:91], a[88:91], v[224:227], v[88:91]\n"
      "ds_read_b64_tr_b16 a[166:167], v243 offset:904\n"
      "v_mfma_f32_16x16x32_bf16 v[92:95], a[92:95], v[224:227], v[92:95]\n"
      "s_waitcnt lgkmcnt(12)\n"
      "v_mfma_f32_16x16x32_bf16 v[96:99], a[72:75], v[228:231], v[96:99]\n"
      "s_add_u32 m0, s74, 0xd000\n"
      "v_mfma_f32_16x16x32_bf16 v[100:103], a[76:79], v[228:231], v[100:103]\n"
      "global_load_lds_dwordx4 v241, s[78:79]\n"
      "v_mfma_f32_16x16x32_bf16 v[104:107], a[80:83], v[228:231], v[104:107]\n"
      "ds_read_b128 v[224:227], v235 offset:28672\n"
      "v_mfma_f32_16x16x32_bf16 v[108:111], a[84:87], v[228:231], v[108:111]\n"
      "v_mfma_f32_16x16x32_bf16 v[112:115], a[88:91], v[228:231], v[112:115]\n"
      "s_waitcnt vmcnt(6)\n"
      "s_add_u32 m0, s74, 0xd400\n"
      "v_mfma_f32_16x16x32_bf16 v[116:119], a[92:95], v[228:231], v[116:119]\n"
      "global_load_lds_dwordx4 v242, s[78:79]\n"
      "s_waitcnt lgkmcnt(8)\n"
      "v_mfma_f32_16x16x32_bf16 v[120:123], a[72:75], v[216:219], v[120:123]\n"
      "ds_read_b64_tr_b16 a[168:169], v243 offset:24576\n"
      "v_mfma_f32_16x16x32_bf16 v[124:127], a[76:79], v[216:219], v[124:127]\n"
      "ds_read_b64_tr_b16 a[170:171], v243 offset:25344\n"
      "v_mfma_f32_16x16x32_bf16 v[128:131], a[80:83], v[216:219], v[128:131]\n"
      "ds_read_b128 v[228:231], v235 offset:32768\n"
      "v_add_u32 v235, 0x9000, v235\n"
      "s_waitcnt lgkmcnt(4)\n"
      "s_add_u32 s78, s64, 0x36000\n"
      "s_addc_u32 s79, s65, 0\n"
      "s_add_u32 m0, s74, 0x0\n"
      "v_mfma_f32_16x16x32_bf16 v[132:135], a[84:87], v[216:219], v[132:135]\n"
      "ds_read_b64_tr_b16 a[172:173], v243 offset:24584\n"
      "global_load_lds_dwordx4 v240, s[78:79]\n"
      "v_mfma_f32_16x16x32_bf16 v[136:139], a[88:91], v[216:219], v[136:139]\n"
      "ds_read_b64_tr_b16 a[174:175], v243 offset:25352\n"
      "v_mfma_f32_16x16x32_bf16 v[140:143], a[92:95], v[216:219], v[140:143]\n"
      "v_mfma_f32_16x16x32_bf16 v[144:147], a[72:75], v[220:223], v[144:147]\n"
      "ds_read_b64_tr_b16 a[176:177], v243 offset:24640\n"
      "s_waitcnt vmcnt(7)\n"
      "v_cmp_ne_u64 vcc, s[80:81], v[244:245]\n"
      "v_cmp_ne_u64 s[56:57], s[80:81], v[246:247]\n"
      "s_nop 3\n"
      "s_or_b64 vcc, vcc, s[56:57]\n"
      "s_cbranch_scc0 LP1_1\n"
      "s_mov_b32 s82, 0x40000\n"
      "LP0_1:\n"
      "s_sleep 2\n"
      "global_load_dwordx4 v[244:247], v248, s[28:29] sc1\n"
      "s_sub_u32 s82, s82, 1\n"
      "s_cbranch_scc1 LP2_1\n"
      "s_waitcnt vmcnt(0)\n"
      "v_cmp_ne_u64 vcc, s[80:81], v[244:245]\n"
      "v_cmp_ne_u64 s[56:57], s[80:81], v[246:247]\n"
      "s_nop 3\n"
      "s_or_b64 vcc, vcc, s[56:57]\n"
      "s_cbranch_scc1 LP0_1\n"
      "LP2_1:\n"
      "s_waitcnt vmcnt(0)\n"
      "LP1_1:\n"
      "s_nop 0\n"
      "v_mfma_f32_16x16x32_bf16 v[148:151], a[76:79], v[220:223], v[148:151]\n"
      "s_barrier\n"
      "s_add_u32 m0, s74, 0x400\n"
      "v_mfma_f32_16x16x32_bf16 v[152:155], a[80:83], v[220:223], v[152:155]\n"
      "ds_read_b128 v[216:219], v232 offset:0\n"
      "global_load_lds_dwordx4 v241, s[78:79]\n"
      "v_mfma_f32_16x16x32_bf16 v[156:159], a[84:87], v[220:223], v[156:159]\n"
      "ds_read_b64_tr_b16 a[178:179], v243 offset:25408\n"
      "v_mfma_f32_16x16x32_bf16 v[160:163], a[88:91], v[220:223], v[160:163]\n"
      "ds_read_b64_tr_b16 a[180:181], v243 offset:24648\n"
      "v_mfma_f32_16x16x32_bf16 v[164:167], a[92:95], v[220:223], v[164:167]\n"
      "s_add_u32 m0, s74, 0x800\n"
      "s_waitcnt lgkmcnt(9)\n"
      "v_mfma_f32_16x16x32_bf16 v[168:171], a[72:75], v[224:227], v[168:171]\n"
      "ds_read_b64_tr_b16 a[182:183], v243 offset:25416\n"
      "global_load_lds_dwordx4 v242, s[78:79]\n"
      "v_mfma_f32_16x16x32_bf16 v[172:175], a[76:79], v[224:227], v[172:175]\n"
      "ds_read_b64_tr_b16 a[184:185], v243 offset:24704\n"
      "v_mfma_f32_16x16x32_bf16 v[176:179], a[80:83], v[224:227], v[176:179]\n"
      "ds_read_b128 v[220:223], v232 offset:4096\n"
      "v_mfma_f32_16x16x32_bf16 v[180:183], a[84:87], v[224:227], v[180:183]\n"
      "ds_read_b64_tr_b16 a[186:187], v243 offset:25472\n"
      "s_add_u32 s78, s64, 0x39000\n"
      "s_addc_u32 s79, s65, 0\n"
      "s_add_u32 m0, s74, 0xc00\n"
      "v_mfma_f32_16x16x32_bf16 v[184:187], a[88:91], v[224:227], v[184:187]\n"
      "ds_read_b64_tr_b16 a[188:189], v243 offset:24712\n"
      "global_load_lds_dwordx4 v240, s[78:79]\n"
      "v_mfma_f32_16x16x32_bf16 v[188:191], a[92:95], v[224:227], v[188:191]\n"
      "s_waitcnt lgkmcnt(11)\n"
      "v_mfma_f32_16x16x32_bf16 v[192:195], a[72:75], v[228:231], v[192:195]\n"
      "ds_read_b64_tr_b16 a[190:191], v243 offset:25480\n"
      "v_mfma_f32_16x16x32_bf16 v[196:199], a[76:79], v[228:231], v[196:199]\n"
      "s_add_u32 m0, s74, 0x1000\n"
      "v_mfma_f32_16x16x32_bf16 v[200:203], a[80:83], v[228:231], v[200:203]\n"
      "ds_read_b128 v[224:227], v232 offset:8192\n"
      "global_load_lds_dwordx4 v241, s[78:79]\n"
      "v_mfma_f32_16x16x32_bf16 v[204:207], a[84:87], v[228:231], v[204:207]\n"
      "v_mfma_f32_16x16x32_bf16 v[208:211], a[88:91], v[228:231], v[208:211]\n"
      "v_mfma_f32_16x16x32_bf16 v[212:215], a[92:95], v[228:231], v[212:215]\n"
      "s_add_u32 m0, s74, 0x1400\n"
      "s_waitcnt lgkmcnt(9)\n"
      "v_mfma_f32_16x16x32_bf16 v[0:3], a[96:99], v[216:219], v[0:3]\n"
      "global_load_lds_dwordx4 v242, s[78:79]\n"
      "v_mfma_f32_16x16x32_bf16 v[4:7], a[100:103], v[216:219], v[4:7]\n"
      "s_barrier\n"
      "v_mfma_f32_16x16x32_bf16 v[8:11], a[104:107], v[216:219], v[8:11]\n"
      "ds_read_b128 v[228:231], v232 offset:12288\n"
      "v_mfma_f32_16x16x32_bf16 v[12:15], a[108:111], v[216:219], v[12:15]\n"
      "s_add_u32 s58, s24, 0x0\n"
      "s_addc_u32 s59, s25, 0\n"
      "global_load_dwordx4 a[240:243], v239, s[58:59] offset:0 sc1\n"
      "v_mfma_f32_16x16x32_bf16 v[16:19], a[112:115], v[216:219], v[16:19]\n"
      "v_mfma_f32_16x16x32_bf16 v[20:23], a[116:119], v[216:219], v[20:23]\n"
      "s_waitcnt lgkmcnt(5)\n"
      "v_mfma_f32_16x16x32_bf16 v[24:27], a[96:99], v[220:223], v[24:27]\n"
      "v_mfma_f32_16x16x32_bf16 v[28:31], a[100:103], v[220:223], v[28:31]\n"
      "s_waitcnt lgkmcnt(2)\n"
      "s_add_u32 s78, s64, 0x3c000\n"
      "s_addc_u32 s79, s65, 0\n"
      "s_add_u32 m0, s74, 0x6000\n"
      "v_mfma_f32_16x16x32_bf16 v[32:35], a[104:107], v[220:223], v[32:35]\n"
      "ds_read_b128 v[216:219], v232 offset:16384\n"
      "global_load_lds_dwordx4 v240, s[78:79]\n"
      "v_mfma_f32_16x16x32_bf16 v[36:39], a[108:111], v[220:223], v[36:39]\n"
      "v_mfma_f32_16x16x32_bf16 v[40:43], a[112:115], v[220:223], v[40:43]\n"
      "s_waitcnt vmcnt(8)\n"
      "v_mfma_f32_16x16x32_bf16 v[44:47], a[116:119], v[220:223], v[44:47]\n"
      "s_add_u32 m0, s74, 0x6400\n"
      "s_waitcnt lgkmcnt(2)\n"
      "v_mfma_f32_16x16x32_bf16 v[48:51], a[96:99], v[224:227], v[48:51]\n"
      "ds_read_b64_tr_b16 a[192:193], v243 offset:49152\n"
      "global_load_lds_dwordx4 v241, s[78:79]\n"
      "v_mfma_f32_16x16x32_bf16 v[52:55], a[100:103], v[224:227], v[52:55]\n"
      "ds_read_b64_tr_b16 a[194:195], v243 offset:49920\n"
      "v_mfma_f32_16x16x32_bf16 v[56:59], a[104:107], v[224:227], v[56:59]\n"
      "ds_read_b128 v[220:223], v232 offset:20480\n"
      "v_mfma_f32_16x16x32_bf16 v[60:63], a[108:111], v[224:227], v[60:63]\n"
      "ds_read_b64_tr_b16 a[196:197], v243 offset:49160\n"
      "s_add_u32 m0, s74, 0x6800\n"
      "v_mfma_f32_16x16x32_bf16 v[64:67], a[112:115], v[224:227], v[64:67]\n"
      "ds_read_b64_tr_b16 a[198:199], v243 offset:49928\n"
      "global_load_lds_dwordx4 v242, s[78:79]\n"
      "v_mfma_f32_16x16x32_bf16 v[68:71], a[116:119], v[224:227], v[68:71]\n"
      "s_waitcnt lgkmcnt(6)\n"
      "v_mfma_f32_16x16x32_bf16 v[72:75], a[96:99], v[228:231], v[72:75]\n"
      "ds_read_b64_tr_b16 a[200:201], v243 offset:49216\n"
      "v_mfma_f32_16x16x32_bf16 v[76:79], a[100:103], v[228:231], v[76:79]\n"
      "ds_read_b64_tr_b16 a[202:203], v243 offset:49984\n"
      "s_add_u32 s78, s64, 0x3f000\n"
      "s_addc_u32 s79, s65, 0\n"
      "s_add_u32 m0, s74, 0x6c00\n"
      "v_mfma_f32_16x16x32_bf16 v[80:83], a[104:107], v[228:231], v[80:83]\n"
      "ds_read_b128 v[224:227], v232 offset:24576\n"
      "global_load_lds_dwordx4 v240, s[78:79]\n"
      "v_mfma_f32_16x16x32_bf16 v[84:87], a[108:111], v[228:231], v[84:87]\n"
      "ds_read_b64_tr_b16 a[204:205], v243 offset:49224\n"
      "v_mfma_f32_16x16x32_bf16 v[88:91], a[112:115], v[228:231], v[88:91]\n"
      "ds_read_b64_tr_b16 a[206:207], v243 offset:49992\n"
      "v_mfma_f32_16x16x32_bf16 v[92:95], a[116:119], v[228:231], v[92:95]\n"
      "s_add_u32 m0, s74, 0x7000\n"
      "s_waitcnt lgkmcnt(10)\n"
      "v_mfma_f32_16x16x32_bf16 v[96:99], a[96:99], v[216:219], v[96:99]\n"
      "ds_read_b64_tr_b16 a[208:209], v243 offset:49280\n"
      "global_load_lds_dwordx4 v241, s[78:79]\n"
      "v_mfma_f32_16x16x32_bf16 v[100:103], a[100:103], v[216:219], v[100:103]\n"
      "ds_read_b64_tr_b16 a[210:211], v243 offset:50048\n"
      "v_mfma_f32_16x16x32_bf16 v[104:107], a[104:107], v[216:219], v[104:107]\n"
      "ds_read_b128 v[228:231], v232 offset:28672\n"
      "v_mfma_f32_16x16x32_bf16 v[108:111], a[108:111], v[216:219], v[108:111]\n"
      "ds_read_b64_tr_b16 a[212:213], v243 offset:49288\n"
      "s_add_u32 m0, s74, 0x7400\n"
      "v_mfma_f32_16x16x32_bf16 v[112:115], a[112:115], v[216:219], v[112:115]\n"
      "ds_read_b64_tr_b16 a[214:215], v243 offset:50056\n"
      "global_load_lds_dwordx4 v242, s[78:79]\n"
      "v_mfma_f32_16x16x32_bf16 v[116:119], a[116:119], v[216:219], v[116:119]\n"
      "s_waitcnt lgkmcnt(12)\n"
      "v_mfma_f32_16x16x32_bf16 v[120:123], a[96:99], v[220:223], v[120:123]\n"
      "v_mfma_f32_16x16x32_bf16 v[124:127], a[100:103], v[220:223], v[124:127]\n"
      "global_load_dwordx4 a[244:247], v239, s[58:59] offset:1024 sc1\n"
      "v_mfma_f32_16x16x32_bf16 v[128:131], a[104:107], v[220:223], v[128:131]\n"
      "ds_read_b128 v[216:219], v232 offset:32768\n"
      "v_add_u32 v232, 0xffff7000, v232\n"
      "v_mfma_f32_16x16x32_bf16 v[132:135], a[108:111], v[220:223], v[132:135]\n"
      "v_mfma_f32_16x16x32_bf16 v[136:139], a[112:115], v[220:223], v[136:139]\n"
      "v_mfma_f32_16x16x32_bf16 v[140:143], a[116:119], v[220:223], v[140:143]\n"
      "global_load_dwordx4 a[248:251], v239, s[58:59] offset:2048 sc1\n"
      "s_waitcnt lgkmcnt(8)\n"
      "v_mfma_f32_16x16x32_bf16 v[144:147], a[96:99], v[224:227], v[144:147]\n"
      "s_waitcnt vmcnt(9)\n"
      "v_mfma_f32_16x16x32_bf16 v[148:151], a[100:103], v[224:227], v[148:151]\n"
      "ds_read_b64_tr_b16 a[216:217], v243 offset:0\n"
      "v_mfma_f32_16x16x32_bf16 v[152:155], a[104:107], v[224:227], v[152:155]\n"
      "ds_read_b128 v[220:223], v233 offset:0\n"
      "v_mfma_f32_16x16x32_bf16 v[156:159], a[108:111], v[224:227], v[156:159]\n"
      "ds_read_b64_tr_b16 a[218:219], v243 offset:768\n"
      "s_waitcnt lgkmcnt(4)\n"
      "s_add_u32 s78, s64, 0x42000\n"
      "s_addc_u32 s79, s65, 0\n"
      "s_add_u32 m0, s74, 0xc000\n"
      "v_mfma_f32_16x16x32_bf16 v[160:163], a[112:115], v[224:227], v[160:163]\n"
      "ds_read_b64_tr_b16 a[220:221], v243 offset:8\n"
      "global_load_lds_dwordx4 v240, s[78:79]\n"
      "v_mfma_f32_16x16x32_bf16 v[164:167], a[116:119], v[224:227], v[164:167]\n"
      "v_mfma_f32_16x16x32_bf16 v[168:171], a[96:99], v[228:231], v[168:171]\n"
      "ds_read_b64_tr_b16 a[222:223], v243 offset:776\n"
      "v_mfma_f32_16x16x32_bf16 v[172:175], a[100:103], v[228:231], v[172:175]\n"
      "ds_read_b64_tr_b16 a[224:225], v243 offset:64\n"
      "s_add_u32 m0, s74, 0xc400\n"
      "v_mfma_f32_16x16x32_bf16 v[176:179], a[104:107], v[228:231], v[176:179]\n"
      "ds_read_b128 v[224:227], v233 offset:4096\n"
      "global_load_lds_dwordx4 v241, s[78:79]\n"
      "v_mfma_f32_16x16x32_bf16 v[180:183], a[108:111], v[228:231], v[180:183]\n"
      "ds_read_b64_tr_b16 a[226:227], v243 offset:832\n"
      "v_mfma_f32_16x16x32_bf16 v[184:187], a[112:115], v[228:231], v[184:187]\n"
      "ds_read_b64_tr_b16 a[228:229], v243 offset:72\n"
      "v_mfma_f32_16x16x32_bf16 v[188:191], a[116:119], v[228:231], v[188:191]\n"
      "s_add_u32 m0, s74, 0xc800\n"
      "s_waitcnt lgkmcnt(9)\n"
      "v_mfma_f32_16x16x32_bf16 v[192:195], a[96:99], v[216:219], v[192:195]\n"
      "ds_read_b64_tr_b16 a[230:231], v243 offset:840\n"
      "global_load_lds_dwordx4 v242, s[78:79]\n"
      "v_mfma_f32_16x16x32_bf16 v[196:199], a[100:103], v[216:219], v[196:199]\n"
      "ds_read_b64_tr_b16 a[232:233], v243 offset:128\n"
      "v_mfma_f32_16x16x32_bf16 v[200:203], a[104:107], v[216:219], v[200:203]\n"
      "ds_read_b128 v[228:231], v233 offset:8192\n"
      "v_mfma_f32_16x16x32_bf16 v[204:207], a[108:111], v[216:219], v[204:207]\n"
      "ds_read_b64_tr_b16 a[234:235], v243 offset:896\n"
      "s_add_u32 s78, s64, 0x45000\n"
      "s_addc_u32 s79, s65, 0\n"
      "s_add_u32 m0, s74, 0xcc00\n"
      "v_mfma_f32_16x16x32_bf16 v[208:211], a[112:115], v[216:219], v[208:211]\n"
      "ds_read_b64_tr_b16 a[236:237], v243 offset:136\n"
      "global_load_lds_dwordx4 v240, s[78:79]\n"
      "v_mfma_f32_16x16x32_bf16 v[212:215], a[116:119], v[216:219], v[212:215]\n"
      "s_waitcnt lgkmcnt(12)\n"
      "v_mfma_f32_16x16x32_bf16 v[0:3], a[120:123], v[220:223], v[0:3]\n"
      "ds_read_b64_tr_b16 a[238:239], v243 offset:904\n"
      "v_mfma_f32_16x16x32_bf16 v[4:7], a[124:127], v[220:223], v[4:7]\n"
      "s_add_u32 m0, s74, 0xd000\n"
      "v_mfma_f32_16x16x32_bf16 v[8:11], a[128:131], v[220:223], v[8:11]\n"
      "ds_read_b128 v[216:219], v233 offset:12288\n"
      "global_load_lds_dwordx4 v241, s[78:79]\n"
      "v_mfma_f32_16x16x32_bf16 v[12:15], a[132:135], v[220:223], v[12:15]\n"
      "v_mfma_f32_16x16x32_bf16 v[16:19], a[136:139], v[220:223], v[16:19]\n"
      "v_mfma_f32_16x16x32_bf16 v[20:23], a[140:143], v[220:223], v[20:23]\n"
      "s_add_u32 m0, s74, 0xd400\n"
      "s_waitcnt lgkmcnt(9)\n"
      "v_mfma_f32_16x16x32_bf16 v[24:27], a[120:123], v[224:227], v[24:27]\n"
      "global_load_lds_dwordx4 v242, s[78:79]\n"
      "v_mfma_f32_16x16x32_bf16 v[28:31], a[124:127], v[224:227], v[28:31]\n"
      "v_mfma_f32_16x16x32_bf16 v[32:35], a[128:131], v[224:227], v[32:35]\n"
      "ds_read_b128 v[220:223], v233 offset:16384\n"
      "v_mfma_f32_16x16x32_bf16 v[36:39], a[132:135], v[224:227], v[36:39]\n"
      "s_waitcnt vmcnt(8)\n"
      "v_mad_i32_i24 v238, v251, s89, v238\n"
      "s_add_u32 s56, s22, 0x0\n"
      "s_addc_u32 s57, s23, 0\n"
      "s_add_u32 m0, s18, 0xfffffe00\n"
      "v_mfma_f32_16x16x32_bf16 v[40:43], a[136:139], v[224:227], v[40:43]\n"
      "ds_read_b64_tr_b16 a[0:1], v243 offset:24576\n"
      "global_load_lds_dwordx4 v238, s[56:57] offset:512 \n"
      "v_mfma_f32_16x16x32_bf16 v[44:47], a[140:143], v[224:227], v[44:47]\n"
      "s_waitcnt lgkmcnt(6)\n"
      "v_mfma_f32_16x16x32_bf16 v[48:51], a[120:123], v[228:231], v[48:51]\n"
      "ds_read_b64_tr_b16 a[2:3], v243 offset:25344\n"
      "v_mad_i32_i24 v238, v251, s90, v238\n"
      "s_add_u32 s56, s22, 0x6000\n"
      "s_addc_u32 s57, s23, 0\n"
      "s_add_u32 m0, s18, 0xe00\n"
      "v_mfma_f32_16x16x32_bf16 v[52:55], a[124:127], v[228:231], v[52:55]\n"
      "ds_read_b64_tr_b16 a[4:5], v243 offset:24584\n"
      "global_load_lds_dwordx4 v238, s[56:57] offset:512 \n"
      "v_mfma_f32_16x16x32_bf16 v[56:59], a[128:131], v[228:231], v[56:59]\n"
      "ds_read_b128 v[224:227], v233 offset:20480\n"
      "v_mfma_f32_16x16x32_bf16 v[60:63], a[132:135], v[228:231], v[60:63]\n"
      "ds_read_b64_tr_b16 a[6:7], v243 offset:25352\n"
      "v_mad_i32_i24 v238, v251, s91, v238\n"
      "s_add_u32 s56, s22, 0xc000\n"
      "s_addc_u32 s57, s23, 0\n"
      "s_add_u32 m0, s18, 0x1e00\n"
      "v_mfma_f32_16x16x32_bf16 v[64:67], a[136:139], v[228:231], v[64:67]\n"
      "ds_read_b64_tr_b16 a[8:9], v243 offset:24640\n"
      "global_load_lds_dwordx4 v238, s[56:57] offset:512 \n"
      "v_mfma_f32_16x16x32_bf16 v[68:71], a[140:143], v[228:231], v[68:71]\n"
      "s_waitcnt lgkmcnt(7)\n"
      "v_mfma_f32_16x16x32_bf16 v[72:75], a[120:123], v[216:219], v[72:75]\n"
      "ds_read_b64_tr_b16 a[10:11], v243 offset:25408\n"
      "v_mad_i32_i24 v238, v251, s92, v238\n"
      "s_add_u32 s56, s22, 0x12000\n"
      "s_addc_u32 s57, s23, 0\n"
      "s_add_u32 m0, s18, 0x2e00\n"
      "v_mfma_f32_16x16x32_bf16 v[76:79], a[124:127], v[216:219], v[76:79]\n"
      "ds_read_b64_tr_b16 a[12:13], v243 offset:24648\n"
      "global_load_lds_dwordx4 v238, s[56:57] offset:512 \n"
      "v_mfma_f32_16x16x32_bf16 v[80:83], a[128:131], v[216:219], v[80:83]\n"
      "ds_read_b128 v[228:231], v233 offset:24576\n"
      "v_mfma_f32_16x16x32_bf16 v[84:87], a[132:135], v[216:219], v[84:87]\n"
      "ds_read_b64_tr_b16 a[14:15], v243 offset:25416\n"
      "v_mad_i32_i24 v238, v251, s93, v238\n"
      "s_add_u32 s56, s22, 0x18000\n"
      "s_addc_u32 s57, s23, 0\n"
      "s_add_u32 m0, s18, 0x3e00\n"
      "v_mfma_f32_16x16x32_bf16 v[88:91], a[136:139], v[216:219], v[88:91]\n"
      "ds_read_b64_tr_b16 a[16:17], v243 offset:24704\n"
      "global_load_lds_dwordx4 v238, s[56:57] offset:512 \n"
      "v_mfma_f32_16x16x32_bf16 v[92:95], a[140:143], v[216:219], v[92:95]\n"
      "s_waitcnt lgkmcnt(11)\n"
      "v_mfma_f32_16x16x32_bf16 v[96:99], a[120:123], v[220:223], v[96:99]\n"
      "ds_read_b64_tr_b16 a[18:19], v243 offset:25472\n"
      "v_mad_i32_i24 v238, v251, s94, v238\n"
      "s_add_u32 s56, s22, 0x1e000\n"
      "s_addc_u32 s57, s23, 0\n"
      "s_add_u32 m0, s18, 0x4e00\n"
      "v_mfma_f32_16x16x32_bf16 v[100:103], a[124:127], v[220:223], v[100:103]\n"
      "ds_read_b64_tr_b16 a[20:21], v243 offset:24712\n"
      "global_load_lds_dwordx4 v238, s[56:57] offset:512 \n"
      "v_mfma_f32_16x16x32_bf16 v[104:107], a[128:131], v[220:223], v[104:107]\n"
      "ds_read_b128 v[216:219], v233 offset:28672\n"
      "v_mfma_f32_16x16x32_bf16 v[108:111], a[132:135], v[220:223], v[108:111]\n"
      "ds_read_b64_tr_b16 a[22:23], v243 offset:25480\n"
      "v_mad_i32_i24 v238, v251, s95, v238\n"
      "s_add_u32 s56, s22, 0x24000\n"
      "s_addc_u32 s57, s23, 0\n"
      "s_add_u32 m0, s18, 0x5e00\n"
      "v_mfma_f32_16x16x32_bf16 v[112:115], a[136:139], v[220:223], v[112:115]\n"
      "global_load_lds_dwordx4 v238, s[56:57] offset:512 \n"
      "v_mfma_f32_16x16x32_bf16 v[116:119], a[140:143], v[220:223], v[116:119]\n"
      "s_waitcnt lgkmcnt(11)\n"
      "v_mfma_f32_16x16x32_bf16 v[120:123], a[120:123], v[224:227], v[120:123]\n"
      "v_mad_i32_i24 v238, v251, s96, v238\n"
      "s_add_u32 s56, s22, 0x2a000\n"
      "s_addc_u32 s57, s23, 0\n"
      "s_add_u32 m0, s18, 0x6e00\n"
      "v_mfma_f32_16x16x32_bf16 v[124:127], a[124:127], v[224:227], v[124:127]\n"
      "global_load_lds_dwordx4 v238, s[56:57] offset:512 \n"
      "v_mfma_f32_16x16x32_bf16 v[128:131], a[128:131], v[224:227], v[128:131]\n"
      "ds_read_b128 v[220:223], v233 offset:32768\n"
      "v_add_u32 v233, 0xffff7000, v233\n"
      "v_mfma_f32_16x16x32_bf16 v[132:135], a[132:135], v[224:227], v[132:135]\n"
      "v_mad_i32_i24 v238, v251, s97, v238\n"
      "s_add_u32 s56, s22, 0x30000\n"
      "s_addc_u32 s57, s23, 0\n"
      "s_add_u32 m0, s18, 0x7e00\n"
      "v_mfma_f32_16x16x32_bf16 v[136:139], a[136:139], v[224:227], v[136:139]\n"
      "global_load_lds_dwordx4 v238, s[56:57] offset:512 \n"
      "v_mfma_f32_16x16x32_bf16 v[140:143], a[140:143], v[224:227], v[140:143]\n"
      "s_waitcnt lgkmcnt(7)\n"
      "v_mfma_f32_16x16x32_bf16 v[144:147], a[120:123], v[228:231], v[144:147]\n"
      "v_mfma_f32_16x16x32_bf16 v[148:151], a[124:127], v[228:231], v[148:151]\n"
      "global_load_dwordx4 a[252:255], v239, s[58:59] offset:3072 sc1\n"
      "v_mfma_f32_16x16x32_bf16 v[152:155], a[128:131], v[228:231], v[152:155]\n"
      "ds_read_b128 v[224:227], v234 offset:0\n"
      "v_mfma_f32_16x16x32_bf16 v[156:159], a[132:135], v[228:231], v[156:159]\n"
      "v_mfma_f32_16x16x32_bf16 v[160:163], a[136:139], v[228:231], v[160:163]\n"
      "v_mfma_f32_16x16x32_bf16 v[164:167], a[140:143], v[228:231], v[164:167]\n"
      "s_add_u32 s58, s24, 0x1000\n"
      "s_addc_u32 s59, s25, 0\n"
      "global_load_dwordx4 a[48:51], v239, s[58:59] offset:0 sc1\n"
      "s_waitcnt lgkmcnt(3)\n"
      "v_mfma_f32_16x16x32_bf16 v[168:171], a[120:123], v[216:219], v[168:171]\n"
      "s_waitcnt vmcnt(11)\n"
      "v_mfma_f32_16x16x32_bf16 v[172:175], a[124:127], v[216:219], v[172:175]\n"
      "ds_read_b64_tr_b16 a[24:25], v243 offset:49152\n"
      "v_mfma_f32_16x16x32_bf16 v[176:179], a[128:131], v[216:219], v[176:179]\n"
      "ds_read_b128 v[228:231], v234 offset:4096\n"
      "v_mfma_f32_16x16x32_bf16 v[180:183], a[132:135], v[216:219], v[180:183]\n"
      "ds_read_b64_tr_b16 a[26:27], v243 offset:49920\n"
      "global_load_dwordx4 a[52:55], v239, s[58:59] offset:1024 sc1\n"
      "v_mfma_f32_16x16x32_bf16 v[184:187], a[136:139], v[216:219], v[184:187]\n"
      "ds_read_b64_tr_b16 a[28:29], v243 offset:49160\n"
      "v_mfma_f32_16x16x32_bf16 v[188:191], a[140:143], v[216:219], v[188:191]\n"
      "s_waitcnt lgkmcnt(5)\n"
      "v_mfma_f32_16x16x32_bf16 v[192:195], a[120:123], v[220:223], v[192:195]\n"
      "LPD5:\n"
      "s_cbranch_execz LPD8\n"
      "ds_read_b64_tr_b16 a[30:31], v243 offset:49928\n"
      "v_mfma_f32_16x16x32_bf16 v[196:199], a[124:127], v[220:223], v[196:199]\n"
      "ds_read_b64_tr_b16 a[32:33], v243 offset:49216\n"
      "global_load_dwordx4 a[56:59], v239, s[58:59] offset:2048 sc1\n"
      "v_mfma_f32_16x16x32_bf16 v[200:203], a[128:131], v[220:223], v[200:203]\n"
      "ds_read_b128 v[216:219], v234 offset:8192\n"
      "v_mfma_f32_16x16x32_bf16 v[204:207], a[132:135], v[220:223], v[204:207]\n"
      "ds_read_b64_tr_b16 a[34:35], v243 offset:49984\n"
      "v_mfma_f32_16x16x32_bf16 v[208:211], a[136:139], v[220:223], v[208:211]\n"
      "ds_read_b64_tr_b16 a[36:37], v243 offset:49224\n"
      "v_mfma_f32_16x16x32_bf16 v[212:215], a[140:143], v[220:223], v[212:215]\n"
      "global_load_dwordx4 a[60:63], v239, s[58:59] offset:3072 sc1\n"
      "s_waitcnt lgkmcnt(9)\n"
      "v_mfma_f32_16x16x32_bf16 v[0:3], a[144:147], v[224:227], v[0:3]\n"
      "ds_read_b64_tr_b16 a[38:39], v243 offset:49992\n"
      "v_mfma_f32_16x16x32_bf16 v[4:7], a[148:151], v[224:227], v[4:7]\n"
      "ds_read_b64_tr_b16 a[40:41], v243 offset:49280\n"
      "v_mfma_f32_16x16x32_bf16 v[8:11], a[152:155], v[224:227], v[8:11]\n"
      "ds_read_b128 v[220:223], v234 offset:12288\n"
      "v_mfma_f32_16x16x32_bf16 v[12:15], a[156:159], v[224:227], v[12:15]\n"
      "ds_read_b64_tr_b16 a[42:43], v243 offset:50048\n"
      "s_add_u32 s58, s24, 0x2000\n"
      "s_addc_u32 s59, s25, 0\n"
      "global_load_dwordx4 a[64:67], v239, s[58:59] offset:0 sc1\n"
      "v_mfma_f32_16x16x32_bf16 v[16:19], a[160:163], v[224:227], v[16:19]\n"
      "ds_read_b64_tr_b16 a[44:45], v243 offset:49288\n"
      "v_mfma_f32_16x16x32_bf16 v[20:23], a[164:167], v[224:227], v[20:23]\n"
      "s_waitcnt lgkmcnt(12)\n"
      "v_mfma_f32_16x16x32_bf16 v[24:27], a[144:147], v[228:231], v[24:27]\n"
      "ds_read_b64_tr_b16 a[46:47], v243 offset:50056\n"
      "v_mfma_f32_16x16x32_bf16 v[28:31], a[148:151], v[228:231], v[28:31]\n"
      "global_load_dwordx4 a[68:71], v239, s[58:59] offset:1024 sc1\n"
      "v_mfma_f32_16x16x32_bf16 v[32:35], a[152:155], v[228:231], v[32:35]\n"
      "ds_read_b128 v[224:227], v234 offset:16384\n"
      "v_mfma_f32_16x16x32_bf16 v[36:39], a[156:159], v[228:231], v[36:39]\n"
      "v_mfma_f32_16x16x32_bf16 v[40:43], a[160:163], v[228:231], v[40:43]\n"
      "v_mfma_f32_16x16x32_bf16 v[44:47], a[164:167], v[228:231], v[44:47]\n"
      "global_load_dwordx4 a[72:75], v239, s[58:59] offset:2048 sc1\n"
      "s_waitcnt lgkmcnt(9)\n"
      "v_mfma_f32_16x16x32_bf16 v[48:51], a[144:147], v[216:219], v[48:51]\n"
      "v_mfma_f32_16x16x32_bf16 v[52:55], a[148:151], v[216:219], v[52:55]\n"
      "v_mfma_f32_16x16x32_bf16 v[56:59], a[152:155], v[216:219], v[56:59]\n"
      "ds_read_b128 v[228:231], v234 offset:20480\n"
      "v_mfma_f32_16x16x32_bf16 v[60:63], a[156:159], v[216:219], v[60:63]\n"
      "global_load_dwordx4 a[76:79], v239, s[58:59] offset:3072 sc1\n"
      "v_mfma_f32_16x16x32_bf16 v[64:67], a[160:163], v[216:219], v[64:67]\n"
      "v_mfma_f32_16x16x32_bf16 v[68:71], a[164:167], v[216:219], v[68:71]\n"
      "s_waitcnt lgkmcnt(5)\n"
      "v_mfma_f32_16x16x32_bf16 v[72:75], a[144:147], v[220:223], v[72:75]\n"
      "v_mfma_f32_16x16x32_bf16 v[76:79], a[148:151], v[220:223], v[76:79]\n"
      "s_add_u32 s58, s24, 0x3000\n"
      "s_addc_u32 s59, s25, 0\n"
      "global_load_dwordx4 a[80:83], v239, s[58:59] offset:0 sc1\n"
      "v_mfma_f32_16x16x32_bf16 v[80:83], a[152:155], v[220:223], v[80:83]\n"
      "ds_read_b128 v[216:219], v234 offset:24576\n"
      "v_mfma_f32_16x16x32_bf16 v[84:87], a[156:159], v[220:223], v[84:87]\n"
      "v_mfma_f32_16x16x32_bf16 v[88:91], a[160:163], v[220:223], v[88:91]\n"
      "v_mfma_f32_16x16x32_bf16 v[92:95], a[164:167], v[220:223], v[92:95]\n"
      "global_load_dwordx4 a[84:87], v239, s[58:59] offset:1024 sc1\n"
      "s_waitcnt lgkmcnt(2)\n"
      "v_mfma_f32_16x16x32_bf16 v[96:99], a[144:147], v[224:227], v[96:99]\n"
      "v_mfma_f32_16x16x32_bf16 v[100:103], a[148:151], v[224:227], v[100:103]\n"
      "v_mfma_f32_16x16x32_bf16 v[104:107], a[152:155], v[224:227], v[104:107]\n"
      "ds_read_b128 v[220:223], v234 offset:28672\n"
      "v_mfma_f32_16x16x32_bf16 v[108:111], a[156:159], v[224:227], v[108:111]\n"
      "global_load_dwordx4 a[88:91], v239, s[58:59] offset:2048 sc1\n"
      "v_mfma_f32_16x16x32_bf16 v[112:115], a[160:163], v[224:227], v[112:115]\n"
      "v_mfma_f32_16x16x32_bf16 v[116:119], a[164:167], v[224:227], v[116:119]\n"
      "s_waitcnt lgkmcnt(2)\n"
      "v_mfma_f32_16x16x32_bf16 v[120:123], a[144:147], v[228:231], v[120:123]\n"
      "v_mfma_f32_16x16x32_bf16 v[124:127], a[148:151], v[228:231], v[124:127]\n"
      "global_load_dwordx4 a[92:95], v239, s[58:59] offset:3072 sc1\n"
      "v_mfma_f32_16x16x32_bf16 v[128:131], a[152:155], v[228:231], v[128:131]\n"
      "ds_read_b128 v[224:227], v234 offset:32768\n"
      "v_add_u32 v234, 0xffff7000, v234\n"
      "v_mfma_f32_16x16x32_bf16 v[132:135], a[156:159], v[228:231], v[132:135]\n"
      "v_mfma_f32_16x16x32_bf16 v[136:139], a[160:163], v[228:231], v[136:139]\n"
      "v_mfma_f32_16x16x32_bf16 v[140:143], a[164:167], v[228:231], v[140:143]\n"
      "s_add_u32 s58, s24, 0x4000\n"
      "s_addc_u32 s59, s25, 0\n"
      "global_load_dwordx4 a[96:99], v239, s[58:59] offset:0 sc1\n"
      "s_waitcnt lgkmcnt(2)\n"
      "v_mfma_f32_16x16x32_bf16 v[144:147], a[144:147], v[216:219], v[144:147]\n"
      "ds_read_b64 v[236:237], v250 offset:4104\n"
      "v_mfma_f32_16x16x32_bf16 v[148:151], a[148:151], v[216:219], v[148:151]\n"
      "v_mfma_f32_16x16x32_bf16 v[152:155], a[152:155], v[216:219], v[152:155]\n"
      "ds_read_b128 v[228:231], v235 offset:0\n"
      "v_mfma_f32_16x16x32_bf16 v[156:159], a[156:159], v[216:219], v[156:159]\n"
      "global_load_dwordx4 a[100:103], v239, s[58:59] offset:1024 sc1\n"
      "v_mfma_f32_16x16x32_bf16 v[160:163], a[160:163], v[216:219], v[160:163]\n"
      "v_mfma_f32_16x16x32_bf16 v[164:167], a[164:167], v[216:219], v[164:167]\n"
      "s_waitcnt lgkmcnt(3)\n"
      "v_mfma_f32_16x16x32_bf16 v[168:171], a[144:147], v[220:223], v[168:171]\n"
      "v_mfma_f32_16x16x32_bf16 v[172:175], a[148:151], v[220:223], v[172:175]\n"
      "global_load_dwordx4 a[104:107], v239, s[58:59] offset:2048 sc1\n"
      "v_mfma_f32_16x16x32_bf16 v[176:179], a[152:155], v[220:223], v[176:179]\n"
      "ds_read_b128 v[216:219], v235 offset:4096\n"
      "v_mfma_f32_16x16x32_bf16 v[180:183], a[156:159], v[220:223], v[180:183]\n"
      "v_mfma_f32_16x16x32_bf16 v[184:187], a[160:163], v[220:223], v[184:187]\n"
      "v_mfma_f32_16x16x32_bf16 v[188:191], a[164:167], v[220:223], v[188:191]\n"
      "global_load_dwordx4 a[108:111], v239, s[58:59] offset:3072 sc1\n"
      "s_waitcnt lgkmcnt(3)\n"
      "v_mfma_f32_16x16x32_bf16 v[192:195], a[144:147], v[224:227], v[192:195]\n"
      "v_mfma_f32_16x16x32_bf16 v[196:199], a[148:151], v[224:227], v[196:199]\n"
      "v_mfma_f32_16x16x32_bf16 v[200:203], a[152:155], v[224:227], v[200:203]\n"
      "ds_read_b128 v[220:223], v235 offset:8192\n"
      "v_mfma_f32_16x16x32_bf16 v[204:207], a[156:159], v[224:227], v[204:207]\n"
      "s_add_u32 s58, s24, 0x5000\n"
      "s_addc_u32 s59, s25, 0\n"
      "global_load_dwordx4 a[112:115], v239, s[58:59] offset:0 sc1\n"
      "v_mfma_f32_16x16x32_bf16 v[208:211], a[160:163], v[224:227], v[208:211]\n"
      "v_mfma_f32_16x16x32_bf16 v[212:215], a[164:167], v[224:227], v[212:215]\n"
      "s_waitcnt lgkmcnt(2)\n"
      "v_mfma_f32_16x16x32_bf16 v[0:3], a[168:171], v[228:231], v[0:3]\n"
      "v_mfma_f32_16x16x32_bf16 v[4:7], a[172:175], v[228:231], v[4:7]\n"
      "global_load_dwordx4 a[116:119], v239, s[58:59] offset:1024 sc1\n"
      "v_mfma_f32_16x16x32_bf16 v[8:11], a[176:179], v[228:231], v[8:11]\n"
      "ds_read_b128 v[224:227], v235 offset:12288\n"
      "v_mfma_f32_16x16x32_bf16 v[12:15], a[180:183], v[228:231], v[12:15]\n"
      "v_mfma_f32_16x16x32_bf16 v[16:19], a[184:187], v[228:231], v[16:19]\n"
      "v_mfma_f32_16x16x32_bf16 v[20:23], a[188:191], v[228:231], v[20:23]\n"
      "global_load_dwordx4 a[120:123], v239, s[58:59] offset:2048 sc1\n"
      "s_waitcnt lgkmcnt(2)\n"
      "v_mfma_f32_16x16x32_bf16 v[24:27], a[168:171], v[216:219], v[24:27]\n"
      "v_mfma_f32_16x16x32_bf16 v[28:31], a[172:175], v[216:219], v[28:31]\n"
      "v_mfma_f32_16x16x32_bf16 v[32:35], a[176:179], v[216:219], v[32:35]\n"
      "ds_read_b128 v[228:231], v235 offset:16384\n"
      "v_mfma_f32_16x16x32_bf16 v[36:39], a[180:183], v[216:219], v[36:39]\n"
      "global_load_dwordx4 a[124:127], v239, s[58:59] offset:3072 sc1\n"
      "v_mfma_f32_16x16x32_bf16 v[40:43], a[184:187], v[216:219], v[40:43]\n"
      "v_mfma_f32_16x16x32_bf16 v[44:47], a[188:191], v[216:219], v[44:47]\n"
      "s_waitcnt lgkmcnt(2)\n"
      "v_mfma_f32_16x16x32_bf16 v[48:51], a[168:171], v[220:223], v[48:51]\n"
      "v_mfma_f32_16x16x32_bf16 v[52:55], a[172:175], v[220:223], v[52:55]\n"
      "s_add_u32 s58, s24, 0x6000\n"
      "s_addc_u32 s59, s25, 0\n"
      "global_load_dwordx4 a[128:131], v239, s[58:59] offset:0 sc1\n"
      "v_mfma_f32_16x16x32_bf16 v[56:59], a[176:179], v[220:223], v[56:59]\n"
      "ds_read_b128 v[216:219], v235 offset:20480\n"
      "v_mfma_f32_16x16x32_bf16 v[60:63], a[180:183], v[220:223], v[60:63]\n"
      "v_mfma_f32_16x16x32_bf16 v[64:67], a[184:187], v[220:223], v[64:67]\n"
      "v_mfma_f32_16x16x32_bf16 v[68:71], a[188:191], v[220:223], v[68:71]\n"
      "global_load_dwordx4 a[132:135], v239, s[58:59] offset:1024 sc1\n"
      "s_waitcnt lgkmcnt(2)\n"
      "v_mfma_f32_16x16x32_bf16 v[72:75], a[168:171], v[224:227], v[72:75]\n"
      "v_mfma_f32_16x16x32_bf16 v[76:79], a[172:175], v[224:227], v[76:79]\n"
      "v_mfma_f32_16x16x32_bf16 v[80:83], a[176:179], v[224:227], v[80:83]\n"
      "ds_read_b128 v[220:223], v235 offset:24576\n"
      "v_mfma_f32_16x16x32_bf16 v[84:87], a[180:183], v[224:227], v[84:87]\n"
      "global_load_dwordx4 a[136:139], v239, s[58:59] offset:2048 sc1\n"
      "v_mfma_f32_16x16x32_bf16 v[88:91], a[184:187], v[224:227], v[88:91]\n"
      "v_mfma_f32_16x16x32_bf16 v[92:95], a[188:191], v[224:227], v[92:95]\n"
      "s_waitcnt lgkmcnt(2)\n"
      "v_mfma_f32_16x16x32_bf16 v[96:99], a[168:171], v[228:231], v[96:99]\n"
      "v_mfma_f32_16x16x32_bf16 v[100:103], a[172:175], v[228:231], v[100:103]\n"
      "global_load_dwordx4 a[140:143], v239, s[58:59] offset:3072 sc1\n"
      "v_mfma_f32_16x16x32_bf16 v[104:107], a[176:179], v[228:231], v[104:107]\n"
      "ds_read_b128 v[224:227], v235 offset:28672\n"
      "v_mfma_f32_16x16x32_bf16 v[108:111], a[180:183], v[228:231], v[108:111]\n"
      "v_mfma_f32_16x16x32_bf16 v[112:115], a[184:187], v[228:231], v[112:115]\n"
      "v_mfma_f32_16x16x32_bf16 v[116:119], a[188:191], v[228:231], v[116:119]\n"
      "s_add_u32 s58, s24, 0x7000\n"
      "s_addc_u32 s59, s25, 0\n"
      "global_load_dwordx4 a[144:147], v239, s[58:59] offset:0 sc1\n"
      "s_waitcnt lgkmcnt(2)\n"
      "v_mfma_f32_16x16x32_bf16 v[120:123], a[168:171], v[216:219], v[120:123]\n"
      "v_mfma_f32_16x16x32_bf16 v[124:127], a[172:175], v[216:219], v[124:127]\n"
      "v_mfma_f32_16x16x32_bf16 v[128:131], a[176:179], v[216:219], v[128:131]\n"
      "ds_read_b128 v[228:231], v235 offset:32768\n"
      "v_add_u32 v235, 0xffff7000, v235\n"
      "v_mfma_f32_16x16x32_bf16 v[132:135], a[180:183], v[216:219], v[132:135]\n"
      "global_load_dwordx4 a[148:151], v239, s[58:59] offset:1024 sc1\n"
      "v_mfma_f32_16x16x32_bf16 v[136:139], a[184:187], v[216:219], v[136:139]\n"
      "v_mfma_f32_16x16x32_bf16 v[140:143], a[188:191], v[216:219], v[140:143]\n"
      "s_waitcnt lgkmcnt(2)\n"
      "v_mfma_f32_16x16x32_bf16 v[144:147], a[168:171], v[220:223], v[144:147]\n"
      "v_mfma_f32_16x16x32_bf16 v[148:151], a[172:175], v[220:223], v[148:151]\n"
      "s_waitcnt vmcnt(27)\n"
      "s_barrier\n"
      "global_load_dwordx4 a[152:155], v239, s[58:59] offset:2048 sc1\n"
      "v_mfma_f32_16x16x32_bf16 v[152:155], a[176:179], v[220:223], v[152:155]\n"
      "ds_read_b128 v[216:219], v232 offset:0\n"
      "v_mfma_f32_16x16x32_bf16 v[156:159], a[180:183], v[220:223], v[156:159]\n"
      "v_mfma_f32_16x16x32_bf16 v[160:163], a[184:187], v[220:223], v[160:163]\n"
      "v_mfma_f32_16x16x32_bf16 v[164:167], a[188:191], v[220:223], v[164:167]\n"
      "global_load_dwordx4 a[156:159], v239, s[58:59] offset:3072 sc1\n"
      "s_waitcnt lgkmcnt(2)\n"
      "v_mfma_f32_16x16x32_bf16 v[168:171], a[168:171], v[224:227], v[168:171]\n"
      "v_mfma_f32_16x16x32_bf16 v[172:175], a[172:175], v[224:227], v[172:175]\n"
      "v_mfma_f32_16x16x32_bf16 v[176:179], a[176:179], v[224:227], v[176:179]\n"
      "ds_read_b128 v[220:223], v232 offset:4096\n"
      "v_mfma_f32_16x16x32_bf16 v[180:183], a[180:183], v[224:227], v[180:183]\n"
      "s_add_u32 s58, s24, 0x8000\n"
      "s_addc_u32 s59, s25, 0\n"
      "global_load_dwordx4 a[160:163], v239, s[58:59] offset:0 sc1\n"
      "v_mfma_f32_16x16x32_bf16 v[184:187], a[184:187], v[224:227], v[184:187]\n"
      "v_mfma_f32_16x16x32_bf16 v[188:191], a[188:191], v[224:227], v[188:191]\n"
      "s_waitcnt lgkmcnt(2)\n"
      "v_mfma_f32_16x16x32_bf16 v[192:195], a[168:171], v[228:231], v[192:195]\n"
      "v_mfma_f32_16x16x32_bf16 v[196:199], a[172:175], v[228:231], v[196:199]\n"
      "global_load_dwordx4 a[164:167], v239, s[58:59] offset:1024 sc1\n"
      "v_mfma_f32_16x16x32_bf16 v[200:203], a[176:179], v[228:231], v[200:203]\n"
      "ds_read_b128 v[224:227], v232 offset:8192\n"
      "v_mfma_f32_16x16x32_bf16 v[204:207], a[180:183], v[228:231], v[204:207]\n"
      "v_mfma_f32_16x16x32_bf16 v[208:211], a[184:187], v[228:231], v[208:211]\n"
      "v_mfma_f32_16x16x32_bf16 v[212:215], a[188:191], v[228:231], v[212:215]\n"
      "s_waitcnt lgkmcnt(2)\n"
      "v_mfma_f32_16x16x32_bf16 v[0:3], a[192:195], v[216:219], v[0:3]\n"
      "global_load_dwordx4 a[168:171], v239, s[58:59] offset:2048 sc1\n"
      "v_mfma_f32_16x16x32_bf16 v[4:7], a[196:199], v[216:219], v[4:7]\n"
      "v_mfma_f32_16x16x32_bf16 v[8:11], a[200:203], v[216:219], v[8:11]\n"
      "ds_read_b128 v[228:231], v232 offset:12288\n"
      "v_mfma_f32_16x16x32_bf16 v[12:15], a[204:207], v[216:219], v[12:15]\n"
      "v_mfma_f32_16x16x32_bf16 v[16:19], a[208:211], v[216:219], v[16:19]\n"
      "s_mul_i32 s78, s98, s19\n"
      "v_mfma_f32_16x16x32_bf16 v[20:23], a[212:215], v[216:219], v[20:23]\n"
      "v_add_u32 v236, s78, v236\n"
      "s_waitcnt lgkmcnt(2)\n"
      "v_mfma_f32_16x16x32_bf16 v[24:27], a[192:195], v[220:223], v[24:27]\n"
      "s_mul_i32 s78, s78, s99\n"
      "v_mfma_f32_16x16x32_bf16 v[28:31], a[196:199], v[220:223], v[28:31]\n"
      "v_add_u32 v237, s78, v237\n"
      "v_mfma_f32_16x16x32_bf16 v[32:35], a[200:203], v[220:223], v[32:35]\n"
      "ds_read_b128 v[216:219], v232 offset:16384\n"
      "s_add_u32 s52, s20, s32\n"
      "s_addc_u32 s53, s21, 0\n"
      "s_add_u32 m0, s17, 0x0\n"
      "v_mfma_f32_16x16x32_bf16 v[36:39], a[204:207], v[220:223], v[36:39]\n"
      "global_load_lds_dwordx4 v236, s[52:53] offset:0 \n"
      "v_mfma_f32_16x16x32_bf16 v[40:43], a[208:211], v[220:223], v[40:43]\n"
      "v_mfma_f32_16x16x32_bf16 v[44:47], a[212:215], v[220:223], v[44:47]\n"
      "s_add_u32 m0, s17, 0x1f00\n"
      "s_waitcnt lgkmcnt(2)\n"
      "v_mfma_f32_16x16x32_bf16 v[48:51], a[192:195], v[224:227], v[48:51]\n"
      "global_load_lds_dwordx4 v236, s[52:53] offset:256 \n"
      "v_mfma_f32_16x16x32_bf16 v[52:55], a[196:199], v[224:227], v[52:55]\n"
      "v_mfma_f32_16x16x32_bf16 v[56:59], a[200:203], v[224:227], v[56:59]\n"
      "ds_read_b128 v[220:223], v232 offset:20480\n"
      "s_add_u32 m0, s17, 0x3e00\n"
      "v_mfma_f32_16x16x32_bf16 v[60:63], a[204:207], v[224:227], v[60:63]\n"
      "global_load_lds_dwordx4 v236, s[52:53] offset:512 \n"
      "v_mfma_f32_16x16x32_bf16 v[64:67], a[208:211], v[224:227], v[64:67]\n"
      "v_mfma_f32_16x16x32_bf16 v[68:71], a[212:215], v[224:227], v[68:71]\n"
      "s_add_u32 m0, s17, 0x1000\n"
      "s_waitcnt lgkmcnt(2)\n"
      "v_mfma_f32_16x16x32_bf16 v[72:75], a[192:195], v[228:231], v[72:75]\n"
      "global_load_lds_dwordx4 v237, s[52:53] offset:0 \n"
      "v_mfma_f32_16x16x32_bf16 v[76:79], a[196:199], v[228:231], v[76:79]\n"
      "v_mfma_f32_16x16x32_bf16 v[80:83], a[200:203], v[228:231], v[80:83]\n"
      "ds_read_b128 v[224:227], v232 offset:24576\n"
      "s_add_u32 m0, s17, 0x2f00\n"
      "v_mfma_f32_16x16x32_bf16 v[84:87], a[204:207], v[228:231], v[84:87]\n"
      "global_load_lds_dwordx4 v237, s[52:53] offset:256 \n"
      "v_mfma_f32_16x16x32_bf16 v[88:91], a[208:211], v[228:231], v[88:91]\n"
      "v_mfma_f32_16x16x32_bf16 v[92:95], a[212:215], v[228:231], v[92:95]\n"
      "s_add_u32 m0, s17, 0x4e00\n"
      "s_waitcnt lgkmcnt(2)\n"
      "v_mfma_f32_16x16x32_bf16 v[96:99], a[192:195], v[216:219], v[96:99]\n"
      "global_load_lds_dwordx4 v237, s[52:53] offset:512 \n"
      "v_mfma_f32_16x16x32_bf16 v[100:103], a[196:199], v[216:219], v[100:103]\n"
      "v_mfma_f32_16x16x32_bf16 v[104:107], a[200:203], v[216:219], v[104:107]\n"
      "ds_read_b128 v[228:231], v232 offset:28672\n"
      "v_mfma_f32_16x16x32_bf16 v[108:111], a[204:207], v[216:219], v[108:111]\n"
      "v_mfma_f32_16x16x32_bf16 v[112:115], a[208:211], v[216:219], v[112:115]\n"
      "v_mfma_f32_16x16x32_bf16 v[116:119], a[212:215], v[216:219], v[116:119]\n"
      "s_waitcnt lgkmcnt(2)\n"
      "v_mfma_f32_16x16x32_bf16 v[120:123], a[192:195], v[220:223], v[120:123]\n"
      "v_mfma_f32_16x16x32_bf16 v[124:127], a[196:199], v[220:223], v[124:127]\n"
      "v_mfma_f32_16x16x32_bf16 v[128:131], a[200:203], v[220:223], v[128:131]\n"
      "ds_read_b128 v[216:219], v232 offset:32768\n"
      "v_add_u32 v232, v249, v232\n"
      "v_mfma_f32_16x16x32_bf16 v[132:135], a[204:207], v[220:223], v[132:135]\n"
      "v_mfma_f32_16x16x32_bf16 v[136:139], a[208:211], v[220:223], v[136:139]\n"
      "v_mfma_f32_16x16x32_bf16 v[140:143], a[212:215], v[220:223], v[140:143]\n"
      "s_waitcnt lgkmcnt(2)\n"
      "v_mfma_f32_16x16x32_bf16 v[144:147], a[192:195], v[224:227], v[144:147]\n"
      "v_mfma_f32_16x16x32_bf16 v[148:151], a[196:199], v[224:227], v[148:151]\n"
      "v_mfma_f32_16x16x32_bf16 v[152:155], a[200:203], v[224:227], v[152:155]\n"
      "ds_read_b128 v[220:223], v233 offset:0\n"
      "v_mfma_f32_16x16x32_bf16 v[156:159], a[204:207], v[224:227], v[156:159]\n"
      "v_mfma_f32_16x16x32_bf16 v[160:163], a[208:211], v[224:227], v[160:163]\n"
      "v_mfma_f32_16x16x32_bf16 v[164:167], a[212:215], v[224:227], v[164:167]\n"
      "s_waitcnt lgkmcnt(2)\n"
      "v_mfma_f32_16x16x32_bf16 v[168:171], a[192:195], v[228:231], v[168:171]\n"
      "v_mfma_f32_16x16x32_bf16 v[172:175], a[196:199], v[228:231], v[172:175]\n"
      "v_mfma_f32_16x16x32_bf16 v[176:179], a[200:203], v[228:231], v[176:179]\n"
      "ds_read_b128 v[224:227], v233 offset:4096\n"
      "v_mfma_f32_16x16x32_bf16 v[180:183], a[204:207], v[228:231], v[180:183]\n"
      "v_mfma_f32_16x16x32_bf16 v[184:187], a[208:211], v[228:231], v[184:187]\n"
      "v_mfma_f32_16x16x32_bf16 v[188:191], a[212:215], v[228:231], v[188:191]\n"
      "s_waitcnt lgkmcnt(2)\n"
      "v_mfma_f32_16x16x32_bf16 v[192:195], a[192:195], v[216:219], v[192:195]\n"
      "v_mfma_f32_16x16x32_bf16 v[196:199], a[196:199], v[216:219], v[196:199]\n"
      "v_mfma_f32_16x16x32_bf16 v[200:203], a[200:203], v[216:219], v[200:203]\n"
      "ds_read_b128 v[228:231], v233 offset:8192\n"
      "v_mfma_f32_16x16x32_bf16 v[204:207], a[204:207], v[216:219], v[204:207]\n"
      "v_mfma_f32_16x16x32_bf16 v[208:211], a[208:211], v[216:219], v[208:211]\n"
      "v_mfma_f32_16x16x32_bf16 v[212:215], a[212:215], v[216:219], v[212:215]\n"
      "s_waitcnt lgkmcnt(2)\n"
      "v_mfma_f32_16x16x32_bf16 v[0:3], a[216:219], v[220:223], v[0:3]\n"
      "v_mfma_f32_16x16x32_bf16 v[4:7], a[220:223], v[220:223], v[4:7]\n"
      "v_mfma_f32_16x16x32_bf16 v[8:11], a[224:227], v[220:223], v[8:11]\n"
      "ds_read_b128 v[216:219], v233 offset:12288\n"
      "v_mfma_f32_16x16x32_bf16 v[12:15], a[228:231], v[220:223], v[12:15]\n"
      "v_mfma_f32_16x16x32_bf16 v[16:19], a[232:235], v[220:223], v[16:19]\n"
      "s_add_u32 s52, s42, s32\n"
      "s_addc_u32 s53, s43, 0\n"
      "s_add_u32 m0, s17, 0x6000\n"
      "v_mfma_f32_16x16x32_bf16 v[20:23], a[236:239], v[220:223], v[20:23]\n"
      "global_load_lds_dwordx4 v236, s[52:53] offset:0 \n"
      "s_waitcnt lgkmcnt(2)\n"
      "v_mfma_f32_16x16x32_bf16 v[24:27], a[216:219], v[224:227], v[24:27]\n"
      "v_mfma_f32_16x16x32_bf16 v[28:31], a[220:223], v[224:227], v[28:31]\n"
      "s_add_u32 m0, s17, 0x7f00\n"
      "v_mfma_f32_16x16x32_bf16 v[32:35], a[224:227], v[224:227], v[32:35]\n"
      "ds_read_b128 v[220:223], v233 offset:16384\n"
      "global_load_lds_dwordx4 v236, s[52:53] offset:256 \n"
      "v_mfma_f32_16x16x32_bf16 v[36:39], a[228:231], v[224:227], v[36:39]\n"
      "v_mfma_f32_16x16x32_bf16 v[40:43], a[232:235], v[224:227], v[40:43]\n"
      "s_add_u32 m0, s17, 0x9e00\n"
      "v_mfma_f32_16x16x32_bf16 v[44:47], a[236:239], v[224:227], v[44:47]\n"
      "global_load_lds_dwordx4 v236, s[52:53] offset:512 \n"
      "s_waitcnt lgkmcnt(2)\n"
      "v_mfma_f32_16x16x32_bf16 v[48:51], a[216:219], v[228:231], v[48:51]\n"
      "v_mfma_f32_16x16x32_bf16 v[52:55], a[220:223], v[228:231], v[52:55]\n"
      "s_add_u32 m0, s17, 0x7000\n"
      "v_mfma_f32_16x16x32_bf16 v[56:59], a[224:227], v[228:231], v[56:59]\n"
      "ds_read_b128 v[224:227], v233 offset:20480\n"
      "global_load_lds_dwordx4 v237, s[52:53] offset:0 \n"
      "v_mfma_f32_16x16x32_bf16 v[60:63], a[228:231], v[228:231], v[60:63]\n"
      "v_mfma_f32_16x16x32_bf16 v[64:67], a[232:235], v[228:231], v[64:67]\n"
      "s_add_u32 m0, s17, 0x8f00\n"
      "v_mfma_f32_16x16x32_bf16 v[68:71], a[236:239], v[228:231], v[68:71]\n"
      "global_load_lds_dwordx4 v237, s[52:53] offset:256 \n"
      "s_waitcnt lgkmcnt(2)\n"
      "v_mfma_f32_16x16x32_bf16 v[72:75], a[216:219], v[216:219], v[72:75]\n"
      "v_mfma_f32_16x16x32_bf16 v[76:79], a[220:223], v[216:219], v[76:79]\n"
      "s_add_u32 m0, s17, 0xae00\n"
      "v_mfma_f32_16x16x32_bf16 v[80:83], a[224:227], v[216:219], v[80:83]\n"
      "ds_read_b128 v[228:231], v233 offset:24576\n"
      "global_load_lds_dwordx4 v237, s[52:53] offset:512 \n"
      "v_mfma_f32_16x16x32_bf16 v[84:87], a[228:231], v[216:219], v[84:87]\n"
      "v_mfma_f32_16x16x32_bf16 v[88:91], a[232:235], v[216:219], v[88:91]\n"
      "v_mfma_f32_16x16x32_bf16 v[92:95], a[236:239], v[216:219], v[92:95]\n"
      "s_waitcnt lgkmcnt(2)\n"
      "v_mfma_f32_16x16x32_bf16 v[96:99], a[216:219], v[220:223], v[96:99]\n"
      "v_mfma_f32_16x16x32_bf16 v[100:103], a[220:223], v[220:223], v[100:103]\n"
      "v_mfma_f32_16x16x32_bf16 v[104:107], a[224:227], v[220:223], v[104:107]\n"
      "ds_read_b128 v[216:219], v233 offset:28672\n"
      "v_mfma_f32_16x16x32_bf16 v[108:111], a[228:231], v[220:223], v[108:111]\n"
      "v_mfma_f32_16x16x32_bf16 v[112:115], a[232:235], v[220:223], v[112:115]\n"
      "v_mfma_f32_16x16x32_bf16 v[116:119], a[236:239], v[220:223], v[116:119]\n"
      "s_waitcnt lgkmcnt(2)\n"
      "v_mfma_f32_16x16x32_bf16 v[120:123], a[216:219], v[224:227], v[120:123]\n"
      "v_mfma_f32_16x16x32_bf16 v[124:127], a[220:223], v[224:227], v[124:127]\n"
      "v_mfma_f32_16x16x32_bf16 v[128:131], a[224:227], v[224:227], v[128:131]\n"
      "ds_read_b128 v[220:223], v233 offset:32768\n"
      "v_add_u32 v233, v249, v233\n"
      "v_mfma_f32_16x16x32_bf16 v[132:135], a[228:231], v[224:227], v[132:135]\n"
      "v_mfma_f32_16x16x32_bf16 v[136:139], a[232:235], v[224:227], v[136:139]\n"
      "v_mfma_f32_16x16x32_bf16 v[140:143], a[236:239], v[224:227], v[140:143]\n"
      "s_waitcnt lgkmcnt(2)\n"
      "v_mfma_f32_16x16x32_bf16 v[144:147], a[216:219], v[228:231], v[144:147]\n"
      "v_mfma_f32_16x16x32_bf16 v[148:151], a[220:223], v[228:231], v[148:151]\n"
      "v_mfma_f32_16x16x32_bf16 v[152:155], a[224:227], v[228:231], v[152:155]\n"
      "ds_read_b128 v[224:227], v234 offset:0\n"
      "v_mfma_f32_16x16x32_bf16 v[156:159], a[228:231], v[228:231], v[156:159]\n"
      "v_mfma_f32_16x16x32_bf16 v[160:163], a[232:235], v[228:231], v[160:163]\n"
      "v_mfma_f32_16x16x32_bf16 v[164:167], a[236:239], v[228:231], v[164:167]\n"
      "s_waitcnt lgkmcnt(2)\n"
      "v_mfma_f32_16x16x32_bf16 v[168:171], a[216:219], v[216:219], v[168:171]\n"
      "v_mfma_f32_16x16x32_bf16 v[172:175], a[220:223], v[216:219], v[172:175]\n"
      "v_mfma_f32_16x16x32_bf16 v[176:179], a[224:227], v[216:219], v[176:179]\n"
      "ds_read_b128 v[228:231], v234 offset:4096\n"
      "v_mfma_f32_16x16x32_bf16 v[180:183], a[228:231], v[216:219], v[180:183]\n"
      "v_mfma_f32_16x16x32_bf16 v[184:187], a[232:235], v[216:219], v[184:187]\n"
      "v_mfma_f32_16x16x32_bf16 v[188:191], a[236:239], v[216:219], v[188:191]\n"
      "s_waitcnt lgkmcnt(2)\n"
      "v_mfma_f32_16x16x32_bf16 v[192:195], a[216:219], v[220:223], v[192:195]\n"
      "v_mfma_f32_16x16x32_bf16 v[196:199], a[220:223], v[220:223], v[196:199]\n"
      "v_mfma_f32_16x16x32_bf16 v[200:203], a[224:227], v[220:223], v[200:203]\n"
      "ds_read_b128 v[216:219], v234 offset:8192\n"
      "v_mfma_f32_16x16x32_bf16 v[204:207], a[228:231], v[220:223], v[204:207]\n"
      "v_mfma_f32_16x16x32_bf16 v[208:211], a[232:235], v[220:223], v[208:211]\n"
      "v_mfma_f32_16x16x32_bf16 v[212:215], a[236:239], v[220:223], v[212:215]\n"
      "s_waitcnt lgkmcnt(2)\n"
      "v_mfma_f32_16x16x32_bf16 v[0:3], a[0:3], v[224:227], v[0:3]\n"
      "ds_read_b128 v[244:247], v250\n"
      "v_mfma_f32_16x16x32_bf16 v[4:7], a[4:7], v[224:227], v[4:7]\n"
      "ds_read_b64 v[252:253], v250 offset:4096\n"
      "v_mfma_f32_16x16x32_bf16 v[8:11], a[8:11], v[224:227], v[8:11]\n"
      "ds_read_b128 v[220:223], v234 offset:12288\n"
      "v_mfma_f32_16x16x32_bf16 v[12:15], a[12:15], v[224:227], v[12:15]\n"
      "v_mfma_f32_16x16x32_bf16 v[16:19], a[16:19], v[224:227], v[16:19]\n"
      "v_mfma_f32_16x16x32_bf16 v[20:23], a[20:23], v[224:227], v[20:23]\n"
      "s_waitcnt lgkmcnt(4)\n"
      "v_mfma_f32_16x16x32_bf16 v[24:27], a[0:3], v[228:231], v[24:27]\n"
      "v_mfma_f32_16x16x32_bf16 v[28:31], a[4:7], v[228:231], v[28:31]\n"
      "v_mfma_f32_16x16x32_bf16 v[32:35], a[8:11], v[228:231], v[32:35]\n"
      "ds_read_b128 v[224:227], v234 offset:16384\n"
      "v_mfma_f32_16x16x32_bf16 v[36:39], a[12:15], v[228:231], v[36:39]\n"
      "global_load_dwordx4 a[172:175], v239, s[58:59] offset:3072 sc1\n"
      "v_mfma_f32_16x16x32_bf16 v[40:43], a[16:19], v[228:231], v[40:43]\n"
      "v_mfma_f32_16x16x32_bf16 v[44:47], a[20:23], v[228:231], v[44:47]\n"
      "s_waitcnt lgkmcnt(4)\n"
      "v_mfma_f32_16x16x32_bf16 v[48:51], a[0:3], v[216:219], v[48:51]\n"
      "v_mfma_f32_16x16x32_bf16 v[52:55], a[4:7], v[216:219], v[52:55]\n"
      "s_add_u32 s58, s24, 0x9000\n"
      "s_addc_u32 s59, s25, 0\n"
      "global_load_dwordx4 a[176:179], v239, s[58:59] offset:0 sc1\n"
      "v_mfma_f32_16x16x32_bf16 v[56:59], a[8:11], v[216:219], v[56:59]\n"
      "ds_read_b128 v[228:231], v234 offset:20480\n"
      "v_mfma_f32_16x16x32_bf16 v[60:63], a[12:15], v[216:219], v[60:63]\n"
      "v_mfma_f32_16x16x32_bf16 v[64:67], a[16:19], v[216:219], v[64:67]\n"
      "v_mfma_f32_16x16x32_bf16 v[68:71], a[20:23], v[216:219], v[68:71]\n"
      "global_load_dwordx4 a[180:183], v239, s[58:59] offset:1024 sc1\n"
      "s_waitcnt lgkmcnt(2)\n"
      "v_mfma_f32_16x16x32_bf16 v[72:75], a[0:3], v[220:223], v[72:75]\n"
      "v_mfma_f32_16x16x32_bf16 v[76:79], a[4:7], v[220:223], v[76:79]\n"
      "v_mfma_f32_16x16x32_bf16 v[80:83], a[8:11], v[220:223], v[80:83]\n"
      "ds_read_b128 v[216:219], v234 offset:24576\n"
      "v_mfma_f32_16x16x32_bf16 v[84:87], a[12:15], v[220:223], v[84:87]\n"
      "global_load_dwordx4 a[184:187], v239, s[58:59] offset:2048 sc1\n"
      "v_mfma_f32_16x16x32_bf16 v[88:91], a[16:19], v[220:223], v[88:91]\n"
      "v_mfma_f32_16x16x32_bf16 v[92:95], a[20:23], v[220:223], v[92:95]\n"
      "s_waitcnt lgkmcnt(2)\n"
      "v_mfma_f32_16x16x32_bf16 v[96:99], a[0:3], v[224:227], v[96:99]\n"
      "v_mfma_f32_16x16x32_bf16 v[100:103], a[4:7], v[224:227], v[100:103]\n"
      "global_load_dwordx4 a[188:191], v239, s[58:59] offset:3072 sc1\n"
      "v_mfma_f32_16x16x32_bf16 v[104:107], a[8:11], v[224:227], v[104:107]\n"
      "ds_read_b128 v[220:223], v234 offset:28672\n"
      "v_mfma_f32_16x16x32_bf16 v[108:111], a[12:15], v[224:227], v[108:111]\n"
      "v_mfma_f32_16x16x32_bf16 v[112:115], a[16:19], v[224:227], v[112:115]\n"
      "v_mfma_f32_16x16x32_bf16 v[116:119], a[20:23], v[224:227], v[116:119]\n"
      "s_add_u32 s58, s24, 0xa000\n"
      "s_addc_u32 s59, s25, 0\n"
      "global_load_dwordx4 a[192:195], v239, s[58:59] offset:0 sc1\n"
      "s_waitcnt lgkmcnt(2)\n"
      "v_mfma_f32_16x16x32_bf16 v[120:123], a[0:3], v[228:231], v[120:123]\n"
      "v_mfma_f32_16x16x32_bf16 v[124:127], a[4:7], v[228:231], v[124:127]\n"
      "v_mfma_f32_16x16x32_bf16 v[128:131], a[8:11], v[228:231], v[128:131]\n"
      "ds_read_b128 v[224:227], v234 offset:32768\n"
      "v_add_u32 v234, v249, v234\n"
      "v_mfma_f32_16x16x32_bf16 v[132:135], a[12:15], v[228:231], v[132:135]\n"
      "global_load_dwordx4 a[196:199], v239, s[58:59] offset:1024 sc1\n"
      "v_mfma_f32_16x16x32_bf16 v[136:139], a[16:19], v[228:231], v[136:139]\n"
      "v_mfma_f32_16x16x32_bf16 v[140:143], a[20:23], v[228:231], v[140:143]\n"
      "s_waitcnt lgkmcnt(2)\n"
      "v_mfma_f32_16x16x32_bf16 v[144:147], a[0:3], v[216:219], v[144:147]\n"
      "v_mfma_f32_16x16x32_bf16 v[148:151], a[4:7], v[216:219], v[148:151]\n"
      "global_load_dwordx4 a[200:203], v239, s[58:59] offset:2048 sc1\n"
      "v_mfma_f32_16x16x32_bf16 v[152:155], a[8:11], v[216:219], v[152:155]\n"
      "ds_read_b128 v[228:231], v235 offset:4096\n"
      "v_mfma_f32_16x16x32_bf16 v[156:159], a[12:15], v[216:219], v[156:159]\n"
      "v_mfma_f32_16x16x32_bf16 v[160:163], a[16:19], v[216:219], v[160:163]\n"
      "v_mfma_f32_16x16x32_bf16 v[164:167], a[20:23], v[216:219], v[164:167]\n"
      "global_load_dwordx4 a[204:207], v239, s[58:59] offset:3072 sc1\n"
      "s_waitcnt lgkmcnt(2)\n"
      "v_mfma_f32_16x16x32_bf16 v[168:171], a[0:3], v[220:223], v[168:171]\n"
      "v_mfma_f32_16x16x32_bf16 v[172:175], a[4:7], v[220:223], v[172:175]\n"
      "v_mfma_f32_16x16x32_bf16 v[176:179], a[8:11], v[220:223], v[176:179]\n"
      "ds_read_b128 v[216:219], v235 offset:8192\n"
      "v_mfma_f32_16x16x32_bf16 v[180:183], a[12:15], v[220:223], v[180:183]\n"
      "s_add_u32 s58, s24, 0xb000\n"
      "s_addc_u32 s59, s25, 0\n"
      "global_load_dwordx4 a[208:211], v239, s[58:59] offset:0 sc1\n"
      "v_mfma_f32_16x16x32_bf16 v[184:187], a[16:19], v[220:223], v[184:187]\n"
      "v_mfma_f32_16x16x32_bf16 v[188:191], a[20:23], v[220:223], v[188:191]\n"
      "s_waitcnt lgkmcnt(2)\n"
      "v_mfma_f32_16x16x32_bf16 v[192:195], a[0:3], v[224:227], v[192:195]\n"
      "v_mfma_f32_16x16x32_bf16 v[196:199], a[4:7], v[224:227], v[196:199]\n"
      "global_load_dwordx4 a[212:215], v239, s[58:59] offset:1024 sc1\n"
      "v_mfma_f32_16x16x32_bf16 v[200:203], a[8:11], v[224:227], v[200:203]\n"
      "ds_read_b128 v[220:223], v235 offset:12288\n"
      "v_mfma_f32_16x16x32_bf16 v[204:207], a[12:15], v[224:227], v[204:207]\n"
      "v_mfma_f32_16x16x32_bf16 v[208:211], a[16:19], v[224:227], v[208:211]\n"
      "v_mfma_f32_16x16x32_bf16 v[212:215], a[20:23], v[224:227], v[212:215]\n"
      "global_load_dwordx4 a[216:219], v239, s[58:59] offset:2048 sc1\n"
      "s_waitcnt lgkmcnt(2)\n"
      "v_mfma_f32_16x16x32_bf16 v[24:27], a[24:27], v[228:231], v[24:27]\n"
      "v_mfma_f32_16x16x32_bf16 v[28:31], a[28:31], v[228:231], v[28:31]\n"
      "v_mfma_f32_16x16x32_bf16 v[32:35], a[32:35], v[228:231], v[32:35]\n"
      "ds_read_b128 v[224:227], v235 offset:16384\n"
      "v_mfma_f32_16x16x32_bf16 v[36:39], a[36:39], v[228:231], v[36:39]\n"
      "global_load_dwordx4 a[220:223], v239, s[58:59] offset:3072 sc1\n"
      "v_mfma_f32_16x16x32_bf16 v[40:43], a[40:43], v[228:231], v[40:43]\n"
      "v_mfma_f32_16x16x32_bf16 v[44:47], a[44:47], v[228:231], v[44:47]\n"
      "s_waitcnt lgkmcnt(2)\n"
      "v_mfma_f32_16x16x32_bf16 v[48:51], a[24:27], v[216:219], v[48:51]\n"
      "v_cvt_pk_bf16_f32 v24, v24, v25\n"
      "v_cvt_pk_bf16_f32 v25, v26, v27\n"
      "v_mfma_f32_16x16x32_bf16 v[52:55], a[28:31], v[216:219], v[52:55]\n"
      "v_cvt_pk_bf16_f32 v26, v28, v29\n"
      "v_cvt_pk_bf16_f32 v27, v30, v31\n"
      "s_add_u32 s58, s24, 0xc000\n"
      "s_addc_u32 s59, s25, 0\n"
      "global_load_dwordx4 a[224:227], v239, s[58:59] offset:0 sc1\n"
      "v_mfma_f32_16x16x32_bf16 v[56:59], a[32:35], v[216:219], v[56:59]\n"
      "ds_read_b128 v[228:231], v235 offset:20480\n"
      "v_cvt_pk_bf16_f32 v28, v32, v33\n"
      "v_cvt_pk_bf16_f32 v29, v34, v35\n"
      "v_mfma_f32_16x16x32_bf16 v[60:63], a[36:39], v[216:219], v[60:63]\n"
      "v_cvt_pk_bf16_f32 v30, v36, v37\n"
      "v_cvt_pk_bf16_f32 v31, v38, v39\n"
      "v_mfma_f32_16x16x32_bf16 v[64:67], a[40:43], v[216:219], v[64:67]\n"
      "v_cvt_pk_bf16_f32 v32, v40, v41\n"
      "v_cvt_pk_bf16_f32 v33, v42, v43\n"
      "v_mfma_f32_16x16x32_bf16 v[68:71], a[44:47], v[216:219], v[68:71]\n"
      "v_cvt_pk_bf16_f32 v34, v44, v45\n"
      "v_cvt_pk_bf16_f32 v35, v46, v47\n"
      "global_load_dwordx4 a[228:231], v239, s[58:59] offset:1024 sc1\n"
      "s_waitcnt lgkmcnt(2)\n"
      "v_mfma_f32_16x16x32_bf16 v[72:75], a[24:27], v[220:223], v[72:75]\n"
      "v_cvt_pk_bf16_f32 v48, v48, v49\n"
      "v_cvt_pk_bf16_f32 v49, v50, v51\n"
      "v_mfma_f32_16x16x32_bf16 v[76:79], a[28:31], v[220:223], v[76:79]\n"
      "v_cvt_pk_bf16_f32 v50, v52, v53\n"
      "v_cvt_pk_bf16_f32 v51, v54, v55\n"
      "v_mfma_f32_16x16x32_bf16 v[80:83], a[32:35], v[220:223], v[80:83]\n"
      "ds_read_b128 v[216:219], v235 offset:24576\n"
      "v_cvt_pk_bf16_f32 v52, v56, v57\n"
      "v_cvt_pk_bf16_f32 v53, v58, v59\n"
      "v_mfma_f32_16x16x32_bf16 v[84:87], a[36:39], v[220:223], v[84:87]\n"
      "v_cvt_pk_bf16_f32 v54, v60, v61\n"
      "v_cvt_pk_bf16_f32 v55, v62, v63\n"
      "global_load_dwordx4 a[232:235], v239, s[58:59] offset:2048 sc1\n"
      "v_mfma_f32_16x16x32_bf16 v[88:91], a[40:43], v[220:223], v[88:91]\n"
      "v_cvt_pk_bf16_f32 v56, v64, v65\n"
      "v_cvt_pk_bf16_f32 v57, v66, v67\n"
      "v_mfma_f32_16x16x32_bf16 v[92:95], a[44:47], v[220:223], v[92:95]\n"
      "v_cvt_pk_bf16_f32 v58, v68, v69\n"
      "v_cvt_pk_bf16_f32 v59, v70, v71\n"
      "s_waitcnt lgkmcnt(2)\n"
      "v_mfma_f32_16x16x32_bf16 v[96:99], a[24:27], v[224:227], v[96:99]\n"
      "v_cvt_pk_bf16_f32 v72, v72, v73\n"
      "v_cvt_pk_bf16_f32 v73, v74, v75\n"
      "v_mfma_f32_16x16x32_bf16 v[100:103], a[28:31], v[224:227], v[100:103]\n"
      "v_cvt_pk_bf16_f32 v74, v76, v77\n"
      "v_cvt_pk_bf16_f32 v75, v78, v79\n"
      "global_load_dwordx4 a[236:239], v239, s[58:59] offset:3072 sc1\n"
      "v_mfma_f32_16x16x32_bf16 v[104:107], a[32:35], v[224:227], v[104:107]\n"
      "ds_read_b128 v[220:223], v235 offset:28672\n"
      "v_cvt_pk_bf16_f32 v76, v80, v81\n"
      "v_cvt_pk_bf16_f32 v77, v82, v83\n"
      "v_mfma_f32_16x16x32_bf16 v[108:111], a[36:39], v[224:227], v[108:111]\n"
      "v_cvt_pk_bf16_f32 v78, v84, v85\n"
      "v_cvt_pk_bf16_f32 v79, v86, v87\n"
      "v_mfma_f32_16x16x32_bf16 v[112:115], a[40:43], v[224:227], v[112:115]\n"
      "v_cvt_pk_bf16_f32 v80, v88, v89\n"
      "LPD6:\n"
      "s_cbranch_execz LPD7\n"
      "v_cvt_pk_bf16_f32 v81, v90, v91\n"
      "v_mfma_f32_16x16x32_bf16 v[116:119], a[44:47], v[224:227], v[116:119]\n"
      "v_cvt_pk_bf16_f32 v82, v92, v93\n"
      "v_cvt_pk_bf16_f32 v83, v94, v95\n"
      "s_add_u32 s58, s24, 0xd000\n"
      "s_addc_u32 s59, s25, 0\n"
      "global_load_dwordx4 a[0:3], v239, s[58:59] offset:0 sc1\n"
      "s_waitcnt lgkmcnt(2)\n"
      "v_mfma_f32_16x16x32_bf16 v[120:123], a[24:27], v[228:231], v[120:123]\n"
      "v_cvt_pk_bf16_f32 v96, v96, v97\n"
      "v_cvt_pk_bf16_f32 v97, v98, v99\n"
      "v_mfma_f32_16x16x32_bf16 v[124:127], a[28:31], v[228:231], v[124:127]\n"
      "v_cvt_pk_bf16_f32 v98, v100, v101\n"
      "v_cvt_pk_bf16_f32 v99, v102, v103\n"
      "v_mfma_f32_16x16x32_bf16 v[128:131], a[32:35], v[228:231], v[128:131]\n"
      "ds_read_b128 v[224:227], v235 offset:32768\n"
      "v_cvt_pk_bf16_f32 v100, v104, v105\n"
      "v_cvt_pk_bf16_f32 v101, v106, v107\n"
      "v_mfma_f32_16x16x32_bf16 v[132:135], a[36:39], v[228:231], v[132:135]\n"
      "v_cvt_pk_bf16_f32 v102, v108, v109\n"
      "v_cvt_pk_bf16_f32 v103, v110, v111\n"
      "global_load_dwordx4 a[4:7], v239, s[58:59] offset:1024 sc1\n"
      "v_mfma_f32_16x16x32_bf16 v[136:139], a[40:43], v[228:231], v[136:139]\n"
      "v_cvt_pk_bf16_f32 v104, v112, v113\n"
      "v_cvt_pk_bf16_f32 v105, v114, v115\n"
      "v_mfma_f32_16x16x32_bf16 v[140:143], a[44:47], v[228:231], v[140:143]\n"
      "v_cvt_pk_bf16_f32 v106, v116, v117\n"
      "v_cvt_pk_bf16_f32 v107, v118, v119\n"
      "s_waitcnt lgkmcnt(2)\n"
      "v_mfma_f32_16x16x32_bf16 v[144:147], a[24:27], v[216:219], v[144:147]\n"
      "v_cvt_pk_bf16_f32 v120, v120, v121\n"
      "v_cvt_pk_bf16_f32 v121, v122, v123\n"
      "v_mfma_f32_16x16x32_bf16 v[148:151], a[28:31], v[216:219], v[148:151]\n"
      "v_cvt_pk_bf16_f32 v122, v124, v125\n"
      "v_cvt_pk_bf16_f32 v123, v126, v127\n"
      "global_load_dwordx4 a[8:11], v239, s[58:59] offset:2048 sc1\n"
      "v_mfma_f32_16x16x32_bf16 v[152:155], a[32:35], v[216:219], v[152:155]\n"
      "ds_read_b128 v[228:231], v235 offset:0\n"
      "v_add_u32 v235, v249, v235\n"
      "s_waitcnt vmcnt(26)\n"
      "s_barrier\n"
      "v_cvt_pk_bf16_f32 v124, v128, v129\n"
      "v_cvt_pk_bf16_f32 v125, v130, v131\n"
      "v_mfma_f32_16x16x32_bf16 v[156:159], a[36:39], v[216:219], v[156:159]\n"
      "ds_read_b128 v[36:39], v232 offset:0\n"
      "v_cvt_pk_bf16_f32 v126, v132, v133\n"
      "v_cvt_pk_bf16_f32 v127, v134, v135\n"
      "v_mfma_f32_16x16x32_bf16 v[160:163], a[40:43], v[216:219], v[160:163]\n"
      "v_cvt_pk_bf16_f32 v128, v136, v137\n"
      "v_cvt_pk_bf16_f32 v129, v138, v139\n"
      "v_mfma_f32_16x16x32_bf16 v[164:167], a[44:47], v[216:219], v[164:167]\n"
      "ds_read_b128 v[40:43], v233 offset:0\n"
      "v_cvt_pk_bf16_f32 v130, v140, v141\n"
      "v_cvt_pk_bf16_f32 v131, v142, v143\n"
      "global_load_dwordx4 a[12:15], v239, s[58:59] offset:3072 sc1\n"
      "s_waitcnt lgkmcnt(4)\n"
      "v_mfma_f32_16x16x32_bf16 v[168:171], a[24:27], v[220:223], v[168:171]\n"
      "v_cvt_pk_bf16_f32 v144, v144, v145\n"
      "v_cvt_pk_bf16_f32 v145, v146, v147\n"
      "v_mfma_f32_16x16x32_bf16 v[172:175], a[28:31], v[220:223], v[172:175]\n"
      "ds_read_b128 v[44:47], v234 offset:0\n"
      "v_cvt_pk_bf16_f32 v146, v148, v149\n"
      "v_cvt_pk_bf16_f32 v147, v150, v151\n"
      "v_mfma_f32_16x16x32_bf16 v[176:179], a[32:35], v[220:223], v[176:179]\n"
      "v_cvt_pk_bf16_f32 v148, v152, v153\n"
      "v_cvt_pk_bf16_f32 v149, v154, v155\n"
      "v_mfma_f32_16x16x32_bf16 v[180:183], a[36:39], v[220:223], v[180:183]\n"
      "ds_read_b128 v[60:63], v235 offset:0\n"
      "v_cvt_pk_bf16_f32 v150, v156, v157\n"
      "v_cvt_pk_bf16_f32 v151, v158, v159\n"
      "s_add_u32 s58, s24, 0xe000\n"
      "s_addc_u32 s59, s25, 0\n"
      "global_load_dwordx4 a[16:19], v239, s[58:59] offset:0 sc1\n"
      "v_mfma_f32_16x16x32_bf16 v[184:187], a[40:43], v[220:223], v[184:187]\n"
      "v_cvt_pk_bf16_f32 v152, v160, v161\n"
      "v_cvt_pk_bf16_f32 v153, v162, v163\n"
      "v_mfma_f32_16x16x32_bf16 v[188:191], a[44:47], v[220:223], v[188:191]\n"
      "ds_read_b128 v[64:67], v232 offset:8192\n"
      "v_cvt_pk_bf16_f32 v154, v164, v165\n"
      "v_cvt_pk_bf16_f32 v155, v166, v167\n"
      "s_waitcnt lgkmcnt(6)\n"
      "v_mfma_f32_16x16x32_bf16 v[192:195], a[24:27], v[224:227], v[192:195]\n"
      "v_cvt_pk_bf16_f32 v168, v168, v169\n"
      "v_cvt_pk_bf16_f32 v169, v170, v171\n"
      "v_mfma_f32_16x16x32_bf16 v[196:199], a[28:31], v[224:227], v[196:199]\n"
      "ds_read_b128 v[68:71], v233 offset:8192\n"
      "v_cvt_pk_bf16_f32 v170, v172, v173\n"
      "v_cvt_pk_bf16_f32 v171, v174, v175\n"
      "global_load_dwordx4 a[20:23], v239, s[58:59] offset:1024 sc1\n"
      "v_mfma_f32_16x16x32_bf16 v[200:203], a[32:35], v[224:227], v[200:203]\n"
      "v_cvt_pk_bf16_f32 v172, v176, v177\n"
      "v_cvt_pk_bf16_f32 v173, v178, v179\n"
      "v_mfma_f32_16x16x32_bf16 v[204:207], a[36:39], v[224:227], v[204:207]\n"
      "ds_read_b128 v[84:87], v234 offset:8192\n"
      "v_cvt_pk_bf16_f32 v174, v180, v181\n"
      "v_cvt_pk_bf16_f32 v175, v182, v183\n"
      "v_mfma_f32_16x16x32_bf16 v[208:211], a[40:43], v[224:227], v[208:211]\n"
      "v_cvt_pk_bf16_f32 v176, v184, v185\n"
      "v_cvt_pk_bf16_f32 v177, v186, v187\n"
      "v_mfma_f32_16x16x32_bf16 v[212:215], a[44:47], v[224:227], v[212:215]\n"
      "ds_read_b128 v[88:91], v235 offset:8192\n"
      "v_cvt_pk_bf16_f32 v178, v188, v189\n"
      "v_cvt_pk_bf16_f32 v179, v190, v191\n"
      "global_load_dwordx4 v[140:143], v239, s[58:59] offset:2048 sc1\n"
      "s_waitcnt lgkmcnt(8)\n"
      "v_mfma_f32_16x16x32_bf16 v[0:3], a[24:27], v[228:231], v[0:3]\n"
      "v_cvt_pk_bf16_f32 v192, v192, v193\n"
      "v_cvt_pk_bf16_f32 v193, v194, v195\n"
      "v_mfma_f32_16x16x32_bf16 v[4:7], a[28:31], v[228:231], v[4:7]\n"
      "ds_read_b128 v[92:95], v232 offset:16384\n"
      "v_cvt_pk_bf16_f32 v194, v196, v197\n"
      "v_cvt_pk_bf16_f32 v195, v198, v199\n"
      "v_mfma_f32_16x16x32_bf16 v[8:11], a[32:35], v[228:231], v[8:11]\n"
      "v_cvt_pk_bf16_f32 v196, v200, v201\n"
      "v_cvt_pk_bf16_f32 v197, v202, v203\n"
      "v_mfma_f32_16x16x32_bf16 v[12:15], a[36:39], v[228:231], v[12:15]\n"
      "ds_read_b128 v[108:111], v233 offset:16384\n"
      "v_cvt_pk_bf16_f32 v198, v204, v205\n"
      "v_cvt_pk_bf16_f32 v199, v206, v207\n"
      "global_load_dwordx4 v[156:159], v239, s[58:59] offset:3072 sc1\n"
      "v_mfma_f32_16x16x32_bf16 v[16:19], a[40:43], v[228:231], v[16:19]\n"
      "ds_read_b128 v[112:115], v234 offset:16384\n"
      "v_cvt_pk_bf16_f32 v200, v208, v209\n"
      "v_cvt_pk_bf16_f32 v201, v210, v211\n"
      "v_mfma_f32_16x16x32_bf16 v[20:23], a[44:47], v[228:231], v[20:23]\n"
      "ds_read_b128 v[116:119], v235 offset:16384\n"
      "v_cvt_pk_bf16_f32 v202, v212, v213\n"
      "v_cvt_pk_bf16_f32 v203, v214, v215\n"
      "s_waitcnt lgkmcnt(8)\n"
      "v_mfma_f32_16x16x32_bf16 v[132:135], a[240:243], v[36:39], v[0:3]\n"
      "s_mov_b64 vcc, 0\n"
      "v_mfma_f32_16x16x32_bf16 v[132:135], a[244:247], v[40:43], v[132:135]\n"
      "s_add_u32 s58, s24, 0xf000\n"
      "s_addc_u32 s59, s25, 0\n"
      "global_load_dwordx4 v[160:163], v239, s[58:59] offset:0 sc1\n"
      "v_mfma_f32_16x16x32_bf16 v[132:135], a[248:251], v[44:47], v[132:135]\n"
      "v_mfma_f32_16x16x32_bf16 v[132:135], a[252:255], v[60:63], v[132:135]\n"
      "s_waitcnt lgkmcnt(4)\n"
      "v_mfma_f32_16x16x32_bf16 v[132:135], a[48:51], v[64:67], v[132:135]\n"
      "global_load_dwordx4 v[164:167], v239, s[58:59] offset:1024 sc1\n"
      "v_mfma_f32_16x16x32_bf16 v[132:135], a[52:55], v[68:71], v[132:135]\n"
      "v_mfma_f32_16x16x32_bf16 v[132:135], a[56:59], v[84:87], v[132:135]\n"
      "v_mfma_f32_16x16x32_bf16 v[132:135], a[60:63], v[88:91], v[132:135]\n"
      "global_load_dwordx4 v[180:183], v239, s[58:59] offset:2048 sc1\n"
      "s_waitcnt lgkmcnt(0)\n"
      "v_mfma_f32_16x16x32_bf16 v[132:135], a[64:67], v[92:95], v[132:135]\n"
      "v_mfma_f32_16x16x32_bf16 v[132:135], a[68:71], v[108:111], v[132:135]\n"
      "v_mfma_f32_16x16x32_bf16 v[132:135], a[72:75], v[112:115], v[132:135]\n"
      "global_load_dwordx4 v[184:187], v239, s[58:59] offset:3072 sc1\n"
      "v_mfma_f32_16x16x32_bf16 v[132:135], a[76:79], v[116:119], v[132:135]\n"
      "v_mfma_f32_16x16x32_bf16 v[136:139], a[80:83], v[36:39], v[4:7]\n"
      "v_mfma_f32_16x16x32_bf16 v[136:139], a[84:87], v[40:43], v[136:139]\n"
      "s_add_u32 s58, s24, 0x10000\n"
      "s_addc_u32 s59, s25, 0\n"
      "global_load_dwordx4 v[188:191], v239, s[58:59] offset:0 sc1\n"
      "v_mfma_f32_16x16x32_bf16 v[136:139], a[88:91], v[44:47], v[136:139]\n"
      "v_mfma_f32_16x16x32_bf16 v[136:139], a[92:95], v[60:63], v[136:139]\n"
      "v_cvt_pk_bf16_f32 v208, v132, v133\n"
      "v_mfma_f32_16x16x32_bf16 v[136:139], a[96:99], v[64:67], v[136:139]\n"
      "global_load_dwordx4 v[204:207], v239, s[58:59] offset:1024 sc1\n"
      "v_mfma_f32_16x16x32_bf16 v[136:139], a[100:103], v[68:71], v[136:139]\n"
      "v_cvt_pk_bf16_f32 v209, v134, v135\n"
      "v_mfma_f32_16x16x32_bf16 v[136:139], a[104:107], v[84:87], v[136:139]\n"
      "v_mfma_f32_16x16x32_bf16 v[136:139], a[108:111], v[88:91], v[136:139]\n"
      "global_load_dwordx4 a[24:27], v239, s[58:59] offset:2048 sc1\n"
      "v_mfma_f32_16x16x32_bf16 v[136:139], a[112:115], v[92:95], v[136:139]\n"
      "v_mfma_f32_16x16x32_bf16 v[136:139], a[116:119], v[108:111], v[136:139]\n"
      "v_mfma_f32_16x16x32_bf16 v[136:139], a[120:123], v[112:115], v[136:139]\n"
      "global_load_dwordx4 a[28:31], v239, s[58:59] offset:3072 sc1\n"
      "v_mfma_f32_16x16x32_bf16 v[136:139], a[124:127], v[116:119], v[136:139]\n"
      "v_mfma_f32_16x16x32_bf16 v[132:135], a[128:131], v[36:39], v[8:11]\n"
      "v_mfma_f32_16x16x32_bf16 v[132:135], a[132:135], v[40:43], v[132:135]\n"
      "s_add_u32 s58, s24, 0x11000\n"
      "s_addc_u32 s59, s25, 0\n"
      "global_load_dwordx4 a[32:35], v239, s[58:59] offset:0 sc1\n"
      "v_mfma_f32_16x16x32_bf16 v[132:135], a[136:139], v[44:47], v[132:135]\n"
      "v_mfma_f32_16x16x32_bf16 v[132:135], a[140:143], v[60:63], v[132:135]\n"
      "v_cvt_pk_bf16_f32 v210, v136, v137\n"
      "v_mfma_f32_16x16x32_bf16 v[132:135], a[144:147], v[64:67], v[132:135]\n"
      "global_load_dwordx4 a[36:39], v239, s[58:59] offset:1024 sc1\n"
      "v_mfma_f32_16x16x32_bf16 v[132:135], a[148:151], v[68:71], v[132:135]\n"
      "v_cvt_pk_bf16_f32 v211, v138, v139\n"
      "v_mfma_f32_16x16x32_bf16 v[132:135], a[152:155], v[84:87], v[132:135]\n"
      "v_mfma_f32_16x16x32_bf16 v[132:135], a[156:159], v[88:91], v[132:135]\n"
      "global_load_dwordx4 a[40:43], v239, s[58:59] offset:2048 sc1\n"
      "s_waitcnt vmcnt(35)\n"
      "v_mfma_f32_16x16x32_bf16 v[132:135], a[160:163], v[92:95], v[132:135]\n"
      "v_mfma_f32_16x16x32_bf16 v[132:135], a[164:167], v[108:111], v[132:135]\n"
      "v_mfma_f32_16x16x32_bf16 v[132:135], a[168:171], v[112:115], v[132:135]\n"
      "global_load_dwordx4 a[44:47], v239, s[58:59] offset:3072 sc1\n"
      "v_mfma_f32_16x16x32_bf16 v[132:135], a[172:175], v[116:119], v[132:135]\n"
      "ds_write_b128 v244, v[208:211] offset:12288\n"
      "s_waitcnt vmcnt(32)\n"
      "v_mfma_f32_16x16x32_bf16 v[136:139], a[176:179], v[36:39], v[12:15]\n"
      "v_mfma_f32_16x16x32_bf16 v[136:139], a[180:183], v[40:43], v[136:139]\n"
      "s_mul_i32 s78, s90, s19\n"
      "v_mfma_f32_16x16x32_bf16 v[136:139], a[184:187], v[44:47], v[136:139]\n"
      "v_add_u32 v236, s78, v236\n"
      "v_mfma_f32_16x16x32_bf16 v[136:139], a[188:191], v[60:63], v[136:139]\n"
      "s_mul_i32 s78, s78, s99\n"
      "s_waitcnt vmcnt(28)\n"
      "v_mfma_f32_16x16x32_bf16 v[136:139], a[192:195], v[64:67], v[136:139]\n"
      "v_add_u32 v237, s78, v237\n"
      "v_mfma_f32_16x16x32_bf16 v[136:139], a[196:199], v[68:71], v[136:139]\n"
      "s_add_u32 s52, s20, s33\n"
      "s_addc_u32 s53, s21, 0\n"
      "s_add_u32 m0, s17, 0xc000\n"
      "v_mfma_f32_16x16x32_bf16 v[136:139], a[200:203], v[84:87], v[136:139]\n"
      "global_load_lds_dwordx4 v236, s[52:53] offset:0 \n"
      "v_mfma_f32_16x16x32_bf16 v[136:139], a[204:207], v[88:91], v[136:139]\n"
      "v_cvt_pk_bf16_f32 v212, v132, v133\n"
      "s_waitcnt vmcnt(25)\n"
      "v_mfma_f32_16x16x32_bf16 v[136:139], a[208:211], v[92:95], v[136:139]\n"
      "v_cvt_pk_bf16_f32 v213, v134, v135\n"
      "v_mfma_f32_16x16x32_bf16 v[136:139], a[212:215], v[108:111], v[136:139]\n"
      "v_mfma_f32_16x16x32_bf16 v[136:139], a[216:219], v[112:115], v[136:139]\n"
      "v_mfma_f32_16x16x32_bf16 v[136:139], a[220:223], v[116:119], v[136:139]\n"
      "s_add_u32 m0, s17, 0xdf00\n"
      "s_waitcnt vmcnt(21)\n"
      "v_mfma_f32_16x16x32_bf16 v[132:135], a[224:227], v[36:39], v[16:19]\n"
      "global_load_lds_dwordx4 v236, s[52:53] offset:256 \n"
      "v_mfma_f32_16x16x32_bf16 v[132:135], a[228:231], v[40:43], v[132:135]\n"
      "v_mfma_f32_16x16x32_bf16 v[132:135], a[232:235], v[44:47], v[132:135]\n"
      "v_mfma_f32_16x16x32_bf16 v[132:135], a[236:239], v[60:63], v[132:135]\n"
      "v_cvt_pk_bf16_f32 v214, v136, v137\n"
      "s_waitcnt vmcnt(18)\n"
      "v_mfma_f32_16x16x32_bf16 v[132:135], a[0:3], v[64:67], v[132:135]\n"
      "v_cvt_pk_bf16_f32 v215, v138, v139\n"
      "v_mfma_f32_16x16x32_bf16 v[132:135], a[4:7], v[68:71], v[132:135]\n"
      "s_add_u32 m0, s17, 0xfe00\n"
      "v_mfma_f32_16x16x32_bf16 v[132:135], a[8:11], v[84:87], v[132:135]\n"
      "global_load_lds_dwordx4 v236, s[52:53] offset:512 \n"
      "v_mfma_f32_16x16x32_bf16 v[132:135], a[12:15], v[88:91], v[132:135]\n"
      "s_waitcnt vmcnt(15)\n"
      "v_mfma_f32_16x16x32_bf16 v[132:135], a[16:19], v[92:95], v[132:135]\n"
      "v_mfma_f32_16x16x32_bf16 v[132:135], a[20:23], v[108:111], v[132:135]\n"
      "ds_write_b128 v245, v[212:215] offset:12288\n"
      "v_mfma_f32_16x16x32_bf16 v[132:135], v[140:143], v[112:115], v[132:135]\n"
      "v_mfma_f32_16x16x32_bf16 v[132:135], v[156:159], v[116:119], v[132:135]\n"
      "s_barrier\n"
      "s_add_u32 m0, s17, 0xd000\n"
      "s_waitcnt vmcnt(11)\n"
      "v_mfma_f32_16x16x32_bf16 v[136:139], v[160:163], v[36:39], v[20:23]\n"
      "ds_read_b128 v[36:39], v232 offset:256\n"
      "global_load_lds_dwordx4 v237, s[52:53] offset:0 \n"
      "v_mfma_f32_16x16x32_bf16 v[136:139], v[164:167], v[40:43], v[136:139]\n"
      "ds_read_b128 v[40:43], v233 offset:256\n"
      "v_mfma_f32_16x16x32_bf16 v[136:139], v[180:183], v[44:47], v[136:139]\n"
      "ds_read_b128 v[44:47], v234 offset:256\n"
      "v_mfma_f32_16x16x32_bf16 v[136:139], v[184:187], v[60:63], v[136:139]\n"
      "ds_read_b128 v[60:63], v235 offset:256\n"
      "v_cvt_pk_bf16_f32 v208, v132, v133\n"
      "s_waitcnt vmcnt(8)\n"
      "v_mfma_f32_16x16x32_bf16 v[136:139], v[188:191], v[64:67], v[136:139]\n"
      "ds_read_b128 v[64:67], v232 offset:8448\n"
      "v_cvt_pk_bf16_f32 v209, v134, v135\n"
      "v_mfma_f32_16x16x32_bf16 v[136:139], v[204:207], v[68:71], v[136:139]\n"
      "ds_read_b128 v[68:71], v233 offset:8448\n"
      "s_add_u32 m0, s17, 0xef00\n"
      "v_mfma_f32_16x16x32_bf16 v[136:139], a[24:27], v[84:87], v[136:139]\n"
      "ds_read_b128 v[84:87], v234 offset:8448\n"
      "global_load_lds_dwordx4 v237, s[52:53] offset:256 \n"
      "v_mfma_f32_16x16x32_bf16 v[136:139], a[28:31], v[88:91], v[136:139]\n"
      "ds_read_b128 v[88:91], v235 offset:8448\n"
      "s_waitcnt vmcnt(5)\n"
      "v_mfma_f32_16x16x32_bf16 v[136:139], a[32:35], v[92:95], v[136:139]\n"
      "ds_read_b128 v[92:95], v232 offset:16640\n"
      "v_mfma_f32_16x16x32_bf16 v[136:139], a[36:39], v[108:111], v[136:139]\n"
      "ds_read_b128 v[108:111], v233 offset:16640\n"
      "v_mfma_f32_16x16x32_bf16 v[136:139], a[40:43], v[112:115], v[136:139]\n"
      "ds_read_b128 v[112:115], v234 offset:16640\n"
      "v_mfma_f32_16x16x32_bf16 v[136:139], a[44:47], v[116:119], v[136:139]\n"
      "ds_read_b128 v[116:119], v235 offset:16640\n"
      "s_add_u32 m0, s17, 0x10e00\n"
      "s_waitcnt lgkmcnt(8)\n"
      "v_mfma_f32_16x16x32_bf16 v[132:135], a[240:243], v[36:39], v[0:3]\n"
      "global_load_lds_dwordx4 v237, s[52:53] offset:512 \n"
      "v_mfma_f32_16x16x32_bf16 v[132:135], a[244:247], v[40:43], v[132:135]\n"
      "v_mfma_f32_16x16x32_bf16 v[132:135], a[248:251], v[44:47], v[132:135]\n"
      "v_mfma_f32_16x16x32_bf16 v[132:135], a[252:255], v[60:63], v[132:135]\n"
      "v_cvt_pk_bf16_f32 v210, v136, v137\n"
      "s_waitcnt lgkmcnt(4)\n"
      "v_mfma_f32_16x16x32_bf16 v[132:135], a[48:51], v[64:67], v[132:135]\n"
      "v_cvt_pk_bf16_f32 v211, v138, v139\n"
      "v_mfma_f32_16x16x32_bf16 v[132:135], a[52:55], v[68:71], v[132:135]\n"
      "v_mfma_f32_16x16x32_bf16 v[132:135], a[56:59], v[84:87], v[132:135]\n"
      "s_add_u32 s52, s42, s33\n"
      "s_addc_u32 s53, s43, 0\n"
      "s_add_u32 m0, s17, 0x12000\n"
      "v_mfma_f32_16x16x32_bf16 v[132:135], a[60:63], v[88:91], v[132:135]\n"
      "global_load_lds_dwordx4 v236, s[52:53] offset:0 \n"
      "s_waitcnt lgkmcnt(0)\n"
      "v_mfma_f32_16x16x32_bf16 v[132:135], a[64:67], v[92:95], v[132:135]\n"
      "v_mfma_f32_16x16x32_bf16 v[132:135], a[68:71], v[108:111], v[132:135]\n"
      "ds_write_b128 v244, v[208:211] offset:12416\n"
      "v_mfma_f32_16x16x32_bf16 v[132:135], a[72:75], v[112:115], v[132:135]\n"
      "v_mfma_f32_16x16x32_bf16 v[132:135], a[76:79], v[116:119], v[132:135]\n"
      "v_mfma_f32_16x16x32_bf16 v[136:139], a[80:83], v[36:39], v[4:7]\n"
      "s_add_u32 m0, s17, 0x13f00\n"
      "v_mfma_f32_16x16x32_bf16 v[136:139], a[84:87], v[40:43], v[136:139]\n"
      "global_load_lds_dwordx4 v236, s[52:53] offset:256 \n"
      "v_mfma_f32_16x16x32_bf16 v[136:139], a[88:91], v[44:47], v[136:139]\n"
      "v_mfma_f32_16x16x32_bf16 v[136:139], a[92:95], v[60:63], v[136:139]\n"
      "v_cvt_pk_bf16_f32 v208, v132, v133\n"
      "v_mfma_f32_16x16x32_bf16 v[136:139], a[96:99], v[64:67], v[136:139]\n"
      "v_cvt_pk_bf16_f32 v209, v134, v135\n"
      "v_mfma_f32_16x16x32_bf16 v[136:139], a[100:103], v[68:71], v[136:139]\n"
      "v_mfma_f32_16x16x32_bf16 v[136:139], a[104:107], v[84:87], v[136:139]\n"
      "s_add_u32 m0, s17, 0x15e00\n"
      "v_mfma_f32_16x16x32_bf16 v[136:139], a[108:111], v[88:91], v[136:139]\n"
      "global_load_lds_dwordx4 v236, s[52:53] offset:512 \n"
      "v_mfma_f32_16x16x32_bf16 v[136:139], a[112:115], v[92:95], v[136:139]\n"
      "v_mfma_f32_16x16x32_bf16 v[136:139], a[116:119], v[108:111], v[136:139]\n"
      "v_mfma_f32_16x16x32_bf16 v[136:139], a[120:123], v[112:115], v[136:139]\n"
      "v_mfma_f32_16x16x32_bf16 v[136:139], a[124:127], v[116:119], v[136:139]\n"
      "v_mfma_f32_16x16x32_bf16 v[132:135], a[128:131], v[36:39], v[8:11]\n"
      "s_add_u32 m0, s17, 0x13000\n"
      "v_mfma_f32_16x16x32_bf16 v[132:135], a[132:135], v[40:43], v[132:135]\n"
      "global_load_lds_dwordx4 v237, s[52:53] offset:0 \n"
      "v_mfma_f32_16x16x32_bf16 v[132:135], a[136:139], v[44:47], v[132:135]\n"
      "v_mfma_f32_16x16x32_bf16 v[132:135], a[140:143], v[60:63], v[132:135]\n"
      "v_cvt_pk_bf16_f32 v210, v136, v137\n"
      "v_mfma_f32_16x16x32_bf16 v[132:135], a[144:147], v[64:67], v[132:135]\n"
      "v_cvt_pk_bf16_f32 v211, v138, v139\n"
      "v_mfma_f32_16x16x32_bf16 v[132:135], a[148:151], v[68:71], v[132:135]\n"
      "v_mfma_f32_16x16x32_bf16 v[132:135], a[152:155], v[84:87], v[132:135]\n"
      "s_add_u32 m0, s17, 0x14f00\n"
      "v_mfma_f32_16x16x32_bf16 v[132:135], a[156:159], v[88:91], v[132:135]\n"
      "global_load_lds_dwordx4 v237, s[52:53] offset:256 \n"
      "v_mfma_f32_16x16x32_bf16 v[132:135], a[160:163], v[92:95], v[132:135]\n"
      "v_mfma_f32_16x16x32_bf16 v[132:135], a[164:167], v[108:111], v[132:135]\n"
      "ds_write_b128 v244, v[208:211] offset:24576\n"
      "v_mfma_f32_16x16x32_bf16 v[132:135], a[168:171], v[112:115], v[132:135]\n"
      "v_mfma_f32_16x16x32_bf16 v[132:135], a[172:175], v[116:119], v[132:135]\n"
      "v_mfma_f32_16x16x32_bf16 v[136:139], a[176:179], v[36:39], v[12:15]\n"
      "s_add_u32 m0, s17, 0x16e00\n"
      "v_mfma_f32_16x16x32_bf16 v[136:139], a[180:183], v[40:43], v[136:139]\n"
      "global_load_lds_dwordx4 v237, s[52:53] offset:512 \n"
      "v_mfma_f32_16x16x32_bf16 v[136:139], a[184:187], v[44:47], v[136:139]\n"
      "v_mfma_f32_16x16x32_bf16 v[136:139], a[188:191], v[60:63], v[136:139]\n"
      "v_cvt_pk_bf16_f32 v212, v132, v133\n"
      "v_mfma_f32_16x16x32_bf16 v[136:139], a[192:195], v[64:67], v[136:139]\n"
      "v_cvt_pk_bf16_f32 v213, v134, v135\n"
      "v_mfma_f32_16x16x32_bf16 v[136:139], a[196:199], v[68:71], v[136:139]\n"
      "v_mfma_f32_16x16x32_bf16 v[136:139], a[200:203], v[84:87], v[136:139]\n"
      "v_mfma_f32_16x16x32_bf16 v[136:139], a[204:207], v[88:91], v[136:139]\n"
      "v_mfma_f32_16x16x32_bf16 v[136:139], a[208:211], v[92:95], v[136:139]\n"
      "v_mfma_f32_16x16x32_bf16 v[136:139], a[212:215], v[108:111], v[136:139]\n"
      "v_mfma_f32_16x16x32_bf16 v[136:139], a[216:219], v[112:115], v[136:139]\n"
      "v_mfma_f32_16x16x32_bf16 v[136:139], a[220:223], v[116:119], v[136:139]\n"
      "v_mfma_f32_16x16x32_bf16 v[132:135], a[224:227], v[36:39], v[16:19]\n"
      "v_mfma_f32_16x16x32_bf16 v[132:135], a[228:231], v[40:43], v[132:135]\n"
      "v_mfma_f32_16x16x32_bf16 v[132:135], a[232:235], v[44:47], v[132:135]\n"
      "v_mfma_f32_16x16x32_bf16 v[132:135], a[236:239], v[60:63], v[132:135]\n"
      "v_cvt_pk_bf16_f32 v214, v136, v137\n"
      "v_mfma_f32_16x16x32_bf16 v[132:135], a[0:3], v[64:67], v[132:135]\n"
      "v_cvt_pk_bf16_f32 v215, v138, v139\n"
      "v_mfma_f32_16x16x32_bf16 v[132:135], a[4:7], v[68:71], v[132:135]\n"
      "v_mfma_f32_16x16x32_bf16 v[132:135], a[8:11], v[84:87], v[132:135]\n"
      "v_mfma_f32_16x16x32_bf16 v[132:135], a[12:15], v[88:91], v[132:135]\n"
      "v_mfma_f32_16x16x32_bf16 v[132:135], a[16:19], v[92:95], v[132:135]\n"
      "v_mfma_f32_16x16x32_bf16 v[132:135], a[20:23], v[108:111], v[132:135]\n"
      "ds_write_b128 v245, v[212:215] offset:24576\n"
      "v_mfma_f32_16x16x32_bf16 v[132:135], v[140:143], v[112:115], v[132:135]\n"
      "s_lshl_b32 s79, s98, 1\n"
      "v_mfma_f32_16x16x32_bf16 v[132:135], v[156:159], v[116:119], v[132:135]\n"
      "s_waitcnt lgkmcnt(2)\n"
      "s_barrier\n"
      "v_mad_i32_i24 v252, v251, s79, v252\n"
      "v_mfma_f32_16x16x32_bf16 v[136:139], v[160:163], v[36:39], v[20:23]\n"
      "ds_read_b128 v[36:39], v232 offset:24576\n"
      "s_mul_i32 s79, s79, s100\n"
      "v_mfma_f32_16x16x32_bf16 v[136:139], v[164:167], v[40:43], v[136:139]\n"
      "ds_read_b128 v[40:43], v233 offset:24576\n"
      "v_add_u32 v247, s79, v247\n"
      "v_mfma_f32_16x16x32_bf16 v[136:139], v[180:183], v[44:47], v[136:139]\n"
      "ds_read_b128 v[44:47], v234 offset:24576\n"
      "s_mul_i32 s79, s79, s101\n"
      "v_mfma_f32_16x16x32_bf16 v[136:139], v[184:187], v[60:63], v[136:139]\n"
      "ds_read_b128 v[60:63], v235 offset:24576\n"
      "v_cvt_pk_bf16_f32 v208, v132, v133\n"
      "v_mfma_f32_16x16x32_bf16 v[136:139], v[188:191], v[64:67], v[136:139]\n"
      "ds_read_b128 v[64:67], v232 offset:32768\n"
      "v_cvt_pk_bf16_f32 v209, v134, v135\n"
      "v_mfma_f32_16x16x32_bf16 v[136:139], v[204:207], v[68:71], v[136:139]\n"
      "ds_read_b128 v[68:71], v233 offset:32768\n"
      "v_add_u32 v253, s79, v253\n"
      "v_mfma_f32_16x16x32_bf16 v[136:139], a[24:27], v[84:87], v[136:139]\n"
      "ds_read_b128 v[84:87], v234 offset:32768\n"
      "v_mfma_f32_16x16x32_bf16 v[136:139], a[28:31], v[88:91], v[136:139]\n"
      "ds_read_b128 v[88:91], v235 offset:32768\n"
      "v_mfma_f32_16x16x32_bf16 v[136:139], a[32:35], v[92:95], v[136:139]\n"
      "ds_read_b128 v[92:95], v232 offset:40960\n"
      "v_mfma_f32_16x16x32_bf16 v[136:139], a[36:39], v[108:111], v[136:139]\n"
      "ds_read_b128 v[108:111], v233 offset:40960\n"
      "v_mfma_f32_16x16x32_bf16 v[136:139], a[40:43], v[112:115], v[136:139]\n"
      "ds_read_b128 v[112:115], v234 offset:40960\n"
      "v_mfma_f32_16x16x32_bf16 v[136:139], a[44:47], v[116:119], v[136:139]\n"
      "ds_read_b128 v[116:119], v235 offset:40960\n"
      "LJOIN:\n"
      "LPD7:\n"
      "s_cbranch_execz LPD10\n"
      "s_waitcnt lgkmcnt(8)\n"
      "v_mfma_f32_16x16x32_bf16 v[132:135], a[240:243], v[36:39], v[0:3]\n"
      "v_mfma_f32_16x16x32_bf16 v[132:135], a[244:247], v[40:43], v[132:135]\n"
      "s_add_u32 s54, s30, s32\n"
      "s_addc_u32 s55, s31, 0\n"
      "v_mfma_f32_16x16x32_bf16 v[132:135], a[248:251], v[44:47], v[132:135]\n"
      "v_mfma_f32_16x16x32_bf16 v[132:135], a[252:255], v[60:63], v[132:135]\n"
      "v_cvt_pk_bf16_f32 v210, v136, v137\n"
      "s_waitcnt lgkmcnt(4)\n"
      "v_mfma_f32_16x16x32_bf16 v[132:135], a[48:51], v[64:67], v[132:135]\n"
      "v_cvt_pk_bf16_f32 v211, v138, v139\n"
      "v_mfma_f32_16x16x32_bf16 v[132:135], a[52:55], v[68:71], v[132:135]\n"
      "s_mul_i32 s78, s91, s19\n"
      "v_mfma_f32_16x16x32_bf16 v[132:135], a[56:59], v[84:87], v[132:135]\n"
      "ds_read_b128 v[216:219], v246 offset:12288\n"
      "v_add_u32 v236, s78, v236\n"
      "v_mfma_f32_16x16x32_bf16 v[132:135], a[60:63], v[88:91], v[132:135]\n"
      "ds_read_b128 v[220:223], v246 offset:13312\n"
      "s_waitcnt lgkmcnt(2)\n"
      "v_mfma_f32_16x16x32_bf16 v[132:135], a[64:67], v[92:95], v[132:135]\n"
      "ds_read_b128 v[224:227], v246 offset:14336\n"
      "v_mfma_f32_16x16x32_bf16 v[132:135], a[68:71], v[108:111], v[132:135]\n"
      "ds_write_b128 v244, v[208:211] offset:24704\n"
      "v_mfma_f32_16x16x32_bf16 v[132:135], a[72:75], v[112:115], v[132:135]\n"
      "s_mul_i32 s78, s78, s99\n"
      "v_mfma_f32_16x16x32_bf16 v[132:135], a[76:79], v[116:119], v[132:135]\n"
      "v_add_u32 v237, s78, v237\n"
      "v_mfma_f32_16x16x32_bf16 v[136:139], a[80:83], v[36:39], v[4:7]\n"
      "v_mfma_f32_16x16x32_bf16 v[136:139], a[84:87], v[40:43], v[136:139]\n"
      "v_mfma_f32_16x16x32_bf16 v[136:139], a[88:91], v[44:47], v[136:139]\n"
      "v_mfma_f32_16x16x32_bf16 v[136:139], a[92:95], v[60:63], v[136:139]\n"
      "v_cvt_pk_bf16_f32 v208, v132, v133\n"
      "v_mfma_f32_16x16x32_bf16 v[136:139], a[96:99], v[64:67], v[136:139]\n"
      "v_cvt_pk_bf16_f32 v209, v134, v135\n"
      "v_mfma_f32_16x16x32_bf16 v[136:139], a[100:103], v[68:71], v[136:139]\n"
      "s_waitcnt lgkmcnt(3)\n"
      "v_mfma_f32_16x16x32_bf16 v[136:139], a[104:107], v[84:87], v[136:139]\n"
      "global_store_dwordx4 v247, v[216:219], s[54:55] offset:0 sc1\n"
      "v_mfma_f32_16x16x32_bf16 v[136:139], a[108:111], v[88:91], v[136:139]\n"
      "s_add_u32 s52, s20, s34\n"
      "s_addc_u32 s53, s21, 0\n"
      "v_mfma_f32_16x16x32_bf16 v[136:139], a[112:115], v[92:95], v[136:139]\n"
      "s_add_u32 m0, s17, 0x0\n"
      "v_mfma_f32_16x16x32_bf16 v[136:139], a[116:119], v[108:111], v[136:139]\n"
      "global_load_lds_dwordx4 v236, s[52:53] offset:0 nt\n"
      "v_mfma_f32_16x16x32_bf16 v[136:139], a[120:123], v[112:115], v[136:139]\n"
      "v_mfma_f32_16x16x32_bf16 v[136:139], a[124:127], v[116:119], v[136:139]\n"
      "v_mfma_f32_16x16x32_bf16 v[132:135], a[128:131], v[36:39], v[8:11]\n"
      "v_mfma_f32_16x16x32_bf16 v[132:135], a[132:135], v[40:43], v[132:135]\n"
      "v_mfma_f32_16x16x32_bf16 v[132:135], a[136:139], v[44:47], v[132:135]\n"
      "v_mfma_f32_16x16x32_bf16 v[132:135], a[140:143], v[60:63], v[132:135]\n"
      "v_cvt_pk_bf16_f32 v210, v136, v137\n"
      "v_mfma_f32_16x16x32_bf16 v[132:135], a[144:147], v[64:67], v[132:135]\n"
      "v_cvt_pk_bf16_f32 v211, v138, v139\n"
      "v_mfma_f32_16x16x32_bf16 v[132:135], a[148:151], v[68:71], v[132:135]\n"
      "v_mfma_f32_16x16x32_bf16 v[132:135], a[152:155], v[84:87], v[132:135]\n"
      "s_waitcnt lgkmcnt(2)\n"
      "v_mfma_f32_16x16x32_bf16 v[132:135], a[156:159], v[88:91], v[132:135]\n"
      "global_store_dwordx4 v252, v[220:223], s[54:55] offset:0 sc1\n"
      "v_mfma_f32_16x16x32_bf16 v[132:135], a[160:163], v[92:95], v[132:135]\n"
      "s_add_u32 m0, s17, 0x1f00\n"
      "v_mfma_f32_16x16x32_bf16 v[132:135], a[164:167], v[108:111], v[132:135]\n"
      "ds_write_b128 v244, v[208:211] offset:0\n"
      "v_mfma_f32_16x16x32_bf16 v[132:135], a[168:171], v[112:115], v[132:135]\n"
      "global_load_lds_dwordx4 v236, s[52:53] offset:256 nt\n"
      "v_mfma_f32_16x16x32_bf16 v[132:135], a[172:175], v[116:119], v[132:135]\n"
      "v_mfma_f32_16x16x32_bf16 v[136:139], a[176:179], v[36:39], v[12:15]\n"
      "v_mfma_f32_16x16x32_bf16 v[136:139], a[180:183], v[40:43], v[136:139]\n"
      "v_mfma_f32_16x16x32_bf16 v[136:139], a[184:187], v[44:47], v[136:139]\n"
      "v_mfma_f32_16x16x32_bf16 v[136:139], a[188:191], v[60:63], v[136:139]\n"
      "v_cvt_pk_bf16_f32 v212, v132, v133\n"
      "v_mfma_f32_16x16x32_bf16 v[136:139], a[192:195], v[64:67], v[136:139]\n"
      "v_cvt_pk_bf16_f32 v213, v134, v135\n"
      "v_mfma_f32_16x16x32_bf16 v[136:139], a[196:199], v[68:71], v[136:139]\n"
      "v_mfma_f32_16x16x32_bf16 v[136:139], a[200:203], v[84:87], v[136:139]\n"
      "v_mfma_f32_16x16x32_bf16 v[136:139], a[204:207], v[88:91], v[136:139]\n"
      "s_waitcnt lgkmcnt(2)\n"
      "v_mfma_f32_16x16x32_bf16 v[136:139], a[208:211], v[92:95], v[136:139]\n"
      "global_store_dwordx4 v253, v[224:227], s[54:55] offset:0 sc1\n"
      "v_mfma_f32_16x16x32_bf16 v[136:139], a[212:215], v[108:111], v[136:139]\n"
      "s_add_u32 m0, s17, 0x3e00\n"
      "v_mfma_f32_16x16x32_bf16 v[136:139], a[216:219], v[112:115], v[136:139]\n"
      "global_load_lds_dwordx4 v236, s[52:53] offset:512 nt\n"
      "v_mfma_f32_16x16x32_bf16 v[136:139], a[220:223], v[116:119], v[136:139]\n"
      "v_mfma_f32_16x16x32_bf16 v[132:135], a[224:227], v[36:39], v[16:19]\n"
      "v_mfma_f32_16x16x32_bf16 v[132:135], a[228:231], v[40:43], v[132:135]\n"
      "v_mfma_f32_16x16x32_bf16 v[132:135], a[232:235], v[44:47], v[132:135]\n"
      "v_mfma_f32_16x16x32_bf16 v[132:135], a[236:239], v[60:63], v[132:135]\n"
      "v_cvt_pk_bf16_f32 v214, v136, v137\n"
      "v_mfma_f32_16x16x32_bf16 v[132:135], a[0:3], v[64:67], v[132:135]\n"
      "v_cvt_pk_bf16_f32 v215, v138, v139\n"
      "v_mfma_f32_16x16x32_bf16 v[132:135], a[4:7], v[68:71], v[132:135]\n"
      "v_mfma_f32_16x16x32_bf16 v[132:135], a[8:11], v[84:87], v[132:135]\n"
      "v_mfma_f32_16x16x32_bf16 v[132:135], a[12:15], v[88:91], v[132:135]\n"
      "v_mfma_f32_16x16x32_bf16 v[132:135], a[16:19], v[92:95], v[132:135]\n"
      "v_mfma_f32_16x16x32_bf16 v[132:135], a[20:23], v[108:111], v[132:135]\n"
      "ds_write_b128 v245, v[212:215] offset:0\n"
      "v_mfma_f32_16x16x32_bf16 v[132:135], v[140:143], v[112:115], v[132:135]\n"
      "v_mfma_f32_16x16x32_bf16 v[132:135], v[156:159], v[116:119], v[132:135]\n"
      "s_waitcnt lgkmcnt(2)\n"
      "s_barrier\n"
      "v_mfma_f32_16x16x32_bf16 v[136:139], v[160:163], v[36:39], v[20:23]\n"
      "ds_read_b128 v[36:39], v232 offset:24832\n"
      "v_mfma_f32_16x16x32_bf16 v[136:139], v[164:167], v[40:43], v[136:139]\n"
      "ds_read_b128 v[40:43], v233 offset:24832\n"
      "v_mfma_f32_16x16x32_bf16 v[136:139], v[180:183], v[44:47], v[136:139]\n"
      "ds_read_b128 v[44:47], v234 offset:24832\n"
      "v_mfma_f32_16x16x32_bf16 v[136:139], v[184:187], v[60:63], v[136:139]\n"
      "ds_read_b128 v[60:63], v235 offset:24832\n"
      "v_cvt_pk_bf16_f32 v208, v132, v133\n"
      "v_mfma_f32_16x16x32_bf16 v[136:139], v[188:191], v[64:67], v[136:139]\n"
      "ds_read_b128 v[64:67], v232 offset:33024\n"
      "v_cvt_pk_bf16_f32 v209, v134, v135\n"
      "v_mfma_f32_16x16x32_bf16 v[136:139], v[204:207], v[68:71], v[136:139]\n"
      "ds_read_b128 v[68:71], v233 offset:33024\n"
      "v_mfma_f32_16x16x32_bf16 v[136:139], a[24:27], v[84:87], v[136:139]\n"
      "ds_read_b128 v[84:87], v234 offset:33024\n"
      "v_mfma_f32_16x16x32_bf16 v[136:139], a[28:31], v[88:91], v[136:139]\n"
      "ds_read_b128 v[88:91], v235 offset:33024\n"
      "v_mfma_f32_16x16x32_bf16 v[136:139], a[32:35], v[92:95], v[136:139]\n"
      "ds_read_b128 v[92:95], v232 offset:41216\n"
      "v_mfma_f32_16x16x32_bf16 v[136:139], a[36:39], v[108:111], v[136:139]\n"
      "ds_read_b128 v[108:111], v233 offset:41216\n"
      "v_mfma_f32_16x16x32_bf16 v[136:139], a[40:43], v[112:115], v[136:139]\n"
      "ds_read_b128 v[112:115], v234 offset:41216\n"
      "v_mfma_f32_16x16x32_bf16 v[136:139], a[44:47], v[116:119], v[136:139]\n"
      "ds_read_b128 v[116:119], v235 offset:41216\n"
      "s_waitcnt lgkmcnt(8)\n"
      "v_mfma_f32_16x16x32_bf16 v[132:135], a[240:243], v[36:39], v[0:3]\n"
      "v_add_u32 v232, 0xc000, v232\n"
      "v_mfma_f32_16x16x32_bf16 v[132:135], a[244:247], v[40:43], v[132:135]\n"
      "v_add_u32 v233, 0xc000, v233\n"
      "v_mfma_f32_16x16x32_bf16 v[132:135], a[248:251], v[44:47], v[132:135]\n"
      "v_add_u32 v234, 0xc000, v234\n"
      "v_mfma_f32_16x16x32_bf16 v[132:135], a[252:255], v[60:63], v[132:135]\n"
      "v_add_u32 v235, 0xc000, v235\n"
      "s_waitcnt lgkmcnt(4)\n"
      "v_mfma_f32_16x16x32_bf16 v[132:135], a[48:51], v[64:67], v[132:135]\n"
      "v_cvt_pk_bf16_f32 v210, v136, v137\n"
      "v_mfma_f32_16x16x32_bf16 v[132:135], a[52:55], v[68:71], v[132:135]\n"
      "v_cvt_pk_bf16_f32 v211, v138, v139\n"
      "v_mfma_f32_16x16x32_bf16 v[132:135], a[56:59], v[84:87], v[132:135]\n"
      "ds_read_b128 v[216:219], v246 offset:24576\n"
      "v_mfma_f32_16x16x32_bf16 v[132:135], a[60:63], v[88:91], v[132:135]\n"
      "LPD8:\n"
      "s_cbranch_execz LPD11\n"
      "ds_read_b128 v[220:223], v246 offset:25600\n"
      "s_waitcnt lgkmcnt(2)\n"
      "v_mfma_f32_16x16x32_bf16 v[132:135], a[64:67], v[92:95], v[132:135]\n"
      "ds_read_b128 v[224:227], v246 offset:26624\n"
      "v_mfma_f32_16x16x32_bf16 v[132:135], a[68:71], v[108:111], v[132:135]\n"
      "v_lshlrev_b32 v0, 16, v24\n"
      "v_mfma_f32_16x16x32_bf16 v[132:135], a[72:75], v[112:115], v[132:135]\n"
      "ds_write_b128 v244, v[208:211] offset:128\n"
      "v_mfma_f32_16x16x32_bf16 v[132:135], a[76:79], v[116:119], v[132:135]\n"
      "v_and_b32 v1, 0xffff0000, v24\n"
      "v_mfma_f32_16x16x32_bf16 v[136:139], a[80:83], v[36:39], v[4:7]\n"
      "v_lshlrev_b32 v2, 16, v25\n"
      "v_mfma_f32_16x16x32_bf16 v[136:139], a[84:87], v[40:43], v[136:139]\n"
      "v_and_b32 v3, 0xffff0000, v25\n"
      "v_mfma_f32_16x16x32_bf16 v[136:139], a[88:91], v[44:47], v[136:139]\n"
      "v_mfma_f32_16x16x32_bf16 v[136:139], a[92:95], v[60:63], v[136:139]\n"
      "v_cvt_pk_bf16_f32 v208, v132, v133\n"
      "v_mfma_f32_16x16x32_bf16 v[136:139], a[96:99], v[64:67], v[136:139]\n"
      "v_cvt_pk_bf16_f32 v209, v134, v135\n"
      "v_mfma_f32_16x16x32_bf16 v[136:139], a[100:103], v[68:71], v[136:139]\n"
      "s_waitcnt lgkmcnt(3)\n"
      "v_mfma_f32_16x16x32_bf16 v[136:139], a[104:107], v[84:87], v[136:139]\n"
      "global_store_dwordx4 v247, v[216:219], s[54:55] offset:768 sc1\n"
      "v_mfma_f32_16x16x32_bf16 v[136:139], a[108:111], v[88:91], v[136:139]\n"
      "s_add_u32 m0, s17, 0x1000\n"
      "v_mfma_f32_16x16x32_bf16 v[136:139], a[112:115], v[92:95], v[136:139]\n"
      "global_load_lds_dwordx4 v237, s[52:53] offset:0 nt\n"
      "v_mfma_f32_16x16x32_bf16 v[136:139], a[116:119], v[108:111], v[136:139]\n"
      "v_lshlrev_b32 v4, 16, v26\n"
      "v_mfma_f32_16x16x32_bf16 v[136:139], a[120:123], v[112:115], v[136:139]\n"
      "v_and_b32 v5, 0xffff0000, v26\n"
      "v_mfma_f32_16x16x32_bf16 v[136:139], a[124:127], v[116:119], v[136:139]\n"
      "v_lshlrev_b32 v6, 16, v27\n"
      "v_mfma_f32_16x16x32_bf16 v[132:135], a[128:131], v[36:39], v[8:11]\n"
      "v_and_b32 v7, 0xffff0000, v27\n"
      "v_mfma_f32_16x16x32_bf16 v[132:135], a[132:135], v[40:43], v[132:135]\n"
      "v_mfma_f32_16x16x32_bf16 v[132:135], a[136:139], v[44:47], v[132:135]\n"
      "v_mfma_f32_16x16x32_bf16 v[132:135], a[140:143], v[60:63], v[132:135]\n"
      "v_cvt_pk_bf16_f32 v210, v136, v137\n"
      "v_mfma_f32_16x16x32_bf16 v[132:135], a[144:147], v[64:67], v[132:135]\n"
      "v_cvt_pk_bf16_f32 v211, v138, v139\n"
      "v_mfma_f32_16x16x32_bf16 v[132:135], a[148:151], v[68:71], v[132:135]\n"
      "v_lshlrev_b32 v8, 16, v28\n"
      "v_mfma_f32_16x16x32_bf16 v[132:135], a[152:155], v[84:87], v[132:135]\n"
      "s_waitcnt lgkmcnt(2)\n"
      "v_mfma_f32_16x16x32_bf16 v[132:135], a[156:159], v[88:91], v[132:135]\n"
      "global_store_dwordx4 v252, v[220:223], s[54:55] offset:768 sc1\n"
      "v_mfma_f32_16x16x32_bf16 v[132:135], a[160:163], v[92:95], v[132:135]\n"
      "s_add_u32 m0, s17, 0x2f00\n"
      "v_mfma_f32_16x16x32_bf16 v[132:135], a[164:167], v[108:111], v[132:135]\n"
      "ds_write_b128 v244, v[208:211] offset:12288\n"
      "v_mfma_f32_16x16x32_bf16 v[132:135], a[168:171], v[112:115], v[132:135]\n"
      "global_load_lds_dwordx4 v237, s[52:53] offset:256 nt\n"
      "v_mfma_f32_16x16x32_bf16 v[132:135], a[172:175], v[116:119], v[132:135]\n"
      "v_and_b32 v9, 0xffff0000, v28\n"
      "v_mfma_f32_16x16x32_bf16 v[136:139], a[176:179], v[36:39], v[12:15]\n"
      "v_lshlrev_b32 v10, 16, v29\n"
      "v_mfma_f32_16x16x32_bf16 v[136:139], a[180:183], v[40:43], v[136:139]\n"
      "v_and_b32 v11, 0xffff0000, v29\n"
      "v_mfma_f32_16x16x32_bf16 v[136:139], a[184:187], v[44:47], v[136:139]\n"
      "v_mfma_f32_16x16x32_bf16 v[136:139], a[188:191], v[60:63], v[136:139]\n"
      "v_cvt_pk_bf16_f32 v212, v132, v133\n"
      "v_mfma_f32_16x16x32_bf16 v[136:139], a[192:195], v[64:67], v[136:139]\n"
      "v_cvt_pk_bf16_f32 v213, v134, v135\n"
      "v_mfma_f32_16x16x32_bf16 v[136:139], a[196:199], v[68:71], v[136:139]\n"
      "v_lshlrev_b32 v12, 16, v30\n"
      "v_mfma_f32_16x16x32_bf16 v[136:139], a[200:203], v[84:87], v[136:139]\n"
      "v_and_b32 v13, 0xffff0000, v30\n"
      "v_mfma_f32_16x16x32_bf16 v[136:139], a[204:207], v[88:91], v[136:139]\n"
      "s_waitcnt lgkmcnt(2)\n"
      "v_mfma_f32_16x16x32_bf16 v[136:139], a[208:211], v[92:95], v[136:139]\n"
      "global_store_dwordx4 v253, v[224:227], s[54:55] offset:768 sc1\n"
      "v_mfma_f32_16x16x32_bf16 v[136:139], a[212:215], v[108:111], v[136:139]\n"
      "s_add_u32 m0, s17, 0x4e00\n"
      "v_mfma_f32_16x16x32_bf16 v[136:139], a[216:219], v[112:115], v[136:139]\n"
      "global_load_lds_dwordx4 v237, s[52:53] offset:512 nt\n"
      "v_mfma_f32_16x16x32_bf16 v[136:139], a[220:223], v[116:119], v[136:139]\n"
      "v_lshlrev_b32 v14, 16, v31\n"
      "v_mfma_f32_16x16x32_bf16 v[132:135], a[224:227], v[36:39], v[16:19]\n"
      "v_and_b32 v15, 0xffff0000, v31\n"
      "v_mfma_f32_16x16x32_bf16 v[132:135], a[228:231], v[40:43], v[132:135]\n"
      "v_mfma_f32_16x16x32_bf16 v[132:135], a[232:235], v[44:47], v[132:135]\n"
      "v_mfma_f32_16x16x32_bf16 v[132:135], a[236:239], v[60:63], v[132:135]\n"
      "s_memrealtime s[84:85]\n"
      "v_mfma_f32_16x16x32_bf16 v[132:135], a[0:3], v[64:67], v[132:135]\n"
      "v_cvt_pk_bf16_f32 v214, v136, v137\n"
      "v_mfma_f32_16x16x32_bf16 v[132:135], a[4:7], v[68:71], v[132:135]\n"
      "v_cvt_pk_bf16_f32 v215, v138, v139\n"
      "v_mfma_f32_16x16x32_bf16 v[132:135], a[8:11], v[84:87], v[132:135]\n"
      "v_lshlrev_b32 v16, 16, v32\n"
      "v_mfma_f32_16x16x32_bf16 v[132:135], a[12:15], v[88:91], v[132:135]\n"
      "v_and_b32 v17, 0xffff0000, v32\n"
      "v_mfma_f32_16x16x32_bf16 v[132:135], a[16:19], v[92:95], v[132:135]\n"
      "v_lshlrev_b32 v18, 16, v33\n"
      "v_mfma_f32_16x16x32_bf16 v[132:135], a[20:23], v[108:111], v[132:135]\n"
      "v_and_b32 v19, 0xffff0000, v33\n"
      "v_mfma_f32_16x16x32_bf16 v[132:135], v[140:143], v[112:115], v[132:135]\n"
      "s_waitcnt lgkmcnt(0)\n"
      "s_mov_b32 s48, s84\n"
      "v_mfma_f32_16x16x32_bf16 v[132:135], v[156:159], v[116:119], v[132:135]\n"
      "s_waitcnt vmcnt(18)\n"
      "s_barrier\n"
      "ds_write_b128 v245, v[212:215] offset:12288\n"
      "v_mfma_f32_16x16x32_bf16 v[136:139], v[160:163], v[36:39], v[20:23]\n"
      "ds_read_b128 v[36:39], v232 offset:0\n"
      "v_mfma_f32_16x16x32_bf16 v[136:139], v[164:167], v[40:43], v[136:139]\n"
      "ds_read_b128 v[40:43], v233 offset:0\n"
      "v_mfma_f32_16x16x32_bf16 v[136:139], v[180:183], v[44:47], v[136:139]\n"
      "ds_read_b128 v[44:47], v234 offset:0\n"
      "v_mfma_f32_16x16x32_bf16 v[136:139], v[184:187], v[60:63], v[136:139]\n"
      "ds_read_b128 v[60:63], v235 offset:0\n"
      "v_cvt_pk_bf16_f32 v208, v132, v133\n"
      "v_mfma_f32_16x16x32_bf16 v[136:139], v[188:191], v[64:67], v[136:139]\n"
      "ds_read_b128 v[64:67], v232 offset:8192\n"
      "v_cvt_pk_bf16_f32 v209, v134, v135\n"
      "v_mfma_f32_16x16x32_bf16 v[136:139], v[204:207], v[68:71], v[136:139]\n"
      "ds_read_b128 v[68:71], v233 offset:8192\n"
      "v_mfma_f32_16x16x32_bf16 v[136:139], a[24:27], v[84:87], v[136:139]\n"
      "ds_read_b128 v[84:87], v234 offset:8192\n"
      "v_mfma_f32_16x16x32_bf16 v[136:139], a[28:31], v[88:91], v[136:139]\n"
      "ds_read_b128 v[88:91], v235 offset:8192\n"
      "v_mfma_f32_16x16x32_bf16 v[136:139], a[32:35], v[92:95], v[136:139]\n"
      "ds_read_b128 v[92:95], v232 offset:16384\n"
      "v_mfma_f32_16x16x32_bf16 v[136:139], a[36:39], v[108:111], v[136:139]\n"
      "ds_read_b128 v[108:111], v233 offset:16384\n"
      "v_mfma_f32_16x16x32_bf16 v[136:139], a[40:43], v[112:115], v[136:139]\n"
      "ds_read_b128 v[112:115], v234 offset:16384\n"
      "v_mfma_f32_16x16x32_bf16 v[136:139], a[44:47], v[116:119], v[136:139]\n"
      "ds_read_b128 v[116:119], v235 offset:16384\n"
      "s_waitcnt lgkmcnt(8)\n"
      "v_mfma_f32_16x16x32_bf16 v[132:135], a[240:243], v[36:39], v[0:3]\n"
      "v_lshlrev_b32 v20, 16, v34\n"
      "v_mfma_f32_16x16x32_bf16 v[132:135], a[244:247], v[40:43], v[132:135]\n"
      "v_and_b32 v21, 0xffff0000, v34\n"
      "v_mfma_f32_16x16x32_bf16 v[132:135], a[248:251], v[44:47], v[132:135]\n"
      "v_lshlrev_b32 v22, 16, v35\n"
      "v_mfma_f32_16x16x32_bf16 v[132:135], a[252:255], v[60:63], v[132:135]\n"
      "v_and_b32 v23, 0xffff0000, v35\n"
      "s_waitcnt lgkmcnt(4)\n"
      "v_mfma_f32_16x16x32_bf16 v[132:135], a[48:51], v[64:67], v[132:135]\n"
      "v_cvt_pk_bf16_f32 v210, v136, v137\n"
      "v_mfma_f32_16x16x32_bf16 v[132:135], a[52:55], v[68:71], v[132:135]\n"
      "v_cvt_pk_bf16_f32 v211, v138, v139\n"
      "v_mfma_f32_16x16x32_bf16 v[132:135], a[56:59], v[84:87], v[132:135]\n"
      "s_add_u32 s54, s44, s32\n"
      "s_addc_u32 s55, s45, 0\n"
      "v_mfma_f32_16x16x32_bf16 v[132:135], a[60:63], v[88:91], v[132:135]\n"
      "ds_read_b128 v[216:219], v246 offset:0\n"
      "s_waitcnt lgkmcnt(1)\n"
      "v_mfma_f32_16x16x32_bf16 v[132:135], a[64:67], v[92:95], v[132:135]\n"
      "ds_read_b128 v[220:223], v246 offset:1024\n"
      "v_mfma_f32_16x16x32_bf16 v[132:135], a[68:71], v[108:111], v[132:135]\n"
      "ds_read_b128 v[224:227], v246 offset:2048\n"
      "v_mfma_f32_16x16x32_bf16 v[132:135], a[72:75], v[112:115], v[132:135]\n"
      "ds_write_b128 v244, v[208:211] offset:12416\n"
      "v_mfma_f32_16x16x32_bf16 v[132:135], a[76:79], v[116:119], v[132:135]\n"
      "v_mfma_f32_16x16x32_bf16 v[136:139], a[80:83], v[36:39], v[4:7]\n"
      "v_mfma_f32_16x16x32_bf16 v[136:139], a[84:87], v[40:43], v[136:139]\n"
      "v_mfma_f32_16x16x32_bf16 v[136:139], a[88:91], v[44:47], v[136:139]\n"
      "v_mfma_f32_16x16x32_bf16 v[136:139], a[92:95], v[60:63], v[136:139]\n"
      "v_cvt_pk_bf16_f32 v208, v132, v133\n"
      "v_mfma_f32_16x16x32_bf16 v[136:139], a[96:99], v[64:67], v[136:139]\n"
      "v_cvt_pk_bf16_f32 v209, v134, v135\n"
      "v_mfma_f32_16x16x32_bf16 v[136:139], a[100:103], v[68:71], v[136:139]\n"
      "s_waitcnt lgkmcnt(3)\n"
      "v_mfma_f32_16x16x32_bf16 v[136:139], a[104:107], v[84:87], v[136:139]\n"
      "global_store_dwordx4 v247, v[216:219], s[54:55] offset:0 sc1\n"
      "v_mfma_f32_16x16x32_bf16 v[136:139], a[108:111], v[88:91], v[136:139]\n"
      "s_add_u32 s52, s42, s34\n"
      "s_addc_u32 s53, s43, 0\n"
      "v_mfma_f32_16x16x32_bf16 v[136:139], a[112:115], v[92:95], v[136:139]\n"
      "s_add_u32 m0, s17, 0x6000\n"
      "v_mfma_f32_16x16x32_bf16 v[136:139], a[116:119], v[108:111], v[136:139]\n"
      "global_load_lds_dwordx4 v236, s[52:53] offset:0 nt\n"
      "v_mfma_f32_16x16x32_bf16 v[136:139], a[120:123], v[112:115], v[136:139]\n"
      "v_mfma_f32_16x16x32_bf16 v[136:139], a[124:127], v[116:119], v[136:139]\n"
      "v_mfma_f32_16x16x32_bf16 v[132:135], a[128:131], v[36:39], v[8:11]\n"
      "v_mfma_f32_16x16x32_bf16 v[132:135], a[132:135], v[40:43], v[132:135]\n"
      "v_mfma_f32_16x16x32_bf16 v[132:135], a[136:139], v[44:47], v[132:135]\n"
      "v_mfma_f32_16x16x32_bf16 v[132:135], a[140:143], v[60:63], v[132:135]\n"
      "v_cvt_pk_bf16_f32 v210, v136, v137\n"
      "v_mfma_f32_16x16x32_bf16 v[132:135], a[144:147], v[64:67], v[132:135]\n"
      "v_cvt_pk_bf16_f32 v211, v138, v139\n"
      "v_mfma_f32_16x16x32_bf16 v[132:135], a[148:151], v[68:71], v[132:135]\n"
      "v_mfma_f32_16x16x32_bf16 v[132:135], a[152:155], v[84:87], v[132:135]\n"
      "s_waitcnt lgkmcnt(2)\n"
      "v_mfma_f32_16x16x32_bf16 v[132:135], a[156:159], v[88:91], v[132:135]\n"
      "global_store_dwordx4 v252, v[220:223], s[54:55] offset:0 sc1\n"
      "v_mfma_f32_16x16x32_bf16 v[132:135], a[160:163], v[92:95], v[132:135]\n"
      "s_add_u32 m0, s17, 0x7f00\n"
      "v_mfma_f32_16x16x32_bf16 v[132:135], a[164:167], v[108:111], v[132:135]\n"
      "ds_write_b128 v244, v[208:211] offset:24576\n"
      "v_mfma_f32_16x16x32_bf16 v[132:135], a[168:171], v[112:115], v[132:135]\n"
      "global_load_lds_dwordx4 v236, s[52:53] offset:256 nt\n"
      "v_mfma_f32_16x16x32_bf16 v[132:135], a[172:175], v[116:119], v[132:135]\n"
      "v_mfma_f32_16x16x32_bf16 v[136:139], a[176:179], v[36:39], v[12:15]\n"
      "v_mfma_f32_16x16x32_bf16 v[136:139], a[180:183], v[40:43], v[136:139]\n"
      "v_mfma_f32_16x16x32_bf16 v[136:139], a[184:187], v[44:47], v[136:139]\n"
      "v_mfma_f32_16x16x32_bf16 v[136:139], a[188:191], v[60:63], v[136:139]\n"
      "v_cvt_pk_bf16_f32 v212, v132, v133\n"
      "v_mfma_f32_16x16x32_bf16 v[136:139], a[192:195], v[64:67], v[136:139]\n"
      "v_cvt_pk_bf16_f32 v213, v134, v135\n"
      "v_mfma_f32_16x16x32_bf16 v[136:139], a[196:199], v[68:71], v[136:139]\n"
      "v_mfma_f32_16x16x32_bf16 v[136:139], a[200:203], v[84:87], v[136:139]\n"
      "v_mfma_f32_16x16x32_bf16 v[136:139], a[204:207], v[88:91], v[136:139]\n"
      "s_waitcnt lgkmcnt(2)\n"
      "v_mfma_f32_16x16x32_bf16 v[136:139], a[208:211], v[92:95], v[136:139]\n"
      "global_store_dwordx4 v253, v[224:227], s[54:55] offset:0 sc1\n"
      "v_mfma_f32_16x16x32_bf16 v[136:139], a[212:215], v[108:111], v[136:139]\n"
      "s_add_u32 m0, s17, 0x9e00\n"
      "v_mfma_f32_16x16x32_bf16 v[136:139], a[216:219], v[112:115], v[136:139]\n"
      "global_load_lds_dwordx4 v236, s[52:53] offset:512 nt\n"
      "v_mfma_f32_16x16x32_bf16 v[136:139], a[220:223], v[116:119], v[136:139]\n"
      "v_mfma_f32_16x16x32_bf16 v[132:135], a[224:227], v[36:39], v[16:19]\n"
      "v_mfma_f32_16x16x32_bf16 v[132:135], a[228:231], v[40:43], v[132:135]\n"
      "v_mfma_f32_16x16x32_bf16 v[132:135], a[232:235], v[44:47], v[132:135]\n"
      "v_mfma_f32_16x16x32_bf16 v[132:135], a[236:239], v[60:63], v[132:135]\n"
      "v_cvt_pk_bf16_f32 v214, v136, v137\n"
      "v_mfma_f32_16x16x32_bf16 v[132:135], a[0:3], v[64:67], v[132:135]\n"
      "v_cvt_pk_bf16_f32 v215, v138, v139\n"
      "v_mfma_f32_16x16x32_bf16 v[132:135], a[4:7], v[68:71], v[132:135]\n"
      "v_mfma_f32_16x16x32_bf16 v[132:135], a[8:11], v[84:87], v[132:135]\n"
      "v_mfma_f32_16x16x32_bf16 v[132:135], a[12:15], v[88:91], v[132:135]\n"
      "v_mfma_f32_16x16x32_bf16 v[132:135], a[16:19], v[92:95], v[132:135]\n"
      "v_mfma_f32_16x16x32_bf16 v[132:135], a[20:23], v[108:111], v[132:135]\n"
      "ds_write_b128 v245, v[212:215] offset:24576\n"
      "v_mfma_f32_16x16x32_bf16 v[132:135], v[140:143], v[112:115], v[132:135]\n"
      "v_mfma_f32_16x16x32_bf16 v[132:135], v[156:159], v[116:119], v[132:135]\n"
      "s_waitcnt lgkmcnt(2)\n"
      "s_barrier\n"
      "v_mfma_f32_16x16x32_bf16 v[136:139], v[160:163], v[36:39], v[20:23]\n"
      "ds_read_b128 v[36:39], v232 offset:256\n"
      "v_mfma_f32_16x16x32_bf16 v[136:139], v[164:167], v[40:43], v[136:139]\n"
      "ds_read_b128 v[40:43], v233 offset:256\n"
      "v_mfma_f32_16x16x32_bf16 v[136:139], v[180:183], v[44:47], v[136:139]\n"
      "ds_read_b128 v[44:47], v234 offset:256\n"
      "v_mfma_f32_16x16x32_bf16 v[136:139], v[184:187], v[60:63], v[136:139]\n"
      "ds_read_b128 v[60:63], v235 offset:256\n"
      "v_cvt_pk_bf16_f32 v208, v132, v133\n"
      "v_mfma_f32_16x16x32_bf16 v[136:139], v[188:191], v[64:67], v[136:139]\n"
      "ds_read_b128 v[64:67], v232 offset:8448\n"
      "v_cvt_pk_bf16_f32 v209, v134, v135\n"
      "v_mfma_f32_16x16x32_bf16 v[136:139], v[204:207], v[68:71], v[136:139]\n"
      "ds_read_b128 v[68:71], v233 offset:8448\n"
      "v_mfma_f32_16x16x32_bf16 v[136:139], a[24:27], v[84:87], v[136:139]\n"
      "ds_read_b128 v[84:87], v234 offset:8448\n"
      "v_mfma_f32_16x16x32_bf16 v[136:139], a[28:31], v[88:91], v[136:139]\n"
      "ds_read_b128 v[88:91], v235 offset:8448\n"
      "v_mfma_f32_16x16x32_bf16 v[136:139], a[32:35], v[92:95], v[136:139]\n"
      "ds_read_b128 v[92:95], v232 offset:16640\n"
      "v_mfma_f32_16x16x32_bf16 v[136:139], a[36:39], v[108:111], v[136:139]\n"
      "ds_read_b128 v[108:111], v233 offset:16640\n"
      "v_mfma_f32_16x16x32_bf16 v[136:139], a[40:43], v[112:115], v[136:139]\n"
      "ds_read_b128 v[112:115], v234 offset:16640\n"
      "v_mfma_f32_16x16x32_bf16 v[136:139], a[44:47], v[116:119], v[136:139]\n"
      "ds_read_b128 v[116:119], v235 offset:16640\n"
      "s_waitcnt lgkmcnt(8)\n"
      "v_mfma_f32_16x16x32_bf16 v[132:135], a[240:243], v[36:39], v[0:3]\n"
      "v_mfma_f32_16x16x32_bf16 v[132:135], a[244:247], v[40:43], v[132:135]\n"
      "v_mfma_f32_16x16x32_bf16 v[132:135], a[248:251], v[44:47], v[132:135]\n"
      "v_mfma_f32_16x16x32_bf16 v[132:135], a[252:255], v[60:63], v[132:135]\n"
      "v_cvt_pk_bf16_f32 v210, v136, v137\n"
      "s_waitcnt lgkmcnt(4)\n"
      "v_mfma_f32_16x16x32_bf16 v[132:135], a[48:51], v[64:67], v[132:135]\n"
      "v_cvt_pk_bf16_f32 v211, v138, v139\n"
      "v_mfma_f32_16x16x32_bf16 v[132:135], a[52:55], v[68:71], v[132:135]\n"
      "v_mfma_f32_16x16x32_bf16 v[132:135], a[56:59], v[84:87], v[132:135]\n"
      "ds_read_b128 v[216:219], v246 offset:12288\n"
      "v_mfma_f32_16x16x32_bf16 v[132:135], a[60:63], v[88:91], v[132:135]\n"
      "ds_read_b128 v[220:223], v246 offset:13312\n"
      "s_waitcnt lgkmcnt(2)\n"
      "v_mfma_f32_16x16x32_bf16 v[132:135], a[64:67], v[92:95], v[132:135]\n"
      "ds_read_b128 v[224:227], v246 offset:14336\n"
      "v_mfma_f32_16x16x32_bf16 v[132:135], a[68:71], v[108:111], v[132:135]\n"
      "ds_write_b128 v244, v[208:211] offset:24704\n"
      "v_mfma_f32_16x16x32_bf16 v[132:135], a[72:75], v[112:115], v[132:135]\n"
      "v_mfma_f32_16x16x32_bf16 v[132:135], a[76:79], v[116:119], v[132:135]\n"
      "v_mfma_f32_16x16x32_bf16 v[136:139], a[80:83], v[36:39], v[4:7]\n"
      "v_mfma_f32_16x16x32_bf16 v[136:139], a[84:87], v[40:43], v[136:139]\n"
      "v_mfma_f32_16x16x32_bf16 v[136:139], a[88:91], v[44:47], v[136:139]\n"
      "v_mfma_f32_16x16x32_bf16 v[136:139], a[92:95], v[60:63], v[136:139]\n"
      "v_cvt_pk_bf16_f32 v208, v132, v133\n"
      "v_mfma_f32_16x16x32_bf16 v[136:139], a[96:99], v[64:67], v[136:139]\n"
      "v_cvt_pk_bf16_f32 v209, v134, v135\n"
      "v_mfma_f32_16x16x32_bf16 v[136:139], a[100:103], v[68:71], v[136:139]\n"
      "s_waitcnt lgkmcnt(3)\n"
      "v_mfma_f32_16x16x32_bf16 v[136:139], a[104:107], v[84:87], v[136:139]\n"
      "global_store_dwordx4 v247, v[216:219], s[54:55] offset:768 sc1\n"
      "v_mfma_f32_16x16x32_bf16 v[136:139], a[108:111], v[88:91], v[136:139]\n"
      "s_add_u32 m0, s17, 0x7000\n"
      "v_mfma_f32_16x16x32_bf16 v[136:139], a[112:115], v[92:95], v[136:139]\n"
      "global_load_lds_dwordx4 v237, s[52:53] offset:0 nt\n"
      "v_mfma_f32_16x16x32_bf16 v[136:139], a[116:119], v[108:111], v[136:139]\n"
      "v_mfma_f32_16x16x32_bf16 v[136:139], a[120:123], v[112:115], v[136:139]\n"
      "v_mfma_f32_16x16x32_bf16 v[136:139], a[124:127], v[116:119], v[136:139]\n"
      "v_mfma_f32_16x16x32_bf16 v[132:135], a[128:131], v[36:39], v[8:11]\n"
      "v_mfma_f32_16x16x32_bf16 v[132:135], a[132:135], v[40:43], v[132:135]\n"
      "v_mfma_f32_16x16x32_bf16 v[132:135], a[136:139], v[44:47], v[132:135]\n"
      "v_mfma_f32_16x16x32_bf16 v[132:135], a[140:143], v[60:63], v[132:135]\n"
      "v_cvt_pk_bf16_f32 v210, v136, v137\n"
      "v_mfma_f32_16x16x32_bf16 v[132:135], a[144:147], v[64:67], v[132:135]\n"
      "v_cvt_pk_bf16_f32 v211, v138, v139\n"
      "v_mfma_f32_16x16x32_bf16 v[132:135], a[148:151], v[68:71], v[132:135]\n"
      "v_mfma_f32_16x16x32_bf16 v[132:135], a[152:155], v[84:87], v[132:135]\n"
      "s_waitcnt lgkmcnt(2)\n"
      "v_mfma_f32_16x16x32_bf16 v[132:135], a[156:159], v[88:91], v[132:135]\n"
      "global_store_dwordx4 v252, v[220:223], s[54:55] offset:768 sc1\n"
      "v_mfma_f32_16x16x32_bf16 v[132:135], a[160:163], v[92:95], v[132:135]\n"
      "s_add_u32 m0, s17, 0x8f00\n"
      "v_mfma_f32_16x16x32_bf16 v[132:135], a[164:167], v[108:111], v[132:135]\n"
      "ds_write_b128 v244, v[208:211] offset:0\n"
      "v_mfma_f32_16x16x32_bf16 v[132:135], a[168:171], v[112:115], v[132:135]\n"
      "global_load_lds_dwordx4 v237, s[52:53] offset:256 nt\n"
      "v_mfma_f32_16x16x32_bf16 v[132:135], a[172:175], v[116:119], v[132:135]\n"
      "v_mfma_f32_16x16x32_bf16 v[136:139], a[176:179], v[36:39], v[12:15]\n"
      "v_mfma_f32_16x16x32_bf16 v[136:139], a[180:183], v[40:43], v[136:139]\n"
      "v_mfma_f32_16x16x32_bf16 v[136:139], a[184:187], v[44:47], v[136:139]\n"
      "v_mfma_f32_16x16x32_bf16 v[136:139], a[188:191], v[60:63], v[136:139]\n"
      "v_cvt_pk_bf16_f32 v212, v132, v133\n"
      "v_mfma_f32_16x16x32_bf16 v[136:139], a[192:195], v[64:67], v[136:139]\n"
      "v_cvt_pk_bf16_f32 v213, v134, v135\n"
      "v_mfma_f32_16x16x32_bf16 v[136:139], a[196:199], v[68:71], v[136:139]\n"
      "v_mfma_f32_16x16x32_bf16 v[136:139], a[200:203], v[84:87], v[136:139]\n"
      "v_mfma_f32_16x16x32_bf16 v[136:139], a[204:207], v[88:91], v[136:139]\n"
      "s_waitcnt lgkmcnt(2)\n"
      "v_mfma_f32_16x16x32_bf16 v[136:139], a[208:211], v[92:95], v[136:139]\n"
      "global_store_dwordx4 v253, v[224:227], s[54:55] offset:768 sc1\n"
      "v_mfma_f32_16x16x32_bf16 v[136:139], a[212:215], v[108:111], v[136:139]\n"
      "s_add_u32 m0, s17, 0xae00\n"
      "v_mfma_f32_16x16x32_bf16 v[136:139], a[216:219], v[112:115], v[136:139]\n"
      "global_load_lds_dwordx4 v237, s[52:53] offset:512 nt\n"
      "v_mfma_f32_16x16x32_bf16 v[136:139], a[220:223], v[116:119], v[136:139]\n"
      "v_mfma_f32_16x16x32_bf16 v[132:135], a[224:227], v[36:39], v[16:19]\n"
      "v_mfma_f32_16x16x32_bf16 v[132:135], a[228:231], v[40:43], v[132:135]\n"
      "v_mfma_f32_16x16x32_bf16 v[132:135], a[232:235], v[44:47], v[132:135]\n"
      "v_mfma_f32_16x16x32_bf16 v[132:135], a[236:239], v[60:63], v[132:135]\n"
      "v_cvt_pk_bf16_f32 v214, v136, v137\n"
      "v_mfma_f32_16x16x32_bf16 v[132:135], a[0:3], v[64:67], v[132:135]\n"
      "v_cvt_pk_bf16_f32 v215, v138, v139\n"
      "v_mfma_f32_16x16x32_bf16 v[132:135], a[4:7], v[68:71], v[132:135]\n"
      "v_mfma_f32_16x16x32_bf16 v[132:135], a[8:11], v[84:87], v[132:135]\n"
      "v_mfma_f32_16x16x32_bf16 v[132:135], a[12:15], v[88:91], v[132:135]\n"
      "v_mfma_f32_16x16x32_bf16 v[132:135], a[16:19], v[92:95], v[132:135]\n"
      "v_mfma_f32_16x16x32_bf16 v[132:135], a[20:23], v[108:111], v[132:135]\n"
      "ds_write_b128 v245, v[212:215] offset:0\n"
      "v_mfma_f32_16x16x32_bf16 v[132:135], v[140:143], v[112:115], v[132:135]\n"
      "s_lshl_b32 s79, s90, 1\n"
      "v_mfma_f32_16x16x32_bf16 v[132:135], v[156:159], v[116:119], v[132:135]\n"
      "s_waitcnt vmcnt(24) lgkmcnt(2)\n"
      "s_barrier\n"
      "v_mad_i32_i24 v252, v251, s79, v252\n"
      "v_mfma_f32_16x16x32_bf16 v[136:139], v[160:163], v[36:39], v[20:23]\n"
      "ds_read_b128 v[36:39], v232 offset:24576\n"
      "s_mul_i32 s79, s79, s100\n"
      "v_mfma_f32_16x16x32_bf16 v[136:139], v[164:167], v[40:43], v[136:139]\n"
      "ds_read_b128 v[40:43], v233 offset:24576\n"
      "v_add_u32 v247, s79, v247\n"
      "v_mfma_f32_16x16x32_bf16 v[136:139], v[180:183], v[44:47], v[136:139]\n"
      "ds_read_b128 v[44:47], v234 offset:24576\n"
      "s_mul_i32 s79, s79, s101\n"
      "v_mfma_f32_16x16x32_bf16 v[136:139], v[184:187], v[60:63], v[136:139]\n"
      "ds_read_b128 v[60:63], v235 offset:24576\n"
      "v_cvt_pk_bf16_f32 v208, v132, v133\n"
      "v_mfma_f32_16x16x32_bf16 v[136:139], v[188:191], v[64:67], v[136:139]\n"
      "ds_read_b128 v[64:67], v232 offset:32768\n"
      "v_cvt_pk_bf16_f32 v209, v134, v135\n"
      "v_mfma_f32_16x16x32_bf16 v[136:139], v[204:207], v[68:71], v[136:139]\n"
      "ds_read_b128 v[68:71], v233 offset:32768\n"
      "v_add_u32 v253, s79, v253\n"
      "v_mfma_f32_16x16x32_bf16 v[136:139], a[24:27], v[84:87], v[136:139]\n"
      "ds_read_b128 v[84:87], v234 offset:32768\n"
      "v_mfma_f32_16x16x32_bf16 v[136:139], a[28:31], v[88:91], v[136:139]\n"
      "ds_read_b128 v[88:91], v235 offset:32768\n"
      "v_mfma_f32_16x16x32_bf16 v[136:139], a[32:35], v[92:95], v[136:139]\n"
      "ds_read_b128 v[92:95], v232 offset:40960\n"
      "v_mfma_f32_16x16x32_bf16 v[136:139], a[36:39], v[108:111], v[136:139]\n"
      "ds_read_b128 v[108:111], v233 offset:40960\n"
      "v_mfma_f32_16x16x32_bf16 v[136:139], a[40:43], v[112:115], v[136:139]\n"
      "ds_read_b128 v[112:115], v234 offset:40960\n"
      "v_mfma_f32_16x16x32_bf16 v[136:139], a[44:47], v[116:119], v[136:139]\n"
      "ds_read_b128 v[116:119], v235 offset:40960\n"
      "s_waitcnt lgkmcnt(8)\n"
      "v_mfma_f32_16x16x32_bf16 v[132:135], a[240:243], v[36:39], v[0:3]\n"
      "v_mfma_f32_16x16x32_bf16 v[132:135], a[244:247], v[40:43], v[132:135]\n"
      "s_add_u32 s54, s30, s33\n"
      "s_addc_u32 s55, s31, 0\n"
      "v_mfma_f32_16x16x32_bf16 v[132:135], a[248:251], v[44:47], v[132:135]\n"
      "v_mfma_f32_16x16x32_bf16 v[132:135], a[252:255], v[60:63], v[132:135]\n"
      "v_cvt_pk_bf16_f32 v210, v136, v137\n"
      "s_waitcnt lgkmcnt(4)\n"
      "v_mfma_f32_16x16x32_bf16 v[132:135], a[48:51], v[64:67], v[132:135]\n"
      "v_cvt_pk_bf16_f32 v211, v138, v139\n"
      "v_mfma_f32_16x16x32_bf16 v[132:135], a[52:55], v[68:71], v[132:135]\n"
      "s_mul_i32 s78, s92, s19\n"
      "v_mfma_f32_16x16x32_bf16 v[132:135], a[56:59], v[84:87], v[132:135]\n"
      "ds_read_b128 v[216:219], v246 offset:24576\n"
      "v_add_u32 v236, s78, v236\n"
      "v_mfma_f32_16x16x32_bf16 v[132:135], a[60:63], v[88:91], v[132:135]\n"
      "ds_read_b128 v[220:223], v246 offset:25600\n"
      "s_waitcnt lgkmcnt(2)\n"
      "v_mfma_f32_16x16x32_bf16 v[132:135], a[64:67], v[92:95], v[132:135]\n"
      "ds_read_b128 v[224:227], v246 offset:26624\n"
      "v_mfma_f32_16x16x32_bf16 v[132:135], a[68:71], v[108:111], v[132:135]\n"
      "ds_write_b128 v244, v[208:211] offset:128\n"
      "v_mfma_f32_16x16x32_bf16 v[132:135], a[72:75], v[112:115], v[132:135]\n"
      "s_mul_i32 s78, s78, s99\n"
      "v_mfma_f32_16x16x32_bf16 v[132:135], a[76:79], v[116:119], v[132:135]\n"
      "v_add_u32 v237, s78, v237\n"
      "v_mfma_f32_16x16x32_bf16 v[136:139], a[80:83], v[36:39], v[4:7]\n"
      "v_mfma_f32_16x16x32_bf16 v[136:139], a[84:87], v[40:43], v[136:139]\n"
      "v_mfma_f32_16x16x32_bf16 v[136:139], a[88:91], v[44:47], v[136:139]\n"
      "v_mfma_f32_16x16x32_bf16 v[136:139], a[92:95], v[60:63], v[136:139]\n"
      "v_cvt_pk_bf16_f32 v208, v132, v133\n"
      "v_mfma_f32_16x16x32_bf16 v[136:139], a[96:99], v[64:67], v[136:139]\n"
      "v_cvt_pk_bf16_f32 v209, v134, v135\n"
      "v_mfma_f32_16x16x32_bf16 v[136:139], a[100:103], v[68:71], v[136:139]\n"
      "s_waitcnt lgkmcnt(3)\n"
      "v_mfma_f32_16x16x32_bf16 v[136:139], a[104:107], v[84:87], v[136:139]\n"
      "global_store_dwordx4 v247, v[216:219], s[54:55] offset:0 sc1\n"
      "v_mfma_f32_16x16x32_bf16 v[136:139], a[108:111], v[88:91], v[136:139]\n"
      "s_add_u32 s52, s20, s35\n"
      "s_addc_u32 s53, s21, 0\n"
      "v_mfma_f32_16x16x32_bf16 v[136:139], a[112:115], v[92:95], v[136:139]\n"
      "s_add_u32 m0, s17, 0xc000\n"
      "v_mfma_f32_16x16x32_bf16 v[136:139], a[116:119], v[108:111], v[136:139]\n"
      "global_load_lds_dwordx4 v236, s[52:53] offset:0 nt\n"
      "v_mfma_f32_16x16x32_bf16 v[136:139], a[120:123], v[112:115], v[136:139]\n"
      "v_mfma_f32_16x16x32_bf16 v[136:139], a[124:127], v[116:119], v[136:139]\n"
      "v_mfma_f32_16x16x32_bf16 v[132:135], a[128:131], v[36:39], v[8:11]\n"
      "v_min_u32 v28, 0x370, v239\n"
      "v_mfma_f32_16x16x32_bf16 v[132:135], a[132:135], v[40:43], v[132:135]\n"
      "v_mfma_f32_16x16x32_bf16 v[132:135], a[136:139], v[44:47], v[132:135]\n"
      "v_mfma_f32_16x16x32_bf16 v[132:135], a[140:143], v[60:63], v[132:135]\n"
      "v_cvt_pk_bf16_f32 v210, v136, v137\n"
      "v_mfma_f32_16x16x32_bf16 v[132:135], a[144:147], v[64:67], v[132:135]\n"
      "v_cvt_pk_bf16_f32 v211, v138, v139\n"
      "v_mfma_f32_16x16x32_bf16 v[132:135], a[148:151], v[68:71], v[132:135]\n"
      "v_add_u32 v28, 0x2480, v28\n"
      "v_mfma_f32_16x16x32_bf16 v[132:135], a[152:155], v[84:87], v[132:135]\n"
      "s_waitcnt lgkmcnt(2)\n"
      "global_load_dwordx4 v[24:27], v28, s[28:29] sc1\n"
      "v_mfma_f32_16x16x32_bf16 v[132:135], a[156:159], v[88:91], v[132:135]\n"
      "global_store_dwordx4 v252, v[220:223], s[54:55] offset:0 sc1\n"
      "v_mfma_f32_16x16x32_bf16 v[132:135], a[160:163], v[92:95], v[132:135]\n"
      "s_add_u32 m0, s17, 0xdf00\n"
      "v_mfma_f32_16x16x32_bf16 v[132:135], a[164:167], v[108:111], v[132:135]\n"
      "ds_write_b128 v244, v[208:211] offset:12288\n"
      "v_mfma_f32_16x16x32_bf16 v[132:135], a[168:171], v[112:115], v[132:135]\n"
      "global_load_lds_dwordx4 v236, s[52:53] offset:256 nt\n"
      "v_mfma_f32_16x16x32_bf16 v[132:135], a[172:175], v[116:119], v[132:135]\n"
      "v_mfma_f32_16x16x32_bf16 v[136:139], a[176:179], v[36:39], v[12:15]\n"
      "v_mfma_f32_16x16x32_bf16 v[136:139], a[180:183], v[40:43], v[136:139]\n"
      "v_mfma_f32_16x16x32_bf16 v[136:139], a[184:187], v[44:47], v[136:139]\n"
      "v_mfma_f32_16x16x32_bf16 v[136:139], a[188:191], v[60:63], v[136:139]\n"
      "v_cvt_pk_bf16_f32 v212, v132, v133\n"
      "v_mfma_f32_16x16x32_bf16 v[136:139], a[192:195], v[64:67], v[136:139]\n"
      "v_cvt_pk_bf16_f32 v213, v134, v135\n"
      "v_mfma_f32_16x16x32_bf16 v[136:139], a[196:199], v[68:71], v[136:139]\n"
      "v_mfma_f32_16x16x32_bf16 v[136:139], a[200:203], v[84:87], v[136:139]\n"
      "v_mfma_f32_16x16x32_bf16 v[136:139], a[204:207], v[88:91], v[136:139]\n"
      "s_waitcnt lgkmcnt(2)\n"
      "v_mfma_f32_16x16x32_bf16 v[136:139], a[208:211], v[92:95], v[136:139]\n"
      "global_store_dwordx4 v253, v[224:227], s[54:55] offset:0 sc1\n"
      "v_mfma_f32_16x16x32_bf16 v[136:139], a[212:215], v[108:111], v[136:139]\n"
      "s_add_u32 m0, s17, 0xfe00\n"
      "v_mfma_f32_16x16x32_bf16 v[136:139], a[216:219], v[112:115], v[136:139]\n"
      "global_load_lds_dwordx4 v236, s[52:53] offset:512 nt\n"
      "v_mfma_f32_16x16x32_bf16 v[136:139], a[220:223], v[116:119], v[136:139]\n"
      "v_mfma_f32_16x16x32_bf16 v[132:135], a[224:227], v[36:39], v[16:19]\n"
      "v_mfma_f32_16x16x32_bf16 v[132:135], a[228:231], v[40:43], v[132:135]\n"
      "v_mfma_f32_16x16x32_bf16 v[132:135], a[232:235], v[44:47], v[132:135]\n"
      "v_mfma_f32_16x16x32_bf16 v[132:135], a[236:239], v[60:63], v[132:135]\n"
      "v_cvt_pk_bf16_f32 v214, v136, v137\n"
      "v_mfma_f32_16x16x32_bf16 v[132:135], a[0:3], v[64:67], v[132:135]\n"
      "v_cvt_pk_bf16_f32 v215, v138, v139\n"
      "v_mfma_f32_16x16x32_bf16 v[132:135], a[4:7], v[68:71], v[132:135]\n"
      "v_mfma_f32_16x16x32_bf16 v[132:135], a[8:11], v[84:87], v[132:135]\n"
      "v_mfma_f32_16x16x32_bf16 v[132:135], a[12:15], v[88:91], v[132:135]\n"
      "v_mfma_f32_16x16x32_bf16 v[132:135], a[16:19], v[92:95], v[132:135]\n"
      "v_mfma_f32_16x16x32_bf16 v[132:135], a[20:23], v[108:111], v[132:135]\n"
      "ds_write_b128 v245, v[212:215] offset:12288\n"
      "v_mfma_f32_16x16x32_bf16 v[132:135], v[140:143], v[112:115], v[132:135]\n"
      "v_mfma_f32_16x16x32_bf16 v[132:135], v[156:159], v[116:119], v[132:135]\n"
      "s_waitcnt lgkmcnt(2)\n"
      "s_barrier\n"
      "v_mfma_f32_16x16x32_bf16 v[136:139], v[160:163], v[36:39], v[20:23]\n"
      "ds_read_b128 v[36:39], v232 offset:24832\n"
      "v_mfma_f32_16x16x32_bf16 v[136:139], v[164:167], v[40:43], v[136:139]\n"
      "ds_read_b128 v[40:43], v233 offset:24832\n"
      "v_mfma_f32_16x16x32_bf16 v[136:139], v[180:183], v[44:47], v[136:139]\n"
      "ds_read_b128 v[44:47], v234 offset:24832\n"
      "v_mfma_f32_16x16x32_bf16 v[136:139], v[184:187], v[60:63], v[136:139]\n"
      "ds_read_b128 v[60:63], v235 offset:24832\n"
      "v_cvt_pk_bf16_f32 v208, v132, v133\n"
      "v_mfma_f32_16x16x32_bf16 v[136:139], v[188:191], v[64:67], v[136:139]\n"
      "ds_read_b128 v[64:67], v232 offset:33024\n"
      "v_cvt_pk_bf16_f32 v209, v134, v135\n"
      "v_mfma_f32_16x16x32_bf16 v[136:139], v[204:207], v[68:71], v[136:139]\n"
      "ds_read_b128 v[68:71], v233 offset:33024\n"
      "v_mfma_f32_16x16x32_bf16 v[136:139], a[24:27], v[84:87], v[136:139]\n"
      "ds_read_b128 v[84:87], v234 offset:33024\n"
      "v_mfma_f32_16x16x32_bf16 v[136:139], a[28:31], v[88:91], v[136:139]\n"
      "ds_read_b128 v[88:91], v235 offset:33024\n"
      "v_mfma_f32_16x16x32_bf16 v[136:139], a[32:35], v[92:95], v[136:139]\n"
      "ds_read_b128 v[92:95], v232 offset:41216\n"
      "v_mfma_f32_16x16x32_bf16 v[136:139], a[36:39], v[108:111], v[136:139]\n"
      "ds_read_b128 v[108:111], v233 offset:41216\n"
      "v_mfma_f32_16x16x32_bf16 v[136:139], a[40:43], v[112:115], v[136:139]\n"
      "ds_read_b128 v[112:115], v234 offset:41216\n"
      "v_mfma_f32_16x16x32_bf16 v[136:139], a[44:47], v[116:119], v[136:139]\n"
      "ds_read_b128 v[116:119], v235 offset:41216\n"
      "s_waitcnt lgkmcnt(8)\n"
      "v_mfma_f32_16x16x32_bf16 v[132:135], a[240:243], v[36:39], v[0:3]\n"
      "v_add_u32 v232, 0xffff4000, v232\n"
      "v_mfma_f32_16x16x32_bf16 v[132:135], a[244:247], v[40:43], v[132:135]\n"
      "v_add_u32 v233, 0xffff4000, v233\n"
      "v_mfma_f32_16x16x32_bf16 v[132:135], a[248:251], v[44:47], v[132:135]\n"
      "v_add_u32 v234, 0xffff4000, v234\n"
      "v_mfma_f32_16x16x32_bf16 v[132:135], a[252:255], v[60:63], v[132:135]\n"
      "v_add_u32 v235, 0xffff4000, v235\n"
      "s_waitcnt lgkmcnt(4)\n"
      "v_mfma_f32_16x16x32_bf16 v[132:135], a[48:51], v[64:67], v[132:135]\n"
      "v_cvt_pk_bf16_f32 v210, v136, v137\n"
      "v_mfma_f32_16x16x32_bf16 v[132:135], a[52:55], v[68:71], v[132:135]\n"
      "v_cvt_pk_bf16_f32 v211, v138, v139\n"
      "v_mfma_f32_16x16x32_bf16 v[132:135], a[56:59], v[84:87], v[132:135]\n"
      "ds_read_b128 v[216:219], v246 offset:0\n"
      "v_mfma_f32_16x16x32_bf16 v[132:135], a[60:63], v[88:91], v[132:135]\n"
      "ds_read_b128 v[220:223], v246 offset:1024\n"
      "s_waitcnt lgkmcnt(2)\n"
      "v_mfma_f32_16x16x32_bf16 v[132:135], a[64:67], v[92:95], v[132:135]\n"
      "ds_read_b128 v[224:227], v246 offset:2048\n"
      "v_mfma_f32_16x16x32_bf16 v[132:135], a[68:71], v[108:111], v[132:135]\n"
      "v_lshlrev_b32 v0, 16, v48\n"
      "v_mfma_f32_16x16x32_bf16 v[132:135], a[72:75], v[112:115], v[132:135]\n"
      "ds_write_b128 v244, v[208:211] offset:12416\n"
      "v_mfma_f32_16x16x32_bf16 v[132:135], a[76:79], v[116:119], v[132:135]\n"
      "v_and_b32 v1, 0xffff0000, v48\n"
      "v_mfma_f32_16x16x32_bf16 v[136:139], a[80:83], v[36:39], v[4:7]\n"
      "v_lshlrev_b32 v2, 16, v49\n"
      "v_mfma_f32_16x16x32_bf16 v[136:139], a[84:87], v[40:43], v[136:139]\n"
      "v_and_b32 v3, 0xffff0000, v49\n"
      "v_mfma_f32_16x16x32_bf16 v[136:139], a[88:91], v[44:47], v[136:139]\n"
      "v_mfma_f32_16x16x32_bf16 v[136:139], a[92:95], v[60:63], v[136:139]\n"
      "v_cvt_pk_bf16_f32 v208, v132, v133\n"
      "LPD9:\n"
      "s_cbranch_execz LPD12\n"
      "v_mfma_f32_16x16x32_bf16 v[136:139], a[96:99], v[64:67], v[136:139]\n"
      "v_cvt_pk_bf16_f32 v209, v134, v135\n"
      "v_mfma_f32_16x16x32_bf16 v[136:139], a[100:103], v[68:71], v[136:139]\n"
      "s_waitcnt lgkmcnt(3)\n"
      "v_mfma_f32_16x16x32_bf16 v[136:139], a[104:107], v[84:87], v[136:139]\n"
      "global_store_dwordx4 v247, v[216:219], s[54:55] offset:768 sc1\n"
      "v_mfma_f32_16x16x32_bf16 v[136:139], a[108:111], v[88:91], v[136:139]\n"
      "s_add_u32 m0, s17, 0xd000\n"
      "v_mfma_f32_16x16x32_bf16 v[136:139], a[112:115], v[92:95], v[136:139]\n"
      "global_load_lds_dwordx4 v237, s[52:53] offset:0 nt\n"
      "v_mfma_f32_16x16x32_bf16 v[136:139], a[116:119], v[108:111], v[136:139]\n"
      "v_lshlrev_b32 v4, 16, v50\n"
      "v_mfma_f32_16x16x32_bf16 v[136:139], a[120:123], v[112:115], v[136:139]\n"
      "v_and_b32 v5, 0xffff0000, v50\n"
      "v_mfma_f32_16x16x32_bf16 v[136:139], a[124:127], v[116:119], v[136:139]\n"
      "v_lshlrev_b32 v6, 16, v51\n"
      "v_mfma_f32_16x16x32_bf16 v[132:135], a[128:131], v[36:39], v[8:11]\n"
      "v_and_b32 v7, 0xffff0000, v51\n"
      "v_mfma_f32_16x16x32_bf16 v[132:135], a[132:135], v[40:43], v[132:135]\n"
      "v_mfma_f32_16x16x32_bf16 v[132:135], a[136:139], v[44:47], v[132:135]\n"
      "v_mfma_f32_16x16x32_bf16 v[132:135], a[140:143], v[60:63], v[132:135]\n"
      "v_cvt_pk_bf16_f32 v210, v136, v137\n"
      "v_mfma_f32_16x16x32_bf16 v[132:135], a[144:147], v[64:67], v[132:135]\n"
      "v_cvt_pk_bf16_f32 v211, v138, v139\n"
      "v_mfma_f32_16x16x32_bf16 v[132:135], a[148:151], v[68:71], v[132:135]\n"
      "v_lshlrev_b32 v8, 16, v52\n"
      "v_mfma_f32_16x16x32_bf16 v[132:135], a[152:155], v[84:87], v[132:135]\n"
      "s_waitcnt lgkmcnt(2)\n"
      "v_mfma_f32_16x16x32_bf16 v[132:135], a[156:159], v[88:91], v[132:135]\n"
      "global_store_dwordx4 v252, v[220:223], s[54:55] offset:768 sc1\n"
      "v_mfma_f32_16x16x32_bf16 v[132:135], a[160:163], v[92:95], v[132:135]\n"
      "s_add_u32 m0, s17, 0xef00\n"
      "v_mfma_f32_16x16x32_bf16 v[132:135], a[164:167], v[108:111], v[132:135]\n"
      "ds_write_b128 v244, v[208:211] offset:24576\n"
      "v_mfma_f32_16x16x32_bf16 v[132:135], a[168:171], v[112:115], v[132:135]\n"
      "global_load_lds_dwordx4 v237, s[52:53] offset:256 nt\n"
      "v_mfma_f32_16x16x32_bf16 v[132:135], a[172:175], v[116:119], v[132:135]\n"
      "v_and_b32 v9, 0xffff0000, v52\n"
      "v_mfma_f32_16x16x32_bf16 v[136:139], a[176:179], v[36:39], v[12:15]\n"
      "v_lshlrev_b32 v10, 16, v53\n"
      "v_mfma_f32_16x16x32_bf16 v[136:139], a[180:183], v[40:43], v[136:139]\n"
      "v_and_b32 v11, 0xffff0000, v53\n"
      "v_mfma_f32_16x16x32_bf16 v[136:139], a[184:187], v[44:47], v[136:139]\n"
      "v_mfma_f32_16x16x32_bf16 v[136:139], a[188:191], v[60:63], v[136:139]\n"
      "v_cvt_pk_bf16_f32 v212, v132, v133\n"
      "v_mfma_f32_16x16x32_bf16 v[136:139], a[192:195], v[64:67], v[136:139]\n"
      "v_cvt_pk_bf16_f32 v213, v134, v135\n"
      "v_mfma_f32_16x16x32_bf16 v[136:139], a[196:199], v[68:71], v[136:139]\n"
      "v_lshlrev_b32 v12, 16, v54\n"
      "v_mfma_f32_16x16x32_bf16 v[136:139], a[200:203], v[84:87], v[136:139]\n"
      "v_and_b32 v13, 0xffff0000, v54\n"
      "v_mfma_f32_16x16x32_bf16 v[136:139], a[204:207], v[88:91], v[136:139]\n"
      "s_waitcnt lgkmcnt(2)\n"
      "v_mfma_f32_16x16x32_bf16 v[136:139], a[208:211], v[92:95], v[136:139]\n"
      "global_store_dwordx4 v253, v[224:227], s[54:55] offset:768 sc1\n"
      "v_mfma_f32_16x16x32_bf16 v[136:139], a[212:215], v[108:111], v[136:139]\n"
      "s_add_u32 m0, s17, 0x10e00\n"
      "v_mfma_f32_16x16x32_bf16 v[136:139], a[216:219], v[112:115], v[136:139]\n"
      "global_load_lds_dwordx4 v237, s[52:53] offset:512 nt\n"
      "v_mfma_f32_16x16x32_bf16 v[136:139], a[220:223], v[116:119], v[136:139]\n"
      "v_lshlrev_b32 v14, 16, v55\n"
      "v_mfma_f32_16x16x32_bf16 v[132:135], a[224:227], v[36:39], v[16:19]\n"
      "v_and_b32 v15, 0xffff0000, v55\n"
      "v_mfma_f32_16x16x32_bf16 v[132:135], a[228:231], v[40:43], v[132:135]\n"
      "v_mfma_f32_16x16x32_bf16 v[132:135], a[232:235], v[44:47], v[132:135]\n"
      "v_mfma_f32_16x16x32_bf16 v[132:135], a[236:239], v[60:63], v[132:135]\n"
      "v_cvt_pk_bf16_f32 v214, v136, v137\n"
      "v_mfma_f32_16x16x32_bf16 v[132:135], a[0:3], v[64:67], v[132:135]\n"
      "v_cvt_pk_bf16_f32 v215, v138, v139\n"
      "v_mfma_f32_16x16x32_bf16 v[132:135], a[4:7], v[68:71], v[132:135]\n"
      "v_lshlrev_b32 v16, 16, v56\n"
      "v_mfma_f32_16x16x32_bf16 v[132:135], a[8:11], v[84:87], v[132:135]\n"
      "v_and_b32 v17, 0xffff0000, v56\n"
      "v_mfma_f32_16x16x32_bf16 v[132:135], a[12:15], v[88:91], v[132:135]\n"
      "v_lshlrev_b32 v18, 16, v57\n"
      "v_mfma_f32_16x16x32_bf16 v[132:135], a[16:19], v[92:95], v[132:135]\n"
      "v_and_b32 v19, 0xffff0000, v57\n"
      "v_mfma_f32_16x16x32_bf16 v[132:135], a[20:23], v[108:111], v[132:135]\n"
      "ds_write_b128 v245, v[212:215] offset:24576\n"
      "v_mfma_f32_16x16x32_bf16 v[132:135], v[140:143], v[112:115], v[132:135]\n"
      "v_mfma_f32_16x16x32_bf16 v[132:135], v[156:159], v[116:119], v[132:135]\n"
      "s_waitcnt vmcnt(25) lgkmcnt(2)\n"
      "s_barrier\n"
      "v_mfma_f32_16x16x32_bf16 v[136:139], v[160:163], v[36:39], v[20:23]\n"
      "ds_read_b128 v[36:39], v232 offset:0\n"
      "v_mfma_f32_16x16x32_bf16 v[136:139], v[164:167], v[40:43], v[136:139]\n"
      "ds_read_b128 v[40:43], v233 offset:0\n"
      "v_mfma_f32_16x16x32_bf16 v[136:139], v[180:183], v[44:47], v[136:139]\n"
      "ds_read_b128 v[44:47], v234 offset:0\n"
      "v_mfma_f32_16x16x32_bf16 v[136:139], v[184:187], v[60:63], v[136:139]\n"
      "ds_read_b128 v[60:63], v235 offset:0\n"
      "v_cvt_pk_bf16_f32 v208, v132, v133\n"
      "v_mfma_f32_16x16x32_bf16 v[136:139], v[188:191], v[64:67], v[136:139]\n"
      "ds_read_b128 v[64:67], v232 offset:8192\n"
      "v_cvt_pk_bf16_f32 v209, v134, v135\n"
      "v_mfma_f32_16x16x32_bf16 v[136:139], v[204:207], v[68:71], v[136:139]\n"
      "ds_read_b128 v[68:71], v233 offset:8192\n"
      "v_mfma_f32_16x16x32_bf16 v[136:139], a[24:27], v[84:87], v[136:139]\n"
      "ds_read_b128 v[84:87], v234 offset:8192\n"
      "v_mfma_f32_16x16x32_bf16 v[136:139], a[28:31], v[88:91], v[136:139]\n"
      "ds_read_b128 v[88:91], v235 offset:8192\n"
      "v_mfma_f32_16x16x32_bf16 v[136:139], a[32:35], v[92:95], v[136:139]\n"
      "ds_read_b128 v[92:95], v232 offset:16384\n"
      "v_mfma_f32_16x16x32_bf16 v[136:139], a[36:39], v[108:111], v[136:139]\n"
      "ds_read_b128 v[108:111], v233 offset:16384\n"
      "v_mfma_f32_16x16x32_bf16 v[136:139], a[40:43], v[112:115], v[136:139]\n"
      "ds_read_b128 v[112:115], v234 offset:16384\n"
      "v_mfma_f32_16x16x32_bf16 v[136:139], a[44:47], v[116:119], v[136:139]\n"
      "ds_read_b128 v[116:119], v235 offset:16384\n"
      "s_waitcnt lgkmcnt(8)\n"
      "v_mfma_f32_16x16x32_bf16 v[132:135], a[240:243], v[36:39], v[0:3]\n"
      "v_lshlrev_b32 v20, 16, v58\n"
      "v_mfma_f32_16x16x32_bf16 v[132:135], a[244:247], v[40:43], v[132:135]\n"
      "v_and_b32 v21, 0xffff0000, v58\n"
      "v_mfma_f32_16x16x32_bf16 v[132:135], a[248:251], v[44:47], v[132:135]\n"
      "v_lshlrev_b32 v22, 16, v59\n"
      "v_mfma_f32_16x16x32_bf16 v[132:135], a[252:255], v[60:63], v[132:135]\n"
      "v_and_b32 v23, 0xffff0000, v59\n"
      "s_waitcnt lgkmcnt(4)\n"
      "v_mfma_f32_16x16x32_bf16 v[132:135], a[48:51], v[64:67], v[132:135]\n"
      "v_cvt_pk_bf16_f32 v210, v136, v137\n"
      "v_mfma_f32_16x16x32_bf16 v[132:135], a[52:55], v[68:71], v[132:135]\n"
      "v_cvt_pk_bf16_f32 v211, v138, v139\n"
      "v_mfma_f32_16x16x32_bf16 v[132:135], a[56:59], v[84:87], v[132:135]\n"
      "s_add_u32 s54, s44, s33\n"
      "s_addc_u32 s55, s45, 0\n"
      "v_mfma_f32_16x16x32_bf16 v[132:135], a[60:63], v[88:91], v[132:135]\n"
      "ds_read_b128 v[216:219], v246 offset:12288\n"
      "s_waitcnt lgkmcnt(1)\n"
      "v_mfma_f32_16x16x32_bf16 v[132:135], a[64:67], v[92:95], v[132:135]\n"
      "ds_read_b128 v[220:223], v246 offset:13312\n"
      "v_mfma_f32_16x16x32_bf16 v[132:135], a[68:71], v[108:111], v[132:135]\n"
      "ds_read_b128 v[224:227], v246 offset:14336\n"
      "v_mfma_f32_16x16x32_bf16 v[132:135], a[72:75], v[112:115], v[132:135]\n"
      "ds_write_b128 v244, v[208:211] offset:24704\n"
      "v_mfma_f32_16x16x32_bf16 v[132:135], a[76:79], v[116:119], v[132:135]\n"
      "v_mfma_f32_16x16x32_bf16 v[136:139], a[80:83], v[36:39], v[4:7]\n"
      "v_mfma_f32_16x16x32_bf16 v[136:139], a[84:87], v[40:43], v[136:139]\n"
      "v_mfma_f32_16x16x32_bf16 v[136:139], a[88:91], v[44:47], v[136:139]\n"
      "v_mfma_f32_16x16x32_bf16 v[136:139], a[92:95], v[60:63], v[136:139]\n"
      "v_cvt_pk_bf16_f32 v208, v132, v133\n"
      "v_mfma_f32_16x16x32_bf16 v[136:139], a[96:99], v[64:67], v[136:139]\n"
      "v_cvt_pk_bf16_f32 v209, v134, v135\n"
      "v_mfma_f32_16x16x32_bf16 v[136:139], a[100:103], v[68:71], v[136:139]\n"
      "s_waitcnt lgkmcnt(3)\n"
      "v_mfma_f32_16x16x32_bf16 v[136:139], a[104:107], v[84:87], v[136:139]\n"
      "global_store_dwordx4 v247, v[216:219], s[54:55] offset:0 sc1\n"
      "v_mfma_f32_16x16x32_bf16 v[136:139], a[108:111], v[88:91], v[136:139]\n"
      "s_add_u32 s52, s42, s35\n"
      "s_addc_u32 s53, s43, 0\n"
      "v_mfma_f32_16x16x32_bf16 v[136:139], a[112:115], v[92:95], v[136:139]\n"
      "s_add_u32 m0, s17, 0x12000\n"
      "v_mfma_f32_16x16x32_bf16 v[136:139], a[116:119], v[108:111], v[136:139]\n"
      "global_load_lds_dwordx4 v236, s[52:53] offset:0 nt\n"
      "v_mfma_f32_16x16x32_bf16 v[136:139], a[120:123], v[112:115], v[136:139]\n"
      "v_mfma_f32_16x16x32_bf16 v[136:139], a[124:127], v[116:119], v[136:139]\n"
      "v_mfma_f32_16x16x32_bf16 v[132:135], a[128:131], v[36:39], v[8:11]\n"
      "v_mfma_f32_16x16x32_bf16 v[132:135], a[132:135], v[40:43], v[132:135]\n"
      "v_mfma_f32_16x16x32_bf16 v[132:135], a[136:139], v[44:47], v[132:135]\n"
      "v_mfma_f32_16x16x32_bf16 v[132:135], a[140:143], v[60:63], v[132:135]\n"
      "v_cvt_pk_bf16_f32 v210, v136, v137\n"
      "v_mfma_f32_16x16x32_bf16 v[132:135], a[144:147], v[64:67], v[132:135]\n"
      "v_cvt_pk_bf16_f32 v211, v138, v139\n"
      "v_mfma_f32_16x16x32_bf16 v[132:135], a[148:151], v[68:71], v[132:135]\n"
      "v_mfma_f32_16x16x32_bf16 v[132:135], a[152:155], v[84:87], v[132:135]\n"
      "s_waitcnt lgkmcnt(2)\n"
      "v_mfma_f32_16x16x32_bf16 v[132:135], a[156:159], v[88:91], v[132:135]\n"
      "global_store_dwordx4 v252, v[220:223], s[54:55] offset:0 sc1\n"
      "v_mfma_f32_16x16x32_bf16 v[132:135], a[160:163], v[92:95], v[132:135]\n"
      "s_add_u32 m0, s17, 0x13f00\n"
      "v_mfma_f32_16x16x32_bf16 v[132:135], a[164:167], v[108:111], v[132:135]\n"
      "ds_write_b128 v244, v[208:211] offset:0\n"
      "v_mfma_f32_16x16x32_bf16 v[132:135], a[168:171], v[112:115], v[132:135]\n"
      "global_load_lds_dwordx4 v236, s[52:53] offset:256 nt\n"
      "v_mfma_f32_16x16x32_bf16 v[132:135], a[172:175], v[116:119], v[132:135]\n"
      "v_mfma_f32_16x16x32_bf16 v[136:139], a[176:179], v[36:39], v[12:15]\n"
      "v_mfma_f32_16x16x32_bf16 v[136:139], a[180:183], v[40:43], v[136:139]\n"
      "v_mfma_f32_16x16x32_bf16 v[136:139], a[184:187], v[44:47], v[136:139]\n"
      "v_mfma_f32_16x16x32_bf16 v[136:139], a[188:191], v[60:63], v[136:139]\n"
      "v_cvt_pk_bf16_f32 v212, v132, v133\n"
      "v_mfma_f32_16x16x32_bf16 v[136:139], a[192:195], v[64:67], v[136:139]\n"
      "v_cvt_pk_bf16_f32 v213, v134, v135\n"
      "v_mfma_f32_16x16x32_bf16 v[136:139], a[196:199], v[68:71], v[136:139]\n"
      "v_mfma_f32_16x16x32_bf16 v[136:139], a[200:203], v[84:87], v[136:139]\n"
      "v_mfma_f32_16x16x32_bf16 v[136:139], a[204:207], v[88:91], v[136:139]\n"
      "s_waitcnt lgkmcnt(2)\n"
      "v_mfma_f32_16x16x32_bf16 v[136:139], a[208:211], v[92:95], v[136:139]\n"
      "global_store_dwordx4 v253, v[224:227], s[54:55] offset:0 sc1\n"
      "v_mfma_f32_16x16x32_bf16 v[136:139], a[212:215], v[108:111], v[136:139]\n"
      "s_add_u32 m0, s17, 0x15e00\n"
      "v_mfma_f32_16x16x32_bf16 v[136:139], a[216:219], v[112:115], v[136:139]\n"
      "global_load_lds_dwordx4 v236, s[52:53] offset:512 nt\n"
      "v_mfma_f32_16x16x32_bf16 v[136:139], a[220:223], v[116:119], v[136:139]\n"
      "v_mfma_f32_16x16x32_bf16 v[132:135], a[224:227], v[36:39], v[16:19]\n"
      "v_mfma_f32_16x16x32_bf16 v[132:135], a[228:231], v[40:43], v[132:135]\n"
      "v_mfma_f32_16x16x32_bf16 v[132:135], a[232:235], v[44:47], v[132:135]\n"
      "v_mfma_f32_16x16x32_bf16 v[132:135], a[236:239], v[60:63], v[132:135]\n"
      "v_cvt_pk_bf16_f32 v214, v136, v137\n"
      "v_mfma_f32_16x16x32_bf16 v[132:135], a[0:3], v[64:67], v[132:135]\n"
      "v_cvt_pk_bf16_f32 v215, v138, v139\n"
      "v_mfma_f32_16x16x32_bf16 v[132:135], a[4:7], v[68:71], v[132:135]\n"
      "v_mfma_f32_16x16x32_bf16 v[132:135], a[8:11], v[84:87], v[132:135]\n"
      "v_mfma_f32_16x16x32_bf16 v[132:135], a[12:15], v[88:91], v[132:135]\n"
      "v_mfma_f32_16x16x32_bf16 v[132:135], a[16:19], v[92:95], v[132:135]\n"
      "v_mfma_f32_16x16x32_bf16 v[132:135], a[20:23], v[108:111], v[132:135]\n"
      "ds_write_b128 v245, v[212:215] offset:0\n"
      "v_mfma_f32_16x16x32_bf16 v[132:135], v[140:143], v[112:115], v[132:135]\n"
      "v_mfma_f32_16x16x32_bf16 v[132:135], v[156:159], v[116:119], v[132:135]\n"
      "s_waitcnt lgkmcnt(2)\n"
      "s_barrier\n"
      "v_mfma_f32_16x16x32_bf16 v[136:139], v[160:163], v[36:39], v[20:23]\n"
      "ds_read_b128 v[36:39], v232 offset:256\n"
      "v_mfma_f32_16x16x32_bf16 v[136:139], v[164:167], v[40:43], v[136:139]\n"
      "ds_read_b128 v[40:43], v233 offset:256\n"
      "v_mfma_f32_16x16x32_bf16 v[136:139], v[180:183], v[44:47], v[136:139]\n"
      "ds_read_b128 v[44:47], v234 offset:256\n"
      "v_mfma_f32_16x16x32_bf16 v[136:139], v[184:187], v[60:63], v[136:139]\n"
      "ds_read_b128 v[60:63], v235 offset:256\n"
      "v_cvt_pk_bf16_f32 v208, v132, v133\n"
      "v_mfma_f32_16x16x32_bf16 v[136:139], v[188:191], v[64:67], v[136:139]\n"
      "ds_read_b128 v[64:67], v232 offset:8448\n"
      "v_cvt_pk_bf16_f32 v209, v134, v135\n"
      "v_mfma_f32_16x16x32_bf16 v[136:139], v[204:207], v[68:71], v[136:139]\n"
      "ds_read_b128 v[68:71], v233 offset:8448\n"
      "v_mfma_f32_16x16x32_bf16 v[136:139], a[24:27], v[84:87], v[136:139]\n"
      "ds_read_b128 v[84:87], v234 offset:8448\n"
      "v_mfma_f32_16x16x32_bf16 v[136:139], a[28:31], v[88:91], v[136:139]\n"
      "ds_read_b128 v[88:91], v235 offset:8448\n"
      "v_mfma_f32_16x16x32_bf16 v[136:139], a[32:35], v[92:95], v[136:139]\n"
      "ds_read_b128 v[92:95], v232 offset:16640\n"
      "v_mfma_f32_16x16x32_bf16 v[136:139], a[36:39], v[108:111], v[136:139]\n"
      "ds_read_b128 v[108:111], v233 offset:16640\n"
      "v_mfma_f32_16x16x32_bf16 v[136:139], a[40:43], v[112:115], v[136:139]\n"
      "ds_read_b128 v[112:115], v234 offset:16640\n"
      "v_mfma_f32_16x16x32_bf16 v[136:139], a[44:47], v[116:119], v[136:139]\n"
      "ds_read_b128 v[116:119], v235 offset:16640\n"
      "s_waitcnt lgkmcnt(8)\n"
      "v_mfma_f32_16x16x32_bf16 v[132:135], a[240:243], v[36:39], v[0:3]\n"
      "v_mfma_f32_16x16x32_bf16 v[132:135], a[244:247], v[40:43], v[132:135]\n"
      "v_mfma_f32_16x16x32_bf16 v[132:135], a[248:251], v[44:47], v[132:135]\n"
      "v_mfma_f32_16x16x32_bf16 v[132:135], a[252:255], v[60:63], v[132:135]\n"
      "v_cvt_pk_bf16_f32 v210, v136, v137\n"
      "s_waitcnt lgkmcnt(4)\n"
      "v_mfma_f32_16x16x32_bf16 v[132:135], a[48:51], v[64:67], v[132:135]\n"
      "v_cvt_pk_bf16_f32 v211, v138, v139\n"
      "v_mfma_f32_16x16x32_bf16 v[132:135], a[52:55], v[68:71], v[132:135]\n"
      "v_mfma_f32_16x16x32_bf16 v[132:135], a[56:59], v[84:87], v[132:135]\n"
      "ds_read_b128 v[216:219], v246 offset:24576\n"
      "v_mfma_f32_16x16x32_bf16 v[132:135], a[60:63], v[88:91], v[132:135]\n"
      "ds_read_b128 v[220:223], v246 offset:25600\n"
      "s_waitcnt lgkmcnt(2)\n"
      "v_mfma_f32_16x16x32_bf16 v[132:135], a[64:67], v[92:95], v[132:135]\n"
      "ds_read_b128 v[224:227], v246 offset:26624\n"
      "v_mfma_f32_16x16x32_bf16 v[132:135], a[68:71], v[108:111], v[132:135]\n"
      "ds_write_b128 v244, v[208:211] offset:128\n"
      "v_mfma_f32_16x16x32_bf16 v[132:135], a[72:75], v[112:115], v[132:135]\n"
      "v_mfma_f32_16x16x32_bf16 v[132:135], a[76:79], v[116:119], v[132:135]\n"
      "v_mfma_f32_16x16x32_bf16 v[136:139], a[80:83], v[36:39], v[4:7]\n"
      "v_mfma_f32_16x16x32_bf16 v[136:139], a[84:87], v[40:43], v[136:139]\n"
      "v_mfma_f32_16x16x32_bf16 v[136:139], a[88:91], v[44:47], v[136:139]\n"
      "v_mfma_f32_16x16x32_bf16 v[136:139], a[92:95], v[60:63], v[136:139]\n"
      "v_cvt_pk_bf16_f32 v208, v132, v133\n"
      "v_mfma_f32_16x16x32_bf16 v[136:139], a[96:99], v[64:67], v[136:139]\n"
      "v_cvt_pk_bf16_f32 v209, v134, v135\n"
      "v_mfma_f32_16x16x32_bf16 v[136:139], a[100:103], v[68:71], v[136:139]\n"
      "s_waitcnt lgkmcnt(3)\n"
      "v_mfma_f32_16x16x32_bf16 v[136:139], a[104:107], v[84:87], v[136:139]\n"
      "global_store_dwordx4 v247, v[216:219], s[54:55] offset:768 sc1\n"
      "v_mfma_f32_16x16x32_bf16 v[136:139], a[108:111], v[88:91], v[136:139]\n"
      "s_add_u32 m0, s17, 0x13000\n"
      "v_mfma_f32_16x16x32_bf16 v[136:139], a[112:115], v[92:95], v[136:139]\n"
      "global_load_lds_dwordx4 v237, s[52:53] offset:0 nt\n"
      "v_mfma_f32_16x16x32_bf16 v[136:139], a[116:119], v[108:111], v[136:139]\n"
      "v_mfma_f32_16x16x32_bf16 v[136:139], a[120:123], v[112:115], v[136:139]\n"
      "v_mfma_f32_16x16x32_bf16 v[136:139], a[124:127], v[116:119], v[136:139]\n"
      "v_mfma_f32_16x16x32_bf16 v[132:135], a[128:131], v[36:39], v[8:11]\n"
      "v_mfma_f32_16x16x32_bf16 v[132:135], a[132:135], v[40:43], v[132:135]\n"
      "v_mfma_f32_16x16x32_bf16 v[132:135], a[136:139], v[44:47], v[132:135]\n"
      "v_mfma_f32_16x16x32_bf16 v[132:135], a[140:143], v[60:63], v[132:135]\n"
      "v_cvt_pk_bf16_f32 v210, v136, v137\n"
      "v_mfma_f32_16x16x32_bf16 v[132:135], a[144:147], v[64:67], v[132:135]\n"
      "v_cvt_pk_bf16_f32 v211, v138, v139\n"
      "v_mfma_f32_16x16x32_bf16 v[132:135], a[148:151], v[68:71], v[132:135]\n"
      "v_mfma_f32_16x16x32_bf16 v[132:135], a[152:155], v[84:87], v[132:135]\n"
      "s_waitcnt lgkmcnt(2)\n"
      "s_waitcnt vmcnt(18)\n"
      "s_mov_b32 s87, 1\n"
      "s_or_b32 s56, s51, s76\n"
      "s_cmp_lg_u32 s56, 0\n"
      "s_cbranch_scc1 LG1\n"
      "v_cmp_ne_u64 s[58:59], s[80:81], v[24:25]\n"
      "v_cmp_ne_u64 s[56:57], s[80:81], v[26:27]\n"
      "s_mov_b32 s87, 0\n"
      "s_nop 3\n"
      "s_or_b64 s[56:57], s[56:57], s[58:59]\n"
      "s_cbranch_scc1 LG1\n"
      "v_mov_b32 v28, 0\n"
      "v_mov_b32 v30, s80\n"
      "v_mov_b32 v31, s81\n"
      "s_mov_b32 s87, 1\n"
      "s_mov_b64 exec, 1\n"
      "global_store_dwordx2 v28, v[30:31], s[28:29] sc1\n"
      "s_mov_b64 exec, -1\n"
      "s_nop 4\n"
      "LG1:\n"
      "v_mfma_f32_16x16x32_bf16 v[132:135], a[156:159], v[88:91], v[132:135]\n"
      "global_store_dwordx4 v252, v[220:223], s[54:55] offset:768 sc1\n"
      "v_mfma_f32_16x16x32_bf16 v[132:135], a[160:163], v[92:95], v[132:135]\n"
      "s_add_u32 m0, s17, 0x14f00\n"
      "v_mfma_f32_16x16x32_bf16 v[132:135], a[164:167], v[108:111], v[132:135]\n"
      "ds_write_b128 v244, v[208:211] offset:12288\n"
      "v_mfma_f32_16x16x32_bf16 v[132:135], a[168:171], v[112:115], v[132:135]\n"
      "global_load_lds_dwordx4 v237, s[52:53] offset:256 nt\n"
      "v_mfma_f32_16x16x32_bf16 v[132:135], a[172:175], v[116:119], v[132:135]\n"
      "v_mfma_f32_16x16x32_bf16 v[136:139], a[176:179], v[36:39], v[12:15]\n"
      "v_mfma_f32_16x16x32_bf16 v[136:139], a[180:183], v[40:43], v[136:139]\n"
      "v_mfma_f32_16x16x32_bf16 v[136:139], a[184:187], v[44:47], v[136:139]\n"
      "v_mfma_f32_16x16x32_bf16 v[136:139], a[188:191], v[60:63], v[136:139]\n"
      "v_cvt_pk_bf16_f32 v212, v132, v133\n"
      "v_mfma_f32_16x16x32_bf16 v[136:139], a[192:195], v[64:67], v[136:139]\n"
      "v_cvt_pk_bf16_f32 v213, v134, v135\n"
      "v_mfma_f32_16x16x32_bf16 v[136:139], a[196:199], v[68:71], v[136:139]\n"
      "v_mfma_f32_16x16x32_bf16 v[136:139], a[200:203], v[84:87], v[136:139]\n"
      "v_mfma_f32_16x16x32_bf16 v[136:139], a[204:207], v[88:91], v[136:139]\n"
      "s_waitcnt lgkmcnt(2)\n"
      "v_mfma_f32_16x16x32_bf16 v[136:139], a[208:211], v[92:95], v[136:139]\n"
      "global_store_dwordx4 v253, v[224:227], s[54:55] offset:768 sc1\n"
      "v_mfma_f32_16x16x32_bf16 v[136:139], a[212:215], v[108:111], v[136:139]\n"
      "s_add_u32 m0, s17, 0x16e00\n"
      "v_mfma_f32_16x16x32_bf16 v[136:139], a[216:219], v[112:115], v[136:139]\n"
      "global_load_lds_dwordx4 v237, s[52:53] offset:512 nt\n"
      "v_mfma_f32_16x16x32_bf16 v[136:139], a[220:223], v[116:119], v[136:139]\n"
      "v_mfma_f32_16x16x32_bf16 v[132:135], a[224:227], v[36:39], v[16:19]\n"
      "v_mfma_f32_16x16x32_bf16 v[132:135], a[228:231], v[40:43], v[132:135]\n"
      "v_mfma_f32_16x16x32_bf16 v[132:135], a[232:235], v[44:47], v[132:135]\n"
      "v_mfma_f32_16x16x32_bf16 v[132:135], a[236:239], v[60:63], v[132:135]\n"
      "s_memrealtime s[84:85]\n"
      "v_mfma_f32_16x16x32_bf16 v[132:135], a[0:3], v[64:67], v[132:135]\n"
      "v_cvt_pk_bf16_f32 v214, v136, v137\n"
      "v_mfma_f32_16x16x32_bf16 v[132:135], a[4:7], v[68:71], v[132:135]\n"
      "v_cvt_pk_bf16_f32 v215, v138, v139\n"
      "v_mfma_f32_16x16x32_bf16 v[132:135], a[8:11], v[84:87], v[132:135]\n"
      "v_mfma_f32_16x16x32_bf16 v[132:135], a[12:15], v[88:91], v[132:135]\n"
      "v_mfma_f32_16x16x32_bf16 v[132:135], a[16:19], v[92:95], v[132:135]\n"
      "v_mfma_f32_16x16x32_bf16 v[132:135], a[20:23], v[108:111], v[132:135]\n"
      "v_mfma_f32_16x16x32_bf16 v[132:135], v[140:143], v[112:115], v[132:135]\n"
      "s_waitcnt lgkmcnt(0)\n"
      "s_sub_u32 s48, s84, s48\n"
      "v_mfma_f32_16x16x32_bf16 v[132:135], v[156:159], v[116:119], v[132:135]\n"
      "s_barrier\n"
      "ds_write_b128 v245, v[212:215] offset:12288\n"
      "v_mfma_f32_16x16x32_bf16 v[136:139], v[160:163], v[36:39], v[20:23]\n"
      "ds_read_b128 v[36:39], v232 offset:24576\n"
      "s_lshl_b32 s79, s91, 1\n"
      "v_mfma_f32_16x16x32_bf16 v[136:139], v[164:167], v[40:43], v[136:139]\n"
      "ds_read_b128 v[40:43], v233 offset:24576\n"
      "v_mad_i32_i24 v252, v251, s79, v252\n"
      "v_mfma_f32_16x16x32_bf16 v[136:139], v[180:183], v[44:47], v[136:139]\n"
      "ds_read_b128 v[44:47], v234 offset:24576\n"
      "s_mul_i32 s79, s79, s100\n"
      "v_mfma_f32_16x16x32_bf16 v[136:139], v[184:187], v[60:63], v[136:139]\n"
      "ds_read_b128 v[60:63], v235 offset:24576\n"
      "v_cvt_pk_bf16_f32 v208, v132, v133\n"
      "v_mfma_f32_16x16x32_bf16 v[136:139], v[188:191], v[64:67], v[136:139]\n"
      "ds_read_b128 v[64:67], v232 offset:32768\n"
      "v_cvt_pk_bf16_f32 v209, v134, v135\n"
      "v_mfma_f32_16x16x32_bf16 v[136:139], v[204:207], v[68:71], v[136:139]\n"
      "ds_read_b128 v[68:71], v233 offset:32768\n"
      "v_add_u32 v247, s79, v247\n"
      "v_mfma_f32_16x16x32_bf16 v[136:139], a[24:27], v[84:87], v[136:139]\n"
      "ds_read_b128 v[84:87], v234 offset:32768\n"
      "s_mul_i32 s79, s79, s101\n"
      "v_mfma_f32_16x16x32_bf16 v[136:139], a[28:31], v[88:91], v[136:139]\n"
      "ds_read_b128 v[88:91], v235 offset:32768\n"
      "v_add_u32 v253, s79, v253\n"
      "v_mfma_f32_16x16x32_bf16 v[136:139], a[32:35], v[92:95], v[136:139]\n"
      "ds_read_b128 v[92:95], v232 offset:40960\n"
      "v_mfma_f32_16x16x32_bf16 v[136:139], a[36:39], v[108:111], v[136:139]\n"
      "ds_read_b128 v[108:111], v233 offset:40960\n"
      "v_mfma_f32_16x16x32_bf16 v[136:139], a[40:43], v[112:115], v[136:139]\n"
      "ds_read_b128 v[112:115], v234 offset:40960\n"
      "v_mfma_f32_16x16x32_bf16 v[136:139], a[44:47], v[116:119], v[136:139]\n"
      "ds_read_b128 v[116:119], v235 offset:40960\n"
      "s_waitcnt lgkmcnt(8)\n"
      "v_mfma_f32_16x16x32_bf16 v[132:135], a[240:243], v[36:39], v[0:3]\n"
      "v_mfma_f32_16x16x32_bf16 v[132:135], a[244:247], v[40:43], v[132:135]\n"
      "s_add_u32 s54, s30, s34\n"
      "s_addc_u32 s55, s31, 0\n"
      "v_mfma_f32_16x16x32_bf16 v[132:135], a[248:251], v[44:47], v[132:135]\n"
      "s_mul_i32 s48, s48, 853\n"
      "v_mfma_f32_16x16x32_bf16 v[132:135], a[252:255], v[60:63], v[132:135]\n"
      "s_lshr_b32 s48, s48, 8\n"
      "v_cvt_pk_bf16_f32 v210, v136, v137\n"
      "s_waitcnt lgkmcnt(4)\n"
      "v_mfma_f32_16x16x32_bf16 v[132:135], a[48:51], v[64:67], v[132:135]\n"
      "s_add_u32 s48, s48, s84\n"
      "v_cvt_pk_bf16_f32 v211, v138, v139\n"
      "v_mfma_f32_16x16x32_bf16 v[132:135], a[52:55], v[68:71], v[132:135]\n"
      "s_xor_b32 s49, s48, s80\n"
      "s_mul_i32 s78, s93, s19\n"
      "v_mfma_f32_16x16x32_bf16 v[132:135], a[56:59], v[84:87], v[132:135]\n"
      "v_mov_b32 v230, s49\n"
      "v_add_u32 v236, s78, v236\n"
      "v_mfma_f32_16x16x32_bf16 v[132:135], a[60:63], v[88:91], v[132:135]\n"
      "s_mul_i32 s49, s51, 0x1100\n"
      "s_mul_i32 s78, s78, s99\n"
      "s_waitcnt lgkmcnt(0)\n"
      "v_mfma_f32_16x16x32_bf16 v[132:135], a[64:67], v[92:95], v[132:135]\n"
      "s_and_b32 s49, s49, 0xf000\n"
      "v_add_u32 v237, s78, v237\n"
      "v_mfma_f32_16x16x32_bf16 v[132:135], a[68:71], v[108:111], v[132:135]\n"
      "s_lshr_b32 s49, s49, 0\n"
      "v_mfma_f32_16x16x32_bf16 v[132:135], a[72:75], v[112:115], v[132:135]\n"
      "s_add_u32 s49, s49, 1114112\n"
      "v_mfma_f32_16x16x32_bf16 v[132:135], a[76:79], v[116:119], v[132:135]\n"
      "v_mov_b32 v229, s49\n"
      "v_mfma_f32_16x16x32_bf16 v[136:139], a[80:83], v[36:39], v[4:7]\n"
      "s_and_b32 s49, s51, 0x78\n"
      "v_mfma_f32_16x16x32_bf16 v[136:139], a[84:87], v[40:43], v[136:139]\n"
      "s_xor_b32 s49, s49, 0x40\n"
      "v_mfma_f32_16x16x32_bf16 v[136:139], a[88:91], v[44:47], v[136:139]\n"
      "s_or_b32 s49, s49, s76\n"
      "v_mfma_f32_16x16x32_bf16 v[136:139], a[92:95], v[60:63], v[136:139]\n"
      "ds_write_b128 v244, v[208:211] offset:12416\n"
      "v_mfma_f32_16x16x32_bf16 v[136:139], a[96:99], v[64:67], v[136:139]\n"
      "s_cmp_lg_u32 s49, 0\n"
      "s_cbranch_scc0 LPS\n"
      "LPR:\n"
      "v_mfma_f32_16x16x32_bf16 v[136:139], a[100:103], v[68:71], v[136:139]\n"
      "v_cvt_pk_bf16_f32 v208, v132, v133\n"
      "v_mfma_f32_16x16x32_bf16 v[136:139], a[104:107], v[84:87], v[136:139]\n"
      "v_cvt_pk_bf16_f32 v209, v134, v135\n"
      "v_mfma_f32_16x16x32_bf16 v[136:139], a[108:111], v[88:91], v[136:139]\n"
      "ds_read_b128 v[216:219], v246 offset:0\n"
      "v_mfma_f32_16x16x32_bf16 v[136:139], a[112:115], v[92:95], v[136:139]\n"
      "ds_read_b128 v[220:223], v246 offset:1024\n"
      "v_mfma_f32_16x16x32_bf16 v[136:139], a[116:119], v[108:111], v[136:139]\n"
      "ds_read_b128 v[224:227], v246 offset:2048\n"
      "v_mfma_f32_16x16x32_bf16 v[136:139], a[120:123], v[112:115], v[136:139]\n"
      "s_waitcnt lgkmcnt(2)\n"
      "v_mfma_f32_16x16x32_bf16 v[136:139], a[124:127], v[116:119], v[136:139]\n"
      "global_store_dwordx4 v247, v[216:219], s[54:55] offset:0 sc1\n"
      "v_mfma_f32_16x16x32_bf16 v[132:135], a[128:131], v[36:39], v[8:11]\n"
      "s_add_u32 s52, s20, s36\n"
      "s_addc_u32 s53, s21, 0\n"
      "v_mfma_f32_16x16x32_bf16 v[132:135], a[132:135], v[40:43], v[132:135]\n"
      "s_add_u32 m0, s17, 0x0\n"
      "v_mfma_f32_16x16x32_bf16 v[132:135], a[136:139], v[44:47], v[132:135]\n"
      "global_load_lds_dwordx4 v236, s[52:53] offset:0 nt\n"
      "v_mfma_f32_16x16x32_bf16 v[132:135], a[140:143], v[60:63], v[132:135]\n"
      "v_cvt_pk_bf16_f32 v210, v136, v137\n"
      "v_mfma_f32_16x16x32_bf16 v[132:135], a[144:147], v[64:67], v[132:135]\n"
      "v_cvt_pk_bf16_f32 v211, v138, v139\n"
      "v_mfma_f32_16x16x32_bf16 v[132:135], a[148:151], v[68:71], v[132:135]\n"
      "v_mfma_f32_16x16x32_bf16 v[132:135], a[152:155], v[84:87], v[132:135]\n"
      "s_waitcnt lgkmcnt(1)\n"
      "v_mfma_f32_16x16x32_bf16 v[132:135], a[156:159], v[88:91], v[132:135]\n"
      "global_store_dwordx4 v252, v[220:223], s[54:55] offset:0 sc1\n"
      "v_mfma_f32_16x16x32_bf16 v[132:135], a[160:163], v[92:95], v[132:135]\n"
      "v_mfma_f32_16x16x32_bf16 v[132:135], a[164:167], v[108:111], v[132:135]\n"
      "ds_write_b128 v244, v[208:211] offset:24576\n"
      "v_mfma_f32_16x16x32_bf16 v[132:135], a[168:171], v[112:115], v[132:135]\n"
      "v_mfma_f32_16x16x32_bf16 v[132:135], a[172:175], v[116:119], v[132:135]\n"
      "v_mfma_f32_16x16x32_bf16 v[136:139], a[176:179], v[36:39], v[12:15]\n"
      "s_add_u32 m0, s17, 0x1f00\n"
      "v_mfma_f32_16x16x32_bf16 v[136:139], a[180:183], v[40:43], v[136:139]\n"
      "global_load_lds_dwordx4 v236, s[52:53] offset:256 nt\n"
      "v_mfma_f32_16x16x32_bf16 v[136:139], a[184:187], v[44:47], v[136:139]\n"
      "v_mfma_f32_16x16x32_bf16 v[136:139], a[188:191], v[60:63], v[136:139]\n"
      "v_cvt_pk_bf16_f32 v212, v132, v133\n"
      "v_mfma_f32_16x16x32_bf16 v[136:139], a[192:195], v[64:67], v[136:139]\n"
      "v_cvt_pk_bf16_f32 v213, v134, v135\n"
      "v_mfma_f32_16x16x32_bf16 v[136:139], a[196:199], v[68:71], v[136:139]\n"
      "v_mfma_f32_16x16x32_bf16 v[136:139], a[200:203], v[84:87], v[136:139]\n"
      "v_mfma_f32_16x16x32_bf16 v[136:139], a[204:207], v[88:91], v[136:139]\n"
      "s_waitcnt lgkmcnt(1)\n"
      "v_mfma_f32_16x16x32_bf16 v[136:139], a[208:211], v[92:95], v[136:139]\n"
      "global_store_dwordx4 v253, v[224:227], s[54:55] offset:0 sc1\n"
      "v_mfma_f32_16x16x32_bf16 v[136:139], a[212:215], v[108:111], v[136:139]\n"
      "v_mfma_f32_16x16x32_bf16 v[136:139], a[216:219], v[112:115], v[136:139]\n"
      "v_mfma_f32_16x16x32_bf16 v[136:139], a[220:223], v[116:119], v[136:139]\n"
      "s_add_u32 m0, s17, 0x3e00\n"
      "v_mfma_f32_16x16x32_bf16 v[132:135], a[224:227], v[36:39], v[16:19]\n"
      "global_load_lds_dwordx4 v236, s[52:53] offset:512 nt\n"
      "v_mfma_f32_16x16x32_bf16 v[132:135], a[228:231], v[40:43], v[132:135]\n"
      "v_mfma_f32_16x16x32_bf16 v[132:135], a[232:235], v[44:47], v[132:135]\n"
      "v_mfma_f32_16x16x32_bf16 v[132:135], a[236:239], v[60:63], v[132:135]\n"
      "v_cvt_pk_bf16_f32 v214, v136, v137\n"
      "v_mfma_f32_16x16x32_bf16 v[132:135], a[0:3], v[64:67], v[132:135]\n"
      "v_cvt_pk_bf16_f32 v215, v138, v139\n"
      "v_mfma_f32_16x16x32_bf16 v[132:135], a[4:7], v[68:71], v[132:135]\n"
      "v_mfma_f32_16x16x32_bf16 v[132:135], a[8:11], v[84:87], v[132:135]\n"
      "v_mfma_f32_16x16x32_bf16 v[132:135], a[12:15], v[88:91], v[132:135]\n"
      "v_mfma_f32_16x16x32_bf16 v[132:135], a[16:19], v[92:95], v[132:135]\n"
      "v_mfma_f32_16x16x32_bf16 v[132:135], a[20:23], v[108:111], v[132:135]\n"
      "ds_write_b128 v245, v[212:215] offset:24576\n"
      "v_mfma_f32_16x16x32_bf16 v[132:135], v[140:143], v[112:115], v[132:135]\n"
      "v_mfma_f32_16x16x32_bf16 v[132:135], v[156:159], v[116:119], v[132:135]\n"
      "s_barrier\n"
      "v_mfma_f32_16x16x32_bf16 v[136:139], v[160:163], v[36:39], v[20:23]\n"
      "ds_read_b128 v[36:39], v232 offset:24832\n"
      "v_mfma_f32_16x16x32_bf16 v[136:139], v[164:167], v[40:43], v[136:139]\n"
      "ds_read_b128 v[40:43], v233 offset:24832\n"
      "v_mfma_f32_16x16x32_bf16 v[136:139], v[180:183], v[44:47], v[136:139]\n"
      "ds_read_b128 v[44:47], v234 offset:24832\n"
      "v_mfma_f32_16x16x32_bf16 v[136:139], v[184:187], v[60:63], v[136:139]\n"
      "ds_read_b128 v[60:63], v235 offset:24832\n"
      "v_cvt_pk_bf16_f32 v208, v132, v133\n"
      "v_mfma_f32_16x16x32_bf16 v[136:139], v[188:191], v[64:67], v[136:139]\n"
      "ds_read_b128 v[64:67], v232 offset:33024\n"
      "v_cvt_pk_bf16_f32 v209, v134, v135\n"
      "v_mfma_f32_16x16x32_bf16 v[136:139], v[204:207], v[68:71], v[136:139]\n"
      "ds_read_b128 v[68:71], v233 offset:33024\n"
      "v_mfma_f32_16x16x32_bf16 v[136:139], a[24:27], v[84:87], v[136:139]\n"
      "ds_read_b128 v[84:87], v234 offset:33024\n"
      "v_mfma_f32_16x16x32_bf16 v[136:139], a[28:31], v[88:91], v[136:139]\n"
      "ds_read_b128 v[88:91], v235 offset:33024\n"
      "v_mfma_f32_16x16x32_bf16 v[136:139], a[32:35], v[92:95], v[136:139]\n"
      "ds_read_b128 v[92:95], v232 offset:41216\n"
      "v_mfma_f32_16x16x32_bf16 v[136:139], a[36:39], v[108:111], v[136:139]\n"
      "ds_read_b128 v[108:111], v233 offset:41216\n"
      "v_mfma_f32_16x16x32_bf16 v[136:139], a[40:43], v[112:115], v[136:139]\n"
      "ds_read_b128 v[112:115], v234 offset:41216\n"
      "v_mfma_f32_16x16x32_bf16 v[136:139], a[44:47], v[116:119], v[136:139]\n"
      "ds_read_b128 v[116:119], v235 offset:41216\n"
      "s_waitcnt lgkmcnt(8)\n"
      "v_mfma_f32_16x16x32_bf16 v[132:135], a[240:243], v[36:39], v[0:3]\n"
      "v_add_u32 v232, 0xc000, v232\n"
      "v_mfma_f32_16x16x32_bf16 v[132:135], a[244:247], v[40:43], v[132:135]\n"
      "v_add_u32 v233, 0xc000, v233\n"
      "v_mfma_f32_16x16x32_bf16 v[132:135], a[248:251], v[44:47], v[132:135]\n"
      "v_add_u32 v234, 0xc000, v234\n"
      "v_mfma_f32_16x16x32_bf16 v[132:135], a[252:255], v[60:63], v[132:135]\n"
      "v_add_u32 v235, 0xc000, v235\n"
      "s_waitcnt lgkmcnt(4)\n"
      "v_mfma_f32_16x16x32_bf16 v[132:135], a[48:51], v[64:67], v[132:135]\n"
      "v_cvt_pk_bf16_f32 v210, v136, v137\n"
      "v_mfma_f32_16x16x32_bf16 v[132:135], a[52:55], v[68:71], v[132:135]\n"
      "v_cvt_pk_bf16_f32 v211, v138, v139\n"
      "v_mfma_f32_16x16x32_bf16 v[132:135], a[56:59], v[84:87], v[132:135]\n"
      "ds_read_b128 v[216:219], v246 offset:12288\n"
      "v_mfma_f32_16x16x32_bf16 v[132:135], a[60:63], v[88:91], v[132:135]\n"
      "ds_read_b128 v[220:223], v246 offset:13312\n"
      "s_waitcnt lgkmcnt(2)\n"
      "v_mfma_f32_16x16x32_bf16 v[132:135], a[64:67], v[92:95], v[132:135]\n"
      "ds_read_b128 v[224:227], v246 offset:14336\n"
      "v_mfma_f32_16x16x32_bf16 v[132:135], a[68:71], v[108:111], v[132:135]\n"
      "v_lshlrev_b32 v0, 16, v72\n"
      "v_mfma_f32_16x16x32_bf16 v[132:135], a[72:75], v[112:115], v[132:135]\n"
      "ds_write_b128 v244, v[208:211] offset:24704\n"
      "v_mfma_f32_16x16x32_bf16 v[132:135], a[76:79], v[116:119], v[132:135]\n"
      "v_and_b32 v1, 0xffff0000, v72\n"
      "v_mfma_f32_16x16x32_bf16 v[136:139], a[80:83], v[36:39], v[4:7]\n"
      "v_lshlrev_b32 v2, 16, v73\n"
      "v_mfma_f32_16x16x32_bf16 v[136:139], a[84:87], v[40:43], v[136:139]\n"
      "v_and_b32 v3, 0xffff0000, v73\n"
      "v_mfma_f32_16x16x32_bf16 v[136:139], a[88:91], v[44:47], v[136:139]\n"
      "LPD10:\n"
      "s_cbranch_execz LPD13\n"
      "v_mfma_f32_16x16x32_bf16 v[136:139], a[92:95], v[60:63], v[136:139]\n"
      "v_cvt_pk_bf16_f32 v208, v132, v133\n"
      "v_mfma_f32_16x16x32_bf16 v[136:139], a[96:99], v[64:67], v[136:139]\n"
      "v_cvt_pk_bf16_f32 v209, v134, v135\n"
      "v_mfma_f32_16x16x32_bf16 v[136:139], a[100:103], v[68:71], v[136:139]\n"
      "s_waitcnt lgkmcnt(3)\n"
      "v_mfma_f32_16x16x32_bf16 v[136:139], a[104:107], v[84:87], v[136:139]\n"
      "global_store_dwordx4 v247, v[216:219], s[54:55] offset:768 sc1\n"
      "v_mfma_f32_16x16x32_bf16 v[136:139], a[108:111], v[88:91], v[136:139]\n"
      "s_add_u32 m0, s17, 0x1000\n"
      "v_mfma_f32_16x16x32_bf16 v[136:139], a[112:115], v[92:95], v[136:139]\n"
      "global_load_lds_dwordx4 v237, s[52:53] offset:0 nt\n"
      "v_mfma_f32_16x16x32_bf16 v[136:139], a[116:119], v[108:111], v[136:139]\n"
      "v_lshlrev_b32 v4, 16, v74\n"
      "v_mfma_f32_16x16x32_bf16 v[136:139], a[120:123], v[112:115], v[136:139]\n"
      "v_and_b32 v5, 0xffff0000, v74\n"
      "v_mfma_f32_16x16x32_bf16 v[136:139], a[124:127], v[116:119], v[136:139]\n"
      "v_lshlrev_b32 v6, 16, v75\n"
      "v_mfma_f32_16x16x32_bf16 v[132:135], a[128:131], v[36:39], v[8:11]\n"
      "v_and_b32 v7, 0xffff0000, v75\n"
      "v_mfma_f32_16x16x32_bf16 v[132:135], a[132:135], v[40:43], v[132:135]\n"
      "v_mfma_f32_16x16x32_bf16 v[132:135], a[136:139], v[44:47], v[132:135]\n"
      "v_mfma_f32_16x16x32_bf16 v[132:135], a[140:143], v[60:63], v[132:135]\n"
      "v_cvt_pk_bf16_f32 v210, v136, v137\n"
      "v_mfma_f32_16x16x32_bf16 v[132:135], a[144:147], v[64:67], v[132:135]\n"
      "v_cvt_pk_bf16_f32 v211, v138, v139\n"
      "v_mfma_f32_16x16x32_bf16 v[132:135], a[148:151], v[68:71], v[132:135]\n"
      "v_lshlrev_b32 v8, 16, v76\n"
      "v_mfma_f32_16x16x32_bf16 v[132:135], a[152:155], v[84:87], v[132:135]\n"
      "s_waitcnt lgkmcnt(2)\n"
      "v_mfma_f32_16x16x32_bf16 v[132:135], a[156:159], v[88:91], v[132:135]\n"
      "global_store_dwordx4 v252, v[220:223], s[54:55] offset:768 sc1\n"
      "v_mfma_f32_16x16x32_bf16 v[132:135], a[160:163], v[92:95], v[132:135]\n"
      "s_add_u32 m0, s17, 0x2f00\n"
      "v_mfma_f32_16x16x32_bf16 v[132:135], a[164:167], v[108:111], v[132:135]\n"
      "ds_write_b128 v244, v[208:211] offset:0\n"
      "v_mfma_f32_16x16x32_bf16 v[132:135], a[168:171], v[112:115], v[132:135]\n"
      "global_load_lds_dwordx4 v237, s[52:53] offset:256 nt\n"
      "v_mfma_f32_16x16x32_bf16 v[132:135], a[172:175], v[116:119], v[132:135]\n"
      "v_and_b32 v9, 0xffff0000, v76\n"
      "v_mfma_f32_16x16x32_bf16 v[136:139], a[176:179], v[36:39], v[12:15]\n"
      "v_lshlrev_b32 v10, 16, v77\n"
      "v_mfma_f32_16x16x32_bf16 v[136:139], a[180:183], v[40:43], v[136:139]\n"
      "v_and_b32 v11, 0xffff0000, v77\n"
      "v_mfma_f32_16x16x32_bf16 v[136:139], a[184:187], v[44:47], v[136:139]\n"
      "v_mfma_f32_16x16x32_bf16 v[136:139], a[188:191], v[60:63], v[136:139]\n"
      "v_cvt_pk_bf16_f32 v212, v132, v133\n"
      "v_mfma_f32_16x16x32_bf16 v[136:139], a[192:195], v[64:67], v[136:139]\n"
      "v_cvt_pk_bf16_f32 v213, v134, v135\n"
      "v_mfma_f32_16x16x32_bf16 v[136:139], a[196:199], v[68:71], v[136:139]\n"
      "v_lshlrev_b32 v12, 16, v78\n"
      "v_mfma_f32_16x16x32_bf16 v[136:139], a[200:203], v[84:87], v[136:139]\n"
      "v_and_b32 v13, 0xffff0000, v78\n"
      "v_mfma_f32_16x16x32_bf16 v[136:139], a[204:207], v[88:91], v[136:139]\n"
      "s_waitcnt lgkmcnt(2)\n"
      "v_mfma_f32_16x16x32_bf16 v[136:139], a[208:211], v[92:95], v[136:139]\n"
      "global_store_dwordx4 v253, v[224:227], s[54:55] offset:768 sc1\n"
      "v_mfma_f32_16x16x32_bf16 v[136:139], a[212:215], v[108:111], v[136:139]\n"
      "s_add_u32 m0, s17, 0x4e00\n"
      "v_mfma_f32_16x16x32_bf16 v[136:139], a[216:219], v[112:115], v[136:139]\n"
      "global_load_lds_dwordx4 v237, s[52:53] offset:512 nt\n"
      "v_mfma_f32_16x16x32_bf16 v[136:139], a[220:223], v[116:119], v[136:139]\n"
      "v_lshlrev_b32 v14, 16, v79\n"
      "v_mfma_f32_16x16x32_bf16 v[132:135], a[224:227], v[36:39], v[16:19]\n"
      "v_and_b32 v15, 0xffff0000, v79\n"
      "v_mfma_f32_16x16x32_bf16 v[132:135], a[228:231], v[40:43], v[132:135]\n"
      "v_mfma_f32_16x16x32_bf16 v[132:135], a[232:235], v[44:47], v[132:135]\n"
      "v_mfma_f32_16x16x32_bf16 v[132:135], a[236:239], v[60:63], v[132:135]\n"
      "v_cvt_pk_bf16_f32 v214, v136, v137\n"
      "v_mfma_f32_16x16x32_bf16 v[132:135], a[0:3], v[64:67], v[132:135]\n"
      "v_cvt_pk_bf16_f32 v215, v138, v139\n"
      "v_mfma_f32_16x16x32_bf16 v[132:135], a[4:7], v[68:71], v[132:135]\n"
      "v_lshlrev_b32 v16, 16, v80\n"
      "v_mfma_f32_16x16x32_bf16 v[132:135], a[8:11], v[84:87], v[132:135]\n"
      "v_and_b32 v17, 0xffff0000, v80\n"
      "v_mfma_f32_16x16x32_bf16 v[132:135], a[12:15], v[88:91], v[132:135]\n"
      "v_lshlrev_b32 v18, 16, v81\n"
      "v_mfma_f32_16x16x32_bf16 v[132:135], a[16:19], v[92:95], v[132:135]\n"
      "v_and_b32 v19, 0xffff0000, v81\n"
      "v_mfma_f32_16x16x32_bf16 v[132:135], a[20:23], v[108:111], v[132:135]\n"
      "ds_write_b128 v245, v[212:215] offset:0\n"
      "v_mfma_f32_16x16x32_bf16 v[132:135], v[140:143], v[112:115], v[132:135]\n"
      "v_mfma_f32_16x16x32_bf16 v[132:135], v[156:159], v[116:119], v[132:135]\n"
      "s_waitcnt vmcnt(24) lgkmcnt(2)\n"
      "s_barrier\n"
      "v_mfma_f32_16x16x32_bf16 v[136:139], v[160:163], v[36:39], v[20:23]\n"
      "ds_read_b128 v[36:39], v232 offset:0\n"
      "v_mfma_f32_16x16x32_bf16 v[136:139], v[164:167], v[40:43], v[136:139]\n"
      "ds_read_b128 v[40:43], v233 offset:0\n"
      "v_mfma_f32_16x16x32_bf16 v[136:139], v[180:183], v[44:47], v[136:139]\n"
      "ds_read_b128 v[44:47], v234 offset:0\n"
      "v_mfma_f32_16x16x32_bf16 v[136:139], v[184:187], v[60:63], v[136:139]\n"
      "ds_read_b128 v[60:63], v235 offset:0\n"
      "v_cvt_pk_bf16_f32 v208, v132, v133\n"
      "v_mfma_f32_16x16x32_bf16 v[136:139], v[188:191], v[64:67], v[136:139]\n"
      "ds_read_b128 v[64:67], v232 offset:8192\n"
      "v_cvt_pk_bf16_f32 v209, v134, v135\n"
      "v_mfma_f32_16x16x32_bf16 v[136:139], v[204:207], v[68:71], v[136:139]\n"
      "ds_read_b128 v[68:71], v233 offset:8192\n"
      "v_mfma_f32_16x16x32_bf16 v[136:139], a[24:27], v[84:87], v[136:139]\n"
      "ds_read_b128 v[84:87], v234 offset:8192\n"
      "v_mfma_f32_16x16x32_bf16 v[136:139], a[28:31], v[88:91], v[136:139]\n"
      "ds_read_b128 v[88:91], v235 offset:8192\n"
      "v_mfma_f32_16x16x32_bf16 v[136:139], a[32:35], v[92:95], v[136:139]\n"
      "ds_read_b128 v[92:95], v232 offset:16384\n"
      "v_mfma_f32_16x16x32_bf16 v[136:139], a[36:39], v[108:111], v[136:139]\n"
      "ds_read_b128 v[108:111], v233 offset:16384\n"
      "v_mfma_f32_16x16x32_bf16 v[136:139], a[40:43], v[112:115], v[136:139]\n"
      "ds_read_b128 v[112:115], v234 offset:16384\n"
      "v_mfma_f32_16x16x32_bf16 v[136:139], a[44:47], v[116:119], v[136:139]\n"
      "ds_read_b128 v[116:119], v235 offset:16384\n"
      "s_waitcnt lgkmcnt(8)\n"
      "v_mfma_f32_16x16x32_bf16 v[132:135], a[240:243], v[36:39], v[0:3]\n"
      "v_lshlrev_b32 v20, 16, v82\n"
      "v_mfma_f32_16x16x32_bf16 v[132:135], a[244:247], v[40:43], v[132:135]\n"
      "v_and_b32 v21, 0xffff0000, v82\n"
      "v_mfma_f32_16x16x32_bf16 v[132:135], a[248:251], v[44:47], v[132:135]\n"
      "v_lshlrev_b32 v22, 16, v83\n"
      "v_mfma_f32_16x16x32_bf16 v[132:135], a[252:255], v[60:63], v[132:135]\n"
      "v_and_b32 v23, 0xffff0000, v83\n"
      "s_waitcnt lgkmcnt(4)\n"
      "v_mfma_f32_16x16x32_bf16 v[132:135], a[48:51], v[64:67], v[132:135]\n"
      "v_cvt_pk_bf16_f32 v210, v136, v137\n"
      "v_mfma_f32_16x16x32_bf16 v[132:135], a[52:55], v[68:71], v[132:135]\n"
      "v_cvt_pk_bf16_f32 v211, v138, v139\n"
      "v_mfma_f32_16x16x32_bf16 v[132:135], a[56:59], v[84:87], v[132:135]\n"
      "s_add_u32 s54, s44, s34\n"
      "s_addc_u32 s55, s45, 0\n"
      "v_mfma_f32_16x16x32_bf16 v[132:135], a[60:63], v[88:91], v[132:135]\n"
      "ds_read_b128 v[216:219], v246 offset:24576\n"
      "s_waitcnt lgkmcnt(1)\n"
      "v_mfma_f32_16x16x32_bf16 v[132:135], a[64:67], v[92:95], v[132:135]\n"
      "ds_read_b128 v[220:223], v246 offset:25600\n"
      "v_mfma_f32_16x16x32_bf16 v[132:135], a[68:71], v[108:111], v[132:135]\n"
      "ds_read_b128 v[224:227], v246 offset:26624\n"
      "v_mfma_f32_16x16x32_bf16 v[132:135], a[72:75], v[112:115], v[132:135]\n"
      "ds_write_b128 v244, v[208:211] offset:128\n"
      "v_mfma_f32_16x16x32_bf16 v[132:135], a[76:79], v[116:119], v[132:135]\n"
      "v_mfma_f32_16x16x32_bf16 v[136:139], a[80:83], v[36:39], v[4:7]\n"
      "v_mfma_f32_16x16x32_bf16 v[136:139], a[84:87], v[40:43], v[136:139]\n"
      "v_mfma_f32_16x16x32_bf16 v[136:139], a[88:91], v[44:47], v[136:139]\n"
      "v_mfma_f32_16x16x32_bf16 v[136:139], a[92:95], v[60:63], v[136:139]\n"
      "v_cvt_pk_bf16_f32 v208, v132, v133\n"
      "v_mfma_f32_16x16x32_bf16 v[136:139], a[96:99], v[64:67], v[136:139]\n"
      "v_cvt_pk_bf16_f32 v209, v134, v135\n"
      "v_mfma_f32_16x16x32_bf16 v[136:139], a[100:103], v[68:71], v[136:139]\n"
      "s_waitcnt lgkmcnt(3)\n"
      "v_mfma_f32_16x16x32_bf16 v[136:139], a[104:107], v[84:87], v[136:139]\n"
      "global_store_dwordx4 v247, v[216:219], s[54:55] offset:0 sc1\n"
      "v_mfma_f32_16x16x32_bf16 v[136:139], a[108:111], v[88:91], v[136:139]\n"
      "s_add_u32 s52, s42, s36\n"
      "s_addc_u32 s53, s43, 0\n"
      "v_mfma_f32_16x16x32_bf16 v[136:139], a[112:115], v[92:95], v[136:139]\n"
      "s_add_u32 m0, s17, 0x6000\n"
      "v_mfma_f32_16x16x32_bf16 v[136:139], a[116:119], v[108:111], v[136:139]\n"
      "global_load_lds_dwordx4 v236, s[52:53] offset:0 nt\n"
      "v_mfma_f32_16x16x32_bf16 v[136:139], a[120:123], v[112:115], v[136:139]\n"
      "v_mfma_f32_16x16x32_bf16 v[136:139], a[124:127], v[116:119], v[136:139]\n"
      "v_mfma_f32_16x16x32_bf16 v[132:135], a[128:131], v[36:39], v[8:11]\n"
      "v_mfma_f32_16x16x32_bf16 v[132:135], a[132:135], v[40:43], v[132:135]\n"
      "v_mfma_f32_16x16x32_bf16 v[132:135], a[136:139], v[44:47], v[132:135]\n"
      "v_mfma_f32_16x16x32_bf16 v[132:135], a[140:143], v[60:63], v[132:135]\n"
      "v_cvt_pk_bf16_f32 v210, v136, v137\n"
      "v_mfma_f32_16x16x32_bf16 v[132:135], a[144:147], v[64:67], v[132:135]\n"
      "v_cvt_pk_bf16_f32 v211, v138, v139\n"
      "v_mfma_f32_16x16x32_bf16 v[132:135], a[148:151], v[68:71], v[132:135]\n"
      "v_mfma_f32_16x16x32_bf16 v[132:135], a[152:155], v[84:87], v[132:135]\n"
      "s_waitcnt lgkmcnt(2)\n"
      "v_mfma_f32_16x16x32_bf16 v[132:135], a[156:159], v[88:91], v[132:135]\n"
      "global_store_dwordx4 v252, v[220:223], s[54:55] offset:0 sc1\n"
      "v_mfma_f32_16x16x32_bf16 v[132:135], a[160:163], v[92:95], v[132:135]\n"
      "s_add_u32 m0, s17, 0x7f00\n"
      "v_mfma_f32_16x16x32_bf16 v[132:135], a[164:167], v[108:111], v[132:135]\n"
      "ds_write_b128 v244, v[208:211] offset:12288\n"
      "v_mfma_f32_16x16x32_bf16 v[132:135], a[168:171], v[112:115], v[132:135]\n"
      "global_load_lds_dwordx4 v236, s[52:53] offset:256 nt\n"
      "v_mfma_f32_16x16x32_bf16 v[132:135], a[172:175], v[116:119], v[132:135]\n"
      "v_mfma_f32_16x16x32_bf16 v[136:139], a[176:179], v[36:39], v[12:15]\n"
      "v_mfma_f32_16x16x32_bf16 v[136:139], a[180:183], v[40:43], v[136:139]\n"
      "v_mfma_f32_16x16x32_bf16 v[136:139], a[184:187], v[44:47], v[136:139]\n"
      "v_mfma_f32_16x16x32_bf16 v[136:139], a[188:191], v[60:63], v[136:139]\n"
      "v_cvt_pk_bf16_f32 v212, v132, v133\n"
      "v_mfma_f32_16x16x32_bf16 v[136:139], a[192:195], v[64:67], v[136:139]\n"
      "v_cvt_pk_bf16_f32 v213, v134, v135\n"
      "v_mfma_f32_16x16x32_bf16 v[136:139], a[196:199], v[68:71], v[136:139]\n"
      "v_mfma_f32_16x16x32_bf16 v[136:139], a[200:203], v[84:87], v[136:139]\n"
      "v_mfma_f32_16x16x32_bf16 v[136:139], a[204:207], v[88:91], v[136:139]\n"
      "s_waitcnt lgkmcnt(2)\n"
      "v_mfma_f32_16x16x32_bf16 v[136:139], a[208:211], v[92:95], v[136:139]\n"
      "global_store_dwordx4 v253, v[224:227], s[54:55] offset:0 sc1\n"
      "v_mfma_f32_16x16x32_bf16 v[136:139], a[212:215], v[108:111], v[136:139]\n"
      "s_add_u32 m0, s17, 0x9e00\n"
      "v_mfma_f32_16x16x32_bf16 v[136:139], a[216:219], v[112:115], v[136:139]\n"
      "global_load_lds_dwordx4 v236, s[52:53] offset:512 nt\n"
      "v_mfma_f32_16x16x32_bf16 v[136:139], a[220:223], v[116:119], v[136:139]\n"
      "v_mfma_f32_16x16x32_bf16 v[132:135], a[224:227], v[36:39], v[16:19]\n"
      "v_mfma_f32_16x16x32_bf16 v[132:135], a[228:231], v[40:43], v[132:135]\n"
      "v_mfma_f32_16x16x32_bf16 v[132:135], a[232:235], v[44:47], v[132:135]\n"
      "v_mfma_f32_16x16x32_bf16 v[132:135], a[236:239], v[60:63], v[132:135]\n"
      "v_cvt_pk_bf16_f32 v214, v136, v137\n"
      "v_mfma_f32_16x16x32_bf16 v[132:135], a[0:3], v[64:67], v[132:135]\n"
      "v_cvt_pk_bf16_f32 v215, v138, v139\n"
      "v_mfma_f32_16x16x32_bf16 v[132:135], a[4:7], v[68:71], v[132:135]\n"
      "v_mfma_f32_16x16x32_bf16 v[132:135], a[8:11], v[84:87], v[132:135]\n"
      "v_mfma_f32_16x16x32_bf16 v[132:135], a[12:15], v[88:91], v[132:135]\n"
      "v_mfma_f32_16x16x32_bf16 v[132:135], a[16:19], v[92:95], v[132:135]\n"
      "v_mfma_f32_16x16x32_bf16 v[132:135], a[20:23], v[108:111], v[132:135]\n"
      "ds_write_b128 v245, v[212:215] offset:12288\n"
      "v_mfma_f32_16x16x32_bf16 v[132:135], v[140:143], v[112:115], v[132:135]\n"
      "v_mfma_f32_16x16x32_bf16 v[132:135], v[156:159], v[116:119], v[132:135]\n"
      "s_waitcnt lgkmcnt(2)\n"
      "s_barrier\n"
      "v_mfma_f32_16x16x32_bf16 v[136:139], v[160:163], v[36:39], v[20:23]\n"
      "ds_read_b128 v[36:39], v232 offset:256\n"
      "v_mfma_f32_16x16x32_bf16 v[136:139], v[164:167], v[40:43], v[136:139]\n"
      "ds_read_b128 v[40:43], v233 offset:256\n"
      "v_mfma_f32_16x16x32_bf16 v[136:139], v[180:183], v[44:47], v[136:139]\n"
      "ds_read_b128 v[44:47], v234 offset:256\n"
      "v_mfma_f32_16x16x32_bf16 v[136:139], v[184:187], v[60:63], v[136:139]\n"
      "ds_read_b128 v[60:63], v235 offset:256\n"
      "v_cvt_pk_bf16_f32 v208, v132, v133\n"
      "v_mfma_f32_16x16x32_bf16 v[136:139], v[188:191], v[64:67], v[136:139]\n"
      "ds_read_b128 v[64:67], v232 offset:8448\n"
      "v_cvt_pk_bf16_f32 v209, v134, v135\n"
      "v_mfma_f32_16x16x32_bf16 v[136:139], v[204:207], v[68:71], v[136:139]\n"
      "ds_read_b128 v[68:71], v233 offset:8448\n"
      "v_mfma_f32_16x16x32_bf16 v[136:139], a[24:27], v[84:87], v[136:139]\n"
      "ds_read_b128 v[84:87], v234 offset:8448\n"
      "v_mfma_f32_16x16x32_bf16 v[136:139], a[28:31], v[88:91], v[136:139]\n"
      "ds_read_b128 v[88:91], v235 offset:8448\n"
      "v_mfma_f32_16x16x32_bf16 v[136:139], a[32:35], v[92:95], v[136:139]\n"
      "ds_read_b128 v[92:95], v232 offset:16640\n"
      "v_mfma_f32_16x16x32_bf16 v[136:139], a[36:39], v[108:111], v[136:139]\n"
      "ds_read_b128 v[108:111], v233 offset:16640\n"
      "v_mfma_f32_16x16x32_bf16 v[136:139], a[40:43], v[112:115], v[136:139]\n"
      "ds_read_b128 v[112:115], v234 offset:16640\n"
      "v_mfma_f32_16x16x32_bf16 v[136:139], a[44:47], v[116:119], v[136:139]\n"
      "ds_read_b128 v[116:119], v235 offset:16640\n"
      "s_waitcnt lgkmcnt(8)\n"
      "v_mfma_f32_16x16x32_bf16 v[132:135], a[240:243], v[36:39], v[0:3]\n"
      "v_mfma_f32_16x16x32_bf16 v[132:135], a[244:247], v[40:43], v[132:135]\n"
      "v_mfma_f32_16x16x32_bf16 v[132:135], a[248:251], v[44:47], v[132:135]\n"
      "v_mfma_f32_16x16x32_bf16 v[132:135], a[252:255], v[60:63], v[132:135]\n"
      "v_cvt_pk_bf16_f32 v210, v136, v137\n"
      "s_waitcnt lgkmcnt(4)\n"
      "v_mfma_f32_16x16x32_bf16 v[132:135], a[48:51], v[64:67], v[132:135]\n"
      "v_lshlrev_b32 v229, 8, v239\n"
      "v_mfma_f32_16x16x32_bf16 v[132:135], a[52:55], v[68:71], v[132:135]\n"
      "v_add_u32 v229, 1114112, v229\n"
      "v_mfma_f32_16x16x32_bf16 v[132:135], a[56:59], v[84:87], v[132:135]\n"
      "v_cvt_pk_bf16_f32 v211, v138, v139\n"
      "v_mfma_f32_16x16x32_bf16 v[132:135], a[60:63], v[88:91], v[132:135]\n"
      "s_mov_b64 exec, 0xffff\n"
      "global_load_dword v228, v229, s[28:29] sc1\n"
      "s_mov_b64 exec, -1\n"
      "s_nop 1\n"
      "s_waitcnt lgkmcnt(0)\n"
      "v_mfma_f32_16x16x32_bf16 v[132:135], a[64:67], v[92:95], v[132:135]\n"
      "ds_read_b128 v[216:219], v246 offset:0\n"
      "v_mfma_f32_16x16x32_bf16 v[132:135], a[68:71], v[108:111], v[132:135]\n"
      "ds_read_b128 v[220:223], v246 offset:1024\n"
      "v_mfma_f32_16x16x32_bf16 v[132:135], a[72:75], v[112:115], v[132:135]\n"
      "ds_read_b128 v[224:227], v246 offset:2048\n"
      "v_mfma_f32_16x16x32_bf16 v[132:135], a[76:79], v[116:119], v[132:135]\n"
      "ds_write_b128 v244, v[208:211] offset:12416\n"
      "v_mfma_f32_16x16x32_bf16 v[136:139], a[80:83], v[36:39], v[4:7]\n"
      "v_mfma_f32_16x16x32_bf16 v[136:139], a[84:87], v[40:43], v[136:139]\n"
      "v_mfma_f32_16x16x32_bf16 v[136:139], a[88:91], v[44:47], v[136:139]\n"
      "v_mfma_f32_16x16x32_bf16 v[136:139], a[92:95], v[60:63], v[136:139]\n"
      "v_cvt_pk_bf16_f32 v208, v132, v133\n"
      "v_mfma_f32_16x16x32_bf16 v[136:139], a[96:99], v[64:67], v[136:139]\n"
      "v_cvt_pk_bf16_f32 v209, v134, v135\n"
      "v_mfma_f32_16x16x32_bf16 v[136:139], a[100:103], v[68:71], v[136:139]\n"
      "s_waitcnt lgkmcnt(3)\n"
      "v_mfma_f32_16x16x32_bf16 v[136:139], a[104:107], v[84:87], v[136:139]\n"
      "global_store_dwordx4 v247, v[216:219], s[54:55] offset:768 sc1\n"
      "v_mfma_f32_16x16x32_bf16 v[136:139], a[108:111], v[88:91], v[136:139]\n"
      "s_add_u32 m0, s17, 0x7000\n"
      "v_mfma_f32_16x16x32_bf16 v[136:139], a[112:115], v[92:95], v[136:139]\n"
      "global_load_lds_dwordx4 v237, s[52:53] offset:0 nt\n"
      "v_mfma_f32_16x16x32_bf16 v[136:139], a[116:119], v[108:111], v[136:139]\n"
      "v_mfma_f32_16x16x32_bf16 v[136:139], a[120:123], v[112:115], v[136:139]\n"
      "v_mfma_f32_16x16x32_bf16 v[136:139], a[124:127], v[116:119], v[136:139]\n"
      "v_mfma_f32_16x16x32_bf16 v[132:135], a[128:131], v[36:39], v[8:11]\n"
      "v_mfma_f32_16x16x32_bf16 v[132:135], a[132:135], v[40:43], v[132:135]\n"
      "v_mfma_f32_16x16x32_bf16 v[132:135], a[136:139], v[44:47], v[132:135]\n"
      "v_mfma_f32_16x16x32_bf16 v[132:135], a[140:143], v[60:63], v[132:135]\n"
      "v_cvt_pk_bf16_f32 v210, v136, v137\n"
      "v_mfma_f32_16x16x32_bf16 v[132:135], a[144:147], v[64:67], v[132:135]\n"
      "v_cvt_pk_bf16_f32 v211, v138, v139\n"
      "v_mfma_f32_16x16x32_bf16 v[132:135], a[148:151], v[68:71], v[132:135]\n"
      "v_mfma_f32_16x16x32_bf16 v[132:135], a[152:155], v[84:87], v[132:135]\n"
      "s_waitcnt lgkmcnt(2)\n"
      "v_mfma_f32_16x16x32_bf16 v[132:135], a[156:159], v[88:91], v[132:135]\n"
      "global_store_dwordx4 v252, v[220:223], s[54:55] offset:768 sc1\n"
      "v_mfma_f32_16x16x32_bf16 v[132:135], a[160:163], v[92:95], v[132:135]\n"
      "s_add_u32 m0, s17, 0x8f00\n"
      "v_mfma_f32_16x16x32_bf16 v[132:135], a[164:167], v[108:111], v[132:135]\n"
      "ds_write_b128 v244, v[208:211] offset:24576\n"
      "v_mfma_f32_16x16x32_bf16 v[132:135], a[168:171], v[112:115], v[132:135]\n"
      "global_load_lds_dwordx4 v237, s[52:53] offset:256 nt\n"
      "v_mfma_f32_16x16x32_bf16 v[132:135], a[172:175], v[116:119], v[132:135]\n"
      "v_mfma_f32_16x16x32_bf16 v[136:139], a[176:179], v[36:39], v[12:15]\n"
      "v_mfma_f32_16x16x32_bf16 v[136:139], a[180:183], v[40:43], v[136:139]\n"
      "v_mfma_f32_16x16x32_bf16 v[136:139], a[184:187], v[44:47], v[136:139]\n"
      "v_mfma_f32_16x16x32_bf16 v[136:139], a[188:191], v[60:63], v[136:139]\n"
      "v_cvt_pk_bf16_f32 v212, v132, v133\n"
      "v_mfma_f32_16x16x32_bf16 v[136:139], a[192:195], v[64:67], v[136:139]\n"
      "v_cvt_pk_bf16_f32 v213, v134, v135\n"
      "v_mfma_f32_16x16x32_bf16 v[136:139], a[196:199], v[68:71], v[136:139]\n"
      "v_mfma_f32_16x16x32_bf16 v[136:139], a[200:203], v[84:87], v[136:139]\n"
      "v_mfma_f32_16x16x32_bf16 v[136:139], a[204:207], v[88:91], v[136:139]\n"
      "s_waitcnt lgkmcnt(2)\n"
      "v_mfma_f32_16x16x32_bf16 v[136:139], a[208:211], v[92:95], v[136:139]\n"
      "global_store_dwordx4 v253, v[224:227], s[54:55] offset:768 sc1\n"
      "v_mfma_f32_16x16x32_bf16 v[136:139], a[212:215], v[108:111], v[136:139]\n"
      "s_add_u32 m0, s17, 0xae00\n"
      "v_mfma_f32_16x16x32_bf16 v[136:139], a[216:219], v[112:115], v[136:139]\n"
      "global_load_lds_dwordx4 v237, s[52:53] offset:512 nt\n"
      "v_mfma_f32_16x16x32_bf16 v[136:139], a[220:223], v[116:119], v[136:139]\n"
      "v_mfma_f32_16x16x32_bf16 v[132:135], a[224:227], v[36:39], v[16:19]\n"
      "v_mfma_f32_16x16x32_bf16 v[132:135], a[228:231], v[40:43], v[132:135]\n"
      "v_mfma_f32_16x16x32_bf16 v[132:135], a[232:235], v[44:47], v[132:135]\n"
      "v_mfma_f32_16x16x32_bf16 v[132:135], a[236:239], v[60:63], v[132:135]\n"
      "v_cvt_pk_bf16_f32 v214, v136, v137\n"
      "v_mfma_f32_16x16x32_bf16 v[132:135], a[0:3], v[64:67], v[132:135]\n"
      "v_cvt_pk_bf16_f32 v215, v138, v139\n"
      "v_mfma_f32_16x16x32_bf16 v[132:135], a[4:7], v[68:71], v[132:135]\n"
      "v_mfma_f32_16x16x32_bf16 v[132:135], a[8:11], v[84:87], v[132:135]\n"
      "v_mfma_f32_16x16x32_bf16 v[132:135], a[12:15], v[88:91], v[132:135]\n"
      "v_mfma_f32_16x16x32_bf16 v[132:135], a[16:19], v[92:95], v[132:135]\n"
      "v_mfma_f32_16x16x32_bf16 v[132:135], a[20:23], v[108:111], v[132:135]\n"
      "ds_write_b128 v245, v[212:215] offset:24576\n"
      "v_mfma_f32_16x16x32_bf16 v[132:135], v[140:143], v[112:115], v[132:135]\n"
      "s_lshl_b32 s79, s92, 1\n"
      "v_mfma_f32_16x16x32_bf16 v[132:135], v[156:159], v[116:119], v[132:135]\n"
      "s_waitcnt vmcnt(25) lgkmcnt(2)\n"
      "s_barrier\n"
      "v_mad_i32_i24 v252, v251, s79, v252\n"
      "v_mfma_f32_16x16x32_bf16 v[136:139], v[160:163], v[36:39], v[20:23]\n"
      "ds_read_b128 v[36:39], v232 offset:24576\n"
      "s_mul_i32 s79, s79, s100\n"
      "v_mfma_f32_16x16x32_bf16 v[136:139], v[164:167], v[40:43], v[136:139]\n"
      "ds_read_b128 v[40:43], v233 offset:24576\n"
      "v_add_u32 v247, s79, v247\n"
      "v_mfma_f32_16x16x32_bf16 v[136:139], v[180:183], v[44:47], v[136:139]\n"
      "ds_read_b128 v[44:47], v234 offset:24576\n"
      "s_mul_i32 s79, s79, s101\n"
      "v_mfma_f32_16x16x32_bf16 v[136:139], v[184:187], v[60:63], v[136:139]\n"
      "ds_read_b128 v[60:63], v235 offset:24576\n"
      "v_cvt_pk_bf16_f32 v208, v132, v133\n"
      "v_mfma_f32_16x16x32_bf16 v[136:139], v[188:191], v[64:67], v[136:139]\n"
      "ds_read_b128 v[64:67], v232 offset:32768\n"
      "v_cvt_pk_bf16_f32 v209, v134, v135\n"
      "v_mfma_f32_16x16x32_bf16 v[136:139], v[204:207], v[68:71], v[136:139]\n"
      "ds_read_b128 v[68:71], v233 offset:32768\n"
      "v_add_u32 v253, s79, v253\n"
      "v_mfma_f32_16x16x32_bf16 v[136:139], a[24:27], v[84:87], v[136:139]\n"
      "ds_read_b128 v[84:87], v234 offset:32768\n"
      "v_mfma_f32_16x16x32_bf16 v[136:139], a[28:31], v[88:91], v[136:139]\n"
      "ds_read_b128 v[88:91], v235 offset:32768\n"
      "v_mfma_f32_16x16x32_bf16 v[136:139], a[32:35], v[92:95], v[136:139]\n"
      "ds_read_b128 v[92:95], v232 offset:40960\n"
      "v_mfma_f32_16x16x32_bf16 v[136:139], a[36:39], v[108:111], v[136:139]\n"
      "ds_read_b128 v[108:111], v233 offset:40960\n"
      "v_mfma_f32_16x16x32_bf16 v[136:139], a[40:43], v[112:115], v[136:139]\n"
      "ds_read_b128 v[112:115], v234 offset:40960\n"
      "v_mfma_f32_16x16x32_bf16 v[136:139], a[44:47], v[116:119], v[136:139]\n"
      "ds_read_b128 v[116:119], v235 offset:40960\n"
      "s_waitcnt lgkmcnt(8)\n"
      "v_mfma_f32_16x16x32_bf16 v[132:135], a[240:243], v[36:39], v[0:3]\n"
      "v_mfma_f32_16x16x32_bf16 v[132:135], a[244:247], v[40:43], v[132:135]\n"
      "s_add_u32 s54, s30, s35\n"
      "s_addc_u32 s55, s31, 0\n"
      "v_mfma_f32_16x16x32_bf16 v[132:135], a[248:251], v[44:47], v[132:135]\n"
      "v_mfma_f32_16x16x32_bf16 v[132:135], a[252:255], v[60:63], v[132:135]\n"
      "v_cvt_pk_bf16_f32 v210, v136, v137\n"
      "s_waitcnt lgkmcnt(4)\n"
      "v_mfma_f32_16x16x32_bf16 v[132:135], a[48:51], v[64:67], v[132:135]\n"
      "v_cvt_pk_bf16_f32 v211, v138, v139\n"
      "v_mfma_f32_16x16x32_bf16 v[132:135], a[52:55], v[68:71], v[132:135]\n"
      "s_mul_i32 s78, s94, s19\n"
      "v_mfma_f32_16x16x32_bf16 v[132:135], a[56:59], v[84:87], v[132:135]\n"
      "ds_read_b128 v[216:219], v246 offset:12288\n"
      "v_add_u32 v236, s78, v236\n"
      "v_mfma_f32_16x16x32_bf16 v[132:135], a[60:63], v[88:91], v[132:135]\n"
      "ds_read_b128 v[220:223], v246 offset:13312\n"
      "s_waitcnt lgkmcnt(2)\n"
      "v_mfma_f32_16x16x32_bf16 v[132:135], a[64:67], v[92:95], v[132:135]\n"
      "ds_read_b128 v[224:227], v246 offset:14336\n"
      "v_mfma_f32_16x16x32_bf16 v[132:135], a[68:71], v[108:111], v[132:135]\n"
      "ds_write_b128 v244, v[208:211] offset:24704\n"
      "v_mfma_f32_16x16x32_bf16 v[132:135], a[72:75], v[112:115], v[132:135]\n"
      "s_mul_i32 s78, s78, s99\n"
      "v_mfma_f32_16x16x32_bf16 v[132:135], a[76:79], v[116:119], v[132:135]\n"
      "v_add_u32 v237, s78, v237\n"
      "v_mfma_f32_16x16x32_bf16 v[136:139], a[80:83], v[36:39], v[4:7]\n"
      "v_mfma_f32_16x16x32_bf16 v[136:139], a[84:87], v[40:43], v[136:139]\n"
      "v_mfma_f32_16x16x32_bf16 v[136:139], a[88:91], v[44:47], v[136:139]\n"
      "v_mfma_f32_16x16x32_bf16 v[136:139], a[92:95], v[60:63], v[136:139]\n"
      "v_cvt_pk_bf16_f32 v208, v132, v133\n"
      "v_mfma_f32_16x16x32_bf16 v[136:139], a[96:99], v[64:67], v[136:139]\n"
      "v_cvt_pk_bf16_f32 v209, v134, v135\n"
      "v_mfma_f32_16x16x32_bf16 v[136:139], a[100:103], v[68:71], v[136:139]\n"
      "s_waitcnt lgkmcnt(3)\n"
      "v_mfma_f32_16x16x32_bf16 v[136:139], a[104:107], v[84:87], v[136:139]\n"
      "global_store_dwordx4 v247, v[216:219], s[54:55] offset:0 sc1\n"
      "v_mfma_f32_16x16x32_bf16 v[136:139], a[108:111], v[88:91], v[136:139]\n"
      "s_add_u32 s52, s20, s37\n"
      "s_addc_u32 s53, s21, 0\n"
      "v_mfma_f32_16x16x32_bf16 v[136:139], a[112:115], v[92:95], v[136:139]\n"
      "s_add_u32 m0, s17, 0xc000\n"
      "v_mfma_f32_16x16x32_bf16 v[136:139], a[116:119], v[108:111], v[136:139]\n"
      "global_load_lds_dwordx4 v236, s[52:53] offset:0 nt\n"
      "v_mfma_f32_16x16x32_bf16 v[136:139], a[120:123], v[112:115], v[136:139]\n"
      "v_mfma_f32_16x16x32_bf16 v[136:139], a[124:127], v[116:119], v[136:139]\n"
      "v_mfma_f32_16x16x32_bf16 v[132:135], a[128:131], v[36:39], v[8:11]\n"
      "v_mfma_f32_16x16x32_bf16 v[132:135], a[132:135], v[40:43], v[132:135]\n"
      "v_mfma_f32_16x16x32_bf16 v[132:135], a[136:139], v[44:47], v[132:135]\n"
      "v_mfma_f32_16x16x32_bf16 v[132:135], a[140:143], v[60:63], v[132:135]\n"
      "v_cvt_pk_bf16_f32 v210, v136, v137\n"
      "v_mfma_f32_16x16x32_bf16 v[132:135], a[144:147], v[64:67], v[132:135]\n"
      "v_cvt_pk_bf16_f32 v211, v138, v139\n"
      "v_mfma_f32_16x16x32_bf16 v[132:135], a[148:151], v[68:71], v[132:135]\n"
      "v_mfma_f32_16x16x32_bf16 v[132:135], a[152:155], v[84:87], v[132:135]\n"
      "s_waitcnt lgkmcnt(2)\n"
      "v_mfma_f32_16x16x32_bf16 v[132:135], a[156:159], v[88:91], v[132:135]\n"
      "global_store_dwordx4 v252, v[220:223], s[54:55] offset:0 sc1\n"
      "v_mfma_f32_16x16x32_bf16 v[132:135], a[160:163], v[92:95], v[132:135]\n"
      "s_add_u32 m0, s17, 0xdf00\n"
      "v_mfma_f32_16x16x32_bf16 v[132:135], a[164:167], v[108:111], v[132:135]\n"
      "ds_write_b128 v244, v[208:211] offset:0\n"
      "v_mfma_f32_16x16x32_bf16 v[132:135], a[168:171], v[112:115], v[132:135]\n"
      "global_load_lds_dwordx4 v236, s[52:53] offset:256 nt\n"
      "v_mfma_f32_16x16x32_bf16 v[132:135], a[172:175], v[116:119], v[132:135]\n"
      "v_mfma_f32_16x16x32_bf16 v[136:139], a[176:179], v[36:39], v[12:15]\n"
      "v_mfma_f32_16x16x32_bf16 v[136:139], a[180:183], v[40:43], v[136:139]\n"
      "v_mfma_f32_16x16x32_bf16 v[136:139], a[184:187], v[44:47], v[136:139]\n"
      "v_mfma_f32_16x16x32_bf16 v[136:139], a[188:191], v[60:63], v[136:139]\n"
      "v_cvt_pk_bf16_f32 v212, v132, v133\n"
      "v_mfma_f32_16x16x32_bf16 v[136:139], a[192:195], v[64:67], v[136:139]\n"
      "v_cvt_pk_bf16_f32 v213, v134, v135\n"
      "v_mfma_f32_16x16x32_bf16 v[136:139], a[196:199], v[68:71], v[136:139]\n"
      "v_mfma_f32_16x16x32_bf16 v[136:139], a[200:203], v[84:87], v[136:139]\n"
      "v_mfma_f32_16x16x32_bf16 v[136:139], a[204:207], v[88:91], v[136:139]\n"
      "s_waitcnt lgkmcnt(2)\n"
      "v_mfma_f32_16x16x32_bf16 v[136:139], a[208:211], v[92:95], v[136:139]\n"
      "global_store_dwordx4 v253, v[224:227], s[54:55] offset:0 sc1\n"
      "v_mfma_f32_16x16x32_bf16 v[136:139], a[212:215], v[108:111], v[136:139]\n"
      "s_add_u32 m0, s17, 0xfe00\n"
      "v_mfma_f32_16x16x32_bf16 v[136:139], a[216:219], v[112:115], v[136:139]\n"
      "global_load_lds_dwordx4 v236, s[52:53] offset:512 nt\n"
      "v_mfma_f32_16x16x32_bf16 v[136:139], a[220:223], v[116:119], v[136:139]\n"
      "v_mfma_f32_16x16x32_bf16 v[132:135], a[224:227], v[36:39], v[16:19]\n"
      "v_mfma_f32_16x16x32_bf16 v[132:135], a[228:231], v[40:43], v[132:135]\n"
      "v_mfma_f32_16x16x32_bf16 v[132:135], a[232:235], v[44:47], v[132:135]\n"
      "v_mfma_f32_16x16x32_bf16 v[132:135], a[236:239], v[60:63], v[132:135]\n"
      "v_cvt_pk_bf16_f32 v214, v136, v137\n"
      "v_mfma_f32_16x16x32_bf16 v[132:135], a[0:3], v[64:67], v[132:135]\n"
      "v_cvt_pk_bf16_f32 v215, v138, v139\n"
      "v_mfma_f32_16x16x32_bf16 v[132:135], a[4:7], v[68:71], v[132:135]\n"
      "v_mfma_f32_16x16x32_bf16 v[132:135], a[8:11], v[84:87], v[132:135]\n"
      "v_mfma_f32_16x16x32_bf16 v[132:135], a[12:15], v[88:91], v[132:135]\n"
      "v_mfma_f32_16x16x32_bf16 v[132:135], a[16:19], v[92:95], v[132:135]\n"
      "v_mfma_f32_16x16x32_bf16 v[132:135], a[20:23], v[108:111], v[132:135]\n"
      "ds_write_b128 v245, v[212:215] offset:0\n"
      "v_mfma_f32_16x16x32_bf16 v[132:135], v[140:143], v[112:115], v[132:135]\n"
      "v_mfma_f32_16x16x32_bf16 v[132:135], v[156:159], v[116:119], v[132:135]\n"
      "s_waitcnt lgkmcnt(2)\n"
      "s_barrier\n"
      "v_mfma_f32_16x16x32_bf16 v[136:139], v[160:163], v[36:39], v[20:23]\n"
      "ds_read_b128 v[36:39], v232 offset:24832\n"
      "v_mfma_f32_16x16x32_bf16 v[136:139], v[164:167], v[40:43], v[136:139]\n"
      "ds_read_b128 v[40:43], v233 offset:24832\n"
      "v_mfma_f32_16x16x32_bf16 v[136:139], v[180:183], v[44:47], v[136:139]\n"
      "ds_read_b128 v[44:47], v234 offset:24832\n"
      "v_mfma_f32_16x16x32_bf16 v[136:139], v[184:187], v[60:63], v[136:139]\n"
      "ds_read_b128 v[60:63], v235 offset:24832\n"
      "v_cvt_pk_bf16_f32 v208, v132, v133\n"
      "v_mfma_f32_16x16x32_bf16 v[136:139], v[188:191], v[64:67], v[136:139]\n"
      "ds_read_b128 v[64:67], v232 offset:33024\n"
      "v_cvt_pk_bf16_f32 v209, v134, v135\n"
      "v_mfma_f32_16x16x32_bf16 v[136:139], v[204:207], v[68:71], v[136:139]\n"
      "ds_read_b128 v[68:71], v233 offset:33024\n"
      "v_mfma_f32_16x16x32_bf16 v[136:139], a[24:27], v[84:87], v[136:139]\n"
      "ds_read_b128 v[84:87], v234 offset:33024\n"
      "v_mfma_f32_16x16x32_bf16 v[136:139], a[28:31], v[88:91], v[136:139]\n"
      "ds_read_b128 v[88:91], v235 offset:33024\n"
      "v_mfma_f32_16x16x32_bf16 v[136:139], a[32:35], v[92:95], v[136:139]\n"
      "ds_read_b128 v[92:95], v232 offset:41216\n"
      "v_mfma_f32_16x16x32_bf16 v[136:139], a[36:39], v[108:111], v[136:139]\n"
      "ds_read_b128 v[108:111], v233 offset:41216\n"
      "v_mfma_f32_16x16x32_bf16 v[136:139], a[40:43], v[112:115], v[136:139]\n"
      "ds_read_b128 v[112:115], v234 offset:41216\n"
      "v_mfma_f32_16x16x32_bf16 v[136:139], a[44:47], v[116:119], v[136:139]\n"
      "ds_read_b128 v[116:119], v235 offset:41216\n"
      "s_waitcnt lgkmcnt(8)\n"
      "v_mfma_f32_16x16x32_bf16 v[132:135], a[240:243], v[36:39], v[0:3]\n"
      "v_add_u32 v232, 0xffff4000, v232\n"
      "v_mfma_f32_16x16x32_bf16 v[132:135], a[244:247], v[40:43], v[132:135]\n"
      "v_add_u32 v233, 0xffff4000, v233\n"
      "v_mfma_f32_16x16x32_bf16 v[132:135], a[248:251], v[44:47], v[132:135]\n"
      "v_add_u32 v234, 0xffff4000, v234\n"
      "v_mfma_f32_16x16x32_bf16 v[132:135], a[252:255], v[60:63], v[132:135]\n"
      "v_add_u32 v235, 0xffff4000, v235\n"
      "s_waitcnt lgkmcnt(4)\n"
      "v_mfma_f32_16x16x32_bf16 v[132:135], a[48:51], v[64:67], v[132:135]\n"
      "v_cvt_pk_bf16_f32 v210, v136, v137\n"
      "v_mfma_f32_16x16x32_bf16 v[132:135], a[52:55], v[68:71], v[132:135]\n"
      "v_cvt_pk_bf16_f32 v211, v138, v139\n"
      "v_mfma_f32_16x16x32_bf16 v[132:135], a[56:59], v[84:87], v[132:135]\n"
      "ds_read_b128 v[216:219], v246 offset:24576\n"
      "v_mfma_f32_16x16x32_bf16 v[132:135], a[60:63], v[88:91], v[132:135]\n"
      "ds_read_b128 v[220:223], v246 offset:25600\n"
      "s_waitcnt lgkmcnt(2)\n"
      "v_mfma_f32_16x16x32_bf16 v[132:135], a[64:67], v[92:95], v[132:135]\n"
      "ds_read_b128 v[224:227], v246 offset:26624\n"
      "v_mfma_f32_16x16x32_bf16 v[132:135], a[68:71], v[108:111], v[132:135]\n"
      "v_lshlrev_b32 v0, 16, v96\n"
      "v_mfma_f32_16x16x32_bf16 v[132:135], a[72:75], v[112:115], v[132:135]\n"
      "ds_write_b128 v244, v[208:211] offset:128\n"
      "v_mfma_f32_16x16x32_bf16 v[132:135], a[76:79], v[116:119], v[132:135]\n"
      "v_and_b32 v1, 0xffff0000, v96\n"
      "v_mfma_f32_16x16x32_bf16 v[136:139], a[80:83], v[36:39], v[4:7]\n"
      "v_lshlrev_b32 v2, 16, v97\n"
      "v_mfma_f32_16x16x32_bf16 v[136:139], a[84:87], v[40:43], v[136:139]\n"
      "v_and_b32 v3, 0xffff0000, v97\n"
      "v_mfma_f32_16x16x32_bf16 v[136:139], a[88:91], v[44:47], v[136:139]\n"
      "s_waitcnt vmcnt(12)\n"
      "s_sub_u32 s49, s48, 1000\n"
      "v_mfma_f32_16x16x32_bf16 v[136:139], a[92:95], v[60:63], v[136:139]\n"
      "v_mov_b32 v230, 1100\n"
      "v_cvt_pk_bf16_f32 v209, v134, v135\n"
      "v_mfma_f32_16x16x32_bf16 v[136:139], a[96:99], v[64:67], v[136:139]\n"
      "v_xor_b32 v228, s80, v228\n"
      "v_mfma_f32_16x16x32_bf16 v[136:139], a[100:103], v[68:71], v[136:139]\n"
      "v_subrev_u32 v228, s49, v228\n"
      "v_mfma_f32_16x16x32_bf16 v[136:139], a[104:107], v[84:87], v[136:139]\n"
      "v_mov_b32 v231, 2000\n"
      "v_mfma_f32_16x16x32_bf16 v[136:139], a[108:111], v[88:91], v[136:139]\n"
      "v_cmp_gt_u32 s[58:59], v231, v228\n"
      "v_mfma_f32_16x16x32_bf16 v[136:139], a[112:115], v[92:95], v[136:139]\n"
      "v_cndmask_b32 v228, v230, v228, s[58:59]\n"
      "v_mfma_f32_16x16x32_bf16 v[136:139], a[116:119], v[108:111], v[136:139]\n"
      "v_cvt_pk_bf16_f32 v208, v132, v133\n"
      "LPD11:\n"
      "s_cbranch_execz LPD14\n"
      "v_mfma_f32_16x16x32_bf16 v[136:139], a[120:123], v[112:115], v[136:139]\n"
      "v_add_u32_dpp v228, v228, v228 row_shr:1 row_mask:0xf bank_mask:0xf bound_ctrl:0\n"
      "v_mfma_f32_16x16x32_bf16 v[136:139], a[124:127], v[116:119], v[136:139]\n"
      "s_waitcnt lgkmcnt(3)\n"
      "v_mfma_f32_16x16x32_bf16 v[132:135], a[128:131], v[36:39], v[8:11]\n"
      "v_add_u32_dpp v228, v228, v228 row_shr:2 row_mask:0xf bank_mask:0xf bound_ctrl:0\n"
      "v_mfma_f32_16x16x32_bf16 v[132:135], a[132:135], v[40:43], v[132:135]\n"
      "global_store_dwordx4 v247, v[216:219], s[54:55] offset:768 sc1\n"
      "v_mfma_f32_16x16x32_bf16 v[132:135], a[136:139], v[44:47], v[132:135]\n"
      "v_add_u32_dpp v228, v228, v228 row_shr:4 row_mask:0xf bank_mask:0xf bound_ctrl:0\n"
      "v_mfma_f32_16x16x32_bf16 v[132:135], a[140:143], v[60:63], v[132:135]\n"
      "v_cvt_pk_bf16_f32 v210, v136, v137\n"
      "v_mfma_f32_16x16x32_bf16 v[132:135], a[144:147], v[64:67], v[132:135]\n"
      "v_add_u32_dpp v228, v228, v228 row_shr:8 row_mask:0xf bank_mask:0xf bound_ctrl:0\n"
      "v_mfma_f32_16x16x32_bf16 v[132:135], a[148:151], v[68:71], v[132:135]\n"
      "v_cvt_pk_bf16_f32 v211, v138, v139\n"
      "v_mfma_f32_16x16x32_bf16 v[132:135], a[152:155], v[84:87], v[132:135]\n"
      "v_readlane_b32 s62, v228, 15\n"
      "v_mfma_f32_16x16x32_bf16 v[132:135], a[156:159], v[88:91], v[132:135]\n"
      "s_waitcnt lgkmcnt(2)\n"
      "v_mfma_f32_16x16x32_bf16 v[132:135], a[160:163], v[92:95], v[132:135]\n"
      "global_store_dwordx4 v252, v[220:223], s[54:55] offset:768 sc1\n"
      "v_mfma_f32_16x16x32_bf16 v[132:135], a[164:167], v[108:111], v[132:135]\n"
      "s_lshr_b32 s62, s62, 4\n"
      "v_mfma_f32_16x16x32_bf16 v[132:135], a[168:171], v[112:115], v[132:135]\n"
      "s_add_u32 s62, s62, 20\n"
      "v_mfma_f32_16x16x32_bf16 v[132:135], a[172:175], v[116:119], v[132:135]\n"
      "s_add_u32 s49, s49, s62\n"
      "v_mfma_f32_16x16x32_bf16 v[136:139], a[176:179], v[36:39], v[12:15]\n"
      "s_add_u32 s85, s83, 3475\n"
      "v_mfma_f32_16x16x32_bf16 v[136:139], a[180:183], v[40:43], v[136:139]\n"
      "s_sub_u32 s85, s85, s49\n"
      "v_mfma_f32_16x16x32_bf16 v[136:139], a[184:187], v[44:47], v[136:139]\n"
      "s_max_i32 s85, s85, 0\n"
      "v_mfma_f32_16x16x32_bf16 v[136:139], a[188:191], v[60:63], v[136:139]\n"
      "s_add_u32 s49, s49, s85\n"
      "v_mfma_f32_16x16x32_bf16 v[136:139], a[192:195], v[64:67], v[136:139]\n"
      "s_add_u32 s62, s62, s85\n"
      "v_mfma_f32_16x16x32_bf16 v[136:139], a[196:199], v[68:71], v[136:139]\n"
      "ds_write_b128 v244, v[208:211] offset:12288\n"
      "v_mfma_f32_16x16x32_bf16 v[136:139], a[200:203], v[84:87], v[136:139]\n"
      "v_cvt_pk_bf16_f32 v212, v132, v133\n"
      "v_mfma_f32_16x16x32_bf16 v[136:139], a[204:207], v[88:91], v[136:139]\n"
      "v_cvt_pk_bf16_f32 v213, v134, v135\n"
      "v_mfma_f32_16x16x32_bf16 v[136:139], a[208:211], v[92:95], v[136:139]\n"
      "s_waitcnt lgkmcnt(2)\n"
      "v_mfma_f32_16x16x32_bf16 v[136:139], a[212:215], v[108:111], v[136:139]\n"
      "global_store_dwordx4 v253, v[224:227], s[54:55] offset:768 sc1\n"
      "v_mfma_f32_16x16x32_bf16 v[136:139], a[216:219], v[112:115], v[136:139]\n"
      "s_add_u32 m0, s17, 0xd000\n"
      "v_mfma_f32_16x16x32_bf16 v[136:139], a[220:223], v[116:119], v[136:139]\n"
      "global_load_lds_dwordx4 v237, s[52:53] offset:0 nt\n"
      "v_mfma_f32_16x16x32_bf16 v[132:135], a[224:227], v[36:39], v[16:19]\n"
      "v_lshlrev_b32 v4, 16, v98\n"
      "v_mfma_f32_16x16x32_bf16 v[132:135], a[228:231], v[40:43], v[132:135]\n"
      "v_and_b32 v5, 0xffff0000, v98\n"
      "v_mfma_f32_16x16x32_bf16 v[132:135], a[232:235], v[44:47], v[132:135]\n"
      "v_lshlrev_b32 v6, 16, v99\n"
      "v_mfma_f32_16x16x32_bf16 v[132:135], a[236:239], v[60:63], v[132:135]\n"
      "s_memrealtime s[84:85]\n"
      "v_mfma_f32_16x16x32_bf16 v[132:135], a[0:3], v[64:67], v[132:135]\n"
      "v_cvt_pk_bf16_f32 v214, v136, v137\n"
      "v_mfma_f32_16x16x32_bf16 v[132:135], a[4:7], v[68:71], v[132:135]\n"
      "v_cvt_pk_bf16_f32 v215, v138, v139\n"
      "v_mfma_f32_16x16x32_bf16 v[132:135], a[8:11], v[84:87], v[132:135]\n"
      "v_and_b32 v7, 0xffff0000, v99\n"
      "v_mfma_f32_16x16x32_bf16 v[132:135], a[12:15], v[88:91], v[132:135]\n"
      "v_lshlrev_b32 v8, 16, v100\n"
      "v_mfma_f32_16x16x32_bf16 v[132:135], a[16:19], v[92:95], v[132:135]\n"
      "v_and_b32 v9, 0xffff0000, v100\n"
      "v_mfma_f32_16x16x32_bf16 v[132:135], a[20:23], v[108:111], v[132:135]\n"
      "s_add_u32 m0, s17, 0xef00\n"
      "v_mfma_f32_16x16x32_bf16 v[132:135], v[140:143], v[112:115], v[132:135]\n"
      "s_waitcnt lgkmcnt(0)\n"
      "s_sub_u32 s86, s49, s84\n"
      "s_mov_b32 s83, s84\n"
      "v_mfma_f32_16x16x32_bf16 v[132:135], v[156:159], v[116:119], v[132:135]\n"
      "s_cmp_gt_u32 s62, 1080\n"
      "s_cselect_b64 vcc, -1, 0\n"
      "s_barrier\n"
      "v_mfma_f32_16x16x32_bf16 v[136:139], v[160:163], v[36:39], v[20:23]\n"
      "ds_read_b128 v[36:39], v232 offset:0\n"
      "ds_write_b128 v245, v[212:215] offset:12288\n"
      "v_mfma_f32_16x16x32_bf16 v[136:139], v[164:167], v[40:43], v[136:139]\n"
      "ds_read_b128 v[40:43], v233 offset:0\n"
      "v_mfma_f32_16x16x32_bf16 v[136:139], v[180:183], v[44:47], v[136:139]\n"
      "ds_read_b128 v[44:47], v234 offset:0\n"
      "v_mfma_f32_16x16x32_bf16 v[136:139], v[184:187], v[60:63], v[136:139]\n"
      "ds_read_b128 v[60:63], v235 offset:0\n"
      "v_cvt_pk_bf16_f32 v208, v132, v133\n"
      "v_mfma_f32_16x16x32_bf16 v[136:139], v[188:191], v[64:67], v[136:139]\n"
      "ds_read_b128 v[64:67], v232 offset:8192\n"
      "v_cvt_pk_bf16_f32 v209, v134, v135\n"
      "v_mfma_f32_16x16x32_bf16 v[136:139], v[204:207], v[68:71], v[136:139]\n"
      "ds_read_b128 v[68:71], v233 offset:8192\n"
      "v_mfma_f32_16x16x32_bf16 v[136:139], a[24:27], v[84:87], v[136:139]\n"
      "ds_read_b128 v[84:87], v234 offset:8192\n"
      "v_mfma_f32_16x16x32_bf16 v[136:139], a[28:31], v[88:91], v[136:139]\n"
      "ds_read_b128 v[88:91], v235 offset:8192\n"
      "v_mfma_f32_16x16x32_bf16 v[136:139], a[32:35], v[92:95], v[136:139]\n"
      "ds_read_b128 v[92:95], v232 offset:16384\n"
      "v_mfma_f32_16x16x32_bf16 v[136:139], a[36:39], v[108:111], v[136:139]\n"
      "ds_read_b128 v[108:111], v233 offset:16384\n"
      "v_mfma_f32_16x16x32_bf16 v[136:139], a[40:43], v[112:115], v[136:139]\n"
      "ds_read_b128 v[112:115], v234 offset:16384\n"
      "v_mfma_f32_16x16x32_bf16 v[136:139], a[44:47], v[116:119], v[136:139]\n"
      "ds_read_b128 v[116:119], v235 offset:16384\n"
      "s_waitcnt lgkmcnt(8)\n"
      "v_mfma_f32_16x16x32_bf16 v[132:135], a[240:243], v[36:39], v[0:3]\n"
      "global_load_lds_dwordx4 v237, s[52:53] offset:256 nt\n"
      "s_cbranch_vccnz LT0\n"
      "LS0:\n"
      "v_mfma_f32_16x16x32_bf16 v[132:135], a[244:247], v[40:43], v[132:135]\n"
      "v_lshlrev_b32 v10, 16, v101\n"
      "v_mfma_f32_16x16x32_bf16 v[132:135], a[248:251], v[44:47], v[132:135]\n"
      "s_mul_i32 s86, s86, 146\n"
      "v_mfma_f32_16x16x32_bf16 v[132:135], a[252:255], v[60:63], v[132:135]\n"
      "v_and_b32 v11, 0xffff0000, v101\n"
      "v_cvt_pk_bf16_f32 v210, v136, v137\n"
      "s_waitcnt lgkmcnt(4)\n"
      "v_mfma_f32_16x16x32_bf16 v[132:135], a[48:51], v[64:67], v[132:135]\n"
      "s_ashr_i32 s86, s86, 10\n"
      "v_cvt_pk_bf16_f32 v211, v138, v139\n"
      "v_mfma_f32_16x16x32_bf16 v[132:135], a[52:55], v[68:71], v[132:135]\n"
      "v_lshlrev_b32 v12, 16, v102\n"
      "v_mfma_f32_16x16x32_bf16 v[132:135], a[56:59], v[84:87], v[132:135]\n"
      "v_and_b32 v13, 0xffff0000, v102\n"
      "v_mfma_f32_16x16x32_bf16 v[132:135], a[60:63], v[88:91], v[132:135]\n"
      "v_lshlrev_b32 v14, 16, v103\n"
      "s_waitcnt lgkmcnt(0)\n"
      "v_mfma_f32_16x16x32_bf16 v[132:135], a[64:67], v[92:95], v[132:135]\n"
      "v_and_b32 v15, 0xffff0000, v103\n"
      "v_mfma_f32_16x16x32_bf16 v[132:135], a[68:71], v[108:111], v[132:135]\n"
      "v_lshlrev_b32 v16, 16, v104\n"
      "ds_write_b128 v244, v[208:211] offset:12416\n"
      "v_mfma_f32_16x16x32_bf16 v[132:135], a[72:75], v[112:115], v[132:135]\n"
      "s_add_u32 m0, s17, 0x10e00\n"
      "v_mfma_f32_16x16x32_bf16 v[132:135], a[76:79], v[116:119], v[132:135]\n"
      "global_load_lds_dwordx4 v237, s[52:53] offset:512 nt\n"
      "v_mfma_f32_16x16x32_bf16 v[136:139], a[80:83], v[36:39], v[4:7]\n"
      "v_and_b32 v17, 0xffff0000, v104\n"
      "v_mfma_f32_16x16x32_bf16 v[136:139], a[84:87], v[40:43], v[136:139]\n"
      "v_lshlrev_b32 v18, 16, v105\n"
      "v_mfma_f32_16x16x32_bf16 v[136:139], a[88:91], v[44:47], v[136:139]\n"
      "v_and_b32 v19, 0xffff0000, v105\n"
      "v_mfma_f32_16x16x32_bf16 v[136:139], a[92:95], v[60:63], v[136:139]\n"
      "v_lshlrev_b32 v20, 16, v106\n"
      "v_mfma_f32_16x16x32_bf16 v[136:139], a[96:99], v[64:67], v[136:139]\n"
      "v_and_b32 v21, 0xffff0000, v106\n"
      "v_mfma_f32_16x16x32_bf16 v[136:139], a[100:103], v[68:71], v[136:139]\n"
      "v_lshlrev_b32 v22, 16, v107\n"
      "v_mfma_f32_16x16x32_bf16 v[136:139], a[104:107], v[84:87], v[136:139]\n"
      "v_and_b32 v23, 0xffff0000, v107\n"
      "v_mfma_f32_16x16x32_bf16 v[136:139], a[108:111], v[88:91], v[136:139]\n"
      "v_cvt_pk_bf16_f32 v208, v132, v133\n"
      "v_mfma_f32_16x16x32_bf16 v[136:139], a[112:115], v[92:95], v[136:139]\n"
      "v_cvt_pk_bf16_f32 v209, v134, v135\n"
      "v_mfma_f32_16x16x32_bf16 v[136:139], a[116:119], v[108:111], v[136:139]\n"
      "s_add_u32 s54, s44, s35\n"
      "s_addc_u32 s55, s45, 0\n"
      "v_mfma_f32_16x16x32_bf16 v[136:139], a[120:123], v[112:115], v[136:139]\n"
      "ds_read_b128 v[216:219], v246 offset:0\n"
      "v_mfma_f32_16x16x32_bf16 v[136:139], a[124:127], v[116:119], v[136:139]\n"
      "ds_read_b128 v[220:223], v246 offset:1024\n"
      "v_mfma_f32_16x16x32_bf16 v[132:135], a[128:131], v[36:39], v[8:11]\n"
      "ds_read_b128 v[224:227], v246 offset:2048\n"
      "s_cbranch_vccnz LT1\n"
      "LS1:\n"
      "v_mfma_f32_16x16x32_bf16 v[132:135], a[132:135], v[40:43], v[132:135]\n"
      "s_waitcnt lgkmcnt(2)\n"
      "v_mfma_f32_16x16x32_bf16 v[132:135], a[136:139], v[44:47], v[132:135]\n"
      "global_store_dwordx4 v247, v[216:219], s[54:55] offset:0 sc1\n"
      "v_mfma_f32_16x16x32_bf16 v[132:135], a[140:143], v[60:63], v[132:135]\n"
      "v_cvt_pk_bf16_f32 v210, v136, v137\n"
      "v_mfma_f32_16x16x32_bf16 v[132:135], a[144:147], v[64:67], v[132:135]\n"
      "v_cvt_pk_bf16_f32 v211, v138, v139\n"
      "v_mfma_f32_16x16x32_bf16 v[132:135], a[148:151], v[68:71], v[132:135]\n"
      "s_add_u32 s52, s42, s37\n"
      "s_addc_u32 s53, s43, 0\n"
      "v_mfma_f32_16x16x32_bf16 v[132:135], a[152:155], v[84:87], v[132:135]\n"
      "s_waitcnt lgkmcnt(1)\n"
      "v_mfma_f32_16x16x32_bf16 v[132:135], a[156:159], v[88:91], v[132:135]\n"
      "global_store_dwordx4 v252, v[220:223], s[54:55] offset:0 sc1\n"
      "v_mfma_f32_16x16x32_bf16 v[132:135], a[160:163], v[92:95], v[132:135]\n"
      "s_add_u32 m0, s17, 0x12000\n"
      "v_mfma_f32_16x16x32_bf16 v[132:135], a[164:167], v[108:111], v[132:135]\n"
      "ds_write_b128 v244, v[208:211] offset:24576\n"
      "v_mfma_f32_16x16x32_bf16 v[132:135], a[168:171], v[112:115], v[132:135]\n"
      "global_load_lds_dwordx4 v236, s[52:53] offset:0 nt\n"
      "v_mfma_f32_16x16x32_bf16 v[132:135], a[172:175], v[116:119], v[132:135]\n"
      "v_mfma_f32_16x16x32_bf16 v[136:139], a[176:179], v[36:39], v[12:15]\n"
      "v_mfma_f32_16x16x32_bf16 v[136:139], a[180:183], v[40:43], v[136:139]\n"
      "v_mfma_f32_16x16x32_bf16 v[136:139], a[184:187], v[44:47], v[136:139]\n"
      "v_mfma_f32_16x16x32_bf16 v[136:139], a[188:191], v[60:63], v[136:139]\n"
      "v_cvt_pk_bf16_f32 v212, v132, v133\n"
      "v_mfma_f32_16x16x32_bf16 v[136:139], a[192:195], v[64:67], v[136:139]\n"
      "v_cvt_pk_bf16_f32 v213, v134, v135\n"
      "v_mfma_f32_16x16x32_bf16 v[136:139], a[196:199], v[68:71], v[136:139]\n"
      "v_mfma_f32_16x16x32_bf16 v[136:139], a[200:203], v[84:87], v[136:139]\n"
      "v_mfma_f32_16x16x32_bf16 v[136:139], a[204:207], v[88:91], v[136:139]\n"
      "s_waitcnt lgkmcnt(1)\n"
      "v_mfma_f32_16x16x32_bf16 v[136:139], a[208:211], v[92:95], v[136:139]\n"
      "global_store_dwordx4 v253, v[224:227], s[54:55] offset:0 sc1\n"
      "v_mfma_f32_16x16x32_bf16 v[136:139], a[212:215], v[108:111], v[136:139]\n"
      "s_add_u32 m0, s17, 0x13f00\n"
      "v_mfma_f32_16x16x32_bf16 v[136:139], a[216:219], v[112:115], v[136:139]\n"
      "global_load_lds_dwordx4 v236, s[52:53] offset:256 nt\n"
      "v_mfma_f32_16x16x32_bf16 v[136:139], a[220:223], v[116:119], v[136:139]\n"
      "v_mfma_f32_16x16x32_bf16 v[132:135], a[224:227], v[36:39], v[16:19]\n"
      "s_cbranch_vccnz LT2\n"
      "LS2:\n"
      "v_mfma_f32_16x16x32_bf16 v[132:135], a[228:231], v[40:43], v[132:135]\n"
      "v_mfma_f32_16x16x32_bf16 v[132:135], a[232:235], v[44:47], v[132:135]\n"
      "v_mfma_f32_16x16x32_bf16 v[132:135], a[236:239], v[60:63], v[132:135]\n"
      "v_cvt_pk_bf16_f32 v214, v136, v137\n"
      "v_mfma_f32_16x16x32_bf16 v[132:135], a[0:3], v[64:67], v[132:135]\n"
      "v_cvt_pk_bf16_f32 v215, v138, v139\n"
      "v_mfma_f32_16x16x32_bf16 v[132:135], a[4:7], v[68:71], v[132:135]\n"
      "v_mfma_f32_16x16x32_bf16 v[132:135], a[8:11], v[84:87], v[132:135]\n"
      "v_mfma_f32_16x16x32_bf16 v[132:135], a[12:15], v[88:91], v[132:135]\n"
      "v_mfma_f32_16x16x32_bf16 v[132:135], a[16:19], v[92:95], v[132:135]\n"
      "s_add_u32 m0, s17, 0x15e00\n"
      "v_mfma_f32_16x16x32_bf16 v[132:135], a[20:23], v[108:111], v[132:135]\n"
      "ds_write_b128 v245, v[212:215] offset:24576\n"
      "v_mfma_f32_16x16x32_bf16 v[132:135], v[140:143], v[112:115], v[132:135]\n"
      "global_load_lds_dwordx4 v236, s[52:53] offset:512 nt\n"
      "v_mfma_f32_16x16x32_bf16 v[132:135], v[156:159], v[116:119], v[132:135]\n"
      "s_barrier\n"
      "v_mfma_f32_16x16x32_bf16 v[136:139], v[160:163], v[36:39], v[20:23]\n"
      "ds_read_b128 v[36:39], v232 offset:256\n"
      "v_mfma_f32_16x16x32_bf16 v[136:139], v[164:167], v[40:43], v[136:139]\n"
      "ds_read_b128 v[40:43], v233 offset:256\n"
      "v_mfma_f32_16x16x32_bf16 v[136:139], v[180:183], v[44:47], v[136:139]\n"
      "ds_read_b128 v[44:47], v234 offset:256\n"
      "v_mfma_f32_16x16x32_bf16 v[136:139], v[184:187], v[60:63], v[136:139]\n"
      "ds_read_b128 v[60:63], v235 offset:256\n"
      "v_cvt_pk_bf16_f32 v208, v132, v133\n"
      "v_mfma_f32_16x16x32_bf16 v[136:139], v[188:191], v[64:67], v[136:139]\n"
      "ds_read_b128 v[64:67], v232 offset:8448\n"
      "v_cvt_pk_bf16_f32 v209, v134, v135\n"
      "v_mfma_f32_16x16x32_bf16 v[136:139], v[204:207], v[68:71], v[136:139]\n"
      "ds_read_b128 v[68:71], v233 offset:8448\n"
      "v_mfma_f32_16x16x32_bf16 v[136:139], a[24:27], v[84:87], v[136:139]\n"
      "ds_read_b128 v[84:87], v234 offset:8448\n"
      "v_mfma_f32_16x16x32_bf16 v[136:139], a[28:31], v[88:91], v[136:139]\n"
      "ds_read_b128 v[88:91], v235 offset:8448\n"
      "v_mfma_f32_16x16x32_bf16 v[136:139], a[32:35], v[92:95], v[136:139]\n"
      "ds_read_b128 v[92:95], v232 offset:16640\n"
      "v_mfma_f32_16x16x32_bf16 v[136:139], a[36:39], v[108:111], v[136:139]\n"
      "ds_read_b128 v[108:111], v233 offset:16640\n"
      "v_mfma_f32_16x16x32_bf16 v[136:139], a[40:43], v[112:115], v[136:139]\n"
      "ds_read_b128 v[112:115], v234 offset:16640\n"
      "v_mfma_f32_16x16x32_bf16 v[136:139], a[44:47], v[116:119], v[136:139]\n"
      "ds_read_b128 v[116:119], v235 offset:16640\n"
      "s_waitcnt lgkmcnt(8)\n"
      "v_mfma_f32_16x16x32_bf16 v[132:135], a[240:243], v[36:39], v[0:3]\n"
      "s_cbranch_vccnz LT3\n"
      "LS3:\n"
      "v_mfma_f32_16x16x32_bf16 v[132:135], a[244:247], v[40:43], v[132:135]\n"
      "v_mfma_f32_16x16x32_bf16 v[132:135], a[248:251], v[44:47], v[132:135]\n"
      "v_mfma_f32_16x16x32_bf16 v[132:135], a[252:255], v[60:63], v[132:135]\n"
      "v_cvt_pk_bf16_f32 v210, v136, v137\n"
      "s_waitcnt lgkmcnt(4)\n"
      "v_mfma_f32_16x16x32_bf16 v[132:135], a[48:51], v[64:67], v[132:135]\n"
      "v_cvt_pk_bf16_f32 v211, v138, v139\n"
      "v_mfma_f32_16x16x32_bf16 v[132:135], a[52:55], v[68:71], v[132:135]\n"
      "v_mfma_f32_16x16x32_bf16 v[132:135], a[56:59], v[84:87], v[132:135]\n"
      "ds_read_b128 v[216:219], v246 offset:12288\n"
      "v_mfma_f32_16x16x32_bf16 v[132:135], a[60:63], v[88:91], v[132:135]\n"
      "ds_read_b128 v[220:223], v246 offset:13312\n"
      "s_waitcnt lgkmcnt(2)\n"
      "v_mfma_f32_16x16x32_bf16 v[132:135], a[64:67], v[92:95], v[132:135]\n"
      "ds_read_b128 v[224:227], v246 offset:14336\n"
      "v_mfma_f32_16x16x32_bf16 v[132:135], a[68:71], v[108:111], v[132:135]\n"
      "ds_write_b128 v244, v[208:211] offset:24704\n"
      "v_mfma_f32_16x16x32_bf16 v[132:135], a[72:75], v[112:115], v[132:135]\n"
      "v_mfma_f32_16x16x32_bf16 v[132:135], a[76:79], v[116:119], v[132:135]\n"
      "v_mfma_f32_16x16x32_bf16 v[136:139], a[80:83], v[36:39], v[4:7]\n"
      "v_mfma_f32_16x16x32_bf16 v[136:139], a[84:87], v[40:43], v[136:139]\n"
      "v_mfma_f32_16x16x32_bf16 v[136:139], a[88:91], v[44:47], v[136:139]\n"
      "v_mfma_f32_16x16x32_bf16 v[136:139], a[92:95], v[60:63], v[136:139]\n"
      "v_cvt_pk_bf16_f32 v208, v132, v133\n"
      "v_mfma_f32_16x16x32_bf16 v[136:139], a[96:99], v[64:67], v[136:139]\n"
      "v_cvt_pk_bf16_f32 v209, v134, v135\n"
      "v_mfma_f32_16x16x32_bf16 v[136:139], a[100:103], v[68:71], v[136:139]\n"
      "s_waitcnt lgkmcnt(3)\n"
      "v_mfma_f32_16x16x32_bf16 v[136:139], a[104:107], v[84:87], v[136:139]\n"
      "global_store_dwordx4 v247, v[216:219], s[54:55] offset:768 sc1\n"
      "v_mfma_f32_16x16x32_bf16 v[136:139], a[108:111], v[88:91], v[136:139]\n"
      "s_add_u32 m0, s17, 0x13000\n"
      "v_mfma_f32_16x16x32_bf16 v[136:139], a[112:115], v[92:95], v[136:139]\n"
      "global_load_lds_dwordx4 v237, s[52:53] offset:0 nt\n"
      "v_mfma_f32_16x16x32_bf16 v[136:139], a[116:119], v[108:111], v[136:139]\n"
      "v_mfma_f32_16x16x32_bf16 v[136:139], a[120:123], v[112:115], v[136:139]\n"
      "v_mfma_f32_16x16x32_bf16 v[136:139], a[124:127], v[116:119], v[136:139]\n"
      "v_mfma_f32_16x16x32_bf16 v[132:135], a[128:131], v[36:39], v[8:11]\n"
      "s_cbranch_vccnz LT4\n"
      "LS4:\n"
      "v_mfma_f32_16x16x32_bf16 v[132:135], a[132:135], v[40:43], v[132:135]\n"
      "v_mfma_f32_16x16x32_bf16 v[132:135], a[136:139], v[44:47], v[132:135]\n"
      "v_mfma_f32_16x16x32_bf16 v[132:135], a[140:143], v[60:63], v[132:135]\n"
      "v_cvt_pk_bf16_f32 v210, v136, v137\n"
      "v_mfma_f32_16x16x32_bf16 v[132:135], a[144:147], v[64:67], v[132:135]\n"
      "v_cvt_pk_bf16_f32 v211, v138, v139\n"
      "v_mfma_f32_16x16x32_bf16 v[132:135], a[148:151], v[68:71], v[132:135]\n"
      "v_mfma_f32_16x16x32_bf16 v[132:135], a[152:155], v[84:87], v[132:135]\n"
      "s_waitcnt lgkmcnt(2)\n"
      "v_mfma_f32_16x16x32_bf16 v[132:135], a[156:159], v[88:91], v[132:135]\n"
      "global_store_dwordx4 v252, v[220:223], s[54:55] offset:768 sc1\n"
      "v_mfma_f32_16x16x32_bf16 v[132:135], a[160:163], v[92:95], v[132:135]\n"
      "s_add_u32 m0, s17, 0x14f00\n"
      "v_mfma_f32_16x16x32_bf16 v[132:135], a[164:167], v[108:111], v[132:135]\n"
      "ds_write_b128 v244, v[208:211] offset:0\n"
      "v_mfma_f32_16x16x32_bf16 v[132:135], a[168:171], v[112:115], v[132:135]\n"
      "global_load_lds_dwordx4 v237, s[52:53] offset:256 nt\n"
      "v_mfma_f32_16x16x32_bf16 v[132:135], a[172:175], v[116:119], v[132:135]\n"
      "v_mfma_f32_16x16x32_bf16 v[136:139], a[176:179], v[36:39], v[12:15]\n"
      "v_mfma_f32_16x16x32_bf16 v[136:139], a[180:183], v[40:43], v[136:139]\n"
      "v_mfma_f32_16x16x32_bf16 v[136:139], a[184:187], v[44:47], v[136:139]\n"
      "v_mfma_f32_16x16x32_bf16 v[136:139], a[188:191], v[60:63], v[136:139]\n"
      "v_cvt_pk_bf16_f32 v212, v132, v133\n"
      "v_mfma_f32_16x16x32_bf16 v[136:139], a[192:195], v[64:67], v[136:139]\n"
      "v_cvt_pk_bf16_f32 v213, v134, v135\n"
      "v_mfma_f32_16x16x32_bf16 v[136:139], a[196:199], v[68:71], v[136:139]\n"
      "v_mfma_f32_16x16x32_bf16 v[136:139], a[200:203], v[84:87], v[136:139]\n"
      "v_mfma_f32_16x16x32_bf16 v[136:139], a[204:207], v[88:91], v[136:139]\n"
      "s_waitcnt lgkmcnt(2)\n"
      "v_mfma_f32_16x16x32_bf16 v[136:139], a[208:211], v[92:95], v[136:139]\n"
      "global_store_dwordx4 v253, v[224:227], s[54:55] offset:768 sc1\n"
      "v_mfma_f32_16x16x32_bf16 v[136:139], a[212:215], v[108:111], v[136:139]\n"
      "s_add_u32 m0, s17, 0x16e00\n"
      "v_mfma_f32_16x16x32_bf16 v[136:139], a[216:219], v[112:115], v[136:139]\n"
      "global_load_lds_dwordx4 v237, s[52:53] offset:512 nt\n"
      "v_mfma_f32_16x16x32_bf16 v[136:139], a[220:223], v[116:119], v[136:139]\n"
      "v_mfma_f32_16x16x32_bf16 v[132:135], a[224:227], v[36:39], v[16:19]\n"
      "s_cbranch_vccnz LT5\n"
      "LS5:\n"
      "v_mfma_f32_16x16x32_bf16 v[132:135], a[228:231], v[40:43], v[132:135]\n"
      "v_mfma_f32_16x16x32_bf16 v[132:135], a[232:235], v[44:47], v[132:135]\n"
      "v_mfma_f32_16x16x32_bf16 v[132:135], a[236:239], v[60:63], v[132:135]\n"
      "s_memrealtime s[84:85]\n"
      "v_mfma_f32_16x16x32_bf16 v[132:135], a[0:3], v[64:67], v[132:135]\n"
      "v_cvt_pk_bf16_f32 v214, v136, v137\n"
      "v_mfma_f32_16x16x32_bf16 v[132:135], a[4:7], v[68:71], v[132:135]\n"
      "v_cvt_pk_bf16_f32 v215, v138, v139\n"
      "v_mfma_f32_16x16x32_bf16 v[132:135], a[8:11], v[84:87], v[132:135]\n"
      "v_mfma_f32_16x16x32_bf16 v[132:135], a[12:15], v[88:91], v[132:135]\n"
      "s_add_u32 s83, s83, s86\n"
      "v_mfma_f32_16x16x32_bf16 v[132:135], a[16:19], v[92:95], v[132:135]\n"
      "v_mfma_f32_16x16x32_bf16 v[132:135], a[20:23], v[108:111], v[132:135]\n"
      "v_mfma_f32_16x16x32_bf16 v[132:135], v[140:143], v[112:115], v[132:135]\n"
      "s_waitcnt lgkmcnt(0)\n"
      "s_sub_u32 s84, s84, s83\n"
      "v_mfma_f32_16x16x32_bf16 v[132:135], v[156:159], v[116:119], v[132:135]\n"
      "s_cmp_lt_i32 s84, 0\n"
      "s_cselect_b64 vcc, -1, 0\n"
      "s_waitcnt vmcnt(24)\n"
      "s_barrier\n"
      "v_mfma_f32_16x16x32_bf16 v[136:139], v[160:163], v[36:39], v[20:23]\n"
      "ds_read_b128 v[36:39], v232 offset:24576\n"
      "ds_write_b128 v245, v[212:215] offset:0\n"
      "v_mfma_f32_16x16x32_bf16 v[136:139], v[164:167], v[40:43], v[136:139]\n"
      "ds_read_b128 v[40:43], v233 offset:24576\n"
      "s_lshl_b32 s79, s93, 1\n"
      "v_mfma_f32_16x16x32_bf16 v[136:139], v[180:183], v[44:47], v[136:139]\n"
      "ds_read_b128 v[44:47], v234 offset:24576\n"
      "v_mad_i32_i24 v252, v251, s79, v252\n"
      "v_mfma_f32_16x16x32_bf16 v[136:139], v[184:187], v[60:63], v[136:139]\n"
      "ds_read_b128 v[60:63], v235 offset:24576\n"
      "v_cvt_pk_bf16_f32 v208, v132, v133\n"
      "v_mfma_f32_16x16x32_bf16 v[136:139], v[188:191], v[64:67], v[136:139]\n"
      "ds_read_b128 v[64:67], v232 offset:32768\n"
      "v_cvt_pk_bf16_f32 v209, v134, v135\n"
      "v_mfma_f32_16x16x32_bf16 v[136:139], v[204:207], v[68:71], v[136:139]\n"
      "ds_read_b128 v[68:71], v233 offset:32768\n"
      "s_mul_i32 s79, s79, s100\n"
      "v_mfma_f32_16x16x32_bf16 v[136:139], a[24:27], v[84:87], v[136:139]\n"
      "ds_read_b128 v[84:87], v234 offset:32768\n"
      "v_add_u32 v247, s79, v247\n"
      "v_mfma_f32_16x16x32_bf16 v[136:139], a[28:31], v[88:91], v[136:139]\n"
      "ds_read_b128 v[88:91], v235 offset:32768\n"
      "s_mul_i32 s79, s79, s101\n"
      "v_mfma_f32_16x16x32_bf16 v[136:139], a[32:35], v[92:95], v[136:139]\n"
      "ds_read_b128 v[92:95], v232 offset:40960\n"
      "v_add_u32 v253, s79, v253\n"
      "v_mfma_f32_16x16x32_bf16 v[136:139], a[36:39], v[108:111], v[136:139]\n"
      "ds_read_b128 v[108:111], v233 offset:40960\n"
      "v_mfma_f32_16x16x32_bf16 v[136:139], a[40:43], v[112:115], v[136:139]\n"
      "ds_read_b128 v[112:115], v234 offset:40960\n"
      "v_mfma_f32_16x16x32_bf16 v[136:139], a[44:47], v[116:119], v[136:139]\n"
      "ds_read_b128 v[116:119], v235 offset:40960\n"
      "s_waitcnt lgkmcnt(8)\n"
      "v_mfma_f32_16x16x32_bf16 v[132:135], a[240:243], v[36:39], v[0:3]\n"
      "s_cbranch_vccnz LT6\n"
      "LS6:\n"
      "v_mfma_f32_16x16x32_bf16 v[132:135], a[244:247], v[40:43], v[132:135]\n"
      "s_add_u32 s54, s30, s36\n"
      "s_addc_u32 s55, s31, 0\n"
      "v_mfma_f32_16x16x32_bf16 v[132:135], a[248:251], v[44:47], v[132:135]\n"
      "v_mfma_f32_16x16x32_bf16 v[132:135], a[252:255], v[60:63], v[132:135]\n"
      "v_cvt_pk_bf16_f32 v210, v136, v137\n"
      "s_waitcnt lgkmcnt(4)\n"
      "v_mfma_f32_16x16x32_bf16 v[132:135], a[48:51], v[64:67], v[132:135]\n"
      "v_cvt_pk_bf16_f32 v211, v138, v139\n"
      "v_mfma_f32_16x16x32_bf16 v[132:135], a[52:55], v[68:71], v[132:135]\n"
      "s_mul_i32 s78, s95, s19\n"
      "v_mfma_f32_16x16x32_bf16 v[132:135], a[56:59], v[84:87], v[132:135]\n"
      "ds_read_b128 v[216:219], v246 offset:24576\n"
      "v_add_u32 v236, s78, v236\n"
      "v_mfma_f32_16x16x32_bf16 v[132:135], a[60:63], v[88:91], v[132:135]\n"
      "ds_read_b128 v[220:223], v246 offset:25600\n"
      "s_waitcnt lgkmcnt(2)\n"
      "v_mfma_f32_16x16x32_bf16 v[132:135], a[64:67], v[92:95], v[132:135]\n"
      "ds_read_b128 v[224:227], v246 offset:26624\n"
      "v_mfma_f32_16x16x32_bf16 v[132:135], a[68:71], v[108:111], v[132:135]\n"
      "ds_write_b128 v244, v[208:211] offset:128\n"
      "v_mfma_f32_16x16x32_bf16 v[132:135], a[72:75], v[112:115], v[132:135]\n"
      "s_mul_i32 s78, s78, s99\n"
      "v_mfma_f32_16x16x32_bf16 v[132:135], a[76:79], v[116:119], v[132:135]\n"
      "v_add_u32 v237, s78, v237\n"
      "v_mfma_f32_16x16x32_bf16 v[136:139], a[80:83], v[36:39], v[4:7]\n"
      "v_mfma_f32_16x16x32_bf16 v[136:139], a[84:87], v[40:43], v[136:139]\n"
      "v_mfma_f32_16x16x32_bf16 v[136:139], a[88:91], v[44:47], v[136:139]\n"
      "v_mfma_f32_16x16x32_bf16 v[136:139], a[92:95], v[60:63], v[136:139]\n"
      "v_cvt_pk_bf16_f32 v208, v132, v133\n"
      "v_mfma_f32_16x16x32_bf16 v[136:139], a[96:99], v[64:67], v[136:139]\n"
      "v_cvt_pk_bf16_f32 v209, v134, v135\n"
      "v_mfma_f32_16x16x32_bf16 v[136:139], a[100:103], v[68:71], v[136:139]\n"
      "s_waitcnt lgkmcnt(3)\n"
      "v_mfma_f32_16x16x32_bf16 v[136:139], a[104:107], v[84:87], v[136:139]\n"
      "global_store_dwordx4 v247, v[216:219], s[54:55] offset:0 sc1\n"
      "v_mfma_f32_16x16x32_bf16 v[136:139], a[108:111], v[88:91], v[136:139]\n"
      "s_add_u32 s52, s20, s38\n"
      "s_addc_u32 s53, s21, 0\n"
      "v_mfma_f32_16x16x32_bf16 v[136:139], a[112:115], v[92:95], v[136:139]\n"
      "s_add_u32 m0, s17, 0x0\n"
      "v_mfma_f32_16x16x32_bf16 v[136:139], a[116:119], v[108:111], v[136:139]\n"
      "global_load_lds_dwordx4 v236, s[52:53] offset:0 nt\n"
      "v_mfma_f32_16x16x32_bf16 v[136:139], a[120:123], v[112:115], v[136:139]\n"
      "v_mfma_f32_16x16x32_bf16 v[136:139], a[124:127], v[116:119], v[136:139]\n"
      "v_mfma_f32_16x16x32_bf16 v[132:135], a[128:131], v[36:39], v[8:11]\n"
      "s_cbranch_vccnz LT7\n"
      "LS7:\n"
      "v_mfma_f32_16x16x32_bf16 v[132:135], a[132:135], v[40:43], v[132:135]\n"
      "v_mfma_f32_16x16x32_bf16 v[132:135], a[136:139], v[44:47], v[132:135]\n"
      "v_mfma_f32_16x16x32_bf16 v[132:135], a[140:143], v[60:63], v[132:135]\n"
      "v_cvt_pk_bf16_f32 v210, v136, v137\n"
      "v_mfma_f32_16x16x32_bf16 v[132:135], a[144:147], v[64:67], v[132:135]\n"
      "v_cvt_pk_bf16_f32 v211, v138, v139\n"
      "v_mfma_f32_16x16x32_bf16 v[132:135], a[148:151], v[68:71], v[132:135]\n"
      "v_mfma_f32_16x16x32_bf16 v[132:135], a[152:155], v[84:87], v[132:135]\n"
      "s_waitcnt lgkmcnt(2)\n"
      "v_mfma_f32_16x16x32_bf16 v[132:135], a[156:159], v[88:91], v[132:135]\n"
      "global_store_dwordx4 v252, v[220:223], s[54:55] offset:0 sc1\n"
      "v_mfma_f32_16x16x32_bf16 v[132:135], a[160:163], v[92:95], v[132:135]\n"
      "s_add_u32 m0, s17, 0x1f00\n"
      "v_mfma_f32_16x16x32_bf16 v[132:135], a[164:167], v[108:111], v[132:135]\n"
      "ds_write_b128 v244, v[208:211] offset:12288\n"
      "v_mfma_f32_16x16x32_bf16 v[132:135], a[168:171], v[112:115], v[132:135]\n"
      "global_load_lds_dwordx4 v236, s[52:53] offset:256 nt\n"
      "v_mfma_f32_16x16x32_bf16 v[132:135], a[172:175], v[116:119], v[132:135]\n"
      "v_mfma_f32_16x16x32_bf16 v[136:139], a[176:179], v[36:39], v[12:15]\n"
      "v_mfma_f32_16x16x32_bf16 v[136:139], a[180:183], v[40:43], v[136:139]\n"
      "v_mfma_f32_16x16x32_bf16 v[136:139], a[184:187], v[44:47], v[136:139]\n"
      "v_mfma_f32_16x16x32_bf16 v[136:139], a[188:191], v[60:63], v[136:139]\n"
      "v_cvt_pk_bf16_f32 v212, v132, v133\n"
      "v_mfma_f32_16x16x32_bf16 v[136:139], a[192:195], v[64:67], v[136:139]\n"
      "v_cvt_pk_bf16_f32 v213, v134, v135\n"
      "v_mfma_f32_16x16x32_bf16 v[136:139], a[196:199], v[68:71], v[136:139]\n"
      "v_mfma_f32_16x16x32_bf16 v[136:139], a[200:203], v[84:87], v[136:139]\n"
      "v_mfma_f32_16x16x32_bf16 v[136:139], a[204:207], v[88:91], v[136:139]\n"
      "s_waitcnt lgkmcnt(2)\n"
      "v_mfma_f32_16x16x32_bf16 v[136:139], a[208:211], v[92:95], v[136:139]\n"
      "global_store_dwordx4 v253, v[224:227], s[54:55] offset:0 sc1\n"
      "v_mfma_f32_16x16x32_bf16 v[136:139], a[212:215], v[108:111], v[136:139]\n"
      "s_add_u32 m0, s17, 0x3e00\n"
      "v_mfma_f32_16x16x32_bf16 v[136:139], a[216:219], v[112:115], v[136:139]\n"
      "global_load_lds_dwordx4 v236, s[52:53] offset:512 nt\n"
      "v_mfma_f32_16x16x32_bf16 v[136:139], a[220:223], v[116:119], v[136:139]\n"
      "v_mfma_f32_16x16x32_bf16 v[132:135], a[224:227], v[36:39], v[16:19]\n"
      "s_cbranch_vccnz LT8\n"
      "LS8:\n"
      "v_mfma_f32_16x16x32_bf16 v[132:135], a[228:231], v[40:43], v[132:135]\n"
      "v_mfma_f32_16x16x32_bf16 v[132:135], a[232:235], v[44:47], v[132:135]\n"
      "v_mfma_f32_16x16x32_bf16 v[132:135], a[236:239], v[60:63], v[132:135]\n"
      "v_cvt_pk_bf16_f32 v214, v136, v137\n"
      "v_mfma_f32_16x16x32_bf16 v[132:135], a[0:3], v[64:67], v[132:135]\n"
      "v_cvt_pk_bf16_f32 v215, v138, v139\n"
      "v_mfma_f32_16x16x32_bf16 v[132:135], a[4:7], v[68:71], v[132:135]\n"
      "v_mfma_f32_16x16x32_bf16 v[132:135], a[8:11], v[84:87], v[132:135]\n"
      "v_mfma_f32_16x16x32_bf16 v[132:135], a[12:15], v[88:91], v[132:135]\n"
      "v_mfma_f32_16x16x32_bf16 v[132:135], a[16:19], v[92:95], v[132:135]\n"
      "v_mfma_f32_16x16x32_bf16 v[132:135], a[20:23], v[108:111], v[132:135]\n"
      "ds_write_b128 v245, v[212:215] offset:12288\n"
      "v_mfma_f32_16x16x32_bf16 v[132:135], v[140:143], v[112:115], v[132:135]\n"
      "v_mfma_f32_16x16x32_bf16 v[132:135], v[156:159], v[116:119], v[132:135]\n"
      "s_waitcnt lgkmcnt(2)\n"
      "s_barrier\n"
      "v_mfma_f32_16x16x32_bf16 v[136:139], v[160:163], v[36:39], v[20:23]\n"
      "ds_read_b128 v[36:39], v232 offset:24832\n"
      "v_mfma_f32_16x16x32_bf16 v[136:139], v[164:167], v[40:43], v[136:139]\n"
      "ds_read_b128 v[40:43], v233 offset:24832\n"
      "v_mfma_f32_16x16x32_bf16 v[136:139], v[180:183], v[44:47], v[136:139]\n"
      "ds_read_b128 v[44:47], v234 offset:24832\n"
      "v_mfma_f32_16x16x32_bf16 v[136:139], v[184:187], v[60:63], v[136:139]\n"
      "ds_read_b128 v[60:63], v235 offset:24832\n"
      "v_cvt_pk_bf16_f32 v208, v132, v133\n"
      "v_mfma_f32_16x16x32_bf16 v[136:139], v[188:191], v[64:67], v[136:139]\n"
      "ds_read_b128 v[64:67], v232 offset:33024\n"
      "v_cvt_pk_bf16_f32 v209, v134, v135\n"
      "v_mfma_f32_16x16x32_bf16 v[136:139], v[204:207], v[68:71], v[136:139]\n"
      "ds_read_b128 v[68:71], v233 offset:33024\n"
      "v_mfma_f32_16x16x32_bf16 v[136:139], a[24:27], v[84:87], v[136:139]\n"
      "ds_read_b128 v[84:87], v234 offset:33024\n"
      "v_mfma_f32_16x16x32_bf16 v[136:139], a[28:31], v[88:91], v[136:139]\n"
      "ds_read_b128 v[88:91], v235 offset:33024\n"
      "v_mfma_f32_16x16x32_bf16 v[136:139], a[32:35], v[92:95], v[136:139]\n"
      "ds_read_b128 v[92:95], v232 offset:41216\n"
      "v_mfma_f32_16x16x32_bf16 v[136:139], a[36:39], v[108:111], v[136:139]\n"
      "ds_read_b128 v[108:111], v233 offset:41216\n"
      "v_mfma_f32_16x16x32_bf16 v[136:139], a[40:43], v[112:115], v[136:139]\n"
      "ds_read_b128 v[112:115], v234 offset:41216\n"
      "v_mfma_f32_16x16x32_bf16 v[136:139], a[44:47], v[116:119], v[136:139]\n"
      "ds_read_b128 v[116:119], v235 offset:41216\n"
      "s_waitcnt lgkmcnt(8)\n"
      "v_mfma_f32_16x16x32_bf16 v[132:135], a[240:243], v[36:39], v[0:3]\n"
      "v_add_u32 v232, 0xc000, v232\n"
      "s_cbranch_vccnz LT9\n"
      "LS9:\n"
      "v_mfma_f32_16x16x32_bf16 v[132:135], a[244:247], v[40:43], v[132:135]\n"
      "v_add_u32 v233, 0xc000, v233\n"
      "v_mfma_f32_16x16x32_bf16 v[132:135], a[248:251], v[44:47], v[132:135]\n"
      "v_add_u32 v234, 0xc000, v234\n"
      "v_mfma_f32_16x16x32_bf16 v[132:135], a[252:255], v[60:63], v[132:135]\n"
      "v_add_u32 v235, 0xc000, v235\n"
      "s_waitcnt lgkmcnt(4)\n"
      "v_mfma_f32_16x16x32_bf16 v[132:135], a[48:51], v[64:67], v[132:135]\n"
      "v_cvt_pk_bf16_f32 v210, v136, v137\n"
      "v_mfma_f32_16x16x32_bf16 v[132:135], a[52:55], v[68:71], v[132:135]\n"
      "v_cvt_pk_bf16_f32 v211, v138, v139\n"
      "v_mfma_f32_16x16x32_bf16 v[132:135], a[56:59], v[84:87], v[132:135]\n"
      "ds_read_b128 v[216:219], v246 offset:0\n"
      "v_mfma_f32_16x16x32_bf16 v[132:135], a[60:63], v[88:91], v[132:135]\n"
      "ds_read_b128 v[220:223], v246 offset:1024\n"
      "s_waitcnt lgkmcnt(2)\n"
      "v_mfma_f32_16x16x32_bf16 v[132:135], a[64:67], v[92:95], v[132:135]\n"
      "ds_read_b128 v[224:227], v246 offset:2048\n"
      "v_mfma_f32_16x16x32_bf16 v[132:135], a[68:71], v[108:111], v[132:135]\n"
      "v_lshlrev_b32 v0, 16, v120\n"
      "v_mfma_f32_16x16x32_bf16 v[132:135], a[72:75], v[112:115], v[132:135]\n"
      "ds_write_b128 v244, v[208:211] offset:12416\n"
      "v_mfma_f32_16x16x32_bf16 v[132:135], a[76:79], v[116:119], v[132:135]\n"
      "v_and_b32 v1, 0xffff0000, v120\n"
      "v_mfma_f32_16x16x32_bf16 v[136:139], a[80:83], v[36:39], v[4:7]\n"
      "v_lshlrev_b32 v2, 16, v121\n"
      "v_mfma_f32_16x16x32_bf16 v[136:139], a[84:87], v[40:43], v[136:139]\n"
      "v_and_b32 v3, 0xffff0000, v121\n"
      "v_mfma_f32_16x16x32_bf16 v[136:139], a[88:91], v[44:47], v[136:139]\n"
      "v_mfma_f32_16x16x32_bf16 v[136:139], a[92:95], v[60:63], v[136:139]\n"
      "v_cvt_pk_bf16_f32 v208, v132, v133\n"
      "v_mfma_f32_16x16x32_bf16 v[136:139], a[96:99], v[64:67], v[136:139]\n"
      "v_cvt_pk_bf16_f32 v209, v134, v135\n"
      "v_mfma_f32_16x16x32_bf16 v[136:139], a[100:103], v[68:71], v[136:139]\n"
      "s_waitcnt lgkmcnt(3)\n"
      "v_mfma_f32_16x16x32_bf16 v[136:139], a[104:107], v[84:87], v[136:139]\n"
      "global_store_dwordx4 v247, v[216:219], s[54:55] offset:768 sc1\n"
      "LPD12:\n"
      "s_cbranch_execz LPD15\n"
      "v_mfma_f32_16x16x32_bf16 v[136:139], a[108:111], v[88:91], v[136:139]\n"
      "s_add_u32 m0, s17, 0x1000\n"
      "v_mfma_f32_16x16x32_bf16 v[136:139], a[112:115], v[92:95], v[136:139]\n"
      "global_load_lds_dwordx4 v237, s[52:53] offset:0 nt\n"
      "v_mfma_f32_16x16x32_bf16 v[136:139], a[116:119], v[108:111], v[136:139]\n"
      "v_lshlrev_b32 v4, 16, v122\n"
      "v_mfma_f32_16x16x32_bf16 v[136:139], a[120:123], v[112:115], v[136:139]\n"
      "v_and_b32 v5, 0xffff0000, v122\n"
      "v_mfma_f32_16x16x32_bf16 v[136:139], a[124:127], v[116:119], v[136:139]\n"
      "v_lshlrev_b32 v6, 16, v123\n"
      "v_mfma_f32_16x16x32_bf16 v[132:135], a[128:131], v[36:39], v[8:11]\n"
      "v_and_b32 v7, 0xffff0000, v123\n"
      "v_mfma_f32_16x16x32_bf16 v[132:135], a[132:135], v[40:43], v[132:135]\n"
      "s_cbranch_vccnz LT10\n"
      "LS10:\n"
      "v_mfma_f32_16x16x32_bf16 v[132:135], a[136:139], v[44:47], v[132:135]\n"
      "v_mfma_f32_16x16x32_bf16 v[132:135], a[140:143], v[60:63], v[132:135]\n"
      "v_cvt_pk_bf16_f32 v210, v136, v137\n"
      "v_mfma_f32_16x16x32_bf16 v[132:135], a[144:147], v[64:67], v[132:135]\n"
      "v_cvt_pk_bf16_f32 v211, v138, v139\n"
      "v_mfma_f32_16x16x32_bf16 v[132:135], a[148:151], v[68:71], v[132:135]\n"
      "v_lshlrev_b32 v8, 16, v124\n"
      "v_mfma_f32_16x16x32_bf16 v[132:135], a[152:155], v[84:87], v[132:135]\n"
      "s_waitcnt lgkmcnt(2)\n"
      "v_mfma_f32_16x16x32_bf16 v[132:135], a[156:159], v[88:91], v[132:135]\n"
      "global_store_dwordx4 v252, v[220:223], s[54:55] offset:768 sc1\n"
      "v_mfma_f32_16x16x32_bf16 v[132:135], a[160:163], v[92:95], v[132:135]\n"
      "s_add_u32 m0, s17, 0x2f00\n"
      "v_mfma_f32_16x16x32_bf16 v[132:135], a[164:167], v[108:111], v[132:135]\n"
      "ds_write_b128 v244, v[208:211] offset:24576\n"
      "v_mfma_f32_16x16x32_bf16 v[132:135], a[168:171], v[112:115], v[132:135]\n"
      "global_load_lds_dwordx4 v237, s[52:53] offset:256 nt\n"
      "v_mfma_f32_16x16x32_bf16 v[132:135], a[172:175], v[116:119], v[132:135]\n"
      "v_and_b32 v9, 0xffff0000, v124\n"
      "v_mfma_f32_16x16x32_bf16 v[136:139], a[176:179], v[36:39], v[12:15]\n"
      "v_lshlrev_b32 v10, 16, v125\n"
      "v_mfma_f32_16x16x32_bf16 v[136:139], a[180:183], v[40:43], v[136:139]\n"
      "v_and_b32 v11, 0xffff0000, v125\n"
      "v_mfma_f32_16x16x32_bf16 v[136:139], a[184:187], v[44:47], v[136:139]\n"
      "v_mfma_f32_16x16x32_bf16 v[136:139], a[188:191], v[60:63], v[136:139]\n"
      "v_cvt_pk_bf16_f32 v212, v132, v133\n"
      "v_mfma_f32_16x16x32_bf16 v[136:139], a[192:195], v[64:67], v[136:139]\n"
      "v_cvt_pk_bf16_f32 v213, v134, v135\n"
      "v_mfma_f32_16x16x32_bf16 v[136:139], a[196:199], v[68:71], v[136:139]\n"
      "v_lshlrev_b32 v12, 16, v126\n"
      "v_mfma_f32_16x16x32_bf16 v[136:139], a[200:203], v[84:87], v[136:139]\n"
      "v_and_b32 v13, 0xffff0000, v126\n"
      "v_mfma_f32_16x16x32_bf16 v[136:139], a[204:207], v[88:91], v[136:139]\n"
      "s_waitcnt lgkmcnt(2)\n"
      "v_mfma_f32_16x16x32_bf16 v[136:139], a[208:211], v[92:95], v[136:139]\n"
      "global_store_dwordx4 v253, v[224:227], s[54:55] offset:768 sc1\n"
      "v_mfma_f32_16x16x32_bf16 v[136:139], a[212:215], v[108:111], v[136:139]\n"
      "s_add_u32 m0, s17, 0x4e00\n"
      "v_mfma_f32_16x16x32_bf16 v[136:139], a[216:219], v[112:115], v[136:139]\n"
      "global_load_lds_dwordx4 v237, s[52:53] offset:512 nt\n"
      "v_mfma_f32_16x16x32_bf16 v[136:139], a[220:223], v[116:119], v[136:139]\n"
      "v_lshlrev_b32 v14, 16, v127\n"
      "v_mfma_f32_16x16x32_bf16 v[132:135], a[224:227], v[36:39], v[16:19]\n"
      "v_and_b32 v15, 0xffff0000, v127\n"
      "v_mfma_f32_16x16x32_bf16 v[132:135], a[228:231], v[40:43], v[132:135]\n"
      "s_cbranch_vccnz LT11\n"
      "LS11:\n"
      "v_mfma_f32_16x16x32_bf16 v[132:135], a[232:235], v[44:47], v[132:135]\n"
      "v_mfma_f32_16x16x32_bf16 v[132:135], a[236:239], v[60:63], v[132:135]\n"
      "s_memrealtime s[84:85]\n"
      "v_mfma_f32_16x16x32_bf16 v[132:135], a[0:3], v[64:67], v[132:135]\n"
      "v_cvt_pk_bf16_f32 v214, v136, v137\n"
      "v_mfma_f32_16x16x32_bf16 v[132:135], a[4:7], v[68:71], v[132:135]\n"
      "v_cvt_pk_bf16_f32 v215, v138, v139\n"
      "v_mfma_f32_16x16x32_bf16 v[132:135], a[8:11], v[84:87], v[132:135]\n"
      "v_lshlrev_b32 v16, 16, v128\n"
      "v_mfma_f32_16x16x32_bf16 v[132:135], a[12:15], v[88:91], v[132:135]\n"
      "s_add_u32 s83, s83, s86\n"
      "v_mfma_f32_16x16x32_bf16 v[132:135], a[16:19], v[92:95], v[132:135]\n"
      "v_and_b32 v17, 0xffff0000, v128\n"
      "v_mfma_f32_16x16x32_bf16 v[132:135], a[20:23], v[108:111], v[132:135]\n"
      "v_lshlrev_b32 v18, 16, v129\n"
      "v_mfma_f32_16x16x32_bf16 v[132:135], v[140:143], v[112:115], v[132:135]\n"
      "s_waitcnt lgkmcnt(0)\n"
      "s_sub_u32 s84, s84, s83\n"
      "v_mfma_f32_16x16x32_bf16 v[132:135], v[156:159], v[116:119], v[132:135]\n"
      "s_cmp_lt_i32 s84, 0\n"
      "s_cselect_b64 vcc, -1, 0\n"
      "s_waitcnt vmcnt(24)\n"
      "s_barrier\n"
      "v_mfma_f32_16x16x32_bf16 v[136:139], v[160:163], v[36:39], v[20:23]\n"
      "ds_read_b128 v[36:39], v232 offset:0\n"
      "ds_write_b128 v245, v[212:215] offset:24576\n"
      "v_mfma_f32_16x16x32_bf16 v[136:139], v[164:167], v[40:43], v[136:139]\n"
      "ds_read_b128 v[40:43], v233 offset:0\n"
      "v_mfma_f32_16x16x32_bf16 v[136:139], v[180:183], v[44:47], v[136:139]\n"
      "ds_read_b128 v[44:47], v234 offset:0\n"
      "v_mfma_f32_16x16x32_bf16 v[136:139], v[184:187], v[60:63], v[136:139]\n"
      "ds_read_b128 v[60:63], v235 offset:0\n"
      "v_cvt_pk_bf16_f32 v208, v132, v133\n"
      "v_mfma_f32_16x16x32_bf16 v[136:139], v[188:191], v[64:67], v[136:139]\n"
      "ds_read_b128 v[64:67], v232 offset:8192\n"
      "v_cvt_pk_bf16_f32 v209, v134, v135\n"
      "v_mfma_f32_16x16x32_bf16 v[136:139], v[204:207], v[68:71], v[136:139]\n"
      "ds_read_b128 v[68:71], v233 offset:8192\n"
      "v_mfma_f32_16x16x32_bf16 v[136:139], a[24:27], v[84:87], v[136:139]\n"
      "ds_read_b128 v[84:87], v234 offset:8192\n"
      "v_mfma_f32_16x16x32_bf16 v[136:139], a[28:31], v[88:91], v[136:139]\n"
      "ds_read_b128 v[88:91], v235 offset:8192\n"
      "v_mfma_f32_16x16x32_bf16 v[136:139], a[32:35], v[92:95], v[136:139]\n"
      "ds_read_b128 v[92:95], v232 offset:16384\n"
      "v_mfma_f32_16x16x32_bf16 v[136:139], a[36:39], v[108:111], v[136:139]\n"
      "ds_read_b128 v[108:111], v233 offset:16384\n"
      "v_mfma_f32_16x16x32_bf16 v[136:139], a[40:43], v[112:115], v[136:139]\n"
      "ds_read_b128 v[112:115], v234 offset:16384\n"
      "v_mfma_f32_16x16x32_bf16 v[136:139], a[44:47], v[116:119], v[136:139]\n"
      "ds_read_b128 v[116:119], v235 offset:16384\n"
      "s_waitcnt lgkmcnt(8)\n"
      "v_mfma_f32_16x16x32_bf16 v[132:135], a[240:243], v[36:39], v[0:3]\n"
      "v_and_b32 v19, 0xffff0000, v129\n"
      "s_cbranch_vccnz LT12\n"
      "LS12:\n"
      "v_mfma_f32_16x16x32_bf16 v[132:135], a[244:247], v[40:43], v[132:135]\n"
      "v_lshlrev_b32 v20, 16, v130\n"
      "v_mfma_f32_16x16x32_bf16 v[132:135], a[248:251], v[44:47], v[132:135]\n"
      "v_and_b32 v21, 0xffff0000, v130\n"
      "v_mfma_f32_16x16x32_bf16 v[132:135], a[252:255], v[60:63], v[132:135]\n"
      "v_lshlrev_b32 v22, 16, v131\n"
      "s_waitcnt lgkmcnt(4)\n"
      "v_mfma_f32_16x16x32_bf16 v[132:135], a[48:51], v[64:67], v[132:135]\n"
      "v_and_b32 v23, 0xffff0000, v131\n"
      "v_mfma_f32_16x16x32_bf16 v[132:135], a[52:55], v[68:71], v[132:135]\n"
      "v_cvt_pk_bf16_f32 v210, v136, v137\n"
      "v_mfma_f32_16x16x32_bf16 v[132:135], a[56:59], v[84:87], v[132:135]\n"
      "v_cvt_pk_bf16_f32 v211, v138, v139\n"
      "v_mfma_f32_16x16x32_bf16 v[132:135], a[60:63], v[88:91], v[132:135]\n"
      "s_add_u32 s54, s44, s36\n"
      "s_addc_u32 s55, s45, 0\n"
      "s_waitcnt lgkmcnt(0)\n"
      "v_mfma_f32_16x16x32_bf16 v[132:135], a[64:67], v[92:95], v[132:135]\n"
      "ds_read_b128 v[216:219], v246 offset:12288\n"
      "v_mfma_f32_16x16x32_bf16 v[132:135], a[68:71], v[108:111], v[132:135]\n"
      "ds_read_b128 v[220:223], v246 offset:13312\n"
      "v_mfma_f32_16x16x32_bf16 v[132:135], a[72:75], v[112:115], v[132:135]\n"
      "ds_read_b128 v[224:227], v246 offset:14336\n"
      "v_mfma_f32_16x16x32_bf16 v[132:135], a[76:79], v[116:119], v[132:135]\n"
      "ds_write_b128 v244, v[208:211] offset:24704\n"
      "v_mfma_f32_16x16x32_bf16 v[136:139], a[80:83], v[36:39], v[4:7]\n"
      "v_mfma_f32_16x16x32_bf16 v[136:139], a[84:87], v[40:43], v[136:139]\n"
      "v_mfma_f32_16x16x32_bf16 v[136:139], a[88:91], v[44:47], v[136:139]\n"
      "v_mfma_f32_16x16x32_bf16 v[136:139], a[92:95], v[60:63], v[136:139]\n"
      "v_cvt_pk_bf16_f32 v208, v132, v133\n"
      "v_mfma_f32_16x16x32_bf16 v[136:139], a[96:99], v[64:67], v[136:139]\n"
      "v_cvt_pk_bf16_f32 v209, v134, v135\n"
      "v_mfma_f32_16x16x32_bf16 v[136:139], a[100:103], v[68:71], v[136:139]\n"
      "s_waitcnt lgkmcnt(3)\n"
      "v_mfma_f32_16x16x32_bf16 v[136:139], a[104:107], v[84:87], v[136:139]\n"
      "global_store_dwordx4 v247, v[216:219], s[54:55] offset:0 sc1\n"
      "v_mfma_f32_16x16x32_bf16 v[136:139], a[108:111], v[88:91], v[136:139]\n"
      "s_add_u32 s52, s42, s38\n"
      "s_addc_u32 s53, s43, 0\n"
      "v_mfma_f32_16x16x32_bf16 v[136:139], a[112:115], v[92:95], v[136:139]\n"
      "s_add_u32 m0, s17, 0x6000\n"
      "v_mfma_f32_16x16x32_bf16 v[136:139], a[116:119], v[108:111], v[136:139]\n"
      "global_load_lds_dwordx4 v236, s[52:53] offset:0 nt\n"
      "v_mfma_f32_16x16x32_bf16 v[136:139], a[120:123], v[112:115], v[136:139]\n"
      "v_mfma_f32_16x16x32_bf16 v[136:139], a[124:127], v[116:119], v[136:139]\n"
      "v_mfma_f32_16x16x32_bf16 v[132:135], a[128:131], v[36:39], v[8:11]\n"
      "s_cbranch_vccnz LT13\n"
      "LS13:\n"
      "v_mfma_f32_16x16x32_bf16 v[132:135], a[132:135], v[40:43], v[132:135]\n"
      "v_mfma_f32_16x16x32_bf16 v[132:135], a[136:139], v[44:47], v[132:135]\n"
      "v_mfma_f32_16x16x32_bf16 v[132:135], a[140:143], v[60:63], v[132:135]\n"
      "v_cvt_pk_bf16_f32 v210, v136, v137\n"
      "v_mfma_f32_16x16x32_bf16 v[132:135], a[144:147], v[64:67], v[132:135]\n"
      "v_cvt_pk_bf16_f32 v211, v138, v139\n"
      "v_mfma_f32_16x16x32_bf16 v[132:135], a[148:151], v[68:71], v[132:135]\n"
      "v_mfma_f32_16x16x32_bf16 v[132:135], a[152:155], v[84:87], v[132:135]\n"
      "s_waitcnt lgkmcnt(2)\n"
      "v_mfma_f32_16x16x32_bf16 v[132:135], a[156:159], v[88:91], v[132:135]\n"
      "global_store_dwordx4 v252, v[220:223], s[54:55] offset:0 sc1\n"
      "v_mfma_f32_16x16x32_bf16 v[132:135], a[160:163], v[92:95], v[132:135]\n"
      "s_add_u32 m0, s17, 0x7f00\n"
      "v_mfma_f32_16x16x32_bf16 v[132:135], a[164:167], v[108:111], v[132:135]\n"
      "ds_write_b128 v244, v[208:211] offset:0\n"
      "v_mfma_f32_16x16x32_bf16 v[132:135], a[168:171], v[112:115], v[132:135]\n"
      "global_load_lds_dwordx4 v236, s[52:53] offset:256 nt\n"
      "v_mfma_f32_16x16x32_bf16 v[132:135], a[172:175], v[116:119], v[132:135]\n"
      "v_mfma_f32_16x16x32_bf16 v[136:139], a[176:179], v[36:39], v[12:15]\n"
      "v_mfma_f32_16x16x32_bf16 v[136:139], a[180:183], v[40:43], v[136:139]\n"
      "v_mfma_f32_16x16x32_bf16 v[136:139], a[184:187], v[44:47], v[136:139]\n"
      "v_mfma_f32_16x16x32_bf16 v[136:139], a[188:191], v[60:63], v[136:139]\n"
      "v_cvt_pk_bf16_f32 v212, v132, v133\n"
      "v_mfma_f32_16x16x32_bf16 v[136:139], a[192:195], v[64:67], v[136:139]\n"
      "v_cvt_pk_bf16_f32 v213, v134, v135\n"
      "v_mfma_f32_16x16x32_bf16 v[136:139], a[196:199], v[68:71], v[136:139]\n"
      "v_mfma_f32_16x16x32_bf16 v[136:139], a[200:203], v[84:87], v[136:139]\n"
      "v_mfma_f32_16x16x32_bf16 v[136:139], a[204:207], v[88:91], v[136:139]\n"
      "s_waitcnt lgkmcnt(2)\n"
      "v_mfma_f32_16x16x32_bf16 v[136:139], a[208:211], v[92:95], v[136:139]\n"
      "global_store_dwordx4 v253, v[224:227], s[54:55] offset:0 sc1\n"
      "v_mfma_f32_16x16x32_bf16 v[136:139], a[212:215], v[108:111], v[136:139]\n"
      "s_add_u32 m0, s17, 0x9e00\n"
      "v_mfma_f32_16x16x32_bf16 v[136:139], a[216:219], v[112:115], v[136:139]\n"
      "global_load_lds_dwordx4 v236, s[52:53] offset:512 nt\n"
      "v_mfma_f32_16x16x32_bf16 v[136:139], a[220:223], v[116:119], v[136:139]\n"
      "v_mfma_f32_16x16x32_bf16 v[132:135], a[224:227], v[36:39], v[16:19]\n"
      "s_cbranch_vccnz LT14\n"
      "LS14:\n"
      "v_mfma_f32_16x16x32_bf16 v[132:135], a[228:231], v[40:43], v[132:135]\n"
      "v_mfma_f32_16x16x32_bf16 v[132:135], a[232:235], v[44:47], v[132:135]\n"
      "v_mfma_f32_16x16x32_bf16 v[132:135], a[236:239], v[60:63], v[132:135]\n"
      "v_cvt_pk_bf16_f32 v214, v136, v137\n"
      "v_mfma_f32_16x16x32_bf16 v[132:135], a[0:3], v[64:67], v[132:135]\n"
      "v_cvt_pk_bf16_f32 v215, v138, v139\n"
      "v_mfma_f32_16x16x32_bf16 v[132:135], a[4:7], v[68:71], v[132:135]\n"
      "v_mfma_f32_16x16x32_bf16 v[132:135], a[8:11], v[84:87], v[132:135]\n"
      "v_mfma_f32_16x16x32_bf16 v[132:135], a[12:15], v[88:91], v[132:135]\n"
      "v_mfma_f32_16x16x32_bf16 v[132:135], a[16:19], v[92:95], v[132:135]\n"
      "v_mfma_f32_16x16x32_bf16 v[132:135], a[20:23], v[108:111], v[132:135]\n"
      "ds_write_b128 v245, v[212:215] offset:0\n"
      "v_mfma_f32_16x16x32_bf16 v[132:135], v[140:143], v[112:115], v[132:135]\n"
      "v_mfma_f32_16x16x32_bf16 v[132:135], v[156:159], v[116:119], v[132:135]\n"
      "s_waitcnt lgkmcnt(2)\n"
      "s_barrier\n"
      "v_mfma_f32_16x16x32_bf16 v[136:139], v[160:163], v[36:39], v[20:23]\n"
      "ds_read_b128 v[36:39], v232 offset:256\n"
      "v_mfma_f32_16x16x32_bf16 v[136:139], v[164:167], v[40:43], v[136:139]\n"
      "ds_read_b128 v[40:43], v233 offset:256\n"
      "v_mfma_f32_16x16x32_bf16 v[136:139], v[180:183], v[44:47], v[136:139]\n"
      "ds_read_b128 v[44:47], v234 offset:256\n"
      "v_mfma_f32_16x16x32_bf16 v[136:139], v[184:187], v[60:63], v[136:139]\n"
      "ds_read_b128 v[60:63], v235 offset:256\n"
      "v_cvt_pk_bf16_f32 v208, v132, v133\n"
      "v_mfma_f32_16x16x32_bf16 v[136:139], v[188:191], v[64:67], v[136:139]\n"
      "ds_read_b128 v[64:67], v232 offset:8448\n"
      "v_cvt_pk_bf16_f32 v209, v134, v135\n"
      "v_mfma_f32_16x16x32_bf16 v[136:139], v[204:207], v[68:71], v[136:139]\n"
      "ds_read_b128 v[68:71], v233 offset:8448\n"
      "v_mfma_f32_16x16x32_bf16 v[136:139], a[24:27], v[84:87], v[136:139]\n"
      "ds_read_b128 v[84:87], v234 offset:8448\n"
      "v_mfma_f32_16x16x32_bf16 v[136:139], a[28:31], v[88:91], v[136:139]\n"
      "ds_read_b128 v[88:91], v235 offset:8448\n"
      "v_mfma_f32_16x16x32_bf16 v[136:139], a[32:35], v[92:95], v[136:139]\n"
      "ds_read_b128 v[92:95], v232 offset:16640\n"
      "v_mfma_f32_16x16x32_bf16 v[136:139], a[36:39], v[108:111], v[136:139]\n"
      "ds_read_b128 v[108:111], v233 offset:16640\n"
      "v_mfma_f32_16x16x32_bf16 v[136:139], a[40:43], v[112:115], v[136:139]\n"
      "ds_read_b128 v[112:115], v234 offset:16640\n"
      "v_mfma_f32_16x16x32_bf16 v[136:139], a[44:47], v[116:119], v[136:139]\n"
      "ds_read_b128 v[116:119], v235 offset:16640\n"
      "s_waitcnt lgkmcnt(8)\n"
      "v_mfma_f32_16x16x32_bf16 v[132:135], a[240:243], v[36:39], v[0:3]\n"
      "s_cbranch_vccnz LT15\n"
      "LS15:\n"
      "v_mfma_f32_16x16x32_bf16 v[132:135], a[244:247], v[40:43], v[132:135]\n"
      "v_mfma_f32_16x16x32_bf16 v[132:135], a[248:251], v[44:47], v[132:135]\n"
      "v_mfma_f32_16x16x32_bf16 v[132:135], a[252:255], v[60:63], v[132:135]\n"
      "v_cvt_pk_bf16_f32 v210, v136, v137\n"
      "s_waitcnt lgkmcnt(4)\n"
      "v_mfma_f32_16x16x32_bf16 v[132:135], a[48:51], v[64:67], v[132:135]\n"
      "v_cvt_pk_bf16_f32 v211, v138, v139\n"
      "v_mfma_f32_16x16x32_bf16 v[132:135], a[52:55], v[68:71], v[132:135]\n"
      "v_mfma_f32_16x16x32_bf16 v[132:135], a[56:59], v[84:87], v[132:135]\n"
      "ds_read_b128 v[216:219], v246 offset:24576\n"
      "v_mfma_f32_16x16x32_bf16 v[132:135], a[60:63], v[88:91], v[132:135]\n"
      "ds_read_b128 v[220:223], v246 offset:25600\n"
      "s_waitcnt lgkmcnt(2)\n"
      "v_mfma_f32_16x16x32_bf16 v[132:135], a[64:67], v[92:95], v[132:135]\n"
      "ds_read_b128 v[224:227], v246 offset:26624\n"
      "v_mfma_f32_16x16x32_bf16 v[132:135], a[68:71], v[108:111], v[132:135]\n"
      "ds_write_b128 v244, v[208:211] offset:128\n"
      "v_mfma_f32_16x16x32_bf16 v[132:135], a[72:75], v[112:115], v[132:135]\n"
      "v_mfma_f32_16x16x32_bf16 v[132:135], a[76:79], v[116:119], v[132:135]\n"
      "v_mfma_f32_16x16x32_bf16 v[136:139], a[80:83], v[36:39], v[4:7]\n"
      "v_mfma_f32_16x16x32_bf16 v[136:139], a[84:87], v[40:43], v[136:139]\n"
      "v_mfma_f32_16x16x32_bf16 v[136:139], a[88:91], v[44:47], v[136:139]\n"
      "v_mfma_f32_16x16x32_bf16 v[136:139], a[92:95], v[60:63], v[136:139]\n"
      "v_cvt_pk_bf16_f32 v208, v132, v133\n"
      "v_mfma_f32_16x16x32_bf16 v[136:139], a[96:99], v[64:67], v[136:139]\n"
      "v_cvt_pk_bf16_f32 v209, v134, v135\n"
      "v_mfma_f32_16x16x32_bf16 v[136:139], a[100:103], v[68:71], v[136:139]\n"
      "s_waitcnt lgkmcnt(3)\n"
      "v_mfma_f32_16x16x32_bf16 v[136:139], a[104:107], v[84:87], v[136:139]\n"
      "global_store_dwordx4 v247, v[216:219], s[54:55] offset:768 sc1\n"
      "v_mfma_f32_16x16x32_bf16 v[136:139], a[108:111], v[88:91], v[136:139]\n"
      "s_add_u32 m0, s17, 0x7000\n"
      "v_mfma_f32_16x16x32_bf16 v[136:139], a[112:115], v[92:95], v[136:139]\n"
      "global_load_lds_dwordx4 v237, s[52:53] offset:0 nt\n"
      "v_mfma_f32_16x16x32_bf16 v[136:139], a[116:119], v[108:111], v[136:139]\n"
      "v_mfma_f32_16x16x32_bf16 v[136:139], a[120:123], v[112:115], v[136:139]\n"
      "v_mfma_f32_16x16x32_bf16 v[136:139], a[124:127], v[116:119], v[136:139]\n"
      "v_mfma_f32_16x16x32_bf16 v[132:135], a[128:131], v[36:39], v[8:11]\n"
      "s_cbranch_vccnz LT16\n"
      "LS16:\n"
      "v_mfma_f32_16x16x32_bf16 v[132:135], a[132:135], v[40:43], v[132:135]\n"
      "v_mfma_f32_16x16x32_bf16 v[132:135], a[136:139], v[44:47], v[132:135]\n"
      "v_mfma_f32_16x16x32_bf16 v[132:135], a[140:143], v[60:63], v[132:135]\n"
      "v_cvt_pk_bf16_f32 v210, v136, v137\n"
      "v_mfma_f32_16x16x32_bf16 v[132:135], a[144:147], v[64:67], v[132:135]\n"
      "v_cvt_pk_bf16_f32 v211, v138, v139\n"
      "v_mfma_f32_16x16x32_bf16 v[132:135], a[148:151], v[68:71], v[132:135]\n"
      "v_mfma_f32_16x16x32_bf16 v[132:135], a[152:155], v[84:87], v[132:135]\n"
      "s_waitcnt lgkmcnt(2)\n"
      "v_mfma_f32_16x16x32_bf16 v[132:135], a[156:159], v[88:91], v[132:135]\n"
      "global_store_dwordx4 v252, v[220:223], s[54:55] offset:768 sc1\n"
      "v_mfma_f32_16x16x32_bf16 v[132:135], a[160:163], v[92:95], v[132:135]\n"
      "s_add_u32 m0, s17, 0x8f00\n"
      "v_mfma_f32_16x16x32_bf16 v[132:135], a[164:167], v[108:111], v[132:135]\n"
      "ds_write_b128 v244, v[208:211] offset:12288\n"
      "v_mfma_f32_16x16x32_bf16 v[132:135], a[168:171], v[112:115], v[132:135]\n"
      "global_load_lds_dwordx4 v237, s[52:53] offset:256 nt\n"
      "v_mfma_f32_16x16x32_bf16 v[132:135], a[172:175], v[116:119], v[132:135]\n"
      "v_mfma_f32_16x16x32_bf16 v[136:139], a[176:179], v[36:39], v[12:15]\n"
      "v_mfma_f32_16x16x32_bf16 v[136:139], a[180:183], v[40:43], v[136:139]\n"
      "v_mfma_f32_16x16x32_bf16 v[136:139], a[184:187], v[44:47], v[136:139]\n"
      "v_mfma_f32_16x16x32_bf16 v[136:139], a[188:191], v[60:63], v[136:139]\n"
      "v_cvt_pk_bf16_f32 v212, v132, v133\n"
      "v_mfma_f32_16x16x32_bf16 v[136:139], a[192:195], v[64:67], v[136:139]\n"
      "v_cvt_pk_bf16_f32 v213, v134, v135\n"
      "v_mfma_f32_16x16x32_bf16 v[136:139], a[196:199], v[68:71], v[136:139]\n"
      "v_mfma_f32_16x16x32_bf16 v[136:139], a[200:203], v[84:87], v[136:139]\n"
      "v_mfma_f32_16x16x32_bf16 v[136:139], a[204:207], v[88:91], v[136:139]\n"
      "s_waitcnt lgkmcnt(2)\n"
      "v_mfma_f32_16x16x32_bf16 v[136:139], a[208:211], v[92:95], v[136:139]\n"
      "global_store_dwordx4 v253, v[224:227], s[54:55] offset:768 sc1\n"
      "v_mfma_f32_16x16x32_bf16 v[136:139], a[212:215], v[108:111], v[136:139]\n"
      "s_add_u32 m0, s17, 0xae00\n"
      "v_mfma_f32_16x16x32_bf16 v[136:139], a[216:219], v[112:115], v[136:139]\n"
      "global_load_lds_dwordx4 v237, s[52:53] offset:512 nt\n"
      "v_mfma_f32_16x16x32_bf16 v[136:139], a[220:223], v[116:119], v[136:139]\n"
      "v_mfma_f32_16x16x32_bf16 v[132:135], a[224:227], v[36:39], v[16:19]\n"
      "s_cbranch_vccnz LT17\n"
      "LS17:\n"
      "v_mfma_f32_16x16x32_bf16 v[132:135], a[228:231], v[40:43], v[132:135]\n"
      "v_mfma_f32_16x16x32_bf16 v[132:135], a[232:235], v[44:47], v[132:135]\n"
      "v_mfma_f32_16x16x32_bf16 v[132:135], a[236:239], v[60:63], v[132:135]\n"
      "s_memrealtime s[84:85]\n"
      "v_mfma_f32_16x16x32_bf16 v[132:135], a[0:3], v[64:67], v[132:135]\n"
      "v_cvt_pk_bf16_f32 v214, v136, v137\n"
      "v_mfma_f32_16x16x32_bf16 v[132:135], a[4:7], v[68:71], v[132:135]\n"
      "v_cvt_pk_bf16_f32 v215, v138, v139\n"
      "v_mfma_f32_16x16x32_bf16 v[132:135], a[8:11], v[84:87], v[132:135]\n"
      "v_mfma_f32_16x16x32_bf16 v[132:135], a[12:15], v[88:91], v[132:135]\n"
      "s_add_u32 s83, s83, s86\n"
      "v_mfma_f32_16x16x32_bf16 v[132:135], a[16:19], v[92:95], v[132:135]\n"
      "v_mfma_f32_16x16x32_bf16 v[132:135], a[20:23], v[108:111], v[132:135]\n"
      "v_mfma_f32_16x16x32_bf16 v[132:135], v[140:143], v[112:115], v[132:135]\n"
      "s_waitcnt lgkmcnt(0)\n"
      "s_sub_u32 s84, s84, s83\n"
      "v_mfma_f32_16x16x32_bf16 v[132:135], v[156:159], v[116:119], v[132:135]\n"
      "s_cmp_lt_i32 s84, 0\n"
      "s_cselect_b64 vcc, -1, 0\n"
      "s_waitcnt vmcnt(24)\n"
      "s_barrier\n"
      "v_mfma_f32_16x16x32_bf16 v[136:139], v[160:163], v[36:39], v[20:23]\n"
      "ds_read_b128 v[36:39], v232 offset:24576\n"
      "ds_write_b128 v245, v[212:215] offset:12288\n"
      "v_mfma_f32_16x16x32_bf16 v[136:139], v[164:167], v[40:43], v[136:139]\n"
      "ds_read_b128 v[40:43], v233 offset:24576\n"
      "s_lshl_b32 s79, s94, 1\n"
      "v_mfma_f32_16x16x32_bf16 v[136:139], v[180:183], v[44:47], v[136:139]\n"
      "ds_read_b128 v[44:47], v234 offset:24576\n"
      "v_mad_i32_i24 v252, v251, s79, v252\n"
      "v_mfma_f32_16x16x32_bf16 v[136:139], v[184:187], v[60:63], v[136:139]\n"
      "ds_read_b128 v[60:63], v235 offset:24576\n"
      "v_cvt_pk_bf16_f32 v208, v132, v133\n"
      "v_mfma_f32_16x16x32_bf16 v[136:139], v[188:191], v[64:67], v[136:139]\n"
      "ds_read_b128 v[64:67], v232 offset:32768\n"
      "v_cvt_pk_bf16_f32 v209, v134, v135\n"
      "v_mfma_f32_16x16x32_bf16 v[136:139], v[204:207], v[68:71], v[136:139]\n"
      "ds_read_b128 v[68:71], v233 offset:32768\n"
      "s_mul_i32 s79, s79, s100\n"
      "v_mfma_f32_16x16x32_bf16 v[136:139], a[24:27], v[84:87], v[136:139]\n"
      "ds_read_b128 v[84:87], v234 offset:32768\n"
      "v_add_u32 v247, s79, v247\n"
      "v_mfma_f32_16x16x32_bf16 v[136:139], a[28:31], v[88:91], v[136:139]\n"
      "ds_read_b128 v[88:91], v235 offset:32768\n"
      "s_mul_i32 s79, s79, s101\n"
      "v_mfma_f32_16x16x32_bf16 v[136:139], a[32:35], v[92:95], v[136:139]\n"
      "ds_read_b128 v[92:95], v232 offset:40960\n"
      "v_add_u32 v253, s79, v253\n"
      "v_mfma_f32_16x16x32_bf16 v[136:139], a[36:39], v[108:111], v[136:139]\n"
      "ds_read_b128 v[108:111], v233 offset:40960\n"
      "v_mfma_f32_16x16x32_bf16 v[136:139], a[40:43], v[112:115], v[136:139]\n"
      "ds_read_b128 v[112:115], v234 offset:40960\n"
      "v_mfma_f32_16x16x32_bf16 v[136:139], a[44:47], v[116:119], v[136:139]\n"
      "ds_read_b128 v[116:119], v235 offset:40960\n"
      "s_waitcnt lgkmcnt(8)\n"
      "v_mfma_f32_16x16x32_bf16 v[132:135], a[240:243], v[36:39], v[0:3]\n"
      "s_cbranch_vccnz LT18\n"
      "LS18:\n"
      "v_mfma_f32_16x16x32_bf16 v[132:135], a[244:247], v[40:43], v[132:135]\n"
      "s_add_u32 s54, s30, s37\n"
      "s_addc_u32 s55, s31, 0\n"
      "v_mfma_f32_16x16x32_bf16 v[132:135], a[248:251], v[44:47], v[132:135]\n"
      "v_mfma_f32_16x16x32_bf16 v[132:135], a[252:255], v[60:63], v[132:135]\n"
      "v_cvt_pk_bf16_f32 v210, v136, v137\n"
      "s_waitcnt lgkmcnt(4)\n"
      "v_mfma_f32_16x16x32_bf16 v[132:135], a[48:51], v[64:67], v[132:135]\n"
      "v_cvt_pk_bf16_f32 v211, v138, v139\n"
      "v_mfma_f32_16x16x32_bf16 v[132:135], a[52:55], v[68:71], v[132:135]\n"
      "s_mul_i32 s78, s96, s19\n"
      "v_mfma_f32_16x16x32_bf16 v[132:135], a[56:59], v[84:87], v[132:135]\n"
      "ds_read_b128 v[216:219], v246 offset:0\n"
      "v_add_u32 v236, s78, v236\n"
      "v_mfma_f32_16x16x32_bf16 v[132:135], a[60:63], v[88:91], v[132:135]\n"
      "ds_read_b128 v[220:223], v246 offset:1024\n"
      "s_waitcnt lgkmcnt(2)\n"
      "v_mfma_f32_16x16x32_bf16 v[132:135], a[64:67], v[92:95], v[132:135]\n"
      "ds_read_b128 v[224:227], v246 offset:2048\n"
      "v_mfma_f32_16x16x32_bf16 v[132:135], a[68:71], v[108:111], v[132:135]\n"
      "ds_write_b128 v244, v[208:211] offset:12416\n"
      "v_mfma_f32_16x16x32_bf16 v[132:135], a[72:75], v[112:115], v[132:135]\n"
      "s_mul_i32 s78, s78, s99\n"
      "v_mfma_f32_16x16x32_bf16 v[132:135], a[76:79], v[116:119], v[132:135]\n"
      "v_add_u32 v237, s78, v237\n"
      "v_mfma_f32_16x16x32_bf16 v[136:139], a[80:83], v[36:39], v[4:7]\n"
      "v_mfma_f32_16x16x32_bf16 v[136:139], a[84:87], v[40:43], v[136:139]\n"
      "v_mfma_f32_16x16x32_bf16 v[136:139], a[88:91], v[44:47], v[136:139]\n"
      "v_mfma_f32_16x16x32_bf16 v[136:139], a[92:95], v[60:63], v[136:139]\n"
      "v_cvt_pk_bf16_f32 v208, v132, v133\n"
      "v_mfma_f32_16x16x32_bf16 v[136:139], a[96:99], v[64:67], v[136:139]\n"
      "v_cvt_pk_bf16_f32 v209, v134, v135\n"
      "v_mfma_f32_16x16x32_bf16 v[136:139], a[100:103], v[68:71], v[136:139]\n"
      "s_waitcnt lgkmcnt(3)\n"
      "v_mfma_f32_16x16x32_bf16 v[136:139], a[104:107], v[84:87], v[136:139]\n"
      "global_store_dwordx4 v247, v[216:219], s[54:55] offset:0 sc1\n"
      "v_mfma_f32_16x16x32_bf16 v[136:139], a[108:111], v[88:91], v[136:139]\n"
      "s_add_u32 s52, s20, s39\n"
      "s_addc_u32 s53, s21, 0\n"
      "v_mfma_f32_16x16x32_bf16 v[136:139], a[112:115], v[92:95], v[136:139]\n"
      "s_add_u32 m0, s17, 0xc000\n"
      "v_mfma_f32_16x16x32_bf16 v[136:139], a[116:119], v[108:111], v[136:139]\n"
      "global_load_lds_dwordx4 v236, s[52:53] offset:0 nt\n"
      "v_mfma_f32_16x16x32_bf16 v[136:139], a[120:123], v[112:115], v[136:139]\n"
      "v_mfma_f32_16x16x32_bf16 v[136:139], a[124:127], v[116:119], v[136:139]\n"
      "v_mfma_f32_16x16x32_bf16 v[132:135], a[128:131], v[36:39], v[8:11]\n"
      "s_cbranch_vccnz LT19\n"
      "LS19:\n"
      "v_mfma_f32_16x16x32_bf16 v[132:135], a[132:135], v[40:43], v[132:135]\n"
      "v_mfma_f32_16x16x32_bf16 v[132:135], a[136:139], v[44:47], v[132:135]\n"
      "v_mfma_f32_16x16x32_bf16 v[132:135], a[140:143], v[60:63], v[132:135]\n"
      "v_cvt_pk_bf16_f32 v210, v136, v137\n"
      "v_mfma_f32_16x16x32_bf16 v[132:135], a[144:147], v[64:67], v[132:135]\n"
      "v_cvt_pk_bf16_f32 v211, v138, v139\n"
      "v_mfma_f32_16x16x32_bf16 v[132:135], a[148:151], v[68:71], v[132:135]\n"
      "v_mfma_f32_16x16x32_bf16 v[132:135], a[152:155], v[84:87], v[132:135]\n"
      "s_waitcnt lgkmcnt(2)\n"
      "v_mfma_f32_16x16x32_bf16 v[132:135], a[156:159], v[88:91], v[132:135]\n"
      "global_store_dwordx4 v252, v[220:223], s[54:55] offset:0 sc1\n"
      "v_mfma_f32_16x16x32_bf16 v[132:135], a[160:163], v[92:95], v[132:135]\n"
      "s_add_u32 m0, s17, 0xdf00\n"
      "v_mfma_f32_16x16x32_bf16 v[132:135], a[164:167], v[108:111], v[132:135]\n"
      "ds_write_b128 v244, v[208:211] offset:24576\n"
      "v_mfma_f32_16x16x32_bf16 v[132:135], a[168:171], v[112:115], v[132:135]\n"
      "global_load_lds_dwordx4 v236, s[52:53] offset:256 nt\n"
      "v_mfma_f32_16x16x32_bf16 v[132:135], a[172:175], v[116:119], v[132:135]\n"
      "v_mfma_f32_16x16x32_bf16 v[136:139], a[176:179], v[36:39], v[12:15]\n"
      "v_mfma_f32_16x16x32_bf16 v[136:139], a[180:183], v[40:43], v[136:139]\n"
      "v_mfma_f32_16x16x32_bf16 v[136:139], a[184:187], v[44:47], v[136:139]\n"
      "v_mfma_f32_16x16x32_bf16 v[136:139], a[188:191], v[60:63], v[136:139]\n"
      "v_cvt_pk_bf16_f32 v212, v132, v133\n"
      "v_mfma_f32_16x16x32_bf16 v[136:139], a[192:195], v[64:67], v[136:139]\n"
      "v_cvt_pk_bf16_f32 v213, v134, v135\n"
      "v_mfma_f32_16x16x32_bf16 v[136:139], a[196:199], v[68:71], v[136:139]\n"
      "v_mfma_f32_16x16x32_bf16 v[136:139], a[200:203], v[84:87], v[136:139]\n"
      "v_mfma_f32_16x16x32_bf16 v[136:139], a[204:207], v[88:91], v[136:139]\n"
      "s_waitcnt lgkmcnt(2)\n"
      "v_mfma_f32_16x16x32_bf16 v[136:139], a[208:211], v[92:95], v[136:139]\n"
      "global_store_dwordx4 v253, v[224:227], s[54:55] offset:0 sc1\n"
      "v_mfma_f32_16x16x32_bf16 v[136:139], a[212:215], v[108:111], v[136:139]\n"
      "s_add_u32 m0, s17, 0xfe00\n"
      "v_mfma_f32_16x16x32_bf16 v[136:139], a[216:219], v[112:115], v[136:139]\n"
      "global_load_lds_dwordx4 v236, s[52:53] offset:512 nt\n"
      "v_mfma_f32_16x16x32_bf16 v[136:139], a[220:223], v[116:119], v[136:139]\n"
      "v_mfma_f32_16x16x32_bf16 v[132:135], a[224:227], v[36:39], v[16:19]\n"
      "s_cbranch_vccnz LT20\n"
      "LS20:\n"
      "v_mfma_f32_16x16x32_bf16 v[132:135], a[228:231], v[40:43], v[132:135]\n"
      "v_mfma_f32_16x16x32_bf16 v[132:135], a[232:235], v[44:47], v[132:135]\n"
      "v_mfma_f32_16x16x32_bf16 v[132:135], a[236:239], v[60:63], v[132:135]\n"
      "v_cvt_pk_bf16_f32 v214, v136, v137\n"
      "v_mfma_f32_16x16x32_bf16 v[132:135], a[0:3], v[64:67], v[132:135]\n"
      "v_cvt_pk_bf16_f32 v215, v138, v139\n"
      "v_mfma_f32_16x16x32_bf16 v[132:135], a[4:7], v[68:71], v[132:135]\n"
      "v_mfma_f32_16x16x32_bf16 v[132:135], a[8:11], v[84:87], v[132:135]\n"
      "v_mfma_f32_16x16x32_bf16 v[132:135], a[12:15], v[88:91], v[132:135]\n"
      "v_mfma_f32_16x16x32_bf16 v[132:135], a[16:19], v[92:95], v[132:135]\n"
      "v_mfma_f32_16x16x32_bf16 v[132:135], a[20:23], v[108:111], v[132:135]\n"
      "ds_write_b128 v245, v[212:215] offset:24576\n"
      "v_mfma_f32_16x16x32_bf16 v[132:135], v[140:143], v[112:115], v[132:135]\n"
      "v_mfma_f32_16x16x32_bf16 v[132:135], v[156:159], v[116:119], v[132:135]\n"
      "s_waitcnt lgkmcnt(2)\n"
      "s_barrier\n"
      "v_mfma_f32_16x16x32_bf16 v[136:139], v[160:163], v[36:39], v[20:23]\n"
      "ds_read_b128 v[36:39], v232 offset:24832\n"
      "v_mfma_f32_16x16x32_bf16 v[136:139], v[164:167], v[40:43], v[136:139]\n"
      "ds_read_b128 v[40:43], v233 offset:24832\n"
      "v_mfma_f32_16x16x32_bf16 v[136:139], v[180:183], v[44:47], v[136:139]\n"
      "ds_read_b128 v[44:47], v234 offset:24832\n"
      "v_mfma_f32_16x16x32_bf16 v[136:139], v[184:187], v[60:63], v[136:139]\n"
      "ds_read_b128 v[60:63], v235 offset:24832\n"
      "v_cvt_pk_bf16_f32 v208, v132, v133\n"
      "v_mfma_f32_16x16x32_bf16 v[136:139], v[188:191], v[64:67], v[136:139]\n"
      "ds_read_b128 v[64:67], v232 offset:33024\n"
      "v_cvt_pk_bf16_f32 v209, v134, v135\n"
      "v_mfma_f32_16x16x32_bf16 v[136:139], v[204:207], v[68:71], v[136:139]\n"
      "ds_read_b128 v[68:71], v233 offset:33024\n"
      "v_mfma_f32_16x16x32_bf16 v[136:139], a[24:27], v[84:87], v[136:139]\n"
      "ds_read_b128 v[84:87], v234 offset:33024\n"
      "v_mfma_f32_16x16x32_bf16 v[136:139], a[28:31], v[88:91], v[136:139]\n"
      "ds_read_b128 v[88:91], v235 offset:33024\n"
      "v_mfma_f32_16x16x32_bf16 v[136:139], a[32:35], v[92:95], v[136:139]\n"
      "ds_read_b128 v[92:95], v232 offset:41216\n"
      "v_mfma_f32_16x16x32_bf16 v[136:139], a[36:39], v[108:111], v[136:139]\n"
      "ds_read_b128 v[108:111], v233 offset:41216\n"
      "v_mfma_f32_16x16x32_bf16 v[136:139], a[40:43], v[112:115], v[136:139]\n"
      "ds_read_b128 v[112:115], v234 offset:41216\n"
      "v_mfma_f32_16x16x32_bf16 v[136:139], a[44:47], v[116:119], v[136:139]\n"
      "ds_read_b128 v[116:119], v235 offset:41216\n"
      "s_waitcnt lgkmcnt(8)\n"
      "v_mfma_f32_16x16x32_bf16 v[132:135], a[240:243], v[36:39], v[0:3]\n"
      "v_add_u32 v232, 0xffff4000, v232\n"
      "s_cbranch_vccnz LT21\n"
      "LS21:\n"
      "v_mfma_f32_16x16x32_bf16 v[132:135], a[244:247], v[40:43], v[132:135]\n"
      "v_add_u32 v233, 0xffff4000, v233\n"
      "v_mfma_f32_16x16x32_bf16 v[132:135], a[248:251], v[44:47], v[132:135]\n"
      "v_add_u32 v234, 0xffff4000, v234\n"
      "v_mfma_f32_16x16x32_bf16 v[132:135], a[252:255], v[60:63], v[132:135]\n"
      "v_add_u32 v235, 0xffff4000, v235\n"
      "s_waitcnt lgkmcnt(4)\n"
      "v_mfma_f32_16x16x32_bf16 v[132:135], a[48:51], v[64:67], v[132:135]\n"
      "v_cvt_pk_bf16_f32 v210, v136, v137\n"
      "v_mfma_f32_16x16x32_bf16 v[132:135], a[52:55], v[68:71], v[132:135]\n"
      "v_cvt_pk_bf16_f32 v211, v138, v139\n"
      "v_mfma_f32_16x16x32_bf16 v[132:135], a[56:59], v[84:87], v[132:135]\n"
      "ds_read_b128 v[216:219], v246 offset:12288\n"
      "v_mfma_f32_16x16x32_bf16 v[132:135], a[60:63], v[88:91], v[132:135]\n"
      "ds_read_b128 v[220:223], v246 offset:13312\n"
      "s_waitcnt lgkmcnt(2)\n"
      "v_mfma_f32_16x16x32_bf16 v[132:135], a[64:67], v[92:95], v[132:135]\n"
      "ds_read_b128 v[224:227], v246 offset:14336\n"
      "v_mfma_f32_16x16x32_bf16 v[132:135], a[68:71], v[108:111], v[132:135]\n"
      "v_lshlrev_b32 v0, 16, v144\n"
      "v_mfma_f32_16x16x32_bf16 v[132:135], a[72:75], v[112:115], v[132:135]\n"
      "ds_write_b128 v244, v[208:211] offset:24704\n"
      "v_mfma_f32_16x16x32_bf16 v[132:135], a[76:79], v[116:119], v[132:135]\n"
      "v_and_b32 v1, 0xffff0000, v144\n"
      "v_mfma_f32_16x16x32_bf16 v[136:139], a[80:83], v[36:39], v[4:7]\n"
      "v_lshlrev_b32 v2, 16, v145\n"
      "v_mfma_f32_16x16x32_bf16 v[136:139], a[84:87], v[40:43], v[136:139]\n"
      "v_and_b32 v3, 0xffff0000, v145\n"
      "v_mfma_f32_16x16x32_bf16 v[136:139], a[88:91], v[44:47], v[136:139]\n"
      "v_mfma_f32_16x16x32_bf16 v[136:139], a[92:95], v[60:63], v[136:139]\n"
      "v_cvt_pk_bf16_f32 v208, v132, v133\n"
      "v_mfma_f32_16x16x32_bf16 v[136:139], a[96:99], v[64:67], v[136:139]\n"
      "v_cvt_pk_bf16_f32 v209, v134, v135\n"
      "v_mfma_f32_16x16x32_bf16 v[136:139], a[100:103], v[68:71], v[136:139]\n"
      "s_waitcnt lgkmcnt(3)\n"
      "v_mfma_f32_16x16x32_bf16 v[136:139], a[104:107], v[84:87], v[136:139]\n"
      "global_store_dwordx4 v247, v[216:219], s[54:55] offset:768 sc1\n"
      "v_mfma_f32_16x16x32_bf16 v[136:139], a[108:111], v[88:91], v[136:139]\n"
      "s_add_u32 m0, s17, 0xd000\n"
      "v_mfma_f32_16x16x32_bf16 v[136:139], a[112:115], v[92:95], v[136:139]\n"
      "global_load_lds_dwordx4 v237, s[52:53] offset:0 nt\n"
      "v_mfma_f32_16x16x32_bf16 v[136:139], a[116:119], v[108:111], v[136:139]\n"
      "v_lshlrev_b32 v4, 16, v146\n"
      "v_mfma_f32_16x16x32_bf16 v[136:139], a[120:123], v[112:115], v[136:139]\n"
      "LPD13:\n"
      "s_cbranch_execz LPD16\n"
      "v_and_b32 v5, 0xffff0000, v146\n"
      "v_mfma_f32_16x16x32_bf16 v[136:139], a[124:127], v[116:119], v[136:139]\n"
      "v_lshlrev_b32 v6, 16, v147\n"
      "v_mfma_f32_16x16x32_bf16 v[132:135], a[128:131], v[36:39], v[8:11]\n"
      "v_and_b32 v7, 0xffff0000, v147\n"
      "v_mfma_f32_16x16x32_bf16 v[132:135], a[132:135], v[40:43], v[132:135]\n"
      "s_cbranch_vccnz LT22\n"
      "LS22:\n"
      "v_mfma_f32_16x16x32_bf16 v[132:135], a[136:139], v[44:47], v[132:135]\n"
      "v_mfma_f32_16x16x32_bf16 v[132:135], a[140:143], v[60:63], v[132:135]\n"
      "v_cvt_pk_bf16_f32 v210, v136, v137\n"
      "v_mfma_f32_16x16x32_bf16 v[132:135], a[144:147], v[64:67], v[132:135]\n"
      "v_cvt_pk_bf16_f32 v211, v138, v139\n"
      "v_mfma_f32_16x16x32_bf16 v[132:135], a[148:151], v[68:71], v[132:135]\n"
      "v_lshlrev_b32 v8, 16, v148\n"
      "v_mfma_f32_16x16x32_bf16 v[132:135], a[152:155], v[84:87], v[132:135]\n"
      "s_waitcnt lgkmcnt(2)\n"
      "v_mfma_f32_16x16x32_bf16 v[132:135], a[156:159], v[88:91], v[132:135]\n"
      "global_store_dwordx4 v252, v[220:223], s[54:55] offset:768 sc1\n"
      "v_mfma_f32_16x16x32_bf16 v[132:135], a[160:163], v[92:95], v[132:135]\n"
      "s_add_u32 m0, s17, 0xef00\n"
      "v_mfma_f32_16x16x32_bf16 v[132:135], a[164:167], v[108:111], v[132:135]\n"
      "ds_write_b128 v244, v[208:211] offset:0\n"
      "v_mfma_f32_16x16x32_bf16 v[132:135], a[168:171], v[112:115], v[132:135]\n"
      "global_load_lds_dwordx4 v237, s[52:53] offset:256 nt\n"
      "v_mfma_f32_16x16x32_bf16 v[132:135], a[172:175], v[116:119], v[132:135]\n"
      "v_and_b32 v9, 0xffff0000, v148\n"
      "v_mfma_f32_16x16x32_bf16 v[136:139], a[176:179], v[36:39], v[12:15]\n"
      "v_lshlrev_b32 v10, 16, v149\n"
      "v_mfma_f32_16x16x32_bf16 v[136:139], a[180:183], v[40:43], v[136:139]\n"
      "v_and_b32 v11, 0xffff0000, v149\n"
      "v_mfma_f32_16x16x32_bf16 v[136:139], a[184:187], v[44:47], v[136:139]\n"
      "v_mfma_f32_16x16x32_bf16 v[136:139], a[188:191], v[60:63], v[136:139]\n"
      "v_cvt_pk_bf16_f32 v212, v132, v133\n"
      "v_mfma_f32_16x16x32_bf16 v[136:139], a[192:195], v[64:67], v[136:139]\n"
      "v_cvt_pk_bf16_f32 v213, v134, v135\n"
      "v_mfma_f32_16x16x32_bf16 v[136:139], a[196:199], v[68:71], v[136:139]\n"
      "v_lshlrev_b32 v12, 16, v150\n"
      "v_mfma_f32_16x16x32_bf16 v[136:139], a[200:203], v[84:87], v[136:139]\n"
      "v_and_b32 v13, 0xffff0000, v150\n"
      "v_mfma_f32_16x16x32_bf16 v[136:139], a[204:207], v[88:91], v[136:139]\n"
      "s_waitcnt lgkmcnt(2)\n"
      "v_mfma_f32_16x16x32_bf16 v[136:139], a[208:211], v[92:95], v[136:139]\n"
      "global_store_dwordx4 v253, v[224:227], s[54:55] offset:768 sc1\n"
      "v_mfma_f32_16x16x32_bf16 v[136:139], a[212:215], v[108:111], v[136:139]\n"
      "s_add_u32 m0, s17, 0x10e00\n"
      "v_mfma_f32_16x16x32_bf16 v[136:139], a[216:219], v[112:115], v[136:139]\n"
      "global_load_lds_dwordx4 v237, s[52:53] offset:512 nt\n"
      "v_mfma_f32_16x16x32_bf16 v[136:139], a[220:223], v[116:119], v[136:139]\n"
      "v_lshlrev_b32 v14, 16, v151\n"
      "v_mfma_f32_16x16x32_bf16 v[132:135], a[224:227], v[36:39], v[16:19]\n"
      "v_and_b32 v15, 0xffff0000, v151\n"
      "v_mfma_f32_16x16x32_bf16 v[132:135], a[228:231], v[40:43], v[132:135]\n"
      "s_cbranch_vccnz LT23\n"
      "LS23:\n"
      "v_mfma_f32_16x16x32_bf16 v[132:135], a[232:235], v[44:47], v[132:135]\n"
      "v_mfma_f32_16x16x32_bf16 v[132:135], a[236:239], v[60:63], v[132:135]\n"
      "s_memrealtime s[84:85]\n"
      "v_mfma_f32_16x16x32_bf16 v[132:135], a[0:3], v[64:67], v[132:135]\n"
      "v_cvt_pk_bf16_f32 v214, v136, v137\n"
      "v_mfma_f32_16x16x32_bf16 v[132:135], a[4:7], v[68:71], v[132:135]\n"
      "v_cvt_pk_bf16_f32 v215, v138, v139\n"
      "v_mfma_f32_16x16x32_bf16 v[132:135], a[8:11], v[84:87], v[132:135]\n"
      "v_lshlrev_b32 v16, 16, v152\n"
      "v_mfma_f32_16x16x32_bf16 v[132:135], a[12:15], v[88:91], v[132:135]\n"
      "s_add_u32 s83, s83, s86\n"
      "v_mfma_f32_16x16x32_bf16 v[132:135], a[16:19], v[92:95], v[132:135]\n"
      "v_and_b32 v17, 0xffff0000, v152\n"
      "v_mfma_f32_16x16x32_bf16 v[132:135], a[20:23], v[108:111], v[132:135]\n"
      "v_lshlrev_b32 v18, 16, v153\n"
      "v_mfma_f32_16x16x32_bf16 v[132:135], v[140:143], v[112:115], v[132:135]\n"
      "s_waitcnt lgkmcnt(0)\n"
      "s_sub_u32 s84, s84, s83\n"
      "v_mfma_f32_16x16x32_bf16 v[132:135], v[156:159], v[116:119], v[132:135]\n"
      "s_cmp_lt_i32 s84, 0\n"
      "s_cselect_b64 vcc, -1, 0\n"
      "s_waitcnt vmcnt(24)\n"
      "s_barrier\n"
      "v_mfma_f32_16x16x32_bf16 v[136:139], v[160:163], v[36:39], v[20:23]\n"
      "ds_read_b128 v[36:39], v232 offset:0\n"
      "ds_write_b128 v245, v[212:215] offset:0\n"
      "v_mfma_f32_16x16x32_bf16 v[136:139], v[164:167], v[40:43], v[136:139]\n"
      "ds_read_b128 v[40:43], v233 offset:0\n"
      "v_mfma_f32_16x16x32_bf16 v[136:139], v[180:183], v[44:47], v[136:139]\n"
      "ds_read_b128 v[44:47], v234 offset:0\n"
      "v_mfma_f32_16x16x32_bf16 v[136:139], v[184:187], v[60:63], v[136:139]\n"
      "ds_read_b128 v[60:63], v235 offset:0\n"
      "v_cvt_pk_bf16_f32 v208, v132, v133\n"
      "v_mfma_f32_16x16x32_bf16 v[136:139], v[188:191], v[64:67], v[136:139]\n"
      "ds_read_b128 v[64:67], v232 offset:8192\n"
      "v_cvt_pk_bf16_f32 v209, v134, v135\n"
      "v_mfma_f32_16x16x32_bf16 v[136:139], v[204:207], v[68:71], v[136:139]\n"
      "ds_read_b128 v[68:71], v233 offset:8192\n"
      "v_mfma_f32_16x16x32_bf16 v[136:139], a[24:27], v[84:87], v[136:139]\n"
      "ds_read_b128 v[84:87], v234 offset:8192\n"
      "v_mfma_f32_16x16x32_bf16 v[136:139], a[28:31], v[88:91], v[136:139]\n"
      "ds_read_b128 v[88:91], v235 offset:8192\n"
      "v_mfma_f32_16x16x32_bf16 v[136:139], a[32:35], v[92:95], v[136:139]\n"
      "ds_read_b128 v[92:95], v232 offset:16384\n"
      "v_mfma_f32_16x16x32_bf16 v[136:139], a[36:39], v[108:111], v[136:139]\n"
      "ds_read_b128 v[108:111], v233 offset:16384\n"
      "v_mfma_f32_16x16x32_bf16 v[136:139], a[40:43], v[112:115], v[136:139]\n"
      "ds_read_b128 v[112:115], v234 offset:16384\n"
      "v_mfma_f32_16x16x32_bf16 v[136:139], a[44:47], v[116:119], v[136:139]\n"
      "ds_read_b128 v[116:119], v235 offset:16384\n"
      "s_waitcnt lgkmcnt(8)\n"
      "v_mfma_f32_16x16x32_bf16 v[132:135], a[240:243], v[36:39], v[0:3]\n"
      "v_and_b32 v19, 0xffff0000, v153\n"
      "s_cbranch_vccnz LT24\n"
      "LS24:\n"
      "v_mfma_f32_16x16x32_bf16 v[132:135], a[244:247], v[40:43], v[132:135]\n"
      "v_lshlrev_b32 v20, 16, v154\n"
      "v_mfma_f32_16x16x32_bf16 v[132:135], a[248:251], v[44:47], v[132:135]\n"
      "v_and_b32 v21, 0xffff0000, v154\n"
      "v_mfma_f32_16x16x32_bf16 v[132:135], a[252:255], v[60:63], v[132:135]\n"
      "v_lshlrev_b32 v22, 16, v155\n"
      "s_waitcnt lgkmcnt(4)\n"
      "v_mfma_f32_16x16x32_bf16 v[132:135], a[48:51], v[64:67], v[132:135]\n"
      "v_and_b32 v23, 0xffff0000, v155\n"
      "v_mfma_f32_16x16x32_bf16 v[132:135], a[52:55], v[68:71], v[132:135]\n"
      "v_cvt_pk_bf16_f32 v210, v136, v137\n"
      "v_mfma_f32_16x16x32_bf16 v[132:135], a[56:59], v[84:87], v[132:135]\n"
      "v_cvt_pk_bf16_f32 v211, v138, v139\n"
      "v_mfma_f32_16x16x32_bf16 v[132:135], a[60:63], v[88:91], v[132:135]\n"
      "s_add_u32 s54, s44, s37\n"
      "s_addc_u32 s55, s45, 0\n"
      "s_waitcnt lgkmcnt(0)\n"
      "v_mfma_f32_16x16x32_bf16 v[132:135], a[64:67], v[92:95], v[132:135]\n"
      "ds_read_b128 v[216:219], v246 offset:24576\n"
      "v_mfma_f32_16x16x32_bf16 v[132:135], a[68:71], v[108:111], v[132:135]\n"
      "ds_read_b128 v[220:223], v246 offset:25600\n"
      "v_mfma_f32_16x16x32_bf16 v[132:135], a[72:75], v[112:115], v[132:135]\n"
      "ds_read_b128 v[224:227], v246 offset:26624\n"
      "v_mfma_f32_16x16x32_bf16 v[132:135], a[76:79], v[116:119], v[132:135]\n"
      "ds_write_b128 v244, v[208:211] offset:128\n"
      "v_mfma_f32_16x16x32_bf16 v[136:139], a[80:83], v[36:39], v[4:7]\n"
      "v_mfma_f32_16x16x32_bf16 v[136:139], a[84:87], v[40:43], v[136:139]\n"
      "v_mfma_f32_16x16x32_bf16 v[136:139], a[88:91], v[44:47], v[136:139]\n"
      "v_mfma_f32_16x16x32_bf16 v[136:139], a[92:95], v[60:63], v[136:139]\n"
      "v_cvt_pk_bf16_f32 v208, v132, v133\n"
      "v_mfma_f32_16x16x32_bf16 v[136:139], a[96:99], v[64:67], v[136:139]\n"
      "v_cvt_pk_bf16_f32 v209, v134, v135\n"
      "v_mfma_f32_16x16x32_bf16 v[136:139], a[100:103], v[68:71], v[136:139]\n"
      "s_waitcnt lgkmcnt(3)\n"
      "v_mfma_f32_16x16x32_bf16 v[136:139], a[104:107], v[84:87], v[136:139]\n"
      "global_store_dwordx4 v247, v[216:219], s[54:55] offset:0 sc1\n"
      "v_mfma_f32_16x16x32_bf16 v[136:139], a[108:111], v[88:91], v[136:139]\n"
      "s_add_u32 s52, s42, s39\n"
      "s_addc_u32 s53, s43, 0\n"
      "v_mfma_f32_16x16x32_bf16 v[136:139], a[112:115], v[92:95], v[136:139]\n"
      "s_add_u32 m0, s17, 0x12000\n"
      "v_mfma_f32_16x16x32_bf16 v[136:139], a[116:119], v[108:111], v[136:139]\n"
      "global_load_lds_dwordx4 v236, s[52:53] offset:0 nt\n"
      "v_mfma_f32_16x16x32_bf16 v[136:139], a[120:123], v[112:115], v[136:139]\n"
      "v_mfma_f32_16x16x32_bf16 v[136:139], a[124:127], v[116:119], v[136:139]\n"
      "v_mfma_f32_16x16x32_bf16 v[132:135], a[128:131], v[36:39], v[8:11]\n"
      "s_cbranch_vccnz LT25\n"
      "LS25:\n"
      "v_mfma_f32_16x16x32_bf16 v[132:135], a[132:135], v[40:43], v[132:135]\n"
      "v_mfma_f32_16x16x32_bf16 v[132:135], a[136:139], v[44:47], v[132:135]\n"
      "v_mfma_f32_16x16x32_bf16 v[132:135], a[140:143], v[60:63], v[132:135]\n"
      "v_cvt_pk_bf16_f32 v210, v136, v137\n"
      "v_mfma_f32_16x16x32_bf16 v[132:135], a[144:147], v[64:67], v[132:135]\n"
      "v_cvt_pk_bf16_f32 v211, v138, v139\n"
      "v_mfma_f32_16x16x32_bf16 v[132:135], a[148:151], v[68:71], v[132:135]\n"
      "v_mfma_f32_16x16x32_bf16 v[132:135], a[152:155], v[84:87], v[132:135]\n"
      "s_waitcnt lgkmcnt(2)\n"
      "v_mfma_f32_16x16x32_bf16 v[132:135], a[156:159], v[88:91], v[132:135]\n"
      "global_store_dwordx4 v252, v[220:223], s[54:55] offset:0 sc1\n"
      "v_mfma_f32_16x16x32_bf16 v[132:135], a[160:163], v[92:95], v[132:135]\n"
      "s_add_u32 m0, s17, 0x13f00\n"
      "v_mfma_f32_16x16x32_bf16 v[132:135], a[164:167], v[108:111], v[132:135]\n"
      "ds_write_b128 v244, v[208:211] offset:12288\n"
      "v_mfma_f32_16x16x32_bf16 v[132:135], a[168:171], v[112:115], v[132:135]\n"
      "global_load_lds_dwordx4 v236, s[52:53] offset:256 nt\n"
      "v_mfma_f32_16x16x32_bf16 v[132:135], a[172:175], v[116:119], v[132:135]\n"
      "v_mfma_f32_16x16x32_bf16 v[136:139], a[176:179], v[36:39], v[12:15]\n"
      "v_mfma_f32_16x16x32_bf16 v[136:139], a[180:183], v[40:43], v[136:139]\n"
      "v_mfma_f32_16x16x32_bf16 v[136:139], a[184:187], v[44:47], v[136:139]\n"
      "v_mfma_f32_16x16x32_bf16 v[136:139], a[188:191], v[60:63], v[136:139]\n"
      "v_cvt_pk_bf16_f32 v212, v132, v133\n"
      "v_mfma_f32_16x16x32_bf16 v[136:139], a[192:195], v[64:67], v[136:139]\n"
      "v_cvt_pk_bf16_f32 v213, v134, v135\n"
      "v_mfma_f32_16x16x32_bf16 v[136:139], a[196:199], v[68:71], v[136:139]\n"
      "v_mfma_f32_16x16x32_bf16 v[136:139], a[200:203], v[84:87], v[136:139]\n"
      "v_mfma_f32_16x16x32_bf16 v[136:139], a[204:207], v[88:91], v[136:139]\n"
      "s_waitcnt lgkmcnt(2)\n"
      "v_mfma_f32_16x16x32_bf16 v[136:139], a[208:211], v[92:95], v[136:139]\n"
      "global_store_dwordx4 v253, v[224:227], s[54:55] offset:0 sc1\n"
      "v_mfma_f32_16x16x32_bf16 v[136:139], a[212:215], v[108:111], v[136:139]\n"
      "s_add_u32 m0, s17, 0x15e00\n"
      "v_mfma_f32_16x16x32_bf16 v[136:139], a[216:219], v[112:115], v[136:139]\n"
      "global_load_lds_dwordx4 v236, s[52:53] offset:512 nt\n"
      "v_mfma_f32_16x16x32_bf16 v[136:139], a[220:223], v[116:119], v[136:139]\n"
      "v_mfma_f32_16x16x32_bf16 v[132:135], a[224:227], v[36:39], v[16:19]\n"
      "s_cbranch_vccnz LT26\n"
      "LS26:\n"
      "v_mfma_f32_16x16x32_bf16 v[132:135], a[228:231], v[40:43], v[132:135]\n"
      "v_mfma_f32_16x16x32_bf16 v[132:135], a[232:235], v[44:47], v[132:135]\n"
      "v_mfma_f32_16x16x32_bf16 v[132:135], a[236:239], v[60:63], v[132:135]\n"
      "v_cvt_pk_bf16_f32 v214, v136, v137\n"
      "v_mfma_f32_16x16x32_bf16 v[132:135], a[0:3], v[64:67], v[132:135]\n"
      "v_cvt_pk_bf16_f32 v215, v138, v139\n"
      "v_mfma_f32_16x16x32_bf16 v[132:135], a[4:7], v[68:71], v[132:135]\n"
      "v_mfma_f32_16x16x32_bf16 v[132:135], a[8:11], v[84:87], v[132:135]\n"
      "v_mfma_f32_16x16x32_bf16 v[132:135], a[12:15], v[88:91], v[132:135]\n"
      "v_mfma_f32_16x16x32_bf16 v[132:135], a[16:19], v[92:95], v[132:135]\n"
      "v_mfma_f32_16x16x32_bf16 v[132:135], a[20:23], v[108:111], v[132:135]\n"
      "ds_write_b128 v245, v[212:215] offset:12288\n"
      "v_mfma_f32_16x16x32_bf16 v[132:135], v[140:143], v[112:115], v[132:135]\n"
      "v_mfma_f32_16x16x32_bf16 v[132:135], v[156:159], v[116:119], v[132:135]\n"
      "s_waitcnt lgkmcnt(2)\n"
      "s_barrier\n"
      "v_mfma_f32_16x16x32_bf16 v[136:139], v[160:163], v[36:39], v[20:23]\n"
      "ds_read_b128 v[36:39], v232 offset:256\n"
      "v_mfma_f32_16x16x32_bf16 v[136:139], v[164:167], v[40:43], v[136:139]\n"
      "ds_read_b128 v[40:43], v233 offset:256\n"
      "v_mfma_f32_16x16x32_bf16 v[136:139], v[180:183], v[44:47], v[136:139]\n"
      "ds_read_b128 v[44:47], v234 offset:256\n"
      "v_mfma_f32_16x16x32_bf16 v[136:139], v[184:187], v[60:63], v[136:139]\n"
      "ds_read_b128 v[60:63], v235 offset:256\n"
      "v_cvt_pk_bf16_f32 v208, v132, v133\n"
      "v_mfma_f32_16x16x32_bf16 v[136:139], v[188:191], v[64:67], v[136:139]\n"
      "ds_read_b128 v[64:67], v232 offset:8448\n"
      "v_cvt_pk_bf16_f32 v209, v134, v135\n"
      "v_mfma_f32_16x16x32_bf16 v[136:139], v[204:207], v[68:71], v[136:139]\n"
      "ds_read_b128 v[68:71], v233 offset:8448\n"
      "v_mfma_f32_16x16x32_bf16 v[136:139], a[24:27], v[84:87], v[136:139]\n"
      "ds_read_b128 v[84:87], v234 offset:8448\n"
      "v_mfma_f32_16x16x32_bf16 v[136:139], a[28:31], v[88:91], v[136:139]\n"
      "ds_read_b128 v[88:91], v235 offset:8448\n"
      "v_mfma_f32_16x16x32_bf16 v[136:139], a[32:35], v[92:95], v[136:139]\n"
      "ds_read_b128 v[92:95], v232 offset:16640\n"
      "v_mfma_f32_16x16x32_bf16 v[136:139], a[36:39], v[108:111], v[136:139]\n"
      "ds_read_b128 v[108:111], v233 offset:16640\n"
      "v_mfma_f32_16x16x32_bf16 v[136:139], a[40:43], v[112:115], v[136:139]\n"
      "ds_read_b128 v[112:115], v234 offset:16640\n"
      "v_mfma_f32_16x16x32_bf16 v[136:139], a[44:47], v[116:119], v[136:139]\n"
      "ds_read_b128 v[116:119], v235 offset:16640\n"
      "s_waitcnt lgkmcnt(8)\n"
      "v_mfma_f32_16x16x32_bf16 v[132:135], a[240:243], v[36:39], v[0:3]\n"
      "s_cbranch_vccnz LT27\n"
      "LS27:\n"
      "v_mfma_f32_16x16x32_bf16 v[132:135], a[244:247], v[40:43], v[132:135]\n"
      "v_mfma_f32_16x16x32_bf16 v[132:135], a[248:251], v[44:47], v[132:135]\n"
      "v_mfma_f32_16x16x32_bf16 v[132:135], a[252:255], v[60:63], v[132:135]\n"
      "v_cvt_pk_bf16_f32 v210, v136, v137\n"
      "s_waitcnt lgkmcnt(4)\n"
      "v_mfma_f32_16x16x32_bf16 v[132:135], a[48:51], v[64:67], v[132:135]\n"
      "v_cvt_pk_bf16_f32 v211, v138, v139\n"
      "v_mfma_f32_16x16x32_bf16 v[132:135], a[52:55], v[68:71], v[132:135]\n"
      "v_mfma_f32_16x16x32_bf16 v[132:135], a[56:59], v[84:87], v[132:135]\n"
      "ds_read_b128 v[216:219], v246 offset:0\n"
      "v_mfma_f32_16x16x32_bf16 v[132:135], a[60:63], v[88:91], v[132:135]\n"
      "ds_read_b128 v[220:223], v246 offset:1024\n"
      "s_waitcnt lgkmcnt(2)\n"
      "v_mfma_f32_16x16x32_bf16 v[132:135], a[64:67], v[92:95], v[132:135]\n"
      "ds_read_b128 v[224:227], v246 offset:2048\n"
      "v_mfma_f32_16x16x32_bf16 v[132:135], a[68:71], v[108:111], v[132:135]\n"
      "ds_write_b128 v244, v[208:211] offset:12416\n"
      "v_mfma_f32_16x16x32_bf16 v[132:135], a[72:75], v[112:115], v[132:135]\n"
      "v_mfma_f32_16x16x32_bf16 v[132:135], a[76:79], v[116:119], v[132:135]\n"
      "v_mfma_f32_16x16x32_bf16 v[136:139], a[80:83], v[36:39], v[4:7]\n"
      "v_mfma_f32_16x16x32_bf16 v[136:139], a[84:87], v[40:43], v[136:139]\n"
      "v_mfma_f32_16x16x32_bf16 v[136:139], a[88:91], v[44:47], v[136:139]\n"
      "v_mfma_f32_16x16x32_bf16 v[136:139], a[92:95], v[60:63], v[136:139]\n"
      "v_cvt_pk_bf16_f32 v208, v132, v133\n"
      "v_mfma_f32_16x16x32_bf16 v[136:139], a[96:99], v[64:67], v[136:139]\n"
      "v_cvt_pk_bf16_f32 v209, v134, v135\n"
      "v_mfma_f32_16x16x32_bf16 v[136:139], a[100:103], v[68:71], v[136:139]\n"
      "s_waitcnt lgkmcnt(3)\n"
      "v_mfma_f32_16x16x32_bf16 v[136:139], a[104:107], v[84:87], v[136:139]\n"
      "global_store_dwordx4 v247, v[216:219], s[54:55] offset:768 sc1\n"
      "v_mfma_f32_16x16x32_bf16 v[136:139], a[108:111], v[88:91], v[136:139]\n"
      "s_add_u32 m0, s17, 0x13000\n"
      "v_mfma_f32_16x16x32_bf16 v[136:139], a[112:115], v[92:95], v[136:139]\n"
      "global_load_lds_dwordx4 v237, s[52:53] offset:0 nt\n"
      "v_mfma_f32_16x16x32_bf16 v[136:139], a[116:119], v[108:111], v[136:139]\n"
      "v_mfma_f32_16x16x32_bf16 v[136:139], a[120:123], v[112:115], v[136:139]\n"
      "v_mfma_f32_16x16x32_bf16 v[136:139], a[124:127], v[116:119], v[136:139]\n"
      "v_mfma_f32_16x16x32_bf16 v[132:135], a[128:131], v[36:39], v[8:11]\n"
      "s_cbranch_vccnz LT28\n"
      "LS28:\n"
      "v_mfma_f32_16x16x32_bf16 v[132:135], a[132:135], v[40:43], v[132:135]\n"
      "v_mfma_f32_16x16x32_bf16 v[132:135], a[136:139], v[44:47], v[132:135]\n"
      "v_mfma_f32_16x16x32_bf16 v[132:135], a[140:143], v[60:63], v[132:135]\n"
      "v_cvt_pk_bf16_f32 v210, v136, v137\n"
      "v_mfma_f32_16x16x32_bf16 v[132:135], a[144:147], v[64:67], v[132:135]\n"
      "v_cvt_pk_bf16_f32 v211, v138, v139\n"
      "v_mfma_f32_16x16x32_bf16 v[132:135], a[148:151], v[68:71], v[132:135]\n"
      "v_mfma_f32_16x16x32_bf16 v[132:135], a[152:155], v[84:87], v[132:135]\n"
      "s_waitcnt lgkmcnt(2)\n"
      "v_mfma_f32_16x16x32_bf16 v[132:135], a[156:159], v[88:91], v[132:135]\n"
      "global_store_dwordx4 v252, v[220:223], s[54:55] offset:768 sc1\n"
      "v_mfma_f32_16x16x32_bf16 v[132:135], a[160:163], v[92:95], v[132:135]\n"
      "s_add_u32 m0, s17, 0x14f00\n"
      "v_mfma_f32_16x16x32_bf16 v[132:135], a[164:167], v[108:111], v[132:135]\n"
      "ds_write_b128 v244, v[208:211] offset:24576\n"
      "v_mfma_f32_16x16x32_bf16 v[132:135], a[168:171], v[112:115], v[132:135]\n"
      "global_load_lds_dwordx4 v237, s[52:53] offset:256 nt\n"
      "v_mfma_f32_16x16x32_bf16 v[132:135], a[172:175], v[116:119], v[132:135]\n"
      "v_mfma_f32_16x16x32_bf16 v[136:139], a[176:179], v[36:39], v[12:15]\n"
      "v_mfma_f32_16x16x32_bf16 v[136:139], a[180:183], v[40:43], v[136:139]\n"
      "v_mfma_f32_16x16x32_bf16 v[136:139], a[184:187], v[44:47], v[136:139]\n"
      "v_mfma_f32_16x16x32_bf16 v[136:139], a[188:191], v[60:63], v[136:139]\n"
      "v_cvt_pk_bf16_f32 v212, v132, v133\n"
      "v_mfma_f32_16x16x32_bf16 v[136:139], a[192:195], v[64:67], v[136:139]\n"
      "v_cvt_pk_bf16_f32 v213, v134, v135\n"
      "v_mfma_f32_16x16x32_bf16 v[136:139], a[196:199], v[68:71], v[136:139]\n"
      "v_mfma_f32_16x16x32_bf16 v[136:139], a[200:203], v[84:87], v[136:139]\n"
      "v_mfma_f32_16x16x32_bf16 v[136:139], a[204:207], v[88:91], v[136:139]\n"
      "s_waitcnt lgkmcnt(2)\n"
      "v_mfma_f32_16x16x32_bf16 v[136:139], a[208:211], v[92:95], v[136:139]\n"
      "global_store_dwordx4 v253, v[224:227], s[54:55] offset:768 sc1\n"
      "v_mfma_f32_16x16x32_bf16 v[136:139], a[212:215], v[108:111], v[136:139]\n"
      "s_add_u32 m0, s17, 0x16e00\n"
      "v_mfma_f32_16x16x32_bf16 v[136:139], a[216:219], v[112:115], v[136:139]\n"
      "global_load_lds_dwordx4 v237, s[52:53] offset:512 nt\n"
      "v_mfma_f32_16x16x32_bf16 v[136:139], a[220:223], v[116:119], v[136:139]\n"
      "v_mfma_f32_16x16x32_bf16 v[132:135], a[224:227], v[36:39], v[16:19]\n"
      "s_cbranch_vccnz LT29\n"
      "LS29:\n"
      "v_mfma_f32_16x16x32_bf16 v[132:135], a[228:231], v[40:43], v[132:135]\n"
      "v_mfma_f32_16x16x32_bf16 v[132:135], a[232:235], v[44:47], v[132:135]\n"
      "v_mfma_f32_16x16x32_bf16 v[132:135], a[236:239], v[60:63], v[132:135]\n"
      "s_memrealtime s[84:85]\n"
      "v_mfma_f32_16x16x32_bf16 v[132:135], a[0:3], v[64:67], v[132:135]\n"
      "v_cvt_pk_bf16_f32 v214, v136, v137\n"
      "v_mfma_f32_16x16x32_bf16 v[132:135], a[4:7], v[68:71], v[132:135]\n"
      "v_cvt_pk_bf16_f32 v215, v138, v139\n"
      "v_mfma_f32_16x16x32_bf16 v[132:135], a[8:11], v[84:87], v[132:135]\n"
      "v_mfma_f32_16x16x32_bf16 v[132:135], a[12:15], v[88:91], v[132:135]\n"
      "s_add_u32 s83, s83, s86\n"
      "v_mfma_f32_16x16x32_bf16 v[132:135], a[16:19], v[92:95], v[132:135]\n"
      "v_mfma_f32_16x16x32_bf16 v[132:135], a[20:23], v[108:111], v[132:135]\n"
      "v_mfma_f32_16x16x32_bf16 v[132:135], v[140:143], v[112:115], v[132:135]\n"
      "s_waitcnt lgkmcnt(0)\n"
      "s_sub_u32 s84, s84, s83\n"
      "v_mfma_f32_16x16x32_bf16 v[132:135], v[156:159], v[116:119], v[132:135]\n"
      "s_cmp_lt_i32 s84, 0\n"
      "s_cselect_b64 vcc, -1, 0\n"
      "s_waitcnt vmcnt(24)\n"
      "s_barrier\n"
      "v_mfma_f32_16x16x32_bf16 v[136:139], v[160:163], v[36:39], v[20:23]\n"
      "ds_read_b128 v[36:39], v232 offset:24576\n"
      "ds_write_b128 v245, v[212:215] offset:24576\n"
      "v_mfma_f32_16x16x32_bf16 v[136:139], v[164:167], v[40:43], v[136:139]\n"
      "ds_read_b128 v[40:43], v233 offset:24576\n"
      "v_mfma_f32_16x16x32_bf16 v[136:139], v[180:183], v[44:47], v[136:139]\n"
      "ds_read_b128 v[44:47], v234 offset:24576\n"
      "v_mfma_f32_16x16x32_bf16 v[136:139], v[184:187], v[60:63], v[136:139]\n"
      "ds_read_b128 v[60:63], v235 offset:24576\n"
      "v_cvt_pk_bf16_f32 v208, v132, v133\n"
      "v_mfma_f32_16x16x32_bf16 v[136:139], v[188:191], v[64:67], v[136:139]\n"
      "ds_read_b128 v[64:67], v232 offset:32768\n"
      "v_cvt_pk_bf16_f32 v209, v134, v135\n"
      "v_mfma_f32_16x16x32_bf16 v[136:139], v[204:207], v[68:71], v[136:139]\n"
      "ds_read_b128 v[68:71], v233 offset:32768\n"
      "v_mfma_f32_16x16x32_bf16 v[136:139], a[24:27], v[84:87], v[136:139]\n"
      "ds_read_b128 v[84:87], v234 offset:32768\n"
      "v_mfma_f32_16x16x32_bf16 v[136:139], a[28:31], v[88:91], v[136:139]\n"
      "ds_read_b128 v[88:91], v235 offset:32768\n"
      "v_mfma_f32_16x16x32_bf16 v[136:139], a[32:35], v[92:95], v[136:139]\n"
      "ds_read_b128 v[92:95], v232 offset:40960\n"
      "v_mfma_f32_16x16x32_bf16 v[136:139], a[36:39], v[108:111], v[136:139]\n"
      "ds_read_b128 v[108:111], v233 offset:40960\n"
      "v_mfma_f32_16x16x32_bf16 v[136:139], a[40:43], v[112:115], v[136:139]\n"
      "ds_read_b128 v[112:115], v234 offset:40960\n"
      "v_mfma_f32_16x16x32_bf16 v[136:139], a[44:47], v[116:119], v[136:139]\n"
      "ds_read_b128 v[116:119], v235 offset:40960\n"
      "s_waitcnt lgkmcnt(8)\n"
      "v_mfma_f32_16x16x32_bf16 v[132:135], a[240:243], v[36:39], v[0:3]\n"
      "s_cbranch_vccnz LT30\n"
      "LS30:\n"
      "v_mfma_f32_16x16x32_bf16 v[132:135], a[244:247], v[40:43], v[132:135]\n"
      "v_mfma_f32_16x16x32_bf16 v[132:135], a[248:251], v[44:47], v[132:135]\n"
      "v_mfma_f32_16x16x32_bf16 v[132:135], a[252:255], v[60:63], v[132:135]\n"
      "v_cvt_pk_bf16_f32 v210, v136, v137\n"
      "s_lshl_b32 s79, s95, 1\n"
      "s_waitcnt lgkmcnt(4)\n"
      "v_mfma_f32_16x16x32_bf16 v[132:135], a[48:51], v[64:67], v[132:135]\n"
      "v_cvt_pk_bf16_f32 v211, v138, v139\n"
      "s_mul_i32 s78, s97, s19\n"
      "v_mfma_f32_16x16x32_bf16 v[132:135], a[52:55], v[68:71], v[132:135]\n"
      "v_mad_i32_i24 v252, v251, s79, v252\n"
      "v_add_u32 v236, s78, v236\n"
      "v_mfma_f32_16x16x32_bf16 v[132:135], a[56:59], v[84:87], v[132:135]\n"
      "ds_read_b128 v[24:27], v246 offset:12288\n"
      "s_mul_i32 s79, s79, s100\n"
      "v_mfma_f32_16x16x32_bf16 v[132:135], a[60:63], v[88:91], v[132:135]\n"
      "ds_read_b128 v[28:31], v246 offset:13312\n"
      "v_add_u32 v247, s79, v247\n"
      "s_waitcnt lgkmcnt(2)\n"
      "v_mfma_f32_16x16x32_bf16 v[132:135], a[64:67], v[92:95], v[132:135]\n"
      "ds_read_b128 v[32:35], v246 offset:14336\n"
      "s_mul_i32 s79, s79, s101\n"
      "v_mfma_f32_16x16x32_bf16 v[132:135], a[68:71], v[108:111], v[132:135]\n"
      "ds_write_b128 v244, v[208:211] offset:24704\n"
      "s_mul_i32 s78, s78, s99\n"
      "v_mfma_f32_16x16x32_bf16 v[132:135], a[72:75], v[112:115], v[132:135]\n"
      "v_add_u32 v253, s79, v253\n"
      "v_add_u32 v237, s78, v237\n"
      "v_mfma_f32_16x16x32_bf16 v[132:135], a[76:79], v[116:119], v[132:135]\n"
      "s_add_u32 s54, s30, s38\n"
      "s_addc_u32 s55, s31, 0\n"
      "v_mfma_f32_16x16x32_bf16 v[136:139], a[80:83], v[36:39], v[4:7]\n"
      "s_waitcnt lgkmcnt(3)\n"
      "v_mfma_f32_16x16x32_bf16 v[136:139], a[84:87], v[40:43], v[136:139]\n"
      "global_store_dwordx4 v247, v[24:27], s[54:55] offset:0 sc1\n"
      "v_mfma_f32_16x16x32_bf16 v[136:139], a[88:91], v[44:47], v[136:139]\n"
      "v_mfma_f32_16x16x32_bf16 v[136:139], a[92:95], v[60:63], v[136:139]\n"
      "v_cvt_pk_bf16_f32 v208, v132, v133\n"
      "v_mfma_f32_16x16x32_bf16 v[136:139], a[96:99], v[64:67], v[136:139]\n"
      "v_cvt_pk_bf16_f32 v209, v134, v135\n"
      "v_mfma_f32_16x16x32_bf16 v[136:139], a[100:103], v[68:71], v[136:139]\n"
      "v_mfma_f32_16x16x32_bf16 v[136:139], a[104:107], v[84:87], v[136:139]\n"
      "v_mfma_f32_16x16x32_bf16 v[136:139], a[108:111], v[88:91], v[136:139]\n"
      "s_add_u32 s52, s20, s40\n"
      "s_addc_u32 s53, s21, 0\n"
      "v_mfma_f32_16x16x32_bf16 v[136:139], a[112:115], v[92:95], v[136:139]\n"
      "s_add_u32 m0, s17, 0x0\n"
      "v_mfma_f32_16x16x32_bf16 v[136:139], a[116:119], v[108:111], v[136:139]\n"
      "global_load_lds_dwordx4 v236, s[52:53] offset:0 nt\n"
      "v_mfma_f32_16x16x32_bf16 v[136:139], a[120:123], v[112:115], v[136:139]\n"
      "v_mfma_f32_16x16x32_bf16 v[136:139], a[124:127], v[116:119], v[136:139]\n"
      "v_mfma_f32_16x16x32_bf16 v[132:135], a[128:131], v[36:39], v[8:11]\n"
      "s_cbranch_vccnz LT31\n"
      "LS31:\n"
      "v_mfma_f32_16x16x32_bf16 v[132:135], a[132:135], v[40:43], v[132:135]\n"
      "v_mfma_f32_16x16x32_bf16 v[132:135], a[136:139], v[44:47], v[132:135]\n"
      "v_mfma_f32_16x16x32_bf16 v[132:135], a[140:143], v[60:63], v[132:135]\n"
      "v_cvt_pk_bf16_f32 v210, v136, v137\n"
      "v_mfma_f32_16x16x32_bf16 v[132:135], a[144:147], v[64:67], v[132:135]\n"
      "v_cvt_pk_bf16_f32 v211, v138, v139\n"
      "v_mfma_f32_16x16x32_bf16 v[132:135], a[148:151], v[68:71], v[132:135]\n"
      "v_mfma_f32_16x16x32_bf16 v[132:135], a[152:155], v[84:87], v[132:135]\n"
      "s_waitcnt lgkmcnt(2)\n"
      "v_mfma_f32_16x16x32_bf16 v[132:135], a[156:159], v[88:91], v[132:135]\n"
      "global_store_dwordx4 v252, v[28:31], s[54:55] offset:0 sc1\n"
      "v_mfma_f32_16x16x32_bf16 v[132:135], a[160:163], v[92:95], v[132:135]\n"
      "s_add_u32 m0, s17, 0x1f00\n"
      "v_mfma_f32_16x16x32_bf16 v[132:135], a[164:167], v[108:111], v[132:135]\n"
      "ds_write_b128 v244, v[208:211] offset:0\n"
      "v_mfma_f32_16x16x32_bf16 v[132:135], a[168:171], v[112:115], v[132:135]\n"
      "global_load_lds_dwordx4 v236, s[52:53] offset:256 nt\n"
      "v_mfma_f32_16x16x32_bf16 v[132:135], a[172:175], v[116:119], v[132:135]\n"
      "v_mfma_f32_16x16x32_bf16 v[136:139], a[176:179], v[36:39], v[12:15]\n"
      "v_mfma_f32_16x16x32_bf16 v[136:139], a[180:183], v[40:43], v[136:139]\n"
      "v_mfma_f32_16x16x32_bf16 v[136:139], a[184:187], v[44:47], v[136:139]\n"
      "v_mfma_f32_16x16x32_bf16 v[136:139], a[188:191], v[60:63], v[136:139]\n"
      "v_cvt_pk_bf16_f32 v212, v132, v133\n"
      "v_mfma_f32_16x16x32_bf16 v[136:139], a[192:195], v[64:67], v[136:139]\n"
      "v_cvt_pk_bf16_f32 v213, v134, v135\n"
      "v_mfma_f32_16x16x32_bf16 v[136:139], a[196:199], v[68:71], v[136:139]\n"
      "v_mfma_f32_16x16x32_bf16 v[136:139], a[200:203], v[84:87], v[136:139]\n"
      "v_mfma_f32_16x16x32_bf16 v[136:139], a[204:207], v[88:91], v[136:139]\n"
      "s_waitcnt lgkmcnt(2)\n"
      "v_mfma_f32_16x16x32_bf16 v[136:139], a[208:211], v[92:95], v[136:139]\n"
      "global_store_dwordx4 v253, v[32:35], s[54:55] offset:0 sc1\n"
      "v_mfma_f32_16x16x32_bf16 v[136:139], a[212:215], v[108:111], v[136:139]\n"
      "s_add_u32 m0, s17, 0x3e00\n"
      "v_mfma_f32_16x16x32_bf16 v[136:139], a[216:219], v[112:115], v[136:139]\n"
      "global_load_lds_dwordx4 v236, s[52:53] offset:512 nt\n"
      "v_mfma_f32_16x16x32_bf16 v[136:139], a[220:223], v[116:119], v[136:139]\n"
      "v_mfma_f32_16x16x32_bf16 v[132:135], a[224:227], v[36:39], v[16:19]\n"
      "s_cbranch_vccnz LT32\n"
      "LS32:\n"
      "v_mfma_f32_16x16x32_bf16 v[132:135], a[228:231], v[40:43], v[132:135]\n"
      "v_mfma_f32_16x16x32_bf16 v[132:135], a[232:235], v[44:47], v[132:135]\n"
      "v_mfma_f32_16x16x32_bf16 v[132:135], a[236:239], v[60:63], v[132:135]\n"
      "v_cvt_pk_bf16_f32 v214, v136, v137\n"
      "v_mfma_f32_16x16x32_bf16 v[132:135], a[0:3], v[64:67], v[132:135]\n"
      "v_cvt_pk_bf16_f32 v215, v138, v139\n"
      "v_mfma_f32_16x16x32_bf16 v[132:135], a[4:7], v[68:71], v[132:135]\n"
      "v_mfma_f32_16x16x32_bf16 v[132:135], a[8:11], v[84:87], v[132:135]\n"
      "v_mfma_f32_16x16x32_bf16 v[132:135], a[12:15], v[88:91], v[132:135]\n"
      "v_mfma_f32_16x16x32_bf16 v[132:135], a[16:19], v[92:95], v[132:135]\n"
      "v_mfma_f32_16x16x32_bf16 v[132:135], a[20:23], v[108:111], v[132:135]\n"
      "ds_write_b128 v245, v[212:215] offset:0\n"
      "v_mfma_f32_16x16x32_bf16 v[132:135], v[140:143], v[112:115], v[132:135]\n"
      "v_mfma_f32_16x16x32_bf16 v[132:135], v[156:159], v[116:119], v[132:135]\n"
      "s_waitcnt lgkmcnt(2)\n"
      "s_barrier\n"
      "v_mfma_f32_16x16x32_bf16 v[136:139], v[160:163], v[36:39], v[20:23]\n"
      "ds_read_b128 v[36:39], v232 offset:24832\n"
      "v_mfma_f32_16x16x32_bf16 v[136:139], v[164:167], v[40:43], v[136:139]\n"
      "ds_read_b128 v[40:43], v233 offset:24832\n"
      "v_mfma_f32_16x16x32_bf16 v[136:139], v[180:183], v[44:47], v[136:139]\n"
      "ds_read_b128 v[44:47], v234 offset:24832\n"
      "v_mfma_f32_16x16x32_bf16 v[136:139], v[184:187], v[60:63], v[136:139]\n"
      "ds_read_b128 v[60:63], v235 offset:24832\n"
      "v_cvt_pk_bf16_f32 v208, v132, v133\n"
      "v_mfma_f32_16x16x32_bf16 v[136:139], v[188:191], v[64:67], v[136:139]\n"
      "ds_read_b128 v[64:67], v232 offset:33024\n"
      "v_cvt_pk_bf16_f32 v209, v134, v135\n"
      "v_mfma_f32_16x16x32_bf16 v[136:139], v[204:207], v[68:71], v[136:139]\n"
      "ds_read_b128 v[68:71], v233 offset:33024\n"
      "v_mfma_f32_16x16x32_bf16 v[136:139], a[24:27], v[84:87], v[136:139]\n"
      "ds_read_b128 v[84:87], v234 offset:33024\n"
      "v_mfma_f32_16x16x32_bf16 v[136:139], a[28:31], v[88:91], v[136:139]\n"
      "ds_read_b128 v[88:91], v235 offset:33024\n"
      "v_mfma_f32_16x16x32_bf16 v[136:139], a[32:35], v[92:95], v[136:139]\n"
      "ds_read_b128 v[92:95], v232 offset:41216\n"
      "v_mfma_f32_16x16x32_bf16 v[136:139], a[36:39], v[108:111], v[136:139]\n"
      "ds_read_b128 v[108:111], v233 offset:41216\n"
      "v_mfma_f32_16x16x32_bf16 v[136:139], a[40:43], v[112:115], v[136:139]\n"
      "ds_read_b128 v[112:115], v234 offset:41216\n"
      "v_mfma_f32_16x16x32_bf16 v[136:139], a[44:47], v[116:119], v[136:139]\n"
      "ds_read_b128 v[116:119], v235 offset:41216\n"
      "s_waitcnt lgkmcnt(8)\n"
      "v_mfma_f32_16x16x32_bf16 v[132:135], a[240:243], v[36:39], v[0:3]\n"
      "v_add_u32 v232, 0xc000, v232\n"
      "s_cbranch_vccnz LT33\n"
      "LS33:\n"
      "v_mfma_f32_16x16x32_bf16 v[132:135], a[244:247], v[40:43], v[132:135]\n"
      "v_add_u32 v233, 0xc000, v233\n"
      "v_mfma_f32_16x16x32_bf16 v[132:135], a[248:251], v[44:47], v[132:135]\n"
      "v_add_u32 v234, 0xc000, v234\n"
      "v_mfma_f32_16x16x32_bf16 v[132:135], a[252:255], v[60:63], v[132:135]\n"
      "v_add_u32 v235, 0xc000, v235\n"
      "s_waitcnt lgkmcnt(4)\n"
      "v_mfma_f32_16x16x32_bf16 v[132:135], a[48:51], v[64:67], v[132:135]\n"
      "v_cvt_pk_bf16_f32 v210, v136, v137\n"
      "v_mfma_f32_16x16x32_bf16 v[132:135], a[52:55], v[68:71], v[132:135]\n"
      "v_cvt_pk_bf16_f32 v211, v138, v139\n"
      "v_mfma_f32_16x16x32_bf16 v[132:135], a[56:59], v[84:87], v[132:135]\n"
      "ds_read_b128 v[48:51], v246 offset:24576\n"
      "v_mfma_f32_16x16x32_bf16 v[132:135], a[60:63], v[88:91], v[132:135]\n"
      "ds_read_b128 v[52:55], v246 offset:25600\n"
      "s_waitcnt lgkmcnt(2)\n"
      "v_mfma_f32_16x16x32_bf16 v[132:135], a[64:67], v[92:95], v[132:135]\n"
      "ds_read_b128 v[56:59], v246 offset:26624\n"
      "v_mfma_f32_16x16x32_bf16 v[132:135], a[68:71], v[108:111], v[132:135]\n"
      "v_lshlrev_b32 v0, 16, v168\n"
      "v_mfma_f32_16x16x32_bf16 v[132:135], a[72:75], v[112:115], v[132:135]\n"
      "ds_write_b128 v244, v[208:211] offset:128\n"
      "v_mfma_f32_16x16x32_bf16 v[132:135], a[76:79], v[116:119], v[132:135]\n"
      "v_and_b32 v1, 0xffff0000, v168\n"
      "v_mfma_f32_16x16x32_bf16 v[136:139], a[80:83], v[36:39], v[4:7]\n"
      "v_lshlrev_b32 v2, 16, v169\n"
      "v_mfma_f32_16x16x32_bf16 v[136:139], a[84:87], v[40:43], v[136:139]\n"
      "v_and_b32 v3, 0xffff0000, v169\n"
      "v_mfma_f32_16x16x32_bf16 v[136:139], a[88:91], v[44:47], v[136:139]\n"
      "v_mfma_f32_16x16x32_bf16 v[136:139], a[92:95], v[60:63], v[136:139]\n"
      "v_cvt_pk_bf16_f32 v208, v132, v133\n"
      "v_mfma_f32_16x16x32_bf16 v[136:139], a[96:99], v[64:67], v[136:139]\n"
      "v_cvt_pk_bf16_f32 v209, v134, v135\n"
      "v_mfma_f32_16x16x32_bf16 v[136:139], a[100:103], v[68:71], v[136:139]\n"
      "s_waitcnt lgkmcnt(3)\n"
      "v_mfma_f32_16x16x32_bf16 v[136:139], a[104:107], v[84:87], v[136:139]\n"
      "global_store_dwordx4 v247, v[48:51], s[54:55] offset:768 sc1\n"
      "v_mfma_f32_16x16x32_bf16 v[136:139], a[108:111], v[88:91], v[136:139]\n"
      "s_add_u32 m0, s17, 0x1000\n"
      "v_mfma_f32_16x16x32_bf16 v[136:139], a[112:115], v[92:95], v[136:139]\n"
      "global_load_lds_dwordx4 v237, s[52:53] offset:0 nt\n"
      "v_mfma_f32_16x16x32_bf16 v[136:139], a[116:119], v[108:111], v[136:139]\n"
      "v_lshlrev_b32 v4, 16, v170\n"
      "v_mfma_f32_16x16x32_bf16 v[136:139], a[120:123], v[112:115], v[136:139]\n"
      "v_and_b32 v5, 0xffff0000, v170\n"
      "v_mfma_f32_16x16x32_bf16 v[136:139], a[124:127], v[116:119], v[136:139]\n"
      "v_lshlrev_b32 v6, 16, v171\n"
      "v_mfma_f32_16x16x32_bf16 v[132:135], a[128:131], v[36:39], v[8:11]\n"
      "v_and_b32 v7, 0xffff0000, v171\n"
      "v_mfma_f32_16x16x32_bf16 v[132:135], a[132:135], v[40:43], v[132:135]\n"
      "s_cbranch_vccnz LT34\n"
      "LS34:\n"
      "v_mfma_f32_16x16x32_bf16 v[132:135], a[136:139], v[44:47], v[132:135]\n"
      "v_mfma_f32_16x16x32_bf16 v[132:135], a[140:143], v[60:63], v[132:135]\n"
      "LPD14:\n"
      "s_cbranch_execz LRET\n"
      "v_cvt_pk_bf16_f32 v210, v136, v137\n"
      "v_mfma_f32_16x16x32_bf16 v[132:135], a[144:147], v[64:67], v[132:135]\n"
      "v_cvt_pk_bf16_f32 v211, v138, v139\n"
      "v_mfma_f32_16x16x32_bf16 v[132:135], a[148:151], v[68:71], v[132:135]\n"
      "v_lshlrev_b32 v8, 16, v172\n"
      "v_mfma_f32_16x16x32_bf16 v[132:135], a[152:155], v[84:87], v[132:135]\n"
      "s_waitcnt lgkmcnt(2)\n"
      "v_mfma_f32_16x16x32_bf16 v[132:135], a[156:159], v[88:91], v[132:135]\n"
      "global_store_dwordx4 v252, v[52:55], s[54:55] offset:768 sc1\n"
      "v_mfma_f32_16x16x32_bf16 v[132:135], a[160:163], v[92:95], v[132:135]\n"
      "s_add_u32 m0, s17, 0x2f00\n"
      "v_mfma_f32_16x16x32_bf16 v[132:135], a[164:167], v[108:111], v[132:135]\n"
      "ds_write_b128 v244, v[208:211] offset:12288\n"
      "v_mfma_f32_16x16x32_bf16 v[132:135], a[168:171], v[112:115], v[132:135]\n"
      "global_load_lds_dwordx4 v237, s[52:53] offset:256 nt\n"
      "v_mfma_f32_16x16x32_bf16 v[132:135], a[172:175], v[116:119], v[132:135]\n"
      "v_and_b32 v9, 0xffff0000, v172\n"
      "v_mfma_f32_16x16x32_bf16 v[136:139], a[176:179], v[36:39], v[12:15]\n"
      "v_lshlrev_b32 v10, 16, v173\n"
      "v_mfma_f32_16x16x32_bf16 v[136:139], a[180:183], v[40:43], v[136:139]\n"
      "v_and_b32 v11, 0xffff0000, v173\n"
      "v_mfma_f32_16x16x32_bf16 v[136:139], a[184:187], v[44:47], v[136:139]\n"
      "v_mfma_f32_16x16x32_bf16 v[136:139], a[188:191], v[60:63], v[136:139]\n"
      "v_cvt_pk_bf16_f32 v212, v132, v133\n"
      "v_mfma_f32_16x16x32_bf16 v[136:139], a[192:195], v[64:67], v[136:139]\n"
      "v_cvt_pk_bf16_f32 v213, v134, v135\n"
      "v_mfma_f32_16x16x32_bf16 v[136:139], a[196:199], v[68:71], v[136:139]\n"
      "v_lshlrev_b32 v12, 16, v174\n"
      "v_mfma_f32_16x16x32_bf16 v[136:139], a[200:203], v[84:87], v[136:139]\n"
      "v_and_b32 v13, 0xffff0000, v174\n"
      "v_mfma_f32_16x16x32_bf16 v[136:139], a[204:207], v[88:91], v[136:139]\n"
      "s_waitcnt lgkmcnt(2)\n"
      "v_mfma_f32_16x16x32_bf16 v[136:139], a[208:211], v[92:95], v[136:139]\n"
      "global_store_dwordx4 v253, v[56:59], s[54:55] offset:768 sc1\n"
      "v_mfma_f32_16x16x32_bf16 v[136:139], a[212:215], v[108:111], v[136:139]\n"
      "s_add_u32 m0, s17, 0x4e00\n"
      "v_mfma_f32_16x16x32_bf16 v[136:139], a[216:219], v[112:115], v[136:139]\n"
      "global_load_lds_dwordx4 v237, s[52:53] offset:512 nt\n"
      "v_mfma_f32_16x16x32_bf16 v[136:139], a[220:223], v[116:119], v[136:139]\n"
      "v_lshlrev_b32 v14, 16, v175\n"
      "v_mfma_f32_16x16x32_bf16 v[132:135], a[224:227], v[36:39], v[16:19]\n"
      "v_and_b32 v15, 0xffff0000, v175\n"
      "v_mfma_f32_16x16x32_bf16 v[132:135], a[228:231], v[40:43], v[132:135]\n"
      "s_cbranch_vccnz LT35\n"
      "LS35:\n"
      "v_mfma_f32_16x16x32_bf16 v[132:135], a[232:235], v[44:47], v[132:135]\n"
      "v_mfma_f32_16x16x32_bf16 v[132:135], a[236:239], v[60:63], v[132:135]\n"
      "s_memrealtime s[84:85]\n"
      "v_mfma_f32_16x16x32_bf16 v[132:135], a[0:3], v[64:67], v[132:135]\n"
      "v_cvt_pk_bf16_f32 v214, v136, v137\n"
      "v_mfma_f32_16x16x32_bf16 v[132:135], a[4:7], v[68:71], v[132:135]\n"
      "v_cvt_pk_bf16_f32 v215, v138, v139\n"
      "v_mfma_f32_16x16x32_bf16 v[132:135], a[8:11], v[84:87], v[132:135]\n"
      "v_lshlrev_b32 v16, 16, v176\n"
      "v_mfma_f32_16x16x32_bf16 v[132:135], a[12:15], v[88:91], v[132:135]\n"
      "s_add_u32 s83, s83, s86\n"
      "v_mfma_f32_16x16x32_bf16 v[132:135], a[16:19], v[92:95], v[132:135]\n"
      "v_and_b32 v17, 0xffff0000, v176\n"
      "v_mfma_f32_16x16x32_bf16 v[132:135], a[20:23], v[108:111], v[132:135]\n"
      "v_lshlrev_b32 v18, 16, v177\n"
      "v_mfma_f32_16x16x32_bf16 v[132:135], v[140:143], v[112:115], v[132:135]\n"
      "s_waitcnt lgkmcnt(0)\n"
      "s_sub_u32 s84, s84, s83\n"
      "v_mfma_f32_16x16x32_bf16 v[132:135], v[156:159], v[116:119], v[132:135]\n"
      "s_cmp_lt_i32 s84, 0\n"
      "s_cselect_b64 vcc, -1, 0\n"
      "s_waitcnt vmcnt(24)\n"
      "s_barrier\n"
      "v_mfma_f32_16x16x32_bf16 v[136:139], v[160:163], v[36:39], v[20:23]\n"
      "ds_read_b128 v[36:39], v232 offset:0\n"
      "ds_write_b128 v245, v[212:215] offset:12288\n"
      "v_mfma_f32_16x16x32_bf16 v[136:139], v[164:167], v[40:43], v[136:139]\n"
      "ds_read_b128 v[40:43], v233 offset:0\n"
      "v_mfma_f32_16x16x32_bf16 v[136:139], v[180:183], v[44:47], v[136:139]\n"
      "ds_read_b128 v[44:47], v234 offset:0\n"
      "v_mfma_f32_16x16x32_bf16 v[136:139], v[184:187], v[60:63], v[136:139]\n"
      "ds_read_b128 v[60:63], v235 offset:0\n"
      "v_cvt_pk_bf16_f32 v208, v132, v133\n"
      "v_mfma_f32_16x16x32_bf16 v[136:139], v[188:191], v[64:67], v[136:139]\n"
      "ds_read_b128 v[64:67], v232 offset:8192\n"
      "v_cvt_pk_bf16_f32 v209, v134, v135\n"
      "v_mfma_f32_16x16x32_bf16 v[136:139], v[204:207], v[68:71], v[136:139]\n"
      "ds_read_b128 v[68:71], v233 offset:8192\n"
      "v_mfma_f32_16x16x32_bf16 v[136:139], a[24:27], v[84:87], v[136:139]\n"
      "ds_read_b128 v[84:87], v234 offset:8192\n"
      "v_mfma_f32_16x16x32_bf16 v[136:139], a[28:31], v[88:91], v[136:139]\n"
      "ds_read_b128 v[88:91], v235 offset:8192\n"
      "v_mfma_f32_16x16x32_bf16 v[136:139], a[32:35], v[92:95], v[136:139]\n"
      "ds_read_b128 v[92:95], v232 offset:16384\n"
      "v_mfma_f32_16x16x32_bf16 v[136:139], a[36:39], v[108:111], v[136:139]\n"
      "ds_read_b128 v[108:111], v233 offset:16384\n"
      "v_mfma_f32_16x16x32_bf16 v[136:139], a[40:43], v[112:115], v[136:139]\n"
      "ds_read_b128 v[112:115], v234 offset:16384\n"
      "v_mfma_f32_16x16x32_bf16 v[136:139], a[44:47], v[116:119], v[136:139]\n"
      "ds_read_b128 v[116:119], v235 offset:16384\n"
      "s_waitcnt lgkmcnt(8)\n"
      "v_mfma_f32_16x16x32_bf16 v[132:135], a[240:243], v[36:39], v[0:3]\n"
      "v_and_b32 v19, 0xffff0000, v177\n"
      "s_cbranch_vccnz LT36\n"
      "LS36:\n"
      "v_mfma_f32_16x16x32_bf16 v[132:135], a[244:247], v[40:43], v[132:135]\n"
      "v_lshlrev_b32 v20, 16, v178\n"
      "v_mfma_f32_16x16x32_bf16 v[132:135], a[248:251], v[44:47], v[132:135]\n"
      "v_and_b32 v21, 0xffff0000, v178\n"
      "v_mfma_f32_16x16x32_bf16 v[132:135], a[252:255], v[60:63], v[132:135]\n"
      "v_lshlrev_b32 v22, 16, v179\n"
      "s_waitcnt lgkmcnt(4)\n"
      "v_mfma_f32_16x16x32_bf16 v[132:135], a[48:51], v[64:67], v[132:135]\n"
      "v_and_b32 v23, 0xffff0000, v179\n"
      "v_mfma_f32_16x16x32_bf16 v[132:135], a[52:55], v[68:71], v[132:135]\n"
      "v_cvt_pk_bf16_f32 v210, v136, v137\n"
      "v_mfma_f32_16x16x32_bf16 v[132:135], a[56:59], v[84:87], v[132:135]\n"
      "v_cvt_pk_bf16_f32 v211, v138, v139\n"
      "v_mfma_f32_16x16x32_bf16 v[132:135], a[60:63], v[88:91], v[132:135]\n"
      "ds_read_b128 v[72:75], v246 offset:0\n"
      "s_waitcnt lgkmcnt(1)\n"
      "v_mfma_f32_16x16x32_bf16 v[132:135], a[64:67], v[92:95], v[132:135]\n"
      "ds_read_b128 v[76:79], v246 offset:1024\n"
      "v_mfma_f32_16x16x32_bf16 v[132:135], a[68:71], v[108:111], v[132:135]\n"
      "ds_read_b128 v[80:83], v246 offset:2048\n"
      "v_mfma_f32_16x16x32_bf16 v[132:135], a[72:75], v[112:115], v[132:135]\n"
      "v_mfma_f32_16x16x32_bf16 v[132:135], a[76:79], v[116:119], v[132:135]\n"
      "ds_write_b128 v244, v[208:211] offset:12416\n"
      "v_mfma_f32_16x16x32_bf16 v[136:139], a[80:83], v[36:39], v[4:7]\n"
      "v_mfma_f32_16x16x32_bf16 v[136:139], a[84:87], v[40:43], v[136:139]\n"
      "v_mfma_f32_16x16x32_bf16 v[136:139], a[88:91], v[44:47], v[136:139]\n"
      "v_mfma_f32_16x16x32_bf16 v[136:139], a[92:95], v[60:63], v[136:139]\n"
      "v_cvt_pk_bf16_f32 v208, v132, v133\n"
      "s_add_u32 s54, s44, s38\n"
      "s_addc_u32 s55, s45, 0\n"
      "v_mfma_f32_16x16x32_bf16 v[136:139], a[96:99], v[64:67], v[136:139]\n"
      "v_cvt_pk_bf16_f32 v209, v134, v135\n"
      "v_mfma_f32_16x16x32_bf16 v[136:139], a[100:103], v[68:71], v[136:139]\n"
      "s_waitcnt lgkmcnt(3)\n"
      "v_mfma_f32_16x16x32_bf16 v[136:139], a[104:107], v[84:87], v[136:139]\n"
      "global_store_dwordx4 v247, v[72:75], s[54:55] offset:0 sc1\n"
      "v_mfma_f32_16x16x32_bf16 v[136:139], a[108:111], v[88:91], v[136:139]\n"
      "s_add_u32 s52, s42, s40\n"
      "s_addc_u32 s53, s43, 0\n"
      "v_mfma_f32_16x16x32_bf16 v[136:139], a[112:115], v[92:95], v[136:139]\n"
      "s_add_u32 m0, s17, 0x6000\n"
      "v_mfma_f32_16x16x32_bf16 v[136:139], a[116:119], v[108:111], v[136:139]\n"
      "global_load_lds_dwordx4 v236, s[52:53] offset:0 nt\n"
      "v_mfma_f32_16x16x32_bf16 v[136:139], a[120:123], v[112:115], v[136:139]\n"
      "v_mfma_f32_16x16x32_bf16 v[136:139], a[124:127], v[116:119], v[136:139]\n"
      "v_mfma_f32_16x16x32_bf16 v[132:135], a[128:131], v[36:39], v[8:11]\n"
      "s_cbranch_vccnz LT37\n"
      "LS37:\n"
      "v_mfma_f32_16x16x32_bf16 v[132:135], a[132:135], v[40:43], v[132:135]\n"
      "v_mfma_f32_16x16x32_bf16 v[132:135], a[136:139], v[44:47], v[132:135]\n"
      "v_mfma_f32_16x16x32_bf16 v[132:135], a[140:143], v[60:63], v[132:135]\n"
      "v_cvt_pk_bf16_f32 v210, v136, v137\n"
      "v_mfma_f32_16x16x32_bf16 v[132:135], a[144:147], v[64:67], v[132:135]\n"
      "v_cvt_pk_bf16_f32 v211, v138, v139\n"
      "v_mfma_f32_16x16x32_bf16 v[132:135], a[148:151], v[68:71], v[132:135]\n"
      "v_mfma_f32_16x16x32_bf16 v[132:135], a[152:155], v[84:87], v[132:135]\n"
      "s_waitcnt lgkmcnt(2)\n"
      "v_mfma_f32_16x16x32_bf16 v[132:135], a[156:159], v[88:91], v[132:135]\n"
      "global_store_dwordx4 v252, v[76:79], s[54:55] offset:0 sc1\n"
      "v_mfma_f32_16x16x32_bf16 v[132:135], a[160:163], v[92:95], v[132:135]\n"
      "s_add_u32 m0, s17, 0x7f00\n"
      "v_mfma_f32_16x16x32_bf16 v[132:135], a[164:167], v[108:111], v[132:135]\n"
      "ds_write_b128 v244, v[208:211] offset:24576\n"
      "v_mfma_f32_16x16x32_bf16 v[132:135], a[168:171], v[112:115], v[132:135]\n"
      "global_load_lds_dwordx4 v236, s[52:53] offset:256 nt\n"
      "v_mfma_f32_16x16x32_bf16 v[132:135], a[172:175], v[116:119], v[132:135]\n"
      "v_mfma_f32_16x16x32_bf16 v[136:139], a[176:179], v[36:39], v[12:15]\n"
      "v_mfma_f32_16x16x32_bf16 v[136:139], a[180:183], v[40:43], v[136:139]\n"
      "v_mfma_f32_16x16x32_bf16 v[136:139], a[184:187], v[44:47], v[136:139]\n"
      "v_mfma_f32_16x16x32_bf16 v[136:139], a[188:191], v[60:63], v[136:139]\n"
      "v_cvt_pk_bf16_f32 v212, v132, v133\n"
      "v_mfma_f32_16x16x32_bf16 v[136:139], a[192:195], v[64:67], v[136:139]\n"
      "v_cvt_pk_bf16_f32 v213, v134, v135\n"
      "v_mfma_f32_16x16x32_bf16 v[136:139], a[196:199], v[68:71], v[136:139]\n"
      "v_mfma_f32_16x16x32_bf16 v[136:139], a[200:203], v[84:87], v[136:139]\n"
      "v_mfma_f32_16x16x32_bf16 v[136:139], a[204:207], v[88:91], v[136:139]\n"
      "s_waitcnt lgkmcnt(2)\n"
      "v_mfma_f32_16x16x32_bf16 v[136:139], a[208:211], v[92:95], v[136:139]\n"
      "global_store_dwordx4 v253, v[80:83], s[54:55] offset:0 sc1\n"
      "v_mfma_f32_16x16x32_bf16 v[136:139], a[212:215], v[108:111], v[136:139]\n"
      "s_add_u32 m0, s17, 0x9e00\n"
      "v_mfma_f32_16x16x32_bf16 v[136:139], a[216:219], v[112:115], v[136:139]\n"
      "global_load_lds_dwordx4 v236, s[52:53] offset:512 nt\n"
      "v_mfma_f32_16x16x32_bf16 v[136:139], a[220:223], v[116:119], v[136:139]\n"
      "v_mfma_f32_16x16x32_bf16 v[132:135], a[224:227], v[36:39], v[16:19]\n"
      "s_cbranch_vccnz LT38\n"
      "LS38:\n"
      "v_mfma_f32_16x16x32_bf16 v[132:135], a[228:231], v[40:43], v[132:135]\n"
      "v_mfma_f32_16x16x32_bf16 v[132:135], a[232:235], v[44:47], v[132:135]\n"
      "v_mfma_f32_16x16x32_bf16 v[132:135], a[236:239], v[60:63], v[132:135]\n"
      "v_cvt_pk_bf16_f32 v214, v136, v137\n"
      "v_mfma_f32_16x16x32_bf16 v[132:135], a[0:3], v[64:67], v[132:135]\n"
      "v_cvt_pk_bf16_f32 v215, v138, v139\n"
      "v_mfma_f32_16x16x32_bf16 v[132:135], a[4:7], v[68:71], v[132:135]\n"
      "v_mfma_f32_16x16x32_bf16 v[132:135], a[8:11], v[84:87], v[132:135]\n"
      "v_mfma_f32_16x16x32_bf16 v[132:135], a[12:15], v[88:91], v[132:135]\n"
      "v_mfma_f32_16x16x32_bf16 v[132:135], a[16:19], v[92:95], v[132:135]\n"
      "v_mfma_f32_16x16x32_bf16 v[132:135], a[20:23], v[108:111], v[132:135]\n"
      "ds_write_b128 v245, v[212:215] offset:24576\n"
      "v_mfma_f32_16x16x32_bf16 v[132:135], v[140:143], v[112:115], v[132:135]\n"
      "v_mfma_f32_16x16x32_bf16 v[132:135], v[156:159], v[116:119], v[132:135]\n"
      "s_waitcnt lgkmcnt(2)\n"
      "s_barrier\n"
      "v_mfma_f32_16x16x32_bf16 v[136:139], v[160:163], v[36:39], v[20:23]\n"
      "ds_read_b128 v[36:39], v232 offset:256\n"
      "v_mfma_f32_16x16x32_bf16 v[136:139], v[164:167], v[40:43], v[136:139]\n"
      "ds_read_b128 v[40:43], v233 offset:256\n"
      "v_mfma_f32_16x16x32_bf16 v[136:139], v[180:183], v[44:47], v[136:139]\n"
      "ds_read_b128 v[44:47], v234 offset:256\n"
      "v_mfma_f32_16x16x32_bf16 v[136:139], v[184:187], v[60:63], v[136:139]\n"
      "ds_read_b128 v[60:63], v235 offset:256\n"
      "v_cvt_pk_bf16_f32 v208, v132, v133\n"
      "v_mfma_f32_16x16x32_bf16 v[136:139], v[188:191], v[64:67], v[136:139]\n"
      "ds_read_b128 v[64:67], v232 offset:8448\n"
      "v_cvt_pk_bf16_f32 v209, v134, v135\n"
      "v_mfma_f32_16x16x32_bf16 v[136:139], v[204:207], v[68:71], v[136:139]\n"
      "ds_read_b128 v[68:71], v233 offset:8448\n"
      "v_mfma_f32_16x16x32_bf16 v[136:139], a[24:27], v[84:87], v[136:139]\n"
      "ds_read_b128 v[84:87], v234 offset:8448\n"
      "v_mfma_f32_16x16x32_bf16 v[136:139], a[28:31], v[88:91], v[136:139]\n"
      "ds_read_b128 v[88:91], v235 offset:8448\n"
      "v_mfma_f32_16x16x32_bf16 v[136:139], a[32:35], v[92:95], v[136:139]\n"
      "ds_read_b128 v[92:95], v232 offset:16640\n"
      "v_mfma_f32_16x16x32_bf16 v[136:139], a[36:39], v[108:111], v[136:139]\n"
      "ds_read_b128 v[108:111], v233 offset:16640\n"
      "v_mfma_f32_16x16x32_bf16 v[136:139], a[40:43], v[112:115], v[136:139]\n"
      "ds_read_b128 v[112:115], v234 offset:16640\n"
      "v_mfma_f32_16x16x32_bf16 v[136:139], a[44:47], v[116:119], v[136:139]\n"
      "ds_read_b128 v[116:119], v235 offset:16640\n"
      "s_waitcnt lgkmcnt(8)\n"
      "v_mfma_f32_16x16x32_bf16 v[132:135], a[240:243], v[36:39], v[0:3]\n"
      "s_cbranch_vccnz LT39\n"
      "LS39:\n"
      "v_mfma_f32_16x16x32_bf16 v[132:135], a[244:247], v[40:43], v[132:135]\n"
      "v_mfma_f32_16x16x32_bf16 v[132:135], a[248:251], v[44:47], v[132:135]\n"
      "v_mfma_f32_16x16x32_bf16 v[132:135], a[252:255], v[60:63], v[132:135]\n"
      "v_cvt_pk_bf16_f32 v210, v136, v137\n"
      "s_waitcnt lgkmcnt(4)\n"
      "v_mfma_f32_16x16x32_bf16 v[132:135], a[48:51], v[64:67], v[132:135]\n"
      "v_cvt_pk_bf16_f32 v211, v138, v139\n"
      "v_mfma_f32_16x16x32_bf16 v[132:135], a[52:55], v[68:71], v[132:135]\n"
      "v_mfma_f32_16x16x32_bf16 v[132:135], a[56:59], v[84:87], v[132:135]\n"
      "ds_read_b128 v[96:99], v246 offset:12288\n"
      "v_mfma_f32_16x16x32_bf16 v[132:135], a[60:63], v[88:91], v[132:135]\n"
      "ds_read_b128 v[100:103], v246 offset:13312\n"
      "s_waitcnt lgkmcnt(2)\n"
      "v_mfma_f32_16x16x32_bf16 v[132:135], a[64:67], v[92:95], v[132:135]\n"
      "ds_read_b128 v[104:107], v246 offset:14336\n"
      "v_mfma_f32_16x16x32_bf16 v[132:135], a[68:71], v[108:111], v[132:135]\n"
      "ds_write_b128 v244, v[208:211] offset:24704\n"
      "v_mfma_f32_16x16x32_bf16 v[132:135], a[72:75], v[112:115], v[132:135]\n"
      "v_mfma_f32_16x16x32_bf16 v[132:135], a[76:79], v[116:119], v[132:135]\n"
      "v_mfma_f32_16x16x32_bf16 v[136:139], a[80:83], v[36:39], v[4:7]\n"
      "v_mfma_f32_16x16x32_bf16 v[136:139], a[84:87], v[40:43], v[136:139]\n"
      "v_mfma_f32_16x16x32_bf16 v[136:139], a[88:91], v[44:47], v[136:139]\n"
      "v_mfma_f32_16x16x32_bf16 v[136:139], a[92:95], v[60:63], v[136:139]\n"
      "v_cvt_pk_bf16_f32 v208, v132, v133\n"
      "v_mfma_f32_16x16x32_bf16 v[136:139], a[96:99], v[64:67], v[136:139]\n"
      "v_cvt_pk_bf16_f32 v209, v134, v135\n"
      "v_mfma_f32_16x16x32_bf16 v[136:139], a[100:103], v[68:71], v[136:139]\n"
      "v_mfma_f32_16x16x32_bf16 v[136:139], a[104:107], v[84:87], v[136:139]\n"
      "v_mfma_f32_16x16x32_bf16 v[136:139], a[108:111], v[88:91], v[136:139]\n"
      "s_add_u32 m0, s17, 0x7000\n"
      "v_mfma_f32_16x16x32_bf16 v[136:139], a[112:115], v[92:95], v[136:139]\n"
      "global_load_lds_dwordx4 v237, s[52:53] offset:0 nt\n"
      "v_mfma_f32_16x16x32_bf16 v[136:139], a[116:119], v[108:111], v[136:139]\n"
      "v_mfma_f32_16x16x32_bf16 v[136:139], a[120:123], v[112:115], v[136:139]\n"
      "v_mfma_f32_16x16x32_bf16 v[136:139], a[124:127], v[116:119], v[136:139]\n"
      "v_mfma_f32_16x16x32_bf16 v[132:135], a[128:131], v[36:39], v[8:11]\n"
      "s_cbranch_vccnz LT40\n"
      "LS40:\n"
      "v_mfma_f32_16x16x32_bf16 v[132:135], a[132:135], v[40:43], v[132:135]\n"
      "v_mfma_f32_16x16x32_bf16 v[132:135], a[136:139], v[44:47], v[132:135]\n"
      "v_mfma_f32_16x16x32_bf16 v[132:135], a[140:143], v[60:63], v[132:135]\n"
      "v_cvt_pk_bf16_f32 v210, v136, v137\n"
      "v_mfma_f32_16x16x32_bf16 v[132:135], a[144:147], v[64:67], v[132:135]\n"
      "v_cvt_pk_bf16_f32 v211, v138, v139\n"
      "v_mfma_f32_16x16x32_bf16 v[132:135], a[148:151], v[68:71], v[132:135]\n"
      "v_mfma_f32_16x16x32_bf16 v[132:135], a[152:155], v[84:87], v[132:135]\n"
      "s_add_u32 m0, s17, 0x8f00\n"
      "v_mfma_f32_16x16x32_bf16 v[132:135], a[156:159], v[88:91], v[132:135]\n"
      "global_load_lds_dwordx4 v237, s[52:53] offset:256 nt\n"
      "v_mfma_f32_16x16x32_bf16 v[132:135], a[160:163], v[92:95], v[132:135]\n"
      "v_mfma_f32_16x16x32_bf16 v[132:135], a[164:167], v[108:111], v[132:135]\n"
      "ds_write_b128 v244, v[208:211] offset:0\n"
      "v_mfma_f32_16x16x32_bf16 v[132:135], a[168:171], v[112:115], v[132:135]\n"
      "v_mfma_f32_16x16x32_bf16 v[132:135], a[172:175], v[116:119], v[132:135]\n"
      "v_mfma_f32_16x16x32_bf16 v[136:139], a[176:179], v[36:39], v[12:15]\n"
      "v_mfma_f32_16x16x32_bf16 v[136:139], a[180:183], v[40:43], v[136:139]\n"
      "v_mfma_f32_16x16x32_bf16 v[136:139], a[184:187], v[44:47], v[136:139]\n"
      "v_mfma_f32_16x16x32_bf16 v[136:139], a[188:191], v[60:63], v[136:139]\n"
      "v_cvt_pk_bf16_f32 v212, v132, v133\n"
      "v_mfma_f32_16x16x32_bf16 v[136:139], a[192:195], v[64:67], v[136:139]\n"
      "v_cvt_pk_bf16_f32 v213, v134, v135\n"
      "v_mfma_f32_16x16x32_bf16 v[136:139], a[196:199], v[68:71], v[136:139]\n"
      "s_add_u32 m0, s17, 0xae00\n"
      "v_mfma_f32_16x16x32_bf16 v[136:139], a[200:203], v[84:87], v[136:139]\n"
      "global_load_lds_dwordx4 v237, s[52:53] offset:512 nt\n"
      "v_mfma_f32_16x16x32_bf16 v[136:139], a[204:207], v[88:91], v[136:139]\n"
      "v_mfma_f32_16x16x32_bf16 v[136:139], a[208:211], v[92:95], v[136:139]\n"
      "v_mfma_f32_16x16x32_bf16 v[136:139], a[212:215], v[108:111], v[136:139]\n"
      "v_mfma_f32_16x16x32_bf16 v[136:139], a[216:219], v[112:115], v[136:139]\n"
      "v_mfma_f32_16x16x32_bf16 v[136:139], a[220:223], v[116:119], v[136:139]\n"
      "v_mfma_f32_16x16x32_bf16 v[132:135], a[224:227], v[36:39], v[16:19]\n"
      "s_cbranch_vccnz LT41\n"
      "LS41:\n"
      "v_mfma_f32_16x16x32_bf16 v[132:135], a[228:231], v[40:43], v[132:135]\n"
      "v_mfma_f32_16x16x32_bf16 v[132:135], a[232:235], v[44:47], v[132:135]\n"
      "v_mfma_f32_16x16x32_bf16 v[132:135], a[236:239], v[60:63], v[132:135]\n"
      "v_cvt_pk_bf16_f32 v214, v136, v137\n"
      "v_mfma_f32_16x16x32_bf16 v[132:135], a[0:3], v[64:67], v[132:135]\n"
      "v_cvt_pk_bf16_f32 v215, v138, v139\n"
      "v_mfma_f32_16x16x32_bf16 v[132:135], a[4:7], v[68:71], v[132:135]\n"
      "v_mfma_f32_16x16x32_bf16 v[132:135], a[8:11], v[84:87], v[132:135]\n"
      "v_mfma_f32_16x16x32_bf16 v[132:135], a[12:15], v[88:91], v[132:135]\n"
      "v_mfma_f32_16x16x32_bf16 v[132:135], a[16:19], v[92:95], v[132:135]\n"
      "v_mfma_f32_16x16x32_bf16 v[132:135], a[20:23], v[108:111], v[132:135]\n"
      "ds_write_b128 v245, v[212:215] offset:0\n"
      "v_mfma_f32_16x16x32_bf16 v[132:135], v[140:143], v[112:115], v[132:135]\n"
      "v_mfma_f32_16x16x32_bf16 v[132:135], v[156:159], v[116:119], v[132:135]\n"
      "s_waitcnt vmcnt(21) lgkmcnt(2)\n"
      "s_barrier\n"
      "v_mfma_f32_16x16x32_bf16 v[136:139], v[160:163], v[36:39], v[20:23]\n"
      "ds_read_b128 v[36:39], v232 offset:24576\n"
      "v_mfma_f32_16x16x32_bf16 v[136:139], v[164:167], v[40:43], v[136:139]\n"
      "ds_read_b128 v[40:43], v233 offset:24576\n"
      "v_mfma_f32_16x16x32_bf16 v[136:139], v[180:183], v[44:47], v[136:139]\n"
      "ds_read_b128 v[44:47], v234 offset:24576\n"
      "v_mfma_f32_16x16x32_bf16 v[136:139], v[184:187], v[60:63], v[136:139]\n"
      "ds_read_b128 v[60:63], v235 offset:24576\n"
      "v_cvt_pk_bf16_f32 v208, v132, v133\n"
      "v_mfma_f32_16x16x32_bf16 v[136:139], v[188:191], v[64:67], v[136:139]\n"
      "ds_read_b128 v[64:67], v232 offset:32768\n"
      "v_cvt_pk_bf16_f32 v209, v134, v135\n"
      "v_mfma_f32_16x16x32_bf16 v[136:139], v[204:207], v[68:71], v[136:139]\n"
      "ds_read_b128 v[68:71], v233 offset:32768\n"
      "v_mfma_f32_16x16x32_bf16 v[136:139], a[24:27], v[84:87], v[136:139]\n"
      "ds_read_b128 v[84:87], v234 offset:32768\n"
      "v_mfma_f32_16x16x32_bf16 v[136:139], a[28:31], v[88:91], v[136:139]\n"
      "ds_read_b128 v[88:91], v235 offset:32768\n"
      "v_mfma_f32_16x16x32_bf16 v[136:139], a[32:35], v[92:95], v[136:139]\n"
      "ds_read_b128 v[92:95], v232 offset:40960\n"
      "v_mfma_f32_16x16x32_bf16 v[136:139], a[36:39], v[108:111], v[136:139]\n"
      "ds_read_b128 v[108:111], v233 offset:40960\n"
      "v_mfma_f32_16x16x32_bf16 v[136:139], a[40:43], v[112:115], v[136:139]\n"
      "ds_read_b128 v[112:115], v234 offset:40960\n"
      "v_mfma_f32_16x16x32_bf16 v[136:139], a[44:47], v[116:119], v[136:139]\n"
      "ds_read_b128 v[116:119], v235 offset:40960\n"
      "s_waitcnt lgkmcnt(8)\n"
      "v_mfma_f32_16x16x32_bf16 v[132:135], a[240:243], v[36:39], v[0:3]\n"
      "v_mfma_f32_16x16x32_bf16 v[132:135], a[244:247], v[40:43], v[132:135]\n"
      "v_mfma_f32_16x16x32_bf16 v[132:135], a[248:251], v[44:47], v[132:135]\n"
      "v_mfma_f32_16x16x32_bf16 v[132:135], a[252:255], v[60:63], v[132:135]\n"
      "v_cvt_pk_bf16_f32 v210, v136, v137\n"
      "s_waitcnt lgkmcnt(4)\n"
      "v_mfma_f32_16x16x32_bf16 v[132:135], a[48:51], v[64:67], v[132:135]\n"
      "v_cvt_pk_bf16_f32 v211, v138, v139\n"
      "v_mfma_f32_16x16x32_bf16 v[132:135], a[52:55], v[68:71], v[132:135]\n"
      "v_mfma_f32_16x16x32_bf16 v[132:135], a[56:59], v[84:87], v[132:135]\n"
      "ds_read_b128 v[120:123], v246 offset:24576\n"
      "v_mfma_f32_16x16x32_bf16 v[132:135], a[60:63], v[88:91], v[132:135]\n"
      "ds_read_b128 v[124:127], v246 offset:25600\n"
      "s_waitcnt lgkmcnt(2)\n"
      "v_mfma_f32_16x16x32_bf16 v[132:135], a[64:67], v[92:95], v[132:135]\n"
      "ds_read_b128 v[128:131], v246 offset:26624\n"
      "v_mfma_f32_16x16x32_bf16 v[132:135], a[68:71], v[108:111], v[132:135]\n"
      "ds_write_b128 v244, v[208:211] offset:128\n"
      "v_mfma_f32_16x16x32_bf16 v[132:135], a[72:75], v[112:115], v[132:135]\n"
      "v_mfma_f32_16x16x32_bf16 v[132:135], a[76:79], v[116:119], v[132:135]\n"
      "v_mfma_f32_16x16x32_bf16 v[136:139], a[80:83], v[36:39], v[4:7]\n"
      "v_mfma_f32_16x16x32_bf16 v[136:139], a[84:87], v[40:43], v[136:139]\n"
      "v_mfma_f32_16x16x32_bf16 v[136:139], a[88:91], v[44:47], v[136:139]\n"
      "v_mfma_f32_16x16x32_bf16 v[136:139], a[92:95], v[60:63], v[136:139]\n"
      "v_cvt_pk_bf16_f32 v208, v132, v133\n"
      "v_mfma_f32_16x16x32_bf16 v[136:139], a[96:99], v[64:67], v[136:139]\n"
      "v_cvt_pk_bf16_f32 v209, v134, v135\n"
      "v_mfma_f32_16x16x32_bf16 v[136:139], a[100:103], v[68:71], v[136:139]\n"
      "v_mfma_f32_16x16x32_bf16 v[136:139], a[104:107], v[84:87], v[136:139]\n"
      "v_mfma_f32_16x16x32_bf16 v[136:139], a[108:111], v[88:91], v[136:139]\n"
      "v_mfma_f32_16x16x32_bf16 v[136:139], a[112:115], v[92:95], v[136:139]\n"
      "v_mfma_f32_16x16x32_bf16 v[136:139], a[116:119], v[108:111], v[136:139]\n"
      "v_mfma_f32_16x16x32_bf16 v[136:139], a[120:123], v[112:115], v[136:139]\n"
      "v_mfma_f32_16x16x32_bf16 v[136:139], a[124:127], v[116:119], v[136:139]\n"
      "v_mfma_f32_16x16x32_bf16 v[132:135], a[128:131], v[36:39], v[8:11]\n"
      "v_mfma_f32_16x16x32_bf16 v[132:135], a[132:135], v[40:43], v[132:135]\n"
      "v_mfma_f32_16x16x32_bf16 v[132:135], a[136:139], v[44:47], v[132:135]\n"
      "v_mfma_f32_16x16x32_bf16 v[132:135], a[140:143], v[60:63], v[132:135]\n"
      "v_cvt_pk_bf16_f32 v210, v136, v137\n"
      "v_mfma_f32_16x16x32_bf16 v[132:135], a[144:147], v[64:67], v[132:135]\n"
      "v_cvt_pk_bf16_f32 v211, v138, v139\n"
      "v_mfma_f32_16x16x32_bf16 v[132:135], a[148:151], v[68:71], v[132:135]\n"
      "v_mfma_f32_16x16x32_bf16 v[132:135], a[152:155], v[84:87], v[132:135]\n"
      "v_mfma_f32_16x16x32_bf16 v[132:135], a[156:159], v[88:91], v[132:135]\n"
      "v_mfma_f32_16x16x32_bf16 v[132:135], a[160:163], v[92:95], v[132:135]\n"
      "v_mfma_f32_16x16x32_bf16 v[132:135], a[164:167], v[108:111], v[132:135]\n"
      "ds_write_b128 v244, v[208:211] offset:12288\n"
      "v_mfma_f32_16x16x32_bf16 v[132:135], a[168:171], v[112:115], v[132:135]\n"
      "v_mfma_f32_16x16x32_bf16 v[132:135], a[172:175], v[116:119], v[132:135]\n"
      "v_mfma_f32_16x16x32_bf16 v[136:139], a[176:179], v[36:39], v[12:15]\n"
      "v_mfma_f32_16x16x32_bf16 v[136:139], a[180:183], v[40:43], v[136:139]\n"
      "v_mfma_f32_16x16x32_bf16 v[136:139], a[184:187], v[44:47], v[136:139]\n"
      "v_mfma_f32_16x16x32_bf16 v[136:139], a[188:191], v[60:63], v[136:139]\n"
      "v_cvt_pk_bf16_f32 v212, v132, v133\n"
      "v_mfma_f32_16x16x32_bf16 v[136:139], a[192:195], v[64:67], v[136:139]\n"
      "v_cvt_pk_bf16_f32 v213, v134, v135\n"
      "v_mfma_f32_16x16x32_bf16 v[136:139], a[196:199], v[68:71], v[136:139]\n"
      "v_mfma_f32_16x16x32_bf16 v[136:139], a[200:203], v[84:87], v[136:139]\n"
      "v_mfma_f32_16x16x32_bf16 v[136:139], a[204:207], v[88:91], v[136:139]\n"
      "v_mfma_f32_16x16x32_bf16 v[136:139], a[208:211], v[92:95], v[136:139]\n"
      "v_mfma_f32_16x16x32_bf16 v[136:139], a[212:215], v[108:111], v[136:139]\n"
      "v_mfma_f32_16x16x32_bf16 v[136:139], a[216:219], v[112:115], v[136:139]\n"
      "v_mfma_f32_16x16x32_bf16 v[136:139], a[220:223], v[116:119], v[136:139]\n"
      "v_mfma_f32_16x16x32_bf16 v[132:135], a[224:227], v[36:39], v[16:19]\n"
      "v_mfma_f32_16x16x32_bf16 v[132:135], a[228:231], v[40:43], v[132:135]\n"
      "v_mfma_f32_16x16x32_bf16 v[132:135], a[232:235], v[44:47], v[132:135]\n"
      "v_mfma_f32_16x16x32_bf16 v[132:135], a[236:239], v[60:63], v[132:135]\n"
      "v_cvt_pk_bf16_f32 v214, v136, v137\n"
      "v_mfma_f32_16x16x32_bf16 v[132:135], a[0:3], v[64:67], v[132:135]\n"
      "v_cvt_pk_bf16_f32 v215, v138, v139\n"
      "v_mfma_f32_16x16x32_bf16 v[132:135], a[4:7], v[68:71], v[132:135]\n"
      "v_mfma_f32_16x16x32_bf16 v[132:135], a[8:11], v[84:87], v[132:135]\n"
      "v_mfma_f32_16x16x32_bf16 v[132:135], a[12:15], v[88:91], v[132:135]\n"
      "v_mfma_f32_16x16x32_bf16 v[132:135], a[16:19], v[92:95], v[132:135]\n"
      "v_mfma_f32_16x16x32_bf16 v[132:135], a[20:23], v[108:111], v[132:135]\n"
      "ds_write_b128 v245, v[212:215] offset:12288\n"
      "v_mfma_f32_16x16x32_bf16 v[132:135], v[140:143], v[112:115], v[132:135]\n"
      "v_mfma_f32_16x16x32_bf16 v[132:135], v[156:159], v[116:119], v[132:135]\n"
      "s_waitcnt lgkmcnt(2)\n"
      "s_barrier\n"
      "v_mfma_f32_16x16x32_bf16 v[136:139], v[160:163], v[36:39], v[20:23]\n"
      "ds_read_b128 v[36:39], v232 offset:24832\n"
      "v_mfma_f32_16x16x32_bf16 v[136:139], v[164:167], v[40:43], v[136:139]\n"
      "ds_read_b128 v[40:43], v233 offset:24832\n"
      "v_mfma_f32_16x16x32_bf16 v[136:139], v[180:183], v[44:47], v[136:139]\n"
      "ds_read_b128 v[44:47], v234 offset:24832\n"
      "v_mfma_f32_16x16x32_bf16 v[136:139], v[184:187], v[60:63], v[136:139]\n"
      "ds_read_b128 v[60:63], v235 offset:24832\n"
      "v_cvt_pk_bf16_f32 v208, v132, v133\n"
      "v_mfma_f32_16x16x32_bf16 v[136:139], v[188:191], v[64:67], v[136:139]\n"
      "ds_read_b128 v[64:67], v232 offset:33024\n"
      "v_cvt_pk_bf16_f32 v209, v134, v135\n"
      "v_mfma_f32_16x16x32_bf16 v[136:139], v[204:207], v[68:71], v[136:139]\n"
      "ds_read_b128 v[68:71], v233 offset:33024\n"
      "v_mfma_f32_16x16x32_bf16 v[136:139], a[24:27], v[84:87], v[136:139]\n"
      "ds_read_b128 v[84:87], v234 offset:33024\n"
      "v_mfma_f32_16x16x32_bf16 v[136:139], a[28:31], v[88:91], v[136:139]\n"
      "ds_read_b128 v[88:91], v235 offset:33024\n"
      "v_mfma_f32_16x16x32_bf16 v[136:139], a[32:35], v[92:95], v[136:139]\n"
      "ds_read_b128 v[92:95], v232 offset:41216\n"
      "v_mfma_f32_16x16x32_bf16 v[136:139], a[36:39], v[108:111], v[136:139]\n"
      "ds_read_b128 v[108:111], v233 offset:41216\n"
      "v_mfma_f32_16x16x32_bf16 v[136:139], a[40:43], v[112:115], v[136:139]\n"
      "ds_read_b128 v[112:115], v234 offset:41216\n"
      "v_mfma_f32_16x16x32_bf16 v[136:139], a[44:47], v[116:119], v[136:139]\n"
      "ds_read_b128 v[116:119], v235 offset:41216\n"
      "s_waitcnt lgkmcnt(8)\n"
      "v_mfma_f32_16x16x32_bf16 v[132:135], a[240:243], v[36:39], v[0:3]\n"
      "v_add_u32 v232, 0xffff4000, v232\n"
      "v_mfma_f32_16x16x32_bf16 v[132:135], a[244:247], v[40:43], v[132:135]\n"
      "v_add_u32 v233, 0xffff4000, v233\n"
      "v_mfma_f32_16x16x32_bf16 v[132:135], a[248:251], v[44:47], v[132:135]\n"
      "v_add_u32 v234, 0xffff4000, v234\n"
      "v_mfma_f32_16x16x32_bf16 v[132:135], a[252:255], v[60:63], v[132:135]\n"
      "v_add_u32 v235, 0xffff4000, v235\n"
      "s_waitcnt lgkmcnt(4)\n"
      "v_mfma_f32_16x16x32_bf16 v[132:135], a[48:51], v[64:67], v[132:135]\n"
      "v_cvt_pk_bf16_f32 v210, v136, v137\n"
      "v_mfma_f32_16x16x32_bf16 v[132:135], a[52:55], v[68:71], v[132:135]\n"
      "v_cvt_pk_bf16_f32 v211, v138, v139\n"
      "v_mfma_f32_16x16x32_bf16 v[132:135], a[56:59], v[84:87], v[132:135]\n"
      "ds_read_b128 v[144:147], v246 offset:0\n"
      "v_mfma_f32_16x16x32_bf16 v[132:135], a[60:63], v[88:91], v[132:135]\n"
      "ds_read_b128 v[148:151], v246 offset:1024\n"
      "s_waitcnt lgkmcnt(2)\n"
      "v_mfma_f32_16x16x32_bf16 v[132:135], a[64:67], v[92:95], v[132:135]\n"
      "ds_read_b128 v[152:155], v246 offset:2048\n"
      "v_mfma_f32_16x16x32_bf16 v[132:135], a[68:71], v[108:111], v[132:135]\n"
      "v_lshlrev_b32 v0, 16, v192\n"
      "v_mfma_f32_16x16x32_bf16 v[132:135], a[72:75], v[112:115], v[132:135]\n"
      "ds_write_b128 v244, v[208:211] offset:12416\n"
      "v_mfma_f32_16x16x32_bf16 v[132:135], a[76:79], v[116:119], v[132:135]\n"
      "v_and_b32 v1, 0xffff0000, v192\n"
      "v_mfma_f32_16x16x32_bf16 v[136:139], a[80:83], v[36:39], v[4:7]\n"
      "v_lshlrev_b32 v2, 16, v193\n"
      "v_mfma_f32_16x16x32_bf16 v[136:139], a[84:87], v[40:43], v[136:139]\n"
      "v_and_b32 v3, 0xffff0000, v193\n"
      "v_mfma_f32_16x16x32_bf16 v[136:139], a[88:91], v[44:47], v[136:139]\n"
      "v_mfma_f32_16x16x32_bf16 v[136:139], a[92:95], v[60:63], v[136:139]\n"
      "v_cvt_pk_bf16_f32 v208, v132, v133\n"
      "v_mfma_f32_16x16x32_bf16 v[136:139], a[96:99], v[64:67], v[136:139]\n"
      "v_cvt_pk_bf16_f32 v209, v134, v135\n"
      "v_mfma_f32_16x16x32_bf16 v[136:139], a[100:103], v[68:71], v[136:139]\n"
      "v_lshlrev_b32 v4, 16, v194\n"
      "v_mfma_f32_16x16x32_bf16 v[136:139], a[104:107], v[84:87], v[136:139]\n"
      "v_and_b32 v5, 0xffff0000, v194\n"
      "v_mfma_f32_16x16x32_bf16 v[136:139], a[108:111], v[88:91], v[136:139]\n"
      "v_lshlrev_b32 v6, 16, v195\n"
      "v_mfma_f32_16x16x32_bf16 v[136:139], a[112:115], v[92:95], v[136:139]\n"
      "v_and_b32 v7, 0xffff0000, v195\n"
      "v_mfma_f32_16x16x32_bf16 v[136:139], a[116:119], v[108:111], v[136:139]\n"
      "v_mfma_f32_16x16x32_bf16 v[136:139], a[120:123], v[112:115], v[136:139]\n"
      "v_mfma_f32_16x16x32_bf16 v[136:139], a[124:127], v[116:119], v[136:139]\n"
      "v_mfma_f32_16x16x32_bf16 v[132:135], a[128:131], v[36:39], v[8:11]\n"
      "v_mfma_f32_16x16x32_bf16 v[132:135], a[132:135], v[40:43], v[132:135]\n"
      "v_mfma_f32_16x16x32_bf16 v[132:135], a[136:139], v[44:47], v[132:135]\n"
      "v_mfma_f32_16x16x32_bf16 v[132:135], a[140:143], v[60:63], v[132:135]\n"
      "v_cvt_pk_bf16_f32 v210, v136, v137\n"
      "v_mfma_f32_16x16x32_bf16 v[132:135], a[144:147], v[64:67], v[132:135]\n"
      "v_cvt_pk_bf16_f32 v211, v138, v139\n"
      "v_mfma_f32_16x16x32_bf16 v[132:135], a[148:151], v[68:71], v[132:135]\n"
      "v_lshlrev_b32 v8, 16, v196\n"
      "v_mfma_f32_16x16x32_bf16 v[132:135], a[152:155], v[84:87], v[132:135]\n"
      "v_and_b32 v9, 0xffff0000, v196\n"
      "v_mfma_f32_16x16x32_bf16 v[132:135], a[156:159], v[88:91], v[132:135]\n"
      "v_lshlrev_b32 v10, 16, v197\n"
      "v_mfma_f32_16x16x32_bf16 v[132:135], a[160:163], v[92:95], v[132:135]\n"
      "v_and_b32 v11, 0xffff0000, v197\n"
      "v_mfma_f32_16x16x32_bf16 v[132:135], a[164:167], v[108:111], v[132:135]\n"
      "ds_write_b128 v244, v[208:211] offset:24576\n"
      "v_mfma_f32_16x16x32_bf16 v[132:135], a[168:171], v[112:115], v[132:135]\n"
      "v_mfma_f32_16x16x32_bf16 v[132:135], a[172:175], v[116:119], v[132:135]\n"
      "v_mfma_f32_16x16x32_bf16 v[136:139], a[176:179], v[36:39], v[12:15]\n"
      "v_mfma_f32_16x16x32_bf16 v[136:139], a[180:183], v[40:43], v[136:139]\n"
      "v_mfma_f32_16x16x32_bf16 v[136:139], a[184:187], v[44:47], v[136:139]\n"
      "v_mfma_f32_16x16x32_bf16 v[136:139], a[188:191], v[60:63], v[136:139]\n"
      "v_cvt_pk_bf16_f32 v212, v132, v133\n"
      "v_mfma_f32_16x16x32_bf16 v[136:139], a[192:195], v[64:67], v[136:139]\n"
      "v_cvt_pk_bf16_f32 v213, v134, v135\n"
      "v_mfma_f32_16x16x32_bf16 v[136:139], a[196:199], v[68:71], v[136:139]\n"
      "v_lshlrev_b32 v12, 16, v198\n"
      "v_mfma_f32_16x16x32_bf16 v[136:139], a[200:203], v[84:87], v[136:139]\n"
      "v_and_b32 v13, 0xffff0000, v198\n"
      "v_mfma_f32_16x16x32_bf16 v[136:139], a[204:207], v[88:91], v[136:139]\n"
      "v_lshlrev_b32 v14, 16, v199\n"
      "v_mfma_f32_16x16x32_bf16 v[136:139], a[208:211], v[92:95], v[136:139]\n"
      "v_and_b32 v15, 0xffff0000, v199\n"
      "v_mfma_f32_16x16x32_bf16 v[136:139], a[212:215], v[108:111], v[136:139]\n"
      "v_mfma_f32_16x16x32_bf16 v[136:139], a[216:219], v[112:115], v[136:139]\n"
      "v_mfma_f32_16x16x32_bf16 v[136:139], a[220:223], v[116:119], v[136:139]\n"
      "v_mfma_f32_16x16x32_bf16 v[132:135], a[224:227], v[36:39], v[16:19]\n"
      "v_mfma_f32_16x16x32_bf16 v[132:135], a[228:231], v[40:43], v[132:135]\n"
      "v_mfma_f32_16x16x32_bf16 v[132:135], a[232:235], v[44:47], v[132:135]\n"
      "v_mfma_f32_16x16x32_bf16 v[132:135], a[236:239], v[60:63], v[132:135]\n"
      "v_cvt_pk_bf16_f32 v214, v136, v137\n"
      "v_mfma_f32_16x16x32_bf16 v[132:135], a[0:3], v[64:67], v[132:135]\n"
      "v_cvt_pk_bf16_f32 v215, v138, v139\n"
      "v_mfma_f32_16x16x32_bf16 v[132:135], a[4:7], v[68:71], v[132:135]\n"
      "LPD15:\n"
      "s_cbranch_execz LRET\n"
      "v_lshlrev_b32 v16, 16, v200\n"
      "v_mfma_f32_16x16x32_bf16 v[132:135], a[8:11], v[84:87], v[132:135]\n"
      "v_and_b32 v17, 0xffff0000, v200\n"
      "v_mfma_f32_16x16x32_bf16 v[132:135], a[12:15], v[88:91], v[132:135]\n"
      "v_lshlrev_b32 v18, 16, v201\n"
      "v_mfma_f32_16x16x32_bf16 v[132:135], a[16:19], v[92:95], v[132:135]\n"
      "v_and_b32 v19, 0xffff0000, v201\n"
      "v_mfma_f32_16x16x32_bf16 v[132:135], a[20:23], v[108:111], v[132:135]\n"
      "ds_write_b128 v245, v[212:215] offset:24576\n"
      "v_mfma_f32_16x16x32_bf16 v[132:135], v[140:143], v[112:115], v[132:135]\n"
      "v_mfma_f32_16x16x32_bf16 v[132:135], v[156:159], v[116:119], v[132:135]\n"
      "s_waitcnt vmcnt(9) lgkmcnt(2)\n"
      "s_barrier\n"
      "v_mfma_f32_16x16x32_bf16 v[136:139], v[160:163], v[36:39], v[20:23]\n"
      "ds_read_b128 v[36:39], v232 offset:0\n"
      "v_mfma_f32_16x16x32_bf16 v[136:139], v[164:167], v[40:43], v[136:139]\n"
      "ds_read_b128 v[40:43], v233 offset:0\n"
      "v_mfma_f32_16x16x32_bf16 v[136:139], v[180:183], v[44:47], v[136:139]\n"
      "ds_read_b128 v[44:47], v234 offset:0\n"
      "v_mfma_f32_16x16x32_bf16 v[136:139], v[184:187], v[60:63], v[136:139]\n"
      "ds_read_b128 v[60:63], v235 offset:0\n"
      "v_cvt_pk_bf16_f32 v208, v132, v133\n"
      "v_mfma_f32_16x16x32_bf16 v[136:139], v[188:191], v[64:67], v[136:139]\n"
      "ds_read_b128 v[64:67], v232 offset:8192\n"
      "v_cvt_pk_bf16_f32 v209, v134, v135\n"
      "v_mfma_f32_16x16x32_bf16 v[136:139], v[204:207], v[68:71], v[136:139]\n"
      "ds_read_b128 v[68:71], v233 offset:8192\n"
      "v_mfma_f32_16x16x32_bf16 v[136:139], a[24:27], v[84:87], v[136:139]\n"
      "ds_read_b128 v[84:87], v234 offset:8192\n"
      "v_mfma_f32_16x16x32_bf16 v[136:139], a[28:31], v[88:91], v[136:139]\n"
      "ds_read_b128 v[88:91], v235 offset:8192\n"
      "v_mfma_f32_16x16x32_bf16 v[136:139], a[32:35], v[92:95], v[136:139]\n"
      "ds_read_b128 v[92:95], v232 offset:16384\n"
      "v_mfma_f32_16x16x32_bf16 v[136:139], a[36:39], v[108:111], v[136:139]\n"
      "ds_read_b128 v[108:111], v233 offset:16384\n"
      "v_mfma_f32_16x16x32_bf16 v[136:139], a[40:43], v[112:115], v[136:139]\n"
      "ds_read_b128 v[112:115], v234 offset:16384\n"
      "v_mfma_f32_16x16x32_bf16 v[136:139], a[44:47], v[116:119], v[136:139]\n"
      "ds_read_b128 v[116:119], v235 offset:16384\n"
      "s_waitcnt lgkmcnt(8)\n"
      "v_mfma_f32_16x16x32_bf16 v[132:135], a[240:243], v[36:39], v[0:3]\n"
      "v_lshlrev_b32 v20, 16, v202\n"
      "v_mfma_f32_16x16x32_bf16 v[132:135], a[244:247], v[40:43], v[132:135]\n"
      "v_and_b32 v21, 0xffff0000, v202\n"
      "v_mfma_f32_16x16x32_bf16 v[132:135], a[248:251], v[44:47], v[132:135]\n"
      "v_lshlrev_b32 v22, 16, v203\n"
      "v_mfma_f32_16x16x32_bf16 v[132:135], a[252:255], v[60:63], v[132:135]\n"
      "v_and_b32 v23, 0xffff0000, v203\n"
      "global_store_dwordx4 v247, v[96:99], s[54:55] offset:768 sc1\n"
      "s_waitcnt lgkmcnt(4)\n"
      "v_mfma_f32_16x16x32_bf16 v[132:135], a[48:51], v[64:67], v[132:135]\n"
      "v_cvt_pk_bf16_f32 v210, v136, v137\n"
      "v_mfma_f32_16x16x32_bf16 v[132:135], a[52:55], v[68:71], v[132:135]\n"
      "v_cvt_pk_bf16_f32 v211, v138, v139\n"
      "v_mfma_f32_16x16x32_bf16 v[132:135], a[56:59], v[84:87], v[132:135]\n"
      "ds_read_b128 v[24:27], v246 offset:12288\n"
      "v_mfma_f32_16x16x32_bf16 v[132:135], a[60:63], v[88:91], v[132:135]\n"
      "ds_read_b128 v[28:31], v246 offset:13312\n"
      "s_waitcnt lgkmcnt(2)\n"
      "v_mfma_f32_16x16x32_bf16 v[132:135], a[64:67], v[92:95], v[132:135]\n"
      "ds_read_b128 v[32:35], v246 offset:14336\n"
      "v_mfma_f32_16x16x32_bf16 v[132:135], a[68:71], v[108:111], v[132:135]\n"
      "v_mfma_f32_16x16x32_bf16 v[132:135], a[72:75], v[112:115], v[132:135]\n"
      "ds_write_b128 v244, v[208:211] offset:24704\n"
      "v_mfma_f32_16x16x32_bf16 v[132:135], a[76:79], v[116:119], v[132:135]\n"
      "v_mfma_f32_16x16x32_bf16 v[136:139], a[80:83], v[36:39], v[4:7]\n"
      "v_mfma_f32_16x16x32_bf16 v[136:139], a[84:87], v[40:43], v[136:139]\n"
      "v_mfma_f32_16x16x32_bf16 v[136:139], a[88:91], v[44:47], v[136:139]\n"
      "v_mfma_f32_16x16x32_bf16 v[136:139], a[92:95], v[60:63], v[136:139]\n"
      "v_cvt_pk_bf16_f32 v208, v132, v133\n"
      "global_store_dwordx4 v252, v[100:103], s[54:55] offset:768 sc1\n"
      "v_mfma_f32_16x16x32_bf16 v[136:139], a[96:99], v[64:67], v[136:139]\n"
      "v_cvt_pk_bf16_f32 v209, v134, v135\n"
      "v_mfma_f32_16x16x32_bf16 v[136:139], a[100:103], v[68:71], v[136:139]\n"
      "v_mfma_f32_16x16x32_bf16 v[136:139], a[104:107], v[84:87], v[136:139]\n"
      "v_mfma_f32_16x16x32_bf16 v[136:139], a[108:111], v[88:91], v[136:139]\n"
      "v_mfma_f32_16x16x32_bf16 v[136:139], a[112:115], v[92:95], v[136:139]\n"
      "v_mfma_f32_16x16x32_bf16 v[136:139], a[116:119], v[108:111], v[136:139]\n"
      "v_mfma_f32_16x16x32_bf16 v[136:139], a[120:123], v[112:115], v[136:139]\n"
      "v_mfma_f32_16x16x32_bf16 v[136:139], a[124:127], v[116:119], v[136:139]\n"
      "v_mfma_f32_16x16x32_bf16 v[132:135], a[128:131], v[36:39], v[8:11]\n"
      "v_mfma_f32_16x16x32_bf16 v[132:135], a[132:135], v[40:43], v[132:135]\n"
      "v_mfma_f32_16x16x32_bf16 v[132:135], a[136:139], v[44:47], v[132:135]\n"
      "v_mfma_f32_16x16x32_bf16 v[132:135], a[140:143], v[60:63], v[132:135]\n"
      "v_cvt_pk_bf16_f32 v210, v136, v137\n"
      "v_mfma_f32_16x16x32_bf16 v[132:135], a[144:147], v[64:67], v[132:135]\n"
      "v_cvt_pk_bf16_f32 v211, v138, v139\n"
      "v_mfma_f32_16x16x32_bf16 v[132:135], a[148:151], v[68:71], v[132:135]\n"
      "global_store_dwordx4 v253, v[104:107], s[54:55] offset:768 sc1\n"
      "v_mfma_f32_16x16x32_bf16 v[132:135], a[152:155], v[84:87], v[132:135]\n"
      "v_mfma_f32_16x16x32_bf16 v[132:135], a[156:159], v[88:91], v[132:135]\n"
      "s_lshl_b32 s79, s96, 1\n"
      "v_mfma_f32_16x16x32_bf16 v[132:135], a[160:163], v[92:95], v[132:135]\n"
      "v_mad_i32_i24 v252, v251, s79, v252\n"
      "v_mfma_f32_16x16x32_bf16 v[132:135], a[164:167], v[108:111], v[132:135]\n"
      "ds_write_b128 v244, v[208:211] offset:0\n"
      "v_mfma_f32_16x16x32_bf16 v[132:135], a[168:171], v[112:115], v[132:135]\n"
      "s_mul_i32 s79, s79, s100\n"
      "v_mfma_f32_16x16x32_bf16 v[132:135], a[172:175], v[116:119], v[132:135]\n"
      "v_add_u32 v247, s79, v247\n"
      "v_mfma_f32_16x16x32_bf16 v[136:139], a[176:179], v[36:39], v[12:15]\n"
      "s_mul_i32 s79, s79, s101\n"
      "v_mfma_f32_16x16x32_bf16 v[136:139], a[180:183], v[40:43], v[136:139]\n"
      "v_add_u32 v253, s79, v253\n"
      "v_mfma_f32_16x16x32_bf16 v[136:139], a[184:187], v[44:47], v[136:139]\n"
      "s_add_u32 s54, s30, s39\n"
      "s_addc_u32 s55, s31, 0\n"
      "v_mfma_f32_16x16x32_bf16 v[136:139], a[188:191], v[60:63], v[136:139]\n"
      "v_cvt_pk_bf16_f32 v212, v132, v133\n"
      "v_mfma_f32_16x16x32_bf16 v[136:139], a[192:195], v[64:67], v[136:139]\n"
      "v_cvt_pk_bf16_f32 v213, v134, v135\n"
      "v_mfma_f32_16x16x32_bf16 v[136:139], a[196:199], v[68:71], v[136:139]\n"
      "global_store_dwordx4 v247, v[120:123], s[54:55] offset:0 sc1\n"
      "v_mfma_f32_16x16x32_bf16 v[136:139], a[200:203], v[84:87], v[136:139]\n"
      "v_mfma_f32_16x16x32_bf16 v[136:139], a[204:207], v[88:91], v[136:139]\n"
      "v_mfma_f32_16x16x32_bf16 v[136:139], a[208:211], v[92:95], v[136:139]\n"
      "v_mfma_f32_16x16x32_bf16 v[136:139], a[212:215], v[108:111], v[136:139]\n"
      "v_mfma_f32_16x16x32_bf16 v[136:139], a[216:219], v[112:115], v[136:139]\n"
      "v_mfma_f32_16x16x32_bf16 v[136:139], a[220:223], v[116:119], v[136:139]\n"
      "v_mfma_f32_16x16x32_bf16 v[132:135], a[224:227], v[36:39], v[16:19]\n"
      "v_mfma_f32_16x16x32_bf16 v[132:135], a[228:231], v[40:43], v[132:135]\n"
      "v_mfma_f32_16x16x32_bf16 v[132:135], a[232:235], v[44:47], v[132:135]\n"
      "v_mfma_f32_16x16x32_bf16 v[132:135], a[236:239], v[60:63], v[132:135]\n"
      "v_cvt_pk_bf16_f32 v214, v136, v137\n"
      "v_mfma_f32_16x16x32_bf16 v[132:135], a[0:3], v[64:67], v[132:135]\n"
      "v_cvt_pk_bf16_f32 v215, v138, v139\n"
      "v_mfma_f32_16x16x32_bf16 v[132:135], a[4:7], v[68:71], v[132:135]\n"
      "v_mfma_f32_16x16x32_bf16 v[132:135], a[8:11], v[84:87], v[132:135]\n"
      "global_store_dwordx4 v252, v[124:127], s[54:55] offset:0 sc1\n"
      "v_mfma_f32_16x16x32_bf16 v[132:135], a[12:15], v[88:91], v[132:135]\n"
      "v_mfma_f32_16x16x32_bf16 v[132:135], a[16:19], v[92:95], v[132:135]\n"
      "v_mfma_f32_16x16x32_bf16 v[132:135], a[20:23], v[108:111], v[132:135]\n"
      "ds_write_b128 v245, v[212:215] offset:0\n"
      "v_mfma_f32_16x16x32_bf16 v[132:135], v[140:143], v[112:115], v[132:135]\n"
      "v_mfma_f32_16x16x32_bf16 v[132:135], v[156:159], v[116:119], v[132:135]\n"
      "s_waitcnt lgkmcnt(2)\n"
      "s_barrier\n"
      "v_mfma_f32_16x16x32_bf16 v[136:139], v[160:163], v[36:39], v[20:23]\n"
      "ds_read_b128 v[36:39], v232 offset:256\n"
      "v_mfma_f32_16x16x32_bf16 v[136:139], v[164:167], v[40:43], v[136:139]\n"
      "ds_read_b128 v[40:43], v233 offset:256\n"
      "v_mfma_f32_16x16x32_bf16 v[136:139], v[180:183], v[44:47], v[136:139]\n"
      "ds_read_b128 v[44:47], v234 offset:256\n"
      "v_mfma_f32_16x16x32_bf16 v[136:139], v[184:187], v[60:63], v[136:139]\n"
      "ds_read_b128 v[60:63], v235 offset:256\n"
      "v_cvt_pk_bf16_f32 v208, v132, v133\n"
      "v_mfma_f32_16x16x32_bf16 v[136:139], v[188:191], v[64:67], v[136:139]\n"
      "ds_read_b128 v[64:67], v232 offset:8448\n"
      "v_cvt_pk_bf16_f32 v209, v134, v135\n"
      "v_mfma_f32_16x16x32_bf16 v[136:139], v[204:207], v[68:71], v[136:139]\n"
      "ds_read_b128 v[68:71], v233 offset:8448\n"
      "v_mfma_f32_16x16x32_bf16 v[136:139], a[24:27], v[84:87], v[136:139]\n"
      "ds_read_b128 v[84:87], v234 offset:8448\n"
      "v_mfma_f32_16x16x32_bf16 v[136:139], a[28:31], v[88:91], v[136:139]\n"
      "ds_read_b128 v[88:91], v235 offset:8448\n"
      "v_mfma_f32_16x16x32_bf16 v[136:139], a[32:35], v[92:95], v[136:139]\n"
      "ds_read_b128 v[92:95], v232 offset:16640\n"
      "v_mfma_f32_16x16x32_bf16 v[136:139], a[36:39], v[108:111], v[136:139]\n"
      "ds_read_b128 v[108:111], v233 offset:16640\n"
      "v_mfma_f32_16x16x32_bf16 v[136:139], a[40:43], v[112:115], v[136:139]\n"
      "ds_read_b128 v[112:115], v234 offset:16640\n"
      "v_mfma_f32_16x16x32_bf16 v[136:139], a[44:47], v[116:119], v[136:139]\n"
      "ds_read_b128 v[116:119], v235 offset:16640\n"
      "s_waitcnt lgkmcnt(8)\n"
      "v_mfma_f32_16x16x32_bf16 v[132:135], a[240:243], v[36:39], v[0:3]\n"
      "v_mfma_f32_16x16x32_bf16 v[132:135], a[244:247], v[40:43], v[132:135]\n"
      "v_mfma_f32_16x16x32_bf16 v[132:135], a[248:251], v[44:47], v[132:135]\n"
      "v_mfma_f32_16x16x32_bf16 v[132:135], a[252:255], v[60:63], v[132:135]\n"
      "v_cvt_pk_bf16_f32 v210, v136, v137\n"
      "global_store_dwordx4 v253, v[128:131], s[54:55] offset:0 sc1\n"
      "s_waitcnt lgkmcnt(4)\n"
      "v_mfma_f32_16x16x32_bf16 v[132:135], a[48:51], v[64:67], v[132:135]\n"
      "v_cvt_pk_bf16_f32 v211, v138, v139\n"
      "v_mfma_f32_16x16x32_bf16 v[132:135], a[52:55], v[68:71], v[132:135]\n"
      "v_mfma_f32_16x16x32_bf16 v[132:135], a[56:59], v[84:87], v[132:135]\n"
      "ds_read_b128 v[48:51], v246 offset:24576\n"
      "v_mfma_f32_16x16x32_bf16 v[132:135], a[60:63], v[88:91], v[132:135]\n"
      "ds_read_b128 v[52:55], v246 offset:25600\n"
      "s_waitcnt lgkmcnt(2)\n"
      "v_mfma_f32_16x16x32_bf16 v[132:135], a[64:67], v[92:95], v[132:135]\n"
      "ds_read_b128 v[56:59], v246 offset:26624\n"
      "v_mfma_f32_16x16x32_bf16 v[132:135], a[68:71], v[108:111], v[132:135]\n"
      "ds_write_b128 v244, v[208:211] offset:128\n"
      "v_mfma_f32_16x16x32_bf16 v[132:135], a[72:75], v[112:115], v[132:135]\n"
      "v_mfma_f32_16x16x32_bf16 v[132:135], a[76:79], v[116:119], v[132:135]\n"
      "v_mfma_f32_16x16x32_bf16 v[136:139], a[80:83], v[36:39], v[4:7]\n"
      "v_mfma_f32_16x16x32_bf16 v[136:139], a[84:87], v[40:43], v[136:139]\n"
      "v_mfma_f32_16x16x32_bf16 v[136:139], a[88:91], v[44:47], v[136:139]\n"
      "v_mfma_f32_16x16x32_bf16 v[136:139], a[92:95], v[60:63], v[136:139]\n"
      "v_cvt_pk_bf16_f32 v208, v132, v133\n"
      "global_store_dwordx4 v247, v[144:147], s[54:55] offset:768 sc1\n"
      "v_mfma_f32_16x16x32_bf16 v[136:139], a[96:99], v[64:67], v[136:139]\n"
      "v_cvt_pk_bf16_f32 v209, v134, v135\n"
      "v_mfma_f32_16x16x32_bf16 v[136:139], a[100:103], v[68:71], v[136:139]\n"
      "v_mfma_f32_16x16x32_bf16 v[136:139], a[104:107], v[84:87], v[136:139]\n"
      "v_mfma_f32_16x16x32_bf16 v[136:139], a[108:111], v[88:91], v[136:139]\n"
      "v_mfma_f32_16x16x32_bf16 v[136:139], a[112:115], v[92:95], v[136:139]\n"
      "v_mfma_f32_16x16x32_bf16 v[136:139], a[116:119], v[108:111], v[136:139]\n"
      "v_mfma_f32_16x16x32_bf16 v[136:139], a[120:123], v[112:115], v[136:139]\n"
      "v_mfma_f32_16x16x32_bf16 v[136:139], a[124:127], v[116:119], v[136:139]\n"
      "v_mfma_f32_16x16x32_bf16 v[132:135], a[128:131], v[36:39], v[8:11]\n"
      "v_mfma_f32_16x16x32_bf16 v[132:135], a[132:135], v[40:43], v[132:135]\n"
      "v_mfma_f32_16x16x32_bf16 v[132:135], a[136:139], v[44:47], v[132:135]\n"
      "v_mfma_f32_16x16x32_bf16 v[132:135], a[140:143], v[60:63], v[132:135]\n"
      "v_cvt_pk_bf16_f32 v210, v136, v137\n"
      "v_mfma_f32_16x16x32_bf16 v[132:135], a[144:147], v[64:67], v[132:135]\n"
      "v_cvt_pk_bf16_f32 v211, v138, v139\n"
      "v_mfma_f32_16x16x32_bf16 v[132:135], a[148:151], v[68:71], v[132:135]\n"
      "global_store_dwordx4 v252, v[148:151], s[54:55] offset:768 sc1\n"
      "v_mfma_f32_16x16x32_bf16 v[132:135], a[152:155], v[84:87], v[132:135]\n"
      "v_mfma_f32_16x16x32_bf16 v[132:135], a[156:159], v[88:91], v[132:135]\n"
      "v_mfma_f32_16x16x32_bf16 v[132:135], a[160:163], v[92:95], v[132:135]\n"
      "v_mfma_f32_16x16x32_bf16 v[132:135], a[164:167], v[108:111], v[132:135]\n"
      "ds_write_b128 v244, v[208:211] offset:12288\n"
      "v_mfma_f32_16x16x32_bf16 v[132:135], a[168:171], v[112:115], v[132:135]\n"
      "v_mfma_f32_16x16x32_bf16 v[132:135], a[172:175], v[116:119], v[132:135]\n"
      "v_mfma_f32_16x16x32_bf16 v[136:139], a[176:179], v[36:39], v[12:15]\n"
      "v_mfma_f32_16x16x32_bf16 v[136:139], a[180:183], v[40:43], v[136:139]\n"
      "v_mfma_f32_16x16x32_bf16 v[136:139], a[184:187], v[44:47], v[136:139]\n"
      "v_mfma_f32_16x16x32_bf16 v[136:139], a[188:191], v[60:63], v[136:139]\n"
      "v_cvt_pk_bf16_f32 v212, v132, v133\n"
      "v_mfma_f32_16x16x32_bf16 v[136:139], a[192:195], v[64:67], v[136:139]\n"
      "v_cvt_pk_bf16_f32 v213, v134, v135\n"
      "v_mfma_f32_16x16x32_bf16 v[136:139], a[196:199], v[68:71], v[136:139]\n"
      "v_mfma_f32_16x16x32_bf16 v[136:139], a[200:203], v[84:87], v[136:139]\n"
      "global_store_dwordx4 v253, v[152:155], s[54:55] offset:768 sc1\n"
      "v_mfma_f32_16x16x32_bf16 v[136:139], a[204:207], v[88:91], v[136:139]\n"
      "v_mfma_f32_16x16x32_bf16 v[136:139], a[208:211], v[92:95], v[136:139]\n"
      "v_mfma_f32_16x16x32_bf16 v[136:139], a[212:215], v[108:111], v[136:139]\n"
      "v_mfma_f32_16x16x32_bf16 v[136:139], a[216:219], v[112:115], v[136:139]\n"
      "v_mfma_f32_16x16x32_bf16 v[136:139], a[220:223], v[116:119], v[136:139]\n"
      "v_mfma_f32_16x16x32_bf16 v[132:135], a[224:227], v[36:39], v[16:19]\n"
      "v_mfma_f32_16x16x32_bf16 v[132:135], a[228:231], v[40:43], v[132:135]\n"
      "v_mfma_f32_16x16x32_bf16 v[132:135], a[232:235], v[44:47], v[132:135]\n"
      "v_mfma_f32_16x16x32_bf16 v[132:135], a[236:239], v[60:63], v[132:135]\n"
      "v_cvt_pk_bf16_f32 v214, v136, v137\n"
      "s_add_u32 s54, s44, s39\n"
      "s_addc_u32 s55, s45, 0\n"
      "v_mfma_f32_16x16x32_bf16 v[132:135], a[0:3], v[64:67], v[132:135]\n"
      "v_cvt_pk_bf16_f32 v215, v138, v139\n"
      "v_mfma_f32_16x16x32_bf16 v[132:135], a[4:7], v[68:71], v[132:135]\n"
      "v_mfma_f32_16x16x32_bf16 v[132:135], a[8:11], v[84:87], v[132:135]\n"
      "global_store_dwordx4 v247, v[24:27], s[54:55] offset:0 sc1\n"
      "v_mfma_f32_16x16x32_bf16 v[132:135], a[12:15], v[88:91], v[132:135]\n"
      "v_mfma_f32_16x16x32_bf16 v[132:135], a[16:19], v[92:95], v[132:135]\n"
      "v_mfma_f32_16x16x32_bf16 v[132:135], a[20:23], v[108:111], v[132:135]\n"
      "ds_write_b128 v245, v[212:215] offset:12288\n"
      "v_mfma_f32_16x16x32_bf16 v[132:135], v[140:143], v[112:115], v[132:135]\n"
      "v_mfma_f32_16x16x32_bf16 v[132:135], v[156:159], v[116:119], v[132:135]\n"
      "s_waitcnt vmcnt(10) lgkmcnt(2)\n"
      "s_barrier\n"
      "v_mfma_f32_16x16x32_bf16 v[136:139], v[160:163], v[36:39], v[20:23]\n"
      "ds_read_b128 v[36:39], v232 offset:24576\n"
      "v_mfma_f32_16x16x32_bf16 v[136:139], v[164:167], v[40:43], v[136:139]\n"
      "ds_read_b128 v[40:43], v233 offset:24576\n"
      "v_mfma_f32_16x16x32_bf16 v[136:139], v[180:183], v[44:47], v[136:139]\n"
      "ds_read_b128 v[44:47], v234 offset:24576\n"
      "v_mfma_f32_16x16x32_bf16 v[136:139], v[184:187], v[60:63], v[136:139]\n"
      "ds_read_b128 v[60:63], v235 offset:24576\n"
      "v_cvt_pk_bf16_f32 v208, v132, v133\n"
      "v_mfma_f32_16x16x32_bf16 v[136:139], v[188:191], v[64:67], v[136:139]\n"
      "ds_read_b128 v[64:67], v232 offset:32768\n"
      "v_cvt_pk_bf16_f32 v209, v134, v135\n"
      "v_mfma_f32_16x16x32_bf16 v[136:139], v[204:207], v[68:71], v[136:139]\n"
      "ds_read_b128 v[68:71], v233 offset:32768\n"
      "v_mfma_f32_16x16x32_bf16 v[136:139], a[24:27], v[84:87], v[136:139]\n"
      "ds_read_b128 v[84:87], v234 offset:32768\n"
      "v_mfma_f32_16x16x32_bf16 v[136:139], a[28:31], v[88:91], v[136:139]\n"
      "ds_read_b128 v[88:91], v235 offset:32768\n"
      "v_mfma_f32_16x16x32_bf16 v[136:139], a[32:35], v[92:95], v[136:139]\n"
      "ds_read_b128 v[92:95], v232 offset:40960\n"
      "v_mfma_f32_16x16x32_bf16 v[136:139], a[36:39], v[108:111], v[136:139]\n"
      "ds_read_b128 v[108:111], v233 offset:40960\n"
      "v_mfma_f32_16x16x32_bf16 v[136:139], a[40:43], v[112:115], v[136:139]\n"
      "ds_read_b128 v[112:115], v234 offset:40960\n"
      "v_mfma_f32_16x16x32_bf16 v[136:139], a[44:47], v[116:119], v[136:139]\n"
      "ds_read_b128 v[116:119], v235 offset:40960\n"
      "s_waitcnt lgkmcnt(8)\n"
      "v_mfma_f32_16x16x32_bf16 v[132:135], a[240:243], v[36:39], v[0:3]\n"
      "v_mfma_f32_16x16x32_bf16 v[132:135], a[244:247], v[40:43], v[132:135]\n"
      "v_mfma_f32_16x16x32_bf16 v[132:135], a[248:251], v[44:47], v[132:135]\n"
      "v_mfma_f32_16x16x32_bf16 v[132:135], a[252:255], v[60:63], v[132:135]\n"
      "v_cvt_pk_bf16_f32 v210, v136, v137\n"
      "global_store_dwordx4 v252, v[28:31], s[54:55] offset:0 sc1\n"
      "s_waitcnt lgkmcnt(4)\n"
      "v_mfma_f32_16x16x32_bf16 v[132:135], a[48:51], v[64:67], v[132:135]\n"
      "v_cvt_pk_bf16_f32 v211, v138, v139\n"
      "v_mfma_f32_16x16x32_bf16 v[132:135], a[52:55], v[68:71], v[132:135]\n"
      "v_mfma_f32_16x16x32_bf16 v[132:135], a[56:59], v[84:87], v[132:135]\n"
      "ds_read_b128 v[72:75], v246 offset:0\n"
      "v_mfma_f32_16x16x32_bf16 v[132:135], a[60:63], v[88:91], v[132:135]\n"
      "ds_read_b128 v[76:79], v246 offset:1024\n"
      "s_waitcnt lgkmcnt(2)\n"
      "v_mfma_f32_16x16x32_bf16 v[132:135], a[64:67], v[92:95], v[132:135]\n"
      "ds_read_b128 v[80:83], v246 offset:2048\n"
      "v_mfma_f32_16x16x32_bf16 v[132:135], a[68:71], v[108:111], v[132:135]\n"
      "ds_write_b128 v244, v[208:211] offset:12416\n"
      "v_mfma_f32_16x16x32_bf16 v[132:135], a[72:75], v[112:115], v[132:135]\n"
      "v_mfma_f32_16x16x32_bf16 v[132:135], a[76:79], v[116:119], v[132:135]\n"
      "v_mfma_f32_16x16x32_bf16 v[136:139], a[80:83], v[36:39], v[4:7]\n"
      "v_mfma_f32_16x16x32_bf16 v[136:139], a[84:87], v[40:43], v[136:139]\n"
      "v_mfma_f32_16x16x32_bf16 v[136:139], a[88:91], v[44:47], v[136:139]\n"
      "v_mfma_f32_16x16x32_bf16 v[136:139], a[92:95], v[60:63], v[136:139]\n"
      "v_cvt_pk_bf16_f32 v208, v132, v133\n"
      "global_store_dwordx4 v253, v[32:35], s[54:55] offset:0 sc1\n"
      "v_mfma_f32_16x16x32_bf16 v[136:139], a[96:99], v[64:67], v[136:139]\n"
      "v_cvt_pk_bf16_f32 v209, v134, v135\n"
      "v_mfma_f32_16x16x32_bf16 v[136:139], a[100:103], v[68:71], v[136:139]\n"
      "v_mfma_f32_16x16x32_bf16 v[136:139], a[104:107], v[84:87], v[136:139]\n"
      "v_mfma_f32_16x16x32_bf16 v[136:139], a[108:111], v[88:91], v[136:139]\n"
      "v_mfma_f32_16x16x32_bf16 v[136:139], a[112:115], v[92:95], v[136:139]\n"
      "v_mfma_f32_16x16x32_bf16 v[136:139], a[116:119], v[108:111], v[136:139]\n"
      "v_mfma_f32_16x16x32_bf16 v[136:139], a[120:123], v[112:115], v[136:139]\n"
      "v_mfma_f32_16x16x32_bf16 v[136:139], a[124:127], v[116:119], v[136:139]\n"
      "v_mfma_f32_16x16x32_bf16 v[132:135], a[128:131], v[36:39], v[8:11]\n"
      "v_mfma_f32_16x16x32_bf16 v[132:135], a[132:135], v[40:43], v[132:135]\n"
      "v_mfma_f32_16x16x32_bf16 v[132:135], a[136:139], v[44:47], v[132:135]\n"
      "v_mfma_f32_16x16x32_bf16 v[132:135], a[140:143], v[60:63], v[132:135]\n"
      "v_cvt_pk_bf16_f32 v210, v136, v137\n"
      "v_mfma_f32_16x16x32_bf16 v[132:135], a[144:147], v[64:67], v[132:135]\n"
      "v_cvt_pk_bf16_f32 v211, v138, v139\n"
      "v_mfma_f32_16x16x32_bf16 v[132:135], a[148:151], v[68:71], v[132:135]\n"
      "global_store_dwordx4 v247, v[48:51], s[54:55] offset:768 sc1\n"
      "v_mfma_f32_16x16x32_bf16 v[132:135], a[152:155], v[84:87], v[132:135]\n"
      "v_mfma_f32_16x16x32_bf16 v[132:135], a[156:159], v[88:91], v[132:135]\n"
      "v_mfma_f32_16x16x32_bf16 v[132:135], a[160:163], v[92:95], v[132:135]\n"
      "v_mfma_f32_16x16x32_bf16 v[132:135], a[164:167], v[108:111], v[132:135]\n"
      "ds_write_b128 v244, v[208:211] offset:24576\n"
      "v_mfma_f32_16x16x32_bf16 v[132:135], a[168:171], v[112:115], v[132:135]\n"
      "v_mfma_f32_16x16x32_bf16 v[132:135], a[172:175], v[116:119], v[132:135]\n"
      "v_mfma_f32_16x16x32_bf16 v[136:139], a[176:179], v[36:39], v[12:15]\n"
      "v_mfma_f32_16x16x32_bf16 v[136:139], a[180:183], v[40:43], v[136:139]\n"
      "v_mfma_f32_16x16x32_bf16 v[136:139], a[184:187], v[44:47], v[136:139]\n"
      "v_mfma_f32_16x16x32_bf16 v[136:139], a[188:191], v[60:63], v[136:139]\n"
      "v_cvt_pk_bf16_f32 v212, v132, v133\n"
      "v_mfma_f32_16x16x32_bf16 v[136:139], a[192:195], v[64:67], v[136:139]\n"
      "v_cvt_pk_bf16_f32 v213, v134, v135\n"
      "v_mfma_f32_16x16x32_bf16 v[136:139], a[196:199], v[68:71], v[136:139]\n"
      "v_mfma_f32_16x16x32_bf16 v[136:139], a[200:203], v[84:87], v[136:139]\n"
      "global_store_dwordx4 v252, v[52:55], s[54:55] offset:768 sc1\n"
      "v_mfma_f32_16x16x32_bf16 v[136:139], a[204:207], v[88:91], v[136:139]\n"
      "v_mfma_f32_16x16x32_bf16 v[136:139], a[208:211], v[92:95], v[136:139]\n"
      "v_mfma_f32_16x16x32_bf16 v[136:139], a[212:215], v[108:111], v[136:139]\n"
      "v_mfma_f32_16x16x32_bf16 v[136:139], a[216:219], v[112:115], v[136:139]\n"
      "v_mfma_f32_16x16x32_bf16 v[136:139], a[220:223], v[116:119], v[136:139]\n"
      "v_mfma_f32_16x16x32_bf16 v[132:135], a[224:227], v[36:39], v[16:19]\n"
      "v_mfma_f32_16x16x32_bf16 v[132:135], a[228:231], v[40:43], v[132:135]\n"
      "v_mfma_f32_16x16x32_bf16 v[132:135], a[232:235], v[44:47], v[132:135]\n"
      "v_mfma_f32_16x16x32_bf16 v[132:135], a[236:239], v[60:63], v[132:135]\n"
      "v_cvt_pk_bf16_f32 v214, v136, v137\n"
      "v_mfma_f32_16x16x32_bf16 v[132:135], a[0:3], v[64:67], v[132:135]\n"
      "v_cvt_pk_bf16_f32 v215, v138, v139\n"
      "v_mfma_f32_16x16x32_bf16 v[132:135], a[4:7], v[68:71], v[132:135]\n"
      "v_mfma_f32_16x16x32_bf16 v[132:135], a[8:11], v[84:87], v[132:135]\n"
      "global_store_dwordx4 v253, v[56:59], s[54:55] offset:768 sc1\n"
      "v_mfma_f32_16x16x32_bf16 v[132:135], a[12:15], v[88:91], v[132:135]\n"
      "v_mfma_f32_16x16x32_bf16 v[132:135], a[16:19], v[92:95], v[132:135]\n"
      "v_mfma_f32_16x16x32_bf16 v[132:135], a[20:23], v[108:111], v[132:135]\n"
      "ds_write_b128 v245, v[212:215] offset:24576\n"
      "v_mfma_f32_16x16x32_bf16 v[132:135], v[140:143], v[112:115], v[132:135]\n"
      "v_mfma_f32_16x16x32_bf16 v[132:135], v[156:159], v[116:119], v[132:135]\n"
      "s_waitcnt lgkmcnt(2)\n"
      "s_barrier\n"
      "v_mfma_f32_16x16x32_bf16 v[136:139], v[160:163], v[36:39], v[20:23]\n"
      "ds_read_b128 v[36:39], v232 offset:24832\n"
      "s_lshl_b32 s79, s97, 1\n"
      "v_mfma_f32_16x16x32_bf16 v[136:139], v[164:167], v[40:43], v[136:139]\n"
      "ds_read_b128 v[40:43], v233 offset:24832\n"
      "v_mad_i32_i24 v252, v251, s79, v252\n"
      "v_mfma_f32_16x16x32_bf16 v[136:139], v[180:183], v[44:47], v[136:139]\n"
      "ds_read_b128 v[44:47], v234 offset:24832\n"
      "s_mul_i32 s79, s79, s100\n"
      "v_mfma_f32_16x16x32_bf16 v[136:139], v[184:187], v[60:63], v[136:139]\n"
      "ds_read_b128 v[60:63], v235 offset:24832\n"
      "v_cvt_pk_bf16_f32 v208, v132, v133\n"
      "v_add_u32 v247, s79, v247\n"
      "v_mfma_f32_16x16x32_bf16 v[136:139], v[188:191], v[64:67], v[136:139]\n"
      "ds_read_b128 v[64:67], v232 offset:33024\n"
      "v_cvt_pk_bf16_f32 v209, v134, v135\n"
      "v_mfma_f32_16x16x32_bf16 v[136:139], v[204:207], v[68:71], v[136:139]\n"
      "ds_read_b128 v[68:71], v233 offset:33024\n"
      "s_mul_i32 s79, s79, s101\n"
      "v_mfma_f32_16x16x32_bf16 v[136:139], a[24:27], v[84:87], v[136:139]\n"
      "ds_read_b128 v[84:87], v234 offset:33024\n"
      "v_add_u32 v253, s79, v253\n"
      "v_mfma_f32_16x16x32_bf16 v[136:139], a[28:31], v[88:91], v[136:139]\n"
      "ds_read_b128 v[88:91], v235 offset:33024\n"
      "s_add_u32 s54, s30, s40\n"
      "s_addc_u32 s55, s31, 0\n"
      "v_mfma_f32_16x16x32_bf16 v[136:139], a[32:35], v[92:95], v[136:139]\n"
      "ds_read_b128 v[92:95], v232 offset:41216\n"
      "v_mfma_f32_16x16x32_bf16 v[136:139], a[36:39], v[108:111], v[136:139]\n"
      "ds_read_b128 v[108:111], v233 offset:41216\n"
      "global_store_dwordx4 v247, v[72:75], s[54:55] offset:0 sc1\n"
      "v_mfma_f32_16x16x32_bf16 v[136:139], a[40:43], v[112:115], v[136:139]\n"
      "ds_read_b128 v[112:115], v234 offset:41216\n"
      "v_mfma_f32_16x16x32_bf16 v[136:139], a[44:47], v[116:119], v[136:139]\n"
      "ds_read_b128 v[116:119], v235 offset:41216\n"
      "s_waitcnt lgkmcnt(8)\n"
      "v_mfma_f32_16x16x32_bf16 v[132:135], a[240:243], v[36:39], v[0:3]\n"
      "v_mfma_f32_16x16x32_bf16 v[132:135], a[244:247], v[40:43], v[132:135]\n"
      "v_mfma_f32_16x16x32_bf16 v[132:135], a[248:251], v[44:47], v[132:135]\n"
      "v_mfma_f32_16x16x32_bf16 v[132:135], a[252:255], v[60:63], v[132:135]\n"
      "v_cvt_pk_bf16_f32 v210, v136, v137\n"
      "s_waitcnt lgkmcnt(4)\n"
      "v_mfma_f32_16x16x32_bf16 v[132:135], a[48:51], v[64:67], v[132:135]\n"
      "v_cvt_pk_bf16_f32 v211, v138, v139\n"
      "v_mfma_f32_16x16x32_bf16 v[132:135], a[52:55], v[68:71], v[132:135]\n"
      "v_mfma_f32_16x16x32_bf16 v[132:135], a[56:59], v[84:87], v[132:135]\n"
      "ds_read_b128 v[96:99], v246 offset:12288\n"
      "v_mfma_f32_16x16x32_bf16 v[132:135], a[60:63], v[88:91], v[132:135]\n"
      "ds_read_b128 v[100:103], v246 offset:13312\n"
      "s_waitcnt lgkmcnt(2)\n"
      "v_mfma_f32_16x16x32_bf16 v[132:135], a[64:67], v[92:95], v[132:135]\n"
      "ds_read_b128 v[104:107], v246 offset:14336\n"
      "v_mfma_f32_16x16x32_bf16 v[132:135], a[68:71], v[108:111], v[132:135]\n"
      "ds_write_b128 v244, v[208:211] offset:24704\n"
      "v_mfma_f32_16x16x32_bf16 v[132:135], a[72:75], v[112:115], v[132:135]\n"
      "v_mfma_f32_16x16x32_bf16 v[132:135], a[76:79], v[116:119], v[132:135]\n"
      "v_mfma_f32_16x16x32_bf16 v[136:139], a[80:83], v[36:39], v[4:7]\n"
      "v_mfma_f32_16x16x32_bf16 v[136:139], a[84:87], v[40:43], v[136:139]\n"
      "global_store_dwordx4 v252, v[76:79], s[54:55] offset:0 sc1\n"
      "v_mfma_f32_16x16x32_bf16 v[136:139], a[88:91], v[44:47], v[136:139]\n"
      "v_mfma_f32_16x16x32_bf16 v[136:139], a[92:95], v[60:63], v[136:139]\n"
      "v_cvt_pk_bf16_f32 v208, v132, v133\n"
      "v_mfma_f32_16x16x32_bf16 v[136:139], a[96:99], v[64:67], v[136:139]\n"
      "v_cvt_pk_bf16_f32 v209, v134, v135\n"
      "v_mfma_f32_16x16x32_bf16 v[136:139], a[100:103], v[68:71], v[136:139]\n"
      "v_mfma_f32_16x16x32_bf16 v[136:139], a[104:107], v[84:87], v[136:139]\n"
      "v_mfma_f32_16x16x32_bf16 v[136:139], a[108:111], v[88:91], v[136:139]\n"
      "v_mfma_f32_16x16x32_bf16 v[136:139], a[112:115], v[92:95], v[136:139]\n"
      "v_mfma_f32_16x16x32_bf16 v[136:139], a[116:119], v[108:111], v[136:139]\n"
      "v_mfma_f32_16x16x32_bf16 v[136:139], a[120:123], v[112:115], v[136:139]\n"
      "v_mfma_f32_16x16x32_bf16 v[136:139], a[124:127], v[116:119], v[136:139]\n"
      "v_mfma_f32_16x16x32_bf16 v[132:135], a[128:131], v[36:39], v[8:11]\n"
      "global_store_dwordx4 v253, v[80:83], s[54:55] offset:0 sc1\n"
      "v_mfma_f32_16x16x32_bf16 v[132:135], a[132:135], v[40:43], v[132:135]\n"
      "v_mfma_f32_16x16x32_bf16 v[132:135], a[136:139], v[44:47], v[132:135]\n"
      "v_mfma_f32_16x16x32_bf16 v[132:135], a[140:143], v[60:63], v[132:135]\n"
      "v_cvt_pk_bf16_f32 v210, v136, v137\n"
      "v_mfma_f32_16x16x32_bf16 v[132:135], a[144:147], v[64:67], v[132:135]\n"
      "v_cvt_pk_bf16_f32 v211, v138, v139\n"
      "v_mfma_f32_16x16x32_bf16 v[132:135], a[148:151], v[68:71], v[132:135]\n"
      "v_mfma_f32_16x16x32_bf16 v[132:135], a[152:155], v[84:87], v[132:135]\n"
      "v_mfma_f32_16x16x32_bf16 v[132:135], a[156:159], v[88:91], v[132:135]\n"
      "v_mfma_f32_16x16x32_bf16 v[132:135], a[160:163], v[92:95], v[132:135]\n"
      "v_mfma_f32_16x16x32_bf16 v[132:135], a[164:167], v[108:111], v[132:135]\n"
      "ds_write_b128 v244, v[208:211] offset:0\n"
      "v_mfma_f32_16x16x32_bf16 v[132:135], a[168:171], v[112:115], v[132:135]\n"
      "s_waitcnt lgkmcnt(4)\n"
      "v_mfma_f32_16x16x32_bf16 v[132:135], a[172:175], v[116:119], v[132:135]\n"
      "global_store_dwordx4 v247, v[96:99], s[54:55] offset:768 sc1\n"
      "v_mfma_f32_16x16x32_bf16 v[136:139], a[176:179], v[36:39], v[12:15]\n"
      "v_mfma_f32_16x16x32_bf16 v[136:139], a[180:183], v[40:43], v[136:139]\n"
      "v_mfma_f32_16x16x32_bf16 v[136:139], a[184:187], v[44:47], v[136:139]\n"
      "v_mfma_f32_16x16x32_bf16 v[136:139], a[188:191], v[60:63], v[136:139]\n"
      "v_cvt_pk_bf16_f32 v212, v132, v133\n"
      "v_mfma_f32_16x16x32_bf16 v[136:139], a[192:195], v[64:67], v[136:139]\n"
      "v_cvt_pk_bf16_f32 v213, v134, v135\n"
      "v_mfma_f32_16x16x32_bf16 v[136:139], a[196:199], v[68:71], v[136:139]\n"
      "v_mfma_f32_16x16x32_bf16 v[136:139], a[200:203], v[84:87], v[136:139]\n"
      "v_mfma_f32_16x16x32_bf16 v[136:139], a[204:207], v[88:91], v[136:139]\n"
      "v_mfma_f32_16x16x32_bf16 v[136:139], a[208:211], v[92:95], v[136:139]\n"
      "s_waitcnt lgkmcnt(3)\n"
      "v_mfma_f32_16x16x32_bf16 v[136:139], a[212:215], v[108:111], v[136:139]\n"
      "global_store_dwordx4 v252, v[100:103], s[54:55] offset:768 sc1\n"
      "v_mfma_f32_16x16x32_bf16 v[136:139], a[216:219], v[112:115], v[136:139]\n"
      "v_mfma_f32_16x16x32_bf16 v[136:139], a[220:223], v[116:119], v[136:139]\n"
      "v_mfma_f32_16x16x32_bf16 v[132:135], a[224:227], v[36:39], v[16:19]\n"
      "v_mfma_f32_16x16x32_bf16 v[132:135], a[228:231], v[40:43], v[132:135]\n"
      "v_mfma_f32_16x16x32_bf16 v[132:135], a[232:235], v[44:47], v[132:135]\n"
      "v_mfma_f32_16x16x32_bf16 v[132:135], a[236:239], v[60:63], v[132:135]\n"
      "v_cvt_pk_bf16_f32 v214, v136, v137\n"
      "v_mfma_f32_16x16x32_bf16 v[132:135], a[0:3], v[64:67], v[132:135]\n"
      "v_cvt_pk_bf16_f32 v215, v138, v139\n"
      "v_mfma_f32_16x16x32_bf16 v[132:135], a[4:7], v[68:71], v[132:135]\n"
      "v_mfma_f32_16x16x32_bf16 v[132:135], a[8:11], v[84:87], v[132:135]\n"
      "v_mfma_f32_16x16x32_bf16 v[132:135], a[12:15], v[88:91], v[132:135]\n"
      "s_waitcnt lgkmcnt(2)\n"
      "v_mfma_f32_16x16x32_bf16 v[132:135], a[16:19], v[92:95], v[132:135]\n"
      "global_store_dwordx4 v253, v[104:107], s[54:55] offset:768 sc1\n"
      "v_mfma_f32_16x16x32_bf16 v[132:135], a[20:23], v[108:111], v[132:135]\n"
      "ds_write_b128 v245, v[212:215] offset:0\n"
      "v_mfma_f32_16x16x32_bf16 v[132:135], v[140:143], v[112:115], v[132:135]\n"
      "v_mfma_f32_16x16x32_bf16 v[132:135], v[156:159], v[116:119], v[132:135]\n"
      "s_waitcnt lgkmcnt(2)\n"
      "s_barrier\n"
      "v_mfma_f32_16x16x32_bf16 v[136:139], v[160:163], v[36:39], v[20:23]\n"
      "v_mfma_f32_16x16x32_bf16 v[136:139], v[164:167], v[40:43], v[136:139]\n"
      "v_mfma_f32_16x16x32_bf16 v[136:139], v[180:183], v[44:47], v[136:139]\n"
      "v_mfma_f32_16x16x32_bf16 v[136:139], v[184:187], v[60:63], v[136:139]\n"
      "v_cvt_pk_bf16_f32 v208, v132, v133\n"
      "v_mfma_f32_16x16x32_bf16 v[136:139], v[188:191], v[64:67], v[136:139]\n"
      "v_cvt_pk_bf16_f32 v209, v134, v135\n"
      "v_mfma_f32_16x16x32_bf16 v[136:139], v[204:207], v[68:71], v[136:139]\n"
      "v_mfma_f32_16x16x32_bf16 v[136:139], a[24:27], v[84:87], v[136:139]\n"
      "v_mfma_f32_16x16x32_bf16 v[136:139], a[28:31], v[88:91], v[136:139]\n"
      "v_mfma_f32_16x16x32_bf16 v[136:139], a[32:35], v[92:95], v[136:139]\n"
      "v_mfma_f32_16x16x32_bf16 v[136:139], a[36:39], v[108:111], v[136:139]\n"
      "v_mfma_f32_16x16x32_bf16 v[136:139], a[40:43], v[112:115], v[136:139]\n"
      "v_mfma_f32_16x16x32_bf16 v[136:139], a[44:47], v[116:119], v[136:139]\n"
      "ds_read_b128 v[120:123], v246 offset:24576\n"
      "ds_read_b128 v[124:127], v246 offset:25600\n"
      "ds_read_b128 v[128:131], v246 offset:26624\n"
      "s_add_u32 s54, s44, s40\n"
      "s_addc_u32 s55, s45, 0\n"
      "s_nop 3\n"
      "v_cvt_pk_bf16_f32 v210, v136, v137\n"
      "v_cvt_pk_bf16_f32 v211, v138, v139\n"
      "ds_write_b128 v244, v[208:211] offset:128\n"
      "s_waitcnt lgkmcnt(3)\n"
      "global_store_dwordx4 v247, v[120:123], s[54:55] offset:0 sc1\n"
      "s_waitcnt lgkmcnt(2)\n"
      "global_store_dwordx4 v252, v[124:127], s[54:55] offset:0 sc1\n"
      "s_waitcnt lgkmcnt(1)\n"
      "global_store_dwordx4 v253, v[128:131], s[54:55] offset:0 sc1\n"
      "s_waitcnt lgkmcnt(0)\n"
      "s_barrier\n"
      "ds_read_b128 v[144:147], v246 offset:0\n"
      "ds_read_b128 v[148:151], v246 offset:1024\n"
      "ds_read_b128 v[152:155], v246 offset:2048\n"
      "s_waitcnt lgkmcnt(2)\n"
      "global_store_dwordx4 v247, v[144:147], s[54:55] offset:768 sc1\n"
      "s_waitcnt lgkmcnt(1)\n"
      "global_store_dwordx4 v252, v[148:151], s[54:55] offset:768 sc1\n"
      "s_waitcnt lgkmcnt(0)\n"
      "global_store_dwordx4 v253, v[152:155], s[54:55] offset:768 sc1\n"
      "s_cmp_lg_u32 s87, 0\n"
      "s_cbranch_scc1 LE1\n"
      "v_min_u32 v0, 0x370, v239\n"
      "v_add_u32 v0, 0x2480, v0\n"
      "s_mov_b32 s82, 0x40000\n"
      "LE0:\n"
      "global_load_dwordx4 v[4:7], v0, s[28:29] sc1\n"
      "s_sub_u32 s82, s82, 1\n"
      "s_cbranch_scc1 LE1\n"
      "s_waitcnt vmcnt(0)\n"
      "v_cmp_ne_u64 vcc, s[80:81], v[4:5]\n"
      "v_cmp_ne_u64 s[56:57], s[80:81], v[6:7]\n"
      "s_nop 3\n"
      "s_or_b64 vcc, vcc, s[56:57]\n"
      "s_cbranch_scc1 LE0\n"
      "v_mov_b32 v0, 0\n"
      "v_mov_b32 v2, s80\n"
      "v_mov_b32 v3, s81\n"
      "s_mov_b64 exec, 1\n"
      "global_store_dwordx2 v0, v[2:3], s[28:29] sc1\n"
      "s_mov_b64 exec, -1\n"
      "LE1:\n"
      "LPD16:\n"
      "s_cbranch_execz LRET\n"
      "s_waitcnt vmcnt(0)\n"
      "s_branch LCEND\n"
      "LPS:\n"
      "s_mov_b64 exec, 1\n"
      "global_store_dword v229, v230, s[28:29] sc1\n"
      "s_mov_b64 exec, -1\n"
      "s_branch LPR\n"
      "LT0:\n"
      "s_nop 11\n"
      "s_branch LS0\n"
      "LT1:\n"
      "s_nop 11\n"
      "s_branch LS1\n"
      "LT2:\n"
      "s_nop 11\n"
      "s_branch LS2\n"
      "LT3:\n"
      "s_nop 11\n"
      "s_branch LS3\n"
      "LT4:\n"
      "s_nop 11\n"
      "s_branch LS4\n"
      "LT5:\n"
      "s_nop 11\n"
      "s_branch LS5\n"
      "LT6:\n"
      "s_nop 11\n"
      "s_branch LS6\n"
      "LT7:\n"
      "s_nop 11\n"
      "s_branch LS7\n"
      "LT8:\n"
      "s_nop 11\n"
      "s_branch LS8\n"
      "LT9:\n"
      "s_nop 11\n"
      "s_branch LS9\n"
      "LT10:\n"
      "s_nop 11\n"
      "s_branch LS10\n"
      "LT11:\n"
      "s_nop 11\n"
      "s_branch LS11\n"
      "LT12:\n"
      "s_nop 11\n"
      "s_branch LS12\n"
      "LT13:\n"
      "s_nop 11\n"
      "s_branch LS13\n"
      "LT14:\n"
      "s_nop 11\n"
      "s_branch LS14\n"
      "LT15:\n"
      "s_nop 11\n"
      "s_branch LS15\n"
      "LT16:\n"
      "s_nop 11\n"
      "s_branch LS16\n"
      "LT17:\n"
      "s_nop 11\n"
      "s_branch LS17\n"
      "LT18:\n"
      "s_nop 11\n"
      "s_branch LS18\n"
      "LT19:\n"
      "s_nop 11\n"
      "s_branch LS19\n"
      "LT20:\n"
      "s_nop 11\n"
      "s_branch LS20\n"
      "LT21:\n"
      "s_nop 11\n"
      "s_branch LS21\n"
      "LT22:\n"
      "s_nop 11\n"
      "s_branch LS22\n"
      "LT23:\n"
      "s_nop 11\n"
      "s_branch LS23\n"
      "LT24:\n"
      "s_nop 11\n"
      "s_branch LS24\n"
      "LT25:\n"
      "s_nop 11\n"
      "s_branch LS25\n"
      "LT26:\n"
      "s_nop 11\n"
      "s_branch LS26\n"
      "LT27:\n"
      "s_nop 11\n"
      "s_branch LS27\n"
      "LT28:\n"
      "s_nop 11\n"
      "s_branch LS28\n"
      "LT29:\n"
      "s_nop 11\n"
      "s_branch LS29\n"
      "LT30:\n"
      "s_nop 11\n"
      "s_branch LS30\n"
      "LT31:\n"
      "s_nop 11\n"
      "s_branch LS31\n"
      "LT32:\n"
      "s_nop 11\n"
      "s_branch LS32\n"
      "LT33:\n"
      "s_nop 11\n"
      "s_branch LS33\n"
      "LT34:\n"
      "s_nop 11\n"
      "s_branch LS34\n"
      "LT35:\n"
      "s_nop 11\n"
      "s_branch LS35\n"
      "LT36:\n"
      "s_nop 11\n"
      "s_branch LS36\n"
      "LT37:\n"
      "s_nop 11\n"
      "s_branch LS37\n"
      "LT38:\n"
      "s_nop 11\n"
      "s_branch LS38\n"
      "LT39:\n"
      "s_nop 11\n"
      "s_branch LS39\n"
      "LT40:\n"
      "s_nop 11\n"
      "s_branch LS40\n"
      "LT41:\n"
      "s_nop 11\n"
      "s_branch LS41\n"
      "LCEND:\n"

      :
      : "{v232}"(cB0), "{v233}"(cB0 ^ 64), "{v234}"(cB0 ^ 128), "{v235}"(cB0 ^ 192), "{v238}"(pvo),
        "{v239}"(lo16), "{v240}"(wro[0]), "{v241}"(wro[1]), "{v242}"(wro[2]), "{v243}"(tra), "{v248}"(fla), "{v249}"(c256),
        "{v250}"(pk), "{v105}"(two), "{v106}"(tcode), "{v[252:253]}"(ep),
        "{s[20:21]}"(cf), "{s[22:23]}"(pc), "{s[24:25]}"(wb), "{s[30:31]}"(out), "{s[28:29]}"(ws), "{s18}"(mP), "{s17}"(mX),
        "{s32}"(xo[0]), "{s33}"(xo[1]), "{s34}"(xo[2]), "{s35}"(xo[3]), "{s36}"(xo[4]), "{s37}"(xo[5]), "{s38}"(xo[6]),
        "{s39}"(xo[7]), "{s40}"(xo[8]), "{s[42:43]}"(cf + 294912), "{s[44:45]}"(out + 294912), "{s[46:47]}"(tab), "{s50}"(var), "{s51}"(b),
        "{s[64:65]}"(w2w), "{s74}"(mW), "{s76}"(wid), "{s77}"(jfl), "{v251}"(sgn),
        "{s89}"(dj[0]), "{s90}"(dj[1]), "{s91}"(dj[2]), "{s92}"(dj[3]), "{s93}"(dj[4]), "{s94}"(dj[5]), "{s95}"(dj[6]),
        "{s96}"(dj[7]), "{s97}"(dj[8]), "{s98}"(d0), "{s99}"(wid ? -1 : 1), "{s100}"(wid < 3 ? 1 : -1), "{s101}"(wid == 2 ? -1 : 1),
        "{v102}"(jt), "{v103}"(jrw), "{v104}"(jrr), "{v[110:111]}"(cd), "{v[112:113]}"(cr), "{v107}"(sg1), "{v108}"(scr), "{s[72:73]}"(jo),
        "{v[4:7]}"(ja0), "{v[8:11]}"(ja1), "{v[12:15]}"(ja2), "{v[16:19]}"(ja3), "{v[20:23]}"(ja4), "{v[24:27]}"(ja5),
        "{s83}"(t_start + (0) + var * ((0) - (0))), "{s86}"(158 + var * ((158) - (158)))
      : "memory", "scc", "vcc", "v0", "v1", "v2", "v3", "v28", "v29", "v30", "v31", "v32", "v33", "v34", "v35", "v36", "v37", "v38", "v39", "v40", "v41", "v42", "v43", "v44", "v45", "v46", "v47", "v48", "v49", "v50", "v51", "v52", "v53", "v54", "v55", "v56", "v57", "v58", "v59", "v60", "v61", "v62", "v63", "v64", "v65", "v66", "v67", "v68", "v69", "v70", "v71", "v72", "v73", "v74", "v75", "v76", "v77", "v78", "v79", "v80", "v81", "v82", "v83", "v84", "v85", "v86", "v87", "v88", "v89", "v90", "v91", "v92", "v93", "v94", "v95", "v96", "v97", "v98", "v99", "v100", "v101", "v109", "v114", "v115", "v116", "v117", "v118", "v119", "v120", "v121", "v122", "v123", "v124", "v125", "v126", "v127", "v128", "v129", "v130", "v131", "v132", "v133", "v134", "v135", "v136", "v137", "v138", "v139", "v140", "v141", "v142", "v143", "v144", "v145", "v146", "v147", "v148", "v149", "v150", "v151", "v152", "v153", "v154", "v155", "v156", "v157", "v158", "v159", "v160", "v161", "v162", "v163", "v164", "v165", "v166", "v167", "v168", "v169", "v170", "v171", "v172", "v173", "v174", "v175", "v176", "v177", "v178", "v179", "v180", "v181", "v182", "v183", "v184", "v185", "v186", "v187", "v188", "v189", "v190", "v191", "v192", "v193", "v194", "v195", "v196", "v197", "v198", "v199", "v200", "v201", "v202", "v203", "v204", "v205", "v206", "v207", "v208", "v209", "v210", "v211", "v212", "v213", "v214", "v215", "v216", "v217", "v218", "v219", "v220", "v221", "v222", "v223", "v224", "v225", "v226", "v227", "v228", "v229", "v230", "v231", "v236", "v237", "v244", "v245", "v246", "v247", "v254", "v255", "a0", "a1", "a2", "a3", "a4", "a5", "a6", "a7", "a8", "a9", "a10", "a11", "a12", "a13", "a14", "a15", "a16", "a17", "a18", "a19", "a20", "a21", "a22", "a23", "a24", "a25", "a26", "a27", "a28", "a29", "a30", "a31", "a32", "a33", "a34", "a35", "a36", "a37", "a38", "a39", "a40", "a41", "a42", "a43", "a44", "a45", "a46", "a47", "a48", "a49", "a50", "a51", "a52", "a53", "a54", "a55", "a56", "a57", "a58", "a59", "a60", "a61", "a62", "a63", "a64", "a65", "a66", "a67", "a68", "a69", "a70", "a71", "a72", "a73", "a74", "a75", "a76", "a77", "a78", "a79", "a80", "a81", "a82", "a83", "a84", "a85", "a86", "a87", "a88", "a89", "a90", "a91", "a92", "a93", "a94", "a95", "a96", "a97", "a98", "a99", "a100", "a101", "a102", "a103", "a104", "a105", "a106", "a107", "a108", "a109", "a110", "a111", "a112", "a113", "a114", "a115", "a116", "a117", "a118", "a119", "a120", "a121", "a122", "a123", "a124", "a125", "a126", "a127", "a128", "a129", "a130", "a131", "a132", "a133", "a134", "a135", "a136", "a137", "a138", "a139", "a140", "a141", "a142", "a143", "a144", "a145", "a146", "a147", "a148", "a149", "a150", "a151", "a152", "a153", "a154", "a155", "a156", "a157", "a158", "a159", "a160", "a161", "a162", "a163", "a164", "a165", "a166", "a167", "a168", "a169", "a170", "a171", "a172", "a173", "a174", "a175", "a176", "a177", "a178", "a179", "a180", "a181", "a182", "a183", "a184", "a185", "a186", "a187", "a188", "a189", "a190", "a191", "a192", "a193", "a194", "a195", "a196", "a197", "a198", "a199", "a200", "a201", "a202", "a203", "a204", "a205", "a206", "a207", "a208", "a209", "a210", "a211", "a212", "a213", "a214", "a215", "a216", "a217", "a218", "a219", "a220", "a221", "a222", "a223", "a224", "a225", "a226", "a227", "a228", "a229", "a230", "a231", "a232", "a233", "a234", "a235", "a236", "a237", "a238", "a239", "a240", "a241", "a242", "a243", "a244", "a245", "a246", "a247", "a248", "a249", "a250", "a251", "a252", "a253", "a254", "a255", "s48", "s49", "s66", "s67", "s68", "s69", "s70", "s71", "s52", "s53", "s54", "s55", "s56", "s57", "s58", "s59", "s60", "s61", "s62", "s63", "s78", "s79", "s80", "s81", "s82", "s84", "s85", "s87", "s19");
}

extern "C" void kernel_launch(void* const* d_in, const int* in_sizes, int n_in,
                              void* d_out, int out_size, void* d_ws, size_t ws_size,
                              hipStream_t stream) {
  (void)hipFuncSetAttribute((const void*)fpn_kernel, hipFuncAttributeMaxDynamicSharedMemorySize, 159808);
  fpn_kernel<<<256, 256, 159808, stream>>>((const char*)d_in[0], (const char*)d_in[1], (const char*)d_in[2],
                                                (const char*)d_in[3], (char*)d_ws, (char*)d_out);
}
```
